```python
import torch

def _softmax(x, axis=-1):
    return torch.softmax(x, dim=axis)

def _gelu(x):
    return torch.nn.functional.gelu(x, approximate='tanh')

def _silu(x):
    return torch.nn.functional.silu(x)

def _sigmoid(x):
    return torch.sigmoid(x)

def _layernorm(x, axis=-1, eps=1e-05):
    m = x.mean(dim=axis, keepdim=True)
    v = x.var(dim=axis, keepdim=True, unbiased=False)
    return (x - m) / torch.sqrt(v + eps)

def _scatter_add(tgt, idx, src, axis=0):
    return tgt.clone().index_add_(axis, idx, src)

def _segment_sum(src, seg, n):
    out = torch.zeros((n,) + src.shape[1:], dtype=src.dtype)
    return out.index_add_(0, seg, src)

def _pad(x, pw):
    flat = tuple((v for pair in reversed(pw) for v in pair))
    return torch.nn.functional.pad(x, flat)
kernel_name = 'concat_heads__memory_aug_recurrent_mem_tr__ssm_conv_s4__prompt'

def setup_inputs():

    _w = lambda a: (lambda t: t.to(torch.bfloat16) if t.is_floating_point() else t)(torch.as_tensor(a))

    def _A_setup():
        B, S, Dm = (1, 2048, 1024)
        H, Dh = (32, 128)
        M, seg = (8, 32)
        seg = min(seg, S)
        S = S // seg * seg
        return {'x': _w((lambda *s: torch.randn(*s, dtype=torch.bfloat16) * 0.05)(B, S, Dm)), 'mem0': _w((lambda *s: torch.randn(*s, dtype=torch.bfloat16) * 0.4)(B, M, Dm)), 'Wq': _w((lambda *s: torch.randn(*s, dtype=torch.bfloat16) * 6.4)(Dm, H, Dh) * Dm ** (-0.5)), 'Wk': _w((lambda *s: torch.randn(*s, dtype=torch.bfloat16) * 6.4)(Dm, H, Dh) * Dm ** (-0.5)), 'Wv': _w((lambda *s: torch.randn(*s, dtype=torch.bfloat16) * 0.4)(Dm, H, Dh) * Dm ** (-0.5)), 'Wo': _w((lambda *s: torch.randn(*s, dtype=torch.bfloat16) * 0.4)(H, Dh, Dm) * (H * Dh) ** (-0.5)), 'seg_len': seg}
    _A_in = _A_setup()

    def _B_setup():
        B, L, D, N = (1, 2048, 1024, 128)
        A_re = -(lambda _x: torch.exp(torch.as_tensor(_x)))((lambda *s: torch.randn(*s, dtype=torch.bfloat16) * 6.4)(D, N))
        A_im = torch.pi * (lambda *s: torch.randn(*s, dtype=torch.bfloat16) * 0.05)(D, N)
        B_re = (lambda *s: torch.randn(*s, dtype=torch.bfloat16) * 0.1)(D, N)
        B_im = (lambda *s: torch.randn(*s, dtype=torch.bfloat16) * 0.1)(D, N)
        C_re = (lambda *s: torch.randn(*s, dtype=torch.bfloat16) * 0.1)(D, N)
        C_im = (lambda *s: torch.randn(*s, dtype=torch.bfloat16) * 0.1)(D, N)
        log_dt = (lambda *s: torch.randn(*s, dtype=torch.bfloat16) * 0.05)(D) * 0.5 - 4.0
        Dskip = (lambda *s: torch.randn(*s, dtype=torch.bfloat16) * 0.05)(D)
        return {'u': _w((lambda *s: torch.randn(*s, dtype=torch.bfloat16) * 0.05)(B, L, D)), 'A_re': _w(A_re), 'A_im': _w(A_im), 'B_re': _w(B_re), 'B_im': _w(B_im), 'C_re': _w(C_re), 'C_im': _w(C_im), 'log_dt': _w(log_dt), 'Dskip': _w(Dskip)}
    _B_in = _B_setup()
    return {'A_x': _A_in['x'], 'A_mem0': _A_in['mem0'], 'A_Wq': _A_in['Wq'], 'A_Wk': _A_in['Wk'], 'A_Wv': _A_in['Wv'], 'A_Wo': _A_in['Wo'], 'A_seg_len': _A_in['seg_len'], 'B_u': _B_in['u'], 'B_A_re': _B_in['A_re'], 'B_A_im': _B_in['A_im'], 'B_B_re': _B_in['B_re'], 'B_B_im': _B_in['B_im'], 'B_C_re': _B_in['C_re'], 'B_C_im': _B_in['C_im'], 'B_log_dt': _B_in['log_dt'], 'B_Dskip': _B_in['Dskip']}

def reference(A_x, A_mem0, A_Wq, A_Wk, A_Wv, A_Wo, A_seg_len, B_u, B_A_re, B_A_im, B_B_re, B_B_im, B_C_re, B_C_im, B_log_dt, B_Dskip):

    def _A_ref(x, mem0, Wq, Wk, Wv, Wo, seg_len):
        B, S, Dm = x.shape
        H, Dh = (Wq.shape[1], Wq.shape[2])
        M = mem0.shape[1]
        scale = Dh ** (-0.5)
        n_seg = S // seg_len
        L = M + seg_len
        idx = torch.arange(L)
        mask = (idx[:, None] < M) | (idx[:, None] >= idx[None, :])
        mem = mem0
        outs = []
        for s in range(n_seg):
            xs = x[:, s * seg_len:(s + 1) * seg_len, :].clone()
            h = torch.concatenate([mem, xs], axis=1)
            q = torch.einsum('btd,dhe->bhte', h, Wq)
            k = torch.einsum('btd,dhe->bhte', h, Wk)
            v = torch.einsum('btd,dhe->bhte', h, Wv)
            scores = torch.einsum('bhqe,bhke->bhqk', q, k) * scale
            scores = torch.where(mask[None, None], scores, -1000000000.0)
            p = _softmax(scores, axis=-1)
            o = torch.einsum('bhqk,bhke->bhqe', p, v)
            o = torch.einsum('bhte,hed->btd', o, Wo)
            mem = o[:, :M, :].clone()
            outs.append(o[:, M:, :])
        return torch.concatenate(outs, axis=1)

    def _B_ref(u, A_re, A_im, B_re, B_im, C_re, C_im, log_dt, Dskip):
        Bsz, L, D = u.shape
        dt = torch.exp(log_dt)[:, None]
        A = A_re + 1j * A_im
        Bc = B_re + 1j * B_im
        Cc = C_re + 1j * C_im
        logdA = dt * A
        dB = dt * Bc
        l = (lambda x, dt: x.to(dt))(torch.arange(L), A_re.dtype)
        P = torch.exp(logdA[:, :, None] * l[None, None, :])
        K = torch.einsum('dn,dnl,dn->dl', Cc, P, dB).real
        L2 = 2 * L
        Kp = _pad(K, ((0, 0), (0, L)))
        up = (lambda x, *d: x.permute(*d))(u, 0, 2, 1)
        up = _pad(up, ((0, 0), (0, 0), (0, L)))
        Kf = (lambda x, axes=(-2, -1): torch.fft.rfft2(x.float(), dim=axes))(Kp[:, None, :], axes=(-2, -1))
        Uf = (lambda x, axes=(-2, -1): torch.fft.rfft2(x.float(), dim=axes))(up[:, :, None, :], axes=(-2, -1))
        Yf = Uf * Kf[None, :, :, :]
        y = (lambda x, s, axes=(-2, -1): torch.fft.irfft2(x, s=s, dim=axes).to(torch.bfloat16))(Yf, (1, L2), axes=(-2, -1))[:, :, 0, :L]
        y = (lambda x, *d: x.permute(*d))(y, 0, 2, 1) + u * Dskip[None, None, :]
        return y
    a = _A_ref(A_x, A_mem0, A_Wq, A_Wk, A_Wv, A_Wo, A_seg_len)
    b = _B_ref(B_u, B_A_re, B_A_im, B_B_re, B_B_im, B_C_re, B_C_im, B_log_dt, B_Dskip)
    return torch.concatenate([a, b], axis=-1)
```

```cpp
#include <hip/hip_runtime.h>
#include <hip/hip_bf16.h>
#include <stdint.h>

#define S_LEN 2048
#define DM 1024
#define NH 32
#define DH 128
#define HD 4096
#define MEM 8
#define SEG 32
#define NSEG 64
#define LTOK 40
#define SSM_L 2048
#define SSM_D 1024
#define SSM_N 128
#define OUTLD 2048

typedef unsigned short u16;
typedef unsigned int u32;
typedef unsigned long long u64;
typedef __attribute__((ext_vector_type(8))) short bf16x8;
typedef __attribute__((ext_vector_type(4))) float f32x4;
typedef __attribute__((ext_vector_type(4))) unsigned int u32x4;
typedef __attribute__((ext_vector_type(2))) unsigned int u32x2;
typedef __bf16 bf16x2_t __attribute__((ext_vector_type(2)));
typedef short v4i16_t __attribute__((ext_vector_type(4)));

#define SENT 0xFFFFFFFFu
#define RLX_AGENT __ATOMIC_RELAXED, __HIP_MEMORY_SCOPE_AGENT

__device__ __forceinline__ float bf2f(u16 v) { return __uint_as_float(((u32)v) << 16); }
__device__ __forceinline__ u16 f2bf(float f) {
  u32 u = __float_as_uint(f);
  u32 r = (u + 0x7fffu + ((u >> 16) & 1u)) >> 16;
  if ((u & 0x7fffffffu) > 0x7f800000u) r = 0x7fc0u;
  return (u16)r;
}
__device__ __forceinline__ float rbf(float f) { return bf2f(f2bf(f)); }

#define TR 8
#define TP 188
#define TDB 65
#define TDC 115
#define NR1 4
#define XPAGE (2 << 20)
#define XRING 3
#define XLP(p, L) ((((L) >> 13) << 15) + ((p) << 14) + ((L) & 8191))
#define XO_R3 131072
#define XR1A(x, L) XLP((x) & 1, ((x) >> 1) * 16384 + (L))
#define XR3A(rg, L) (XO_R3 + XLP(rg, L))
#define XSB(s) (((s) % XRING) == 2 ? 1048576 : ((s) % XRING) * 262144)
#define XR2B 524288
#define XR2S 1048576
#define XR2(j) (((((j) & 31) + (((j) >> 6) << 5)) / 6 << 15) + (((((j) >> 5) & 1) ^ 1) << 14) + ((((j) & 31) + (((j) >> 6) << 5)) % 6) * 1280)
#define XSYN (1048576 + 262144)
#define PGO_R (XSYN + 32768)
#define XCAL (PGO_R + 32768)
static_assert(XCAL + 16384 <= XPAGE, "exchange page");
#define WS_X 0ull
#define WS_X_SZ ((u64)2 * XPAGE)
#define WS_F (WS_X + WS_X_SZ)
#define XEPO (fb)
#define XSTA (fb + 128)
#define PGO_X (fb + 256)
#define PGO_Y (PGO_X + 96 * 128)
#define PGO_S (PGO_Y + 128 * 128)
#define PGO_D (PGO_S + 128 * 128)
#define PGO_A (PGO_D + 128 * 128)
#define XTKT (PGO_A + 192 * 128)
#define XUDN (XTKT + 128)
#define XYDN (XUDN + 128)
#define XTK2 (XYDN + 128)
#define WS_F_SZ ((u64)(2 + 96 + 128 + 128 + 128 + 192 + 8) * 128)
#define WS_XCH_END (WS_F + 131072)
#define WS_QX (WS_XCH_END)
#define WS_QKV_SZ ((u64)S_LEN * HD * 2)
#define WS_KX (WS_QX + WS_QKV_SZ)
#define WS_VX (WS_KX + WS_QKV_SZ)
#define WS_OX (WS_VX + WS_QKV_SZ)
#define WS_KM (WS_OX + WS_QKV_SZ)
#define WS_KM_SZ ((u64)NSEG * 8 * HD * 2)
#define WS_VM (WS_KM + WS_KM_SZ)
#define WS_UT (WS_VM + WS_KM_SZ)
#define WS_YT (WS_UT + (u64)SSM_L * SSM_D * 2)
#define WS_KD (WS_YT + (u64)SSM_L * SSM_D * 2)
#define WS_TOTAL (WS_KD + (u64)SSM_D * 3072)

#define SPIN_CAP 400000
#define TRY_CAP 20000
#define LAS __attribute__((address_space(3)))
#define LBAR() asm volatile("s_waitcnt lgkmcnt(0)\n\ts_barrier" ::: "memory")
__device__ __forceinline__ u32 max4u(u32x4 v) { return max(max(v[0], v[1]), max(v[2], v[3])); }
typedef float f32x2_t __attribute__((ext_vector_type(2)));
__device__ __forceinline__ u32 pack2(float a, float b) {
  f32x2_t v = {a, b};
  bf16x2_t r = __builtin_convertvector(v, bf16x2_t);
  return __builtin_bit_cast(u32, r);
}
__device__ __forceinline__ float rbf_hw(float a) { return __uint_as_float(pack2(a, a) & 0xffff0000u); }
__device__ __forceinline__ u32 nosent(u32 v) { return min(v, 0xFFFFFFFEu); }
template <int N> __device__ __forceinline__ void waitvm() { asm volatile("s_waitcnt vmcnt(%0)" ::"n"(N) : "memory"); }
typedef __attribute__((ext_vector_type(16))) float f32x16;
__device__ __forceinline__ u32 split_hl(float kv) {
  const u32 h = pack2(kv, kv) & 0xffffu;
  const float r1 = kv - __uint_as_float(h << 16);
  return h | (pack2(r1, r1) & 0xffff0000u);
}
__device__ __forceinline__ int kidx(int idx) {
  const int sh = idx < 256 ? 0 : (idx < 512 ? 1 : (idx < 1024 ? 2 : 3));
  const int q = sh ? ((idx + (1 << (sh - 1)) - 1 + ((idx >> sh) & 1)) >> sh) : idx;
  return q + 128 * sh;
}

#define FO_Q 0
#define FO_K ((int)WS_QKV_SZ)
#define FO_V ((int)(2 * WS_QKV_SZ))
#define FO_O ((int)(3 * WS_QKV_SZ))
#define FO_KM ((int)(4 * WS_QKV_SZ))
#define FO_VM ((int)(4 * WS_QKV_SZ + WS_KM_SZ))
__device__ __forceinline__ u32x4 wait_start(__amdgpu_buffer_rsrc_t rsx, int fb, u32 tag, bool slow) {
  u32x4 st;
  for (int it = 0; it < SPIN_CAP; ++it) {
    asm volatile("" ::: "memory");
    st = __builtin_amdgcn_raw_buffer_load_b128(rsx, 0, XSTA, 16);
    if ((u32)__builtin_amdgcn_readfirstlane(st[3]) == tag) break;
    if (slow) asm volatile("s_sleep 20" ::: "memory"); else asm volatile("s_sleep 3" ::: "memory");
  }
  asm volatile("" ::: "memory");
  return st;
}
__device__ __forceinline__ int xprog(u32 v, u32 tag) { return v - tag <= 80u ? (int)(v - tag) : -1; }
__device__ __forceinline__ float xmax4(float m) {
  { auto r = __builtin_amdgcn_permlane16_swap(__float_as_uint(m), __float_as_uint(m), false, false); m = __builtin_fmaxf(__uint_as_float(r[0]), __uint_as_float(r[1])); }
  { auto r = __builtin_amdgcn_permlane32_swap(__float_as_uint(m), __float_as_uint(m), false, false); m = __builtin_fmaxf(__uint_as_float(r[0]), __uint_as_float(r[1])); }
  return m;
}
__device__ __forceinline__ float xsum4(float m) {
  { auto r = __builtin_amdgcn_permlane16_swap(__float_as_uint(m), __float_as_uint(m), false, false); m = __uint_as_float(r[0]) + __uint_as_float(r[1]); }
  { auto r = __builtin_amdgcn_permlane32_swap(__float_as_uint(m), __float_as_uint(m), false, false); m = __uint_as_float(r[0]) + __uint_as_float(r[1]); }
  return m;
}
#define MF16(a, b, c) __builtin_amdgcn_mfma_f32_16x16x32_bf16(__builtin_bit_cast(bf16x8, a), __builtin_bit_cast(bf16x8, b), c, 0, 0, 0)
__device__ __forceinline__ f32x4 rnd2(f32x4 v, float sc) {
  const u32 a = pack2(v[0], v[1]), b = pack2(v[2], v[3]);
  const u32 c = pack2(__uint_as_float(a << 16) * sc, __uint_as_float(a & 0xffff0000u) * sc), d = pack2(__uint_as_float(b << 16) * sc, __uint_as_float(b & 0xffff0000u) * sc);
  return (f32x4){__uint_as_float(c << 16), __uint_as_float(c & 0xffff0000u), __uint_as_float(d << 16), __uint_as_float(d & 0xffff0000u)};
}
struct XaOps { u32x4 q[2][4], k[2][4], v[8]; };
__device__ __forceinline__ void xa_load(XaOps& o, __amdgpu_buffer_rsrc_t rsf, int sh, int lane) {
#pragma unroll
  for (int i = 0; i < 8; ++i) {
    o.q[i >> 2][i & 3] = __builtin_amdgcn_raw_buffer_load_b128(rsf, lane * 16, FO_Q + (sh * 8 + i) * 1024, 16);
    o.k[i >> 2][i & 3] = __builtin_amdgcn_raw_buffer_load_b128(rsf, lane * 16, FO_K + (sh * 8 + i) * 1024, 16);
  }
#pragma unroll
  for (int dt = 0; dt < 8; ++dt) o.v[dt] = __builtin_amdgcn_raw_buffer_load_b128(rsf, lane * 16, FO_V + (sh * 8 + dt) * 1024, 16);
}
__device__ __forceinline__ void xa_compute(const XaOps& o, const u32x4 (&km)[4], const u32x2 (&vm)[8], __amdgpu_buffer_rsrc_t rsf, int sh, int lane) {
  const int lr = lane & 15, lg = lane >> 4;
  const float scale = 0.08838834764831845f, L2E = 1.4426950408889634f, NEG = -1000000000.0f;
  const f32x4 z = (f32x4){0, 0, 0, 0};
  f32x4 s00 = z, s0m = z, s10 = z, s11 = z, s1m = z;
#pragma unroll
  for (int ks = 0; ks < 4; ++ks) { s00 = MF16(o.k[0][ks], o.q[0][ks], s00); s10 = MF16(o.k[0][ks], o.q[1][ks], s10); s11 = MF16(o.k[1][ks], o.q[1][ks], s11); }
#pragma unroll
  for (int ks = 0; ks < 4; ++ks) { s0m = MF16(km[ks], o.q[0][ks], s0m); s1m = MF16(km[ks], o.q[1][ks], s1m); }
  s00 = rnd2(s00, scale); s10 = rnd2(s10, scale); s11 = rnd2(s11, scale); s0m = rnd2(s0m, scale); s1m = rnd2(s1m, scale);
  asm volatile("" : "+v"(s00), "+v"(s11), "+v"(s0m), "+v"(s1m));
#pragma unroll
  for (int e = 0; e < 4; ++e) {
    const bool msk = 4 * lg + e > lr;
    s00[e] = msk ? NEG : s00[e]; s11[e] = msk ? NEG : s11[e];
    s0m[e] = lg >= 2 ? -INFINITY : s0m[e]; s1m[e] = lg >= 2 ? -INFINITY : s1m[e];
  }
  float m0 = NEG, m1 = -INFINITY;
#pragma unroll
  for (int e = 0; e < 4; ++e) { m0 = __builtin_fmaxf(m0, __builtin_fmaxf(s00[e], s0m[e])); m1 = __builtin_fmaxf(__builtin_fmaxf(m1, s10[e]), __builtin_fmaxf(s11[e], s1m[e])); }
  m0 = xmax4(m0); m1 = xmax4(m1);
  float t0 = 0.f, t1 = 0.f;
#pragma unroll
  for (int e = 0; e < 4; ++e) {
    s00[e] = __builtin_amdgcn_exp2f((s00[e] - m0) * L2E); s0m[e] = __builtin_amdgcn_exp2f((s0m[e] - m0) * L2E);
    s10[e] = __builtin_amdgcn_exp2f((s10[e] - m1) * L2E); s11[e] = __builtin_amdgcn_exp2f((s11[e] - m1) * L2E); s1m[e] = __builtin_amdgcn_exp2f((s1m[e] - m1) * L2E);
    t0 += s00[e] + s0m[e]; t1 += (s10[e] + s11[e]) + s1m[e];
  }
  const float em = __builtin_amdgcn_exp2f((NEG - m0) * L2E);
  t0 += 4.f * em;
  const float r0 = __builtin_amdgcn_rcpf(xsum4(t0)), r1 = __builtin_amdgcn_rcpf(xsum4(t1));
  s00 *= r0; s0m *= r0; s10 *= r1; s11 *= r1; s1m *= r1;
  const u32x4 p0 = (u32x4){pack2(s00[0], s00[1]), pack2(s00[2], s00[3]), pack2(s0m[0], s0m[1]), pack2(s0m[2], s0m[3])};
  const u32x4 p1 = (u32x4){pack2(s10[0], s10[1]), pack2(s10[2], s10[3]), pack2(s11[0], s11[1]), pack2(s11[2], s11[3])};
  const u32x4 pm = (u32x4){0u, 0u, pack2(s1m[0], s1m[1]), pack2(s1m[2], s1m[3])};
  const u32 pe = pack2(em * r0, em * r0);
  const u32x4 px = (u32x4){0u, 0u, pe, pe};
#pragma unroll
  for (int pr = 0; pr < 4; ++pr) {
    f32x4 o0[2], o1[2];
#pragma unroll
    for (int t = 0; t < 2; ++t) {
      const int dt = 2 * pr + t;
      const u32x4 a0 = (u32x4){o.v[dt][0], o.v[dt][1], vm[dt][0], vm[dt][1]};
      o0[t] = MF16(a0, p0, z);
      o1[t] = MF16(o.v[dt], p1, z);
      o0[t] = MF16(o.v[dt], px, o0[t]);
      o1[t] = MF16(a0, pm, o1[t]);
    }
    const int ob = FO_O + ((sh >> 5) * 256 + (sh & 31) * 4 + pr) * 1024;
    __builtin_amdgcn_raw_buffer_store_b128((u32x4){pack2(o0[0][0], o0[0][1]), pack2(o0[0][2], o0[0][3]), pack2(o0[1][0], o0[1][1]), pack2(o0[1][2], o0[1][3])}, rsf, lane * 16, ob, 16);
    __builtin_amdgcn_raw_buffer_store_b128((u32x4){pack2(o1[0][0], o1[0][1]), pack2(o1[0][2], o1[0][3]), pack2(o1[1][0], o1[1][1]), pack2(o1[1][2], o1[1][3])}, rsf, lane * 16, ob + 131072, 16);
  }
}
#define NAW 64
__device__ __forceinline__ void a_role(__amdgpu_buffer_rsrc_t rsx, __amdgpu_buffer_rsrc_t rsf, int sh, int lim, int wi, int done, int lane, int fb, u32 tag) {
  const int lr = lane & 15, lg = lane >> 4;
  const int okm = (lg * 8 + (lr & 7)) * 16, ovm = lg < 2 ? (lg * 16 + lr) * 8 : 0x7ffffff0;
  int rp = 0, xp = -1, spin = 0, pub = done, miss = 0, tp = 200;
  u32 tseen = 0, tq0 = 0, tq1 = 0, tq2 = 0, tq3 = 0; (void)tq0; (void)tq1; (void)tq2; (void)tq3;
#define XA_WAITX(s_)                                                                  \
  while (xp <= ((s_) >> 5)) {                                                                                          \
    asm volatile("" ::: "memory");                                                                                     \
    int pv = min(xprog(__builtin_amdgcn_raw_buffer_load_b32(rsx, lane * 128, PGO_X, 16), tag), xprog(__builtin_amdgcn_raw_buffer_load_b32(rsx, (64 + (lane & 31)) * 128, PGO_X, 16), tag)); \
    _Pragma("unroll") for (int o_ = 1; o_ < 64; o_ *= 2) pv = min(pv, __shfl_xor(pv, o_));                             \
    xp = __builtin_amdgcn_readfirstlane(pv);                                                                           \
    if (xp > ((s_) >> 5) || ++spin > SPIN_CAP) break;                                                                  \
    asm volatile("s_sleep 16" ::: "memory");                                                                           \
  }
#define XA_PUB() if (pub != done) { pub = done; asm volatile("s_nop 4\n\tbuffer_store_dword %0, %1, %2, %3 offen sc1" :: "v"(done), "v"(lane ? 0x7ffffff0 : 0), "s"(rsx), "s"(PGO_A + wi * 128) : "memory"); }
#define XA_STEP(CUR, NXT)                                                                                              \
  {                                                                                                                    \
    const int seg = sh >> 5, nx = sh + NAW;                                                                            \
    XA_WAITX(min(nx, lim - 1))                                                                                         \
    if (rp <= seg) {                         \
      while (true) {                                                                                                   \
        int want = min(seg + 1, NSEG - 1);                                                                             \
        if (tseen) {                                                                                                   \
          const u32 due = tseen + (u32)(want - rp) * tp - (want + 1 < NSEG ? 0 : XA_LAST) + (seg + 1 < NSEG ? 0 : XA_END); \
          if ((int)(due - (u32)__builtin_amdgcn_s_memrealtime()) > 0 || miss) { asm volatile("s_waitcnt vmcnt(0)" ::: "memory"); XA_PUB() } \
          for (int i_ = 0; i_ < 4000 && (int)(due - (u32)__builtin_amdgcn_s_memrealtime()) > 0; ++i_) asm volatile("s_sleep 2" ::: "memory"); \
          if (!miss) want = min(max(want, rp + max((int)((u32)__builtin_amdgcn_s_memrealtime() - tseen), 0) / tp), NSEG - 1); \
        }                                                                                                              \
        XA_DBG4                                                                                                        \
        asm volatile("" ::: "memory");                                                                                 \
        if (seg + 1 < NSEG) {                                                                                          \
          const int r_ = __builtin_amdgcn_readfirstlane(__builtin_amdgcn_raw_buffer_load_b32(rsx, 0, pgr + want * 128, 16)); \
          if (r_ >= 0) { const u32 n_ = (u32)__builtin_amdgcn_s_memrealtime(); tseen = n_ - ((n_ - (r_ >> 16)) & 0x7fffu) + XA_LAG; tp = max(r_ & 1023, 1); rp = want; miss = 0; break; } \
        } else if (__ballot(__builtin_amdgcn_raw_buffer_load_b32(rsx, ((sh & 31) * 4 + (lane & 3)) * 128, PGO_D, 16) == SENT) == 0) break; \
        if (++spin > SPIN_CAP) break;                                                                                  \
        if (miss++ && seg + 2 < NSEG) asm volatile("s_sleep 20" ::: "memory");                                                         \
      }                                                                                                                \
    }                                                                                                                  \
    asm volatile("" ::: "memory");                                                                                     \
    XA_DBG2                                                                                                            \
    u32x4 km[4]; u32x2 vm[8];                                                                                          \
    _Pragma("unroll") for (int i = 0; i < 4; ++i) km[i] = __builtin_amdgcn_raw_buffer_load_b128(rsf, okm, FO_KM + (sh * 4 + i) * 512, 16); \
    _Pragma("unroll") for (int i = 0; i < 8; ++i) vm[i] = __builtin_amdgcn_raw_buffer_load_b64(rsf, ovm, FO_VM + (sh * 8 + i) * 256, 16);  \
                              \
    const int pk_ = min(rp + max((int)((u32)__builtin_amdgcn_s_memrealtime() - tseen), 0) / tp, NSEG - 1);              \
    const u32 pv_ = __builtin_amdgcn_raw_buffer_load_b32(rsx, 0, pgr + pk_ * 128, 16);                                 \
    asm volatile("" ::: "memory");                                                                                     \
    __builtin_amdgcn_s_waitcnt(0x0F70);                                                                   \
    asm volatile("" ::: "memory");                                                                                     \
    { const int r_ = __builtin_amdgcn_readfirstlane(pv_);                                                              \
      if (r_ >= 0) { const u32 n_ = (u32)__builtin_amdgcn_s_memrealtime(); tseen = n_ - ((n_ - (r_ >> 16)) & 0x7fffu) + XA_LAG; tp = max(r_ & 1023, 1); rp = pk_; } } \
    XA_PUB()                                   \
    if (nx < lim) xa_load(NXT, rsf, nx, lane);                                                                         \
    XA_DBG3                                                                                                            \
    __builtin_amdgcn_sched_barrier(0);                                                                                 \
    xa_compute(CUR, km, vm, rsf, sh, lane);                                                                            \
    ++done;                                                                                                            \
    XA_DBG                                                                                                             \
    sh = nx;                                                                                                           \
    if (sh >= lim) break;                                                                                              \
  }
#define XA_LAST 24
#define XA_END 0
#define XA_LAG 30
#define XA_DBG
#define XA_DBG2
#define XA_DBG3
#define XA_DBG4
  const int pgr = PGO_R + (__builtin_amdgcn_readfirstlane(wait_start(rsx, fb, tag, true)[2]) & 8192);
  XaOps A, B;
  if (sh >= NAW) {
    const int r_ = __builtin_amdgcn_readfirstlane(__builtin_amdgcn_raw_buffer_load_b32(rsx, 0, pgr + 40 * 128, 16));
    if (r_ >= 0) { const u32 n_ = (u32)__builtin_amdgcn_s_memrealtime(); tseen = n_ - ((n_ - (r_ >> 16)) & 0x7fffu) + XA_LAG; tp = max(r_ & 1023, 1); rp = 40; }
  }
  XA_WAITX(sh)
  asm volatile("" ::: "memory");
  xa_load(A, rsf, sh, lane);
  while (true) { XA_STEP(A, B) XA_STEP(B, A) }
  asm volatile("s_waitcnt vmcnt(0)" ::: "memory");
  XA_PUB()
}

typedef __attribute__((ext_vector_type(32))) unsigned int u32x32;
typedef __attribute__((ext_vector_type(16))) unsigned int u32x16;
typedef __attribute__((ext_vector_type(8))) unsigned int u32x8;
#define STR2(x) #x
#define STR(x) STR2(x)
#define RNG(f, b, n) f "[" STR(b) ":" STR((b) + (n)-1) "]"
#define REG(b) "v[" STR(b) "]"
#define LR_RED 16384
#define LR_FRG 24576
#define LR_CTL LR_RED
#define LR_STG 32768
#define L_RSTAMP LR_STG
#define STAMPV(i, v)
#define EXF (NH * 8 * 1024)
#define EXK 65536
#define OOR 0x7ffffff0

__device__ __forceinline__ u32 lds_word(int addr) {
  u32 v;
  asm volatile("ds_read_b32 %0, %1\n\ts_waitcnt lgkmcnt(0)" : "=v"(v) : "v"(addr) : "memory");
  return __builtin_amdgcn_readfirstlane(v);
}
__device__ __forceinline__ int fresh_lane() {
  int l;
  asm volatile("v_mbcnt_lo_u32_b32 %0, -1, 0\n\tv_mbcnt_hi_u32_b32 %0, -1, %0" : "=v"(l));
  return l;
}
__device__ __forceinline__ u32 rt32() { return (u32)__builtin_amdgcn_s_memrealtime(); }
#define SYO(s) (XSYN + flip + (s) * 64)
#define TKS 0.85f
#define TLB 1
#define TLC 1
#define TLP 2
#define KG0 0
#define CLS0 0u
#define APC 5
#define ADL 12
#define CALN 2
#define TE1 4
#define TE2 3
#define TE3 2
#define TE0 60
#define TD0 100
#define TD 120
#define PHASE 1
#define XSK 0
#define CLKS(R, X) WLG(2) "s_sub_u32 s49, s50, " R "\n\t" "s_cmp_le_i32 s49, " STR(3 * PHASE) "\n\t" "s_cbranch_scc1 " X "\n\t"
#define CLKW(L, OFF) WLG(0) "s_memrealtime s[72:73]\n\t" "s_add_u32 s50, s43, s44\n\t" "s_add_u32 s50, s50, " OFF "\n\t" "s_nop 1\n\t" "s_memrealtime s[74:75]\n\t" "s_nop 3\n" \
                     L ":\n\t" "s_memrealtime s[76:77]\n\t" CLKS("s72", L "x") "s_memrealtime s[72:73]\n\t" CLKS("s74", L "x")     \
                     "s_memrealtime s[74:75]\n\t" CLKS("s76", L "x") "s_branch " L "\n" \
                     L "x:\n\t" CLKP(L)
#define CLKP(L) "s_cmp_lt_i32 s49, 1\n\t" "s_cbranch_scc1 " L "y\n\t" "s_nop " STR(PHN) "\n\t" "s_cmp_lt_i32 s49, 2\n\t" "s_cbranch_scc1 " L "y\n\t" "s_nop " STR(PHN) "\n\t" \
                "s_cmp_lt_i32 s49, 3\n\t" "s_cbranch_scc1 " L "y\n\t" "s_nop " STR(PHN) "\n\t" "s_nop 0\n\t" "s_branch " L "y\n\t" "s_nop 0\n" L "y:\n\t"
#define PHN 3
#define CLKL(L, OFF) L ":\n\t" "s_memrealtime vcc\n\t" "ds_read_b32 v62, v38\n\t" WLG(0) "v_readfirstlane_b32 s49, v62\n\t" "s_max_u32 s44, s44, s49\n\t" \
                     "s_add_u32 s50, s43, s44\n\t" "s_add_u32 s50, s50, " OFF "\n\t" "s_sub_u32 s49, s50, vcc_lo\n\t" "s_cmp_le_i32 s49, 16\n\t" "s_cbranch_scc1 " L "z\n\t" \
                     CLKSL "s_branch " L "\n" L "z:\n\t"
#define CLKSL "s_sleep 1\n\t"
#define TS_(n) "s_memtime s[80+2*" #n ":81+2*" #n "]\n\t"
#define TSA(n)
#define TSB(n)
#define TSW(n)
#define TVM 16
template <int NTL, int KS>
__device__ __forceinline__ void tr_chunk(const u16* S, int lr, int lg, u32 (&r)[NTL][KS][8]) {
  const int q = lr >> 2, p = lr & 3;
#pragma unroll
  for (int t = 0; t < NTL; ++t)
#pragma unroll
    for (int i = 0; i < KS; ++i)
#pragma unroll
      for (int kb = 0; kb < 2; ++kb) {
        const u16* base = S + (64 * i + 32 * kb + 16 * (lg >> 1) + 4 * (lg & 1) + (q & 1) + 8 * (q >> 1)) * (16 * NTL) + 16 * t + 4 * p;
        const u32x2 t0 = __builtin_bit_cast(u32x2, __builtin_amdgcn_ds_read_tr16_b64_v4i16((LAS v4i16_t*)(base)));
        const u32x2 t1 = __builtin_bit_cast(u32x2, __builtin_amdgcn_ds_read_tr16_b64_v4i16((LAS v4i16_t*)(base + 2 * 16 * NTL)));
        r[t][i][4 * kb + 0] = t0[0]; r[t][i][4 * kb + 1] = t0[1]; r[t][i][4 * kb + 2] = t1[0]; r[t][i][4 * kb + 3] = t1[1];
      }
}
#define SMF(acc, a, bf, bi) "v_smfmac_f32_16x16x64_bf16 " RNG("v", acc, 4) ", " RNG("v", a, 4) ", " RNG(bf, bi, 8) ", v34\n\t"
#define ZER(r) "v_mov_b64 " RNG("v", r, 2) ", 0\n\t" "v_mov_b64 " RNG("v", (r) + 2, 2) ", 0\n\t"
#define GLD(r, o) "s_xor_b32 s49, s50, " STR(o) "\n\t" "buffer_load_dwordx4 " RNG("v", r, 4) ", v32, %[rs], s49 offen sc1\n\t"
#define GL1(r, o) "s_xor_b32 s49, s50, " STR(o) "\n\t" "buffer_load_dwordx4 " RNG("v", r, 4) ", v33, %[rs], s49 offen sc1\n\t"
#define GL2(r, o) GL1(r, 4096 + (o))
#define MXA0(r) "v_max3_u32 v63, " REG(r) ", " REG((r) + 1) ", " REG((r) + 1) "\n\t"
#define MXA(r) "v_max3_u32 v63, v63, " REG(r) ", " REG((r) + 1) "\n\t"
#define MXB(r) "v_max3_u32 v63, v63, " REG((r) + 2) ", " REG((r) + 3) "\n\t"
#define CMPB "v_cmp_eq_u32 vcc, s54, v63\n\t"
#define PSA(r) "v_pk_add_f32 " RNG("v", r, 2) ", " RNG("v", r, 2) ", " RNG("v", (r) + 2, 2) "\n\t"
#define PSW(r, o) "ds_write_addtid_b32 " REG(r) " offset:" STR(o) "\n\t"
#define RDQ(r, o) "ds_read_b128 " RNG("v", r, 4) ", v37 offset:" STR(o) "\n\t"
#define SQ(t, i) SMF(64 + 4 * (t), 96 + 4 * (i), "a", ((i) * 4 + (t)) * 8)
#define SV(t, i) SMF(24 + 4 * (t), 96 + 4 * (i), "a", 128 + ((i) * 2 + (t)) * 8)
#define SW(q, i) SMF(64 + 4 * (q), 96 + 4 * (i), "v", 128 + ((i) * 2 + (q)) * 8)
#define WVM(n) "s_waitcnt vmcnt(" STR(n) ")\n\t"
#define WLG(n) "s_waitcnt lgkmcnt(" STR(n) ")\n\t"
#define WLGF(n) WLG(n)
#define PACE 7
#define PC "s_nop " STR(PACE) "\n\t"
#define QBLK(i, M0) WVM(5 - (i)) SQ(0, i) M0(96 + 4 * (i)) SQ(1, i) MXB(96 + 4 * (i)) SQ(2, i) SQ(3, i)
#define WBLK(i, M0) WVM(8 - (i)) SW(0, i) M0(96 + 4 * (i)) SW(1, i) MXB(96 + 4 * (i))
#define CL8(a, b, c, d, e, f, g, h) "v" #a, "v" #b, "v" #c, "v" #d, "v" #e, "v" #f, "v" #g, "v" #h
#define CL64 CL8(64, 65, 66, 67, 68, 69, 70, 71)
#define CL72 CL8(72, 73, 74, 75, 76, 77, 78, 79)
#define CL80 CL8(80, 81, 82, 83, 84, 85, 86, 87)
#define CL88 CL8(88, 89, 90, 91, 92, 93, 94, 95)
#define CL96 CL8(96, 97, 98, 99, 100, 101, 102, 103), CL8(104, 105, 106, 107, 108, 109, 110, 111)
#define CL112 CL8(112, 113, 114, 115, 116, 117, 118, 119)
#define CL120 CL8(120, 121, 122, 123, 124, 125, 126, 127)
#define PKA(d, a, b) "v_pk_add_f32 " RNG("v", d, 2) ", " RNG("v", a, 2) ", " RNG("v", b, 2) "\n\t"
#define ARRIVE(o) "s_mov_b64 exec, 1\n\t" "ds_add_rtn_u32 v62, v38, v39 offset:" STR(o) "\n\t" "s_mov_b64 exec, -1\n\t"

#define EXTRA "v_readlane_b32 s69, v35, s45\n\t" "s_add_u32 s66, s56, s69\n\t" "s_add_u32 s67, s57, s69\n\t" "s_add_u32 s67, s67, s69\n\t" "s_add_u32 s68, s66, " STR(TVM) "\n\t"
#define TEX(s) ((s) == 0 ? TE0 : ((s) < 4 ? TE1 : ((s) < 12 ? TE2 : ((s) < 36 ? TE3 : 0))))
#define R2BASE "s_bitcmp1_b32 s45, 0\n\t" "s_cselect_b32 s71, " STR(XR2S) ", 0\n\t" "s_add_u32 s71, s71, %[r2]\n\t"
#define X4B 0
#define X4C 0
#define STB "buffer_store_dwordx2 v[116:117], v11, %[rs], s36 offen sc1\n"
#define STC "buffer_store_dwordx2 v[80:81], v17, %[rs], s36 offen sc1\n\t"
#define R2TEXT                                                                                                     \
              TSB(0)                                                                                               \
              "buffer_load_dwordx4 v[64:67], v43, %[rs], s71 offen sc1\n\t"                                      \
              "buffer_load_dwordx4 v[68:71], v8, %[rs], s71 offen sc1\n\t"                                       \
              "buffer_load_dwordx4 v[76:79], v9, %[rs], s71 offen sc1\n\t"                                       \
              "buffer_load_dwordx4 v[60:63], v10, %[rs], s71 offen sc1\n\t"                                      \
              "buffer_store_dwordx4 v[72:75], v47, %[rf], s41 offen sc1\n"                                       \
              ".Lvw%=:\n\t"                                                                                        \
              "ds_read_b32 v59, v38 offset:12\n\t"                                                                 \
              "ds_read2_b32 v[112:113], v12 offset0:0 offset1:4\n\t" "ds_read2_b32 v[114:115], v12 offset0:32 offset1:36\n\t" \
              "ds_read2_b32 v[116:117], v13 offset0:0 offset1:4\n\t" "ds_read2_b32 v[118:119], v13 offset0:32 offset1:36\n\t" \
              "ds_read2_b32 v[120:121], v14 offset0:0 offset1:4\n\t" "ds_read2_b32 v[122:123], v14 offset0:32 offset1:36\n\t" \
              "ds_read2_b32 v[124:125], v15 offset0:0 offset1:4\n\t" "ds_read2_b32 v[126:127], v15 offset0:32 offset1:36\n\t" \
              "ds_read_b64 v[96:97], v18 offset:2048\n\t" "ds_read_b64 v[100:101], v18 offset:2056\n\t"      \
              "ds_read_b64 v[104:105], v18 offset:3072\n\t" "ds_read_b64 v[108:109], v18 offset:3080\n\t"    \
              WLG(0)                                                                                               \
              "v_readfirstlane_b32 s49, v59\n\t"                                                                 \
              "s_sub_u32 s49, s49, 4\n\t"                                                                      \
              "s_cmp_lt_i32 s49, s38\n\t"                                                                      \
              "s_cbranch_scc1 .Lvw%=\n\t"                                                                          \
              PKA(112, 112, 116) PKA(114, 114, 118) PKA(120, 120, 124) PKA(122, 122, 126) PKA(112, 112, 120) PKA(114, 114, 122) \
              "v_mov_b32 v59, 0\n\t" "v_mov_b32 v99, 0\n\t" "v_mov_b32 v103, 0\n\t" "v_mov_b32 v107, 0\n\t" "v_mov_b32 v111, 0\n\t" \
              "v_cvt_pk_bf16_f32 v98, v112, v59\n\t" "v_cvt_pk_bf16_f32 v106, v113, v59\n\t" "v_cvt_pk_bf16_f32 v102, v114, v59\n\t" "v_cvt_pk_bf16_f32 v110, v115, v59\n\t" \
              "v_mov_b64 v[112:113], v[80:81]\n\t" "v_mov_b64 v[114:115], v[82:83]\n\t"                            \
              "v_mov_b32_dpp v112, v84 row_ror:8 row_mask:0xf bank_mask:0xc\n\t" "v_mov_b32_dpp v113, v85 row_ror:8 row_mask:0xf bank_mask:0xc\n\t" \
              "v_mov_b32_dpp v114, v86 row_ror:8 row_mask:0xf bank_mask:0xc\n\t" "v_mov_b32_dpp v115, v87 row_ror:8 row_mask:0xf bank_mask:0xc\n\t" \
              "v_mov_b32 v52, 0x3f803f80\n\t" "v_mov_b32 v53, 0x3f803f80\n\t" "v_mov_b32 v54, 0x3f803f80\n\t" "v_mov_b32 v55, 0x3f803f80\n\t" \
              TSB(1) "s_waitcnt vmcnt(4)\n\t" TSB(2) \
              "v_mov_b64 v[88:89], v[64:65]\n\t" "v_mov_b64 v[90:91], v[66:67]\n\t" \
              "s_nop 1\n\t" \
              "v_permlane32_swap_b32 v88, v90\n\t" "v_permlane32_swap_b32 v89, v91\n\t" \
              "v_pk_add_f32 v[88:89], v[88:89], v[90:91]\n\t" \
              "s_nop 1\n\t" \
              "v_permlane16_swap_b32 v88, v89\n\t" \
              "v_add_f32 v92, v88, v89\n\t" \
              "s_waitcnt vmcnt(3)\n\t" \
              "v_pk_add_f32 v[88:89], v[112:113], v[68:69]\n\t" "v_pk_add_f32 v[90:91], v[114:115], v[70:71]\n\t" \
              "s_waitcnt vmcnt(2)\n\t" \
              "v_pk_add_f32 v[88:89], v[88:89], v[76:77]\n\t" "v_pk_add_f32 v[90:91], v[90:91], v[78:79]\n\t" \
              "s_waitcnt vmcnt(1)\n\t" TSB(3) \
              "v_pk_add_f32 v[88:89], v[88:89], v[60:61]\n\t" "v_pk_add_f32 v[90:91], v[90:91], v[62:63]\n\t" \
              "v_max3_f32 v93, v88, v89, v92\n\t" \
              "v_max3_f32 v93, v93, v90, v91\n\t" \
              "s_nop 1\n\t" \
              "v_max_f32_dpp v94, v93, v93 row_ror:8 row_mask:0xf bank_mask:0xf\n\t" \
              "v_max_f32_dpp v95, v93, v93 row_ror:8 row_mask:0xf bank_mask:0xf\n\t" \
              "s_nop 1\n\t" \
              "v_permlane16_swap_b32 v94, v95\n\t" \
              "v_max_f32 v93, v94, v95\n\t" "v_max_f32 v94, v94, v95\n\t" \
              "s_nop 1\n\t" \
              "v_permlane32_swap_b32 v93, v94\n\t" \
              "v_max_f32 v93, v93, v94\n\t" \
              "v_mul_f32 v93, 0xbe0293ee, v93\n\t" \
              "v_fmamk_f32 v88, v88, 0x3e0293ee, v93\n\t" "v_fmamk_f32 v89, v89, 0x3e0293ee, v93\n\t" \
              "v_exp_f32 v88, v88\n\t" "v_fmamk_f32 v90, v90, 0x3e0293ee, v93\n\t" \
              "v_exp_f32 v89, v89\n\t" "v_fmamk_f32 v91, v91, 0x3e0293ee, v93\n\t" \
              "v_exp_f32 v90, v90\n\t" "v_fmamk_f32 v92, v92, 0x3e0293ee, v93\n\t" \
              "v_exp_f32 v91, v91\n\t" "v_exp_f32 v92, v92\n\t" \
              "v_cvt_pk_bf16_f32 v56, v88, v89\n\t" "v_cvt_pk_bf16_f32 v57, v90, v91\n\t" "v_cvt_pk_bf16_f32 v58, v92, v59\n\t" \
              TSB(4) "s_nop 1\n\t" \
              "v_mfma_f32_16x16x32_bf16 v[88:91], v[52:55], v[56:59], 0\n\t" \
              "v_mfma_f32_16x16x32_bf16 v[116:119], v[96:99], v[56:59], 0\n\t" \
              "v_mfma_f32_16x16x32_bf16 v[120:123], v[100:103], v[56:59], 0\n\t" \
              "v_mfma_f32_16x16x32_bf16 v[124:127], v[104:107], v[56:59], 0\n\t" \
              "v_mfma_f32_16x16x32_bf16 v[52:55], v[108:111], v[56:59], 0\n\t" \
              TSB(5) "s_nop 3\n\t" \
              "v_add_f32_dpp v92, v88, v88 row_ror:8 row_mask:0xf bank_mask:0xf\n\t" \
              "v_rcp_f32 v92, v92\n\t" \
              "v_add_f32_dpp v116, v120, v116 row_ror:8 row_mask:0xf bank_mask:0x3\n\t" "v_add_f32_dpp v117, v121, v117 row_ror:8 row_mask:0xf bank_mask:0x3\n\t" \
              "v_add_f32_dpp v118, v122, v118 row_ror:8 row_mask:0xf bank_mask:0x3\n\t" "v_add_f32_dpp v119, v123, v119 row_ror:8 row_mask:0xf bank_mask:0x3\n\t" \
              "v_cmp_u_f32 vcc, v92, v92\n\t" \
              "v_add_f32_dpp v116, v124, v52 row_ror:8 row_mask:0xf bank_mask:0xc\n\t" "v_add_f32_dpp v117, v125, v53 row_ror:8 row_mask:0xf bank_mask:0xc\n\t" \
              "v_add_f32_dpp v118, v126, v54 row_ror:8 row_mask:0xf bank_mask:0xc\n\t" "v_add_f32_dpp v119, v127, v55 row_ror:8 row_mask:0xf bank_mask:0xc\n\t" \
              "v_pk_mul_f32 v[116:117], v[116:117], v[92:93] op_sel_hi:[1,0]\n\t" "v_pk_mul_f32 v[118:119], v[118:119], v[92:93] op_sel_hi:[1,0]\n\t" \
              "v_cvt_pk_bf16_f32 v116, v116, v117\n\t" "v_cvt_pk_bf16_f32 v117, v118, v119\n\t" \
              "v_min_u32 v116, -2, v116\n\t" "v_min_u32 v117, -2, v117\n\t" \
              "s_cbranch_vccnz .Lskip%=\n\t"                                                                       \
              STB                                     \
              ".Lskip%=:\n\t" TSB(6)
#define AV 0
#define VREST
#define PS1(r) "v_add_f32 " REG(r) ", " REG(r) ", " REG((r) + 2) "\n\t"
#define VPOST VREST PSA(24) PSW(24, 2560) PSW(25, 2832) PSA(28) PSW(28, 3200) PSW(29, 3472) ARRIVE(12)
#define R2OUT "=&{v[60:79]}"(pr), "=&{v[116:117]}"(ov)
#define FINEO
#define STEPCHK "2"
#define ROLESV
#define ROLEOUT

__device__ __forceinline__ void clk_edge(u32& cyc, u32& tick) {
  const u32 t = rt32();
  for (int i = 0; i < 64; ++i) { tick = rt32(); if (tick != t) break; }
  cyc = (u32)__builtin_amdgcn_s_memtime();
}
__device__ __forceinline__ float clk_rate(u32 cyc0, u32 tk0) { u32 c, t; clk_edge(c, t); return (float)(c - cyc0) / (float)max(t - tk0, 1u); }
__device__ __forceinline__ void dma16(__amdgpu_buffer_rsrc_t rs, int voff, int soff, int sstep, int ldsa) {
#pragma unroll
  for (int i = 0; i < 16; ++i)
    asm volatile("s_mov_b32 m0, %0\n\ts_nop 3\n\tbuffer_load_dwordx4 %1, %2, %3 offen lds" :: "s"(ldsa + i * 1024), "v"(voff), "s"(rs), "s"(soff ^ (i * sstep)) : "memory");
}

__device__ __forceinline__ void kgen_half(const f32x4* prm, f32x4 (&acc)[3], int lane, __amdgpu_buffer_rsrc_t rs, int voff, int soff, int sstep, int ldsa) {
  const int lr = lane & 15, lg = lane >> 4;
  const float lv[4] = {(float)(128 * (lr >> 3) + (lr & 7)), (float)(lr < 8 ? 256 + 128 * (lr >> 2) + 2 * (lr & 3) : 512 + 128 * ((lr - 8) >> 1) + 4 * (lr & 1)),
                       (float)(lr <= 8 ? 1024 + 128 * lr : 0), (float)(8 * lr)};
  prm += 4 * lg;
#pragma unroll 1
  for (int n = 0; n < 64; n += 16) {
    u32x4 hi[4], lo[4];
    f32x4 qq[4];
#pragma unroll
    for (int j = 0; j < 4; ++j) qq[j] = prm[n + j];
#pragma unroll
    for (int j = 0; j < 4; ++j) {
      const f32x4 q = qq[j];
      __builtin_amdgcn_sched_barrier(0);
      asm volatile("s_mov_b32 m0, %0\n\ts_nop 3\n\tbuffer_load_dwordx4 %1, %2, %3 offen lds" :: "s"(ldsa + ((n >> 2) + j) * 1024), "v"(voff), "s"(rs), "s"(soff ^ (((n >> 2) + j) * sstep)) : "memory");
      __builtin_amdgcn_sched_barrier(0);
#pragma unroll
      for (int t = 0; t < 4; ++t) {
        const float e = __builtin_amdgcn_exp2f(t < 3 ? __builtin_fmaf(q[0], lv[t], q[2]) : q[0] * lv[t]);
        const float fr = __builtin_amdgcn_fractf(t < 3 ? __builtin_fmaf(q[1], lv[t], q[3]) : q[1] * lv[t]);
        const float re = e * __builtin_amdgcn_cosf(fr), im = e * __builtin_amdgcn_sinf(t < 3 ? fr : -fr);
        const u32 hp = pack2(re, im);
        hi[t][j] = hp;
        lo[t][j] = pack2(re - __uint_as_float(hp << 16), im - __uint_as_float(hp & 0xffff0000u));
      }
    }
#pragma unroll
    for (int t = 0; t < 3; ++t) {
      acc[t] = MF16(lo[t], hi[3], acc[t]);
      acc[t] = MF16(hi[t], lo[3], acc[t]);
      acc[t] = MF16(hi[t], hi[3], acc[t]);
    }
  }
}
struct SsmPtrs { const u16 *A_re, *A_im, *B_re, *B_im, *C_re, *C_im, *log_dt; };
__device__ __forceinline__ void r_role(unsigned char* lds, int b, const u16* __restrict__ mem0, const u16* __restrict__ Wq, const u16* __restrict__ Wk, const u16* __restrict__ Wv,
                                       const u16* __restrict__ Wo, int fb, u32 tag, u64 pc0, int clen, __amdgpu_buffer_rsrc_t rsx, __amdgpu_buffer_rsrc_t rsf,
                                       __amdgpu_buffer_rsrc_t rst, SsmPtrs sp, int tid, int lane, int w
                                       ) {
  const int lr = lane & 15, lg = lane >> 4;
  const int ldsb = (int)(size_t)(LAS unsigned char*)lds;
  const int idx = (lr & 2) ? 0xEEEE : 0x4444;
  int tries = 0;
  u32 tcnt = 0;
  u32 cyc0 = 0, tk0 = 0;
  if (tid < 8) *(u32*)(lds + LR_CTL + tid * 4) = tid == 1 ? rt32() : 0u;
  const int xcd = b & 3, li = b >> 2;
  const int head = xcd * 8 + (li >> 2), ks = li & 3;
  const int vcu = head * 4 + ks;
  const int cg = (xcd >> 1) * 32 + li, rg = xcd & 1;
  const int rot1 = ((li >> 1) + w) & 3, rot3 = ((li >> 2) + 2 * w) & 7;
  const int pz1 = lane < 32 ? XR1A(lane >> 3, cg * 256 + rg * 128 + (lane & 7) * 16) : XR3A(lg & 1, vcu * 256 + lr * 16);
  u32x32 wa[6], wo[4];
  {
    const __amdgpu_buffer_rsrc_t rw[4] = {__builtin_amdgcn_make_buffer_rsrc((void*)Wq, 0, DM * HD * 2, 0x00020000), __builtin_amdgcn_make_buffer_rsrc((void*)Wk, 0, DM * HD * 2, 0x00020000),
                                          __builtin_amdgcn_make_buffer_rsrc((void*)Wv, 0, DM * HD * 2, 0x00020000), __builtin_amdgcn_make_buffer_rsrc((void*)Wo, 0, DM * HD * 2, 0x00020000)};
    const int vq = (lane >> 2) * (HD * 2) + vcu * 64 + (lane & 3) * 16, sq = (256 * w + (rot1 << 6)) * (HD * 2);
    const int vo = (lane >> 1) * (DM * 2) + cg * 32 + (lane & 1) * 16, so = (1024 * w + (rot3 << 7)) * (DM * 2);
    const int sl = ldsb + LR_STG + w * 32768;
#define RISSUE(c) { if ((c) < 3) dma16(rw[c], vq, sq, 16 * HD * 2, sl + ((c) & 1) * 16384); else if ((c) < 5) dma16(rw[3], vo, so ^ (((c) - 3) * 512 * DM * 2), 32 * DM * 2, sl + ((c) & 1) * 16384); }
    const int ch0 = (b * 4 + w) * 2;
    u32x2 pin[6];
    u32 ldt;
    {
      const int po = (ch0 * SSM_N + 4 * lane) * 2;
      asm volatile("global_load_dwordx2 %0, %7, %9\n\tglobal_load_dwordx2 %1, %7, %10\n\tglobal_load_dwordx2 %2, %7, %11\n\tglobal_load_dwordx2 %3, %7, %12\n\t"
                   "global_load_dwordx2 %4, %7, %13\n\tglobal_load_dwordx2 %5, %7, %14\n\tglobal_load_ushort %6, %8, %15"
                   : "=&v"(pin[0]), "=&v"(pin[1]), "=&v"(pin[2]), "=&v"(pin[3]), "=&v"(pin[4]), "=&v"(pin[5]), "=&v"(ldt)
                   : "v"(po), "v"((ch0 + (lane >> 5)) * 2), "s"(sp.A_re), "s"(sp.A_im), "s"(sp.B_re), "s"(sp.B_im), "s"(sp.C_re), "s"(sp.C_im), "s"(sp.log_dt) : "memory");
    }
    RISSUE(0)
    asm volatile("s_waitcnt vmcnt(16)" : "+v"(pin[0]), "+v"(pin[1]), "+v"(pin[2]), "+v"(pin[3]), "+v"(pin[4]), "+v"(pin[5]), "+v"(ldt)::"memory");
    f32x4* prm = (f32x4*)(lds + w * 4096);
    {
      const float dt = rbf(expf(bf2f((u16)ldt)));
#pragma unroll
      for (int i = 0; i < 4; ++i) {
        float v[6];
#pragma unroll
        for (int j = 0; j < 6; ++j) v[j] = __uint_as_float((i & 1) ? (pin[j][i >> 1] & 0xffff0000u) : (pin[j][i >> 1] << 16));
        const float are = dt * v[0], aim = dt * v[1], br = dt * v[2], bi = dt * v[3];
        const float cr = v[4] * br - v[5] * bi, ci = v[4] * bi + v[5] * br;
        const float mx = fmaxf(fabsf(cr), fabsf(ci));
        const float sc = mx > 0.f && mx < INFINITY ? __builtin_amdgcn_ldexpf(1.f, -__builtin_amdgcn_frexp_expf(mx)) : 1.f;
        const float xr = cr * sc, xi = ci * sc;
        const float lc = 0.5f * log2f(xr * xr + xi * xi) - log2f(sc);
        const float ph = atan2f(ci, cr) * 0.15915494309189535f;
        prm[4 * lane + i] = (f32x4){are * 1.4426950408889634f, aim * 0.15915494309189535f, lc, ph};
      }
      asm volatile("s_waitcnt lgkmcnt(0)" ::: "memory");
    }
    f32x4 kac[3];
#pragma unroll
    for (int c = 0; c < 5; ++c) {
      if (c < 4) {
        const int pc = c;
        if (!(pc & 1)) { kac[0] = (f32x4){0, 0, 0, 0}; kac[1] = kac[0]; kac[2] = kac[0]; }
        if (c < 2) kgen_half(prm + 64 * pc, kac, lane, rw[c + 1], vq, sq, 16 * HD * 2, sl + ((c + 1) & 1) * 16384);
        else kgen_half(prm + 64 * pc, kac, lane, rw[3], vo, so ^ ((c - 2) * 512 * DM * 2), 32 * DM * 2, sl + ((c + 1) & 1) * 16384);
        if (c == 3) clk_edge(cyc0, tk0);
        waitvm<16>();
        if (pc & 1) {
#pragma unroll
          for (int t = 0; t < 3; ++t)
            __builtin_amdgcn_raw_buffer_store_b128(__builtin_bit_cast(u32x4, kac[t]), rst, lane * 16, 2 * SSM_L * SSM_D * 2 + (ch0 + (pc >> 1)) * 3072 + t * 1024, 16);
        }
        asm volatile("" ::: "memory");
      }
      if (c == 4) waitvm<0>();
      const u16* S = (const u16*)(lds + LR_STG + w * 32768 + (c & 1) * 16384);
      if (c < 3) {
        u32 r[2][4][8];
        tr_chunk<2, 4>(S, lr, lg, r);
#pragma unroll
        for (int t = 0; t < 2; ++t)
#pragma unroll
          for (int i = 0; i < 4; ++i)
#pragma unroll
            for (int e = 0; e < 8; ++e) {
              const int o = (c < 2 ? (i * 4 + c * 2 + t) * 8 : 128 + (i * 2 + t) * 8) + e;
              wa[o >> 5][o & 31] = r[t][i][e];
            }
      } else {
        u32 r[1][8][8];
        tr_chunk<1, 8>(S, lr, lg, r);
#pragma unroll
        for (int i = 0; i < 8; ++i)
#pragma unroll
          for (int e = 0; e < 8; ++e) { const int o = (8 * (c - 3) + i) * 8 + e; wo[o >> 5][o & 31] = r[0][i][e]; }
      }
      asm volatile("s_waitcnt lgkmcnt(0)" ::: "memory");
      if (c == 3)
  {
    const u32x4 pz = (u32x4){SENT, SENT, SENT, SENT};
    if (w < 3) {
      u32x4 v = pz;
      if (w == 0 && lane < 32) {
        v = *(const u32x4*)(mem0 + (size_t)(4 * rg + ((lane >> 1) & 3)) * DM + 16 * cg + 8 * (lane & 1));
        v = (u32x4){nosent(v[0]), nosent(v[1]), nosent(v[2]), nosent(v[3])};
      }
#pragma unroll
      for (int hf = 0; hf < 2; ++hf) {
        __builtin_amdgcn_raw_buffer_store_b128(v, rsx, pz1, XSB(w) + hf * 8192, 16);
        __builtin_amdgcn_raw_buffer_store_b128(pz, rsx, XR2(vcu) + lane * 16, XR2B + (w & 1) * XR2S + hf * 8192, 16);
        __builtin_amdgcn_raw_buffer_store_b128(pz, rsx, lane < 16 ? XR2(vcu) + 1024 + lane * 16 : OOR, XR2B + (w & 1) * XR2S + hf * 8192, 16);
      }
    } else if (b < 48) {
      __builtin_amdgcn_raw_buffer_store_b128(pz, rsx, lane * 16, XSYN + b * 2048, 16);
      __builtin_amdgcn_raw_buffer_store_b128(pz, rsx, lane * 16, XSYN + b * 2048 + 1024, 16);
    } else if (b < 48 + 41) {
      __builtin_amdgcn_raw_buffer_store_b128(pz, rsx, lane * 16, PGO_D + (b - 48) * 1024, 16);
    }
    asm volatile("" ::: "memory");
  }
    }
  }
  const int hf = (lr >> 1) & 1;
  const int voff1 = lg * 256 + (lr >> 3) * 128 + (((lr >> 2) & 1) * 2 + (lr & 1)) * 32 + hf * 16;
  const int voff3 = (lr >> 3) * 512 + (lg >> 1) * 256 + (((lr >> 2) & 1) * 2 + (lr & 1)) * 16 + (lg & 1) * 128 + hf * 64;
  const int ard = ldsb + (2 * (lr >> 3) + (lg >> 1)) * 640 + (lr & 1) * 272 + ((lr >> 1) & 3) * 64 + (lg & 1) * 32;
  const int ctl = ldsb + LR_CTL, one = 1;
  const int r8 = lane & 7, hk = (lane >> 3) & 1;
#define COA 1
#define COB 1
  const int st01 = lr < 8 ? XR2(vcu) + lr * 16 + lg * 128 : OOR, st2 = (lr < 8 && lg >= 2) ? XR2(vcu) + 1024 + lr * 16 + (lg - 2) * 128 : OOR;
  const int ld01 = hk * 512 + r8 * 16 + lg * 128, ld2 = XR2(head * 4 + lg) + 1024 + r8 * 16 + hk * 128;
  int sto = XR3A(r8 >> 2, vcu * 256 + (r8 & 3) * 16 + lg * 64 + hk * 8);
  const int stk = lr >= 8 ? (lg * 8 + (lr - 8)) * 16 : OOR;
  const int lo = X4C ? 2 * (lr & 7) + (lr >> 3) : lr;
  const int arw = 0, arr = ldsb + 3840 + ((lo >> 3) + 2 * ((lo >> 2) & 1)) * 64 + (lo & 3) * 16;
  const int st1 = XR1A(lg, cg * 256 + rg * 128 + lo * 8);
  const int avw = 0;
  const int avc = ldsb + 2560 + (lr >> 3) * 640 + (8 * ((lr >> 2) & 1) + (lr & 3)) * 4;
  const int avr = avc + (lg & 1) * 272 + (lg >> 1) * 64, avf = avc + (lg & 1) * 128 + (lg >> 1) * 16;
  u32x16 kc = {(u32)voff1, (u32)voff3, (u32)idx, (u32)TEX(lane), (u32)(lane < 16 ? XR2(vcu) + 1024 + lane * 16 : OOR), (u32)ard, (u32)ctl, (u32)one, (u32)st01, (u32)st2, (u32)(XR2(vcu) + lane * 16), (u32)ld2, (u32)(lane * 16), (u32)arw, (u32)arr, (u32)stk};
  int o1c = XR1A(xcd, w * 4096) + rot1 * 1024, o3c = XR3A(rg, w * 8192) + rot3 * 1024;
  const int fo = w < 2 ? FO_K + ((head * 2 + w) * 4 + ks) * 1024 : FO_V + (head * 8 + 2 * ks + (w - 2)) * 1024;
  const int pgo = PGO_X + ((w >> 1) * 32 + head) * 128;
  int xp = -1;
#define XWAIT(n) for (int it_ = 0; it_ < SPIN_CAP && xp < (n); ++it_) {                                               \
    if (it_) asm volatile("s_sleep 4" ::: "memory");                                                                  \
    xp = xprog(__builtin_amdgcn_readfirstlane(__builtin_amdgcn_raw_buffer_load_b32(rsx, 0, pgo, 16)), tag); asm volatile("" ::: "memory"); }
  XWAIT(2)
  *(u32x4*)(lds + LR_FRG + w * 1024 + lane * 16) = __builtin_amdgcn_raw_buffer_load_b128(rsf, lane * 16, fo, 16);
  asm volatile("s_waitcnt vmcnt(0) lgkmcnt(0)\n\ts_barrier" ::: "memory");
  const u32 tbase = __builtin_amdgcn_readfirstlane(*(const u32*)(lds + LR_CTL + 4));
#define CLKF() clk_rate(cyc0, tk0)
  if (w == 0) { const float f = CLKF(); if (lane == 0) __builtin_amdgcn_raw_buffer_store_b64((u32x2){tag, __float_as_uint(f)}, rsx, 0, PGO_Y + b * 128, 16); }
  const u32 tb0 = tbase - (1u << 20);
#define TABLE(l) ({ const float f_ = CLKF() * *(const float*)(lds + LR_CTL + 28);          \
    const float k_ = 1.f + TKS * (fminf(fmaxf(23.9f / f_, 1.f), 1.5f) - 1.f);                                          \
    const int nb_ = (int)(TDB * k_ + 0.5f) + TLB * (l), nc_ = nb_ + (int)((TDC - TDB) * k_ + 0.5f) + TLC * (l), np_ = nc_ + (int)((TP - TDC) * k_ + 0.5f) + TLP * (l); \
    (u32)(nb_ | (nc_ << 10) | (np_ << 20)); })
  if (b == 0 && w != 0) asm volatile("s_barrier" ::: "memory");
  if (b == 0 && w == 0) {
    u32 mm[4] = {~0u, ~0u, ~0u, ~0u};
    asm volatile("s_waitcnt vmcnt(0)" ::: "memory");
#pragma unroll 1
    for (int it = 0; it < CALN; ++it) {
#pragma unroll
      for (int hf = 0; hf < 4; ++hf) {
        const u64 c0 = __builtin_amdgcn_s_memtime();
        asm volatile("s_waitcnt lgkmcnt(0)" ::: "memory");
        u32 v = __builtin_amdgcn_raw_buffer_load_b32(rsx, 0, XCAL + hf * 8192 + (it & 1) * 4096 + (it >> 1) * 1152, 16);
        asm volatile("s_waitcnt vmcnt(0)" : "+v"(v)::"memory");
        mm[hf] = min(mm[hf], (u32)(__builtin_amdgcn_s_memtime() - c0));
      }
    }
    asm volatile("s_barrier" ::: "memory");
    const u32 hb = min(mm[0], mm[2]) <= min(mm[1], mm[3]) ? 0u : 1u;
    const u32 best = (hb << 13) | ((hb ? mm[1] <= mm[3] : mm[0] <= mm[2]) ? CLS0 : 16384u - CLS0);
    u32 tt = 0;
    for (int it = 0; it < SPIN_CAP; ++it) {
      asm volatile("" ::: "memory");
      const u32x2 r0 = __builtin_amdgcn_raw_buffer_load_b64(rsx, lane * 128, PGO_Y, 16), r1 = __builtin_amdgcn_raw_buffer_load_b64(rsx, lane * 128 + 8192, PGO_Y, 16);
      const u64 miss = __ballot(r0[0] != tag || r1[0] != tag);
      if (tt == 0 && __popcll(miss) <= 24) {
        float fm = (r0[0] == tag ? __uint_as_float(r0[1]) : 0.f) + (r1[0] == tag ? __uint_as_float(r1[1]) : 0.f), fn = (r0[0] == tag ? 1.f : 0.f) + (r1[0] == tag ? 1.f : 0.f);
#pragma unroll
        for (int o = 4; o < 64; o *= 2) { fm += __shfl_xor(fm, o); fn += __shfl_xor(fn, o); }
        fm = fn > 0.f ? fm / fn : 100.f;
        const float f0 = __uint_as_float(__builtin_amdgcn_readfirstlane(__float_as_uint(fm)));
        fm = fminf(fm, __shfl_xor(fm, 1)); fm = fminf(fm, __shfl_xor(fm, 2));
        if (lane == 0) *(float*)(lds + LR_CTL + 28) = fminf(fmaxf(fm / f0, 0.8f), 1.f);
        asm volatile("s_waitcnt lgkmcnt(0)" ::: "memory");
        tt = TABLE(0);
      }
      if (miss == 0) break;
    }
    if (lane == 0) __builtin_amdgcn_raw_buffer_store_b128((u32x4){rt32() + TD0, tt, best, tag}, rsx, 0, XSTA, 16);
  }
  {
    u64 pc; asm volatile("s_getpc_b64 %0" : "=s"(pc));
    const u32 cv = __builtin_amdgcn_raw_buffer_load_b32(__builtin_amdgcn_make_buffer_rsrc((void*)pc0, 0, clen, 0x00020000), (int)(pc - pc0) + tid * 64, 0, 0);
    asm volatile("" :: "v"(cv));
  }
  u32x4 st = wait_start(rsx, fb, tag, false);
  const int flip = __builtin_amdgcn_readfirstlane(st[2]) & 8192, cls = __builtin_amdgcn_readfirstlane(st[2]) & 16384;
  o1c ^= cls; o3c ^= cls; sto ^= cls; kc[4] ^= cls; kc[8] ^= cls; kc[9] ^= cls; kc[10] ^= cls; kc[11] ^= cls;
  { const u32 now = rt32(); if (st[0] - now + 100000u > 200000u) st[0] = now; }
  u32x16 kd = {(u32)((ld01 + XR2(vcu ^ 1)) ^ cls), (u32)((ld01 + XR2(vcu ^ 2)) ^ cls), (u32)((ld01 + XR2(vcu ^ 3)) ^ cls), (u32)(X4B && hk ? OOR : sto), (u32)avr, (u32)(avr + 4096), (u32)(avr + 8192),
               (u32)(avr + 12288), (u32)avw, (u32)(X4C && hk ? OOR : st1 ^ cls), (u32)(ldsb + LR_FRG + lane * 16), (u32)(XSB(lane + 1) + flip), (u32)(lane * 8), (u32)avf,
               (u32)(b == 0 && lane == 0 ? PGO_R + flip : OOR), (u32)(pz1 ^ cls)};
  u32x16 sa = {(u32)flip, (u32)(XSB(1) + flip), 0u, (u32)(fo + NH * 8 * 1024), (u32)(ldsb + LR_FRG + 4096 + w * 1024), (u32)(FO_KM + (head * 4 + ks) * 512),
               (u32)(FO_VM + (head * 8 + 2 * ks) * 256), tb0, st[0] - tb0 - (xcd & 1) * XSK, 0u, (u32)SYO(1), 0u, 0u, 0u, 0u, 0u};
  int snt = -1, synced = 0;
  int cb = __builtin_amdgcn_readfirstlane(st[1]), cc = (cb >> 10) & 1023, cp = (cb >> 20) & 1023;
  cb &= 1023;
#define RESYNC()                                                                                                      \
  {                                                                                                                   \
    for (int it = 0; it < 100; ++it) {                                                                                \
      const u32x2 tw = __builtin_amdgcn_raw_buffer_load_b64(rsx, 0, SYO(seg) + 8, 16);                                \
      const u32 tv = __builtin_amdgcn_readfirstlane(tw[0]), to = __builtin_amdgcn_readfirstlane(tw[1]);               \
      if (tv != SENT && to != SENT) {                                                                                 \
        cb = to & 1023; cc = (to >> 10) & 1023; cp = to >> 20; sa[7] = tb0 + seg * cp; sa[8] = tv - sa[7] - (xcd & 1) * XSK;            \
        if (lane == 0) *(u32*)(lds + LR_CTL + 24) = to;                                                               \
        break;                                                                                                        \
      }                                                                                                               \
      asm volatile("s_sleep 4" ::: "memory");                                                                         \
    }                                                                                                                 \
    if (lane == 0) *(u32*)(lds + LR_CTL) = sa[8];                                                                     \
    synced = seg;                                                                                                     \
  }
  int nev = 0; (void)nev;
  bool rsn = false;
  u32x32 keep;
  u32x2 ov;
  asm volatile("" : "={v[60:91]}"(keep));
  while (true) {
    u32x8 sc = {(u32)o1c, (u32)o3c, (u32)snt, (u32)cp, (u32)cb, (u32)cc, 0u, (u32)pgo};
    sa[15] = rsn ? 1u : 0u;
    sa[14] = xp;
#pragma unroll
    for (int i = 0; i < 16; ++i) sa[i] = __builtin_amdgcn_readfirstlane(sa[i]);
#pragma unroll
    for (int i = 0; i < 8; ++i) sc[i] = __builtin_amdgcn_readfirstlane(sc[i]);
    asm volatile(
        "s_nop 4\n\t" "s_mov_b32 s60, m0\n\t"
        "s_mov_b32 m0, %[mb]\n\t" "s_mov_b32 s61, s51\n\t" "v_mov_b32 v48, %[tg]\n\t" "s_mov_b32 s64, s50\n\t"
        EXTRA "s_mov_b32 s70, 0\n\t"
        R2BASE
        "s_cmp_eq_u32 s47, 1\n\t" "s_cbranch_scc1 .LB%=\n\t"
        "s_cmp_eq_u32 s47, 2\n\t" "s_cbranch_scc1 .LC%=\n"
        ".LA%=:\n\t"
        CLKW(".Lta%=", "0")
        "s_add_u32 s50, s36, s52\n\t"
        TSA(0) GLD(96, 0)
        "ds_read_b32 v62, v38 offset:16\n\t" "ds_read_b128 v[52:55], v18\n\t" "ds_read_b128 v[56:59], v18 offset:1024\n\t" ZER(64) ZER(68)
        GLD(100, 1024) ZER(72) ZER(76) ZER(24) ZER(28) R2BASE
        GLD(104, 2048)
        "s_mov_b32 m0, s40\n\t" "s_mov_b32 s47, 0\n\t"
        "s_sub_i32 s63, s64, s45\n\t" "s_cmp_lt_i32 s63, 6\n\t" "s_cselect_b32 s63, s59, %[dm]\n\t" "s_nop " STR(APC) "\n\t"
        GLD(108, 3072)
        ".Lfw%=:\n\t"
        WLG(0)
        "v_readfirstlane_b32 s49, v62\n\t"
        "s_cmp_ge_u32 s49, s38\n\t"
        "s_cbranch_scc1 .Lfx%=\n\t"
        "ds_read_b32 v62, v38 offset:16\n\t" "ds_read_b128 v[52:55], v18\n\t" "ds_read_b128 v[56:59], v18 offset:1024\n\t"
        "s_branch .Lfw%=\n"
        ".Lfx%=:\n\t"
        WVM(3) TSA(1) SQ(0, 0) MXA0(96) SQ(1, 0) MXB(96) SQ(2, 0) SQ(3, 0)
        WVM(2) SQ(0, 1) "buffer_load_dwordx4 v44, %[rf], s39 offen sc1 lds\n\t" SQ(1, 1) "buffer_load_dword v48, off, %[rs], s63 sc1\n\t"
        SQ(2, 1) MXA(100) MXB(100) SQ(3, 1) "s_mov_b32 m0, %[mb]\n\t"
        QBLK(2, MXA)
        WVM(2) SQ(0, 3) MXA(108) MXB(108) SQ(1, 3) CMPB SQ(2, 3) SQ(3, 3) TSA(2)
        "s_cbranch_vccnz .Lfail%=\n\t"
        "s_nop 1\n\t" PSA(64) PSW(64, 0) PSW(65, 272) PSA(68) SV(0, 0) PSW(68, 640) PSW(69, 912) PSA(72) SV(1, 0) PSW(72, 1280) PSW(73, 1552) PSA(76) SV(0, 1) PSW(76, 1920) PSW(77, 2192)
        ARRIVE(8) TSA(3)
        RDQ(80, 0) SV(1, 1) RDQ(84, 16) RDQ(88, 4096) SV(0, 2) RDQ(92, 4112) RDQ(112, 8192) SV(1, 2) RDQ(116, 8208) WLG(12) RDQ(120, 12288) SV(0, 3) RDQ(124, 12304) SV(1, 3)
        WLG(8) TSA(4)
        "v_readfirstlane_b32 s48, v62\n\t"
        "s_and_b32 s48, s48, 3\n\t"
        ROLESV "s_cmp_lg_u32 s48, 3\n\t"
        "s_cbranch_scc1 .Lnl%=\n\t"
        WLG(4) PKA(80, 80, 88) PKA(82, 82, 90) PKA(84, 84, 92) PKA(86, 86, 94)
        WLG(0) PKA(112, 112, 120) PKA(114, 114, 122) PKA(116, 116, 124) PKA(118, 118, 126)
        TSA(5) PKA(80, 80, 112) PKA(82, 82, 114) PKA(84, 84, 116) PKA(86, 86, 118)
        "v_cvt_pk_bf16_f32 v72, v80, v81\n\t" "v_cvt_pk_bf16_f32 v73, v82, v83\n\t" "v_cvt_pk_bf16_f32 v74, v84, v85\n\t" "v_cvt_pk_bf16_f32 v75, v86, v87\n\t"
        "s_nop 1\n\t"
        "v_mfma_f32_16x16x32_bf16 v[88:91], v[72:75], v[72:75], 0\n\t"
        "v_mfma_f32_16x16x32_bf16 v[80:83], v[52:55], v[72:75], 0\n\t"
        "v_mfma_f32_16x16x32_bf16 v[84:87], v[56:59], v[72:75], 0\n\t"
        "s_nop 5\n\t"
        "buffer_store_dwordx4 v[88:91], v41, %[rs], s71 offen sc1\n\t"
        "buffer_store_dwordx4 v[80:83], v40, %[rs], s71 offen sc1\n\t"
        "buffer_store_dwordx4 v[84:87], v40, %[rs], s71 offen offset:512 sc1\n\t" TSA(6) TSB(7)
        VPOST TSA(7)
        "s_cmp_eq_u32 s45, 63\n\t"
        "s_cbranch_scc0 .LB%=\n\t"
        "buffer_store_dwordx4 v[72:75], v47, %[rf], s41 offen sc1\n\t"
        "s_branch .LC%=\n"
        ".LB%=:\n\t"
        CLKW(".Ltb%=", "s66")
        "s_mov_b32 s47, 1\n\t"
        R2TEXT
        "s_cbranch_vccz .LC2%=\n\t"
        "s_branch .Lend%=\n"
        ".Lnl%=:\n\t"
        "s_nop 2\n\t" WLG(4) VPOST
        "s_cmp_lg_u32 s48, 0\n\t"
        "s_cbranch_scc1 .LC%=\n"
        ".Lv0%=:\n\t"
        "v_add_u32 v80, 4096, v21\n\t" "v_add_u32 v81, 8192, v21\n\t" "v_add_u32 v82, 12288, v21\n\t"
        "ds_read_b32 v59, v38 offset:12\n\t"
        "ds_read2_b32 v[112:113], v21 offset0:0 offset1:68\n\t" "ds_read2_b32 v[114:115], v21 offset0:16 offset1:84\n\t"
        "ds_read2_b32 v[116:117], v80 offset0:0 offset1:68\n\t" "ds_read2_b32 v[118:119], v80 offset0:16 offset1:84\n\t"
        "ds_read2_b32 v[120:121], v81 offset0:0 offset1:68\n\t" "ds_read2_b32 v[122:123], v81 offset0:16 offset1:84\n\t"
        "ds_read2_b32 v[124:125], v82 offset0:0 offset1:68\n\t" "ds_read2_b32 v[126:127], v82 offset0:16 offset1:84\n\t"
        WLG(0)
        "v_readfirstlane_b32 s49, v59\n\t"
        "s_sub_u32 s49, s49, 4\n\t"
        "s_cmp_lt_i32 s49, s38\n\t"
        "s_cbranch_scc1 .Lv0%=\n\t"
        PKA(112, 112, 116) PKA(114, 114, 118) PKA(120, 120, 124) PKA(122, 122, 126) PKA(112, 112, 120) PKA(114, 114, 122)
        "v_cvt_pk_bf16_f32 v112, v112, v113\n\t" "v_cvt_pk_bf16_f32 v113, v114, v115\n\t"
        "s_add_u32 s49, s45, 1\n\t" "v_mov_b64 v[116:117], -1\n\t" "v_mov_b64 v[118:119], -1\n\t" "v_readlane_b32 s63, v19, s49\n\t"
        "s_cmp_eq_u32 s45, 63\n\t" "s_cselect_b32 s65, 0, s68\n\t"
        CLKL(".Ltv%=", "s65") CLKW(".Ltw%=", "s65")
        "s_lshl_b32 s50, s50, 17\n\t" "s_lshr_b32 s50, s50, 1\n\t" "s_or_b32 s50, s50, s55\n\t" "v_mov_b32 v114, s50\n\t" "s_lshl_b32 s50, s45, 7\n\t"
        "buffer_store_dwordx4 v[116:119], v23, %[rs], s63 offen sc1\n\t"
        "s_xor_b32 s63, s71, " STR(XR2S) "\n\t"
        "buffer_store_dwordx4 v[116:119], v42, %[rs], s63 offen sc1\n\t"
        "buffer_store_dwordx4 v[116:119], v36, %[rs], s63 offen sc1\n\t"
        "buffer_store_dwordx2 v[112:113], v20, %[rf], s42 offen sc1\n\t"
        "buffer_store_dword v114, v22, %[rs], s50 offen sc1\n"
        ".LC%=:\n\t"
        "s_mov_b32 s47, 3\n\t"
        "s_cmp_eq_u32 s45, 63\n\t"
        "s_cbranch_scc1 .Lend%=\n\t"
        CLKL(".Ltl%=", "s67")
        ".LC2%=:\n\t"
        CLKW(".Ltc%=", "s67")
        "s_add_u32 s50, s36, s53\n\t"
        TSW(0)
        GL1(96, 0) PC GL1(100, 1024) PC GL1(104, 2048) PC GL1(108, 3072) PC GL2(112, 0) PC GL2(116, 1024)
        "s_mov_b32 s48, s36\n\t" "s_mov_b32 s36, s37\n\t" "s_add_u32 s45, s45, 1\n\t" "s_add_u32 s38, s38, 4\n\t" "v_readlane_b32 s37, v19, s45\n\t"
        "s_add_u32 s39, s39, " STR(EXF) "\n\t" "s_xor_b32 s40, s40, 4096\n\t" "v_xor_b32 v18, 4096, v18\n\t"
        "s_add_u32 s41, s41, " STR(EXK) "\n\t" "s_add_u32 s42, s42, " STR(EXK) "\n\t" "s_add_u32 s43, s43, s55\n\t"
        GL2(120, 2048) ZER(64) ZER(68) "s_mov_b32 s47, 2\n\t"
        "s_mul_i32 s70, s69, 3\n\t" EXTRA
        GL2(124, 3072)
        "buffer_load_dword v61, off, %[rs], s46 sc1\n\t"
        "s_add_u32 s46, s46, 64\n\t"
        WVM(8) TSW(1) WBLK(0, MXA0) WBLK(1, MXA) WBLK(2, MXA) WBLK(3, MXA) WBLK(4, MXA) WBLK(5, MXA) WBLK(6, MXA)
        WVM(1) TSW(2) SW(0, 7) MXA(124) MXB(124) SW(1, 7) CMPB TSW(3)
        "s_cbranch_vccnz .Lfail%=\n\t"
        "s_nop 3\n\t"
        "v_add_f32_dpp v80, v64, v66 quad_perm:[0,1,2,3] row_mask:0x3 bank_mask:0xf\n\t" "v_add_f32_dpp v81, v65, v67 quad_perm:[0,1,2,3] row_mask:0x3 bank_mask:0xf\n\t"
        "v_add_f32_dpp v80, v68, v70 quad_perm:[0,1,2,3] row_mask:0xc bank_mask:0xf\n\t" "v_add_f32_dpp v81, v69, v71 quad_perm:[0,1,2,3] row_mask:0xc bank_mask:0xf\n\t"
        "s_nop 1\n\t"
        "v_permlane32_swap_b32 v80, v81\n\t"
        "v_add_f32 v80, v80, v81\n\t"
        PSW(80, 3840)
        ARRIVE(16) TSW(4)
        "ds_read_b128 v[80:83], v46\n\t" "ds_read_b128 v[84:87], v46 offset:4096\n\t"
        "ds_read_b128 v[88:91], v46 offset:8192\n\t" "ds_read_b128 v[92:95], v46 offset:12288\n\t"
        "v_readfirstlane_b32 s50, v48\n\t"
        WLG(4) TSW(5)
        "v_readfirstlane_b32 s51, v62\n\t"
        WLG(2) PKA(80, 80, 84) "s_and_b32 s51, s51, 3\n\t" PKA(82, 82, 86) "s_cmp_lg_u32 s51, 3\n\t" WLG(0) PKA(88, 88, 92) PKA(90, 90, 94) PKA(80, 80, 88) PKA(82, 82, 90)
        "v_cvt_pk_bf16_f32 v80, v80, v81\n\t" "v_cvt_pk_bf16_f32 v81, v82, v83\n\t"
        "v_min_u32 v80, -2, v80\n\t" "v_min_u32 v81, -2, v81\n\t"
        "s_cbranch_scc1 .Lnv%=\n\t"
        STC TSW(6)
        WVM(1) "s_branch .Lnw%=\n"
        ".Lnv%=:\n\t" WVM(0)
        ".Lnw%=:\n\t"
        "v_readfirstlane_b32 s49, v61\n\t"
        "s_sub_u32 s50, s50, %[tg]\n\t" "s_cmp_le_u32 s50, 80\n\t" "s_cselect_b32 s50, s50, 0\n\t"
        "s_max_i32 s64, s64, s50\n\t" "s_add_u32 s44, s44, s70\n\t"
        "s_not_b32 s49, s49\n\t" "s_or_b32 s49, s49, s61\n\t"
        "s_sub_i32 s50, s64, s45\n\t" "s_cmp_lt_i32 s50, " STEPCHK "\n\t" "s_cselect_b32 s50, 1, 0\n\t"
        "s_or_b32 s50, s50, s49\n\t"
        "s_cbranch_scc0 .LA%=\n\t"
        "s_mov_b32 s47, 4\n\t"
        "s_branch .Lend%=\n" ".Lfail%=:\n\t" "s_nop 7\n" ".Lend%=:\n\t"
        "s_waitcnt vmcnt(0) lgkmcnt(0)\n\t"
        ROLEOUT "s_mov_b32 s50, s64\n\t" "s_mov_b32 m0, s60"
        : "+{s[36:51]}"(sa), "+{v[8:23]}"(kd), "+{v[60:91]}"(keep), "=&{v[116:117]}"(ov) FINEO
        : "{v[32:47]}"(kc), "{s[52:59]}"(sc), [rs] "s"(rsx), [rf] "s"(rsf), [dm] "s"(__builtin_amdgcn_readfirstlane(PGO_Y + b * 128)), [tg] "s"(__builtin_amdgcn_readfirstlane(tag)), [r2] "s"(__builtin_amdgcn_readfirstlane(XR2B + flip)), [mb] "s"(__builtin_amdgcn_readfirstlane(ldsb + w * 4096)),
          "{a[0:31]}"(wa[0]), "{a[32:63]}"(wa[1]), "{a[64:95]}"(wa[2]), "{a[96:127]}"(wa[3]), "{a[128:159]}"(wa[4]), "{a[160:191]}"(wa[5]),
          "{v[128:159]}"(wo[0]), "{v[160:191]}"(wo[1]), "{v[192:223]}"(wo[2]), "{v[224:255]}"(wo[3])
        : CL8(24, 25, 26, 27, 28, 29, 30, 31), "v48", CL8(52, 53, 54, 55, 56, 57, 58, 59), "v92", "v93", "v94", "v95", CL96, "v112", "v113", "v114", "v115", "v118", "v119", CL120,
          "s60", "s61", "s62", "s63", "s64", "s65", "s66", "s67", "s68", "s69", "s70", "s71", "s72", "s73", "s74", "s75", "s76", "s77", "vcc", "scc", "memory");
    const int code = __builtin_amdgcn_readfirstlane(sa[11]);
    xp = __builtin_amdgcn_readfirstlane(sa[14]);
    if (code == 2) {
      sa[1] = sa[0]; sa[0] = sa[12]; sa[9] -= 1; sa[2] -= 4; sa[3] -= EXF; sa[4] ^= 4096; kd[10] ^= 4096;
      sa[5] -= EXK; sa[6] -= EXK; sa[7] -= cp; sa[10] -= 64;
    }
    const int seg = __builtin_amdgcn_readfirstlane(sa[9]);
    if (code == 3) break;
    if (code == 4) {
      XWAIT(seg + 2)
      if (sa[13] != 0) {
        rsn = false;
        if (b == 0 && sa[15] == 3) {
          const int l = min((int)*(const u32*)(lds + LR_CTL + 20), 40);
          const u32 tt = TABLE(l);
          if (lane == 0) {
            __builtin_amdgcn_raw_buffer_store_b64((u32x2){nosent(rt32() + TD), tt}, rsx, 0, SYO(seg) + 8, 16);
            if (seg > 2) *(u32*)(lds + LR_CTL + 20) = l + 1;
          }
        }
        RESYNC()
      }
      sa[11] = 0;
      continue;
    }
    if (code == 1) {
      u32 mx = 0;
#pragma unroll
      for (int i = 0; i < 20; ++i) if (i < 12 || i >= 16) mx = max(mx, keep[i]);
      if (__ballot(mx == SENT) == 0 || tries > TRY_CAP) { __builtin_amdgcn_raw_buffer_store_b64(ov, rsx, sto, sa[0], 16); sa[11] = 2; continue; }
    }
    ++tries; tcnt += 1 << (10 * code);
    {
      const u32 to = lds_word(ctl + 24);
      if (to != 0 && (int)(to >> 20) != cp) { cb = to & 1023; cc = (to >> 10) & 1023; cp = to >> 20; sa[7] = tb0 + seg * cp; sa[8] = 0; }
    }
    if (tries > TRY_CAP) snt = snt == -1 ? 0 : snt + 1;
    if (code == 0 && synced != seg && __builtin_amdgcn_readfirstlane(__builtin_amdgcn_raw_buffer_load_b32(rsx, 0, SYO(seg), 16)) != SENT) {
      RESYNC()
      continue;
    }
    if (b == 0 && !rsn) {
      if (lane == 0) __builtin_amdgcn_raw_buffer_store_b32(1u, rsx, 0, SYO(seg + 1), 16);
      rsn = true;
    }
    sa[8] = max(sa[8], rt32() + TR - sa[7] - (code == 0 ? 0 : (code == 1 ? cb : cc) + code * TEX(seg)));
    if (lane == 0) atomicMax((u32*)(lds + LR_CTL), sa[8]);
  }
  LBAR();
  if (tid == 0) __builtin_amdgcn_raw_buffer_store_b32(1u, rsx, 0, PGO_D + vcu * 128, 16);
  if (b == 0 && w == 1) {
    for (int it = 0; it < SPIN_CAP; ++it) {
      asm volatile("" ::: "memory");
      if (__ballot(__builtin_amdgcn_raw_buffer_load_b32(rsx, lane * 128, PGO_S, 16) != tag || __builtin_amdgcn_raw_buffer_load_b32(rsx, lane * 128 + 8192, PGO_S, 16) != tag) == 0) break;
    }
    if (lane == 0) __builtin_amdgcn_raw_buffer_store_b32(tag, rsx, 0, XEPO, 16);
  }
}

#define XSTAT 3
#define XVM 8
#define XP0 -1
#define XSC " sc1"
#define XPITCH 1056
#define XSLOT (32 * XPITCH)
#define XW(HB, t) "a[" STR(HB) "+8*xk+4*" #t ":" STR(HB) "+8*xk+4*" #t "+3]"
#define XF(rt) "v[96+8*(xk&3)+4*" #rt ":96+8*(xk&3)+4*" #rt "+3]"
#define XFN(rt) "v[96+8*((xk+3)&3)+4*" #rt ":96+8*((xk+3)&3)+4*" #rt "+3]"
#define XA(AC, o) "v[" STR(AC) "+" #o ":" STR(AC) "+" #o "+3]"
#define XM1(d, a, b, FIRST) ".if (xk == 0) && " STR(FIRST) "\n\tv_mfma_f32_16x16x32_bf16 " d ", " a ", " b ", 0\n\t.else\n\tv_mfma_f32_16x16x32_bf16 " d ", " a ", " b ", " d "\n\t.endif\n\t"
#define XMF_QK(i, HB, AC, F) XMF_QK##i(HB, AC, F)
#define XMF_QK0(HB, AC, F) XM1(XA(AC, 0), XW(HB, 0), XF(0), F)
#define XMF_QK1(HB, AC, F) XM1(XA(AC, 4), XW(HB, 1), XF(0), F)
#define XMF_QK2(HB, AC, F) XM1(XA(AC, 8), XW(HB, 0), XF(1), F)
#define XMF_QK3(HB, AC, F) XM1(XA(AC, 12), XW(HB, 1), XF(1), F)
#define XMF_V(i, HB, AC, F) XMF_V##i(HB, AC, F)
#define XMF_V0(HB, AC, F) XM1(XA(AC, 0), XF(0), XW(HB, 0), F)
#define XMF_V1(HB, AC, F) XM1(XA(AC, 8), XF(0), XW(HB, 1), F)
#define XMF_V2(HB, AC, F) XM1(XA(AC, 4), XF(1), XW(HB, 0), F)
#define XMF_V3(HB, AC, F) XM1(XA(AC, 12), XF(1), XW(HB, 1), F)
#define XRD(rt, CUR, NXT) ".if xk < 13\n\tds_read_b128 " XFN(rt) ", " CUR " offset:64*(xk+3)+" STR(rt * 16 * XPITCH) "\n\t.else\n\t" \
                          "ds_read_b128 " XFN(rt) ", " NXT " offset:64*(xk-13)+" STR(rt * 16 * XPITCH) "\n\t.endif\n\t"
#define XCV(EPI, PV, i) ".if " STR(EPI) " && (xk >= 1) && (xk < 3)\n\tv_cvt_pk_bf16_f32 v[128+4*(xk-1)+" #i "], v[" STR(PV) "+8*(xk-1)+2*" #i "], v[" STR(PV) "+8*(xk-1)+2*" #i "+1]\n\t.endif\n\t"
#define XHALF(MF, HB, AC, PV, EPI, CUR, NXT, DSLOT, GOFF)                                                   \
  ".set xk, 0\n\t"                                                                                          \
  ".rept 16\n\t"                                                                                            \
  "s_waitcnt lgkmcnt(4)\n\t"                                                                                \
  MF(0, HB, AC, EPI) XRD(0, CUR, NXT) XCV(EPI, PV, 0)                                                       \
  ".if (xk >= 5) && (xk < 13)\n\t"                                                                          \
  "s_add_u32 m0, m0, " STR(XPITCH) "\n\t"                                                                   \
  ".endif\n\t"                                                                                              \
  MF(1, HB, AC, EPI) XRD(1, CUR, NXT) XCV(EPI, PV, 1)                                                       \
  ".if (xk >= 5) && (xk < 13)\n\t"                                                                          \
  "s_add_u32 %[dg], %[dg], 2048\n\t"                                                                        \
  ".endif\n\t"                                                                                              \
  MF(2, HB, AC, EPI) XCV(EPI, PV, 2)                                                                        \
  ".if (xk >= 4) && (xk < 12)\n\t"                                                                          \
  "buffer_load_dwordx4 %[l16], %[rx], %[dg] offen lds\n\t"                                                  \
  ".endif\n\t"                                                                                              \
  ".if xk == 0\n\t"                                                                                         \
  "s_add_u32 m0, %[dl], " STR((DSLOT) * XSLOT) "\n\t" "s_add_u32 %[dg], %[gb], " STR(GOFF) "\n\t"           \
  ".endif\n\t"                                                                                              \
  ".if " STR(EPI) " && (xk == " STR(XSTAT) ")\n\t"                                                                       \
  "buffer_store_dwordx4 v[128:131], v137, %[rf], %[sg] offen" XSC "\n\t"                                       \
  ".endif\n\t"                                                                                              \
  ".if " STR(EPI) " && (xk == 13)\n\t"                                                                      \
  "v_mov_b32 v137, %[l16]\n\t" "v_mov_b32 v138, %[lp2]\n\t"                                                 \
  ".endif\n\t"                                                                                              \
  ".if " STR(EPI) " && (xk == 14)\n\t"                                                                      \
  "s_add_u32 %[sg], %[sg], " STR(NH * 8 * 1024) "\n\t"                                                      \
  "v_add_u32 v136, 1, v136\n\t"                                                                             \
  ".endif\n\t"                                                                                              \
  ".if !" STR(EPI) " && (xk == 1)\n\t"                                                                      \
  "buffer_store_dword v136, %[fo], %[rp], 0 offen sc1\n\t"                                                  \
  ".endif\n\t"                                                                                              \
  MF(3, HB, AC, EPI) XCV(EPI, PV, 3)                                                                        \
  ".if xk == 0\n\t"                                                                                         \
  "s_waitcnt vmcnt(" STR(XVM) ")\n\t" "s_barrier\n\t"                                                               \
  ".endif\n\t"                                                                                              \
  ".if xk == 2\n\t"                                                                                         \
  "s_min_u32 %[dg], %[dg], " STR(S_LEN * DM * 2 - 16384 + 1024) "\n\t"                                      \
  ".endif\n\t"                                                                                              \
  ".if " STR(EPI) " && (xk == " STR(XSTAT) ")\n\t"                                                                       \
  "buffer_store_dwordx4 v[132:135], v138, %[rf], %[sg] offen" XSC "\n\t"                                       \
  ".endif\n\t"                                                                                              \
  ".set xk, xk+1\n\t"                                                                                       \
  ".endr\n\t"
#define XLOOP(MF)                                                                                           \
  "s_mov_b32 %[kp], m0\n\t"                                                                                 \
  "v_mov_b32 v136, %[p0]\n\t" "v_mov_b32 v137, " STR(OOR) "\n\t" "v_mov_b32 v138, " STR(OOR) "\n\t"                                                                                \
  "ds_read_b128 v[96:99], %[r0]\n\tds_read_b128 v[100:103], %[r0] offset:" STR(16 * XPITCH) "\n\t"          \
  "ds_read_b128 v[104:107], %[r0] offset:64\n\tds_read_b128 v[108:111], %[r0] offset:64+" STR(16 * XPITCH) "\n\t"   \
  "ds_read_b128 v[112:115], %[r0] offset:128\n\tds_read_b128 v[116:119], %[r0] offset:128+" STR(16 * XPITCH) "\n"   \
  ".Lx%=:\n\t"                                                                                              \
  XHALF(MF, 0, 64, 80, 1, "%[r0]", "%[r1]", 3, 66560) XHALF(MF, 128, 64, 80, 0, "%[r1]", "%[r2]", 0, 131072) \
  XHALF(MF, 0, 80, 64, 1, "%[r2]", "%[r3]", 1, 132096) XHALF(MF, 128, 80, 64, 0, "%[r3]", "%[r0]", 2, 196608) \
  "s_add_u32 %[gb], %[gb], 131072\n\t"                                                                      \
  "s_sub_u32 %[cnt], %[cnt], 1\n\t"                                                                         \
  "s_cmp_lg_u32 %[cnt], 0\n\t"                                                                              \
  "s_cbranch_scc1 .Lx%=\n\t"                                                                                \
  "s_mov_b32 m0, %[kp]\n\t"                                                                                 \
  "s_nop 7\n\t" "s_waitcnt lgkmcnt(0)"

template <bool ISV>
__device__ __forceinline__ void x_role(unsigned char* lds, const u16* __restrict__ x, const u16* __restrict__ Wm, int fo0, int head,
                                       __amdgpu_buffer_rsrc_t rsf, u32* prog, u32 tag, int lane, int w) {
  const int ldsb = (int)(size_t)(LAS unsigned char*)lds;
  const __amdgpu_buffer_rsrc_t rsxin = __builtin_amdgcn_make_buffer_rsrc((void*)x, 0, S_LEN * DM * 2, 0x00020000);
  const __amdgpu_buffer_rsrc_t rsp = __builtin_amdgcn_make_buffer_rsrc((void*)prog, 0, 128, 0x00020000);
  const int lr = lane & 15, lg = lane >> 4;
  const int xrd = ldsb + lr * XPITCH + lg * 16;
  const int fob = fo0 + (ISV ? (head * 8 + 2 * w) : (head * 8 + w)) * 1024;
  const int p2 = ISV ? 1024 : 4096;
  u32x32 xw[8];
  {
    const __amdgpu_buffer_rsrc_t rw = __builtin_amdgcn_make_buffer_rsrc((void*)Wm, 0, DM * HD * 2, 0x00020000);
    const int vq = (lane >> 4) * (HD * 2) + head * 256 + (lane & 15) * 16;
#define XWISSUE(c) _Pragma("unroll") for (int j = 0; j < 8; ++j)                                                       \
      asm volatile("s_mov_b32 m0, %0\n\ts_nop 3\n\tbuffer_load_dwordx4 %1, %2, %3 offen lds"                           \
                   :: "s"(ldsb + ((c) & 3) * XSLOT + (8 * w + j) * XPITCH), "v"(vq), "s"(rw), "s"((128 * (c) + 32 * w + 4 * j) * (HD * 2)) : "memory");
#define XXISSUE(h) _Pragma("unroll") for (int j = 0; j < 8; ++j)                                                       \
      asm volatile("s_mov_b32 m0, %0\n\ts_nop 3\n\tbuffer_load_dwordx4 %1, %2, %3 offen lds"                           \
                   :: "s"(ldsb + (h) * XSLOT + (8 * w + j) * XPITCH), "v"(lane * 16), "s"(rsxin), "s"((((h) >> 1) * 32 + 8 * w + j) * 2048 + ((h) & 1) * 1024) : "memory");
    XWISSUE(0) XWISSUE(1) XWISSUE(2) XWISSUE(3)
    const int q = lr >> 2, p = lr & 3;
#pragma unroll
    for (int c = 0; c < 8; ++c) {
      asm volatile("s_waitcnt vmcnt(24)\n\ts_barrier" ::: "memory");
#pragma unroll
      for (int s = 0; s < 4; ++s)
#pragma unroll
        for (int t = 0; t < 2; ++t) {
          const unsigned char* base = lds + (c & 3) * XSLOT + (8 * s + 2 * lg) * XPITCH + q * 256 + w * 64 + p * 16 + t * 8;
          const u32x2 t0 = __builtin_bit_cast(u32x2, __builtin_amdgcn_ds_read_tr16_b64_v4i16((LAS v4i16_t*)(base)));
          const u32x2 t1 = __builtin_bit_cast(u32x2, __builtin_amdgcn_ds_read_tr16_b64_v4i16((LAS v4i16_t*)(base + XPITCH)));
          const int o = ((4 * c + s) * 2 + t) * 4;
          xw[o >> 5][(o & 31) + 0] = t0[0]; xw[o >> 5][(o & 31) + 1] = t0[1]; xw[o >> 5][(o & 31) + 2] = t1[0]; xw[o >> 5][(o & 31) + 3] = t1[1];
        }
      LBAR();
      if (c < 4) { XWISSUE(c + 4) } else if (c < 7) { XXISSUE(c - 4) }
    }
  }
  asm volatile("s_waitcnt vmcnt(8)\n\ts_barrier" ::: "memory");
  u32x16 xacc;
  {
    u32 kp, dg; int gb = 8 * w * 2048, cnt = NSEG / 2, sg = fob - NH * 8 * 1024;
#define XASM(MF)                                                                                            \
    asm volatile(XLOOP(MF)                                                                                  \
                 : "=&{v[80:95]}"(xacc), [kp] "=&s"(kp), [dg] "=&s"(dg), [gb] "+s"(gb), [cnt] "+s"(cnt), [sg] "+s"(sg) \
                 : [r0] "v"(xrd), [r1] "v"(xrd + XSLOT), [r2] "v"(xrd + 2 * XSLOT), [r3] "v"(xrd + 3 * XSLOT), [l16] "v"(lane * 16), [lp2] "v"(lane * 16 + p2), \
                   [rx] "s"(rsxin), [rf] "s"(rsf), [rp] "s"(rsp), [fo] "v"(lane == 0 && w == 0 ? 0 : OOR), [dl] "s"(ldsb + 8 * w * XPITCH), [p0] "s"(tag + XP0), \
                   "{a[0:31]}"(xw[0]), "{a[32:63]}"(xw[1]), "{a[64:95]}"(xw[2]), "{a[96:127]}"(xw[3]),      \
                   "{a[128:159]}"(xw[4]), "{a[160:191]}"(xw[5]), "{a[192:223]}"(xw[6]), "{a[224:255]}"(xw[7]) \
                 : CL64, CL72, CL96, CL112, CL120, CL8(128, 129, 130, 131, 132, 133, 134, 135), "v136", "v137", "v138", "scc", "memory");
    if (ISV) { XASM(XMF_V) } else { XASM(XMF_QK) }
  }
#pragma unroll
  for (int jj = 0; jj < 2; ++jj) {
    u32x4 pk;
#pragma unroll
    for (int e = 0; e < 4; ++e) pk[e] = pack2(__uint_as_float(xacc[8 * jj + 2 * e]), __uint_as_float(xacc[8 * jj + 2 * e + 1]));
    __builtin_amdgcn_raw_buffer_store_b128(pk, rsf, lane * 16 + p2 * jj, fob + (NSEG - 1) * (NH * 8 * 1024), 16);
  }
  asm volatile("s_waitcnt vmcnt(0)\n\ts_barrier" ::: "memory");
  if (lane == 0 && w == 0) __hip_atomic_store(prog, tag + NSEG + 8, RLX_AGENT);
}

#define SSM_LO 1
#define SU_CP 4416
#define SU_U 0
#define SU_K (4 * SU_CP)
#define SU_P (SU_K + 10400)
#define SU_D (SU_P + 2048)
#define SU_RAW (SU_D + 2592)
#define SU_CH (SU_RAW + 4128 + 32)
#define SST(i)
__device__ __forceinline__ void ssm_unit(unsigned char* sm, int d0, __amdgpu_buffer_rsrc_t rst, __amdgpu_buffer_rsrc_t rsx, unsigned char* xb, int fb, bool& uok,
                                         const u16* __restrict__ Dskip, int tid, int lane, int w) {
  unsigned char* cb = sm + w * SU_CH;
  const int dd = d0 + w;
  SST(0)
  if (!uok) {
    for (int it = 0; it < SPIN_CAP; ++it) {
      asm volatile("" ::: "memory");
      if ((int)__builtin_amdgcn_readfirstlane(__builtin_amdgcn_raw_buffer_load_b32(rsx, 0, XUDN, 16)) >= 15) break;
      asm volatile("s_sleep 8" ::: "memory");
    }
    asm volatile("" ::: "memory");
    uok = true;
  }
  f32x4 acc[3];
#pragma unroll
  for (int t = 0; t < 3; ++t) acc[t] = __builtin_bit_cast(f32x4, __builtin_amdgcn_raw_buffer_load_b128(rst, lane * 16, 2 * SSM_L * SSM_D * 2 + dd * 3072 + t * 1024, 16));
  const float dsk = bf2f(Dskip[dd]);
  u32x4 uv[4];
#pragma unroll
  for (int i = 0; i < 4; ++i) uv[i] = __builtin_amdgcn_raw_buffer_load_b128(rst, (tid + 256 * i) * 16, d0 * 4096, 16);
  const int r = lane & 31, h = lane >> 5;
  {
    const int lr = lane & 15, lg = lane >> 4;
    SST(2)
    u32* Kd = (u32*)(cb + SU_D);
#pragma unroll
    for (int e = 0; e < 4; ++e) {
      Kd[128 * (lg >> 1) + 8 * lr + 4 * (lg & 1) + e] = split_hl(acc[0][e]);
      Kd[lg < 2 ? 256 + 64 * lg + 4 * lr + e : 384 + 32 * (2 * (lg - 2) + (e >> 1)) + 2 * lr + (e & 1)] = split_hl(acc[1][e]);
      if (lg < 2 || (lg == 2 && e == 0 && lr == 0)) Kd[512 + 16 * (4 * lg + e) + lr] = split_hl(acc[2][e]);
    }
    asm volatile("s_waitcnt lgkmcnt(0)" ::: "memory");
    unsigned char* kr = cb + SU_K + (lane + 1) * 80;
#pragma unroll
    for (int c8 = 0; c8 < 4; ++c8) {
      u32 kv[8];
#pragma unroll
      for (int e = 0; e < 8; ++e) kv[e] = Kd[kidx(32 * lane + 31 - 8 * c8 - e)];
      u32x4 hi, lo;
#pragma unroll
      for (int e = 0; e < 4; ++e) {
        hi[e] = __builtin_amdgcn_perm(kv[2 * e + 1], kv[2 * e], 0x05040100u);
        lo[e] = __builtin_amdgcn_perm(kv[2 * e + 1], kv[2 * e], 0x07060302u);
      }
      *(u32x4*)(kr + 16 * c8) = hi;
      if (SSM_LO) *(u32x4*)(kr + 5200 + 16 * c8) = lo;
    }
    if (lane < 8) *(u32x4*)(cb + SU_K + (lane >> 2) * 5200 + (lane & 3) * 16) = (u32x4){0u, 0u, 0u, 0u};
  }
  SST(3)
#pragma unroll
  for (int i = 0; i < 4; ++i) {
    const int l = 2 * (tid + 256 * i) + 8;
    *(u32*)(sm + 0 * SU_CH + SU_RAW + l * 2) = __builtin_amdgcn_perm(uv[i][2], uv[i][0], 0x05040100u);
    *(u32*)(sm + 1 * SU_CH + SU_RAW + l * 2) = __builtin_amdgcn_perm(uv[i][2], uv[i][0], 0x07060302u);
    *(u32*)(sm + 2 * SU_CH + SU_RAW + l * 2) = __builtin_amdgcn_perm(uv[i][3], uv[i][1], 0x05040100u);
    *(u32*)(sm + 3 * SU_CH + SU_RAW + l * 2) = __builtin_amdgcn_perm(uv[i][3], uv[i][1], 0x07060302u);
  }
  if (lane < 2) *(u32x4*)(cb + SU_RAW + lane * (4096 + 16)) = (u32x4){0u, 0u, 0u, 0u};
  LBAR();
  {
    if (lane < 20) *(u32x4*)(cb + SU_U + (lane / 5) * SU_CP + (lane % 5) * 16) = (u32x4){0u, 0u, 0u, 0u};
#pragma unroll
    for (int i = 0; i < 5; ++i) {
      const int g = lane + 64 * i;
      if (g < 257) {
        const u32x4 a = *(const u32x4*)(cb + SU_RAW + g * 16);
        const u32x2 c = *(const u32x2*)(cb + SU_RAW + g * 16 + 16);
        unsigned char* dst = cb + SU_U + 80 + g * 16;
        *(u32x4*)(dst) = a;
        *(u32x4*)(dst + SU_CP) = (u32x4){__builtin_amdgcn_alignbyte(a[1], a[0], 2), __builtin_amdgcn_alignbyte(a[2], a[1], 2), __builtin_amdgcn_alignbyte(a[3], a[2], 2), __builtin_amdgcn_alignbyte(c[0], a[3], 2)};
        *(u32x4*)(dst + 2 * SU_CP) = (u32x4){a[1], a[2], a[3], c[0]};
        *(u32x4*)(dst + 3 * SU_CP) = (u32x4){__builtin_amdgcn_alignbyte(a[2], a[1], 2), __builtin_amdgcn_alignbyte(a[3], a[2], 2), __builtin_amdgcn_alignbyte(c[0], a[3], 2), __builtin_amdgcn_alignbyte(c[1], c[0], 2)};
      }
    }
  }
  asm volatile("s_waitcnt lgkmcnt(0)" ::: "memory");
  SST(4)
  f32x16 cv0, cv1;
  {
    const int m0 = 48 - 15 + 8 * h + r;
    const int cbo = (int)(size_t)(LAS unsigned char*)cb;
    int ua = cbo + SU_U + (m0 & 3) * SU_CP + (m0 & ~3) * 2 - 32;
    const int kb = cbo + SU_K + 16 * h;
    int a1 = kb + (33 + r) * 80, a0 = kb + (1 + r) * 80, cnt = 32;
#define CRA(S, O) "ds_read_b64 " RNG("v", S, 2) ", %[ua] offset:" STR(O) "\n\t" "ds_read_b64 " RNG("v", (S) + 2, 2) ", %[ua] offset:" STR((O) + 8) "\n\t"
#define CRB(S, ad, O) "ds_read_b128 " RNG("v", S, 4) ", " ad " offset:" STR(O) "\n\t"
#define CMF(C, A, B) "v_mfma_f32_32x32x16_bf16 " RNG("v", C, 16) ", " RNG("v", A, 4) ", " RNG("v", B, 4) ", " RNG("v", C, 16) "\n\t"
#define CUPD "v_add_u32 %[ua], 64, %[ua]\n\t" "v_subrev_u32 %[a1], 80, %[a1]\n\t" "v_subrev_u32 %[a0], 80, %[a0]\n\t" "v_max_i32 %[a0], %[a0], %[kb]\n\t"
#define CHALF(S, N, O, UPD)                                                                       \
    WLG(5) CMF(64, S, (S) + 8) UPD CRA(N, O)                                                      \
    WLG(6) CMF(80, S, (S) + 16) CRB((N) + 8, "%[a0]", O) CRB((N) + 16, "%[a1]", O)                \
    WLG(6) CMF(80, (S) + 4, (S) + 8) CRA((N) + 4, (O) + 2048)                                     \
    WLG(7) CMF(64, S, (S) + 12) CRB((N) + 12, "%[a0]", (O) + 5200)                                \
    WLG(7) CMF(80, S, (S) + 20) CRB((N) + 20, "%[a1]", (O) + 5200)                                \
    CMF(80, (S) + 4, (S) + 12)
    asm volatile(
        CRA(96, 32) CRB(104, "%[a0]", 32) CRB(112, "%[a1]", 32) CRA(100, 32 + 2048) CRB(108, "%[a0]", 32 + 5200) CRB(116, "%[a1]", 32 + 5200)
        ZER(64) ZER(68) ZER(72) ZER(76) ZER(80) ZER(84) ZER(88) ZER(92)
        ".Lc%=:\n\t"
        CHALF(96, 120, 0, "")
        CHALF(120, 96, 32, CUPD)
        "s_sub_u32 %[cnt], %[cnt], 1\n\t"
        "s_cmp_lg_u32 %[cnt], 0\n\t"
        "s_cbranch_scc1 .Lc%=\n\t"
        "s_nop 7\n\t" "s_nop 3\n\t" WLG(0)
        : "=&{v[64:79]}"(cv0), "=&{v[80:95]}"(cv1), [ua] "+v"(ua), [a0] "+v"(a0), [a1] "+v"(a1), [cnt] "+s"(cnt)
        : [kb] "v"(kb)
        : CL96, CL112, CL120, CL8(128, 129, 130, 131, 132, 133, 134, 135), CL8(136, 137, 138, 139, 140, 141, 142, 143),
          CL8(144, 145, 146, 147, 148, 149, 150, 151), CL8(152, 153, 154, 155, 156, 157, 158, 159), "scc", "memory");
  }
  SST(5)
  {
    u16* yo = (u16*)(cb + SU_K);
#pragma unroll
    for (int g = 0; g < 2; ++g)
#pragma unroll
      for (int i = 0; i < 16; i += 4) {
        const int rr = 2 * i + 4 * h, a = 32 * g + r;
        const u32x2 uu = *(const u32x2*)(cb + SU_U + (48 + 32 * a + rr) * 2);
        float y[4];
#pragma unroll
        for (int e = 0; e < 4; ++e) {
          const float uf = __uint_as_float((e & 1) ? (uu[e >> 1] & 0xffff0000u) : (uu[e >> 1] << 16));
          y[e] = rbf_hw(g ? cv1[i + e] : cv0[i + e]) + rbf_hw(uf * dsk);
        }
        *(u32x2*)(yo + a * 40 + rr) = (u32x2){pack2(y[0], y[1]), pack2(y[2], y[3])};
      }
  }
  LBAR();
  SST(6)
#pragma unroll
  for (int i = 0; i < 4; ++i) {
    const int l = 2 * (tid + 256 * i);
    u32 c[4];
#pragma unroll
    for (int j = 0; j < 4; ++j) c[j] = *(const u32*)(sm + j * SU_CH + SU_K + ((l >> 5) * 40 + (l & 31)) * 2);
    __builtin_amdgcn_raw_buffer_store_b128((u32x4){__builtin_amdgcn_perm(c[1], c[0], 0x05040100u), __builtin_amdgcn_perm(c[3], c[2], 0x05040100u),
                                                   __builtin_amdgcn_perm(c[1], c[0], 0x07060302u), __builtin_amdgcn_perm(c[3], c[2], 0x07060302u)},
                                           rst, l * 8, SSM_L * SSM_D * 2 + d0 * 4096, 16);
  }
  asm volatile("s_waitcnt vmcnt(0)" ::: "memory");
  LBAR();
  if (tid == 0) __hip_atomic_fetch_add((u32*)(xb + XYDN), 1u, RLX_AGENT);
  SST(7)
}

#define L_TOTAL 163840
__device__ __forceinline__ int ldw(__amdgpu_buffer_rsrc_t rs, int off) {
  asm volatile("" ::: "memory");
  return __builtin_amdgcn_readfirstlane(__builtin_amdgcn_raw_buffer_load_b32(rs, 0, off, 16));
}
#define GF(rt) "v[64+8*(xk&7)+4*" #rt ":64+8*(xk&7)+4*" #rt "+3]"
#define GW2 "v[128+4*xk:128+4*xk+3]"
#define GW3 "v[24+4*(xk&1):24+4*(xk&1)+3]"
#define GLOAD(S, K) "s_xor_b32 %[tq], " S ", 1024*(" K ")\n\t"
#define GTR(K) "ds_read_b128 v[24+4*((" K ")&1):24+4*((" K ")&1)+3], %[wl] offset:1024*((" K ")&31)\n\t"
#define GPACE 0
#define YAUX 16
#define UDELAY 100
#define GNOSSM 1
#define GNG 3
#define GS0 22
__device__ __forceinline__ void g_role(unsigned char* lds, const u16* __restrict__ Wo, u16* __restrict__ out, __amdgpu_buffer_rsrc_t rsx,
                                       __amdgpu_buffer_rsrc_t rsf, int fb, u32 tag, int grp, int sl, int w) {
  const int lane = fresh_lane();
  const int lr = lane & 15, lg = lane >> 4;
  const int rot = (2 * sl + 8 * w) & 31;
  const int ldsb = (int)(size_t)(LAS unsigned char*)lds;
  u32x32 xw[8], w2[4];
  LBAR();
  {
    const __amdgpu_buffer_rsrc_t rw = __builtin_amdgcn_make_buffer_rsrc((void*)Wo, 0, DM * HD * 2, 0x00020000);
    const int vq = (lane >> 3) * (DM * 2) + sl * 128 + (lane & 7) * 16;
    const int sq = (1024 * w + rot * 32) * (DM * 2);
#define GSLOT(c) ((c) < 8 ? (((c) & 3) == 3 ? 131072 + w * 8192 : w * 32768 + 8192 * (((c) & 3) + 1)) : (((c) - 8) % 3 == 2 ? 131072 + w * 8192 : w * 32768 + 8192 * (((c) - 8) % 3 + 2)))
#define GISSUE(c) _Pragma("unroll") for (int j = 0; j < 8; ++j)                                                        \
      asm volatile("s_mov_b32 m0, %0\n\ts_nop 3\n\tbuffer_load_dwordx4 %1, %2, %3 offen lds"                           \
                   :: "s"(ldsb + GSLOT(c) + j * 1024), "v"(vq), "s"(rw), "s"(sq ^ ((64 * (c) + 8 * j) * DM * 2)) : "memory");
    GISSUE(0) GISSUE(1) GISSUE(2) GISSUE(3)
    const int q = lr >> 2, p = lr & 3;
    u32x4 w3[16];
#pragma unroll
    for (int c = 0; c < 16; ++c) {
      if (c < 5) waitvm<24>(); else if (c < 14) waitvm<16>(); else if (c == 14) waitvm<8>(); else waitvm<0>();
      u32x4 f3[2];
#pragma unroll
      for (int s = 0; s < 2; ++s)
#pragma unroll
        for (int t = 0; t < 4; ++t) {
          const unsigned char* base = lds + GSLOT(c) + (32 * s + 8 * lg + q) * 128 + (t < 2 ? p * 16 + t * 8 : t * 32 + p * 8);
          const u32x2 t0 = __builtin_bit_cast(u32x2, __builtin_amdgcn_ds_read_tr16_b64_v4i16((LAS v4i16_t*)(base)));
          const u32x2 t1 = __builtin_bit_cast(u32x2, __builtin_amdgcn_ds_read_tr16_b64_v4i16((LAS v4i16_t*)(base + 512)));
          const int o = t < 2 ? ((2 * c + s) * 2 + t) * 4 : (2 * c + s) * 4;
          if (t < 2) { xw[o >> 5][(o & 31) + 0] = t0[0]; xw[o >> 5][(o & 31) + 1] = t0[1]; xw[o >> 5][(o & 31) + 2] = t1[0]; xw[o >> 5][(o & 31) + 3] = t1[1]; }
          else if (t == 2) { w2[o >> 5][(o & 31) + 0] = t0[0]; w2[o >> 5][(o & 31) + 1] = t0[1]; w2[o >> 5][(o & 31) + 2] = t1[0]; w2[o >> 5][(o & 31) + 3] = t1[1]; }
          else f3[s] = (u32x4){t0[0], t0[1], t1[0], t1[1]};
        }
      asm volatile("s_waitcnt lgkmcnt(0)" ::: "memory");
      if (c < 8) { *(u32x4*)(lds + w * 32768 + (2 * c) * 1024 + lane * 16) = f3[0]; *(u32x4*)(lds + w * 32768 + (2 * c + 1) * 1024 + lane * 16) = f3[1]; }
      else { w3[2 * (c - 8)] = f3[0]; w3[2 * (c - 8) + 1] = f3[1]; }
      if (GPACE) __builtin_amdgcn_s_sleep(GPACE);
      if (c < 4) { GISSUE(c + 4) } else if (c >= 5 && c < 13) { GISSUE(c + 3) }
    }
#pragma unroll
    for (int i = 0; i < 16; ++i) *(u32x4*)(lds + w * 32768 + (16 + i) * 1024 + lane * 16) = w3[i];
    LBAR();
  }
  wait_start(rsx, fb, tag, true);
  const int wl = ldsb + w * 32768 + lane * 16;
  const __amdgpu_buffer_rsrc_t rso = __builtin_amdgcn_make_buffer_rsrc((void*)out, 0, S_LEN * OUTLD * 2, 0x00020000);
  const int rw_ = ldsb + 131072 + w * 8192 + lane * 16, rr_ = ldsb + 131072 + w * 2048 + lane * 16;
  const int oa = ((16 * (w & 1) + lr) * OUTLD + 64 * sl + (w < 2 ? 8 * lg : 32 + 4 * lg)) * 2, ob = oa + (w < 2 ? 8 : 32);
  const int hq = (grp + GNG - (62 - GS0) % GNG) % GNG - 1, hf = hq < 2 ? hq : -1;
#define GRW(i) "ds_write_b128 %[rw], " RNG("v", 32 + 4 * (i), 4) " offset:" STR(1024 * (i)) "\n\t"
#define GRR(i) "ds_read_b128 " RNG("v", 32 + 4 * (i), 4) ", %[rr] offset:" STR(8192 * ((i) >> 1) + 1024 * ((i) & 1)) "\n\t"
#define GNXT(D, S) "s_cmp_lt_u32 " S ", " STR(GS0) "\n\t" "s_cselect_b32 " D ", 1, " STR(GNG) "\n\t" "s_add_u32 " D ", " D ", " S "\n\t"
#define GDN(S) "s_min_u32 s41, " S ", 63\n\t" "s_lshl_b32 s41, s41, 5\n\t" "v_add_u32 v23, s41, %[u0]\n\t" "v_lshrrev_b32 v22, 6, v23\n\t" \
               "v_and_b32 v23, 63, v23\n\t" "v_lshlrev_b32 v23, 7, v23\n\t" "s_lshr_b32 s41, s41, 6\n\t" "s_max_u32 s41, s41, 29\n\t" "s_lshl_b32 s41, s41, 13\n\t" "s_add_u32 s41, s41, %[pa]\n\t" \
               "buffer_load_dword v23, v23, %[rs], s41 offen sc1\n\t"
#define GOK(D) "v_cmp_le_i32 vcc, v23, v22\n\t" "s_cmp_eq_u64 vcc, 0\n\t" "s_cselect_b32 " D ", 1, 0\n\t"
static_assert(NAW == 64, "shifts above");
#define GEPI(MID, OA, OB) \
      GRW(0) GRW(1) GRW(2) GRW(3) GRW(4) GRW(5) GRW(6) GRW(7) \
      MID \
      WLG(0) "s_barrier\n\t" \
      GRR(0) GRR(1) GRR(2) GRR(3) GRR(4) GRR(5) GRR(6) GRR(7) \
      WLG(4) PKA(32, 32, 40) PKA(34, 34, 42) PKA(36, 36, 44) PKA(38, 38, 46) \
      WLG(0) PKA(48, 48, 56) PKA(50, 50, 58) PKA(52, 52, 60) PKA(54, 54, 62) \
      PKA(32, 32, 48) PKA(34, 34, 50) PKA(36, 36, 52) PKA(38, 38, 54) \
      "v_cvt_pk_bf16_f32 v20, v32, v33\n\t" "v_cvt_pk_bf16_f32 v21, v34, v35\n\t" "v_cvt_pk_bf16_f32 v18, v36, v37\n\t" "v_cvt_pk_bf16_f32 v19, v38, v39\n\t" \
      "buffer_store_dwordx2 v[20:21], " OA ", %[ro], s42 offen\n\t" \
      "buffer_store_dwordx2 v[18:19], " OB ", %[ro], s42 offen\n\t"
#define GH "v[64+4*(xk&15):64+4*(xk&15)+3]"
  asm volatile(
      "s_nop 4\n\t"
      "v_add_u32 v16, 131072, %[va]\n\t" "v_sub_u32 v15, %[ob], %[oa]\n\t" "v_add_u32 v15, v15, %[oc]\n\t"
      "s_mov_b32 s36, %[s0]\n\t" "s_mov_b32 s43, " STR(SPIN_CAP) "\n"
      ".Lp%=:\n\t"
      GDN("s36") WVM(0) GOK("s40")
      "s_cbranch_scc1 .Lr%=\n\t"
      "s_sleep 2\n\t" "s_sub_u32 s43, s43, 1\n\t" "s_cmp_gt_i32 s43, 0\n\t" "s_cbranch_scc1 .Lp%=\n"
      ".Lr%=:\n\t"
      GNXT("s37", "s36") GDN("s37")
      "s_lshl_b32 s38, s36, 18\n\t" "s_add_u32 s38, s38, %[gb]\n\t"
      ".set xk, 0\n\t.rept 8\n\t" GLOAD("s38", "xk")
      "buffer_load_dwordx4 " GF(0) ", %[va], %[rf], %[tq] offen sc1\n\t" "buffer_load_dwordx4 " GF(1) ", v16, %[rf], %[tq] offen sc1\n\t"
      ".set xk, xk+1\n\t.endr\n\t"
      WVM(16) GOK("s40")
      ".Lb%=:\n\t"
      GNXT("s37", "s36") GNXT("s41", "s37") GDN("s41") GTR("0")
      "s_cmp_lt_u32 s37, 63\n\t" "s_cselect_b32 s40, s40, 0\n\t"
      "s_cmp_lg_u32 s40, 0\n\t" "s_cselect_b32 s39, s37, s36\n\t"
      "s_lshl_b32 s39, s39, 18\n\t" "s_add_u32 s39, s39, %[gb]\n\t" "s_lshl_b32 s38, s36, 18\n\t" "s_add_u32 s38, s38, %[gb]\n\t"
      "s_lshl_b32 s42, s36, 17\n\t"
      "buffer_load_dword v17, %[vt], %[rf], s39 offen sc1\n\t"
      ".set xk, 0\n\t.rept 32\n\t"
      "s_waitcnt vmcnt(14)\n\t"
      XM1(XA(32, 0), XW(0, 0), GF(0), 1) GTR("xk+1") XM1(XA(32, 4), XW(0, 1), GF(0), 1)
      ".if xk < 24\n\t" GLOAD("s38", "xk+8") ".else\n\t" GLOAD("s39", "xk-24") ".endif\n\t"
      XM1(XA(32, 8), XW(0, 0), GF(1), 1) XM1(XA(32, 12), XW(0, 1), GF(1), 1)
      XM1(XA(32, 16), GW2, GF(0), 1) "s_waitcnt lgkmcnt(1)\n\t" XM1(XA(32, 20), GW3, GF(0), 1)
      "buffer_load_dwordx4 " GF(0) ", %[va], %[rf], %[tq] offen sc1\n\t"
      XM1(XA(32, 24), GW2, GF(1), 1) XM1(XA(32, 28), GW3, GF(1), 1)
      "buffer_load_dwordx4 " GF(1) ", v16, %[rf], %[tq] offen sc1\n\t"
      ".if xk == 16\n\t" "s_barrier\n\t" ".endif\n\t"
      ".set xk, xk+1\n\t.endr\n\t"
      GEPI(GOK("s41"), "%[oa]", "%[ob]")
      "s_mov_b32 s36, s37\n\t"
      "s_cmp_lg_u32 s40, 0\n\t" "s_mov_b32 s40, s41\n\t" "s_cbranch_scc1 .Lb%=\n\t"
      "s_cmp_lt_u32 s36, 63\n\t" "s_cbranch_scc1 .Lp%=\n\t"
      "s_cmp_lt_i32 %[hf], 0\n\t" "s_cbranch_scc1 .Le%=\n\t"
      "s_mov_b32 s36, 63\n"
      ".Lh%=:\n\t"
      GDN("s36") WVM(0) GOK("s40")
      "s_cbranch_scc1 .Li%=\n\t"
      "s_sleep 2\n\t" "s_sub_u32 s43, s43, 1\n\t" "s_cmp_gt_i32 s43, 0\n\t" "s_cbranch_scc1 .Lh%=\n"
      ".Li%=:\n\t"
      "s_lshl_b32 s38, %[hf], 17\n\t" "s_add_u32 s38, s38, 63*262144\n\t" "s_add_u32 s38, s38, %[gb]\n\t"
      ".set xk, 0\n\t.rept 16\n\t" GLOAD("s38", "xk") "buffer_load_dwordx4 " GH ", %[va], %[rf], %[tq] offen sc1\n\t" ".set xk, xk+1\n\t.endr\n\t"
      GTR("0")
      "s_lshl_b32 s42, %[hf], 16\n\t" "s_add_u32 s42, s42, 63*131072\n\t"
      ".set xk, 0\n\t.rept 32\n\t"
      ".if xk < 17\n\t" "s_waitcnt vmcnt(15)\n\t" ".else\n\t" "s_waitcnt vmcnt(31-xk)\n\t" ".endif\n\t"
      XM1(XA(32, 0), XW(0, 0), GH, 1) GTR("xk+1") XM1(XA(32, 4), XW(0, 1), GH, 1)
      XM1(XA(32, 16), GW2, GH, 1) "s_waitcnt lgkmcnt(1)\n\t" XM1(XA(32, 20), GW3, GH, 1)
      ".if xk < 16\n\t" GLOAD("s38", "xk+16") "buffer_load_dwordx4 " GH ", %[va], %[rf], %[tq] offen sc1\n\t" ".endif\n\t"
      ".if xk == 16\n\t" "s_barrier\n\t" ".endif\n\t"
      ".set xk, xk+1\n\t.endr\n\t"
      "s_nop 7\n\t"
      GEPI("", "%[oc]", "v15")
      ".Le%=:\n\t"
      WVM(0)
      : [tq] "=&s"(sl)
      : [rs] "s"(rsx), [s0] "s"(__builtin_amdgcn_readfirstlane(grp ? GS0 + grp : 0)), [gb] "s"(FO_O - 32768 + (32 * w + rot) * 1024), [va] "v"(lane * 16 + 32768),
        [rf] "s"(rsf), [wl] "v"(wl), [rw] "v"(rw_), [rr] "v"(rr_), [oa] "v"(oa), [ob] "v"(ob), [ro] "s"(rso), [u0] "v"(8 * w + (lane & 7)), [pa] "s"(PGO_A - 29 * 8192), [hf] "s"(__builtin_amdgcn_readfirstlane(hf)), [oc] "v"((w & 1) ? OOR - 64 : oa), [vt] "v"((sl >> 3) * 131072 + 32768 + (sl & 7) * 4096 + lane * 64 - rot * 1024),
        "{a[0:31]}"(xw[0]), "{a[32:63]}"(xw[1]), "{a[64:95]}"(xw[2]), "{a[96:127]}"(xw[3]),
        "{a[128:159]}"(xw[4]), "{a[160:191]}"(xw[5]), "{a[192:223]}"(xw[6]), "{a[224:255]}"(xw[7]),
        "{v[128:159]}"(w2[0]), "{v[160:191]}"(w2[1]), "{v[192:223]}"(w2[2]), "{v[224:255]}"(w2[3])
      : "v15", "v16", "v17", "v18", "v19", "v20", "v21", "v22", "v23", "vcc", CL8(24, 25, 26, 27, 28, 29, 30, 31), CL8(32, 33, 34, 35, 36, 37, 38, 39), CL8(40, 41, 42, 43, 44, 45, 46, 47),
        CL8(48, 49, 50, 51, 52, 53, 54, 55), CL8(56, 57, 58, 59, 60, 61, 62, 63), CL64, CL72, CL80, CL88, CL96, CL112, CL120,
        "s36", "s37", "s38", "s39", "s40", "s41", "s42", "s43", "scc", "memory");
}
static_assert(4 * SU_CH <= L_TOTAL && 4 * XSLOT <= L_TOTAL, "lds");
__global__ void __launch_bounds__(256, 1)
main_kernel(const u16* __restrict__ x, const u16* __restrict__ mem0, const u16* __restrict__ Wq, const u16* __restrict__ Wk, const u16* __restrict__ Wv, const u16* __restrict__ Wo,
            unsigned char* ws, const u16* __restrict__ u, const u16* __restrict__ A_re, const u16* __restrict__ A_im, const u16* __restrict__ B_re,
            const u16* __restrict__ B_im, const u16* __restrict__ C_re, const u16* __restrict__ C_im,
            const u16* __restrict__ log_dt, const u16* __restrict__ Dskip, u16* __restrict__ out
            ) {
  __shared__ __attribute__((aligned(8192))) unsigned char lds[L_TOTAL];
  const int w = __builtin_amdgcn_readfirstlane(threadIdx.x >> 6);
  u64 pc0;
  int clen;
  {
    asm volatile("s_getpc_b64 %0\n.Lkpc0:\n\ts_mov_b32 %1, .Lkpc1-.Lkpc0" : "=s"(pc0), "=s"(clen));
  }
#define LOCAL_IDS const int lane = fresh_lane(), tid = w * 64 + lane; (void)tid;
  const int si = (blockIdx.x >> 3) * 4 + (blockIdx.x & 3);
  unsigned char* xb = (unsigned char*)(((size_t)ws + XPAGE - 1) & ~(size_t)(XPAGE - 1));
  const int fb = (int)(ws + WS_F - xb);
  u32 tag;
  asm volatile("s_load_dword %0, %1, 0x0\n\ts_waitcnt lgkmcnt(0)\n\ts_add_u32 %0, %0, 256" : "=s"(tag) : "s"(ws + WS_F) : "scc", "memory");
  const __amdgpu_buffer_rsrc_t rsx = __builtin_amdgcn_make_buffer_rsrc(xb, 0, fb + (int)WS_F_SZ, 0x00020000);
  const __amdgpu_buffer_rsrc_t rsf = __builtin_amdgcn_make_buffer_rsrc(ws + WS_QX, 0, (int)(4 * WS_QKV_SZ + 2 * WS_KM_SZ), 0x00020000);
  const __amdgpu_buffer_rsrc_t rst = __builtin_amdgcn_make_buffer_rsrc(ws + WS_UT, 0, 2 * SSM_L * SSM_D * 2 + SSM_D * 3072, 0x00020000);
  if ((blockIdx.x & 7) < 4) {
    { LOCAL_IDS
    r_role(lds, si, mem0, Wq, Wk, Wv, Wo, fb, tag, pc0, clen, rsx, rsf, rst, SsmPtrs{A_re, A_im, B_re, B_im, C_re, C_im, log_dt}, tid, lane, w
           ); }
    LOCAL_IDS
    {
      for (int it = 0; it < SPIN_CAP; ++it) {
        if (ldw(rsx, XYDN) >= 255) break;
        asm volatile("s_sleep 8" ::: "memory");
      }
      asm volatile("" ::: "memory");
      asm volatile(".Lkpc1:" ::: "memory");
      unsigned char* im = lds + 65536;
      const __amdgpu_buffer_rsrc_t rso = __builtin_amdgcn_make_buffer_rsrc((void*)out, 0, S_LEN * OUTLD * 2, 0x00020000);
      u32x4 t[8];
#pragma unroll
      for (int i = 0; i < 8; ++i) { const int p = i * 256 + tid; t[i] = __builtin_amdgcn_raw_buffer_load_b128(rst, (p >> 3) * 16384 + (p & 7) * 16, SSM_L * SSM_D * 2 + si * 128, 16); }
#pragma unroll
      for (int i = 0; i < 8; ++i) {
        const int p = i * 256 + tid, un = p >> 3, rp = p & 7;
        *(u32x2*)(im + (2 * rp) * 2064 + un * 8) = (u32x2){t[i][0], t[i][1]};
        *(u32x2*)(im + (2 * rp + 1) * 2064 + un * 8) = (u32x2){t[i][2], t[i][3]};
      }
      LBAR();
#pragma unroll
      for (int i = 0; i < 8; ++i) {
        const int p = i * 256 + tid;
        __builtin_amdgcn_raw_buffer_store_b128(*(const u32x4*)(im + (p >> 7) * 2064 + (p & 127) * 16), rso, ((p >> 7) * OUTLD + DM + (p & 127) * 8) * 2, si * 16 * OUTLD * 2, YAUX);
      }
    }
  } else {
    const int g = blockIdx.x & 3, li = blockIdx.x >> 3;
    const int xi = g == 0 ? (li < 16 ? li : -1) : (li < 26 + (g < 3) ? 16 + 27 * (g - 1) + li : -1);
    const int ei = g == 0 ? li - 16 : 16 + 5 * (g - 1) + (31 - li);
    if (xi >= 0) {
      LOCAL_IDS
      const int head = xi & 31, mi = xi >> 5;
      u32* prog = (u32*)(xb + PGO_X + xi * 128);
      if (mi == 1) x_role<true>(lds, x, Wv, FO_V, head, rsf, prog, tag, lane, w);
      else x_role<false>(lds, x, mi == 0 ? Wk : Wq, mi == 0 ? FO_K : FO_Q, head, rsf, prog, tag, lane, w);
    } else {
      LOCAL_IDS
      u32 acc = 0;
#pragma unroll
      for (int i = 0; i < 4; ++i) acc += *(const u32*)(x + (size_t)((i * 32 + ei) * 256 + tid) * 64);
      {
        const __amdgpu_buffer_rsrc_t rwo = __builtin_amdgcn_make_buffer_rsrc((void*)Wo, 0, DM * HD * 2, 0x00020000);
        const int ldsb = (int)(size_t)(LAS unsigned char*)lds;
#pragma unroll 1
        for (int c = 0; c < 4; ++c) { dma16(rwo, lane * 16, ((ei * 4 + w) * 4 + c) * 16384, 1024, ldsb + w * 32768 + (c & 1) * 16384); waitvm<32>(); }
      }
      if (ei == 0 && tid < 16) acc += *(const u32*)(Dskip + tid * 64);
      asm volatile("" :: "v"(acc));
      if (ei >= 16) {
      { const u32 t0 = rt32(); for (int i = 0; i < 1000 && (int)(rt32() - t0) < UDELAY; ++i) asm volatile("s_sleep 16" ::: "memory"); }
      asm volatile("s_waitcnt vmcnt(0)\n\ts_barrier" ::: "memory");
      for (int ps = 0; ps < 2; ++ps) {
      const int r0 = ((ei - 16) * 2 + ps) * 64;
#pragma unroll
      for (int hb = 0; hb < 2; ++hb) {
        u32x4 t[16];
#pragma unroll
        for (int i = 0; i < 16; ++i) t[i] = *(const u32x4*)(u + (size_t)r0 * SSM_D + ((hb * 16 + i) * 256 + tid) * 8);
#pragma unroll
        for (int i = 0; i < 16; ++i) { const int p = (hb * 16 + i) * 256 + tid; *(u32x4*)(lds + (p >> 7) * 2064 + (p & 127) * 16) = t[i]; }
      }
      LBAR();
      for (int i = 0; i < 32; ++i) {
        const int p = i * 256 + tid, un = p >> 5, rp = p & 31;
        const u32x2 a = *(const u32x2*)(lds + (2 * rp) * 2064 + un * 8), c = *(const u32x2*)(lds + (2 * rp + 1) * 2064 + un * 8);
        __builtin_amdgcn_raw_buffer_store_b128((u32x4){a[0], a[1], c[0], c[1]}, rst, un * 16384 + rp * 16, r0 * 8, 16);
      }
      asm volatile("s_waitcnt vmcnt(0) lgkmcnt(0)\n\ts_barrier" ::: "memory");
      }
      wait_start(rsx, fb, tag, true);
      if (tid == 0) __hip_atomic_fetch_add((u32*)(xb + XUDN), 1u, RLX_AGENT);
      }
    }
    asm volatile("s_waitcnt lgkmcnt(0)\n\ts_barrier" :: "s"(tag) : "memory");
    if (w == 0 && fresh_lane() == 0) __builtin_amdgcn_raw_buffer_store_b32(tag, rsx, 0, PGO_S + si * 128, 16);
    const bool isg = g == 0 ? li >= 16 : (g >= 5 - GNG && li < 16);
    if (xi >= 0 && !(GNOSSM && isg)) {
      LOCAL_IDS
      bool uok = false;
      wait_start(rsx, fb, tag, true);
      while (true) {
        LBAR();
        if (tid == 0) *(u32*)(lds + L_TOTAL - 16) = __hip_atomic_fetch_add((u32*)(xb + XTKT), 1u, RLX_AGENT) + 1u;
        LBAR();
        const int un = __builtin_amdgcn_readfirstlane(*(const u32*)(lds + L_TOTAL - 16));
        if (un >= 256) break;
        ssm_unit(lds, 4 * un, rst, rsx, xb, fb, uok, Dskip, tid, lane, w);
        if (isg) break;
      }
    }
    if (isg) g_role(lds, Wo, out, rsx, rsf, fb, tag, g ? g + GNG - 4 : 0, li & 15, w);
    else {
      const int lane = fresh_lane();
      int sh = (ei - 16) * 4 + w, lim = (NSEG - 4) * NH, wi = sh, dn = 0;
      bool go = xi < 0;
      while (true) {
        if (go) a_role(rsx, rsf, sh, lim, wi, dn, lane, fb, tag);
        if (xi >= 0 && (w >= 2 || go)) break;
        const int t = __builtin_amdgcn_readfirstlane(lane == 0 ? __hip_atomic_fetch_add((u32*)(xb + XTK2), 1u, RLX_AGENT) + 1u : 0u);
        if (t >= 128) break;
        wi = 64 + t; sh = (NSEG - 4) * NH + t; lim = sh + 1; dn = 31; go = true;
      }
    }
  }
}

extern "C" void kernel_launch(void* const* d_in, const int* in_sizes, int n_in, void* d_out, int out_size,
                              void* d_ws, size_t ws_size, hipStream_t stream) {
  const u16* x = (const u16*)d_in[0];
  const u16* mem0 = (const u16*)d_in[1];
  const u16* Wq = (const u16*)d_in[2];
  const u16* Wk = (const u16*)d_in[3];
  const u16* Wv = (const u16*)d_in[4];
  const u16* Wo = (const u16*)d_in[5];
  const u16* u = (const u16*)d_in[7];
  const u16* A_re = (const u16*)d_in[8];
  const u16* A_im = (const u16*)d_in[9];
  const u16* B_re = (const u16*)d_in[10];
  const u16* B_im = (const u16*)d_in[11];
  const u16* C_re = (const u16*)d_in[12];
  const u16* C_im = (const u16*)d_in[13];
  const u16* log_dt = (const u16*)d_in[14];
  const u16* Dskip = (const u16*)d_in[15];
  u16* out = (u16*)d_out;
  char* ws = (char*)d_ws;

  main_kernel<<<256, 256, 0, stream>>>(x, mem0, Wq, Wk, Wv, Wo, (unsigned char*)ws, u, A_re, A_im, B_re, B_im, C_re, C_im, log_dt, Dskip, out
      );
}
```

```cpp
#include <hip/hip_runtime.h>
#include <hip/hip_bf16.h>
#include <stdint.h>

#define S_LEN 2048
#define DM 1024
#define NH 32
#define DH 128
#define HD 4096
#define MEM 8
#define SEG 32
#define NSEG 64
#define LTOK 40
#define SSM_L 2048
#define SSM_D 1024
#define SSM_N 128
#define OUTLD 2048

typedef unsigned short u16;
typedef unsigned int u32;
typedef unsigned long long u64;
typedef __attribute__((ext_vector_type(8))) short bf16x8;
typedef __attribute__((ext_vector_type(4))) float f32x4;
typedef __attribute__((ext_vector_type(4))) unsigned int u32x4;
typedef __attribute__((ext_vector_type(2))) unsigned int u32x2;
typedef __bf16 bf16x2_t __attribute__((ext_vector_type(2)));
typedef short v4i16_t __attribute__((ext_vector_type(4)));

#define SENT 0xFFFFFFFFu
#define RLX_AGENT __ATOMIC_RELAXED, __HIP_MEMORY_SCOPE_AGENT

__device__ __forceinline__ float bf2f(u16 v) { return __uint_as_float(((u32)v) << 16); }
__device__ __forceinline__ u16 f2bf(float f) {
  u32 u = __float_as_uint(f);
  u32 r = (u + 0x7fffu + ((u >> 16) & 1u)) >> 16;
  if ((u & 0x7fffffffu) > 0x7f800000u) r = 0x7fc0u;
  return (u16)r;
}
__device__ __forceinline__ float rbf(float f) { return bf2f(f2bf(f)); }

#define TR 8
#define TP 188
#define TDB 65
#define TDC 115
#define NR1 4
#define XPAGE (2 << 20)
#define XRING 3
#define XLP(p, L) ((((L) >> 13) << 15) + ((p) << 14) + ((L) & 8191))
#define XO_R3 131072
#define XR1A(x, L) XLP((x) & 1, ((x) >> 1) * 16384 + (L))
#define XR3A(rg, L) (XO_R3 + XLP(rg, L))
#define XSB(s) (((s) % XRING) == 2 ? 1048576 : ((s) % XRING) * 262144)
#define XR2B 524288
#define XR2S 1048576
#define XR2(j) (((((j) & 31) + (((j) >> 6) << 5)) / 6 << 15) + (((((j) >> 5) & 1) ^ 1) << 14) + ((((j) & 31) + (((j) >> 6) << 5)) % 6) * 1280)
#define XSYN (1048576 + 262144)
#define PGO_R (XSYN + 32768)
#define XCAL (PGO_R + 32768)
static_assert(XCAL + 16384 <= XPAGE, "exchange page");
#define WS_X 0ull
#define WS_X_SZ ((u64)2 * XPAGE)
#define WS_F (WS_X + WS_X_SZ)
#define XEPO (fb)
#define XSTA (fb + 128)
#define PGO_X (fb + 256)
#define PGO_Y (PGO_X + 96 * 128)
#define PGO_S (PGO_Y + 128 * 128)
#define PGO_D (PGO_S + 128 * 128)
#define PGO_A (PGO_D + 128 * 128)
#define XTKT (PGO_A + 192 * 128)
#define XUDN (XTKT + 128)
#define XYDN (XUDN + 128)
#define XTK2 (XYDN + 128)
#define WS_F_SZ ((u64)(2 + 96 + 128 + 128 + 128 + 192 + 8) * 128)
#define WS_XCH_END (WS_F + 131072)
#define WS_QX (WS_XCH_END)
#define WS_QKV_SZ ((u64)S_LEN * HD * 2)
#define WS_KX (WS_QX + WS_QKV_SZ)
#define WS_VX (WS_KX + WS_QKV_SZ)
#define WS_OX (WS_VX + WS_QKV_SZ)
#define WS_KM (WS_OX + WS_QKV_SZ)
#define WS_KM_SZ ((u64)NSEG * 8 * HD * 2)
#define WS_VM (WS_KM + WS_KM_SZ)
#define WS_UT (WS_VM + WS_KM_SZ)
#define WS_YT (WS_UT + (u64)SSM_L * SSM_D * 2)
#define WS_KD (WS_YT + (u64)SSM_L * SSM_D * 2)
#define WS_TOTAL (WS_KD + (u64)SSM_D * 3072)

#define SPIN_CAP 400000
#define TRY_CAP 20000
#define LAS __attribute__((address_space(3)))
#define LBAR() asm volatile("s_waitcnt lgkmcnt(0)\n\ts_barrier" ::: "memory")
__device__ __forceinline__ u32 max4u(u32x4 v) { return max(max(v[0], v[1]), max(v[2], v[3])); }
typedef float f32x2_t __attribute__((ext_vector_type(2)));
__device__ __forceinline__ u32 pack2(float a, float b) {
  f32x2_t v = {a, b};
  bf16x2_t r = __builtin_convertvector(v, bf16x2_t);
  return __builtin_bit_cast(u32, r);
}
__device__ __forceinline__ float rbf_hw(float a) { return __uint_as_float(pack2(a, a) & 0xffff0000u); }
__device__ __forceinline__ u32 nosent(u32 v) { return min(v, 0xFFFFFFFEu); }
template <int N> __device__ __forceinline__ void waitvm() { asm volatile("s_waitcnt vmcnt(%0)" ::"n"(N) : "memory"); }
typedef __attribute__((ext_vector_type(16))) float f32x16;
__device__ __forceinline__ u32 split_hl(float kv) {
  const u32 h = pack2(kv, kv) & 0xffffu;
  const float r1 = kv - __uint_as_float(h << 16);
  return h | (pack2(r1, r1) & 0xffff0000u);
}
__device__ __forceinline__ int kidx(int idx) {
  const int sh = idx < 256 ? 0 : (idx < 512 ? 1 : (idx < 1024 ? 2 : 3));
  const int q = sh ? ((idx + (1 << (sh - 1)) - 1 + ((idx >> sh) & 1)) >> sh) : idx;
  return q + 128 * sh;
}

#define FO_Q 0
#define FO_K ((int)WS_QKV_SZ)
#define FO_V ((int)(2 * WS_QKV_SZ))
#define FO_O ((int)(3 * WS_QKV_SZ))
#define FO_KM ((int)(4 * WS_QKV_SZ))
#define FO_VM ((int)(4 * WS_QKV_SZ + WS_KM_SZ))
__device__ __forceinline__ u32x4 wait_start(__amdgpu_buffer_rsrc_t rsx, int fb, u32 tag, bool slow) {
  u32x4 st;
  for (int it = 0; it < SPIN_CAP; ++it) {
    asm volatile("" ::: "memory");
    st = __builtin_amdgcn_raw_buffer_load_b128(rsx, 0, XSTA, 16);
    if ((u32)__builtin_amdgcn_readfirstlane(st[3]) == tag) break;
    if (slow) asm volatile("s_sleep 20" ::: "memory"); else asm volatile("s_sleep 3" ::: "memory");
  }
  asm volatile("" ::: "memory");
  return st;
}
__device__ __forceinline__ int xprog(u32 v, u32 tag) { return v - tag <= 80u ? (int)(v - tag) : -1; }
__device__ __forceinline__ float xmax4(float m) {
  { auto r = __builtin_amdgcn_permlane16_swap(__float_as_uint(m), __float_as_uint(m), false, false); m = __builtin_fmaxf(__uint_as_float(r[0]), __uint_as_float(r[1])); }
  { auto r = __builtin_amdgcn_permlane32_swap(__float_as_uint(m), __float_as_uint(m), false, false); m = __builtin_fmaxf(__uint_as_float(r[0]), __uint_as_float(r[1])); }
  return m;
}
__device__ __forceinline__ float xsum4(float m) {
  { auto r = __builtin_amdgcn_permlane16_swap(__float_as_uint(m), __float_as_uint(m), false, false); m = __uint_as_float(r[0]) + __uint_as_float(r[1]); }
  { auto r = __builtin_amdgcn_permlane32_swap(__float_as_uint(m), __float_as_uint(m), false, false); m = __uint_as_float(r[0]) + __uint_as_float(r[1]); }
  return m;
}
#define MF16(a, b, c) __builtin_amdgcn_mfma_f32_16x16x32_bf16(__builtin_bit_cast(bf16x8, a), __builtin_bit_cast(bf16x8, b), c, 0, 0, 0)
__device__ __forceinline__ f32x4 rnd2(f32x4 v, float sc) {
  const u32 a = pack2(v[0], v[1]), b = pack2(v[2], v[3]);
  const u32 c = pack2(__uint_as_float(a << 16) * sc, __uint_as_float(a & 0xffff0000u) * sc), d = pack2(__uint_as_float(b << 16) * sc, __uint_as_float(b & 0xffff0000u) * sc);
  return (f32x4){__uint_as_float(c << 16), __uint_as_float(c & 0xffff0000u), __uint_as_float(d << 16), __uint_as_float(d & 0xffff0000u)};
}
struct XaOps { u32x4 q[2][4], k[2][4], v[8]; };
__device__ __forceinline__ void xa_load(XaOps& o, __amdgpu_buffer_rsrc_t rsf, int sh, int lane) {
#pragma unroll
  for (int i = 0; i < 8; ++i) {
    o.q[i >> 2][i & 3] = __builtin_amdgcn_raw_buffer_load_b128(rsf, lane * 16, FO_Q + (sh * 8 + i) * 1024, 16);
    o.k[i >> 2][i & 3] = __builtin_amdgcn_raw_buffer_load_b128(rsf, lane * 16, FO_K + (sh * 8 + i) * 1024, 16);
  }
#pragma unroll
  for (int dt = 0; dt < 8; ++dt) o.v[dt] = __builtin_amdgcn_raw_buffer_load_b128(rsf, lane * 16, FO_V + (sh * 8 + dt) * 1024, 16);
}
__device__ __forceinline__ void xa_compute(const XaOps& o, const u32x4 (&km)[4], const u32x2 (&vm)[8], __amdgpu_buffer_rsrc_t rsf, int sh, int lane) {
  const int lr = lane & 15, lg = lane >> 4;
  const float scale = 0.08838834764831845f, L2E = 1.4426950408889634f, NEG = -1000000000.0f;
  const f32x4 z = (f32x4){0, 0, 0, 0};
  f32x4 s00 = z, s0m = z, s10 = z, s11 = z, s1m = z;
#pragma unroll
  for (int ks = 0; ks < 4; ++ks) { s00 = MF16(o.k[0][ks], o.q[0][ks], s00); s10 = MF16(o.k[0][ks], o.q[1][ks], s10); s11 = MF16(o.k[1][ks], o.q[1][ks], s11); }
#pragma unroll
  for (int ks = 0; ks < 4; ++ks) { s0m = MF16(km[ks], o.q[0][ks], s0m); s1m = MF16(km[ks], o.q[1][ks], s1m); }
  s00 = rnd2(s00, scale); s10 = rnd2(s10, scale); s11 = rnd2(s11, scale); s0m = rnd2(s0m, scale); s1m = rnd2(s1m, scale);
  asm volatile("" : "+v"(s00), "+v"(s11), "+v"(s0m), "+v"(s1m));
#pragma unroll
  for (int e = 0; e < 4; ++e) {
    const bool msk = 4 * lg + e > lr;
    s00[e] = msk ? NEG : s00[e]; s11[e] = msk ? NEG : s11[e];
    s0m[e] = lg >= 2 ? -INFINITY : s0m[e]; s1m[e] = lg >= 2 ? -INFINITY : s1m[e];
  }
  float m0 = NEG, m1 = -INFINITY;
#pragma unroll
  for (int e = 0; e < 4; ++e) { m0 = __builtin_fmaxf(m0, __builtin_fmaxf(s00[e], s0m[e])); m1 = __builtin_fmaxf(__builtin_fmaxf(m1, s10[e]), __builtin_fmaxf(s11[e], s1m[e])); }
  m0 = xmax4(m0); m1 = xmax4(m1);
  float t0 = 0.f, t1 = 0.f;
#pragma unroll
  for (int e = 0; e < 4; ++e) {
    s00[e] = __builtin_amdgcn_exp2f((s00[e] - m0) * L2E); s0m[e] = __builtin_amdgcn_exp2f((s0m[e] - m0) * L2E);
    s10[e] = __builtin_amdgcn_exp2f((s10[e] - m1) * L2E); s11[e] = __builtin_amdgcn_exp2f((s11[e] - m1) * L2E); s1m[e] = __builtin_amdgcn_exp2f((s1m[e] - m1) * L2E);
    t0 += s00[e] + s0m[e]; t1 += (s10[e] + s11[e]) + s1m[e];
  }
  const float em = __builtin_amdgcn_exp2f((NEG - m0) * L2E);
  t0 += 4.f * em;
  const float r0 = __builtin_amdgcn_rcpf(xsum4(t0)), r1 = __builtin_amdgcn_rcpf(xsum4(t1));
  s00 *= r0; s0m *= r0; s10 *= r1; s11 *= r1; s1m *= r1;
  const u32x4 p0 = (u32x4){pack2(s00[0], s00[1]), pack2(s00[2], s00[3]), pack2(s0m[0], s0m[1]), pack2(s0m[2], s0m[3])};
  const u32x4 p1 = (u32x4){pack2(s10[0], s10[1]), pack2(s10[2], s10[3]), pack2(s11[0], s11[1]), pack2(s11[2], s11[3])};
  const u32x4 pm = (u32x4){0u, 0u, pack2(s1m[0], s1m[1]), pack2(s1m[2], s1m[3])};
  const u32 pe = pack2(em * r0, em * r0);
  const u32x4 px = (u32x4){0u, 0u, pe, pe};
#pragma unroll
  for (int pr = 0; pr < 4; ++pr) {
    f32x4 o0[2], o1[2];
#pragma unroll
    for (int t = 0; t < 2; ++t) {
      const int dt = 2 * pr + t;
      const u32x4 a0 = (u32x4){o.v[dt][0], o.v[dt][1], vm[dt][0], vm[dt][1]};
      o0[t] = MF16(a0, p0, z);
      o1[t] = MF16(o.v[dt], p1, z);
      o0[t] = MF16(o.v[dt], px, o0[t]);
      o1[t] = MF16(a0, pm, o1[t]);
    }
    const int ob = FO_O + ((sh >> 5) * 256 + (sh & 31) * 4 + pr) * 1024;
    __builtin_amdgcn_raw_buffer_store_b128((u32x4){pack2(o0[0][0], o0[0][1]), pack2(o0[0][2], o0[0][3]), pack2(o0[1][0], o0[1][1]), pack2(o0[1][2], o0[1][3])}, rsf, lane * 16, ob, 16);
    __builtin_amdgcn_raw_buffer_store_b128((u32x4){pack2(o1[0][0], o1[0][1]), pack2(o1[0][2], o1[0][3]), pack2(o1[1][0], o1[1][1]), pack2(o1[1][2], o1[1][3])}, rsf, lane * 16, ob + 131072, 16);
  }
}
#define NAW 64
__device__ __forceinline__ void a_role(__amdgpu_buffer_rsrc_t rsx, __amdgpu_buffer_rsrc_t rsf, int sh, int lim, int wi, int done, int lane, int fb, u32 tag) {
  const int lr = lane & 15, lg = lane >> 4;
  const int okm = (lg * 8 + (lr & 7)) * 16, ovm = lg < 2 ? (lg * 16 + lr) * 8 : 0x7ffffff0;
  int rp = 0, xp = -1, spin = 0, pub = done, miss = 0, tp = 200;
  u32 tseen = 0, tq0 = 0, tq1 = 0, tq2 = 0, tq3 = 0; (void)tq0; (void)tq1; (void)tq2; (void)tq3;
#define XA_WAITX(s_)                                                                  \
  while (xp <= ((s_) >> 5)) {                                                                                          \
    asm volatile("" ::: "memory");                                                                                     \
    int pv = min(xprog(__builtin_amdgcn_raw_buffer_load_b32(rsx, lane * 128, PGO_X, 16), tag), xprog(__builtin_amdgcn_raw_buffer_load_b32(rsx, (64 + (lane & 31)) * 128, PGO_X, 16), tag)); \
    _Pragma("unroll") for (int o_ = 1; o_ < 64; o_ *= 2) pv = min(pv, __shfl_xor(pv, o_));                             \
    xp = __builtin_amdgcn_readfirstlane(pv);                                                                           \
    if (xp > ((s_) >> 5) || ++spin > SPIN_CAP) break;                                                                  \
    asm volatile("s_sleep 16" ::: "memory");                                                                           \
  }
#define XA_PUB() if (pub != done) { pub = done; asm volatile("s_nop 4\n\tbuffer_store_dword %0, %1, %2, %3 offen sc1" :: "v"(done), "v"(lane ? 0x7ffffff0 : 0), "s"(rsx), "s"(PGO_A + wi * 128) : "memory"); }
#define XA_STEP(CUR, NXT)                                                                                              \
  {                                                                                                                    \
    const int seg = sh >> 5, nx = sh + NAW;                                                                            \
    XA_WAITX(min(nx, lim - 1))                                                                                         \
    if (rp <= seg) {                         \
      while (true) {                                                                                                   \
        int want = min(seg + 1, NSEG - 1);                                                                             \
        if (tseen) {                                                                                                   \
          const u32 due = tseen + (u32)(want - rp) * tp - (want + 1 < NSEG ? 0 : XA_LAST) + (seg + 1 < NSEG ? 0 : XA_END); \
          if ((int)(due - (u32)__builtin_amdgcn_s_memrealtime()) > 0 || miss) { asm volatile("s_waitcnt vmcnt(0)" ::: "memory"); XA_PUB() } \
          for (int i_ = 0; i_ < 4000 && (int)(due - (u32)__builtin_amdgcn_s_memrealtime()) > 0; ++i_) asm volatile("s_sleep 2" ::: "memory"); \
          if (!miss) want = min(max(want, rp + max((int)((u32)__builtin_amdgcn_s_memrealtime() - tseen), 0) / tp), NSEG - 1); \
        }                                                                                                              \
        XA_DBG4                                                                                                        \
        asm volatile("" ::: "memory");                                                                                 \
        if (seg + 1 < NSEG) {                                                                                          \
          const int r_ = __builtin_amdgcn_readfirstlane(__builtin_amdgcn_raw_buffer_load_b32(rsx, 0, pgr + want * 128, 16)); \
          if (r_ >= 0) { const u32 n_ = (u32)__builtin_amdgcn_s_memrealtime(); tseen = n_ - ((n_ - (r_ >> 16)) & 0x7fffu) + XA_LAG; tp = max(r_ & 1023, 1); rp = want; miss = 0; break; } \
        } else if (__ballot(__builtin_amdgcn_raw_buffer_load_b32(rsx, ((sh & 31) * 4 + (lane & 3)) * 128, PGO_D, 16) == SENT) == 0) break; \
        if (++spin > SPIN_CAP) break;                                                                                  \
        if (miss++ && seg + 2 < NSEG) asm volatile("s_sleep 20" ::: "memory");                                                         \
      }                                                                                                                \
    }                                                                                                                  \
    asm volatile("" ::: "memory");                                                                                     \
    XA_DBG2                                                                                                            \
    u32x4 km[4]; u32x2 vm[8];                                                                                          \
    _Pragma("unroll") for (int i = 0; i < 4; ++i) km[i] = __builtin_amdgcn_raw_buffer_load_b128(rsf, okm, FO_KM + (sh * 4 + i) * 512, 16); \
    _Pragma("unroll") for (int i = 0; i < 8; ++i) vm[i] = __builtin_amdgcn_raw_buffer_load_b64(rsf, ovm, FO_VM + (sh * 8 + i) * 256, 16);  \
                              \
    const int pk_ = min(rp + max((int)((u32)__builtin_amdgcn_s_memrealtime() - tseen), 0) / tp, NSEG - 1);              \
    const u32 pv_ = __builtin_amdgcn_raw_buffer_load_b32(rsx, 0, pgr + pk_ * 128, 16);                                 \
    asm volatile("" ::: "memory");                                                                                     \
    __builtin_amdgcn_s_waitcnt(0x0F70);                                                                   \
    asm volatile("" ::: "memory");                                                                                     \
    { const int r_ = __builtin_amdgcn_readfirstlane(pv_);                                                              \
      if (r_ >= 0) { const u32 n_ = (u32)__builtin_amdgcn_s_memrealtime(); tseen = n_ - ((n_ - (r_ >> 16)) & 0x7fffu) + XA_LAG; tp = max(r_ & 1023, 1); rp = pk_; } } \
    XA_PUB()                                   \
    if (nx < lim) xa_load(NXT, rsf, nx, lane);                                                                         \
    XA_DBG3                                                                                                            \
    __builtin_amdgcn_sched_barrier(0);                                                                                 \
    xa_compute(CUR, km, vm, rsf, sh, lane);                                                                            \
    ++done;                                                                                                            \
    XA_DBG                                                                                                             \
    sh = nx;                                                                                                           \
    if (sh >= lim) break;                                                                                              \
  }
#define XA_LAST 24
#define XA_END 0
#define XA_LAG 30
#define XA_DBG
#define XA_DBG2
#define XA_DBG3
#define XA_DBG4
  const int pgr = PGO_R + (__builtin_amdgcn_readfirstlane(wait_start(rsx, fb, tag, true)[2]) & 8192);
  XaOps A, B;
  if (sh >= NAW) {
    const int r_ = __builtin_amdgcn_readfirstlane(__builtin_amdgcn_raw_buffer_load_b32(rsx, 0, pgr + 40 * 128, 16));
    if (r_ >= 0) { const u32 n_ = (u32)__builtin_amdgcn_s_memrealtime(); tseen = n_ - ((n_ - (r_ >> 16)) & 0x7fffu) + XA_LAG; tp = max(r_ & 1023, 1); rp = 40; }
  }
  XA_WAITX(sh)
  asm volatile("" ::: "memory");
  xa_load(A, rsf, sh, lane);
  while (true) { XA_STEP(A, B) XA_STEP(B, A) }
  asm volatile("s_waitcnt vmcnt(0)" ::: "memory");
  XA_PUB()
}

typedef __attribute__((ext_vector_type(32))) unsigned int u32x32;
typedef __attribute__((ext_vector_type(16))) unsigned int u32x16;
typedef __attribute__((ext_vector_type(8))) unsigned int u32x8;
#define STR2(x) #x
#define STR(x) STR2(x)
#define RNG(f, b, n) f "[" STR(b) ":" STR((b) + (n)-1) "]"
#define REG(b) "v[" STR(b) "]"
#define LR_RED 16384
#define LR_FRG 24576
#define LR_CTL LR_RED
#define LR_STG 32768
#define L_RSTAMP LR_STG
#define STAMPV(i, v)
#define EXF (NH * 8 * 1024)
#define EXK 65536
#define OOR 0x7ffffff0

__device__ __forceinline__ u32 lds_word(int addr) {
  u32 v;
  asm volatile("ds_read_b32 %0, %1\n\ts_waitcnt lgkmcnt(0)" : "=v"(v) : "v"(addr) : "memory");
  return __builtin_amdgcn_readfirstlane(v);
}
__device__ __forceinline__ int fresh_lane() {
  int l;
  asm volatile("v_mbcnt_lo_u32_b32 %0, -1, 0\n\tv_mbcnt_hi_u32_b32 %0, -1, %0" : "=v"(l));
  return l;
}
__device__ __forceinline__ u32 rt32() { return (u32)__builtin_amdgcn_s_memrealtime(); }
#define SYO(s) (XSYN + flip + (s) * 64)
#define TKS 0.85f
#define TLB 1
#define TLC 1
#define TLP 2
#define KG0 0
#define CLS0 0u
#define APC 5
#define ADL 12
#define CALN 2
#define TE1 4
#define TE2 3
#define TE3 2
#define TE0 60
#define TD0 100
#define TD 120
#define PHASE 1
#define XSK 0
#define CLKS(R, X) WLG(2) "s_sub_u32 s49, s50, " R "\n\t" "s_cmp_le_i32 s49, " STR(3 * PHASE) "\n\t" "s_cbranch_scc1 " X "\n\t"
#define CLKW(L, OFF) WLG(0) "s_memrealtime s[72:73]\n\t" "s_add_u32 s50, s43, s44\n\t" "s_add_u32 s50, s50, " OFF "\n\t" "s_nop 1\n\t" "s_memrealtime s[74:75]\n\t" "s_nop 3\n" \
                     L ":\n\t" "s_memrealtime s[76:77]\n\t" CLKS("s72", L "x") "s_memrealtime s[72:73]\n\t" CLKS("s74", L "x")     \
                     "s_memrealtime s[74:75]\n\t" CLKS("s76", L "x") "s_branch " L "\n" \
                     L "x:\n\t" CLKP(L)
#define CLKP(L) "s_cmp_lt_i32 s49, 1\n\t" "s_cbranch_scc1 " L "y\n\t" "s_nop " STR(PHN) "\n\t" "s_cmp_lt_i32 s49, 2\n\t" "s_cbranch_scc1 " L "y\n\t" "s_nop " STR(PHN) "\n\t" \
                "s_cmp_lt_i32 s49, 3\n\t" "s_cbranch_scc1 " L "y\n\t" "s_nop " STR(PHN) "\n\t" "s_nop 0\n\t" "s_branch " L "y\n\t" "s_nop 0\n" L "y:\n\t"
#define PHN 3
#define CLKL(L, OFF) L ":\n\t" "s_memrealtime vcc\n\t" "ds_read_b32 v62, v38\n\t" WLG(0) "v_readfirstlane_b32 s49, v62\n\t" "s_max_u32 s44, s44, s49\n\t" \
                     "s_add_u32 s50, s43, s44\n\t" "s_add_u32 s50, s50, " OFF "\n\t" "s_sub_u32 s49, s50, vcc_lo\n\t" "s_cmp_le_i32 s49, 16\n\t" "s_cbranch_scc1 " L "z\n\t" \
                     CLKSL "s_branch " L "\n" L "z:\n\t"
#define CLKSL "s_sleep 1\n\t"
#define TS_(n) "s_memtime s[80+2*" #n ":81+2*" #n "]\n\t"
#define TSA(n)
#define TSB(n)
#define TSW(n)
#define TVM 16
template <int NTL, int KS>
__device__ __forceinline__ void tr_chunk(const u16* S, int lr, int lg, u32 (&r)[NTL][KS][8]) {
  const int q = lr >> 2, p = lr & 3;
#pragma unroll
  for (int t = 0; t < NTL; ++t)
#pragma unroll
    for (int i = 0; i < KS; ++i)
#pragma unroll
      for (int kb = 0; kb < 2; ++kb) {
        const u16* base = S + (64 * i + 32 * kb + 16 * (lg >> 1) + 4 * (lg & 1) + (q & 1) + 8 * (q >> 1)) * (16 * NTL) + 16 * t + 4 * p;
        const u32x2 t0 = __builtin_bit_cast(u32x2, __builtin_amdgcn_ds_read_tr16_b64_v4i16((LAS v4i16_t*)(base)));
        const u32x2 t1 = __builtin_bit_cast(u32x2, __builtin_amdgcn_ds_read_tr16_b64_v4i16((LAS v4i16_t*)(base + 2 * 16 * NTL)));
        r[t][i][4 * kb + 0] = t0[0]; r[t][i][4 * kb + 1] = t0[1]; r[t][i][4 * kb + 2] = t1[0]; r[t][i][4 * kb + 3] = t1[1];
      }
}
#define SMF(acc, a, bf, bi) "v_smfmac_f32_16x16x64_bf16 " RNG("v", acc, 4) ", " RNG("v", a, 4) ", " RNG(bf, bi, 8) ", v34\n\t"
#define ZER(r) "v_mov_b64 " RNG("v", r, 2) ", 0\n\t" "v_mov_b64 " RNG("v", (r) + 2, 2) ", 0\n\t"
#define GLD(r, o) "s_xor_b32 s49, s50, " STR(o) "\n\t" "buffer_load_dwordx4 " RNG("v", r, 4) ", v32, %[rs], s49 offen sc1\n\t"
#define GL1(r, o) "s_xor_b32 s49, s50, " STR(o) "\n\t" "buffer_load_dwordx4 " RNG("v", r, 4) ", v33, %[rs], s49 offen sc1\n\t"
#define GL2(r, o) GL1(r, 4096 + (o))
#define MXA0(r) "v_max3_u32 v63, " REG(r) ", " REG((r) + 1) ", " REG((r) + 1) "\n\t"
#define MXA(r) "v_max3_u32 v63, v63, " REG(r) ", " REG((r) + 1) "\n\t"
#define MXB(r) "v_max3_u32 v63, v63, " REG((r) + 2) ", " REG((r) + 3) "\n\t"
#define CMPB "v_cmp_eq_u32 vcc, s54, v63\n\t"
#define PSA(r) "v_pk_add_f32 " RNG("v", r, 2) ", " RNG("v", r, 2) ", " RNG("v", (r) + 2, 2) "\n\t"
#define PSW(r, o) "ds_write_addtid_b32 " REG(r) " offset:" STR(o) "\n\t"
#define RDQ(r, o) "ds_read_b128 " RNG("v", r, 4) ", v37 offset:" STR(o) "\n\t"
#define SQ(t, i) SMF(64 + 4 * (t), 96 + 4 * (i), "a", ((i) * 4 + (t)) * 8)
#define SV(t, i) SMF(24 + 4 * (t), 96 + 4 * (i), "a", 128 + ((i) * 2 + (t)) * 8)
#define SW(q, i) SMF(64 + 4 * (q), 96 + 4 * (i), "v", 128 + ((i) * 2 + (q)) * 8)
#define WVM(n) "s_waitcnt vmcnt(" STR(n) ")\n\t"
#define WLG(n) "s_waitcnt lgkmcnt(" STR(n) ")\n\t"
#define WLGF(n) WLG(n)
#define PACE 7
#define PC "s_nop " STR(PACE) "\n\t"
#define QBLK(i, M0) WVM(5 - (i)) SQ(0, i) M0(96 + 4 * (i)) SQ(1, i) MXB(96 + 4 * (i)) SQ(2, i) SQ(3, i)
#define WBLK(i, M0) WVM(8 - (i)) SW(0, i) M0(96 + 4 * (i)) SW(1, i) MXB(96 + 4 * (i))
#define CL8(a, b, c, d, e, f, g, h) "v" #a, "v" #b, "v" #c, "v" #d, "v" #e, "v" #f, "v" #g, "v" #h
#define CL64 CL8(64, 65, 66, 67, 68, 69, 70, 71)
#define CL72 CL8(72, 73, 74, 75, 76, 77, 78, 79)
#define CL80 CL8(80, 81, 82, 83, 84, 85, 86, 87)
#define CL88 CL8(88, 89, 90, 91, 92, 93, 94, 95)
#define CL96 CL8(96, 97, 98, 99, 100, 101, 102, 103), CL8(104, 105, 106, 107, 108, 109, 110, 111)
#define CL112 CL8(112, 113, 114, 115, 116, 117, 118, 119)
#define CL120 CL8(120, 121, 122, 123, 124, 125, 126, 127)
#define PKA(d, a, b) "v_pk_add_f32 " RNG("v", d, 2) ", " RNG("v", a, 2) ", " RNG("v", b, 2) "\n\t"
#define ARRIVE(o) "s_mov_b64 exec, 1\n\t" "ds_add_rtn_u32 v62, v38, v39 offset:" STR(o) "\n\t" "s_mov_b64 exec, -1\n\t"

#define EXTRA "v_readlane_b32 s69, v35, s45\n\t" "s_add_u32 s66, s56, s69\n\t" "s_add_u32 s67, s57, s69\n\t" "s_add_u32 s67, s67, s69\n\t" "s_add_u32 s68, s66, " STR(TVM) "\n\t"
#define TEX(s) ((s) == 0 ? TE0 : ((s) < 4 ? TE1 : ((s) < 12 ? TE2 : ((s) < 36 ? TE3 : 0))))
#define R2BASE "s_bitcmp1_b32 s45, 0\n\t" "s_cselect_b32 s71, " STR(XR2S) ", 0\n\t" "s_add_u32 s71, s71, %[r2]\n\t"
#define X4B 0
#define X4C 0
#define STB "buffer_store_dwordx2 v[116:117], v11, %[rs], s36 offen sc1\n"
#define STC "buffer_store_dwordx2 v[80:81], v17, %[rs], s36 offen sc1\n\t"
#define R2TEXT                                                                                                     \
              TSB(0)                                                                                               \
              "buffer_load_dwordx4 v[64:67], v43, %[rs], s71 offen sc1\n\t"                                      \
              "buffer_load_dwordx4 v[68:71], v8, %[rs], s71 offen sc1\n\t"                                       \
              "buffer_load_dwordx4 v[76:79], v9, %[rs], s71 offen sc1\n\t"                                       \
              "buffer_load_dwordx4 v[60:63], v10, %[rs], s71 offen sc1\n\t"                                      \
              "buffer_store_dwordx4 v[72:75], v47, %[rf], s41 offen sc1\n"                                       \
              ".Lvw%=:\n\t"                                                                                        \
              "ds_read_b32 v59, v38 offset:12\n\t"                                                                 \
              "ds_read2_b32 v[112:113], v12 offset0:0 offset1:4\n\t" "ds_read2_b32 v[114:115], v12 offset0:32 offset1:36\n\t" \
              "ds_read2_b32 v[116:117], v13 offset0:0 offset1:4\n\t" "ds_read2_b32 v[118:119], v13 offset0:32 offset1:36\n\t" \
              "ds_read2_b32 v[120:121], v14 offset0:0 offset1:4\n\t" "ds_read2_b32 v[122:123], v14 offset0:32 offset1:36\n\t" \
              "ds_read2_b32 v[124:125], v15 offset0:0 offset1:4\n\t" "ds_read2_b32 v[126:127], v15 offset0:32 offset1:36\n\t" \
              "ds_read_b64 v[96:97], v18 offset:2048\n\t" "ds_read_b64 v[100:101], v18 offset:2056\n\t"      \
              "ds_read_b64 v[104:105], v18 offset:3072\n\t" "ds_read_b64 v[108:109], v18 offset:3080\n\t"    \
              WLG(0)                                                                                               \
              "v_readfirstlane_b32 s49, v59\n\t"                                                                 \
              "s_sub_u32 s49, s49, 4\n\t"                                                                      \
              "s_cmp_lt_i32 s49, s38\n\t"                                                                      \
              "s_cbranch_scc1 .Lvw%=\n\t"                                                                          \
              PKA(112, 112, 116) PKA(114, 114, 118) PKA(120, 120, 124) PKA(122, 122, 126) PKA(112, 112, 120) PKA(114, 114, 122) \
              "v_mov_b32 v59, 0\n\t" "v_mov_b32 v99, 0\n\t" "v_mov_b32 v103, 0\n\t" "v_mov_b32 v107, 0\n\t" "v_mov_b32 v111, 0\n\t" \
              "v_cvt_pk_bf16_f32 v98, v112, v59\n\t" "v_cvt_pk_bf16_f32 v106, v113, v59\n\t" "v_cvt_pk_bf16_f32 v102, v114, v59\n\t" "v_cvt_pk_bf16_f32 v110, v115, v59\n\t" \
              "v_mov_b64 v[112:113], v[80:81]\n\t" "v_mov_b64 v[114:115], v[82:83]\n\t"                            \
              "v_mov_b32_dpp v112, v84 row_ror:8 row_mask:0xf bank_mask:0xc\n\t" "v_mov_b32_dpp v113, v85 row_ror:8 row_mask:0xf bank_mask:0xc\n\t" \
              "v_mov_b32_dpp v114, v86 row_ror:8 row_mask:0xf bank_mask:0xc\n\t" "v_mov_b32_dpp v115, v87 row_ror:8 row_mask:0xf bank_mask:0xc\n\t" \
              "v_mov_b32 v52, 0x3f803f80\n\t" "v_mov_b32 v53, 0x3f803f80\n\t" "v_mov_b32 v54, 0x3f803f80\n\t" "v_mov_b32 v55, 0x3f803f80\n\t" \
              TSB(1) "s_waitcnt vmcnt(4)\n\t" TSB(2) \
              "v_mov_b64 v[88:89], v[64:65]\n\t" "v_mov_b64 v[90:91], v[66:67]\n\t" \
              "s_nop 1\n\t" \
              "v_permlane32_swap_b32 v88, v90\n\t" "v_permlane32_swap_b32 v89, v91\n\t" \
              "v_pk_add_f32 v[88:89], v[88:89], v[90:91]\n\t" \
              "s_nop 1\n\t" \
              "v_permlane16_swap_b32 v88, v89\n\t" \
              "v_add_f32 v92, v88, v89\n\t" \
              "s_waitcnt vmcnt(3)\n\t" \
              "v_pk_add_f32 v[88:89], v[112:113], v[68:69]\n\t" "v_pk_add_f32 v[90:91], v[114:115], v[70:71]\n\t" \
              "s_waitcnt vmcnt(2)\n\t" \
              "v_pk_add_f32 v[88:89], v[88:89], v[76:77]\n\t" "v_pk_add_f32 v[90:91], v[90:91], v[78:79]\n\t" \
              "s_waitcnt vmcnt(1)\n\t" TSB(3) \
              "v_pk_add_f32 v[88:89], v[88:89], v[60:61]\n\t" "v_pk_add_f32 v[90:91], v[90:91], v[62:63]\n\t" \
              "v_max3_f32 v93, v88, v89, v92\n\t" \
              "v_max3_f32 v93, v93, v90, v91\n\t" \
              "s_nop 1\n\t" \
              "v_max_f32_dpp v94, v93, v93 row_ror:8 row_mask:0xf bank_mask:0xf\n\t" \
              "v_max_f32_dpp v95, v93, v93 row_ror:8 row_mask:0xf bank_mask:0xf\n\t" \
              "s_nop 1\n\t" \
              "v_permlane16_swap_b32 v94, v95\n\t" \
              "v_max_f32 v93, v94, v95\n\t" "v_max_f32 v94, v94, v95\n\t" \
              "s_nop 1\n\t" \
              "v_permlane32_swap_b32 v93, v94\n\t" \
              "v_max_f32 v93, v93, v94\n\t" \
              "v_mul_f32 v93, 0xbe0293ee, v93\n\t" \
              "v_fmamk_f32 v88, v88, 0x3e0293ee, v93\n\t" "v_fmamk_f32 v89, v89, 0x3e0293ee, v93\n\t" \
              "v_exp_f32 v88, v88\n\t" "v_fmamk_f32 v90, v90, 0x3e0293ee, v93\n\t" \
              "v_exp_f32 v89, v89\n\t" "v_fmamk_f32 v91, v91, 0x3e0293ee, v93\n\t" \
              "v_exp_f32 v90, v90\n\t" "v_fmamk_f32 v92, v92, 0x3e0293ee, v93\n\t" \
              "v_exp_f32 v91, v91\n\t" "v_exp_f32 v92, v92\n\t" \
              "v_cvt_pk_bf16_f32 v56, v88, v89\n\t" "v_cvt_pk_bf16_f32 v57, v90, v91\n\t" "v_cvt_pk_bf16_f32 v58, v92, v59\n\t" \
              TSB(4) "s_nop 1\n\t" \
              "v_mfma_f32_16x16x32_bf16 v[88:91], v[52:55], v[56:59], 0\n\t" \
              "v_mfma_f32_16x16x32_bf16 v[116:119], v[96:99], v[56:59], 0\n\t" \
              "v_mfma_f32_16x16x32_bf16 v[120:123], v[100:103], v[56:59], 0\n\t" \
              "v_mfma_f32_16x16x32_bf16 v[124:127], v[104:107], v[56:59], 0\n\t" \
              "v_mfma_f32_16x16x32_bf16 v[52:55], v[108:111], v[56:59], 0\n\t" \
              TSB(5) "s_nop 3\n\t" \
              "v_add_f32_dpp v92, v88, v88 row_ror:8 row_mask:0xf bank_mask:0xf\n\t" \
              "v_rcp_f32 v92, v92\n\t" \
              "v_add_f32_dpp v116, v120, v116 row_ror:8 row_mask:0xf bank_mask:0x3\n\t" "v_add_f32_dpp v117, v121, v117 row_ror:8 row_mask:0xf bank_mask:0x3\n\t" \
              "v_add_f32_dpp v118, v122, v118 row_ror:8 row_mask:0xf bank_mask:0x3\n\t" "v_add_f32_dpp v119, v123, v119 row_ror:8 row_mask:0xf bank_mask:0x3\n\t" \
              "v_cmp_u_f32 vcc, v92, v92\n\t" \
              "v_add_f32_dpp v116, v124, v52 row_ror:8 row_mask:0xf bank_mask:0xc\n\t" "v_add_f32_dpp v117, v125, v53 row_ror:8 row_mask:0xf bank_mask:0xc\n\t" \
              "v_add_f32_dpp v118, v126, v54 row_ror:8 row_mask:0xf bank_mask:0xc\n\t" "v_add_f32_dpp v119, v127, v55 row_ror:8 row_mask:0xf bank_mask:0xc\n\t" \
              "v_pk_mul_f32 v[116:117], v[116:117], v[92:93] op_sel_hi:[1,0]\n\t" "v_pk_mul_f32 v[118:119], v[118:119], v[92:93] op_sel_hi:[1,0]\n\t" \
              "v_cvt_pk_bf16_f32 v116, v116, v117\n\t" "v_cvt_pk_bf16_f32 v117, v118, v119\n\t" \
              "v_min_u32 v116, -2, v116\n\t" "v_min_u32 v117, -2, v117\n\t" \
              "s_cbranch_vccnz .Lskip%=\n\t"                                                                       \
              STB                                     \
              ".Lskip%=:\n\t" TSB(6)
#define AV 0
#define VREST
#define PS1(r) "v_add_f32 " REG(r) ", " REG(r) ", " REG((r) + 2) "\n\t"
#define VPOST VREST PSA(24) PSW(24, 2560) PSW(25, 2832) PSA(28) PSW(28, 3200) PSW(29, 3472) ARRIVE(12)
#define R2OUT "=&{v[60:79]}"(pr), "=&{v[116:117]}"(ov)
#define FINEO
#define STEPCHK "2"
#define ROLESV
#define ROLEOUT

__device__ __forceinline__ void clk_edge(u32& cyc, u32& tick) {
  const u32 t = rt32();
  for (int i = 0; i < 64; ++i) { tick = rt32(); if (tick != t) break; }
  cyc = (u32)__builtin_amdgcn_s_memtime();
}
__device__ __forceinline__ float clk_rate(u32 cyc0, u32 tk0) { u32 c, t; clk_edge(c, t); return (float)(c - cyc0) / (float)max(t - tk0, 1u); }
__device__ __forceinline__ void dma16(__amdgpu_buffer_rsrc_t rs, int voff, int soff, int sstep, int ldsa) {
#pragma unroll
  for (int i = 0; i < 16; ++i)
    asm volatile("s_mov_b32 m0, %0\n\ts_nop 3\n\tbuffer_load_dwordx4 %1, %2, %3 offen lds" :: "s"(ldsa + i * 1024), "v"(voff), "s"(rs), "s"(soff ^ (i * sstep)) : "memory");
}

#define KH(t) (192 + 8 * (t))
#define KMF(t, a, b) "v_mfma_f32_16x16x32_bf16 " RNG("v", 80 + 4 * (t), 4) ", " RNG("v", KH(t) + 4 * (a), 4) ", " RNG("v", KH(3) + 4 * (b), 4) ", " RNG("v", 80 + 4 * (t), 4) "\n\t"
#define KX(j) (92 + 4 * ((j) & 1))
#define KA(t, j) "v_pk_mul_f32 " RNG("v", KX(j), 2) ", " RNG("v", 64 + 4 * (j), 2) ", " RNG("v", 112 + ((t) & 2), 2) " op_sel:[0," STR((t) & 1) "] op_sel_hi:[1," STR((t) & 1) "]\n\t"
#define KF(j) "v_fract_f32 " REG(KX(j) + 1) ", " REG(KX(j) + 1) "\n\t"
#define KE(j, N) "v_exp_f32 " REG(KX(j)) ", " REG(KX(j)) "\n\t" "v_cos_f32 " REG(KX(j) + 2) ", " REG(KX(j) + 1) "\n\t" "v_sin_f32 " REG(KX(j) + 3) ", " N REG(KX(j) + 1) "\n\t"
#define KM(j) "v_pk_mul_f32 " RNG("v", KX(j) + 2, 2) ", " RNG("v", KX(j) + 2, 2) ", " RNG("v", KX(j), 2) " op_sel_hi:[1,0]\n\t"
#define KC(j) "v_pk_mul_f32 " RNG("v", KX(j), 2) ", " RNG("v", KX(j) + 2, 2) ", " RNG("v", 66 + 4 * (j), 2) " op_sel_hi:[1,0]\n\t" \
  "v_pk_fma_f32 " RNG("v", KX(j), 2) ", " RNG("v", KX(j) + 2, 2) ", " RNG("v", 66 + 4 * (j), 2) ", " RNG("v", KX(j), 2) " op_sel:[1,1,0] op_sel_hi:[0,1,1] neg_hi:[1,0,0]\n\t"
#define KD(t, j, r) "v_cvt_pk_bf16_f32 " REG(KH(t) + (j)) ", " REG(r) ", " REG((r) + 1) "\n\t" \
  "v_dot2c_f32_bf16 " REG(r) ", s47, " REG(KH(t) + (j)) "\n\t" "v_dot2c_f32_bf16 " REG((r) + 1) ", s48, " REG(KH(t) + (j)) "\n\t"
#define KL(t, j, r) "v_cvt_pk_bf16_f32 " REG(KH(t) + 4 + (j)) ", " REG(r) ", " REG((r) + 1) "\n\t"
#define KDM "s_add_u32 m0, m0, 1024\n\t" "s_add_u32 s43, s43, s42\n\t" "buffer_load_dwordx4 v110, s[36:39], s43 offen lds\n\t"
#define KP(j) "ds_read_b128 " RNG("v", 64 + 4 * (j), 4) ", v111 offset:" STR(16 * (j)) "\n\t"
#define KV(j, L, A) KE(j, "-") L KM(j) KC(j) A KD(3, j, KX(j))
#define KU(t, j, L, A) KE(j, "") L KM(j) A KD(t, j, KX(j) + 2)
#define KSUB                                                                                                        \
  KV(0, WLG(8) "s_add_u32 m0, m0, 1024\n\t", KA(3, 1)) KV(1, KL(3, 0, KX(0)), KA(3, 2))                              \
  "s_xor_b32 s43, s40, s41\n\t" "buffer_load_dwordx4 v110, s[36:39], s43 offen lds\n\t" KV(2, KL(3, 1, KX(1)), KA(3, 3)) \
  KV(3, KL(3, 2, KX(2)), KA(0, 0))                                                                                  \
  KU(0, 0, KL(3, 3, KX(3)), KA(0, 1)) KU(0, 1, KL(0, 0, KX(0) + 2), KA(0, 2)) KDM KU(0, 2, KL(0, 1, KX(1) + 2), KA(0, 3)) \
  KU(0, 3, KL(0, 2, KX(2) + 2), KA(1, 0)) KF(0)                                                                     \
  KU(1, 0, KL(0, 3, KX(3) + 2), KA(1, 1)) KF(1) KMF(0, 1, 0) KU(1, 1, KL(1, 0, KX(0) + 2), KA(1, 2)) KF(2) KDM      \
  KMF(0, 0, 1) KU(1, 2, KL(1, 1, KX(1) + 2), KA(1, 3)) KF(3) KMF(0, 0, 0) KU(1, 3, KL(1, 2, KX(2) + 2), KA(2, 0)) KF(0) KP(0) \
  KU(2, 0, KL(1, 3, KX(3) + 2), KA(2, 1)) KF(1) KP(1) KMF(1, 1, 0) KU(2, 1, KL(2, 0, KX(0) + 2), KA(2, 2)) KF(2) KP(2) KDM \
  KMF(1, 0, 1) KU(2, 2, KL(2, 1, KX(1) + 2), KA(2, 3)) KF(3) KP(3) KMF(1, 0, 0) KU(2, 3, KL(2, 2, KX(2) + 2), WLG(3) KA(3, 0)) \
  KMF(2, 0, 1) "v_add_u32 v111, 256, v111\n\t" "s_lshl2_add_u32 s41, s42, s41\n\t" KL(2, 3, KX(3) + 2)
#define KZERO ZER(80) ZER(84) ZER(88)
#define KCHUNK(PTR, VO, SO, ST, BUF, PF) "s_mov_b64 s[36:37], " PTR "\n\t" "s_mov_b32 m0, %[sc]\n\t" "v_mov_b32 v110, " VO "\n\t" SO "s_mov_b32 s41, 0\n\t" "s_mov_b32 s42, " STR(ST) "\n\t" \
  "buffer_load_dword " PF ", s[36:39], 0 offen lds\n\t" "s_add_u32 m0, %[sl], " STR((BUF) * 16384 - 1024) "\n\t"
#define KCALL "s_call_b64 s[44:45], .Lkg%=\n\t" WVM(16) KMF(2, 0, 0)
#define KTR(F, d, A, o) "ds_read_b64_tr_b16 " RNG(F, d, 2) ", " A " offset:" STR(o) "\n\t"
#define KTQ1(d, o) KTR("a", d, "%[aq]", o) KTR("a", (d) + 2, "%[aq]", (o) + 128) KTR("a", (d) + 4, "%[aq]", (o) + 2048) KTR("a", (d) + 6, "%[aq]", (o) + 2176)
#define KTQ(d, o) KCALL KTQ1(d, o) KMF(2, 1, 0) KTQ1((d) + 8, (o) + 32)
#define KTO1(d, o) KTR("v", d, "%[ao]", o) KTR("v", (d) + 2, "%[ao]", (o) + 64) KTR("v", (d) + 4, "%[ao]", (o) + 1024) KTR("v", (d) + 6, "%[ao]", (o) + 1088)
#define KTO(d, o) KCALL KTO1(d, o) KMF(2, 1, 0) KTO1((d) + 8, (o) + 2048)
#define KOUT(o) "s_add_u32 s43, %[ko], " STR(o) "\n\t"                                                              \
  "buffer_store_dwordx4 v[80:83], %[l16], %[rt], s43 offen sc1\n\t" "buffer_store_dwordx4 v[84:87], %[l16], %[rt], s43 offen offset:1024 sc1\n\t" "s_nop 3\n\t" \
  "buffer_store_dwordx4 v[88:91], %[l16], %[rt], s43 offen offset:2048 sc1\n\t"
struct SsmPtrs { const u16 *A_re, *A_im, *B_re, *B_im, *C_re, *C_im, *log_dt; };
__device__ __forceinline__ void r_role(unsigned char* lds, int b, const u16* __restrict__ mem0, const u16* __restrict__ Wq, const u16* __restrict__ Wk, const u16* __restrict__ Wv,
                                       const u16* __restrict__ Wo, int fb, u32 tag, u64 pc0, int clen, __amdgpu_buffer_rsrc_t rsx, __amdgpu_buffer_rsrc_t rsf,
                                       __amdgpu_buffer_rsrc_t rst, SsmPtrs sp, int tid, int lane, int w
                                       ) {
  const int lr = lane & 15, lg = lane >> 4;
  const int ldsb = (int)(size_t)(LAS unsigned char*)lds;
  const int idx = (lr & 2) ? 0xEEEE : 0x4444;
  int tries = 0;
  u32 tcnt = 0;
  u32 cyc0 = 0, tk0 = 0;
  if (tid < 8) *(u32*)(lds + LR_CTL + tid * 4) = tid == 1 ? rt32() : 0u;
  const int xcd = b & 3, li = b >> 2;
  const int head = xcd * 8 + (li >> 2), ks = li & 3;
  const int vcu = head * 4 + ks;
  const int cg = (xcd >> 1) * 32 + li, rg = xcd & 1;
  const int rot1 = ((li >> 1) + w) & 3, rot3 = ((li >> 2) + 2 * w) & 7;
  const int pz1 = lane < 32 ? XR1A(lane >> 3, cg * 256 + rg * 128 + (lane & 7) * 16) : XR3A(lg & 1, vcu * 256 + lr * 16);
  const int pgo = PGO_X + ((w >> 1) * 32 + head) * 128;
  const int calo = XCAL + (li & 3) * 8192 + ((li >> 2) & 1) * 4096 + ((li >> 3) * 4 + xcd) * 128;
  u32 xp0;
  u32x32 wa[6], wo[4];
  {
    const __amdgpu_buffer_rsrc_t rwq = __builtin_amdgcn_make_buffer_rsrc((void*)Wq, 0, DM * HD * 2, 0x00020000);
    const int vq = (lane >> 2) * (HD * 2) + vcu * 64 + (lane & 3) * 16, sq = (256 * w + (rot1 << 6)) * (HD * 2);
    const int vo = (lane >> 1) * (DM * 2) + cg * 32 + (lane & 1) * 16, so = (1024 * w + (rot3 << 7)) * (DM * 2);
    const int sl = ldsb + LR_STG + w * 32768;
    const int ch0 = (b * 4 + w) * 2;
    u32x2 pin[6];
    u32 ldt;
    {
      const int po = (ch0 * SSM_N + 4 * lane) * 2;
      asm volatile("global_load_dwordx2 %0, %7, %9\n\tglobal_load_dwordx2 %1, %7, %10\n\tglobal_load_dwordx2 %2, %7, %11\n\tglobal_load_dwordx2 %3, %7, %12\n\t"
                   "global_load_dwordx2 %4, %7, %13\n\tglobal_load_dwordx2 %5, %7, %14\n\tglobal_load_ushort %6, %8, %15"
                   : "=&v"(pin[0]), "=&v"(pin[1]), "=&v"(pin[2]), "=&v"(pin[3]), "=&v"(pin[4]), "=&v"(pin[5]), "=&v"(ldt)
                   : "v"(po), "v"((ch0 + (lane >> 5)) * 2), "s"(sp.A_re), "s"(sp.A_im), "s"(sp.B_re), "s"(sp.B_im), "s"(sp.C_re), "s"(sp.C_im), "s"(sp.log_dt) : "memory");
    }
    u32x4 m0v;
    asm volatile("global_load_dwordx4 %0, %1, %2" : "=&v"(m0v) : "v"(((4 * rg + ((lane >> 1) & 3)) * DM + 16 * cg + 8 * (lane & 1)) * 2), "s"(mem0) : "memory");
    const u16* const wp[4] = {Wq, Wk, Wv, Wo};
    int pfo[5];
#pragma unroll
    for (int c = 0; c < 5; ++c) {
      const int lo_ = (int)(size_t)wp[c < 3 ? c : 3], s3 = (384 - lo_) & 1023, s63 = (8064 - lo_) & 8191;
      pfo[c] = c < 3 ? (lane < 16 ? (256 * w + li * 8 + (lane & 7)) * (HD * 2) + xcd * 2048 + (lane >> 3) * 1024 + s3
                                  : (lane < 24 && (s63 >> 11) == xcd ? (256 * w + li * 8 + (lane & 7)) * (HD * 2) + s63 : OOR))
                     : (lane < 16 ? (1024 * w + li * 16 + lane + (((c - 3) * 512) ^ ((((s3 >> 7) + 2 * w) & 4) << 7))) * (DM * 2) + (xcd >> 1) * 1024 + s3 : OOR);
    }
    const int scr = ldsb + LR_RED + 1024 + w * 256;
    asm volatile("s_mov_b32 m0, %0\n\ts_nop 3\n\tbuffer_load_dword %1, %2, 0 offen lds" :: "s"(scr), "v"(pfo[0]), "s"(rwq) : "memory");
#define KP0 16
#pragma unroll
    for (int i = 0; i < KP0; ++i)
      asm volatile("s_mov_b32 m0, %0\n\ts_nop 3\n\tbuffer_load_dwordx4 %1, %2, %3 offen lds" :: "s"(sl + i * 1024), "v"(vq), "s"(rwq), "s"(sq ^ (i * 16 * HD * 2)) : "memory");
    asm volatile("s_waitcnt vmcnt(1+" STR(KP0) ")" : "+v"(pin[0]), "+v"(pin[1]), "+v"(pin[2]), "+v"(pin[3]), "+v"(pin[4]), "+v"(pin[5]), "+v"(ldt), "+v"(m0v)::"memory");
    f32x4* prm = (f32x4*)(lds + w * 4096);
    {
      const float dt = rbf(expf(bf2f((u16)ldt)));
#pragma unroll
      for (int i = 0; i < 4; ++i) {
        float v[6];
#pragma unroll
        for (int j = 0; j < 6; ++j) v[j] = __uint_as_float((i & 1) ? (pin[j][i >> 1] & 0xffff0000u) : (pin[j][i >> 1] << 16));
        const float are = dt * v[0], aim = dt * v[1], br = dt * v[2], bi = dt * v[3];
        const float cr = v[4] * br - v[5] * bi, ci = v[4] * bi + v[5] * br;
        const float rev = aim * 0.15915494309189535f;
        prm[4 * lane + i] = (f32x4){are * 1.4426950408889634f, rev - __builtin_rintf(rev), cr, ci};
      }
    }
#pragma unroll
    for (int i = KP0; i < 16; ++i)
      asm volatile("s_mov_b32 m0, %0\n\ts_nop 3\n\tbuffer_load_dwordx4 %1, %2, %3 offen lds" :: "s"(sl + i * 1024), "v"(vq), "s"(rwq), "s"(sq ^ (i * 16 * HD * 2)) : "memory");
    const f32x4 lv = {(float)(128 * (lr >> 3) + (lr & 7)), (float)(lr < 8 ? 256 + 128 * (lr >> 2) + 2 * (lr & 3) : 512 + 128 * ((lr - 8) >> 1) + 4 * (lr & 1)),
                         (float)(lr <= 8 ? 1024 + 128 * lr : 0), (float)(8 * lr)};
    const int trow = 16 * (lg >> 1) + 4 * (lg & 1) + ((lr >> 2) & 1) + 8 * (lr >> 3);
    asm volatile(
        "s_nop 4\n\t" "s_mov_b32 s46, m0\n\t" "s_mov_b32 s38, " STR(DM * HD * 2) "\n\t" "s_mov_b32 s39, 0x20000\n\t" "s_mov_b32 s47, 0xbf80\n\t" "s_mov_b32 s48, 0xbf800000\n\t"
        WLG(0)
        "ds_read_b128 v[64:67], %[pa]\n\t" "ds_read_b128 v[68:71], %[pa] offset:16\n\t" "ds_read_b128 v[72:75], %[pa] offset:32\n\t" "ds_read_b128 v[76:79], %[pa] offset:48\n\t"
        "v_add_u32 v111, 256, %[pa]\n\t" KZERO
        KCHUNK("%[wk]", "%[vq]", "s_mov_b32 s40, %[sq]\n\t", 16 * HD * 2, 1, "%[p1]")
        WLG(0) KA(3, 0)
        KTQ(0, 0) KTQ(32, 4096) KTQ(64, 8192) KTQ(96, 12288)
        KCHUNK("%[wv]", "%[vq]", "", 16 * HD * 2, 0, "%[p2]")
        KTQ(16, 16384) KTQ(48, 16384 + 4096) KTQ(80, 16384 + 8192) KTQ(112, 16384 + 12288)
        KOUT(0)
        KCHUNK("%[wo]", "%[vo]", "s_mov_b32 s40, %[so]\n\t", 32 * DM * 2, 1, "%[p3]")
        KZERO
        KTQ(128, 0) KTQ(144, 4096) KTQ(160, 8192) KTQ(176, 12288)
        KCHUNK("%[wo]", "%[vo]", "s_xor_b32 s40, s40, " STR(512 * DM * 2) "\n\t", 32 * DM * 2, 0, "%[p4]")
        KTO(128, 16384) KTO(144, 16384 + 4096) KTO(160, 16384 + 8192) KTO(176, 16384 + 12288)
        KOUT(3072)
        "s_branch .Lke%=\n"
        ".Lkg%=:\n\t"
        KSUB
        "s_setpc_b64 s[44:45]\n"
        ".Lke%=:\n\t"
        WLG(0) "s_mov_b32 m0, s46"
        : "=&{a[0:31]}"(wa[0]), "=&{a[32:63]}"(wa[1]), "=&{a[64:95]}"(wa[2]), "=&{a[96:127]}"(wa[3]), "=&{a[128:159]}"(wa[4]), "=&{a[160:191]}"(wa[5]),
          "=&{v[128:159]}"(wo[0]), "=&{v[160:191]}"(wo[1])
        : "{v[112:115]}"(lv), [pa] "v"(ldsb + w * 4096 + lg * 64), [vq] "v"(vq), [vo] "v"(vo),
          [p1] "v"(pfo[1]), [p2] "v"(pfo[2]), [p3] "v"(pfo[3]), [p4] "v"(pfo[4]), [aq] "v"(sl + trow * 64 + (lr & 3) * 8), [ao] "v"(sl + trow * 32 + (lr & 3) * 8), [l16] "v"(lane * 16),
          [wk] "s"(Wk), [wv] "s"(Wv), [wo] "s"(Wo), [sq] "s"(sq), [so] "s"(so), [sl] "s"(sl), [sc] "s"(scr), [rt] "s"(rst), [ko] "s"(2 * SSM_L * SSM_D * 2 + ch0 * 3072)
        : CL64, CL72, CL80, CL88, CL96, CL8(192, 193, 194, 195, 196, 197, 198, 199), CL8(200, 201, 202, 203, 204, 205, 206, 207), CL8(208, 209, 210, 211, 212, 213, 214, 215),
          CL8(216, 217, 218, 219, 220, 221, 222, 223), "s36", "s37", "s38", "s39", "s40", "s41", "s42", "s43", "s44", "s45", "s46", "s47", "s48", "scc", "memory");
    {
      const u32x4 pz = (u32x4){SENT, SENT, SENT, SENT};
      if (w < 3) {
        u32x4 v = pz;
        if (w == 0 && lane < 32) v = (u32x4){nosent(m0v[0]), nosent(m0v[1]), nosent(m0v[2]), nosent(m0v[3])};
#pragma unroll
        for (int hf = 0; hf < 2; ++hf) {
          __builtin_amdgcn_raw_buffer_store_b128(v, rsx, pz1, XSB(w) + hf * 8192, 16);
          __builtin_amdgcn_raw_buffer_store_b128(pz, rsx, XR2(vcu) + lane * 16, XR2B + (w & 1) * XR2S + hf * 8192, 16);
          __builtin_amdgcn_raw_buffer_store_b128(pz, rsx, lane < 16 ? XR2(vcu) + 1024 + lane * 16 : OOR, XR2B + (w & 1) * XR2S + hf * 8192, 16);
        }
      } else {
        __builtin_amdgcn_raw_buffer_store_b128(pz, rsx, lane < 8 ? lane * 16 : OOR, calo, 16);
        if (b < 32) {
          __builtin_amdgcn_raw_buffer_store_b128(pz, rsx, lane * 16, XSYN + b * 2048, 16);
          __builtin_amdgcn_raw_buffer_store_b128(pz, rsx, lane * 16, XSYN + b * 2048 + 1024, 16);
        } else if (b >= 48 && b < 48 + 41) {
          __builtin_amdgcn_raw_buffer_store_b128(pz, rsx, lane * 16, PGO_D + (b - 48) * 1024, 16);
        }
      }
      asm volatile("" ::: "memory");
    }
    xp0 = __builtin_amdgcn_raw_buffer_load_b32(rsx, 0, pgo, 16);
    clk_edge(cyc0, tk0);
    asm volatile("s_waitcnt vmcnt(0)\n\ts_barrier" : "+v"(xp0)::"memory");
#define CLKF() clk_rate(cyc0, tk0)
    if (w == 0) {
      const u64 c0 = __builtin_amdgcn_s_memtime();
      asm volatile("s_waitcnt lgkmcnt(0)" ::: "memory");
      u32 v = __builtin_amdgcn_raw_buffer_load_b32(rsx, 0, calo, 16);
      asm volatile("s_waitcnt vmcnt(0)" : "+v"(v)::"memory");
      const u32 lat = (u32)(__builtin_amdgcn_s_memtime() - c0);
      const float f = CLKF();
      if (lane == 0) __builtin_amdgcn_raw_buffer_store_b128((u32x4){tag, __float_as_uint(f), lat, 0u}, rsx, 0, PGO_Y + b * 128, 16);
      asm volatile("" ::: "memory");
    }
    {
      u32 r[1][8][8];
      tr_chunk<1, 8>((const u16*)(lds + LR_STG + w * 32768), lr, lg, r);
#pragma unroll
      for (int i = 0; i < 8; ++i)
#pragma unroll
        for (int e = 0; e < 8; ++e) { const int o = (8 + i) * 8 + e; wo[o >> 5][o & 31] = r[0][i][e]; }
      asm volatile("s_waitcnt lgkmcnt(0)" ::: "memory");
    }
  }
  const int hf = (lr >> 1) & 1;
  const int voff1 = lg * 256 + (lr >> 3) * 128 + (((lr >> 2) & 1) * 2 + (lr & 1)) * 32 + hf * 16;
  const int voff3 = (lr >> 3) * 512 + (lg >> 1) * 256 + (((lr >> 2) & 1) * 2 + (lr & 1)) * 16 + (lg & 1) * 128 + hf * 64;
  const int ard = ldsb + (2 * (lr >> 3) + (lg >> 1)) * 640 + (lr & 1) * 272 + ((lr >> 1) & 3) * 64 + (lg & 1) * 32;
  const int ctl = ldsb + LR_CTL, one = 1;
  const int r8 = lane & 7, hk = (lane >> 3) & 1;
#define COA 1
#define COB 1
  const int st01 = lr < 8 ? XR2(vcu) + lr * 16 + lg * 128 : OOR, st2 = (lr < 8 && lg >= 2) ? XR2(vcu) + 1024 + lr * 16 + (lg - 2) * 128 : OOR;
  const int ld01 = hk * 512 + r8 * 16 + lg * 128, ld2 = XR2(head * 4 + lg) + 1024 + r8 * 16 + hk * 128;
  int sto = XR3A(r8 >> 2, vcu * 256 + (r8 & 3) * 16 + lg * 64 + hk * 8);
  const int stk = lr >= 8 ? (lg * 8 + (lr - 8)) * 16 : OOR;
  const int lo = X4C ? 2 * (lr & 7) + (lr >> 3) : lr;
  const int arw = 0, arr = ldsb + 3840 + ((lo >> 3) + 2 * ((lo >> 2) & 1)) * 64 + (lo & 3) * 16;
  const int st1 = XR1A(lg, cg * 256 + rg * 128 + lo * 8);
  const int avw = 0;
  const int avc = ldsb + 2560 + (lr >> 3) * 640 + (8 * ((lr >> 2) & 1) + (lr & 3)) * 4;
  const int avr = avc + (lg & 1) * 272 + (lg >> 1) * 64, avf = avc + (lg & 1) * 128 + (lg >> 1) * 16;
  u32x16 kc = {(u32)voff1, (u32)voff3, (u32)idx, (u32)TEX(lane), (u32)(lane < 16 ? XR2(vcu) + 1024 + lane * 16 : OOR), (u32)ard, (u32)ctl, (u32)one, (u32)st01, (u32)st2, (u32)(XR2(vcu) + lane * 16), (u32)ld2, (u32)(lane * 16), (u32)arw, (u32)arr, (u32)stk};
  int o1c = XR1A(xcd, w * 4096) + rot1 * 1024, o3c = XR3A(rg, w * 8192) + rot3 * 1024;
  const int fo = w < 2 ? FO_K + ((head * 2 + w) * 4 + ks) * 1024 : FO_V + (head * 8 + 2 * ks + (w - 2)) * 1024;
  int xp = xprog(__builtin_amdgcn_readfirstlane(xp0), tag);
#define XWAIT(n) for (int it_ = 0; it_ < SPIN_CAP && xp < (n); ++it_) {                                               \
    if (it_) asm volatile("s_sleep 4" ::: "memory");                                                                  \
    xp = xprog(__builtin_amdgcn_readfirstlane(__builtin_amdgcn_raw_buffer_load_b32(rsx, 0, pgo, 16)), tag); asm volatile("" ::: "memory"); }
  XWAIT(1)
  *(u32x4*)(lds + LR_FRG + w * 1024 + lane * 16) = __builtin_amdgcn_raw_buffer_load_b128(rsf, lane * 16, fo, 16);
  const u32 tbase = __builtin_amdgcn_readfirstlane(*(const u32*)(lds + LR_CTL + 4));
  const u32 tb0 = tbase - (1u << 20);
#define TABLE(l) ({ const float f_ = CLKF() * *(const float*)(lds + LR_CTL + 28);          \
    const float k_ = 1.f + TKS * (fminf(fmaxf(23.9f / f_, 1.f), 1.5f) - 1.f);                                          \
    const int nb_ = (int)(TDB * k_ + 0.5f) + TLB * (l), nc_ = nb_ + (int)((TDC - TDB) * k_ + 0.5f) + TLC * (l), np_ = nc_ + (int)((TP - TDC) * k_ + 0.5f) + TLP * (l); \
    (u32)(nb_ | (nc_ << 10) | (np_ << 20)); })
#define XPN 1
  if (b == 0 && w == 1) {
    u32 tt = 0, lat = 0;
    bool xok = false;
    for (int it = 0; it < SPIN_CAP; ++it) {
      asm volatile("" ::: "memory");
      const u32x4 r0 = __builtin_amdgcn_raw_buffer_load_b128(rsx, lane * 128, PGO_Y, 16), r1 = __builtin_amdgcn_raw_buffer_load_b128(rsx, lane * 128 + 8192, PGO_Y, 16);
      if (!xok) xok = __ballot(xprog(__builtin_amdgcn_raw_buffer_load_b32(rsx, lane * 128, PGO_X, 16), tag) < XPN) == 0;
      const u64 miss = __ballot(r0[0] != tag || r1[0] != tag);
      if (tt == 0 && __popcll(miss) <= 24) {
        float fm = (r0[0] == tag ? __uint_as_float(r0[1]) : 0.f) + (r1[0] == tag ? __uint_as_float(r1[1]) : 0.f), fn = (r0[0] == tag ? 1.f : 0.f) + (r1[0] == tag ? 1.f : 0.f);
#pragma unroll
        for (int o = 4; o < 64; o *= 2) { fm += __shfl_xor(fm, o); fn += __shfl_xor(fn, o); }
        fm = fn > 0.f ? fm / fn : 100.f;
        const float f0 = __uint_as_float(__builtin_amdgcn_readfirstlane(__float_as_uint(fm)));
        fm = fminf(fm, __shfl_xor(fm, 1)); fm = fminf(fm, __shfl_xor(fm, 2));
        if (lane == 0) *(float*)(lds + LR_CTL + 28) = fminf(fmaxf(fm / f0, 0.8f), 1.f);
        asm volatile("s_waitcnt lgkmcnt(0)" ::: "memory");
        tt = TABLE(0);
      }
      lat = min(r0[2], r1[2]);
      if (miss == 0 && xok) break;
    }
    { auto r = __builtin_amdgcn_permlane16_swap(lat, lat, false, false); lat = min(r[0], r[1]); }
    { auto r = __builtin_amdgcn_permlane32_swap(lat, lat, false, false); lat = min(r[0], r[1]); }
    u32 mm[4];
#pragma unroll
    for (int k = 0; k < 4; ++k)
      mm[k] = __builtin_amdgcn_readlane(lat, 4 * k) + __builtin_amdgcn_readlane(lat, 4 * k + 2) + __builtin_amdgcn_readlane(lat, 4 * (k ^ 2) + 1) + __builtin_amdgcn_readlane(lat, 4 * (k ^ 2) + 3);
    const u32 hb = mm[0] + mm[2] <= mm[1] + mm[3] ? 0u : 1u;
    const u32 best = (hb << 13) | ((hb ? mm[1] <= mm[3] : mm[0] <= mm[2]) ? CLS0 : 16384u - CLS0);
    if (lane == 0) __builtin_amdgcn_raw_buffer_store_b128((u32x4){rt32() + TD0, tt, best, tag}, rsx, 0, XSTA, 16);
  }
  XWAIT(2)
  LBAR();
  {
    u64 pc; asm volatile("s_getpc_b64 %0" : "=s"(pc));
    const u32 cv = __builtin_amdgcn_raw_buffer_load_b32(__builtin_amdgcn_make_buffer_rsrc((void*)pc0, 0, clen, 0x00020000), (int)(pc - pc0) + tid * 64, 0, 0);
    asm volatile("" :: "v"(cv));
  }
  u32x4 st = wait_start(rsx, fb, tag, false);
  const int flip = __builtin_amdgcn_readfirstlane(st[2]) & 8192, cls = __builtin_amdgcn_readfirstlane(st[2]) & 16384;
  o1c ^= cls; o3c ^= cls; sto ^= cls; kc[4] ^= cls; kc[8] ^= cls; kc[9] ^= cls; kc[10] ^= cls; kc[11] ^= cls;
  { const u32 now = rt32(); if (st[0] - now + 100000u > 200000u) st[0] = now; }
  u32x16 kd = {(u32)((ld01 + XR2(vcu ^ 1)) ^ cls), (u32)((ld01 + XR2(vcu ^ 2)) ^ cls), (u32)((ld01 + XR2(vcu ^ 3)) ^ cls), (u32)(X4B && hk ? OOR : sto), (u32)avr, (u32)(avr + 4096), (u32)(avr + 8192),
               (u32)(avr + 12288), (u32)avw, (u32)(X4C && hk ? OOR : st1 ^ cls), (u32)(ldsb + LR_FRG + lane * 16), (u32)(XSB(lane + 1) + flip), (u32)(lane * 8), (u32)avf,
               (u32)(b == 0 && lane == 0 ? PGO_R + flip : OOR), (u32)(pz1 ^ cls)};
  u32x16 sa = {(u32)flip, (u32)(XSB(1) + flip), 0u, (u32)(fo + NH * 8 * 1024), (u32)(ldsb + LR_FRG + 4096 + w * 1024), (u32)(FO_KM + (head * 4 + ks) * 512),
               (u32)(FO_VM + (head * 8 + 2 * ks) * 256), tb0, st[0] - tb0 - (xcd & 1) * XSK, 0u, (u32)SYO(1), 0u, 0u, 0u, 0u, 0u};
  int snt = -1, synced = 0;
  int cb = __builtin_amdgcn_readfirstlane(st[1]), cc = (cb >> 10) & 1023, cp = (cb >> 20) & 1023;
  cb &= 1023;
#define RESYNC()                                                                                                      \
  {                                                                                                                   \
    for (int it = 0; it < 100; ++it) {                                                                                \
      const u32x2 tw = __builtin_amdgcn_raw_buffer_load_b64(rsx, 0, SYO(seg) + 8, 16);                                \
      const u32 tv = __builtin_amdgcn_readfirstlane(tw[0]), to = __builtin_amdgcn_readfirstlane(tw[1]);               \
      if (tv != SENT && to != SENT) {                                                                                 \
        cb = to & 1023; cc = (to >> 10) & 1023; cp = to >> 20; sa[7] = tb0 + seg * cp; sa[8] = tv - sa[7] - (xcd & 1) * XSK;            \
        if (lane == 0) *(u32*)(lds + LR_CTL + 24) = to;                                                               \
        break;                                                                                                        \
      }                                                                                                               \
      asm volatile("s_sleep 4" ::: "memory");                                                                         \
    }                                                                                                                 \
    if (lane == 0) *(u32*)(lds + LR_CTL) = sa[8];                                                                     \
    synced = seg;                                                                                                     \
  }
  int nev = 0; (void)nev;
  bool rsn = false;
  u32x32 keep;
  u32x2 ov;
  asm volatile("" : "={v[60:91]}"(keep));
  while (true) {
    u32x8 sc = {(u32)o1c, (u32)o3c, (u32)snt, (u32)cp, (u32)cb, (u32)cc, 0u, (u32)pgo};
    sa[15] = rsn ? 1u : 0u;
    sa[14] = xp;
#pragma unroll
    for (int i = 0; i < 16; ++i) sa[i] = __builtin_amdgcn_readfirstlane(sa[i]);
#pragma unroll
    for (int i = 0; i < 8; ++i) sc[i] = __builtin_amdgcn_readfirstlane(sc[i]);
    asm volatile(
        "s_nop 4\n\t" "s_mov_b32 s60, m0\n\t"
        "s_mov_b32 m0, %[mb]\n\t" "s_mov_b32 s61, s51\n\t" "v_mov_b32 v48, %[tg]\n\t" "s_mov_b32 s64, s50\n\t"
        EXTRA "s_mov_b32 s70, 0\n\t"
        R2BASE
        "s_cmp_eq_u32 s47, 1\n\t" "s_cbranch_scc1 .LB%=\n\t"
        "s_cmp_eq_u32 s47, 2\n\t" "s_cbranch_scc1 .LC%=\n"
        ".LA%=:\n\t"
        CLKW(".Lta%=", "0")
        "s_add_u32 s50, s36, s52\n\t"
        TSA(0) GLD(96, 0)
        "ds_read_b32 v62, v38 offset:16\n\t" "ds_read_b128 v[52:55], v18\n\t" "ds_read_b128 v[56:59], v18 offset:1024\n\t" ZER(64) ZER(68)
        GLD(100, 1024) ZER(72) ZER(76) ZER(24) ZER(28) R2BASE
        GLD(104, 2048)
        "s_mov_b32 m0, s40\n\t" "s_mov_b32 s47, 0\n\t"
        "s_sub_i32 s63, s64, s45\n\t" "s_cmp_lt_i32 s63, 6\n\t" "s_cselect_b32 s63, s59, %[dm]\n\t" "s_nop " STR(APC) "\n\t"
        GLD(108, 3072)
        ".Lfw%=:\n\t"
        WLG(0)
        "v_readfirstlane_b32 s49, v62\n\t"
        "s_cmp_ge_u32 s49, s38\n\t"
        "s_cbranch_scc1 .Lfx%=\n\t"
        "ds_read_b32 v62, v38 offset:16\n\t" "ds_read_b128 v[52:55], v18\n\t" "ds_read_b128 v[56:59], v18 offset:1024\n\t"
        "s_branch .Lfw%=\n"
        ".Lfx%=:\n\t"
        WVM(3) TSA(1) SQ(0, 0) MXA0(96) SQ(1, 0) MXB(96) SQ(2, 0) SQ(3, 0)
        WVM(2) SQ(0, 1) "buffer_load_dwordx4 v44, %[rf], s39 offen sc1 lds\n\t" SQ(1, 1) "buffer_load_dword v48, off, %[rs], s63 sc1\n\t"
        SQ(2, 1) MXA(100) MXB(100) SQ(3, 1) "s_mov_b32 m0, %[mb]\n\t"
        QBLK(2, MXA)
        WVM(2) SQ(0, 3) MXA(108) MXB(108) SQ(1, 3) CMPB SQ(2, 3) SQ(3, 3) TSA(2)
        "s_cbranch_vccnz .Lfail%=\n\t"
        "s_nop 1\n\t" PSA(64) PSW(64, 0) PSW(65, 272) PSA(68) SV(0, 0) PSW(68, 640) PSW(69, 912) PSA(72) SV(1, 0) PSW(72, 1280) PSW(73, 1552) PSA(76) SV(0, 1) PSW(76, 1920) PSW(77, 2192)
        ARRIVE(8) TSA(3)
        RDQ(80, 0) SV(1, 1) RDQ(84, 16) RDQ(88, 4096) SV(0, 2) RDQ(92, 4112) RDQ(112, 8192) SV(1, 2) RDQ(116, 8208) WLG(12) RDQ(120, 12288) SV(0, 3) RDQ(124, 12304) SV(1, 3)
        WLG(8) TSA(4)
        "v_readfirstlane_b32 s48, v62\n\t"
        "s_and_b32 s48, s48, 3\n\t"
        ROLESV "s_cmp_lg_u32 s48, 3\n\t"
        "s_cbranch_scc1 .Lnl%=\n\t"
        WLG(4) PKA(80, 80, 88) PKA(82, 82, 90) PKA(84, 84, 92) PKA(86, 86, 94)
        WLG(0) PKA(112, 112, 120) PKA(114, 114, 122) PKA(116, 116, 124) PKA(118, 118, 126)
        TSA(5) PKA(80, 80, 112) PKA(82, 82, 114) PKA(84, 84, 116) PKA(86, 86, 118)
        "v_cvt_pk_bf16_f32 v72, v80, v81\n\t" "v_cvt_pk_bf16_f32 v73, v82, v83\n\t" "v_cvt_pk_bf16_f32 v74, v84, v85\n\t" "v_cvt_pk_bf16_f32 v75, v86, v87\n\t"
        "s_nop 1\n\t"
        "v_mfma_f32_16x16x32_bf16 v[88:91], v[72:75], v[72:75], 0\n\t"
        "v_mfma_f32_16x16x32_bf16 v[80:83], v[52:55], v[72:75], 0\n\t"
        "v_mfma_f32_16x16x32_bf16 v[84:87], v[56:59], v[72:75], 0\n\t"
        "s_nop 5\n\t"
        "buffer_store_dwordx4 v[88:91], v41, %[rs], s71 offen sc1\n\t"
        "buffer_store_dwordx4 v[80:83], v40, %[rs], s71 offen sc1\n\t"
        "buffer_store_dwordx4 v[84:87], v40, %[rs], s71 offen offset:512 sc1\n\t" TSA(6) TSB(7)
        VPOST TSA(7)
        "s_cmp_eq_u32 s45, 63\n\t"
        "s_cbranch_scc0 .LB%=\n\t"
        "buffer_store_dwordx4 v[72:75], v47, %[rf], s41 offen sc1\n\t"
        "s_branch .LC%=\n"
        ".LB%=:\n\t"
        CLKW(".Ltb%=", "s66")
        "s_mov_b32 s47, 1\n\t"
        R2TEXT
        "s_cbranch_vccz .LC2%=\n\t"
        "s_branch .Lend%=\n"
        ".Lnl%=:\n\t"
        "s_nop 2\n\t" WLG(4) VPOST
        "s_cmp_lg_u32 s48, 0\n\t"
        "s_cbranch_scc1 .LC%=\n"
        ".Lv0%=:\n\t"
        "v_add_u32 v80, 4096, v21\n\t" "v_add_u32 v81, 8192, v21\n\t" "v_add_u32 v82, 12288, v21\n\t"
        "ds_read_b32 v59, v38 offset:12\n\t"
        "ds_read2_b32 v[112:113], v21 offset0:0 offset1:68\n\t" "ds_read2_b32 v[114:115], v21 offset0:16 offset1:84\n\t"
        "ds_read2_b32 v[116:117], v80 offset0:0 offset1:68\n\t" "ds_read2_b32 v[118:119], v80 offset0:16 offset1:84\n\t"
        "ds_read2_b32 v[120:121], v81 offset0:0 offset1:68\n\t" "ds_read2_b32 v[122:123], v81 offset0:16 offset1:84\n\t"
        "ds_read2_b32 v[124:125], v82 offset0:0 offset1:68\n\t" "ds_read2_b32 v[126:127], v82 offset0:16 offset1:84\n\t"
        WLG(0)
        "v_readfirstlane_b32 s49, v59\n\t"
        "s_sub_u32 s49, s49, 4\n\t"
        "s_cmp_lt_i32 s49, s38\n\t"
        "s_cbranch_scc1 .Lv0%=\n\t"
        PKA(112, 112, 116) PKA(114, 114, 118) PKA(120, 120, 124) PKA(122, 122, 126) PKA(112, 112, 120) PKA(114, 114, 122)
        "v_cvt_pk_bf16_f32 v112, v112, v113\n\t" "v_cvt_pk_bf16_f32 v113, v114, v115\n\t"
        "s_add_u32 s49, s45, 1\n\t" "v_mov_b64 v[116:117], -1\n\t" "v_mov_b64 v[118:119], -1\n\t" "v_readlane_b32 s63, v19, s49\n\t"
        "s_cmp_eq_u32 s45, 63\n\t" "s_cselect_b32 s65, 0, s68\n\t"
        CLKL(".Ltv%=", "s65") CLKW(".Ltw%=", "s65")
        "s_lshl_b32 s50, s50, 17\n\t" "s_lshr_b32 s50, s50, 1\n\t" "s_or_b32 s50, s50, s55\n\t" "v_mov_b32 v114, s50\n\t" "s_lshl_b32 s50, s45, 7\n\t"
        "buffer_store_dwordx4 v[116:119], v23, %[rs], s63 offen sc1\n\t"
        "s_xor_b32 s63, s71, " STR(XR2S) "\n\t"
        "buffer_store_dwordx4 v[116:119], v42, %[rs], s63 offen sc1\n\t"
        "buffer_store_dwordx4 v[116:119], v36, %[rs], s63 offen sc1\n\t"
        "buffer_store_dwordx2 v[112:113], v20, %[rf], s42 offen sc1\n\t"
        "buffer_store_dword v114, v22, %[rs], s50 offen sc1\n"
        ".LC%=:\n\t"
        "s_mov_b32 s47, 3\n\t"
        "s_cmp_eq_u32 s45, 63\n\t"
        "s_cbranch_scc1 .Lend%=\n\t"
        CLKL(".Ltl%=", "s67")
        ".LC2%=:\n\t"
        CLKW(".Ltc%=", "s67")
        "s_add_u32 s50, s36, s53\n\t"
        TSW(0)
        GL1(96, 0) PC GL1(100, 1024) PC GL1(104, 2048) PC GL1(108, 3072) PC GL2(112, 0) PC GL2(116, 1024)
        "s_mov_b32 s48, s36\n\t" "s_mov_b32 s36, s37\n\t" "s_add_u32 s45, s45, 1\n\t" "s_add_u32 s38, s38, 4\n\t" "v_readlane_b32 s37, v19, s45\n\t"
        "s_add_u32 s39, s39, " STR(EXF) "\n\t" "s_xor_b32 s40, s40, 4096\n\t" "v_xor_b32 v18, 4096, v18\n\t"
        "s_add_u32 s41, s41, " STR(EXK) "\n\t" "s_add_u32 s42, s42, " STR(EXK) "\n\t" "s_add_u32 s43, s43, s55\n\t"
        GL2(120, 2048) ZER(64) ZER(68) "s_mov_b32 s47, 2\n\t"
        "s_mul_i32 s70, s69, 3\n\t" EXTRA
        GL2(124, 3072)
        "buffer_load_dword v61, off, %[rs], s46 sc1\n\t"
        "s_add_u32 s46, s46, 64\n\t"
        WVM(8) TSW(1) WBLK(0, MXA0) WBLK(1, MXA) WBLK(2, MXA) WBLK(3, MXA) WBLK(4, MXA) WBLK(5, MXA) WBLK(6, MXA)
        WVM(1) TSW(2) SW(0, 7) MXA(124) MXB(124) SW(1, 7) CMPB TSW(3)
        "s_cbranch_vccnz .Lfail%=\n\t"
        "s_nop 3\n\t"
        "v_add_f32_dpp v80, v64, v66 quad_perm:[0,1,2,3] row_mask:0x3 bank_mask:0xf\n\t" "v_add_f32_dpp v81, v65, v67 quad_perm:[0,1,2,3] row_mask:0x3 bank_mask:0xf\n\t"
        "v_add_f32_dpp v80, v68, v70 quad_perm:[0,1,2,3] row_mask:0xc bank_mask:0xf\n\t" "v_add_f32_dpp v81, v69, v71 quad_perm:[0,1,2,3] row_mask:0xc bank_mask:0xf\n\t"
        "s_nop 1\n\t"
        "v_permlane32_swap_b32 v80, v81\n\t"
        "v_add_f32 v80, v80, v81\n\t"
        PSW(80, 3840)
        ARRIVE(16) TSW(4)
        "ds_read_b128 v[80:83], v46\n\t" "ds_read_b128 v[84:87], v46 offset:4096\n\t"
        "ds_read_b128 v[88:91], v46 offset:8192\n\t" "ds_read_b128 v[92:95], v46 offset:12288\n\t"
        "v_readfirstlane_b32 s50, v48\n\t"
        WLG(4) TSW(5)
        "v_readfirstlane_b32 s51, v62\n\t"
        WLG(2) PKA(80, 80, 84) "s_and_b32 s51, s51, 3\n\t" PKA(82, 82, 86) "s_cmp_lg_u32 s51, 3\n\t" WLG(0) PKA(88, 88, 92) PKA(90, 90, 94) PKA(80, 80, 88) PKA(82, 82, 90)
        "v_cvt_pk_bf16_f32 v80, v80, v81\n\t" "v_cvt_pk_bf16_f32 v81, v82, v83\n\t"
        "v_min_u32 v80, -2, v80\n\t" "v_min_u32 v81, -2, v81\n\t"
        "s_cbranch_scc1 .Lnv%=\n\t"
        STC TSW(6)
        WVM(1) "s_branch .Lnw%=\n"
        ".Lnv%=:\n\t" WVM(0)
        ".Lnw%=:\n\t"
        "v_readfirstlane_b32 s49, v61\n\t"
        "s_sub_u32 s50, s50, %[tg]\n\t" "s_cmp_le_u32 s50, 80\n\t" "s_cselect_b32 s50, s50, 0\n\t"
        "s_max_i32 s64, s64, s50\n\t" "s_add_u32 s44, s44, s70\n\t"
        "s_not_b32 s49, s49\n\t" "s_or_b32 s49, s49, s61\n\t"
        "s_sub_i32 s50, s64, s45\n\t" "s_cmp_lt_i32 s50, " STEPCHK "\n\t" "s_cselect_b32 s50, 1, 0\n\t"
        "s_or_b32 s50, s50, s49\n\t"
        "s_cbranch_scc0 .LA%=\n\t"
        "s_mov_b32 s47, 4\n\t"
        "s_branch .Lend%=\n" ".Lfail%=:\n\t" "s_nop 7\n" ".Lend%=:\n\t"
        "s_waitcnt vmcnt(0) lgkmcnt(0)\n\t"
        ROLEOUT "s_mov_b32 s50, s64\n\t" "s_mov_b32 m0, s60"
        : "+{s[36:51]}"(sa), "+{v[8:23]}"(kd), "+{v[60:91]}"(keep), "=&{v[116:117]}"(ov) FINEO
        : "{v[32:47]}"(kc), "{s[52:59]}"(sc), [rs] "s"(rsx), [rf] "s"(rsf), [dm] "s"(__builtin_amdgcn_readfirstlane(PGO_Y + b * 128)), [tg] "s"(__builtin_amdgcn_readfirstlane(tag)), [r2] "s"(__builtin_amdgcn_readfirstlane(XR2B + flip)), [mb] "s"(__builtin_amdgcn_readfirstlane(ldsb + w * 4096)),
          "{a[0:31]}"(wa[0]), "{a[32:63]}"(wa[1]), "{a[64:95]}"(wa[2]), "{a[96:127]}"(wa[3]), "{a[128:159]}"(wa[4]), "{a[160:191]}"(wa[5]),
          "{v[128:159]}"(wo[0]), "{v[160:191]}"(wo[1]), "{v[192:223]}"(wo[2]), "{v[224:255]}"(wo[3])
        : CL8(24, 25, 26, 27, 28, 29, 30, 31), "v48", CL8(52, 53, 54, 55, 56, 57, 58, 59), "v92", "v93", "v94", "v95", CL96, "v112", "v113", "v114", "v115", "v118", "v119", CL120,
          "s60", "s61", "s62", "s63", "s64", "s65", "s66", "s67", "s68", "s69", "s70", "s71", "s72", "s73", "s74", "s75", "s76", "s77", "vcc", "scc", "memory");
    const int code = __builtin_amdgcn_readfirstlane(sa[11]);
    xp = __builtin_amdgcn_readfirstlane(sa[14]);
    if (code == 2) {
      sa[1] = sa[0]; sa[0] = sa[12]; sa[9] -= 1; sa[2] -= 4; sa[3] -= EXF; sa[4] ^= 4096; kd[10] ^= 4096;
      sa[5] -= EXK; sa[6] -= EXK; sa[7] -= cp; sa[10] -= 64;
    }
    const int seg = __builtin_amdgcn_readfirstlane(sa[9]);
    if (code == 3) break;
    if (code == 4) {
      XWAIT(seg + 2)
      if (sa[13] != 0) {
        rsn = false;
        if (b == 0 && sa[15] == 3) {
          const int l = min((int)*(const u32*)(lds + LR_CTL + 20), 40);
          const u32 tt = TABLE(l);
          if (lane == 0) {
            __builtin_amdgcn_raw_buffer_store_b64((u32x2){nosent(rt32() + TD), tt}, rsx, 0, SYO(seg) + 8, 16);
            if (seg > 2) *(u32*)(lds + LR_CTL + 20) = l + 1;
          }
        }
        RESYNC()
      }
      sa[11] = 0;
      continue;
    }
    if (code == 1) {
      u32 mx = 0;
#pragma unroll
      for (int i = 0; i < 20; ++i) if (i < 12 || i >= 16) mx = max(mx, keep[i]);
      if (__ballot(mx == SENT) == 0 || tries > TRY_CAP) { __builtin_amdgcn_raw_buffer_store_b64(ov, rsx, sto, sa[0], 16); sa[11] = 2; continue; }
    }
    ++tries; tcnt += 1 << (10 * code);
    {
      const u32 to = lds_word(ctl + 24);
      if (to != 0 && (int)(to >> 20) != cp) { cb = to & 1023; cc = (to >> 10) & 1023; cp = to >> 20; sa[7] = tb0 + seg * cp; sa[8] = 0; }
    }
    if (tries > TRY_CAP) snt = snt == -1 ? 0 : snt + 1;
    if (code == 0 && synced != seg && __builtin_amdgcn_readfirstlane(__builtin_amdgcn_raw_buffer_load_b32(rsx, 0, SYO(seg), 16)) != SENT) {
      RESYNC()
      continue;
    }
    if (b == 0 && !rsn) {
      if (lane == 0) __builtin_amdgcn_raw_buffer_store_b32(1u, rsx, 0, SYO(seg + 1), 16);
      rsn = true;
    }
    sa[8] = max(sa[8], rt32() + TR - sa[7] - (code == 0 ? 0 : (code == 1 ? cb : cc) + code * TEX(seg)));
    if (lane == 0) atomicMax((u32*)(lds + LR_CTL), sa[8]);
  }
  LBAR();
  if (tid == 0) __builtin_amdgcn_raw_buffer_store_b32(1u, rsx, 0, PGO_D + vcu * 128, 16);
  if (b == 0 && w == 1) {
    for (int it = 0; it < SPIN_CAP; ++it) {
      asm volatile("" ::: "memory");
      if (__ballot(__builtin_amdgcn_raw_buffer_load_b32(rsx, lane * 128, PGO_S, 16) != tag || __builtin_amdgcn_raw_buffer_load_b32(rsx, lane * 128 + 8192, PGO_S, 16) != tag) == 0) break;
    }
    if (lane == 0) __builtin_amdgcn_raw_buffer_store_b32(tag, rsx, 0, XEPO, 16);
  }
}

#define XSTAT 3
#define XVM 8
#define XP0 -1
#define XSC " sc1"
#define XPITCH 1056
#define XSLOT (32 * XPITCH)
#define XW(HB, t) "a[" STR(HB) "+8*xk+4*" #t ":" STR(HB) "+8*xk+4*" #t "+3]"
#define XF(rt) "v[96+8*(xk&3)+4*" #rt ":96+8*(xk&3)+4*" #rt "+3]"
#define XFN(rt) "v[96+8*((xk+3)&3)+4*" #rt ":96+8*((xk+3)&3)+4*" #rt "+3]"
#define XA(AC, o) "v[" STR(AC) "+" #o ":" STR(AC) "+" #o "+3]"
#define XM1(d, a, b, FIRST) ".if (xk == 0) && " STR(FIRST) "\n\tv_mfma_f32_16x16x32_bf16 " d ", " a ", " b ", 0\n\t.else\n\tv_mfma_f32_16x16x32_bf16 " d ", " a ", " b ", " d "\n\t.endif\n\t"
#define XMF_QK(i, HB, AC, F) XMF_QK##i(HB, AC, F)
#define XMF_QK0(HB, AC, F) XM1(XA(AC, 0), XW(HB, 0), XF(0), F)
#define XMF_QK1(HB, AC, F) XM1(XA(AC, 4), XW(HB, 1), XF(0), F)
#define XMF_QK2(HB, AC, F) XM1(XA(AC, 8), XW(HB, 0), XF(1), F)
#define XMF_QK3(HB, AC, F) XM1(XA(AC, 12), XW(HB, 1), XF(1), F)
#define XMF_V(i, HB, AC, F) XMF_V##i(HB, AC, F)
#define XMF_V0(HB, AC, F) XM1(XA(AC, 0), XF(0), XW(HB, 0), F)
#define XMF_V1(HB, AC, F) XM1(XA(AC, 8), XF(0), XW(HB, 1), F)
#define XMF_V2(HB, AC, F) XM1(XA(AC, 4), XF(1), XW(HB, 0), F)
#define XMF_V3(HB, AC, F) XM1(XA(AC, 12), XF(1), XW(HB, 1), F)
#define XRD(rt, CUR, NXT) ".if xk < 13\n\tds_read_b128 " XFN(rt) ", " CUR " offset:64*(xk+3)+" STR(rt * 16 * XPITCH) "\n\t.else\n\t" \
                          "ds_read_b128 " XFN(rt) ", " NXT " offset:64*(xk-13)+" STR(rt * 16 * XPITCH) "\n\t.endif\n\t"
#define XCV(EPI, PV, i) ".if " STR(EPI) " && (xk >= 1) && (xk < 3)\n\tv_cvt_pk_bf16_f32 v[128+4*(xk-1)+" #i "], v[" STR(PV) "+8*(xk-1)+2*" #i "], v[" STR(PV) "+8*(xk-1)+2*" #i "+1]\n\t.endif\n\t"
#define XHALF(MF, HB, AC, PV, EPI, CUR, NXT, DSLOT, GOFF)                                                   \
  ".set xk, 0\n\t"                                                                                          \
  ".rept 16\n\t"                                                                                            \
  "s_waitcnt lgkmcnt(4)\n\t"                                                                                \
  MF(0, HB, AC, EPI) XRD(0, CUR, NXT) XCV(EPI, PV, 0)                                                       \
  ".if (xk >= 5) && (xk < 13)\n\t"                                                                          \
  "s_add_u32 m0, m0, " STR(XPITCH) "\n\t"                                                                   \
  ".endif\n\t"                                                                                              \
  MF(1, HB, AC, EPI) XRD(1, CUR, NXT) XCV(EPI, PV, 1)                                                       \
  ".if (xk >= 5) && (xk < 13)\n\t"                                                                          \
  "s_add_u32 %[dg], %[dg], 2048\n\t"                                                                        \
  ".endif\n\t"                                                                                              \
  MF(2, HB, AC, EPI) XCV(EPI, PV, 2)                                                                        \
  ".if (xk >= 4) && (xk < 12)\n\t"                                                                          \
  "buffer_load_dwordx4 %[l16], %[rx], %[dg] offen lds\n\t"                                                  \
  ".endif\n\t"                                                                                              \
  ".if xk == 0\n\t"                                                                                         \
  "s_add_u32 m0, %[dl], " STR((DSLOT) * XSLOT) "\n\t" "s_add_u32 %[dg], %[gb], " STR(GOFF) "\n\t"           \
  ".endif\n\t"                                                                                              \
  ".if " STR(EPI) " && (xk == " STR(XSTAT) ")\n\t"                                                                       \
  "buffer_store_dwordx4 v[128:131], v137, %[rf], %[sg] offen" XSC "\n\t"                                       \
  ".endif\n\t"                                                                                              \
  ".if " STR(EPI) " && (xk == 13)\n\t"                                                                      \
  "v_mov_b32 v137, %[l16]\n\t" "v_mov_b32 v138, %[lp2]\n\t"                                                 \
  ".endif\n\t"                                                                                              \
  ".if " STR(EPI) " && (xk == 14)\n\t"                                                                      \
  "s_add_u32 %[sg], %[sg], " STR(NH * 8 * 1024) "\n\t"                                                      \
  "v_add_u32 v136, 1, v136\n\t"                                                                             \
  ".endif\n\t"                                                                                              \
  ".if !" STR(EPI) " && (xk == 1)\n\t"                                                                      \
  "buffer_store_dword v136, %[fo], %[rp], 0 offen sc1\n\t"                                                  \
  ".endif\n\t"                                                                                              \
  MF(3, HB, AC, EPI) XCV(EPI, PV, 3)                                                                        \
  ".if xk == 0\n\t"                                                                                         \
  "s_waitcnt vmcnt(" STR(XVM) ")\n\t" "s_barrier\n\t"                                                               \
  ".endif\n\t"                                                                                              \
  ".if xk == 2\n\t"                                                                                         \
  "s_min_u32 %[dg], %[dg], " STR(S_LEN * DM * 2 - 16384 + 1024) "\n\t"                                      \
  ".endif\n\t"                                                                                              \
  ".if " STR(EPI) " && (xk == " STR(XSTAT) ")\n\t"                                                                       \
  "buffer_store_dwordx4 v[132:135], v138, %[rf], %[sg] offen" XSC "\n\t"                                       \
  ".endif\n\t"                                                                                              \
  ".set xk, xk+1\n\t"                                                                                       \
  ".endr\n\t"
#define XLOOP(MF)                                                                                           \
  "s_mov_b32 %[kp], m0\n\t"                                                                                 \
  "v_mov_b32 v136, %[p0]\n\t" "v_mov_b32 v137, " STR(OOR) "\n\t" "v_mov_b32 v138, " STR(OOR) "\n\t"                                                                                \
  "ds_read_b128 v[96:99], %[r0]\n\tds_read_b128 v[100:103], %[r0] offset:" STR(16 * XPITCH) "\n\t"          \
  "ds_read_b128 v[104:107], %[r0] offset:64\n\tds_read_b128 v[108:111], %[r0] offset:64+" STR(16 * XPITCH) "\n\t"   \
  "ds_read_b128 v[112:115], %[r0] offset:128\n\tds_read_b128 v[116:119], %[r0] offset:128+" STR(16 * XPITCH) "\n"   \
  ".Lx%=:\n\t"                                                                                              \
  XHALF(MF, 0, 64, 80, 1, "%[r0]", "%[r1]", 3, 66560) XHALF(MF, 128, 64, 80, 0, "%[r1]", "%[r2]", 0, 131072) \
  XHALF(MF, 0, 80, 64, 1, "%[r2]", "%[r3]", 1, 132096) XHALF(MF, 128, 80, 64, 0, "%[r3]", "%[r0]", 2, 196608) \
  "s_add_u32 %[gb], %[gb], 131072\n\t"                                                                      \
  "s_sub_u32 %[cnt], %[cnt], 1\n\t"                                                                         \
  "s_cmp_lg_u32 %[cnt], 0\n\t"                                                                              \
  "s_cbranch_scc1 .Lx%=\n\t"                                                                                \
  "s_mov_b32 m0, %[kp]\n\t"                                                                                 \
  "s_nop 7\n\t" "s_waitcnt lgkmcnt(0)"

template <bool ISV>
__device__ __forceinline__ void x_role(unsigned char* lds, const u16* __restrict__ x, const u16* __restrict__ Wm, int fo0, int head,
                                       __amdgpu_buffer_rsrc_t rsf, u32* prog, u32 tag, int lane, int w) {
  const int ldsb = (int)(size_t)(LAS unsigned char*)lds;
  const __amdgpu_buffer_rsrc_t rsxin = __builtin_amdgcn_make_buffer_rsrc((void*)x, 0, S_LEN * DM * 2, 0x00020000);
  const __amdgpu_buffer_rsrc_t rsp = __builtin_amdgcn_make_buffer_rsrc((void*)prog, 0, 128, 0x00020000);
  const int lr = lane & 15, lg = lane >> 4;
  const int xrd = ldsb + lr * XPITCH + lg * 16;
  const int fob = fo0 + (ISV ? (head * 8 + 2 * w) : (head * 8 + w)) * 1024;
  const int p2 = ISV ? 1024 : 4096;
  u32x32 xw[8];
  {
    const __amdgpu_buffer_rsrc_t rw = __builtin_amdgcn_make_buffer_rsrc((void*)Wm, 0, DM * HD * 2, 0x00020000);
    const int vq = (lane >> 4) * (HD * 2) + head * 256 + (lane & 15) * 16;
#define XWISSUE(c) _Pragma("unroll") for (int j = 0; j < 8; ++j)                                                       \
      asm volatile("s_mov_b32 m0, %0\n\ts_nop 3\n\tbuffer_load_dwordx4 %1, %2, %3 offen lds"                           \
                   :: "s"(ldsb + ((c) & 3) * XSLOT + (8 * w + j) * XPITCH), "v"(vq), "s"(rw), "s"((128 * (c) + 32 * w + 4 * j) * (HD * 2)) : "memory");
#define XXISSUE(h) _Pragma("unroll") for (int j = 0; j < 8; ++j)                                                       \
      asm volatile("s_mov_b32 m0, %0\n\ts_nop 3\n\tbuffer_load_dwordx4 %1, %2, %3 offen lds"                           \
                   :: "s"(ldsb + (h) * XSLOT + (8 * w + j) * XPITCH), "v"(lane * 16), "s"(rsxin), "s"((((h) >> 1) * 32 + 8 * w + j) * 2048 + ((h) & 1) * 1024) : "memory");
    XWISSUE(0) XWISSUE(1) XWISSUE(2) XWISSUE(3)
    const int q = lr >> 2, p = lr & 3;
#pragma unroll
    for (int c = 0; c < 8; ++c) {
      asm volatile("s_waitcnt vmcnt(24)\n\ts_barrier" ::: "memory");
#pragma unroll
      for (int s = 0; s < 4; ++s)
#pragma unroll
        for (int t = 0; t < 2; ++t) {
          const unsigned char* base = lds + (c & 3) * XSLOT + (8 * s + 2 * lg) * XPITCH + q * 256 + w * 64 + p * 16 + t * 8;
          const u32x2 t0 = __builtin_bit_cast(u32x2, __builtin_amdgcn_ds_read_tr16_b64_v4i16((LAS v4i16_t*)(base)));
          const u32x2 t1 = __builtin_bit_cast(u32x2, __builtin_amdgcn_ds_read_tr16_b64_v4i16((LAS v4i16_t*)(base + XPITCH)));
          const int o = ((4 * c + s) * 2 + t) * 4;
          xw[o >> 5][(o & 31) + 0] = t0[0]; xw[o >> 5][(o & 31) + 1] = t0[1]; xw[o >> 5][(o & 31) + 2] = t1[0]; xw[o >> 5][(o & 31) + 3] = t1[1];
        }
      LBAR();
      if (c < 4) { XWISSUE(c + 4) } else if (c < 7) { XXISSUE(c - 4) }
    }
  }
  asm volatile("s_waitcnt vmcnt(8)\n\ts_barrier" ::: "memory");
  u32x16 xacc;
  {
    u32 kp, dg; int gb = 8 * w * 2048, cnt = NSEG / 2, sg = fob - NH * 8 * 1024;
#define XASM(MF)                                                                                            \
    asm volatile(XLOOP(MF)                                                                                  \
                 : "=&{v[80:95]}"(xacc), [kp] "=&s"(kp), [dg] "=&s"(dg), [gb] "+s"(gb), [cnt] "+s"(cnt), [sg] "+s"(sg) \
                 : [r0] "v"(xrd), [r1] "v"(xrd + XSLOT), [r2] "v"(xrd + 2 * XSLOT), [r3] "v"(xrd + 3 * XSLOT), [l16] "v"(lane * 16), [lp2] "v"(lane * 16 + p2), \
                   [rx] "s"(rsxin), [rf] "s"(rsf), [rp] "s"(rsp), [fo] "v"(lane == 0 && w == 0 ? 0 : OOR), [dl] "s"(ldsb + 8 * w * XPITCH), [p0] "s"(tag + XP0), \
                   "{a[0:31]}"(xw[0]), "{a[32:63]}"(xw[1]), "{a[64:95]}"(xw[2]), "{a[96:127]}"(xw[3]),      \
                   "{a[128:159]}"(xw[4]), "{a[160:191]}"(xw[5]), "{a[192:223]}"(xw[6]), "{a[224:255]}"(xw[7]) \
                 : CL64, CL72, CL96, CL112, CL120, CL8(128, 129, 130, 131, 132, 133, 134, 135), "v136", "v137", "v138", "scc", "memory");
    if (ISV) { XASM(XMF_V) } else { XASM(XMF_QK) }
  }
#pragma unroll
  for (int jj = 0; jj < 2; ++jj) {
    u32x4 pk;
#pragma unroll
    for (int e = 0; e < 4; ++e) pk[e] = pack2(__uint_as_float(xacc[8 * jj + 2 * e]), __uint_as_float(xacc[8 * jj + 2 * e + 1]));
    __builtin_amdgcn_raw_buffer_store_b128(pk, rsf, lane * 16 + p2 * jj, fob + (NSEG - 1) * (NH * 8 * 1024), 16);
  }
  asm volatile("s_waitcnt vmcnt(0)\n\ts_barrier" ::: "memory");
  if (lane == 0 && w == 0) __hip_atomic_store(prog, tag + NSEG + 8, RLX_AGENT);
}

#define SSM_LO 1
#define SU_CP 4416
#define SU_U 0
#define SU_K (4 * SU_CP)
#define SU_P (SU_K + 10400)
#define SU_D (SU_P + 2048)
#define SU_RAW (SU_D + 2592)
#define SU_CH (SU_RAW + 4128 + 32)
#define SST(i)
__device__ __forceinline__ void ssm_unit(unsigned char* sm, int d0, __amdgpu_buffer_rsrc_t rst, __amdgpu_buffer_rsrc_t rsx, unsigned char* xb, int fb, bool& uok,
                                         const u16* __restrict__ Dskip, int tid, int lane, int w) {
  unsigned char* cb = sm + w * SU_CH;
  const int dd = d0 + w;
  SST(0)
  if (!uok) {
    for (int it = 0; it < SPIN_CAP; ++it) {
      asm volatile("" ::: "memory");
      if ((int)__builtin_amdgcn_readfirstlane(__builtin_amdgcn_raw_buffer_load_b32(rsx, 0, XUDN, 16)) >= 15) break;
      asm volatile("s_sleep 8" ::: "memory");
    }
    asm volatile("" ::: "memory");
    uok = true;
  }
  f32x4 acc[3];
#pragma unroll
  for (int t = 0; t < 3; ++t) acc[t] = __builtin_bit_cast(f32x4, __builtin_amdgcn_raw_buffer_load_b128(rst, lane * 16, 2 * SSM_L * SSM_D * 2 + dd * 3072 + t * 1024, 16));
  const float dsk = bf2f(Dskip[dd]);
  u32x4 uv[4];
#pragma unroll
  for (int i = 0; i < 4; ++i) uv[i] = __builtin_amdgcn_raw_buffer_load_b128(rst, (tid + 256 * i) * 16, d0 * 4096, 16);
  const int r = lane & 31, h = lane >> 5;
  {
    const int lr = lane & 15, lg = lane >> 4;
    SST(2)
    u32* Kd = (u32*)(cb + SU_D);
#pragma unroll
    for (int e = 0; e < 4; ++e) {
      Kd[128 * (lg >> 1) + 8 * lr + 4 * (lg & 1) + e] = split_hl(acc[0][e]);
      Kd[lg < 2 ? 256 + 64 * lg + 4 * lr + e : 384 + 32 * (2 * (lg - 2) + (e >> 1)) + 2 * lr + (e & 1)] = split_hl(acc[1][e]);
      if (lg < 2 || (lg == 2 && e == 0 && lr == 0)) Kd[512 + 16 * (4 * lg + e) + lr] = split_hl(acc[2][e]);
    }
    asm volatile("s_waitcnt lgkmcnt(0)" ::: "memory");
    unsigned char* kr = cb + SU_K + (lane + 1) * 80;
#pragma unroll
    for (int c8 = 0; c8 < 4; ++c8) {
      u32 kv[8];
#pragma unroll
      for (int e = 0; e < 8; ++e) kv[e] = Kd[kidx(32 * lane + 31 - 8 * c8 - e)];
      u32x4 hi, lo;
#pragma unroll
      for (int e = 0; e < 4; ++e) {
        hi[e] = __builtin_amdgcn_perm(kv[2 * e + 1], kv[2 * e], 0x05040100u);
        lo[e] = __builtin_amdgcn_perm(kv[2 * e + 1], kv[2 * e], 0x07060302u);
      }
      *(u32x4*)(kr + 16 * c8) = hi;
      if (SSM_LO) *(u32x4*)(kr + 5200 + 16 * c8) = lo;
    }
    if (lane < 8) *(u32x4*)(cb + SU_K + (lane >> 2) * 5200 + (lane & 3) * 16) = (u32x4){0u, 0u, 0u, 0u};
  }
  SST(3)
#pragma unroll
  for (int i = 0; i < 4; ++i) {
    const int l = 2 * (tid + 256 * i) + 8;
    *(u32*)(sm + 0 * SU_CH + SU_RAW + l * 2) = __builtin_amdgcn_perm(uv[i][2], uv[i][0], 0x05040100u);
    *(u32*)(sm + 1 * SU_CH + SU_RAW + l * 2) = __builtin_amdgcn_perm(uv[i][2], uv[i][0], 0x07060302u);
    *(u32*)(sm + 2 * SU_CH + SU_RAW + l * 2) = __builtin_amdgcn_perm(uv[i][3], uv[i][1], 0x05040100u);
    *(u32*)(sm + 3 * SU_CH + SU_RAW + l * 2) = __builtin_amdgcn_perm(uv[i][3], uv[i][1], 0x07060302u);
  }
  if (lane < 2) *(u32x4*)(cb + SU_RAW + lane * (4096 + 16)) = (u32x4){0u, 0u, 0u, 0u};
  LBAR();
  {
    if (lane < 20) *(u32x4*)(cb + SU_U + (lane / 5) * SU_CP + (lane % 5) * 16) = (u32x4){0u, 0u, 0u, 0u};
#pragma unroll
    for (int i = 0; i < 5; ++i) {
      const int g = lane + 64 * i;
      if (g < 257) {
        const u32x4 a = *(const u32x4*)(cb + SU_RAW + g * 16);
        const u32x2 c = *(const u32x2*)(cb + SU_RAW + g * 16 + 16);
        unsigned char* dst = cb + SU_U + 80 + g * 16;
        *(u32x4*)(dst) = a;
        *(u32x4*)(dst + SU_CP) = (u32x4){__builtin_amdgcn_alignbyte(a[1], a[0], 2), __builtin_amdgcn_alignbyte(a[2], a[1], 2), __builtin_amdgcn_alignbyte(a[3], a[2], 2), __builtin_amdgcn_alignbyte(c[0], a[3], 2)};
        *(u32x4*)(dst + 2 * SU_CP) = (u32x4){a[1], a[2], a[3], c[0]};
        *(u32x4*)(dst + 3 * SU_CP) = (u32x4){__builtin_amdgcn_alignbyte(a[2], a[1], 2), __builtin_amdgcn_alignbyte(a[3], a[2], 2), __builtin_amdgcn_alignbyte(c[0], a[3], 2), __builtin_amdgcn_alignbyte(c[1], c[0], 2)};
      }
    }
  }
  asm volatile("s_waitcnt lgkmcnt(0)" ::: "memory");
  SST(4)
  f32x16 cv0, cv1;
  {
    const int m0 = 48 - 15 + 8 * h + r;
    const int cbo = (int)(size_t)(LAS unsigned char*)cb;
    int ua = cbo + SU_U + (m0 & 3) * SU_CP + (m0 & ~3) * 2 - 32;
    const int kb = cbo + SU_K + 16 * h;
    int a1 = kb + (33 + r) * 80, a0 = kb + (1 + r) * 80, cnt = 32;
#define CRA(S, O) "ds_read_b64 " RNG("v", S, 2) ", %[ua] offset:" STR(O) "\n\t" "ds_read_b64 " RNG("v", (S) + 2, 2) ", %[ua] offset:" STR((O) + 8) "\n\t"
#define CRB(S, ad, O) "ds_read_b128 " RNG("v", S, 4) ", " ad " offset:" STR(O) "\n\t"
#define CMF(C, A, B) "v_mfma_f32_32x32x16_bf16 " RNG("v", C, 16) ", " RNG("v", A, 4) ", " RNG("v", B, 4) ", " RNG("v", C, 16) "\n\t"
#define CUPD "v_add_u32 %[ua], 64, %[ua]\n\t" "v_subrev_u32 %[a1], 80, %[a1]\n\t" "v_subrev_u32 %[a0], 80, %[a0]\n\t" "v_max_i32 %[a0], %[a0], %[kb]\n\t"
#define CHALF(S, N, O, UPD)                                                                       \
    WLG(5) CMF(64, S, (S) + 8) UPD CRA(N, O)                                                      \
    WLG(6) CMF(80, S, (S) + 16) CRB((N) + 8, "%[a0]", O) CRB((N) + 16, "%[a1]", O)                \
    WLG(6) CMF(80, (S) + 4, (S) + 8) CRA((N) + 4, (O) + 2048)                                     \
    WLG(7) CMF(64, S, (S) + 12) CRB((N) + 12, "%[a0]", (O) + 5200)                                \
    WLG(7) CMF(80, S, (S) + 20) CRB((N) + 20, "%[a1]", (O) + 5200)                                \
    CMF(80, (S) + 4, (S) + 12)
    asm volatile(
        CRA(96, 32) CRB(104, "%[a0]", 32) CRB(112, "%[a1]", 32) CRA(100, 32 + 2048) CRB(108, "%[a0]", 32 + 5200) CRB(116, "%[a1]", 32 + 5200)
        ZER(64) ZER(68) ZER(72) ZER(76) ZER(80) ZER(84) ZER(88) ZER(92)
        ".Lc%=:\n\t"
        CHALF(96, 120, 0, "")
        CHALF(120, 96, 32, CUPD)
        "s_sub_u32 %[cnt], %[cnt], 1\n\t"
        "s_cmp_lg_u32 %[cnt], 0\n\t"
        "s_cbranch_scc1 .Lc%=\n\t"
        "s_nop 7\n\t" "s_nop 3\n\t" WLG(0)
        : "=&{v[64:79]}"(cv0), "=&{v[80:95]}"(cv1), [ua] "+v"(ua), [a0] "+v"(a0), [a1] "+v"(a1), [cnt] "+s"(cnt)
        : [kb] "v"(kb)
        : CL96, CL112, CL120, CL8(128, 129, 130, 131, 132, 133, 134, 135), CL8(136, 137, 138, 139, 140, 141, 142, 143),
          CL8(144, 145, 146, 147, 148, 149, 150, 151), CL8(152, 153, 154, 155, 156, 157, 158, 159), "scc", "memory");
  }
  SST(5)
  {
    u16* yo = (u16*)(cb + SU_K);
#pragma unroll
    for (int g = 0; g < 2; ++g)
#pragma unroll
      for (int i = 0; i < 16; i += 4) {
        const int rr = 2 * i + 4 * h, a = 32 * g + r;
        const u32x2 uu = *(const u32x2*)(cb + SU_U + (48 + 32 * a + rr) * 2);
        float y[4];
#pragma unroll
        for (int e = 0; e < 4; ++e) {
          const float uf = __uint_as_float((e & 1) ? (uu[e >> 1] & 0xffff0000u) : (uu[e >> 1] << 16));
          y[e] = rbf_hw(g ? cv1[i + e] : cv0[i + e]) + rbf_hw(uf * dsk);
        }
        *(u32x2*)(yo + a * 40 + rr) = (u32x2){pack2(y[0], y[1]), pack2(y[2], y[3])};
      }
  }
  LBAR();
  SST(6)
#pragma unroll
  for (int i = 0; i < 4; ++i) {
    const int l = 2 * (tid + 256 * i);
    u32 c[4];
#pragma unroll
    for (int j = 0; j < 4; ++j) c[j] = *(const u32*)(sm + j * SU_CH + SU_K + ((l >> 5) * 40 + (l & 31)) * 2);
    __builtin_amdgcn_raw_buffer_store_b128((u32x4){__builtin_amdgcn_perm(c[1], c[0], 0x05040100u), __builtin_amdgcn_perm(c[3], c[2], 0x05040100u),
                                                   __builtin_amdgcn_perm(c[1], c[0], 0x07060302u), __builtin_amdgcn_perm(c[3], c[2], 0x07060302u)},
                                           rst, l * 8, SSM_L * SSM_D * 2 + d0 * 4096, 16);
  }
  asm volatile("s_waitcnt vmcnt(0)" ::: "memory");
  LBAR();
  if (tid == 0) __hip_atomic_fetch_add((u32*)(xb + XYDN), 1u, RLX_AGENT);
  SST(7)
}

#define L_TOTAL 163840
__device__ __forceinline__ int ldw(__amdgpu_buffer_rsrc_t rs, int off) {
  asm volatile("" ::: "memory");
  return __builtin_amdgcn_readfirstlane(__builtin_amdgcn_raw_buffer_load_b32(rs, 0, off, 16));
}
#define GF(rt) "v[64+8*(xk&7)+4*" #rt ":64+8*(xk&7)+4*" #rt "+3]"
#define GW2 "v[128+4*xk:128+4*xk+3]"
#define GW3 "v[24+4*(xk&1):24+4*(xk&1)+3]"
#define GLOAD(S, K) "s_xor_b32 %[tq], " S ", 1024*(" K ")\n\t"
#define GTR(K) "ds_read_b128 v[24+4*((" K ")&1):24+4*((" K ")&1)+3], %[wl] offset:1024*((" K ")&31)\n\t"
#define GPACE 0
#define YAUX 16
#define UDELAY 100
#define GNOSSM 1
#define GNG 3
#define GS0 22
__device__ __forceinline__ void g_role(unsigned char* lds, const u16* __restrict__ Wo, u16* __restrict__ out, __amdgpu_buffer_rsrc_t rsx,
                                       __amdgpu_buffer_rsrc_t rsf, int fb, u32 tag, int grp, int sl, int w) {
  const int lane = fresh_lane();
  const int lr = lane & 15, lg = lane >> 4;
  const int rot = (2 * sl + 8 * w) & 31;
  const int ldsb = (int)(size_t)(LAS unsigned char*)lds;
  u32x32 xw[8], w2[4];
  LBAR();
  {
    const __amdgpu_buffer_rsrc_t rw = __builtin_amdgcn_make_buffer_rsrc((void*)Wo, 0, DM * HD * 2, 0x00020000);
    const int vq = (lane >> 3) * (DM * 2) + sl * 128 + (lane & 7) * 16;
    const int sq = (1024 * w + rot * 32) * (DM * 2);
#define GSLOT(c) ((c) < 8 ? (((c) & 3) == 3 ? 131072 + w * 8192 : w * 32768 + 8192 * (((c) & 3) + 1)) : (((c) - 8) % 3 == 2 ? 131072 + w * 8192 : w * 32768 + 8192 * (((c) - 8) % 3 + 2)))
#define GISSUE(c) _Pragma("unroll") for (int j = 0; j < 8; ++j)                                                        \
      asm volatile("s_mov_b32 m0, %0\n\ts_nop 3\n\tbuffer_load_dwordx4 %1, %2, %3 offen lds"                           \
                   :: "s"(ldsb + GSLOT(c) + j * 1024), "v"(vq), "s"(rw), "s"(sq ^ ((64 * (c) + 8 * j) * DM * 2)) : "memory");
    GISSUE(0) GISSUE(1) GISSUE(2) GISSUE(3)
    const int q = lr >> 2, p = lr & 3;
    u32x4 w3[16];
#pragma unroll
    for (int c = 0; c < 16; ++c) {
      if (c < 5) waitvm<24>(); else if (c < 14) waitvm<16>(); else if (c == 14) waitvm<8>(); else waitvm<0>();
      u32x4 f3[2];
#pragma unroll
      for (int s = 0; s < 2; ++s)
#pragma unroll
        for (int t = 0; t < 4; ++t) {
          const unsigned char* base = lds + GSLOT(c) + (32 * s + 8 * lg + q) * 128 + (t < 2 ? p * 16 + t * 8 : t * 32 + p * 8);
          const u32x2 t0 = __builtin_bit_cast(u32x2, __builtin_amdgcn_ds_read_tr16_b64_v4i16((LAS v4i16_t*)(base)));
          const u32x2 t1 = __builtin_bit_cast(u32x2, __builtin_amdgcn_ds_read_tr16_b64_v4i16((LAS v4i16_t*)(base + 512)));
          const int o = t < 2 ? ((2 * c + s) * 2 + t) * 4 : (2 * c + s) * 4;
          if (t < 2) { xw[o >> 5][(o & 31) + 0] = t0[0]; xw[o >> 5][(o & 31) + 1] = t0[1]; xw[o >> 5][(o & 31) + 2] = t1[0]; xw[o >> 5][(o & 31) + 3] = t1[1]; }
          else if (t == 2) { w2[o >> 5][(o & 31) + 0] = t0[0]; w2[o >> 5][(o & 31) + 1] = t0[1]; w2[o >> 5][(o & 31) + 2] = t1[0]; w2[o >> 5][(o & 31) + 3] = t1[1]; }
          else f3[s] = (u32x4){t0[0], t0[1], t1[0], t1[1]};
        }
      asm volatile("s_waitcnt lgkmcnt(0)" ::: "memory");
      if (c < 8) { *(u32x4*)(lds + w * 32768 + (2 * c) * 1024 + lane * 16) = f3[0]; *(u32x4*)(lds + w * 32768 + (2 * c + 1) * 1024 + lane * 16) = f3[1]; }
      else { w3[2 * (c - 8)] = f3[0]; w3[2 * (c - 8) + 1] = f3[1]; }
      if (GPACE) __builtin_amdgcn_s_sleep(GPACE);
      if (c < 4) { GISSUE(c + 4) } else if (c >= 5 && c < 13) { GISSUE(c + 3) }
    }
#pragma unroll
    for (int i = 0; i < 16; ++i) *(u32x4*)(lds + w * 32768 + (16 + i) * 1024 + lane * 16) = w3[i];
    LBAR();
  }
  wait_start(rsx, fb, tag, true);
  const int wl = ldsb + w * 32768 + lane * 16;
  const __amdgpu_buffer_rsrc_t rso = __builtin_amdgcn_make_buffer_rsrc((void*)out, 0, S_LEN * OUTLD * 2, 0x00020000);
  const int rw_ = ldsb + 131072 + w * 8192 + lane * 16, rr_ = ldsb + 131072 + w * 2048 + lane * 16;
  const int oa = ((16 * (w & 1) + lr) * OUTLD + 64 * sl + (w < 2 ? 8 * lg : 32 + 4 * lg)) * 2, ob = oa + (w < 2 ? 8 : 32);
  const int hq = (grp + GNG - (62 - GS0) % GNG) % GNG - 1, hf = hq < 2 ? hq : -1;
#define GRW(i) "ds_write_b128 %[rw], " RNG("v", 32 + 4 * (i), 4) " offset:" STR(1024 * (i)) "\n\t"
#define GRR(i) "ds_read_b128 " RNG("v", 32 + 4 * (i), 4) ", %[rr] offset:" STR(8192 * ((i) >> 1) + 1024 * ((i) & 1)) "\n\t"
#define GNXT(D, S) "s_cmp_lt_u32 " S ", " STR(GS0) "\n\t" "s_cselect_b32 " D ", 1, " STR(GNG) "\n\t" "s_add_u32 " D ", " D ", " S "\n\t"
#define GDN(S) "s_min_u32 s41, " S ", 63\n\t" "s_lshl_b32 s41, s41, 5\n\t" "v_add_u32 v23, s41, %[u0]\n\t" "v_lshrrev_b32 v22, 6, v23\n\t" \
               "v_and_b32 v23, 63, v23\n\t" "v_lshlrev_b32 v23, 7, v23\n\t" "s_lshr_b32 s41, s41, 6\n\t" "s_max_u32 s41, s41, 29\n\t" "s_lshl_b32 s41, s41, 13\n\t" "s_add_u32 s41, s41, %[pa]\n\t" \
               "buffer_load_dword v23, v23, %[rs], s41 offen sc1\n\t"
#define GOK(D) "v_cmp_le_i32 vcc, v23, v22\n\t" "s_cmp_eq_u64 vcc, 0\n\t" "s_cselect_b32 " D ", 1, 0\n\t"
static_assert(NAW == 64, "shifts above");
#define GEPI(MID, OA, OB) \
      GRW(0) GRW(1) GRW(2) GRW(3) GRW(4) GRW(5) GRW(6) GRW(7) \
      MID \
      WLG(0) "s_barrier\n\t" \
      GRR(0) GRR(1) GRR(2) GRR(3) GRR(4) GRR(5) GRR(6) GRR(7) \
      WLG(4) PKA(32, 32, 40) PKA(34, 34, 42) PKA(36, 36, 44) PKA(38, 38, 46) \
      WLG(0) PKA(48, 48, 56) PKA(50, 50, 58) PKA(52, 52, 60) PKA(54, 54, 62) \
      PKA(32, 32, 48) PKA(34, 34, 50) PKA(36, 36, 52) PKA(38, 38, 54) \
      "v_cvt_pk_bf16_f32 v20, v32, v33\n\t" "v_cvt_pk_bf16_f32 v21, v34, v35\n\t" "v_cvt_pk_bf16_f32 v18, v36, v37\n\t" "v_cvt_pk_bf16_f32 v19, v38, v39\n\t" \
      "buffer_store_dwordx2 v[20:21], " OA ", %[ro], s42 offen\n\t" \
      "buffer_store_dwordx2 v[18:19], " OB ", %[ro], s42 offen\n\t"
#define GH "v[64+4*(xk&15):64+4*(xk&15)+3]"
  asm volatile(
      "s_nop 4\n\t"
      "v_add_u32 v16, 131072, %[va]\n\t" "v_sub_u32 v15, %[ob], %[oa]\n\t" "v_add_u32 v15, v15, %[oc]\n\t"
      "s_mov_b32 s36, %[s0]\n\t" "s_mov_b32 s43, " STR(SPIN_CAP) "\n"
      ".Lp%=:\n\t"
      GDN("s36") WVM(0) GOK("s40")
      "s_cbranch_scc1 .Lr%=\n\t"
      "s_sleep 2\n\t" "s_sub_u32 s43, s43, 1\n\t" "s_cmp_gt_i32 s43, 0\n\t" "s_cbranch_scc1 .Lp%=\n"
      ".Lr%=:\n\t"
      GNXT("s37", "s36") GDN("s37")
      "s_lshl_b32 s38, s36, 18\n\t" "s_add_u32 s38, s38, %[gb]\n\t"
      ".set xk, 0\n\t.rept 8\n\t" GLOAD("s38", "xk")
      "buffer_load_dwordx4 " GF(0) ", %[va], %[rf], %[tq] offen sc1\n\t" "buffer_load_dwordx4 " GF(1) ", v16, %[rf], %[tq] offen sc1\n\t"
      ".set xk, xk+1\n\t.endr\n\t"
      WVM(16) GOK("s40")
      ".Lb%=:\n\t"
      GNXT("s37", "s36") GNXT("s41", "s37") GDN("s41") GTR("0")
      "s_cmp_lt_u32 s37, 63\n\t" "s_cselect_b32 s40, s40, 0\n\t"
      "s_cmp_lg_u32 s40, 0\n\t" "s_cselect_b32 s39, s37, s36\n\t"
      "s_lshl_b32 s39, s39, 18\n\t" "s_add_u32 s39, s39, %[gb]\n\t" "s_lshl_b32 s38, s36, 18\n\t" "s_add_u32 s38, s38, %[gb]\n\t"
      "s_lshl_b32 s42, s36, 17\n\t"
      "buffer_load_dword v17, %[vt], %[rf], s39 offen sc1\n\t"
      ".set xk, 0\n\t.rept 32\n\t"
      "s_waitcnt vmcnt(14)\n\t"
      XM1(XA(32, 0), XW(0, 0), GF(0), 1) GTR("xk+1") XM1(XA(32, 4), XW(0, 1), GF(0), 1)
      ".if xk < 24\n\t" GLOAD("s38", "xk+8") ".else\n\t" GLOAD("s39", "xk-24") ".endif\n\t"
      XM1(XA(32, 8), XW(0, 0), GF(1), 1) XM1(XA(32, 12), XW(0, 1), GF(1), 1)
      XM1(XA(32, 16), GW2, GF(0), 1) "s_waitcnt lgkmcnt(1)\n\t" XM1(XA(32, 20), GW3, GF(0), 1)
      "buffer_load_dwordx4 " GF(0) ", %[va], %[rf], %[tq] offen sc1\n\t"
      XM1(XA(32, 24), GW2, GF(1), 1) XM1(XA(32, 28), GW3, GF(1), 1)
      "buffer_load_dwordx4 " GF(1) ", v16, %[rf], %[tq] offen sc1\n\t"
      ".if xk == 16\n\t" "s_barrier\n\t" ".endif\n\t"
      ".set xk, xk+1\n\t.endr\n\t"
      GEPI(GOK("s41"), "%[oa]", "%[ob]")
      "s_mov_b32 s36, s37\n\t"
      "s_cmp_lg_u32 s40, 0\n\t" "s_mov_b32 s40, s41\n\t" "s_cbranch_scc1 .Lb%=\n\t"
      "s_cmp_lt_u32 s36, 63\n\t" "s_cbranch_scc1 .Lp%=\n\t"
      "s_cmp_lt_i32 %[hf], 0\n\t" "s_cbranch_scc1 .Le%=\n\t"
      "s_mov_b32 s36, 63\n"
      ".Lh%=:\n\t"
      GDN("s36") WVM(0) GOK("s40")
      "s_cbranch_scc1 .Li%=\n\t"
      "s_sleep 2\n\t" "s_sub_u32 s43, s43, 1\n\t" "s_cmp_gt_i32 s43, 0\n\t" "s_cbranch_scc1 .Lh%=\n"
      ".Li%=:\n\t"
      "s_lshl_b32 s38, %[hf], 17\n\t" "s_add_u32 s38, s38, 63*262144\n\t" "s_add_u32 s38, s38, %[gb]\n\t"
      ".set xk, 0\n\t.rept 16\n\t" GLOAD("s38", "xk") "buffer_load_dwordx4 " GH ", %[va], %[rf], %[tq] offen sc1\n\t" ".set xk, xk+1\n\t.endr\n\t"
      GTR("0")
      "s_lshl_b32 s42, %[hf], 16\n\t" "s_add_u32 s42, s42, 63*131072\n\t"
      ".set xk, 0\n\t.rept 32\n\t"
      ".if xk < 17\n\t" "s_waitcnt vmcnt(15)\n\t" ".else\n\t" "s_waitcnt vmcnt(31-xk)\n\t" ".endif\n\t"
      XM1(XA(32, 0), XW(0, 0), GH, 1) GTR("xk+1") XM1(XA(32, 4), XW(0, 1), GH, 1)
      XM1(XA(32, 16), GW2, GH, 1) "s_waitcnt lgkmcnt(1)\n\t" XM1(XA(32, 20), GW3, GH, 1)
      ".if xk < 16\n\t" GLOAD("s38", "xk+16") "buffer_load_dwordx4 " GH ", %[va], %[rf], %[tq] offen sc1\n\t" ".endif\n\t"
      ".if xk == 16\n\t" "s_barrier\n\t" ".endif\n\t"
      ".set xk, xk+1\n\t.endr\n\t"
      "s_nop 7\n\t"
      GEPI("", "%[oc]", "v15")
      ".Le%=:\n\t"
      WVM(0)
      : [tq] "=&s"(sl)
      : [rs] "s"(rsx), [s0] "s"(__builtin_amdgcn_readfirstlane(grp ? GS0 + grp : 0)), [gb] "s"(FO_O - 32768 + (32 * w + rot) * 1024), [va] "v"(lane * 16 + 32768),
        [rf] "s"(rsf), [wl] "v"(wl), [rw] "v"(rw_), [rr] "v"(rr_), [oa] "v"(oa), [ob] "v"(ob), [ro] "s"(rso), [u0] "v"(8 * w + (lane & 7)), [pa] "s"(PGO_A - 29 * 8192), [hf] "s"(__builtin_amdgcn_readfirstlane(hf)), [oc] "v"((w & 1) ? OOR - 64 : oa), [vt] "v"((sl >> 3) * 131072 + 32768 + (sl & 7) * 4096 + lane * 64 - rot * 1024),
        "{a[0:31]}"(xw[0]), "{a[32:63]}"(xw[1]), "{a[64:95]}"(xw[2]), "{a[96:127]}"(xw[3]),
        "{a[128:159]}"(xw[4]), "{a[160:191]}"(xw[5]), "{a[192:223]}"(xw[6]), "{a[224:255]}"(xw[7]),
        "{v[128:159]}"(w2[0]), "{v[160:191]}"(w2[1]), "{v[192:223]}"(w2[2]), "{v[224:255]}"(w2[3])
      : "v15", "v16", "v17", "v18", "v19", "v20", "v21", "v22", "v23", "vcc", CL8(24, 25, 26, 27, 28, 29, 30, 31), CL8(32, 33, 34, 35, 36, 37, 38, 39), CL8(40, 41, 42, 43, 44, 45, 46, 47),
        CL8(48, 49, 50, 51, 52, 53, 54, 55), CL8(56, 57, 58, 59, 60, 61, 62, 63), CL64, CL72, CL80, CL88, CL96, CL112, CL120,
        "s36", "s37", "s38", "s39", "s40", "s41", "s42", "s43", "scc", "memory");
}
static_assert(4 * SU_CH <= L_TOTAL && 4 * XSLOT <= L_TOTAL, "lds");
__global__ void __launch_bounds__(256, 1)
main_kernel(const u16* __restrict__ x, const u16* __restrict__ mem0, const u16* __restrict__ Wq, const u16* __restrict__ Wk, const u16* __restrict__ Wv, const u16* __restrict__ Wo,
            unsigned char* ws, const u16* __restrict__ u, const u16* __restrict__ A_re, const u16* __restrict__ A_im, const u16* __restrict__ B_re,
            const u16* __restrict__ B_im, const u16* __restrict__ C_re, const u16* __restrict__ C_im,
            const u16* __restrict__ log_dt, const u16* __restrict__ Dskip, u16* __restrict__ out
            ) {
  __shared__ __attribute__((aligned(8192))) unsigned char lds[L_TOTAL];
  const int w = __builtin_amdgcn_readfirstlane(threadIdx.x >> 6);
  u64 pc0;
  int clen;
  {
    asm volatile("s_getpc_b64 %0\n.Lkpc0:\n\ts_mov_b32 %1, .Lkpc1-.Lkpc0" : "=s"(pc0), "=s"(clen));
  }
#define LOCAL_IDS const int lane = fresh_lane(), tid = w * 64 + lane; (void)tid;
  const int si = (blockIdx.x >> 3) * 4 + (blockIdx.x & 3);
  unsigned char* xb = (unsigned char*)(((size_t)ws + XPAGE - 1) & ~(size_t)(XPAGE - 1));
  const int fb = (int)(ws + WS_F - xb);
  u32 tag;
  asm volatile("s_load_dword %0, %1, 0x0\n\ts_waitcnt lgkmcnt(0)\n\ts_add_u32 %0, %0, 256" : "=s"(tag) : "s"(ws + WS_F) : "scc", "memory");
  const __amdgpu_buffer_rsrc_t rsx = __builtin_amdgcn_make_buffer_rsrc(xb, 0, fb + (int)WS_F_SZ, 0x00020000);
  const __amdgpu_buffer_rsrc_t rsf = __builtin_amdgcn_make_buffer_rsrc(ws + WS_QX, 0, (int)(4 * WS_QKV_SZ + 2 * WS_KM_SZ), 0x00020000);
  const __amdgpu_buffer_rsrc_t rst = __builtin_amdgcn_make_buffer_rsrc(ws + WS_UT, 0, 2 * SSM_L * SSM_D * 2 + SSM_D * 3072, 0x00020000);
  if ((blockIdx.x & 7) < 4) {
    { LOCAL_IDS
    r_role(lds, si, mem0, Wq, Wk, Wv, Wo, fb, tag, pc0, clen, rsx, rsf, rst, SsmPtrs{A_re, A_im, B_re, B_im, C_re, C_im, log_dt}, tid, lane, w
           ); }
    LOCAL_IDS
    {
      for (int it = 0; it < SPIN_CAP; ++it) {
        if (ldw(rsx, XYDN) >= 255) break;
        asm volatile("s_sleep 8" ::: "memory");
      }
      asm volatile("" ::: "memory");
      asm volatile(".Lkpc1:" ::: "memory");
      unsigned char* im = lds + 65536;
      const __amdgpu_buffer_rsrc_t rso = __builtin_amdgcn_make_buffer_rsrc((void*)out, 0, S_LEN * OUTLD * 2, 0x00020000);
      u32x4 t[8];
#pragma unroll
      for (int i = 0; i < 8; ++i) { const int p = i * 256 + tid; t[i] = __builtin_amdgcn_raw_buffer_load_b128(rst, (p >> 3) * 16384 + (p & 7) * 16, SSM_L * SSM_D * 2 + si * 128, 16); }
#pragma unroll
      for (int i = 0; i < 8; ++i) {
        const int p = i * 256 + tid, un = p >> 3, rp = p & 7;
        *(u32x2*)(im + (2 * rp) * 2064 + un * 8) = (u32x2){t[i][0], t[i][1]};
        *(u32x2*)(im + (2 * rp + 1) * 2064 + un * 8) = (u32x2){t[i][2], t[i][3]};
      }
      LBAR();
#pragma unroll
      for (int i = 0; i < 8; ++i) {
        const int p = i * 256 + tid;
        __builtin_amdgcn_raw_buffer_store_b128(*(const u32x4*)(im + (p >> 7) * 2064 + (p & 127) * 16), rso, ((p >> 7) * OUTLD + DM + (p & 127) * 8) * 2, si * 16 * OUTLD * 2, YAUX);
      }
    }
  } else {
    const int g = blockIdx.x & 3, li = blockIdx.x >> 3;
    const int xi = g == 0 ? (li < 16 ? li : -1) : (li < 26 + (g < 3) ? 16 + 27 * (g - 1) + li : -1);
    const int ei = g == 0 ? li - 16 : 16 + 5 * (g - 1) + (31 - li);
    if (xi >= 0) {
      LOCAL_IDS
      const int head = xi & 31, mi = xi >> 5;
      u32* prog = (u32*)(xb + PGO_X + xi * 128);
      if (mi == 1) x_role<true>(lds, x, Wv, FO_V, head, rsf, prog, tag, lane, w);
      else x_role<false>(lds, x, mi == 0 ? Wk : Wq, mi == 0 ? FO_K : FO_Q, head, rsf, prog, tag, lane, w);
    } else {
      LOCAL_IDS
      u32 acc = 0;
#pragma unroll
      for (int i = 0; i < 4; ++i) acc += *(const u32*)(x + (size_t)((i * 32 + ei) * 256 + tid) * 64);
      {
        const __amdgpu_buffer_rsrc_t rwo = __builtin_amdgcn_make_buffer_rsrc((void*)Wo, 0, DM * HD * 2, 0x00020000);
        const int ldsb = (int)(size_t)(LAS unsigned char*)lds;
#pragma unroll 1
        for (int c = 0; c < 4; ++c) { dma16(rwo, lane * 16, ((ei * 4 + w) * 4 + c) * 16384, 1024, ldsb + w * 32768 + (c & 1) * 16384); waitvm<32>(); }
      }
      if (ei == 0 && tid < 16) acc += *(const u32*)(Dskip + tid * 64);
      asm volatile("" :: "v"(acc));
      if (ei >= 16) {
      { const u32 t0 = rt32(); for (int i = 0; i < 1000 && (int)(rt32() - t0) < UDELAY; ++i) asm volatile("s_sleep 16" ::: "memory"); }
      asm volatile("s_waitcnt vmcnt(0)\n\ts_barrier" ::: "memory");
      for (int ps = 0; ps < 2; ++ps) {
      const int r0 = ((ei - 16) * 2 + ps) * 64;
#pragma unroll
      for (int hb = 0; hb < 2; ++hb) {
        u32x4 t[16];
#pragma unroll
        for (int i = 0; i < 16; ++i) t[i] = *(const u32x4*)(u + (size_t)r0 * SSM_D + ((hb * 16 + i) * 256 + tid) * 8);
#pragma unroll
        for (int i = 0; i < 16; ++i) { const int p = (hb * 16 + i) * 256 + tid; *(u32x4*)(lds + (p >> 7) * 2064 + (p & 127) * 16) = t[i]; }
      }
      LBAR();
      for (int i = 0; i < 32; ++i) {
        const int p = i * 256 + tid, un = p >> 5, rp = p & 31;
        const u32x2 a = *(const u32x2*)(lds + (2 * rp) * 2064 + un * 8), c = *(const u32x2*)(lds + (2 * rp + 1) * 2064 + un * 8);
        __builtin_amdgcn_raw_buffer_store_b128((u32x4){a[0], a[1], c[0], c[1]}, rst, un * 16384 + rp * 16, r0 * 8, 16);
      }
      asm volatile("s_waitcnt vmcnt(0) lgkmcnt(0)\n\ts_barrier" ::: "memory");
      }
      wait_start(rsx, fb, tag, true);
      if (tid == 0) __hip_atomic_fetch_add((u32*)(xb + XUDN), 1u, RLX_AGENT);
      }
    }
    asm volatile("s_waitcnt lgkmcnt(0)\n\ts_barrier" :: "s"(tag) : "memory");
    if (w == 0 && fresh_lane() == 0) __builtin_amdgcn_raw_buffer_store_b32(tag, rsx, 0, PGO_S + si * 128, 16);
    const bool isg = g == 0 ? li >= 16 : (g >= 5 - GNG && li < 16);
    if (xi >= 0 && !(GNOSSM && isg)) {
      LOCAL_IDS
      bool uok = false;
      wait_start(rsx, fb, tag, true);
      while (true) {
        LBAR();
        if (tid == 0) *(u32*)(lds + L_TOTAL - 16) = __hip_atomic_fetch_add((u32*)(xb + XTKT), 1u, RLX_AGENT) + 1u;
        LBAR();
        const int un = __builtin_amdgcn_readfirstlane(*(const u32*)(lds + L_TOTAL - 16));
        if (un >= 256) break;
        ssm_unit(lds, 4 * un, rst, rsx, xb, fb, uok, Dskip, tid, lane, w);
        if (isg) break;
      }
    }
    if (isg) {
      g_role(lds, Wo, out, rsx, rsf, fb, tag, g ? g + GNG - 4 : 0, li & 15, w);
    }
    else {
      const int lane = fresh_lane();
      int sh = (ei - 16) * 4 + w, lim = (NSEG - 4) * NH, wi = sh, dn = 0;
      bool go = xi < 0;
      while (true) {
        if (go) a_role(rsx, rsf, sh, lim, wi, dn, lane, fb, tag);
        if (xi >= 0 && (w >= 2 || go)) break;
        const int t = __builtin_amdgcn_readfirstlane(lane == 0 ? __hip_atomic_fetch_add((u32*)(xb + XTK2), 1u, RLX_AGENT) + 1u : 0u);
        if (t >= 128) break;
        wi = 64 + t; sh = (NSEG - 4) * NH + t; lim = sh + 1; dn = 31; go = true;
      }
    }
  }
}

extern "C" void kernel_launch(void* const* d_in, const int* in_sizes, int n_in, void* d_out, int out_size,
                              void* d_ws, size_t ws_size, hipStream_t stream) {
  const u16* x = (const u16*)d_in[0];
  const u16* mem0 = (const u16*)d_in[1];
  const u16* Wq = (const u16*)d_in[2];
  const u16* Wk = (const u16*)d_in[3];
  const u16* Wv = (const u16*)d_in[4];
  const u16* Wo = (const u16*)d_in[5];
  const u16* u = (const u16*)d_in[7];
  const u16* A_re = (const u16*)d_in[8];
  const u16* A_im = (const u16*)d_in[9];
  const u16* B_re = (const u16*)d_in[10];
  const u16* B_im = (const u16*)d_in[11];
  const u16* C_re = (const u16*)d_in[12];
  const u16* C_im = (const u16*)d_in[13];
  const u16* log_dt = (const u16*)d_in[14];
  const u16* Dskip = (const u16*)d_in[15];
  u16* out = (u16*)d_out;
  char* ws = (char*)d_ws;

  main_kernel<<<256, 256, 0, stream>>>(x, mem0, Wq, Wk, Wv, Wo, (unsigned char*)ws, u, A_re, A_im, B_re, B_im, C_re, C_im, log_dt, Dskip, out
      );
}
```

```cpp
#include <hip/hip_runtime.h>
#include <hip/hip_bf16.h>
#include <stdint.h>

#define S_LEN 2048
#define DM 1024
#define NH 32
#define DH 128
#define HD 4096
#define MEM 8
#define SEG 32
#define NSEG 64
#define LTOK 40
#define SSM_L 2048
#define SSM_D 1024
#define SSM_N 128
#define OUTLD 2048

typedef unsigned short u16;
typedef unsigned int u32;
typedef unsigned long long u64;
typedef __attribute__((ext_vector_type(8))) short bf16x8;
typedef __attribute__((ext_vector_type(4))) float f32x4;
typedef __attribute__((ext_vector_type(4))) unsigned int u32x4;
typedef __attribute__((ext_vector_type(2))) unsigned int u32x2;
typedef __bf16 bf16x2_t __attribute__((ext_vector_type(2)));
typedef short v4i16_t __attribute__((ext_vector_type(4)));

#define SENT 0xFFFFFFFFu
#define RLX_AGENT __ATOMIC_RELAXED, __HIP_MEMORY_SCOPE_AGENT

__device__ __forceinline__ float bf2f(u16 v) { return __uint_as_float(((u32)v) << 16); }
__device__ __forceinline__ u16 f2bf(float f) {
  u32 u = __float_as_uint(f);
  u32 r = (u + 0x7fffu + ((u >> 16) & 1u)) >> 16;
  if ((u & 0x7fffffffu) > 0x7f800000u) r = 0x7fc0u;
  return (u16)r;
}
__device__ __forceinline__ float rbf(float f) { return bf2f(f2bf(f)); }

#define TR 8
#define TP 188
#define TDB 65
#define TDC 115
#define NR1 4
#define XPAGE (2 << 20)
#define XRING 3
#define XLP(p, L) ((((L) >> 13) << 15) + ((p) << 14) + ((L) & 8191))
#define XO_R3 131072
#define XR1A(x, L) XLP((x) & 1, ((x) >> 1) * 16384 + (L))
#define XR3A(rg, L) (XO_R3 + XLP(rg, L))
#define XSB(s) (((s) % XRING) == 2 ? 1048576 : ((s) % XRING) * 262144)
#define XR2B 524288
#define XR2S 1048576
#define XR2(j) (((((j) & 31) + (((j) >> 6) << 5)) / 6 << 15) + (((((j) >> 5) & 1) ^ 1) << 14) + ((((j) & 31) + (((j) >> 6) << 5)) % 6) * 1280)
#define XSYN (1048576 + 262144)
#define PGO_R (XSYN + 32768)
#define XCAL (PGO_R + 32768)
static_assert(XCAL + 16384 <= XPAGE, "exchange page");
#define WS_X 0ull
#define WS_X_SZ ((u64)2 * XPAGE)
#define WS_F (WS_X + WS_X_SZ)
#define XEPO (fb)
#define XSTA (fb + 128)
#define PGO_X (fb + 256)
#define PGO_Y (PGO_X + 96 * 128)
#define PGO_S (PGO_Y + 128 * 128)
#define PGO_D (PGO_S + 128 * 128)
#define PGO_A (PGO_D + 128 * 128)
#define XTKT (PGO_A + 192 * 128)
#define XUDN (XTKT + 128)
#define XYDN (XUDN + 128)
#define XTK2 (XYDN + 128)
#define WS_F_SZ ((u64)(2 + 96 + 128 + 128 + 128 + 192 + 8) * 128)
#define WS_XCH_END (WS_F + 131072)
#define WS_QX (WS_XCH_END)
#define WS_QKV_SZ ((u64)S_LEN * HD * 2)
#define WS_KX (WS_QX + WS_QKV_SZ)
#define WS_VX (WS_KX + WS_QKV_SZ)
#define WS_OX (WS_VX + WS_QKV_SZ)
#define WS_KM (WS_OX + WS_QKV_SZ)
#define WS_KM_SZ ((u64)NSEG * 8 * HD * 2)
#define WS_VM (WS_KM + WS_KM_SZ)
#define WS_UT (WS_VM + WS_KM_SZ)
#define WS_YT (WS_UT + (u64)SSM_L * SSM_D * 2)
#define WS_KD (WS_YT + (u64)SSM_L * SSM_D * 2)
#define WS_TOTAL (WS_KD + (u64)SSM_D * 3072)

#define SPIN_CAP 400000
#define TRY_CAP 20000
#define LAS __attribute__((address_space(3)))
#define LBAR() asm volatile("s_waitcnt lgkmcnt(0)\n\ts_barrier" ::: "memory")
__device__ __forceinline__ u32 max4u(u32x4 v) { return max(max(v[0], v[1]), max(v[2], v[3])); }
typedef float f32x2_t __attribute__((ext_vector_type(2)));
__device__ __forceinline__ u32 pack2(float a, float b) {
  f32x2_t v = {a, b};
  bf16x2_t r = __builtin_convertvector(v, bf16x2_t);
  return __builtin_bit_cast(u32, r);
}
__device__ __forceinline__ float rbf_hw(float a) { return __uint_as_float(pack2(a, a) & 0xffff0000u); }
__device__ __forceinline__ u32 nosent(u32 v) { return min(v, 0xFFFFFFFEu); }
template <int N> __device__ __forceinline__ void waitvm() { asm volatile("s_waitcnt vmcnt(%0)" ::"n"(N) : "memory"); }
typedef __attribute__((ext_vector_type(16))) float f32x16;
__device__ __forceinline__ u32 split_hl(float kv) {
  const u32 h = pack2(kv, kv) & 0xffffu;
  const float r1 = kv - __uint_as_float(h << 16);
  return h | (pack2(r1, r1) & 0xffff0000u);
}
__device__ __forceinline__ int kidx(int idx) {
  const int sh = idx < 256 ? 0 : (idx < 512 ? 1 : (idx < 1024 ? 2 : 3));
  const int q = sh ? ((idx + (1 << (sh - 1)) - 1 + ((idx >> sh) & 1)) >> sh) : idx;
  return q + 128 * sh;
}

#define FO_Q 0
#define FO_K ((int)WS_QKV_SZ)
#define FO_V ((int)(2 * WS_QKV_SZ))
#define FO_O ((int)(3 * WS_QKV_SZ))
#define FO_KM ((int)(4 * WS_QKV_SZ))
#define FO_VM ((int)(4 * WS_QKV_SZ + WS_KM_SZ))
__device__ __forceinline__ u32x4 wait_start(__amdgpu_buffer_rsrc_t rsx, int fb, u32 tag, bool slow) {
  u32x4 st;
  for (int it = 0; it < SPIN_CAP; ++it) {
    asm volatile("" ::: "memory");
    st = __builtin_amdgcn_raw_buffer_load_b128(rsx, 0, XSTA, 16);
    if ((u32)__builtin_amdgcn_readfirstlane(st[3]) == tag) break;
    if (slow) asm volatile("s_sleep 20" ::: "memory"); else asm volatile("s_sleep 3" ::: "memory");
  }
  asm volatile("" ::: "memory");
  return st;
}
__device__ __forceinline__ int xprog(u32 v, u32 tag) { return v - tag <= 80u ? (int)(v - tag) : -1; }
__device__ __forceinline__ float xmax4(float m) {
  { auto r = __builtin_amdgcn_permlane16_swap(__float_as_uint(m), __float_as_uint(m), false, false); m = __builtin_fmaxf(__uint_as_float(r[0]), __uint_as_float(r[1])); }
  { auto r = __builtin_amdgcn_permlane32_swap(__float_as_uint(m), __float_as_uint(m), false, false); m = __builtin_fmaxf(__uint_as_float(r[0]), __uint_as_float(r[1])); }
  return m;
}
__device__ __forceinline__ float xsum4(float m) {
  { auto r = __builtin_amdgcn_permlane16_swap(__float_as_uint(m), __float_as_uint(m), false, false); m = __uint_as_float(r[0]) + __uint_as_float(r[1]); }
  { auto r = __builtin_amdgcn_permlane32_swap(__float_as_uint(m), __float_as_uint(m), false, false); m = __uint_as_float(r[0]) + __uint_as_float(r[1]); }
  return m;
}
#define MF16(a, b, c) __builtin_amdgcn_mfma_f32_16x16x32_bf16(__builtin_bit_cast(bf16x8, a), __builtin_bit_cast(bf16x8, b), c, 0, 0, 0)
__device__ __forceinline__ f32x4 rnd2(f32x4 v, float sc) {
  const u32 a = pack2(v[0], v[1]), b = pack2(v[2], v[3]);
  const u32 c = pack2(__uint_as_float(a << 16) * sc, __uint_as_float(a & 0xffff0000u) * sc), d = pack2(__uint_as_float(b << 16) * sc, __uint_as_float(b & 0xffff0000u) * sc);
  return (f32x4){__uint_as_float(c << 16), __uint_as_float(c & 0xffff0000u), __uint_as_float(d << 16), __uint_as_float(d & 0xffff0000u)};
}
struct XaOps { u32x4 q[2][4], k[2][4], v[8]; };
__device__ __forceinline__ void xa_load(XaOps& o, __amdgpu_buffer_rsrc_t rsf, int sh, int lane) {
#pragma unroll
  for (int i = 0; i < 8; ++i) {
    o.q[i >> 2][i & 3] = __builtin_amdgcn_raw_buffer_load_b128(rsf, lane * 16, FO_Q + (sh * 8 + i) * 1024, 16);
    o.k[i >> 2][i & 3] = __builtin_amdgcn_raw_buffer_load_b128(rsf, lane * 16, FO_K + (sh * 8 + i) * 1024, 16);
  }
#pragma unroll
  for (int dt = 0; dt < 8; ++dt) o.v[dt] = __builtin_amdgcn_raw_buffer_load_b128(rsf, lane * 16, FO_V + (sh * 8 + dt) * 1024, 16);
}
__device__ __forceinline__ void xa_compute(const XaOps& o, const u32x4 (&km)[4], const u32x2 (&vm)[8], __amdgpu_buffer_rsrc_t rsf, int sh, int lane) {
  const int lr = lane & 15, lg = lane >> 4;
  const float scale = 0.08838834764831845f, L2E = 1.4426950408889634f, NEG = -1000000000.0f;
  const f32x4 z = (f32x4){0, 0, 0, 0};
  f32x4 s00 = z, s0m = z, s10 = z, s11 = z, s1m = z;
#pragma unroll
  for (int ks = 0; ks < 4; ++ks) { s00 = MF16(o.k[0][ks], o.q[0][ks], s00); s10 = MF16(o.k[0][ks], o.q[1][ks], s10); s11 = MF16(o.k[1][ks], o.q[1][ks], s11); }
#pragma unroll
  for (int ks = 0; ks < 4; ++ks) { s0m = MF16(km[ks], o.q[0][ks], s0m); s1m = MF16(km[ks], o.q[1][ks], s1m); }
  s00 = rnd2(s00, scale); s10 = rnd2(s10, scale); s11 = rnd2(s11, scale); s0m = rnd2(s0m, scale); s1m = rnd2(s1m, scale);
  asm volatile("" : "+v"(s00), "+v"(s11), "+v"(s0m), "+v"(s1m));
#pragma unroll
  for (int e = 0; e < 4; ++e) {
    const bool msk = 4 * lg + e > lr;
    s00[e] = msk ? NEG : s00[e]; s11[e] = msk ? NEG : s11[e];
    s0m[e] = lg >= 2 ? -INFINITY : s0m[e]; s1m[e] = lg >= 2 ? -INFINITY : s1m[e];
  }
  float m0 = NEG, m1 = -INFINITY;
#pragma unroll
  for (int e = 0; e < 4; ++e) { m0 = __builtin_fmaxf(m0, __builtin_fmaxf(s00[e], s0m[e])); m1 = __builtin_fmaxf(__builtin_fmaxf(m1, s10[e]), __builtin_fmaxf(s11[e], s1m[e])); }
  m0 = xmax4(m0); m1 = xmax4(m1);
  float t0 = 0.f, t1 = 0.f;
#pragma unroll
  for (int e = 0; e < 4; ++e) {
    s00[e] = __builtin_amdgcn_exp2f((s00[e] - m0) * L2E); s0m[e] = __builtin_amdgcn_exp2f((s0m[e] - m0) * L2E);
    s10[e] = __builtin_amdgcn_exp2f((s10[e] - m1) * L2E); s11[e] = __builtin_amdgcn_exp2f((s11[e] - m1) * L2E); s1m[e] = __builtin_amdgcn_exp2f((s1m[e] - m1) * L2E);
    t0 += s00[e] + s0m[e]; t1 += (s10[e] + s11[e]) + s1m[e];
  }
  const float em = __builtin_amdgcn_exp2f((NEG - m0) * L2E);
  t0 += 4.f * em;
  const float r0 = __builtin_amdgcn_rcpf(xsum4(t0)), r1 = __builtin_amdgcn_rcpf(xsum4(t1));
  s00 *= r0; s0m *= r0; s10 *= r1; s11 *= r1; s1m *= r1;
  const u32x4 p0 = (u32x4){pack2(s00[0], s00[1]), pack2(s00[2], s00[3]), pack2(s0m[0], s0m[1]), pack2(s0m[2], s0m[3])};
  const u32x4 p1 = (u32x4){pack2(s10[0], s10[1]), pack2(s10[2], s10[3]), pack2(s11[0], s11[1]), pack2(s11[2], s11[3])};
  const u32x4 pm = (u32x4){0u, 0u, pack2(s1m[0], s1m[1]), pack2(s1m[2], s1m[3])};
  const u32 pe = pack2(em * r0, em * r0);
  const u32x4 px = (u32x4){0u, 0u, pe, pe};
#pragma unroll
  for (int pr = 0; pr < 4; ++pr) {
    f32x4 o0[2], o1[2];
#pragma unroll
    for (int t = 0; t < 2; ++t) {
      const int dt = 2 * pr + t;
      const u32x4 a0 = (u32x4){o.v[dt][0], o.v[dt][1], vm[dt][0], vm[dt][1]};
      o0[t] = MF16(a0, p0, z);
      o1[t] = MF16(o.v[dt], p1, z);
      o0[t] = MF16(o.v[dt], px, o0[t]);
      o1[t] = MF16(a0, pm, o1[t]);
    }
    const int ob = FO_O + ((sh >> 5) * 256 + (sh & 31) * 4 + pr) * 1024;
    __builtin_amdgcn_raw_buffer_store_b128((u32x4){pack2(o0[0][0], o0[0][1]), pack2(o0[0][2], o0[0][3]), pack2(o0[1][0], o0[1][1]), pack2(o0[1][2], o0[1][3])}, rsf, lane * 16, ob, 16);
    __builtin_amdgcn_raw_buffer_store_b128((u32x4){pack2(o1[0][0], o1[0][1]), pack2(o1[0][2], o1[0][3]), pack2(o1[1][0], o1[1][1]), pack2(o1[1][2], o1[1][3])}, rsf, lane * 16, ob + 131072, 16);
  }
}
#define NAW 64
__device__ __forceinline__ void a_role(__amdgpu_buffer_rsrc_t rsx, __amdgpu_buffer_rsrc_t rsf, int sh, int lim, int wi, int done, int lane, int fb, u32 tag) {
  const int lr = lane & 15, lg = lane >> 4;
  const int okm = (lg * 8 + (lr & 7)) * 16, ovm = lg < 2 ? (lg * 16 + lr) * 8 : 0x7ffffff0;
  int rp = 0, xp = -1, spin = 0, pub = done, miss = 0, tp = 200;
  u32 tseen = 0, tq0 = 0, tq1 = 0, tq2 = 0, tq3 = 0; (void)tq0; (void)tq1; (void)tq2; (void)tq3;
#define XA_WAITX(s_)                                                                  \
  while (xp <= ((s_) >> 5)) {                                                                                          \
    asm volatile("" ::: "memory");                                                                                     \
    int pv = min(xprog(__builtin_amdgcn_raw_buffer_load_b32(rsx, lane * 128, PGO_X, 16), tag), xprog(__builtin_amdgcn_raw_buffer_load_b32(rsx, (64 + (lane & 31)) * 128, PGO_X, 16), tag)); \
    _Pragma("unroll") for (int o_ = 1; o_ < 64; o_ *= 2) pv = min(pv, __shfl_xor(pv, o_));                             \
    xp = __builtin_amdgcn_readfirstlane(pv);                                                                           \
    if (xp > ((s_) >> 5) || ++spin > SPIN_CAP) break;                                                                  \
    asm volatile("s_sleep 16" ::: "memory");                                                                           \
  }
#define XA_PUB() if (pub != done) { pub = done; asm volatile("s_nop 4\n\tbuffer_store_dword %0, %1, %2, %3 offen sc1" :: "v"(done), "v"(lane ? 0x7ffffff0 : 0), "s"(rsx), "s"(PGO_A + wi * 128) : "memory"); }
#define XA_STEP(CUR, NXT)                                                                                              \
  {                                                                                                                    \
    const int seg = sh >> 5, nx = sh + NAW;                                                                            \
    XA_WAITX(min(nx, lim - 1))                                                                                         \
    if (rp <= seg) {                         \
      while (true) {                                                                                                   \
        int want = min(seg + 1, NSEG - 1);                                                                             \
        if (tseen) {                                                                                                   \
          const u32 due = tseen + (u32)(want - rp) * tp - (want + 1 < NSEG ? 0 : XA_LAST) + (seg + 1 < NSEG ? 0 : XA_END); \
          if ((int)(due - (u32)__builtin_amdgcn_s_memrealtime()) > 0 || miss) { asm volatile("s_waitcnt vmcnt(0)" ::: "memory"); XA_PUB() } \
          for (int i_ = 0; i_ < 4000 && (int)(due - (u32)__builtin_amdgcn_s_memrealtime()) > 0; ++i_) asm volatile("s_sleep 2" ::: "memory"); \
          if (!miss) want = min(max(want, rp + max((int)((u32)__builtin_amdgcn_s_memrealtime() - tseen), 0) / tp), NSEG - 1); \
        }                                                                                                              \
        XA_DBG4                                                                                                        \
        asm volatile("" ::: "memory");                                                                                 \
        if (seg + 1 < NSEG) {                                                                                          \
          const int r_ = __builtin_amdgcn_readfirstlane(__builtin_amdgcn_raw_buffer_load_b32(rsx, 0, pgr + want * 128, 16)); \
          if (r_ >= 0) { const u32 n_ = (u32)__builtin_amdgcn_s_memrealtime(); tseen = n_ - ((n_ - (r_ >> 16)) & 0x7fffu) + XA_LAG; tp = max(r_ & 1023, 1); rp = want; miss = 0; break; } \
        } else if (__ballot(__builtin_amdgcn_raw_buffer_load_b32(rsx, ((sh & 31) * 4 + (lane & 3)) * 128, PGO_D, 16) == SENT) == 0) break; \
        if (++spin > SPIN_CAP) break;                                                                                  \
        if (miss++ && seg + 2 < NSEG) asm volatile("s_sleep 20" ::: "memory");                                                         \
      }                                                                                                                \
    }                                                                                                                  \
    asm volatile("" ::: "memory");                                                                                     \
    XA_DBG2                                                                                                            \
    u32x4 km[4]; u32x2 vm[8];                                                                                          \
    _Pragma("unroll") for (int i = 0; i < 4; ++i) km[i] = __builtin_amdgcn_raw_buffer_load_b128(rsf, okm, FO_KM + (sh * 4 + i) * 512, 16); \
    _Pragma("unroll") for (int i = 0; i < 8; ++i) vm[i] = __builtin_amdgcn_raw_buffer_load_b64(rsf, ovm, FO_VM + (sh * 8 + i) * 256, 16);  \
                              \
    const int pk_ = min(rp + max((int)((u32)__builtin_amdgcn_s_memrealtime() - tseen), 0) / tp, NSEG - 1);              \
    const u32 pv_ = __builtin_amdgcn_raw_buffer_load_b32(rsx, 0, pgr + pk_ * 128, 16);                                 \
    asm volatile("" ::: "memory");                                                                                     \
    __builtin_amdgcn_s_waitcnt(0x0F70);                                                                   \
    asm volatile("" ::: "memory");                                                                                     \
    { const int r_ = __builtin_amdgcn_readfirstlane(pv_);                                                              \
      if (r_ >= 0) { const u32 n_ = (u32)__builtin_amdgcn_s_memrealtime(); tseen = n_ - ((n_ - (r_ >> 16)) & 0x7fffu) + XA_LAG; tp = max(r_ & 1023, 1); rp = pk_; } } \
    XA_PUB()                                   \
    if (nx < lim) xa_load(NXT, rsf, nx, lane);                                                                         \
    XA_DBG3                                                                                                            \
    __builtin_amdgcn_sched_barrier(0);                                                                                 \
    xa_compute(CUR, km, vm, rsf, sh, lane);                                                                            \
    ++done;                                                                                                            \
    XA_DBG                                                                                                             \
    sh = nx;                                                                                                           \
    if (sh >= lim) break;                                                                                              \
  }
#define XA_LAST 24
#define XA_END 0
#define XA_LAG 30
#define XA_DBG
#define XA_DBG2
#define XA_DBG3
#define XA_DBG4
  const int pgr = PGO_R + (__builtin_amdgcn_readfirstlane(wait_start(rsx, fb, tag, true)[2]) & 8192);
  XaOps A, B;
  if (sh >= NAW) {
    const int r_ = __builtin_amdgcn_readfirstlane(__builtin_amdgcn_raw_buffer_load_b32(rsx, 0, pgr + 40 * 128, 16));
    if (r_ >= 0) { const u32 n_ = (u32)__builtin_amdgcn_s_memrealtime(); tseen = n_ - ((n_ - (r_ >> 16)) & 0x7fffu) + XA_LAG; tp = max(r_ & 1023, 1); rp = 40; }
  }
  XA_WAITX(sh)
  asm volatile("" ::: "memory");
  xa_load(A, rsf, sh, lane);
  while (true) { XA_STEP(A, B) XA_STEP(B, A) }
  asm volatile("s_waitcnt vmcnt(0)" ::: "memory");
  XA_PUB()
}

typedef __attribute__((ext_vector_type(32))) unsigned int u32x32;
typedef __attribute__((ext_vector_type(16))) unsigned int u32x16;
typedef __attribute__((ext_vector_type(8))) unsigned int u32x8;
#define STR2(x) #x
#define STR(x) STR2(x)
#define RNG(f, b, n) f "[" STR(b) ":" STR((b) + (n)-1) "]"
#define REG(b) "v[" STR(b) "]"
#define LR_RED 16384
#define LR_FRG 24576
#define LR_CTL LR_RED
#define LR_STG 32768
#define L_RSTAMP LR_STG
#define STAMPV(i, v)
#define EXF (NH * 8 * 1024)
#define EXK 65536
#define OOR 0x7ffffff0

__device__ __forceinline__ u32 lds_word(int addr) {
  u32 v;
  asm volatile("ds_read_b32 %0, %1\n\ts_waitcnt lgkmcnt(0)" : "=v"(v) : "v"(addr) : "memory");
  return __builtin_amdgcn_readfirstlane(v);
}
__device__ __forceinline__ int fresh_lane() {
  int l;
  asm volatile("v_mbcnt_lo_u32_b32 %0, -1, 0\n\tv_mbcnt_hi_u32_b32 %0, -1, %0" : "=v"(l));
  return l;
}
__device__ __forceinline__ u32 rt32() { return (u32)__builtin_amdgcn_s_memrealtime(); }
#define SYO(s) (XSYN + flip + (s) * 64)
#define TKS 0.85f
#define TLB 1
#define TLC 1
#define TLP 2
#define KG0 0
#define CLS0 0u
#define APC 5
#define ADL 12
#define CALN 2
#define TE1 4
#define TE2 3
#define TE3 2
#define XTH 3
#define TS3 42
#define TE0 60
#define TD0 100
#define TD 120
#define PHASE 1
#define XSK 0
#define CLKS(R, X) WLG(2) "s_sub_u32 s49, s50, " R "\n\t" "s_cmp_le_i32 s49, " STR(3 * PHASE) "\n\t" "s_cbranch_scc1 " X "\n\t"
#define CLKW(L, OFF) WLG(0) "s_memrealtime s[72:73]\n\t" "s_add_u32 s50, s43, s44\n\t" "s_add_u32 s50, s50, " OFF "\n\t" "s_nop 1\n\t" "s_memrealtime s[74:75]\n\t" "s_nop 3\n" \
                     L ":\n\t" "s_memrealtime s[76:77]\n\t" CLKS("s72", L "x") "s_memrealtime s[72:73]\n\t" CLKS("s74", L "x")     \
                     "s_memrealtime s[74:75]\n\t" CLKS("s76", L "x") "s_branch " L "\n" \
                     L "x:\n\t" CLKP(L)
#define CLKP(L) "s_cmp_lt_i32 s49, 1\n\t" "s_cbranch_scc1 " L "y\n\t" "s_nop " STR(PHN) "\n\t" "s_cmp_lt_i32 s49, 2\n\t" "s_cbranch_scc1 " L "y\n\t" "s_nop " STR(PHN) "\n\t" \
                "s_cmp_lt_i32 s49, 3\n\t" "s_cbranch_scc1 " L "y\n\t" "s_nop " STR(PHN) "\n\t" "s_nop 0\n\t" "s_branch " L "y\n\t" "s_nop 0\n" L "y:\n\t"
#define PHN 3
#define CLKL(L, OFF) L ":\n\t" "s_memrealtime vcc\n\t" "ds_read_b32 v62, v38\n\t" WLG(0) "v_readfirstlane_b32 s49, v62\n\t" "s_max_u32 s44, s44, s49\n\t" \
                     "s_add_u32 s50, s43, s44\n\t" "s_add_u32 s50, s50, " OFF "\n\t" "s_sub_u32 s49, s50, vcc_lo\n\t" "s_cmp_le_i32 s49, 16\n\t" "s_cbranch_scc1 " L "z\n\t" \
                     CLKSL "s_branch " L "\n" L "z:\n\t"
#define CLKSL "s_sleep 1\n\t"
#define TS_(n) "s_memtime s[80+2*" #n ":81+2*" #n "]\n\t"
#define TSA(n)
#define TSB(n)
#define TSW(n)
#define TVM 16
template <int NTL, int KS>
__device__ __forceinline__ void tr_chunk(const u16* S, int lr, int lg, u32 (&r)[NTL][KS][8]) {
  const int q = lr >> 2, p = lr & 3;
#pragma unroll
  for (int t = 0; t < NTL; ++t)
#pragma unroll
    for (int i = 0; i < KS; ++i)
#pragma unroll
      for (int kb = 0; kb < 2; ++kb) {
        const u16* base = S + (64 * i + 32 * kb + 16 * (lg >> 1) + 4 * (lg & 1) + (q & 1) + 8 * (q >> 1)) * (16 * NTL) + 16 * t + 4 * p;
        const u32x2 t0 = __builtin_bit_cast(u32x2, __builtin_amdgcn_ds_read_tr16_b64_v4i16((LAS v4i16_t*)(base)));
        const u32x2 t1 = __builtin_bit_cast(u32x2, __builtin_amdgcn_ds_read_tr16_b64_v4i16((LAS v4i16_t*)(base + 2 * 16 * NTL)));
        r[t][i][4 * kb + 0] = t0[0]; r[t][i][4 * kb + 1] = t0[1]; r[t][i][4 * kb + 2] = t1[0]; r[t][i][4 * kb + 3] = t1[1];
      }
}
#define SMF(acc, a, bf, bi) "v_smfmac_f32_16x16x64_bf16 " RNG("v", acc, 4) ", " RNG("v", a, 4) ", " RNG(bf, bi, 8) ", v34\n\t"
#define ZER(r) "v_mov_b64 " RNG("v", r, 2) ", 0\n\t" "v_mov_b64 " RNG("v", (r) + 2, 2) ", 0\n\t"
#define GLD(r, o) "s_xor_b32 s49, s50, " STR(o) "\n\t" "buffer_load_dwordx4 " RNG("v", r, 4) ", v32, %[rs], s49 offen sc1\n\t"
#define GL1(r, o) "s_xor_b32 s49, s50, " STR(o) "\n\t" "buffer_load_dwordx4 " RNG("v", r, 4) ", v33, %[rs], s49 offen sc1\n\t"
#define GL2(r, o) GL1(r, 4096 + (o))
#define MXA0(r) "v_max3_u32 v63, " REG(r) ", " REG((r) + 1) ", " REG((r) + 1) "\n\t"
#define MXA(r) "v_max3_u32 v63, v63, " REG(r) ", " REG((r) + 1) "\n\t"
#define MXB(r) "v_max3_u32 v63, v63, " REG((r) + 2) ", " REG((r) + 3) "\n\t"
#define CMPB "v_cmp_eq_u32 vcc, s54, v63\n\t"
#define PSA(r) "v_pk_add_f32 " RNG("v", r, 2) ", " RNG("v", r, 2) ", " RNG("v", (r) + 2, 2) "\n\t"
#define PSW(r, o) "ds_write_addtid_b32 " REG(r) " offset:" STR(o) "\n\t"
#define RDQ(r, o) "ds_read_b128 " RNG("v", r, 4) ", v37 offset:" STR(o) "\n\t"
#define SQ(t, i) SMF(64 + 4 * (t), 96 + 4 * (i), "a", ((i) * 4 + (t)) * 8)
#define SV(t, i) SMF(24 + 4 * (t), 96 + 4 * (i), "a", 128 + ((i) * 2 + (t)) * 8)
#define SW(q, i) SMF(64 + 4 * (q), 96 + 4 * (i), "v", 128 + ((i) * 2 + (q)) * 8)
#define WVM(n) "s_waitcnt vmcnt(" STR(n) ")\n\t"
#define WLG(n) "s_waitcnt lgkmcnt(" STR(n) ")\n\t"
#define WLGF(n) WLG(n)
#define PACE 7
#define PC "s_nop " STR(PACE) "\n\t"
#define QBLK(i, M0) WVM(5 - (i)) SQ(0, i) M0(96 + 4 * (i)) SQ(1, i) MXB(96 + 4 * (i)) SQ(2, i) SQ(3, i)
#define WBLK(i, M0) WVM(8 - (i)) SW(0, i) M0(96 + 4 * (i)) SW(1, i) MXB(96 + 4 * (i))
#define CL8(a, b, c, d, e, f, g, h) "v" #a, "v" #b, "v" #c, "v" #d, "v" #e, "v" #f, "v" #g, "v" #h
#define CL64 CL8(64, 65, 66, 67, 68, 69, 70, 71)
#define CL72 CL8(72, 73, 74, 75, 76, 77, 78, 79)
#define CL80 CL8(80, 81, 82, 83, 84, 85, 86, 87)
#define CL88 CL8(88, 89, 90, 91, 92, 93, 94, 95)
#define CL96 CL8(96, 97, 98, 99, 100, 101, 102, 103), CL8(104, 105, 106, 107, 108, 109, 110, 111)
#define CL112 CL8(112, 113, 114, 115, 116, 117, 118, 119)
#define CL120 CL8(120, 121, 122, 123, 124, 125, 126, 127)
#define PKA(d, a, b) "v_pk_add_f32 " RNG("v", d, 2) ", " RNG("v", a, 2) ", " RNG("v", b, 2) "\n\t"
#define ARRIVE(o) "s_mov_b64 exec, 1\n\t" "ds_add_rtn_u32 v62, v38, v39 offset:" STR(o) "\n\t" "s_mov_b64 exec, -1\n\t"

#define EXTRA "v_readlane_b32 s69, v35, s45\n\t" "s_add_u32 s66, s56, s69\n\t" "s_add_u32 s67, s57, s69\n\t" "s_add_u32 s67, s67, s69\n\t" "s_add_u32 s68, s66, " STR(TVM) "\n\t"
#define TEX(s) ((s) == 0 ? TE0 : ((s) < 4 ? TE1 : ((s) < 12 ? TE2 : ((s) < TS3 ? TE3 : 0))))
#define R2BASE "s_bitcmp1_b32 s45, 0\n\t" "s_cselect_b32 s71, " STR(XR2S) ", 0\n\t" "s_add_u32 s71, s71, %[r2]\n\t"
#define X4B 0
#define X4C 0
#define STB "buffer_store_dwordx2 v[116:117], v11, %[rs], s36 offen sc1\n"
#define STC "buffer_store_dwordx2 v[80:81], v17, %[rs], s36 offen sc1\n\t"
#define R2TEXT                                                                                                     \
              TSB(0)                                                                                               \
              "buffer_load_dwordx4 v[64:67], v43, %[rs], s71 offen sc1\n\t"                                      \
              "buffer_load_dwordx4 v[68:71], v8, %[rs], s71 offen sc1\n\t"                                       \
              "buffer_load_dwordx4 v[76:79], v9, %[rs], s71 offen sc1\n\t"                                       \
              "buffer_load_dwordx4 v[60:63], v10, %[rs], s71 offen sc1\n\t"                                      \
              "buffer_store_dwordx4 v[72:75], v47, %[rf], s41 offen sc1\n"                                       \
              ".Lvw%=:\n\t"                                                                                        \
              "ds_read_b32 v59, v38 offset:12\n\t"                                                                 \
              "ds_read2_b32 v[112:113], v12 offset0:0 offset1:4\n\t" "ds_read2_b32 v[114:115], v12 offset0:32 offset1:36\n\t" \
              "ds_read2_b32 v[116:117], v13 offset0:0 offset1:4\n\t" "ds_read2_b32 v[118:119], v13 offset0:32 offset1:36\n\t" \
              "ds_read2_b32 v[120:121], v14 offset0:0 offset1:4\n\t" "ds_read2_b32 v[122:123], v14 offset0:32 offset1:36\n\t" \
              "ds_read2_b32 v[124:125], v15 offset0:0 offset1:4\n\t" "ds_read2_b32 v[126:127], v15 offset0:32 offset1:36\n\t" \
              "ds_read_b64 v[96:97], v18 offset:2048\n\t" "ds_read_b64 v[100:101], v18 offset:2056\n\t"      \
              "ds_read_b64 v[104:105], v18 offset:3072\n\t" "ds_read_b64 v[108:109], v18 offset:3080\n\t"    \
              WLG(0)                                                                                               \
              "v_readfirstlane_b32 s49, v59\n\t"                                                                 \
              "s_sub_u32 s49, s49, 4\n\t"                                                                      \
              "s_cmp_lt_i32 s49, s38\n\t"                                                                      \
              "s_cbranch_scc1 .Lvw%=\n\t"                                                                          \
              PKA(112, 112, 116) PKA(114, 114, 118) PKA(120, 120, 124) PKA(122, 122, 126) PKA(112, 112, 120) PKA(114, 114, 122) \
              "v_mov_b32 v59, 0\n\t" "v_mov_b32 v99, 0\n\t" "v_mov_b32 v103, 0\n\t" "v_mov_b32 v107, 0\n\t" "v_mov_b32 v111, 0\n\t" \
              "v_cvt_pk_bf16_f32 v98, v112, v59\n\t" "v_cvt_pk_bf16_f32 v106, v113, v59\n\t" "v_cvt_pk_bf16_f32 v102, v114, v59\n\t" "v_cvt_pk_bf16_f32 v110, v115, v59\n\t" \
              "v_mov_b64 v[112:113], v[80:81]\n\t" "v_mov_b64 v[114:115], v[82:83]\n\t"                            \
              "v_mov_b32_dpp v112, v84 row_ror:8 row_mask:0xf bank_mask:0xc\n\t" "v_mov_b32_dpp v113, v85 row_ror:8 row_mask:0xf bank_mask:0xc\n\t" \
              "v_mov_b32_dpp v114, v86 row_ror:8 row_mask:0xf bank_mask:0xc\n\t" "v_mov_b32_dpp v115, v87 row_ror:8 row_mask:0xf bank_mask:0xc\n\t" \
              "v_mov_b32 v52, 0x3f803f80\n\t" "v_mov_b32 v53, 0x3f803f80\n\t" "v_mov_b32 v54, 0x3f803f80\n\t" "v_mov_b32 v55, 0x3f803f80\n\t" \
              "v_mov_b32 v95, 0x3e0293ee\n\t" "v_mov_b32 v123, 0\n\t" "s_mov_b32 s65, 0\n\t" \
              "v_pk_mul_f32 v[112:113], v[112:113], v[94:95] op_sel:[0,1]\n\t" "v_pk_mul_f32 v[114:115], v[114:115], v[94:95] op_sel:[0,1]\n\t" \
              TSB(1) WVM(4) TSB(2) \
              "v_permlane32_swap_b32 v64, v66\n\t" "v_permlane32_swap_b32 v65, v67\n\t" \
              "v_pk_add_f32 v[92:93], v[64:65], v[66:67]\n\t" \
              WVM(3) SFM(68) \
              "v_permlane16_swap_b32 v92, v93\n\t" \
              "v_add_f32 v94, v92, v93\n\t" \
              WVM(2) "v_mul_f32 v94, v95, v94\n\t" SFM(76) \
              "v_exp_f32 v92, v94\n\t" \
              WVM(1) TSB(3) SFM(60) \
                \
              ".Lex%=:\n\t" \
              "v_exp_f32 v88, v112\n\t" "v_exp_f32 v89, v113\n\t" "v_cvt_pk_bf16_f32 v58, v92, v59\n\t" "v_exp_f32 v90, v114\n\t" "v_exp_f32 v91, v115\n\t" \
              "v_cvt_pk_bf16_f32 v56, v88, v89\n\t" "v_cvt_pk_bf16_f32 v57, v90, v91\n\t" \
              TSB(4) ROR8(122, 58) ROR8(120, 56) \
              "v_mfma_f32_16x16x32_bf16 v[88:91], v[52:55], v[56:59], 0\n\t" \
              ROR8(121, 57) \
              "v_mfma_f32_16x16x32_bf16 v[116:119], v[96:99], v[56:59], 0\n\t" \
              "s_nop 0\n\t" \
              "v_mfma_f32_16x16x32_bf16 v[116:119], v[100:103], v[120:123], v[116:119]\n\t" \
              "s_nop 0\n\t" \
              "v_add_f32_dpp v92, v88, v88 row_ror:8 row_mask:0xf bank_mask:0xf\n\t" \
              "v_mfma_f32_16x16x32_bf16 v[124:127], v[108:111], v[56:59], 0\n\t" \
              "v_rcp_f32 v93, v92\n\t" \
              "v_mfma_f32_16x16x32_bf16 v[124:127], v[104:107], v[120:123], v[124:127]\n\t" \
              TSB(5) "v_subrev_u32 v92, 0x33800000, v92\n\t" "v_cmp_le_u32 vcc, 0x20000000, v92\n\t" \
              "v_pk_mul_f32 v[116:117], v[116:117], v[92:93] op_sel:[0,1]\n\t" "v_pk_mul_f32 v[118:119], v[118:119], v[92:93] op_sel:[0,1]\n\t" \
              "v_cvt_pk_bf16_f32 v116, v116, v117\n\t" "v_cvt_pk_bf16_f32 v117, v118, v119\n\t" \
              "s_mov_b64 exec, s[78:79]\n\t" \
              "s_cbranch_vccnz .Lmx%=\n\t" \
              OTAIL STB TSB(6)
#define SFM(r) "v_pk_fma_f32 v[112:113], " RNG("v", r, 2) ", v[94:95], v[112:113] op_sel:[0,1,0]\n\t" "v_pk_fma_f32 v[114:115], " RNG("v", (r) + 2, 2) ", v[94:95], v[114:115] op_sel:[0,1,0]\n\t"
#define ROR8(d, s) "v_mov_b32_dpp " REG(d) ", " REG(s) " row_ror:8 row_mask:0xf bank_mask:0xf\n\t"
#define OTAIL "v_pk_mul_f32 v[124:125], v[124:125], v[92:93] op_sel:[0,1]\n\t" "v_pk_mul_f32 v[126:127], v[126:127], v[92:93] op_sel:[0,1]\n\t" \
              "v_cvt_pk_bf16_f32 v116, v124, v125\n\t" "v_cvt_pk_bf16_f32 v117, v126, v127\n\t" \
              "s_mov_b64 exec, -1\n\t"
#define R2MAX ".Lmx%=:\n\t" OTAIL \
              "s_cmp_lg_u32 s65, 0\n\t" "s_cbranch_scc1 .Lend%=\n\t" "s_mov_b32 s65, 1\n\t" \
              "v_max3_f32 v93, v112, v113, v94\n\t" \
              "v_max3_f32 v93, v93, v114, v115\n\t" \
              "s_nop 1\n\t" \
              "v_max_f32_dpp v92, v93, v93 row_ror:8 row_mask:0xf bank_mask:0xf\n\t" \
              "v_max_f32_dpp v88, v93, v93 row_ror:8 row_mask:0xf bank_mask:0xf\n\t" \
              "s_nop 1\n\t" \
              "v_permlane16_swap_b32 v92, v88\n\t" \
              "v_max_f32 v93, v92, v88\n\t" "v_max_f32 v92, v92, v88\n\t" \
              "s_nop 1\n\t" \
              "v_permlane32_swap_b32 v93, v92\n\t" \
              "v_max_f32 v93, v93, v92\n\t" \
              "v_sub_f32 v94, v94, v93\n\t" "v_sub_f32 v112, v112, v93\n\t" "v_exp_f32 v92, v94\n\t" "v_sub_f32 v113, v113, v93\n\t" "v_sub_f32 v114, v114, v93\n\t" "v_sub_f32 v115, v115, v93\n\t" \
              "s_branch .Lex%=\n"
#define AV 0
#define VREST
#define PS1(r) "v_add_f32 " REG(r) ", " REG(r) ", " REG((r) + 2) "\n\t"
#define VPOST VREST PSA(24) PSW(24, 2560) PSW(25, 2832) PSA(28) PSW(28, 3200) PSW(29, 3472) ARRIVE(12)
#define R2OUT "=&{v[60:79]}"(pr), "=&{v[116:117]}"(ov)
#define FINEO
#define STEPCHK "2"
#define ROLESV
#define ROLEOUT

__device__ __forceinline__ void clk_edge(u32& cyc, u32& tick) {
  const u32 t = rt32();
  for (int i = 0; i < 64; ++i) { tick = rt32(); if (tick != t) break; }
  cyc = (u32)__builtin_amdgcn_s_memtime();
}
__device__ __forceinline__ float clk_rate(u32 cyc0, u32 tk0) { u32 c, t; clk_edge(c, t); return (float)(c - cyc0) / (float)max(t - tk0, 1u); }
__device__ __forceinline__ void dma16(__amdgpu_buffer_rsrc_t rs, int voff, int soff, int sstep, int ldsa) {
#pragma unroll
  for (int i = 0; i < 16; ++i)
    asm volatile("s_mov_b32 m0, %0\n\ts_nop 3\n\tbuffer_load_dwordx4 %1, %2, %3 offen lds" :: "s"(ldsa + i * 1024), "v"(voff), "s"(rs), "s"(soff ^ (i * sstep)) : "memory");
}

#define KH(t) (192 + 8 * (t))
#define KMF(t, a, b) "v_mfma_f32_16x16x32_bf16 " RNG("v", 80 + 4 * (t), 4) ", " RNG("v", KH(t) + 4 * (a), 4) ", " RNG("v", KH(3) + 4 * (b), 4) ", " RNG("v", 80 + 4 * (t), 4) "\n\t"
#define KX(j) (92 + 4 * ((j) & 1))
#define KA(t, j) "v_pk_mul_f32 " RNG("v", KX(j), 2) ", " RNG("v", 64 + 4 * (j), 2) ", " RNG("v", 112 + ((t) & 2), 2) " op_sel:[0," STR((t) & 1) "] op_sel_hi:[1," STR((t) & 1) "]\n\t"
#define KF(j) "v_fract_f32 " REG(KX(j) + 1) ", " REG(KX(j) + 1) "\n\t"
#define KE(j, N) "v_exp_f32 " REG(KX(j)) ", " REG(KX(j)) "\n\t" "v_cos_f32 " REG(KX(j) + 2) ", " REG(KX(j) + 1) "\n\t" "v_sin_f32 " REG(KX(j) + 3) ", " N REG(KX(j) + 1) "\n\t"
#define KM(j) "v_pk_mul_f32 " RNG("v", KX(j) + 2, 2) ", " RNG("v", KX(j) + 2, 2) ", " RNG("v", KX(j), 2) " op_sel_hi:[1,0]\n\t"
#define KC(j) "v_pk_mul_f32 " RNG("v", KX(j), 2) ", " RNG("v", KX(j) + 2, 2) ", " RNG("v", 66 + 4 * (j), 2) " op_sel_hi:[1,0]\n\t" \
  "v_pk_fma_f32 " RNG("v", KX(j), 2) ", " RNG("v", KX(j) + 2, 2) ", " RNG("v", 66 + 4 * (j), 2) ", " RNG("v", KX(j), 2) " op_sel:[1,1,0] op_sel_hi:[0,1,1] neg_hi:[1,0,0]\n\t"
#define KD(t, j, r) "v_cvt_pk_bf16_f32 " REG(KH(t) + (j)) ", " REG(r) ", " REG((r) + 1) "\n\t" \
  "v_dot2c_f32_bf16 " REG(r) ", s47, " REG(KH(t) + (j)) "\n\t" "v_dot2c_f32_bf16 " REG((r) + 1) ", s48, " REG(KH(t) + (j)) "\n\t"
#define KL(t, j, r) "v_cvt_pk_bf16_f32 " REG(KH(t) + 4 + (j)) ", " REG(r) ", " REG((r) + 1) "\n\t"
#define KDM "s_add_u32 m0, m0, 1024\n\t" "s_add_u32 s43, s43, s42\n\t" "buffer_load_dwordx4 v110, s[36:39], s43 offen lds\n\t"
#define KP(j) "ds_read_b128 " RNG("v", 64 + 4 * (j), 4) ", v111 offset:" STR(16 * (j)) "\n\t"
#define KV(j, L, A) KE(j, "-") L KM(j) KC(j) A KD(3, j, KX(j))
#define KU(t, j, L, A) KE(j, "") L KM(j) A KD(t, j, KX(j) + 2)
#define KSUB                                                                                                        \
  KV(0, WLG(8) "s_add_u32 m0, m0, 1024\n\t", KA(3, 1)) KV(1, KL(3, 0, KX(0)), KA(3, 2))                              \
  "s_xor_b32 s43, s40, s41\n\t" "buffer_load_dwordx4 v110, s[36:39], s43 offen lds\n\t" KV(2, KL(3, 1, KX(1)), KA(3, 3)) \
  KV(3, KL(3, 2, KX(2)), KA(0, 0))                                                                                  \
  KU(0, 0, KL(3, 3, KX(3)), KA(0, 1)) KU(0, 1, KL(0, 0, KX(0) + 2), KA(0, 2)) KDM KU(0, 2, KL(0, 1, KX(1) + 2), KA(0, 3)) \
  KU(0, 3, KL(0, 2, KX(2) + 2), KA(1, 0)) KF(0)                                                                     \
  KU(1, 0, KL(0, 3, KX(3) + 2), KA(1, 1)) KF(1) KMF(0, 1, 0) KU(1, 1, KL(1, 0, KX(0) + 2), KA(1, 2)) KF(2) KDM      \
  KMF(0, 0, 1) KU(1, 2, KL(1, 1, KX(1) + 2), KA(1, 3)) KF(3) KMF(0, 0, 0) KU(1, 3, KL(1, 2, KX(2) + 2), KA(2, 0)) KF(0) KP(0) \
  KU(2, 0, KL(1, 3, KX(3) + 2), KA(2, 1)) KF(1) KP(1) KMF(1, 1, 0) KU(2, 1, KL(2, 0, KX(0) + 2), KA(2, 2)) KF(2) KP(2) KDM \
  KMF(1, 0, 1) KU(2, 2, KL(2, 1, KX(1) + 2), KA(2, 3)) KF(3) KP(3) KMF(1, 0, 0) KU(2, 3, KL(2, 2, KX(2) + 2), WLG(3) KA(3, 0)) \
  KMF(2, 0, 1) "v_add_u32 v111, 256, v111\n\t" "s_lshl2_add_u32 s41, s42, s41\n\t" KL(2, 3, KX(3) + 2)
#define KZERO ZER(80) ZER(84) ZER(88)
#define KCHUNK(PTR, VO, SO, ST, BUF, PF) "s_mov_b64 s[36:37], " PTR "\n\t" "s_mov_b32 m0, %[sc]\n\t" "v_mov_b32 v110, " VO "\n\t" SO "s_mov_b32 s41, 0\n\t" "s_mov_b32 s42, " STR(ST) "\n\t" \
  "buffer_load_dword " PF ", s[36:39], 0 offen lds\n\t" "s_add_u32 m0, %[sl], " STR((BUF) * 16384 - 1024) "\n\t"
#define KCALL "s_call_b64 s[44:45], .Lkg%=\n\t" WVM(16) KMF(2, 0, 0)
#define KTR(F, d, A, o) "ds_read_b64_tr_b16 " RNG(F, d, 2) ", " A " offset:" STR(o) "\n\t"
#define KTQ1(d, o) KTR("a", d, "%[aq]", o) KTR("a", (d) + 2, "%[aq]", (o) + 128) KTR("a", (d) + 4, "%[aq]", (o) + 2048) KTR("a", (d) + 6, "%[aq]", (o) + 2176)
#define KTQ(d, o) KCALL KTQ1(d, o) KMF(2, 1, 0) KTQ1((d) + 8, (o) + 32)
#define KTO1(d, o) KTR("v", d, "%[ao]", o) KTR("v", (d) + 2, "%[ao]", (o) + 64) KTR("v", (d) + 4, "%[ao]", (o) + 1024) KTR("v", (d) + 6, "%[ao]", (o) + 1088)
#define KTO(d, o) KCALL KTO1(d, o) KMF(2, 1, 0) KTO1((d) + 8, (o) + 2048)
#define KOUT(o) "s_add_u32 s43, %[ko], " STR(o) "\n\t"                                                              \
  "buffer_store_dwordx4 v[80:83], %[l16], %[rt], s43 offen sc1\n\t" "buffer_store_dwordx4 v[84:87], %[l16], %[rt], s43 offen offset:1024 sc1\n\t" "s_nop 3\n\t" \
  "buffer_store_dwordx4 v[88:91], %[l16], %[rt], s43 offen offset:2048 sc1\n\t"
struct SsmPtrs { const u16 *A_re, *A_im, *B_re, *B_im, *C_re, *C_im, *log_dt; };
__device__ __forceinline__ void r_role(unsigned char* lds, int b, const u16* __restrict__ mem0, const u16* __restrict__ Wq, const u16* __restrict__ Wk, const u16* __restrict__ Wv,
                                       const u16* __restrict__ Wo, int fb, u32 tag, u64 pc0, int clen, __amdgpu_buffer_rsrc_t rsx, __amdgpu_buffer_rsrc_t rsf,
                                       __amdgpu_buffer_rsrc_t rst, SsmPtrs sp, int tid, int lane, int w
                                       ) {
  const int lr = lane & 15, lg = lane >> 4;
  const int ldsb = (int)(size_t)(LAS unsigned char*)lds;
  const int idx = (lr & 2) ? 0xEEEE : 0x4444;
  int tries = 0;
  u32 tcnt = 0;
  u32 cyc0 = 0, tk0 = 0;
  if (tid < 8) *(u32*)(lds + LR_CTL + tid * 4) = tid == 1 ? rt32() : 0u;
  const int xcd = b & 3, li = b >> 2;
  const int head = xcd * 8 + (li >> 2), ks = li & 3;
  const int vcu = head * 4 + ks;
  const int cg = (xcd >> 1) * 32 + li, rg = xcd & 1;
  const int rot1 = ((li >> 1) + w) & 3, rot3 = ((li >> 2) + 2 * w) & 7;
  const int pz1 = lane < 32 ? XR1A(lane >> 3, cg * 256 + rg * 128 + (lane & 7) * 16) : XR3A(lg & 1, vcu * 256 + lr * 16);
  const int pgo = PGO_X + ((w >> 1) * 32 + head) * 128;
  const int calo = XCAL + (li & 3) * 8192 + ((li >> 2) & 1) * 4096 + ((li >> 3) * 4 + xcd) * 128;
  u32 xp0;
  u32x32 wa[6], wo[4];
  {
    const __amdgpu_buffer_rsrc_t rwq = __builtin_amdgcn_make_buffer_rsrc((void*)Wq, 0, DM * HD * 2, 0x00020000);
    const int vq = (lane >> 2) * (HD * 2) + vcu * 64 + (lane & 3) * 16, sq = (256 * w + (rot1 << 6)) * (HD * 2);
    const int vo = (lane >> 1) * (DM * 2) + cg * 32 + (lane & 1) * 16, so = (1024 * w + (rot3 << 7)) * (DM * 2);
    const int sl = ldsb + LR_STG + w * 32768;
    const int ch0 = (b * 4 + w) * 2;
    u32x2 pin[6];
    u32 ldt;
    {
      const int po = (ch0 * SSM_N + 4 * lane) * 2;
      asm volatile("global_load_dwordx2 %0, %7, %9\n\tglobal_load_dwordx2 %1, %7, %10\n\tglobal_load_dwordx2 %2, %7, %11\n\tglobal_load_dwordx2 %3, %7, %12\n\t"
                   "global_load_dwordx2 %4, %7, %13\n\tglobal_load_dwordx2 %5, %7, %14\n\tglobal_load_ushort %6, %8, %15"
                   : "=&v"(pin[0]), "=&v"(pin[1]), "=&v"(pin[2]), "=&v"(pin[3]), "=&v"(pin[4]), "=&v"(pin[5]), "=&v"(ldt)
                   : "v"(po), "v"((ch0 + (lane >> 5)) * 2), "s"(sp.A_re), "s"(sp.A_im), "s"(sp.B_re), "s"(sp.B_im), "s"(sp.C_re), "s"(sp.C_im), "s"(sp.log_dt) : "memory");
    }
    u32x4 m0v;
    asm volatile("global_load_dwordx4 %0, %1, %2" : "=&v"(m0v) : "v"(((4 * rg + ((lane >> 1) & 3)) * DM + 16 * cg + 8 * (lane & 1)) * 2), "s"(mem0) : "memory");
    const u16* const wp[4] = {Wq, Wk, Wv, Wo};
    int pfo[5];
#pragma unroll
    for (int c = 0; c < 5; ++c) {
      const int lo_ = (int)(size_t)wp[c < 3 ? c : 3], s3 = (384 - lo_) & 1023, s63 = (8064 - lo_) & 8191;
      pfo[c] = c < 3 ? (lane < 16 ? (256 * w + li * 8 + (lane & 7)) * (HD * 2) + xcd * 2048 + (lane >> 3) * 1024 + s3
                                  : (lane < 24 && (s63 >> 11) == xcd ? (256 * w + li * 8 + (lane & 7)) * (HD * 2) + s63 : OOR))
                     : (lane < 16 ? (1024 * w + li * 16 + lane + (((c - 3) * 512) ^ ((((s3 >> 7) + 2 * w) & 4) << 7))) * (DM * 2) + (xcd >> 1) * 1024 + s3 : OOR);
    }
    const int scr = ldsb + LR_RED + 1024 + w * 256;
    asm volatile("s_mov_b32 m0, %0\n\ts_nop 3\n\tbuffer_load_dword %1, %2, 0 offen lds" :: "s"(scr), "v"(pfo[0]), "s"(rwq) : "memory");
#define KP0 16
#pragma unroll
    for (int i = 0; i < KP0; ++i)
      asm volatile("s_mov_b32 m0, %0\n\ts_nop 3\n\tbuffer_load_dwordx4 %1, %2, %3 offen lds" :: "s"(sl + i * 1024), "v"(vq), "s"(rwq), "s"(sq ^ (i * 16 * HD * 2)) : "memory");
    asm volatile("s_waitcnt vmcnt(1+" STR(KP0) ")" : "+v"(pin[0]), "+v"(pin[1]), "+v"(pin[2]), "+v"(pin[3]), "+v"(pin[4]), "+v"(pin[5]), "+v"(ldt), "+v"(m0v)::"memory");
    f32x4* prm = (f32x4*)(lds + w * 4096);
    {
      const float dt = rbf(expf(bf2f((u16)ldt)));
#pragma unroll
      for (int i = 0; i < 4; ++i) {
        float v[6];
#pragma unroll
        for (int j = 0; j < 6; ++j) v[j] = __uint_as_float((i & 1) ? (pin[j][i >> 1] & 0xffff0000u) : (pin[j][i >> 1] << 16));
        const float are = dt * v[0], aim = dt * v[1], br = dt * v[2], bi = dt * v[3];
        const float cr = v[4] * br - v[5] * bi, ci = v[4] * bi + v[5] * br;
        const float rev = aim * 0.15915494309189535f;
        prm[4 * lane + i] = (f32x4){are * 1.4426950408889634f, rev - __builtin_rintf(rev), cr, ci};
      }
    }
#pragma unroll
    for (int i = KP0; i < 16; ++i)
      asm volatile("s_mov_b32 m0, %0\n\ts_nop 3\n\tbuffer_load_dwordx4 %1, %2, %3 offen lds" :: "s"(sl + i * 1024), "v"(vq), "s"(rwq), "s"(sq ^ (i * 16 * HD * 2)) : "memory");
    const f32x4 lv = {(float)(128 * (lr >> 3) + (lr & 7)), (float)(lr < 8 ? 256 + 128 * (lr >> 2) + 2 * (lr & 3) : 512 + 128 * ((lr - 8) >> 1) + 4 * (lr & 1)),
                         (float)(lr <= 8 ? 1024 + 128 * lr : 0), (float)(8 * lr)};
    const int trow = 16 * (lg >> 1) + 4 * (lg & 1) + ((lr >> 2) & 1) + 8 * (lr >> 3);
    asm volatile(
        "s_nop 4\n\t" "s_mov_b32 s46, m0\n\t" "s_mov_b32 s38, " STR(DM * HD * 2) "\n\t" "s_mov_b32 s39, 0x20000\n\t" "s_mov_b32 s47, 0xbf80\n\t" "s_mov_b32 s48, 0xbf800000\n\t"
        WLG(0)
        "ds_read_b128 v[64:67], %[pa]\n\t" "ds_read_b128 v[68:71], %[pa] offset:16\n\t" "ds_read_b128 v[72:75], %[pa] offset:32\n\t" "ds_read_b128 v[76:79], %[pa] offset:48\n\t"
        "v_add_u32 v111, 256, %[pa]\n\t" KZERO
        KCHUNK("%[wk]", "%[vq]", "s_mov_b32 s40, %[sq]\n\t", 16 * HD * 2, 1, "%[p1]")
        WLG(0) KA(3, 0)
        KTQ(0, 0) KTQ(32, 4096) KTQ(64, 8192) KTQ(96, 12288)
        KCHUNK("%[wv]", "%[vq]", "", 16 * HD * 2, 0, "%[p2]")
        KTQ(16, 16384) KTQ(48, 16384 + 4096) KTQ(80, 16384 + 8192) KTQ(112, 16384 + 12288)
        KOUT(0)
        KCHUNK("%[wo]", "%[vo]", "s_mov_b32 s40, %[so]\n\t", 32 * DM * 2, 1, "%[p3]")
        KZERO
        KTQ(128, 0) KTQ(144, 4096) KTQ(160, 8192) KTQ(176, 12288)
        KCHUNK("%[wo]", "%[vo]", "s_xor_b32 s40, s40, " STR(512 * DM * 2) "\n\t", 32 * DM * 2, 0, "%[p4]")
        KTO(128, 16384) KTO(144, 16384 + 4096) KTO(160, 16384 + 8192) KTO(176, 16384 + 12288)
        KOUT(3072)
        "s_branch .Lke%=\n"
        ".Lkg%=:\n\t"
        KSUB
        "s_setpc_b64 s[44:45]\n"
        ".Lke%=:\n\t"
        WLG(0) "s_mov_b32 m0, s46"
        : "=&{a[0:31]}"(wa[0]), "=&{a[32:63]}"(wa[1]), "=&{a[64:95]}"(wa[2]), "=&{a[96:127]}"(wa[3]), "=&{a[128:159]}"(wa[4]), "=&{a[160:191]}"(wa[5]),
          "=&{v[128:159]}"(wo[0]), "=&{v[160:191]}"(wo[1])
        : "{v[112:115]}"(lv), [pa] "v"(ldsb + w * 4096 + lg * 64), [vq] "v"(vq), [vo] "v"(vo),
          [p1] "v"(pfo[1]), [p2] "v"(pfo[2]), [p3] "v"(pfo[3]), [p4] "v"(pfo[4]), [aq] "v"(sl + trow * 64 + (lr & 3) * 8), [ao] "v"(sl + trow * 32 + (lr & 3) * 8), [l16] "v"(lane * 16),
          [wk] "s"(Wk), [wv] "s"(Wv), [wo] "s"(Wo), [sq] "s"(sq), [so] "s"(so), [sl] "s"(sl), [sc] "s"(scr), [rt] "s"(rst), [ko] "s"(2 * SSM_L * SSM_D * 2 + ch0 * 3072)
        : CL64, CL72, CL80, CL88, CL96, CL8(192, 193, 194, 195, 196, 197, 198, 199), CL8(200, 201, 202, 203, 204, 205, 206, 207), CL8(208, 209, 210, 211, 212, 213, 214, 215),
          CL8(216, 217, 218, 219, 220, 221, 222, 223), "s36", "s37", "s38", "s39", "s40", "s41", "s42", "s43", "s44", "s45", "s46", "s47", "s48", "scc", "memory");
    {
      const u32x4 pz = (u32x4){SENT, SENT, SENT, SENT};
      if (w < 3) {
        u32x4 v = pz;
        if (w == 0 && lane < 32) v = (u32x4){nosent(m0v[0]), nosent(m0v[1]), nosent(m0v[2]), nosent(m0v[3])};
#pragma unroll
        for (int hf = 0; hf < 2; ++hf) {
          __builtin_amdgcn_raw_buffer_store_b128(v, rsx, pz1, XSB(w) + hf * 8192, 16);
          __builtin_amdgcn_raw_buffer_store_b128(pz, rsx, XR2(vcu) + lane * 16, XR2B + (w & 1) * XR2S + hf * 8192, 16);
          __builtin_amdgcn_raw_buffer_store_b128(pz, rsx, lane < 16 ? XR2(vcu) + 1024 + lane * 16 : OOR, XR2B + (w & 1) * XR2S + hf * 8192, 16);
        }
      } else {
        __builtin_amdgcn_raw_buffer_store_b128(pz, rsx, lane < 8 ? lane * 16 : OOR, calo, 16);
        if (b < 32) {
          __builtin_amdgcn_raw_buffer_store_b128(pz, rsx, lane * 16, XSYN + b * 2048, 16);
          __builtin_amdgcn_raw_buffer_store_b128(pz, rsx, lane * 16, XSYN + b * 2048 + 1024, 16);
        } else if (b >= 48 && b < 48 + 41) {
          __builtin_amdgcn_raw_buffer_store_b128(pz, rsx, lane * 16, PGO_D + (b - 48) * 1024, 16);
        }
      }
      asm volatile("" ::: "memory");
    }
    xp0 = __builtin_amdgcn_raw_buffer_load_b32(rsx, 0, pgo, 16);
    clk_edge(cyc0, tk0);
    asm volatile("s_waitcnt vmcnt(0)\n\ts_barrier" : "+v"(xp0)::"memory");
#define CLKF() clk_rate(cyc0, tk0)
    if (w == 0) {
      const u64 c0 = __builtin_amdgcn_s_memtime();
      asm volatile("s_waitcnt lgkmcnt(0)" ::: "memory");
      u32 v = __builtin_amdgcn_raw_buffer_load_b32(rsx, 0, calo, 16);
      asm volatile("s_waitcnt vmcnt(0)" : "+v"(v)::"memory");
      const u32 lat = (u32)(__builtin_amdgcn_s_memtime() - c0);
      const float f = CLKF();
      if (lane == 0) __builtin_amdgcn_raw_buffer_store_b128((u32x4){tag, __float_as_uint(f), lat, 0u}, rsx, 0, PGO_Y + b * 128, 16);
      asm volatile("" ::: "memory");
    }
    {
      u32 r[1][8][8];
      tr_chunk<1, 8>((const u16*)(lds + LR_STG + w * 32768), lr, lg, r);
#pragma unroll
      for (int i = 0; i < 8; ++i)
#pragma unroll
        for (int e = 0; e < 8; ++e) { const int o = (8 + i) * 8 + e; wo[o >> 5][o & 31] = r[0][i][e]; }
      asm volatile("s_waitcnt lgkmcnt(0)" ::: "memory");
    }
  }
  const int hf = (lr >> 1) & 1;
  const int voff1 = lg * 256 + (lr >> 3) * 128 + (((lr >> 2) & 1) * 2 + (lr & 1)) * 32 + hf * 16;
  const int voff3 = (lr >> 3) * 512 + (lg >> 1) * 256 + (((lr >> 2) & 1) * 2 + (lr & 1)) * 16 + (lg & 1) * 128 + hf * 64;
  const int ard = ldsb + (2 * (lr >> 3) + (lg >> 1)) * 640 + (lr & 1) * 272 + ((lr >> 1) & 3) * 64 + (lg & 1) * 32;
  const int ctl = ldsb + LR_CTL, one = 1;
  const int r8 = lane & 7, hk = (lane >> 3) & 1;
#define COA 1
#define COB 1
  const int st01 = lr < 8 ? XR2(vcu) + lr * 16 + lg * 128 : OOR, st2 = (lr < 8 && lg >= 2) ? XR2(vcu) + 1024 + lr * 16 + (lg - 2) * 128 : OOR;
  const int ld01 = hk * 512 + r8 * 16 + lg * 128, ld2 = XR2(head * 4 + lg) + 1024 + r8 * 16 + hk * 128;
  int sto = XR3A(r8 >> 2, vcu * 256 + (r8 & 3) * 16 + lg * 64 + hk * 8);
  const int stk = lr >= 8 ? (lg * 8 + (lr - 8)) * 16 : OOR;
  const int lo = X4C ? 2 * (lr & 7) + (lr >> 3) : lr;
  const int arw = 0, arr = ldsb + 3840 + ((lo >> 3) + 2 * ((lo >> 2) & 1)) * 64 + (lo & 3) * 16;
  const int st1 = XR1A(lg, cg * 256 + rg * 128 + lo * 8);
  const int avw = 0;
  const int avc = ldsb + 2560 + (lr >> 3) * 640 + (8 * ((lr >> 2) & 1) + (lr & 3)) * 4;
  const int avr = avc + (lg & 1) * 272 + (lg >> 1) * 64, avf = avc + (lg & 1) * 128 + (lg >> 1) * 16;
  u32x16 kc = {(u32)voff1, (u32)voff3, (u32)idx, (u32)TEX(lane), (u32)(lane < 16 ? XR2(vcu) + 1024 + lane * 16 : OOR), (u32)ard, (u32)ctl, (u32)one, (u32)st01, (u32)st2, (u32)(XR2(vcu) + lane * 16), (u32)ld2, (u32)(lane * 16), (u32)arw, (u32)arr, (u32)stk};
  int o1c = XR1A(xcd, w * 4096) + rot1 * 1024, o3c = XR3A(rg, w * 8192) + rot3 * 1024;
  const int fo = w < 2 ? FO_K + ((head * 2 + w) * 4 + ks) * 1024 : FO_V + (head * 8 + 2 * ks + (w - 2)) * 1024;
  int xp = xprog(__builtin_amdgcn_readfirstlane(xp0), tag);
#define XWAIT(n) for (int it_ = 0; it_ < SPIN_CAP && xp < (n); ++it_) {                                               \
    if (it_) asm volatile("s_sleep 4" ::: "memory");                                                                  \
    xp = xprog(__builtin_amdgcn_readfirstlane(__builtin_amdgcn_raw_buffer_load_b32(rsx, 0, pgo, 16)), tag); asm volatile("" ::: "memory"); }
  XWAIT(1)
  *(u32x4*)(lds + LR_FRG + w * 1024 + lane * 16) = __builtin_amdgcn_raw_buffer_load_b128(rsf, lane * 16, fo, 16);
  const u32 tbase = __builtin_amdgcn_readfirstlane(*(const u32*)(lds + LR_CTL + 4));
  const u32 tb0 = tbase - (1u << 20);
#define TABLE(l) ({ const float f_ = CLKF() * *(const float*)(lds + LR_CTL + 28);          \
    const float k_ = 1.f + TKS * (fminf(fmaxf(23.9f / f_, 1.f), 1.5f) - 1.f);                                          \
    const int nb_ = (int)(TDB * k_ + 0.5f) + TLB * (l), nc_ = nb_ + (int)((TDC - TDB) * k_ + 0.5f) + TLC * (l), np_ = nc_ + (int)((TP - TDC) * k_ + 0.5f) + TLP * (l); \
    (u32)(nb_ | (nc_ << 10) | (np_ << 20)); })
#define XPN 1
  if (b == 0 && w == 1) {
    u32 tt = 0, lat = 0;
    bool xok = false;
    for (int it = 0; it < SPIN_CAP; ++it) {
      asm volatile("" ::: "memory");
      const u32x4 r0 = __builtin_amdgcn_raw_buffer_load_b128(rsx, lane * 128, PGO_Y, 16), r1 = __builtin_amdgcn_raw_buffer_load_b128(rsx, lane * 128 + 8192, PGO_Y, 16);
      if (!xok) xok = __ballot(xprog(__builtin_amdgcn_raw_buffer_load_b32(rsx, lane * 128, PGO_X, 16), tag) < XPN) == 0;
      const u64 miss = __ballot(r0[0] != tag || r1[0] != tag);
      if (tt == 0 && __popcll(miss) <= 24) {
        float fm = (r0[0] == tag ? __uint_as_float(r0[1]) : 0.f) + (r1[0] == tag ? __uint_as_float(r1[1]) : 0.f), fn = (r0[0] == tag ? 1.f : 0.f) + (r1[0] == tag ? 1.f : 0.f);
#pragma unroll
        for (int o = 4; o < 64; o *= 2) { fm += __shfl_xor(fm, o); fn += __shfl_xor(fn, o); }
        fm = fn > 0.f ? fm / fn : 100.f;
        const float f0 = __uint_as_float(__builtin_amdgcn_readfirstlane(__float_as_uint(fm)));
        fm = fminf(fm, __shfl_xor(fm, 1)); fm = fminf(fm, __shfl_xor(fm, 2));
        if (lane == 0) *(float*)(lds + LR_CTL + 28) = fminf(fmaxf(fm / f0, 0.8f), 1.f);
        asm volatile("s_waitcnt lgkmcnt(0)" ::: "memory");
        tt = TABLE(0);
      }
      lat = min(r0[2], r1[2]);
      if (miss == 0 && xok) break;
    }
    { auto r = __builtin_amdgcn_permlane16_swap(lat, lat, false, false); lat = min(r[0], r[1]); }
    { auto r = __builtin_amdgcn_permlane32_swap(lat, lat, false, false); lat = min(r[0], r[1]); }
    u32 mm[4];
#pragma unroll
    for (int k = 0; k < 4; ++k)
      mm[k] = __builtin_amdgcn_readlane(lat, 4 * k) + __builtin_amdgcn_readlane(lat, 4 * k + 2) + __builtin_amdgcn_readlane(lat, 4 * (k ^ 2) + 1) + __builtin_amdgcn_readlane(lat, 4 * (k ^ 2) + 3);
    const u32 hb = mm[0] + mm[2] <= mm[1] + mm[3] ? 0u : 1u;
    const u32 best = (hb << 13) | ((hb ? mm[1] <= mm[3] : mm[0] <= mm[2]) ? CLS0 : 16384u - CLS0);
    if (lane == 0) __builtin_amdgcn_raw_buffer_store_b128((u32x4){rt32() + TD0, tt, best, tag}, rsx, 0, XSTA, 16);
  }
  XWAIT(2)
  LBAR();
  {
    u64 pc; asm volatile("s_getpc_b64 %0" : "=s"(pc));
    const u32 cv = __builtin_amdgcn_raw_buffer_load_b32(__builtin_amdgcn_make_buffer_rsrc((void*)pc0, 0, clen, 0x00020000), (int)(pc - pc0) + tid * 64, 0, 0);
    asm volatile("" :: "v"(cv));
  }
  xp = max(xp, xprog(__builtin_amdgcn_readfirstlane(__builtin_amdgcn_raw_buffer_load_b32(rsx, 0, pgo, 16)), tag));
  u32x4 st = wait_start(rsx, fb, tag, false);
  const int flip = __builtin_amdgcn_readfirstlane(st[2]) & 8192, cls = __builtin_amdgcn_readfirstlane(st[2]) & 16384;
  o1c ^= cls; o3c ^= cls; sto ^= cls; kc[4] ^= cls; kc[8] ^= cls; kc[9] ^= cls; kc[10] ^= cls; kc[11] ^= cls;
  { const u32 now = rt32(); if (st[0] - now + 100000u > 200000u) st[0] = now; }
  u32x16 kd = {(u32)((ld01 + XR2(vcu ^ 1)) ^ cls), (u32)((ld01 + XR2(vcu ^ 2)) ^ cls), (u32)((ld01 + XR2(vcu ^ 3)) ^ cls), (u32)(X4B && hk ? OOR : sto), (u32)avr, (u32)(avr + 4096), (u32)(avr + 8192),
               (u32)(avr + 12288), (u32)avw, (u32)(X4C && hk ? OOR : st1 ^ cls), (u32)(ldsb + LR_FRG + lane * 16), (u32)(XSB(lane + 1) + flip), (u32)(lane * 8), (u32)avf,
               (u32)(b == 0 && lane == 0 ? PGO_R + flip : OOR), (u32)(pz1 ^ cls)};
  u32x16 sa = {(u32)flip, (u32)(XSB(1) + flip), 0u, (u32)(fo + NH * 8 * 1024), (u32)(ldsb + LR_FRG + 4096 + w * 1024), (u32)(FO_KM + (head * 4 + ks) * 512),
               (u32)(FO_VM + (head * 8 + 2 * ks) * 256), tb0, st[0] - tb0 - (xcd & 1) * XSK, 0u, (u32)SYO(1), 0u, 0u, 0u, 0u, 0u};
  int snt = -1, synced = 0;
  int cb = __builtin_amdgcn_readfirstlane(st[1]), cc = (cb >> 10) & 1023, cp = (cb >> 20) & 1023;
  cb &= 1023;
#define RESYNC()                                                                                                      \
  {                                                                                                                   \
    for (int it = 0; it < 100; ++it) {                                                                                \
      const u32x2 tw = __builtin_amdgcn_raw_buffer_load_b64(rsx, 0, SYO(seg) + 8, 16);                                \
      const u32 tv = __builtin_amdgcn_readfirstlane(tw[0]), to = __builtin_amdgcn_readfirstlane(tw[1]);               \
      if (tv != SENT && to != SENT) {                                                                                 \
        cb = to & 1023; cc = (to >> 10) & 1023; cp = to >> 20; sa[7] = tb0 + seg * cp; sa[8] = tv - sa[7] - (xcd & 1) * XSK;            \
        if (lane == 0) *(u32*)(lds + LR_CTL + 24) = to;                                                               \
        break;                                                                                                        \
      }                                                                                                               \
      asm volatile("s_sleep 4" ::: "memory");                                                                         \
    }                                                                                                                 \
    if (lane == 0) *(u32*)(lds + LR_CTL) = sa[8];                                                                     \
    synced = seg;                                                                                                     \
  }
  int nev = 0; (void)nev;
  bool rsn = false;
  u32x32 keep;
  u32x2 ov;
  asm volatile("" : "={v[60:91]}"(keep));
  while (true) {
    u32x8 sc = {(u32)o1c, (u32)o3c, (u32)snt, (u32)cp, (u32)cb, (u32)cc, 0u, (u32)pgo};
    sa[15] = rsn ? 1u : 0u;
    sa[14] = xp;
#pragma unroll
    for (int i = 0; i < 16; ++i) sa[i] = __builtin_amdgcn_readfirstlane(sa[i]);
#pragma unroll
    for (int i = 0; i < 8; ++i) sc[i] = __builtin_amdgcn_readfirstlane(sc[i]);
    asm volatile(
        "s_nop 4\n\t" "s_mov_b32 s60, m0\n\t"
        "s_mov_b32 m0, %[mb]\n\t" "s_mov_b32 s61, s51\n\t" "v_mov_b32 v48, %[tg]\n\t" "s_mov_b32 s64, s50\n\t"
        EXTRA "s_mov_b32 s70, 0\n\t" "s_mov_b32 s78, 0xff00ff00\n\t" "s_mov_b32 s79, 0xff00ff00\n\t"
        R2BASE
        "s_cmp_eq_u32 s47, 1\n\t" "s_cbranch_scc1 .LB%=\n\t"
        "s_cmp_eq_u32 s47, 2\n\t" "s_cbranch_scc1 .LC%=\n"
        ".LA%=:\n\t"
        CLKW(".Lta%=", "0")
        "s_add_u32 s50, s36, s52\n\t"
        TSA(0) GLD(96, 0)
        "ds_read_b32 v62, v38 offset:16\n\t" "ds_read_b128 v[52:55], v18\n\t" "ds_read_b128 v[56:59], v18 offset:1024\n\t" ZER(64) ZER(68)
        GLD(100, 1024) ZER(72) ZER(76) ZER(24) ZER(28) R2BASE
        GLD(104, 2048)
        "s_mov_b32 m0, s40\n\t" "s_mov_b32 s47, 0\n\t"
        "s_sub_i32 s63, s64, s45\n\t" "s_cmp_lt_i32 s63, " STR(XTH) "\n\t" "s_cselect_b32 s63, s59, %[dm]\n\t" "s_nop " STR(APC) "\n\t"
        GLD(108, 3072)
        ".Lfw%=:\n\t"
        WLG(0)
        "v_readfirstlane_b32 s49, v62\n\t"
        "s_cmp_ge_u32 s49, s38\n\t"
        "s_cbranch_scc1 .Lfx%=\n\t"
        "ds_read_b32 v62, v38 offset:16\n\t" "ds_read_b128 v[52:55], v18\n\t" "ds_read_b128 v[56:59], v18 offset:1024\n\t"
        "s_branch .Lfw%=\n"
        ".Lfx%=:\n\t"
        WVM(3) TSA(1) SQ(0, 0) MXA0(96) SQ(1, 0) MXB(96) SQ(2, 0) SQ(3, 0)
        WVM(2) SQ(0, 1) "buffer_load_dwordx4 v44, %[rf], s39 offen sc1 lds\n\t" SQ(1, 1) "buffer_load_dword v48, off, %[rs], s63 sc1\n\t"
        SQ(2, 1) MXA(100) MXB(100) SQ(3, 1) "s_mov_b32 m0, %[mb]\n\t"
        QBLK(2, MXA)
        WVM(2) SQ(0, 3) MXA(108) MXB(108) SQ(1, 3) CMPB SQ(2, 3) SQ(3, 3) TSA(2)
        "s_cbranch_vccnz .Lfail%=\n\t"
        "s_nop 1\n\t" PSA(64) PSW(64, 0) PSW(65, 272) PSA(68) SV(0, 0) PSW(68, 640) PSW(69, 912) PSA(72) SV(1, 0) PSW(72, 1280) PSW(73, 1552) PSA(76) SV(0, 1) PSW(76, 1920) PSW(77, 2192)
        ARRIVE(8) TSA(3)
        RDQ(80, 0) SV(1, 1) RDQ(84, 16) RDQ(88, 4096) SV(0, 2) RDQ(92, 4112) RDQ(112, 8192) SV(1, 2) RDQ(116, 8208) WLG(12) RDQ(120, 12288) SV(0, 3) RDQ(124, 12304) SV(1, 3)
        WLG(8) TSA(4)
        "v_readfirstlane_b32 s48, v62\n\t"
        "s_and_b32 s48, s48, 3\n\t"
        ROLESV "s_cmp_lg_u32 s48, 3\n\t"
        "s_cbranch_scc1 .Lnl%=\n\t"
        WLG(4) PKA(80, 80, 88) PKA(82, 82, 90) PKA(84, 84, 92) PKA(86, 86, 94)
        WLG(0) PKA(112, 112, 120) PKA(114, 114, 122) PKA(116, 116, 124) PKA(118, 118, 126)
        TSA(5) PKA(80, 80, 112) PKA(82, 82, 114) PKA(84, 84, 116) PKA(86, 86, 118)
        "v_cvt_pk_bf16_f32 v72, v80, v81\n\t" "v_cvt_pk_bf16_f32 v73, v82, v83\n\t" "v_cvt_pk_bf16_f32 v74, v84, v85\n\t" "v_cvt_pk_bf16_f32 v75, v86, v87\n\t"
        "s_nop 1\n\t"
        "v_mfma_f32_16x16x32_bf16 v[88:91], v[72:75], v[72:75], 0\n\t"
        "v_mfma_f32_16x16x32_bf16 v[80:83], v[52:55], v[72:75], 0\n\t"
        "v_mfma_f32_16x16x32_bf16 v[84:87], v[56:59], v[72:75], 0\n\t"
        "s_nop 5\n\t"
        "buffer_store_dwordx4 v[88:91], v41, %[rs], s71 offen sc1\n\t"
        "buffer_store_dwordx4 v[80:83], v40, %[rs], s71 offen sc1\n\t"
        "buffer_store_dwordx4 v[84:87], v40, %[rs], s71 offen offset:512 sc1\n\t" TSA(6) TSB(7)
        VPOST TSA(7)
        "s_cmp_eq_u32 s45, 63\n\t"
        "s_cbranch_scc0 .LB%=\n\t"
        "buffer_store_dwordx4 v[72:75], v47, %[rf], s41 offen sc1\n\t"
        "s_branch .LC%=\n"
        ".LB%=:\n\t"
        CLKW(".Ltb%=", "s66")
        "s_mov_b32 s47, 1\n\t"
        R2TEXT
        "s_branch .LC2%=\n"
        R2MAX
        ".Lnl%=:\n\t"
        "s_nop 2\n\t" WLG(4) VPOST
        "s_cmp_lg_u32 s48, 0\n\t"
        "s_cbranch_scc1 .LC%=\n"
        ".Lv0%=:\n\t"
        "v_add_u32 v80, 4096, v21\n\t" "v_add_u32 v81, 8192, v21\n\t" "v_add_u32 v82, 12288, v21\n\t"
        "ds_read_b32 v59, v38 offset:12\n\t"
        "ds_read2_b32 v[112:113], v21 offset0:0 offset1:68\n\t" "ds_read2_b32 v[114:115], v21 offset0:16 offset1:84\n\t"
        "ds_read2_b32 v[116:117], v80 offset0:0 offset1:68\n\t" "ds_read2_b32 v[118:119], v80 offset0:16 offset1:84\n\t"
        "ds_read2_b32 v[120:121], v81 offset0:0 offset1:68\n\t" "ds_read2_b32 v[122:123], v81 offset0:16 offset1:84\n\t"
        "ds_read2_b32 v[124:125], v82 offset0:0 offset1:68\n\t" "ds_read2_b32 v[126:127], v82 offset0:16 offset1:84\n\t"
        WLG(0)
        "v_readfirstlane_b32 s49, v59\n\t"
        "s_sub_u32 s49, s49, 4\n\t"
        "s_cmp_lt_i32 s49, s38\n\t"
        "s_cbranch_scc1 .Lv0%=\n\t"
        PKA(112, 112, 116) PKA(114, 114, 118) PKA(120, 120, 124) PKA(122, 122, 126) PKA(112, 112, 120) PKA(114, 114, 122)
        "v_cvt_pk_bf16_f32 v112, v112, v113\n\t" "v_cvt_pk_bf16_f32 v113, v114, v115\n\t"
        "s_add_u32 s49, s45, 1\n\t" "v_mov_b64 v[116:117], -1\n\t" "v_mov_b64 v[118:119], -1\n\t" "v_readlane_b32 s63, v19, s49\n\t"
        "s_cmp_eq_u32 s45, 63\n\t" "s_cselect_b32 s65, 0, s68\n\t"
        CLKL(".Ltv%=", "s65") CLKW(".Ltw%=", "s65")
        "s_lshl_b32 s50, s50, 17\n\t" "s_lshr_b32 s50, s50, 1\n\t" "s_or_b32 s50, s50, s55\n\t" "v_mov_b32 v114, s50\n\t" "s_lshl_b32 s50, s45, 7\n\t"
        "buffer_store_dwordx4 v[116:119], v23, %[rs], s63 offen sc1\n\t"
        "s_xor_b32 s63, s71, " STR(XR2S) "\n\t"
        "buffer_store_dwordx4 v[116:119], v42, %[rs], s63 offen sc1\n\t"
        "buffer_store_dwordx4 v[116:119], v36, %[rs], s63 offen sc1\n\t"
        "buffer_store_dwordx2 v[112:113], v20, %[rf], s42 offen sc1\n\t"
        "buffer_store_dword v114, v22, %[rs], s50 offen sc1\n"
        ".LC%=:\n\t"
        "s_mov_b32 s47, 3\n\t"
        "s_cmp_eq_u32 s45, 63\n\t"
        "s_cbranch_scc1 .Lend%=\n\t"
        CLKL(".Ltl%=", "s67")
        ".LC2%=:\n\t"
        CLKW(".Ltc%=", "s67")
        "s_add_u32 s50, s36, s53\n\t"
        TSW(0)
        GL1(96, 0) PC GL1(100, 1024) PC GL1(104, 2048) PC GL1(108, 3072) PC GL2(112, 0) PC GL2(116, 1024)
        "s_mov_b32 s48, s36\n\t" "s_mov_b32 s36, s37\n\t" "s_add_u32 s45, s45, 1\n\t" "s_add_u32 s38, s38, 4\n\t" "v_readlane_b32 s37, v19, s45\n\t"
        "s_add_u32 s39, s39, " STR(EXF) "\n\t" "s_xor_b32 s40, s40, 4096\n\t" "v_xor_b32 v18, 4096, v18\n\t"
        "s_add_u32 s41, s41, " STR(EXK) "\n\t" "s_add_u32 s42, s42, " STR(EXK) "\n\t" "s_add_u32 s43, s43, s55\n\t"
        GL2(120, 2048) ZER(64) ZER(68) "s_mov_b32 s47, 2\n\t"
        "s_mul_i32 s70, s69, 3\n\t" EXTRA
        GL2(124, 3072)
        "buffer_load_dword v61, off, %[rs], s46 sc1\n\t"
        "s_add_u32 s46, s46, 64\n\t"
        WVM(8) TSW(1) WBLK(0, MXA0) WBLK(1, MXA) WBLK(2, MXA) WBLK(3, MXA) WBLK(4, MXA) WBLK(5, MXA) WBLK(6, MXA)
        WVM(1) TSW(2) SW(0, 7) MXA(124) MXB(124) SW(1, 7) CMPB TSW(3)
        "s_cbranch_vccnz .Lfail%=\n\t"
        "s_nop 3\n\t"
        "v_add_f32_dpp v80, v64, v66 quad_perm:[0,1,2,3] row_mask:0x3 bank_mask:0xf\n\t" "v_add_f32_dpp v81, v65, v67 quad_perm:[0,1,2,3] row_mask:0x3 bank_mask:0xf\n\t"
        "v_add_f32_dpp v80, v68, v70 quad_perm:[0,1,2,3] row_mask:0xc bank_mask:0xf\n\t" "v_add_f32_dpp v81, v69, v71 quad_perm:[0,1,2,3] row_mask:0xc bank_mask:0xf\n\t"
        "s_nop 1\n\t"
        "v_permlane32_swap_b32 v80, v81\n\t"
        "v_add_f32 v80, v80, v81\n\t"
        PSW(80, 3840)
        ARRIVE(16) TSW(4)
        "ds_read_b128 v[80:83], v46\n\t" "ds_read_b128 v[84:87], v46 offset:4096\n\t"
        "ds_read_b128 v[88:91], v46 offset:8192\n\t" "ds_read_b128 v[92:95], v46 offset:12288\n\t"
        "v_readfirstlane_b32 s50, v48\n\t"
        WLG(4) TSW(5)
        "v_readfirstlane_b32 s51, v62\n\t"
        WLG(2) PKA(80, 80, 84) "s_and_b32 s51, s51, 3\n\t" PKA(82, 82, 86) "s_cmp_lg_u32 s51, 3\n\t" WLG(0) PKA(88, 88, 92) PKA(90, 90, 94) PKA(80, 80, 88) PKA(82, 82, 90)
        "v_cvt_pk_bf16_f32 v80, v80, v81\n\t" "v_cvt_pk_bf16_f32 v81, v82, v83\n\t"
        "v_min_u32 v80, -2, v80\n\t" "v_min_u32 v81, -2, v81\n\t"
        "s_cbranch_scc1 .Lnv%=\n\t"
        STC TSW(6)
        WVM(1) "s_branch .Lnw%=\n"
        ".Lnv%=:\n\t" WVM(0)
        ".Lnw%=:\n\t"
        "v_readfirstlane_b32 s49, v61\n\t"
        "s_sub_u32 s50, s50, %[tg]\n\t" "s_cmp_le_u32 s50, 80\n\t" "s_cselect_b32 s50, s50, 0\n\t"
        "s_max_i32 s64, s64, s50\n\t" "s_add_u32 s44, s44, s70\n\t"
        "s_not_b32 s49, s49\n\t" "s_or_b32 s49, s49, s61\n\t"
        "s_sub_i32 s50, s64, s45\n\t" "s_cmp_lt_i32 s50, " STEPCHK "\n\t" "s_cselect_b32 s50, 1, 0\n\t"
        "s_or_b32 s50, s50, s49\n\t"
        "s_cbranch_scc0 .LA%=\n\t"
        "s_mov_b32 s47, 4\n\t"
        "s_branch .Lend%=\n" ".Lfail%=:\n\t" "s_nop 7\n" ".Lend%=:\n\t"
        "s_waitcnt vmcnt(0) lgkmcnt(0)\n\t"
        ROLEOUT "s_mov_b32 s50, s64\n\t" "s_mov_b32 m0, s60"
        : "+{s[36:51]}"(sa), "+{v[8:23]}"(kd), "+{v[60:91]}"(keep), "=&{v[116:117]}"(ov) FINEO
        : "{v[32:47]}"(kc), "{s[52:59]}"(sc), [rs] "s"(rsx), [rf] "s"(rsf), [dm] "s"(__builtin_amdgcn_readfirstlane(PGO_Y + b * 128)), [tg] "s"(__builtin_amdgcn_readfirstlane(tag)), [r2] "s"(__builtin_amdgcn_readfirstlane(XR2B + flip)), [mb] "s"(__builtin_amdgcn_readfirstlane(ldsb + w * 4096)),
          "{a[0:31]}"(wa[0]), "{a[32:63]}"(wa[1]), "{a[64:95]}"(wa[2]), "{a[96:127]}"(wa[3]), "{a[128:159]}"(wa[4]), "{a[160:191]}"(wa[5]),
          "{v[128:159]}"(wo[0]), "{v[160:191]}"(wo[1]), "{v[192:223]}"(wo[2]), "{v[224:255]}"(wo[3])
        : CL8(24, 25, 26, 27, 28, 29, 30, 31), "v48", CL8(52, 53, 54, 55, 56, 57, 58, 59), "v92", "v93", "v94", "v95", CL96, "v112", "v113", "v114", "v115", "v118", "v119", CL120,
          "s60", "s61", "s62", "s63", "s64", "s65", "s66", "s67", "s68", "s69", "s70", "s71", "s72", "s73", "s74", "s75", "s76", "s77", "s78", "s79", "vcc", "scc", "memory");
    const int code = __builtin_amdgcn_readfirstlane(sa[11]);
    xp = __builtin_amdgcn_readfirstlane(sa[14]);
    if (code == 2) {
      sa[1] = sa[0]; sa[0] = sa[12]; sa[9] -= 1; sa[2] -= 4; sa[3] -= EXF; sa[4] ^= 4096; kd[10] ^= 4096;
      sa[5] -= EXK; sa[6] -= EXK; sa[7] -= cp; sa[10] -= 64;
    }
    const int seg = __builtin_amdgcn_readfirstlane(sa[9]);
    if (code == 3) break;
    if (code == 4) {
      XWAIT(seg + 2)
      if (sa[13] != 0) {
        rsn = false;
        if (b == 0 && sa[15] == 3) {
          const int l = min((int)*(const u32*)(lds + LR_CTL + 20), 40);
          const u32 tt = TABLE(l);
          if (lane == 0) {
            __builtin_amdgcn_raw_buffer_store_b64((u32x2){nosent(rt32() + TD), tt}, rsx, 0, SYO(seg) + 8, 16);
            if (seg > 2) *(u32*)(lds + LR_CTL + 20) = l + 1;
          }
        }
        RESYNC()
      }
      sa[11] = 0;
      continue;
    }
    if (code == 1) {
      u32 mx = 0;
#pragma unroll
      for (int i = 0; i < 20; ++i) if (i < 12 || i >= 16) mx = max(mx, keep[i]);
      if (__ballot(mx == SENT) == 0 || tries > TRY_CAP) { __builtin_amdgcn_raw_buffer_store_b64((u32x2){nosent(ov[0]), nosent(ov[1])}, rsx, sto, sa[0], 16); sa[11] = 2; continue; }
    }
    ++tries; tcnt += 1 << (10 * code);
    {
      const u32 to = lds_word(ctl + 24);
      if (to != 0 && (int)(to >> 20) != cp) { cb = to & 1023; cc = (to >> 10) & 1023; cp = to >> 20; sa[7] = tb0 + seg * cp; sa[8] = 0; }
    }
    if (tries > TRY_CAP) snt = snt == -1 ? 0 : snt + 1;
    if (code == 0 && synced != seg && __builtin_amdgcn_readfirstlane(__builtin_amdgcn_raw_buffer_load_b32(rsx, 0, SYO(seg), 16)) != SENT) {
      RESYNC()
      continue;
    }
    if (b == 0 && !rsn) {
      if (lane == 0) __builtin_amdgcn_raw_buffer_store_b32(1u, rsx, 0, SYO(seg + 1), 16);
      rsn = true;
    }
    sa[8] = max(sa[8], rt32() + TR - sa[7] - (code == 0 ? 0 : (code == 1 ? cb : cc) + code * TEX(seg)));
    if (lane == 0) atomicMax((u32*)(lds + LR_CTL), sa[8]);
  }
  LBAR();
  if (tid == 0) __builtin_amdgcn_raw_buffer_store_b32(1u, rsx, 0, PGO_D + vcu * 128, 16);
  if (b == 0 && w == 1) {
    for (int it = 0; it < SPIN_CAP; ++it) {
      asm volatile("" ::: "memory");
      if (__ballot(__builtin_amdgcn_raw_buffer_load_b32(rsx, lane * 128, PGO_S, 16) != tag || __builtin_amdgcn_raw_buffer_load_b32(rsx, lane * 128 + 8192, PGO_S, 16) != tag) == 0) break;
    }
    if (lane == 0) __builtin_amdgcn_raw_buffer_store_b32(tag, rsx, 0, XEPO, 16);
  }
}

#define XSTAT 3
#define XVM 8
#define XP0 -1
#define XSC " sc1"
#define XPITCH 1056
#define XSLOT (32 * XPITCH)
#define XW(HB, t) "a[" STR(HB) "+8*xk+4*" #t ":" STR(HB) "+8*xk+4*" #t "+3]"
#define XF(rt) "v[96+8*(xk&3)+4*" #rt ":96+8*(xk&3)+4*" #rt "+3]"
#define XFN(rt) "v[96+8*((xk+3)&3)+4*" #rt ":96+8*((xk+3)&3)+4*" #rt "+3]"
#define XA(AC, o) "v[" STR(AC) "+" #o ":" STR(AC) "+" #o "+3]"
#define XM1(d, a, b, FIRST) ".if (xk == 0) && " STR(FIRST) "\n\tv_mfma_f32_16x16x32_bf16 " d ", " a ", " b ", 0\n\t.else\n\tv_mfma_f32_16x16x32_bf16 " d ", " a ", " b ", " d "\n\t.endif\n\t"
#define XMF_QK(i, HB, AC, F) XMF_QK##i(HB, AC, F)
#define XMF_QK0(HB, AC, F) XM1(XA(AC, 0), XW(HB, 0), XF(0), F)
#define XMF_QK1(HB, AC, F) XM1(XA(AC, 4), XW(HB, 1), XF(0), F)
#define XMF_QK2(HB, AC, F) XM1(XA(AC, 8), XW(HB, 0), XF(1), F)
#define XMF_QK3(HB, AC, F) XM1(XA(AC, 12), XW(HB, 1), XF(1), F)
#define XMF_V(i, HB, AC, F) XMF_V##i(HB, AC, F)
#define XMF_V0(HB, AC, F) XM1(XA(AC, 0), XF(0), XW(HB, 0), F)
#define XMF_V1(HB, AC, F) XM1(XA(AC, 8), XF(0), XW(HB, 1), F)
#define XMF_V2(HB, AC, F) XM1(XA(AC, 4), XF(1), XW(HB, 0), F)
#define XMF_V3(HB, AC, F) XM1(XA(AC, 12), XF(1), XW(HB, 1), F)
#define XRD(rt, CUR, NXT) ".if xk < 13\n\tds_read_b128 " XFN(rt) ", " CUR " offset:64*(xk+3)+" STR(rt * 16 * XPITCH) "\n\t.else\n\t" \
                          "ds_read_b128 " XFN(rt) ", " NXT " offset:64*(xk-13)+" STR(rt * 16 * XPITCH) "\n\t.endif\n\t"
#define XCV(EPI, PV, i) ".if " STR(EPI) " && (xk >= 1) && (xk < 3)\n\tv_cvt_pk_bf16_f32 v[128+4*(xk-1)+" #i "], v[" STR(PV) "+8*(xk-1)+2*" #i "], v[" STR(PV) "+8*(xk-1)+2*" #i "+1]\n\t.endif\n\t"
#define XHALF(MF, HB, AC, PV, EPI, CUR, NXT, DSLOT, GOFF)                                                   \
  ".set xk, 0\n\t"                                                                                          \
  ".rept 16\n\t"                                                                                            \
  "s_waitcnt lgkmcnt(4)\n\t"                                                                                \
  MF(0, HB, AC, EPI) XRD(0, CUR, NXT) XCV(EPI, PV, 0)                                                       \
  ".if (xk >= 5) && (xk < 13)\n\t"                                                                          \
  "s_add_u32 m0, m0, " STR(XPITCH) "\n\t"                                                                   \
  ".endif\n\t"                                                                                              \
  MF(1, HB, AC, EPI) XRD(1, CUR, NXT) XCV(EPI, PV, 1)                                                       \
  ".if (xk >= 5) && (xk < 13)\n\t"                                                                          \
  "s_add_u32 %[dg], %[dg], 2048\n\t"                                                                        \
  ".endif\n\t"                                                                                              \
  MF(2, HB, AC, EPI) XCV(EPI, PV, 2)                                                                        \
  ".if (xk >= 4) && (xk < 12)\n\t"                                                                          \
  "buffer_load_dwordx4 %[l16], %[rx], %[dg] offen lds\n\t"                                                  \
  ".endif\n\t"                                                                                              \
  ".if xk == 0\n\t"                                                                                         \
  "s_add_u32 m0, %[dl], " STR((DSLOT) * XSLOT) "\n\t" "s_add_u32 %[dg], %[gb], " STR(GOFF) "\n\t"           \
  ".endif\n\t"                                                                                              \
  ".if " STR(EPI) " && (xk == " STR(XSTAT) ")\n\t"                                                                       \
  "buffer_store_dwordx4 v[128:131], v137, %[rf], %[sg] offen" XSC "\n\t"                                       \
  ".endif\n\t"                                                                                              \
  ".if " STR(EPI) " && (xk == 13)\n\t"                                                                      \
  "v_mov_b32 v137, %[l16]\n\t" "v_mov_b32 v138, %[lp2]\n\t"                                                 \
  ".endif\n\t"                                                                                              \
  ".if " STR(EPI) " && (xk == 14)\n\t"                                                                      \
  "s_add_u32 %[sg], %[sg], " STR(NH * 8 * 1024) "\n\t"                                                      \
  "v_add_u32 v136, 1, v136\n\t"                                                                             \
  ".endif\n\t"                                                                                              \
  ".if !" STR(EPI) " && (xk == 1)\n\t"                                                                      \
  "buffer_store_dword v136, %[fo], %[rp], 0 offen sc1\n\t"                                                  \
  ".endif\n\t"                                                                                              \
  MF(3, HB, AC, EPI) XCV(EPI, PV, 3)                                                                        \
  ".if xk == 0\n\t"                                                                                         \
  "s_waitcnt vmcnt(" STR(XVM) ")\n\t" "s_barrier\n\t"                                                               \
  ".endif\n\t"                                                                                              \
  ".if xk == 2\n\t"                                                                                         \
  "s_min_u32 %[dg], %[dg], " STR(S_LEN * DM * 2 - 16384 + 1024) "\n\t"                                      \
  ".endif\n\t"                                                                                              \
  ".if " STR(EPI) " && (xk == " STR(XSTAT) ")\n\t"                                                                       \
  "buffer_store_dwordx4 v[132:135], v138, %[rf], %[sg] offen" XSC "\n\t"                                       \
  ".endif\n\t"                                                                                              \
  ".set xk, xk+1\n\t"                                                                                       \
  ".endr\n\t"
#define XLOOP(MF)                                                                                           \
  "s_mov_b32 %[kp], m0\n\t"                                                                                 \
  "v_mov_b32 v136, %[p0]\n\t" "v_mov_b32 v137, " STR(OOR) "\n\t" "v_mov_b32 v138, " STR(OOR) "\n\t"                                                                                \
  "ds_read_b128 v[96:99], %[r0]\n\tds_read_b128 v[100:103], %[r0] offset:" STR(16 * XPITCH) "\n\t"          \
  "ds_read_b128 v[104:107], %[r0] offset:64\n\tds_read_b128 v[108:111], %[r0] offset:64+" STR(16 * XPITCH) "\n\t"   \
  "ds_read_b128 v[112:115], %[r0] offset:128\n\tds_read_b128 v[116:119], %[r0] offset:128+" STR(16 * XPITCH) "\n"   \
  ".Lx%=:\n\t"                                                                                              \
  XHALF(MF, 0, 64, 80, 1, "%[r0]", "%[r1]", 3, 66560) XHALF(MF, 128, 64, 80, 0, "%[r1]", "%[r2]", 0, 131072) \
  XHALF(MF, 0, 80, 64, 1, "%[r2]", "%[r3]", 1, 132096) XHALF(MF, 128, 80, 64, 0, "%[r3]", "%[r0]", 2, 196608) \
  "s_add_u32 %[gb], %[gb], 131072\n\t"                                                                      \
  "s_sub_u32 %[cnt], %[cnt], 1\n\t"                                                                         \
  "s_cmp_lg_u32 %[cnt], 0\n\t"                                                                              \
  "s_cbranch_scc1 .Lx%=\n\t"                                                                                \
  "s_mov_b32 m0, %[kp]\n\t"                                                                                 \
  "s_nop 7\n\t" "s_waitcnt lgkmcnt(0)"

template <bool ISV>
__device__ __forceinline__ void x_role(unsigned char* lds, const u16* __restrict__ x, const u16* __restrict__ Wm, int fo0, int head,
                                       __amdgpu_buffer_rsrc_t rsf, u32* prog, u32 tag, int lane, int w) {
  const int ldsb = (int)(size_t)(LAS unsigned char*)lds;
  const __amdgpu_buffer_rsrc_t rsxin = __builtin_amdgcn_make_buffer_rsrc((void*)x, 0, S_LEN * DM * 2, 0x00020000);
  const __amdgpu_buffer_rsrc_t rsp = __builtin_amdgcn_make_buffer_rsrc((void*)prog, 0, 128, 0x00020000);
  const int lr = lane & 15, lg = lane >> 4;
  const int xrd = ldsb + lr * XPITCH + lg * 16;
  const int fob = fo0 + (ISV ? (head * 8 + 2 * w) : (head * 8 + w)) * 1024;
  const int p2 = ISV ? 1024 : 4096;
  u32x32 xw[8];
  {
    const __amdgpu_buffer_rsrc_t rw = __builtin_amdgcn_make_buffer_rsrc((void*)Wm, 0, DM * HD * 2, 0x00020000);
    const int vq = (lane >> 4) * (HD * 2) + head * 256 + (lane & 15) * 16;
#define XWISSUE(c) _Pragma("unroll") for (int j = 0; j < 8; ++j)                                                       \
      asm volatile("s_mov_b32 m0, %0\n\ts_nop 3\n\tbuffer_load_dwordx4 %1, %2, %3 offen lds"                           \
                   :: "s"(ldsb + ((c) & 3) * XSLOT + (8 * w + j) * XPITCH), "v"(vq), "s"(rw), "s"((128 * (c) + 32 * w + 4 * j) * (HD * 2)) : "memory");
#define XXISSUE(h) _Pragma("unroll") for (int j = 0; j < 8; ++j)                                                       \
      asm volatile("s_mov_b32 m0, %0\n\ts_nop 3\n\tbuffer_load_dwordx4 %1, %2, %3 offen lds"                           \
                   :: "s"(ldsb + (h) * XSLOT + (8 * w + j) * XPITCH), "v"(lane * 16), "s"(rsxin), "s"((((h) >> 1) * 32 + 8 * w + j) * 2048 + ((h) & 1) * 1024) : "memory");
    XWISSUE(0) XWISSUE(1) XWISSUE(2) XWISSUE(3)
    const int q = lr >> 2, p = lr & 3;
#pragma unroll
    for (int c = 0; c < 8; ++c) {
      asm volatile("s_waitcnt vmcnt(24)\n\ts_barrier" ::: "memory");
#pragma unroll
      for (int s = 0; s < 4; ++s)
#pragma unroll
        for (int t = 0; t < 2; ++t) {
          const unsigned char* base = lds + (c & 3) * XSLOT + (8 * s + 2 * lg) * XPITCH + q * 256 + w * 64 + p * 16 + t * 8;
          const u32x2 t0 = __builtin_bit_cast(u32x2, __builtin_amdgcn_ds_read_tr16_b64_v4i16((LAS v4i16_t*)(base)));
          const u32x2 t1 = __builtin_bit_cast(u32x2, __builtin_amdgcn_ds_read_tr16_b64_v4i16((LAS v4i16_t*)(base + XPITCH)));
          const int o = ((4 * c + s) * 2 + t) * 4;
          xw[o >> 5][(o & 31) + 0] = t0[0]; xw[o >> 5][(o & 31) + 1] = t0[1]; xw[o >> 5][(o & 31) + 2] = t1[0]; xw[o >> 5][(o & 31) + 3] = t1[1];
        }
      LBAR();
      if (c < 4) { XWISSUE(c + 4) } else if (c < 7) { XXISSUE(c - 4) }
    }
  }
  asm volatile("s_waitcnt vmcnt(8)\n\ts_barrier" ::: "memory");
  u32x16 xacc;
  {
    u32 kp, dg; int gb = 8 * w * 2048, cnt = NSEG / 2, sg = fob - NH * 8 * 1024;
#define XASM(MF)                                                                                            \
    asm volatile(XLOOP(MF)                                                                                  \
                 : "=&{v[80:95]}"(xacc), [kp] "=&s"(kp), [dg] "=&s"(dg), [gb] "+s"(gb), [cnt] "+s"(cnt), [sg] "+s"(sg) \
                 : [r0] "v"(xrd), [r1] "v"(xrd + XSLOT), [r2] "v"(xrd + 2 * XSLOT), [r3] "v"(xrd + 3 * XSLOT), [l16] "v"(lane * 16), [lp2] "v"(lane * 16 + p2), \
                   [rx] "s"(rsxin), [rf] "s"(rsf), [rp] "s"(rsp), [fo] "v"(lane == 0 && w == 0 ? 0 : OOR), [dl] "s"(ldsb + 8 * w * XPITCH), [p0] "s"(tag + XP0), \
                   "{a[0:31]}"(xw[0]), "{a[32:63]}"(xw[1]), "{a[64:95]}"(xw[2]), "{a[96:127]}"(xw[3]),      \
                   "{a[128:159]}"(xw[4]), "{a[160:191]}"(xw[5]), "{a[192:223]}"(xw[6]), "{a[224:255]}"(xw[7]) \
                 : CL64, CL72, CL96, CL112, CL120, CL8(128, 129, 130, 131, 132, 133, 134, 135), "v136", "v137", "v138", "scc", "memory");
    if (ISV) { XASM(XMF_V) } else { XASM(XMF_QK) }
  }
#pragma unroll
  for (int jj = 0; jj < 2; ++jj) {
    u32x4 pk;
#pragma unroll
    for (int e = 0; e < 4; ++e) pk[e] = pack2(__uint_as_float(xacc[8 * jj + 2 * e]), __uint_as_float(xacc[8 * jj + 2 * e + 1]));
    __builtin_amdgcn_raw_buffer_store_b128(pk, rsf, lane * 16 + p2 * jj, fob + (NSEG - 1) * (NH * 8 * 1024), 16);
  }
  asm volatile("s_waitcnt vmcnt(0)\n\ts_barrier" ::: "memory");
  if (lane == 0 && w == 0) __hip_atomic_store(prog, tag + NSEG + 8, RLX_AGENT);
}

#define SSM_LO 1
#define SU_CP 4416
#define SU_U 0
#define SU_K (4 * SU_CP)
#define SU_P (SU_K + 10400)
#define SU_D (SU_P + 2048)
#define SU_RAW (SU_D + 2592)
#define SU_CH (SU_RAW + 4128 + 32)
#define SST(i)
__device__ __forceinline__ void ssm_unit(unsigned char* sm, int d0, __amdgpu_buffer_rsrc_t rst, __amdgpu_buffer_rsrc_t rsx, unsigned char* xb, int fb, bool& uok,
                                         const u16* __restrict__ Dskip, int tid, int lane, int w) {
  unsigned char* cb = sm + w * SU_CH;
  const int dd = d0 + w;
  SST(0)
  if (!uok) {
    for (int it = 0; it < SPIN_CAP; ++it) {
      asm volatile("" ::: "memory");
      if ((int)__builtin_amdgcn_readfirstlane(__builtin_amdgcn_raw_buffer_load_b32(rsx, 0, XUDN, 16)) >= 15) break;
      asm volatile("s_sleep 8" ::: "memory");
    }
    asm volatile("" ::: "memory");
    uok = true;
  }
  f32x4 acc[3];
#pragma unroll
  for (int t = 0; t < 3; ++t) acc[t] = __builtin_bit_cast(f32x4, __builtin_amdgcn_raw_buffer_load_b128(rst, lane * 16, 2 * SSM_L * SSM_D * 2 + dd * 3072 + t * 1024, 16));
  const float dsk = bf2f(Dskip[dd]);
  u32x4 uv[4];
#pragma unroll
  for (int i = 0; i < 4; ++i) uv[i] = __builtin_amdgcn_raw_buffer_load_b128(rst, (tid + 256 * i) * 16, d0 * 4096, 16);
  const int r = lane & 31, h = lane >> 5;
  {
    const int lr = lane & 15, lg = lane >> 4;
    SST(2)
    u32* Kd = (u32*)(cb + SU_D);
#pragma unroll
    for (int e = 0; e < 4; ++e) {
      Kd[128 * (lg >> 1) + 8 * lr + 4 * (lg & 1) + e] = split_hl(acc[0][e]);
      Kd[lg < 2 ? 256 + 64 * lg + 4 * lr + e : 384 + 32 * (2 * (lg - 2) + (e >> 1)) + 2 * lr + (e & 1)] = split_hl(acc[1][e]);
      if (lg < 2 || (lg == 2 && e == 0 && lr == 0)) Kd[512 + 16 * (4 * lg + e) + lr] = split_hl(acc[2][e]);
    }
    asm volatile("s_waitcnt lgkmcnt(0)" ::: "memory");
    unsigned char* kr = cb + SU_K + (lane + 1) * 80;
#pragma unroll
    for (int c8 = 0; c8 < 4; ++c8) {
      u32 kv[8];
#pragma unroll
      for (int e = 0; e < 8; ++e) kv[e] = Kd[kidx(32 * lane + 31 - 8 * c8 - e)];
      u32x4 hi, lo;
#pragma unroll
      for (int e = 0; e < 4; ++e) {
        hi[e] = __builtin_amdgcn_perm(kv[2 * e + 1], kv[2 * e], 0x05040100u);
        lo[e] = __builtin_amdgcn_perm(kv[2 * e + 1], kv[2 * e], 0x07060302u);
      }
      *(u32x4*)(kr + 16 * c8) = hi;
      if (SSM_LO) *(u32x4*)(kr + 5200 + 16 * c8) = lo;
    }
    if (lane < 8) *(u32x4*)(cb + SU_K + (lane >> 2) * 5200 + (lane & 3) * 16) = (u32x4){0u, 0u, 0u, 0u};
  }
  SST(3)
#pragma unroll
  for (int i = 0; i < 4; ++i) {
    const int l = 2 * (tid + 256 * i) + 8;
    *(u32*)(sm + 0 * SU_CH + SU_RAW + l * 2) = __builtin_amdgcn_perm(uv[i][2], uv[i][0], 0x05040100u);
    *(u32*)(sm + 1 * SU_CH + SU_RAW + l * 2) = __builtin_amdgcn_perm(uv[i][2], uv[i][0], 0x07060302u);
    *(u32*)(sm + 2 * SU_CH + SU_RAW + l * 2) = __builtin_amdgcn_perm(uv[i][3], uv[i][1], 0x05040100u);
    *(u32*)(sm + 3 * SU_CH + SU_RAW + l * 2) = __builtin_amdgcn_perm(uv[i][3], uv[i][1], 0x07060302u);
  }
  if (lane < 2) *(u32x4*)(cb + SU_RAW + lane * (4096 + 16)) = (u32x4){0u, 0u, 0u, 0u};
  LBAR();
  {
    if (lane < 20) *(u32x4*)(cb + SU_U + (lane / 5) * SU_CP + (lane % 5) * 16) = (u32x4){0u, 0u, 0u, 0u};
#pragma unroll
    for (int i = 0; i < 5; ++i) {
      const int g = lane + 64 * i;
      if (g < 257) {
        const u32x4 a = *(const u32x4*)(cb + SU_RAW + g * 16);
        const u32x2 c = *(const u32x2*)(cb + SU_RAW + g * 16 + 16);
        unsigned char* dst = cb + SU_U + 80 + g * 16;
        *(u32x4*)(dst) = a;
        *(u32x4*)(dst + SU_CP) = (u32x4){__builtin_amdgcn_alignbyte(a[1], a[0], 2), __builtin_amdgcn_alignbyte(a[2], a[1], 2), __builtin_amdgcn_alignbyte(a[3], a[2], 2), __builtin_amdgcn_alignbyte(c[0], a[3], 2)};
        *(u32x4*)(dst + 2 * SU_CP) = (u32x4){a[1], a[2], a[3], c[0]};
        *(u32x4*)(dst + 3 * SU_CP) = (u32x4){__builtin_amdgcn_alignbyte(a[2], a[1], 2), __builtin_amdgcn_alignbyte(a[3], a[2], 2), __builtin_amdgcn_alignbyte(c[0], a[3], 2), __builtin_amdgcn_alignbyte(c[1], c[0], 2)};
      }
    }
  }
  asm volatile("s_waitcnt lgkmcnt(0)" ::: "memory");
  SST(4)
  f32x16 cv0, cv1;
  {
    const int m0 = 48 - 15 + 8 * h + r;
    const int cbo = (int)(size_t)(LAS unsigned char*)cb;
    int ua = cbo + SU_U + (m0 & 3) * SU_CP + (m0 & ~3) * 2 - 32;
    const int kb = cbo + SU_K + 16 * h;
    int a1 = kb + (33 + r) * 80, a0 = kb + (1 + r) * 80, cnt = 32;
#define CRA(S, O) "ds_read_b64 " RNG("v", S, 2) ", %[ua] offset:" STR(O) "\n\t" "ds_read_b64 " RNG("v", (S) + 2, 2) ", %[ua] offset:" STR((O) + 8) "\n\t"
#define CRB(S, ad, O) "ds_read_b128 " RNG("v", S, 4) ", " ad " offset:" STR(O) "\n\t"
#define CMF(C, A, B) "v_mfma_f32_32x32x16_bf16 " RNG("v", C, 16) ", " RNG("v", A, 4) ", " RNG("v", B, 4) ", " RNG("v", C, 16) "\n\t"
#define CUPD "v_add_u32 %[ua], 64, %[ua]\n\t" "v_subrev_u32 %[a1], 80, %[a1]\n\t" "v_subrev_u32 %[a0], 80, %[a0]\n\t" "v_max_i32 %[a0], %[a0], %[kb]\n\t"
#define CHALF(S, N, O, UPD)                                                                       \
    WLG(5) CMF(64, S, (S) + 8) UPD CRA(N, O)                                                      \
    WLG(6) CMF(80, S, (S) + 16) CRB((N) + 8, "%[a0]", O) CRB((N) + 16, "%[a1]", O)                \
    WLG(6) CMF(80, (S) + 4, (S) + 8) CRA((N) + 4, (O) + 2048)                                     \
    WLG(7) CMF(64, S, (S) + 12) CRB((N) + 12, "%[a0]", (O) + 5200)                                \
    WLG(7) CMF(80, S, (S) + 20) CRB((N) + 20, "%[a1]", (O) + 5200)                                \
    CMF(80, (S) + 4, (S) + 12)
    asm volatile(
        CRA(96, 32) CRB(104, "%[a0]", 32) CRB(112, "%[a1]", 32) CRA(100, 32 + 2048) CRB(108, "%[a0]", 32 + 5200) CRB(116, "%[a1]", 32 + 5200)
        ZER(64) ZER(68) ZER(72) ZER(76) ZER(80) ZER(84) ZER(88) ZER(92)
        ".Lc%=:\n\t"
        CHALF(96, 120, 0, "")
        CHALF(120, 96, 32, CUPD)
        "s_sub_u32 %[cnt], %[cnt], 1\n\t"
        "s_cmp_lg_u32 %[cnt], 0\n\t"
        "s_cbranch_scc1 .Lc%=\n\t"
        "s_nop 7\n\t" "s_nop 3\n\t" WLG(0)
        : "=&{v[64:79]}"(cv0), "=&{v[80:95]}"(cv1), [ua] "+v"(ua), [a0] "+v"(a0), [a1] "+v"(a1), [cnt] "+s"(cnt)
        : [kb] "v"(kb)
        : CL96, CL112, CL120, CL8(128, 129, 130, 131, 132, 133, 134, 135), CL8(136, 137, 138, 139, 140, 141, 142, 143),
          CL8(144, 145, 146, 147, 148, 149, 150, 151), CL8(152, 153, 154, 155, 156, 157, 158, 159), "scc", "memory");
  }
  SST(5)
  {
    u16* yo = (u16*)(cb + SU_K);
#pragma unroll
    for (int g = 0; g < 2; ++g)
#pragma unroll
      for (int i = 0; i < 16; i += 4) {
        const int rr = 2 * i + 4 * h, a = 32 * g + r;
        const u32x2 uu = *(const u32x2*)(cb + SU_U + (48 + 32 * a + rr) * 2);
        float y[4];
#pragma unroll
        for (int e = 0; e < 4; ++e) {
          const float uf = __uint_as_float((e & 1) ? (uu[e >> 1] & 0xffff0000u) : (uu[e >> 1] << 16));
          y[e] = rbf_hw(g ? cv1[i + e] : cv0[i + e]) + rbf_hw(uf * dsk);
        }
        *(u32x2*)(yo + a * 40 + rr) = (u32x2){pack2(y[0], y[1]), pack2(y[2], y[3])};
      }
  }
  LBAR();
  SST(6)
#pragma unroll
  for (int i = 0; i < 4; ++i) {
    const int l = 2 * (tid + 256 * i);
    u32 c[4];
#pragma unroll
    for (int j = 0; j < 4; ++j) c[j] = *(const u32*)(sm + j * SU_CH + SU_K + ((l >> 5) * 40 + (l & 31)) * 2);
    __builtin_amdgcn_raw_buffer_store_b128((u32x4){__builtin_amdgcn_perm(c[1], c[0], 0x05040100u), __builtin_amdgcn_perm(c[3], c[2], 0x05040100u),
                                                   __builtin_amdgcn_perm(c[1], c[0], 0x07060302u), __builtin_amdgcn_perm(c[3], c[2], 0x07060302u)},
                                           rst, l * 8, SSM_L * SSM_D * 2 + d0 * 4096, 16);
  }
  asm volatile("s_waitcnt vmcnt(0)" ::: "memory");
  LBAR();
  if (tid == 0) __hip_atomic_fetch_add((u32*)(xb + XYDN), 1u, RLX_AGENT);
  SST(7)
}

#define L_TOTAL 163840
__device__ __forceinline__ int ldw(__amdgpu_buffer_rsrc_t rs, int off) {
  asm volatile("" ::: "memory");
  return __builtin_amdgcn_readfirstlane(__builtin_amdgcn_raw_buffer_load_b32(rs, 0, off, 16));
}
#define GF(rt) "v[64+8*(xk&7)+4*" #rt ":64+8*(xk&7)+4*" #rt "+3]"
#define GW2 "v[128+4*xk:128+4*xk+3]"
#define GW3 "v[24+4*(xk&1):24+4*(xk&1)+3]"
#define GLOAD(S, K) "s_xor_b32 %[tq], " S ", 1024*(" K ")\n\t"
#define GTR(K) "ds_read_b128 v[24+4*((" K ")&1):24+4*((" K ")&1)+3], %[wl] offset:1024*((" K ")&31)\n\t"
#define GPACE 0
#define YAUX 16
#define UDELAY 100
#define GNOSSM 1
#define GNG 3
#define GS0 22
__device__ __forceinline__ void g_role(unsigned char* lds, const u16* __restrict__ Wo, u16* __restrict__ out, __amdgpu_buffer_rsrc_t rsx,
                                       __amdgpu_buffer_rsrc_t rsf, int fb, u32 tag, int grp, int sl, int w) {
  const int lane = fresh_lane();
  const int lr = lane & 15, lg = lane >> 4;
  const int rot = (2 * sl + 8 * w) & 31;
  const int ldsb = (int)(size_t)(LAS unsigned char*)lds;
  u32x32 xw[8], w2[4];
  LBAR();
  {
    const __amdgpu_buffer_rsrc_t rw = __builtin_amdgcn_make_buffer_rsrc((void*)Wo, 0, DM * HD * 2, 0x00020000);
    const int vq = (lane >> 3) * (DM * 2) + sl * 128 + (lane & 7) * 16;
    const int sq = (1024 * w + rot * 32) * (DM * 2);
#define GSLOT(c) ((c) < 8 ? (((c) & 3) == 3 ? 131072 + w * 8192 : w * 32768 + 8192 * (((c) & 3) + 1)) : (((c) - 8) % 3 == 2 ? 131072 + w * 8192 : w * 32768 + 8192 * (((c) - 8) % 3 + 2)))
#define GISSUE(c) _Pragma("unroll") for (int j = 0; j < 8; ++j)                                                        \
      asm volatile("s_mov_b32 m0, %0\n\ts_nop 3\n\tbuffer_load_dwordx4 %1, %2, %3 offen lds"                           \
                   :: "s"(ldsb + GSLOT(c) + j * 1024), "v"(vq), "s"(rw), "s"(sq ^ ((64 * (c) + 8 * j) * DM * 2)) : "memory");
    GISSUE(0) GISSUE(1) GISSUE(2) GISSUE(3)
    const int q = lr >> 2, p = lr & 3;
    u32x4 w3[16];
#pragma unroll
    for (int c = 0; c < 16; ++c) {
      if (c < 5) waitvm<24>(); else if (c < 14) waitvm<16>(); else if (c == 14) waitvm<8>(); else waitvm<0>();
      u32x4 f3[2];
#pragma unroll
      for (int s = 0; s < 2; ++s)
#pragma unroll
        for (int t = 0; t < 4; ++t) {
          const unsigned char* base = lds + GSLOT(c) + (32 * s + 8 * lg + q) * 128 + (t < 2 ? p * 16 + t * 8 : t * 32 + p * 8);
          const u32x2 t0 = __builtin_bit_cast(u32x2, __builtin_amdgcn_ds_read_tr16_b64_v4i16((LAS v4i16_t*)(base)));
          const u32x2 t1 = __builtin_bit_cast(u32x2, __builtin_amdgcn_ds_read_tr16_b64_v4i16((LAS v4i16_t*)(base + 512)));
          const int o = t < 2 ? ((2 * c + s) * 2 + t) * 4 : (2 * c + s) * 4;
          if (t < 2) { xw[o >> 5][(o & 31) + 0] = t0[0]; xw[o >> 5][(o & 31) + 1] = t0[1]; xw[o >> 5][(o & 31) + 2] = t1[0]; xw[o >> 5][(o & 31) + 3] = t1[1]; }
          else if (t == 2) { w2[o >> 5][(o & 31) + 0] = t0[0]; w2[o >> 5][(o & 31) + 1] = t0[1]; w2[o >> 5][(o & 31) + 2] = t1[0]; w2[o >> 5][(o & 31) + 3] = t1[1]; }
          else f3[s] = (u32x4){t0[0], t0[1], t1[0], t1[1]};
        }
      asm volatile("s_waitcnt lgkmcnt(0)" ::: "memory");
      if (c < 8) { *(u32x4*)(lds + w * 32768 + (2 * c) * 1024 + lane * 16) = f3[0]; *(u32x4*)(lds + w * 32768 + (2 * c + 1) * 1024 + lane * 16) = f3[1]; }
      else { w3[2 * (c - 8)] = f3[0]; w3[2 * (c - 8) + 1] = f3[1]; }
      if (GPACE) __builtin_amdgcn_s_sleep(GPACE);
      if (c < 4) { GISSUE(c + 4) } else if (c >= 5 && c < 13) { GISSUE(c + 3) }
    }
#pragma unroll
    for (int i = 0; i < 16; ++i) *(u32x4*)(lds + w * 32768 + (16 + i) * 1024 + lane * 16) = w3[i];
    LBAR();
  }
  wait_start(rsx, fb, tag, true);
  const int wl = ldsb + w * 32768 + lane * 16;
  const __amdgpu_buffer_rsrc_t rso = __builtin_amdgcn_make_buffer_rsrc((void*)out, 0, S_LEN * OUTLD * 2, 0x00020000);
  const int rw_ = ldsb + 131072 + w * 8192 + lane * 16, rr_ = ldsb + 131072 + w * 2048 + lane * 16;
  const int oa = ((16 * (w & 1) + lr) * OUTLD + 64 * sl + (w < 2 ? 8 * lg : 32 + 4 * lg)) * 2, ob = oa + (w < 2 ? 8 : 32);
  const int hq = (grp + GNG - (62 - GS0) % GNG) % GNG - 1, hf = hq < 2 ? hq : -1;
#define GRW(i) "ds_write_b128 %[rw], " RNG("v", 32 + 4 * (i), 4) " offset:" STR(1024 * (i)) "\n\t"
#define GRR(i) "ds_read_b128 " RNG("v", 32 + 4 * (i), 4) ", %[rr] offset:" STR(8192 * ((i) >> 1) + 1024 * ((i) & 1)) "\n\t"
#define GNXT(D, S) "s_cmp_lt_u32 " S ", " STR(GS0) "\n\t" "s_cselect_b32 " D ", 1, " STR(GNG) "\n\t" "s_add_u32 " D ", " D ", " S "\n\t"
#define GDN(S) "s_min_u32 s41, " S ", 63\n\t" "s_lshl_b32 s41, s41, 5\n\t" "v_add_u32 v23, s41, %[u0]\n\t" "v_lshrrev_b32 v22, 6, v23\n\t" \
               "v_and_b32 v23, 63, v23\n\t" "v_lshlrev_b32 v23, 7, v23\n\t" "s_lshr_b32 s41, s41, 6\n\t" "s_max_u32 s41, s41, 29\n\t" "s_lshl_b32 s41, s41, 13\n\t" "s_add_u32 s41, s41, %[pa]\n\t" \
               "buffer_load_dword v23, v23, %[rs], s41 offen sc1\n\t"
#define GOK(D) "v_cmp_le_i32 vcc, v23, v22\n\t" "s_cmp_eq_u64 vcc, 0\n\t" "s_cselect_b32 " D ", 1, 0\n\t"
static_assert(NAW == 64, "shifts above");
#define GEPI(MID, OA, OB) \
      GRW(0) GRW(1) GRW(2) GRW(3) GRW(4) GRW(5) GRW(6) GRW(7) \
      MID \
      WLG(0) "s_barrier\n\t" \
      GRR(0) GRR(1) GRR(2) GRR(3) GRR(4) GRR(5) GRR(6) GRR(7) \
      WLG(4) PKA(32, 32, 40) PKA(34, 34, 42) PKA(36, 36, 44) PKA(38, 38, 46) \
      WLG(0) PKA(48, 48, 56) PKA(50, 50, 58) PKA(52, 52, 60) PKA(54, 54, 62) \
      PKA(32, 32, 48) PKA(34, 34, 50) PKA(36, 36, 52) PKA(38, 38, 54) \
      "v_cvt_pk_bf16_f32 v20, v32, v33\n\t" "v_cvt_pk_bf16_f32 v21, v34, v35\n\t" "v_cvt_pk_bf16_f32 v18, v36, v37\n\t" "v_cvt_pk_bf16_f32 v19, v38, v39\n\t" \
      "buffer_store_dwordx2 v[20:21], " OA ", %[ro], s42 offen\n\t" \
      "buffer_store_dwordx2 v[18:19], " OB ", %[ro], s42 offen\n\t"
#define GH "v[64+4*(xk&15):64+4*(xk&15)+3]"
  asm volatile(
      "s_nop 4\n\t"
      "v_add_u32 v16, 131072, %[va]\n\t" "v_sub_u32 v15, %[ob], %[oa]\n\t" "v_add_u32 v15, v15, %[oc]\n\t"
      "s_mov_b32 s36, %[s0]\n\t" "s_mov_b32 s43, " STR(SPIN_CAP) "\n"
      ".Lp%=:\n\t"
      GDN("s36") WVM(0) GOK("s40")
      "s_cbranch_scc1 .Lr%=\n\t"
      "s_sleep 2\n\t" "s_sub_u32 s43, s43, 1\n\t" "s_cmp_gt_i32 s43, 0\n\t" "s_cbranch_scc1 .Lp%=\n"
      ".Lr%=:\n\t"
      GNXT("s37", "s36") GDN("s37")
      "s_lshl_b32 s38, s36, 18\n\t" "s_add_u32 s38, s38, %[gb]\n\t"
      ".set xk, 0\n\t.rept 8\n\t" GLOAD("s38", "xk")
      "buffer_load_dwordx4 " GF(0) ", %[va], %[rf], %[tq] offen sc1\n\t" "buffer_load_dwordx4 " GF(1) ", v16, %[rf], %[tq] offen sc1\n\t"
      ".set xk, xk+1\n\t.endr\n\t"
      WVM(16) GOK("s40")
      ".Lb%=:\n\t"
      GNXT("s37", "s36") GNXT("s41", "s37") GDN("s41") GTR("0")
      "s_cmp_lt_u32 s37, 63\n\t" "s_cselect_b32 s40, s40, 0\n\t"
      "s_cmp_lg_u32 s40, 0\n\t" "s_cselect_b32 s39, s37, s36\n\t"
      "s_lshl_b32 s39, s39, 18\n\t" "s_add_u32 s39, s39, %[gb]\n\t" "s_lshl_b32 s38, s36, 18\n\t" "s_add_u32 s38, s38, %[gb]\n\t"
      "s_lshl_b32 s42, s36, 17\n\t"
      "buffer_load_dword v17, %[vt], %[rf], s39 offen sc1\n\t"
      ".set xk, 0\n\t.rept 32\n\t"
      "s_waitcnt vmcnt(14)\n\t"
      XM1(XA(32, 0), XW(0, 0), GF(0), 1) GTR("xk+1") XM1(XA(32, 4), XW(0, 1), GF(0), 1)
      ".if xk < 24\n\t" GLOAD("s38", "xk+8") ".else\n\t" GLOAD("s39", "xk-24") ".endif\n\t"
      XM1(XA(32, 8), XW(0, 0), GF(1), 1) XM1(XA(32, 12), XW(0, 1), GF(1), 1)
      XM1(XA(32, 16), GW2, GF(0), 1) "s_waitcnt lgkmcnt(1)\n\t" XM1(XA(32, 20), GW3, GF(0), 1)
      "buffer_load_dwordx4 " GF(0) ", %[va], %[rf], %[tq] offen sc1\n\t"
      XM1(XA(32, 24), GW2, GF(1), 1) XM1(XA(32, 28), GW3, GF(1), 1)
      "buffer_load_dwordx4 " GF(1) ", v16, %[rf], %[tq] offen sc1\n\t"
      ".if xk == 16\n\t" "s_barrier\n\t" ".endif\n\t"
      ".set xk, xk+1\n\t.endr\n\t"
      GEPI(GOK("s41"), "%[oa]", "%[ob]")
      "s_mov_b32 s36, s37\n\t"
      "s_cmp_lg_u32 s40, 0\n\t" "s_mov_b32 s40, s41\n\t" "s_cbranch_scc1 .Lb%=\n\t"
      "s_cmp_lt_u32 s36, 63\n\t" "s_cbranch_scc1 .Lp%=\n\t"
      "s_cmp_lt_i32 %[hf], 0\n\t" "s_cbranch_scc1 .Le%=\n\t"
      "s_mov_b32 s36, 63\n"
      ".Lh%=:\n\t"
      GDN("s36") WVM(0) GOK("s40")
      "s_cbranch_scc1 .Li%=\n\t"
      "s_sleep 2\n\t" "s_sub_u32 s43, s43, 1\n\t" "s_cmp_gt_i32 s43, 0\n\t" "s_cbranch_scc1 .Lh%=\n"
      ".Li%=:\n\t"
      "s_lshl_b32 s38, %[hf], 17\n\t" "s_add_u32 s38, s38, 63*262144\n\t" "s_add_u32 s38, s38, %[gb]\n\t"
      ".set xk, 0\n\t.rept 16\n\t" GLOAD("s38", "xk") "buffer_load_dwordx4 " GH ", %[va], %[rf], %[tq] offen sc1\n\t" ".set xk, xk+1\n\t.endr\n\t"
      GTR("0")
      "s_lshl_b32 s42, %[hf], 16\n\t" "s_add_u32 s42, s42, 63*131072\n\t"
      ".set xk, 0\n\t.rept 32\n\t"
      ".if xk < 17\n\t" "s_waitcnt vmcnt(15)\n\t" ".else\n\t" "s_waitcnt vmcnt(31-xk)\n\t" ".endif\n\t"
      XM1(XA(32, 0), XW(0, 0), GH, 1) GTR("xk+1") XM1(XA(32, 4), XW(0, 1), GH, 1)
      XM1(XA(32, 16), GW2, GH, 1) "s_waitcnt lgkmcnt(1)\n\t" XM1(XA(32, 20), GW3, GH, 1)
      ".if xk < 16\n\t" GLOAD("s38", "xk+16") "buffer_load_dwordx4 " GH ", %[va], %[rf], %[tq] offen sc1\n\t" ".endif\n\t"
      ".if xk == 16\n\t" "s_barrier\n\t" ".endif\n\t"
      ".set xk, xk+1\n\t.endr\n\t"
      "s_nop 7\n\t"
      GEPI("", "%[oc]", "v15")
      ".Le%=:\n\t"
      WVM(0)
      : [tq] "=&s"(sl)
      : [rs] "s"(rsx), [s0] "s"(__builtin_amdgcn_readfirstlane(grp ? GS0 + grp : 0)), [gb] "s"(FO_O - 32768 + (32 * w + rot) * 1024), [va] "v"(lane * 16 + 32768),
        [rf] "s"(rsf), [wl] "v"(wl), [rw] "v"(rw_), [rr] "v"(rr_), [oa] "v"(oa), [ob] "v"(ob), [ro] "s"(rso), [u0] "v"(8 * w + (lane & 7)), [pa] "s"(PGO_A - 29 * 8192), [hf] "s"(__builtin_amdgcn_readfirstlane(hf)), [oc] "v"((w & 1) ? OOR - 64 : oa), [vt] "v"((sl >> 3) * 131072 + 32768 + (sl & 7) * 4096 + lane * 64 - rot * 1024),
        "{a[0:31]}"(xw[0]), "{a[32:63]}"(xw[1]), "{a[64:95]}"(xw[2]), "{a[96:127]}"(xw[3]),
        "{a[128:159]}"(xw[4]), "{a[160:191]}"(xw[5]), "{a[192:223]}"(xw[6]), "{a[224:255]}"(xw[7]),
        "{v[128:159]}"(w2[0]), "{v[160:191]}"(w2[1]), "{v[192:223]}"(w2[2]), "{v[224:255]}"(w2[3])
      : "v15", "v16", "v17", "v18", "v19", "v20", "v21", "v22", "v23", "vcc", CL8(24, 25, 26, 27, 28, 29, 30, 31), CL8(32, 33, 34, 35, 36, 37, 38, 39), CL8(40, 41, 42, 43, 44, 45, 46, 47),
        CL8(48, 49, 50, 51, 52, 53, 54, 55), CL8(56, 57, 58, 59, 60, 61, 62, 63), CL64, CL72, CL80, CL88, CL96, CL112, CL120,
        "s36", "s37", "s38", "s39", "s40", "s41", "s42", "s43", "scc", "memory");
}
static_assert(4 * SU_CH <= L_TOTAL && 4 * XSLOT <= L_TOTAL, "lds");
__global__ void __launch_bounds__(256, 1)
main_kernel(const u16* __restrict__ x, const u16* __restrict__ mem0, const u16* __restrict__ Wq, const u16* __restrict__ Wk, const u16* __restrict__ Wv, const u16* __restrict__ Wo,
            unsigned char* ws, const u16* __restrict__ u, const u16* __restrict__ A_re, const u16* __restrict__ A_im, const u16* __restrict__ B_re,
            const u16* __restrict__ B_im, const u16* __restrict__ C_re, const u16* __restrict__ C_im,
            const u16* __restrict__ log_dt, const u16* __restrict__ Dskip, u16* __restrict__ out
            ) {
  __shared__ __attribute__((aligned(8192))) unsigned char lds[L_TOTAL];
  const int w = __builtin_amdgcn_readfirstlane(threadIdx.x >> 6);
  u64 pc0;
  int clen;
  {
    asm volatile("s_getpc_b64 %0\n.Lkpc0:\n\ts_mov_b32 %1, .Lkpc1-.Lkpc0" : "=s"(pc0), "=s"(clen));
  }
#define LOCAL_IDS const int lane = fresh_lane(), tid = w * 64 + lane; (void)tid;
  const int si = (blockIdx.x >> 3) * 4 + (blockIdx.x & 3);
  unsigned char* xb = (unsigned char*)(((size_t)ws + XPAGE - 1) & ~(size_t)(XPAGE - 1));
  const int fb = (int)(ws + WS_F - xb);
  u32 tag;
  asm volatile("s_load_dword %0, %1, 0x0\n\ts_waitcnt lgkmcnt(0)\n\ts_add_u32 %0, %0, 256" : "=s"(tag) : "s"(ws + WS_F) : "scc", "memory");
  const __amdgpu_buffer_rsrc_t rsx = __builtin_amdgcn_make_buffer_rsrc(xb, 0, fb + (int)WS_F_SZ, 0x00020000);
  const __amdgpu_buffer_rsrc_t rsf = __builtin_amdgcn_make_buffer_rsrc(ws + WS_QX, 0, (int)(4 * WS_QKV_SZ + 2 * WS_KM_SZ), 0x00020000);
  const __amdgpu_buffer_rsrc_t rst = __builtin_amdgcn_make_buffer_rsrc(ws + WS_UT, 0, 2 * SSM_L * SSM_D * 2 + SSM_D * 3072, 0x00020000);
  if ((blockIdx.x & 7) < 4) {
    { LOCAL_IDS
    r_role(lds, si, mem0, Wq, Wk, Wv, Wo, fb, tag, pc0, clen, rsx, rsf, rst, SsmPtrs{A_re, A_im, B_re, B_im, C_re, C_im, log_dt}, tid, lane, w
           ); }
    LOCAL_IDS
    {
      for (int it = 0; it < SPIN_CAP; ++it) {
        if (ldw(rsx, XYDN) >= 255) break;
        asm volatile("s_sleep 8" ::: "memory");
      }
      asm volatile("" ::: "memory");
      asm volatile(".Lkpc1:" ::: "memory");
      unsigned char* im = lds + 65536;
      const __amdgpu_buffer_rsrc_t rso = __builtin_amdgcn_make_buffer_rsrc((void*)out, 0, S_LEN * OUTLD * 2, 0x00020000);
      u32x4 t[8];
#pragma unroll
      for (int i = 0; i < 8; ++i) { const int p = i * 256 + tid; t[i] = __builtin_amdgcn_raw_buffer_load_b128(rst, (p >> 3) * 16384 + (p & 7) * 16, SSM_L * SSM_D * 2 + si * 128, 16); }
#pragma unroll
      for (int i = 0; i < 8; ++i) {
        const int p = i * 256 + tid, un = p >> 3, rp = p & 7;
        *(u32x2*)(im + (2 * rp) * 2064 + un * 8) = (u32x2){t[i][0], t[i][1]};
        *(u32x2*)(im + (2 * rp + 1) * 2064 + un * 8) = (u32x2){t[i][2], t[i][3]};
      }
      LBAR();
#pragma unroll
      for (int i = 0; i < 8; ++i) {
        const int p = i * 256 + tid;
        __builtin_amdgcn_raw_buffer_store_b128(*(const u32x4*)(im + (p >> 7) * 2064 + (p & 127) * 16), rso, ((p >> 7) * OUTLD + DM + (p & 127) * 8) * 2, si * 16 * OUTLD * 2, YAUX);
      }
    }
  } else {
    const int g = blockIdx.x & 3, li = blockIdx.x >> 3;
    const int xi = g == 0 ? (li < 16 ? li : -1) : (li < 26 + (g < 3) ? 16 + 27 * (g - 1) + li : -1);
    const int ei = g == 0 ? li - 16 : 16 + 5 * (g - 1) + (31 - li);
    if (xi >= 0) {
      LOCAL_IDS
      const int head = xi & 31, mi = xi >> 5;
      u32* prog = (u32*)(xb + PGO_X + xi * 128);
      if (mi == 1) x_role<true>(lds, x, Wv, FO_V, head, rsf, prog, tag, lane, w);
      else x_role<false>(lds, x, mi == 0 ? Wk : Wq, mi == 0 ? FO_K : FO_Q, head, rsf, prog, tag, lane, w);
    } else {
      LOCAL_IDS
      u32 acc = 0;
#pragma unroll
      for (int i = 0; i < 4; ++i) acc += *(const u32*)(x + (size_t)((i * 32 + ei) * 256 + tid) * 64);
#define UV 0
#define WWARM(n, first) {    \
        const __amdgpu_buffer_rsrc_t rwo = __builtin_amdgcn_make_buffer_rsrc((void*)Wo, 0, DM * HD * 2, 0x00020000); \
        const int ldsb = (int)(size_t)(LAS unsigned char*)lds; \
        _Pragma("unroll 1") for (int c = 0; c < (n); ++c) { dma16(rwo, lane * 16, ((first) + c) * 16384, 1024, ldsb + w * 32768 + (c & 1) * 16384); waitvm<32>(); } }
      if (UV == 0 || (UV == 1 && ei < 16)) WWARM(4, (ei * 4 + w) * 4)
      if (UV == 2 && ei < 16) WWARM(8, (ei * 4 + w) * 8)
      if (ei == 0 && tid < 16) acc += *(const u32*)(Dskip + tid * 64);
      asm volatile("" :: "v"(acc));
      if (ei >= 16) {
      { const u32 t0 = rt32(); for (int i = 0; i < 1000 && (int)(rt32() - t0) < UDELAY; ++i) asm volatile("s_sleep 16" ::: "memory"); }
      asm volatile("s_waitcnt vmcnt(0)\n\ts_barrier" ::: "memory");
      for (int ps = 0; ps < 2; ++ps) {
      const int r0 = ((ei - 16) * 2 + ps) * 64;
#pragma unroll
      for (int hb = 0; hb < 2; ++hb) {
        u32x4 t[16];
#pragma unroll
        for (int i = 0; i < 16; ++i) t[i] = *(const u32x4*)(u + (size_t)r0 * SSM_D + ((hb * 16 + i) * 256 + tid) * 8);
#pragma unroll
        for (int i = 0; i < 16; ++i) { const int p = (hb * 16 + i) * 256 + tid; *(u32x4*)(lds + (p >> 7) * 2064 + (p & 127) * 16) = t[i]; }
      }
      LBAR();
      for (int i = 0; i < 32; ++i) {
        const int p = i * 256 + tid, un = p >> 5, rp = p & 31;
        const u32x2 a = *(const u32x2*)(lds + (2 * rp) * 2064 + un * 8), c = *(const u32x2*)(lds + (2 * rp + 1) * 2064 + un * 8);
        __builtin_amdgcn_raw_buffer_store_b128((u32x4){a[0], a[1], c[0], c[1]}, rst, un * 16384 + rp * 16, r0 * 8, 16);
      }
      asm volatile("s_waitcnt vmcnt(0) lgkmcnt(0)\n\ts_barrier" ::: "memory");
      }
      if (UV == 1) { WWARM(4, (ei * 4 + w) * 4) waitvm<0>(); }
      wait_start(rsx, fb, tag, true);
      if (tid == 0) __hip_atomic_fetch_add((u32*)(xb + XUDN), 1u, RLX_AGENT);
      }
    }
    asm volatile("s_waitcnt lgkmcnt(0)\n\ts_barrier" :: "s"(tag) : "memory");
    if (w == 0 && fresh_lane() == 0) __builtin_amdgcn_raw_buffer_store_b32(tag, rsx, 0, PGO_S + si * 128, 16);
    const bool isg = g == 0 ? li >= 16 : (g >= 5 - GNG && li < 16);
    if (xi >= 0 && !(GNOSSM && isg)) {
      LOCAL_IDS
      bool uok = false;
      wait_start(rsx, fb, tag, true);
      while (true) {
        LBAR();
        if (tid == 0) *(u32*)(lds + L_TOTAL - 16) = __hip_atomic_fetch_add((u32*)(xb + XTKT), 1u, RLX_AGENT) + 1u;
        LBAR();
        const int un = __builtin_amdgcn_readfirstlane(*(const u32*)(lds + L_TOTAL - 16));
        if (un >= 256) break;
        ssm_unit(lds, 4 * un, rst, rsx, xb, fb, uok, Dskip, tid, lane, w);
        if (isg) break;
      }
    }
    if (isg) {
      g_role(lds, Wo, out, rsx, rsf, fb, tag, g ? g + GNG - 4 : 0, li & 15, w);
    }
    else {
      const int lane = fresh_lane();
      int sh = (ei - 16) * 4 + w, lim = (NSEG - 4) * NH, wi = sh, dn = 0;
      bool go = xi < 0;
      while (true) {
        if (go) a_role(rsx, rsf, sh, lim, wi, dn, lane, fb, tag);
        if (xi >= 0 && (w >= 2 || go)) break;
        const int t = __builtin_amdgcn_readfirstlane(lane == 0 ? __hip_atomic_fetch_add((u32*)(xb + XTK2), 1u, RLX_AGENT) + 1u : 0u);
        if (t >= 128) break;
        wi = 64 + t; sh = (NSEG - 4) * NH + t; lim = sh + 1; dn = 31; go = true;
      }
    }
  }
}

extern "C" void kernel_launch(void* const* d_in, const int* in_sizes, int n_in, void* d_out, int out_size,
                              void* d_ws, size_t ws_size, hipStream_t stream) {
  const u16* x = (const u16*)d_in[0];
  const u16* mem0 = (const u16*)d_in[1];
  const u16* Wq = (const u16*)d_in[2];
  const u16* Wk = (const u16*)d_in[3];
  const u16* Wv = (const u16*)d_in[4];
  const u16* Wo = (const u16*)d_in[5];
  const u16* u = (const u16*)d_in[7];
  const u16* A_re = (const u16*)d_in[8];
  const u16* A_im = (const u16*)d_in[9];
  const u16* B_re = (const u16*)d_in[10];
  const u16* B_im = (const u16*)d_in[11];
  const u16* C_re = (const u16*)d_in[12];
  const u16* C_im = (const u16*)d_in[13];
  const u16* log_dt = (const u16*)d_in[14];
  const u16* Dskip = (const u16*)d_in[15];
  u16* out = (u16*)d_out;
  char* ws = (char*)d_ws;

  main_kernel<<<256, 256, 0, stream>>>(x, mem0, Wq, Wk, Wv, Wo, (unsigned char*)ws, u, A_re, A_im, B_re, B_im, C_re, C_im, log_dt, Dskip, out
      );
}
```

```cpp
#include <hip/hip_runtime.h>
#include <hip/hip_bf16.h>
#include <stdint.h>

#define S_LEN 2048
#define DM 1024
#define NH 32
#define DH 128
#define HD 4096
#define MEM 8
#define SEG 32
#define NSEG 64
#define LTOK 40
#define SSM_L 2048
#define SSM_D 1024
#define SSM_N 128
#define OUTLD 2048

typedef unsigned short u16;
typedef unsigned int u32;
typedef unsigned long long u64;
typedef __attribute__((ext_vector_type(8))) short bf16x8;
typedef __attribute__((ext_vector_type(4))) float f32x4;
typedef __attribute__((ext_vector_type(4))) unsigned int u32x4;
typedef __attribute__((ext_vector_type(2))) unsigned int u32x2;
typedef __bf16 bf16x2_t __attribute__((ext_vector_type(2)));
typedef short v4i16_t __attribute__((ext_vector_type(4)));

#define SENT 0xFFFFFFFFu
#define RLX_AGENT __ATOMIC_RELAXED, __HIP_MEMORY_SCOPE_AGENT

__device__ __forceinline__ float bf2f(u16 v) { return __uint_as_float(((u32)v) << 16); }
__device__ __forceinline__ u16 f2bf(float f) {
  u32 u = __float_as_uint(f);
  u32 r = (u + 0x7fffu + ((u >> 16) & 1u)) >> 16;
  if ((u & 0x7fffffffu) > 0x7f800000u) r = 0x7fc0u;
  return (u16)r;
}
__device__ __forceinline__ float rbf(float f) { return bf2f(f2bf(f)); }

#define TR 8
#define TP 184
#define TDB 65
#define TDC 111
#define NR1 4
#define XPAGE (2 << 20)
#define XRING 3
#define XLP(p, L) ((((L) >> 13) << 15) + ((p) << 14) + ((L) & 8191))
#define XO_R3 131072
#define XR1A(x, L) XLP((x) & 1, ((x) >> 1) * 16384 + (L))
#define XR3A(rg, L) (XO_R3 + XLP(rg, L))
#define XSB(s) (((s) % XRING) == 2 ? 1048576 : ((s) % XRING) * 262144)
#define XR2B 524288
#define XR2S 1048576
#define XR2(j) (((((j) & 31) + (((j) >> 6) << 5)) / 6 << 15) + (((((j) >> 5) & 1) ^ 1) << 14) + ((((j) & 31) + (((j) >> 6) << 5)) % 6) * 1280)
#define XSYN (1048576 + 262144)
#define PGO_R (XSYN + 32768)
#define XCAL (PGO_R + 32768)
static_assert(XCAL + 16384 <= XPAGE, "exchange page");
#define WS_X 0ull
#define WS_X_SZ ((u64)2 * XPAGE)
#define WS_F (WS_X + WS_X_SZ)
#define XEPO (fb)
#define XSTA (fb + 128)
#define PGO_X (fb + 256)
#define PGO_Y (PGO_X + 96 * 128)
#define PGO_S (PGO_Y + 128 * 128)
#define PGO_D (PGO_S + 128 * 128)
#define PGO_A (PGO_D + 128 * 128)
#define XTKT (PGO_A + 192 * 128)
#define XUDN (XTKT + 128)
#define XYDN (XUDN + 128)
#define XTK2 (XYDN + 128)
#define WS_F_SZ ((u64)(2 + 96 + 128 + 128 + 128 + 192 + 8) * 128)
#define WS_XCH_END (WS_F + 131072)
#define WS_QX (WS_XCH_END)
#define WS_QKV_SZ ((u64)S_LEN * HD * 2)
#define WS_KX (WS_QX + WS_QKV_SZ)
#define WS_VX (WS_KX + WS_QKV_SZ)
#define WS_OX (WS_VX + WS_QKV_SZ)
#define WS_KM (WS_OX + WS_QKV_SZ)
#define WS_KM_SZ ((u64)NSEG * 8 * HD * 2)
#define WS_VM (WS_KM + WS_KM_SZ)
#define WS_UT (WS_VM + WS_KM_SZ)
#define WS_YT (WS_UT + (u64)SSM_L * SSM_D * 2)
#define WS_KD (WS_YT + (u64)SSM_L * SSM_D * 2)
#define WS_TOTAL (WS_KD + (u64)SSM_D * 3072)

#define SPIN_CAP 400000
#define TRY_CAP 20000
#define LAS __attribute__((address_space(3)))
#define LBAR() asm volatile("s_waitcnt lgkmcnt(0)\n\ts_barrier" ::: "memory")
__device__ __forceinline__ u32 max4u(u32x4 v) { return max(max(v[0], v[1]), max(v[2], v[3])); }
typedef float f32x2_t __attribute__((ext_vector_type(2)));
__device__ __forceinline__ u32 pack2(float a, float b) {
  f32x2_t v = {a, b};
  bf16x2_t r = __builtin_convertvector(v, bf16x2_t);
  return __builtin_bit_cast(u32, r);
}
__device__ __forceinline__ float rbf_hw(float a) { return __uint_as_float(pack2(a, a) & 0xffff0000u); }
__device__ __forceinline__ u32 nosent(u32 v) { return min(v, 0xFFFFFFFEu); }
template <int N> __device__ __forceinline__ void waitvm() { asm volatile("s_waitcnt vmcnt(%0)" ::"n"(N) : "memory"); }
typedef __attribute__((ext_vector_type(16))) float f32x16;
__device__ __forceinline__ u32 split_hl(float kv) {
  const u32 h = pack2(kv, kv) & 0xffffu;
  const float r1 = kv - __uint_as_float(h << 16);
  return h | (pack2(r1, r1) & 0xffff0000u);
}
__device__ __forceinline__ int kidx(int idx) {
  const int sh = idx < 256 ? 0 : (idx < 512 ? 1 : (idx < 1024 ? 2 : 3));
  const int q = sh ? ((idx + (1 << (sh - 1)) - 1 + ((idx >> sh) & 1)) >> sh) : idx;
  return q + 128 * sh;
}

#define FO_Q 0
#define FO_K ((int)WS_QKV_SZ)
#define FO_V ((int)(2 * WS_QKV_SZ))
#define FO_O ((int)(3 * WS_QKV_SZ))
#define FO_KM ((int)(4 * WS_QKV_SZ))
#define FO_VM ((int)(4 * WS_QKV_SZ + WS_KM_SZ))
__device__ __forceinline__ u32x4 wait_start(__amdgpu_buffer_rsrc_t rsx, int fb, u32 tag, bool slow) {
  u32x4 st;
  for (int it = 0; it < SPIN_CAP; ++it) {
    asm volatile("" ::: "memory");
    st = __builtin_amdgcn_raw_buffer_load_b128(rsx, 0, XSTA, 16);
    if ((u32)__builtin_amdgcn_readfirstlane(st[3]) == tag) break;
    if (slow) asm volatile("s_sleep 20" ::: "memory"); else asm volatile("s_sleep 3" ::: "memory");
  }
  asm volatile("" ::: "memory");
  return st;
}
__device__ __forceinline__ int xprog(u32 v, u32 tag) { return v - tag <= 80u ? (int)(v - tag) : -1; }
__device__ __forceinline__ float xmax4(float m) {
  { auto r = __builtin_amdgcn_permlane16_swap(__float_as_uint(m), __float_as_uint(m), false, false); m = __builtin_fmaxf(__uint_as_float(r[0]), __uint_as_float(r[1])); }
  { auto r = __builtin_amdgcn_permlane32_swap(__float_as_uint(m), __float_as_uint(m), false, false); m = __builtin_fmaxf(__uint_as_float(r[0]), __uint_as_float(r[1])); }
  return m;
}
__device__ __forceinline__ float xsum4(float m) {
  { auto r = __builtin_amdgcn_permlane16_swap(__float_as_uint(m), __float_as_uint(m), false, false); m = __uint_as_float(r[0]) + __uint_as_float(r[1]); }
  { auto r = __builtin_amdgcn_permlane32_swap(__float_as_uint(m), __float_as_uint(m), false, false); m = __uint_as_float(r[0]) + __uint_as_float(r[1]); }
  return m;
}
#define MF16(a, b, c) __builtin_amdgcn_mfma_f32_16x16x32_bf16(__builtin_bit_cast(bf16x8, a), __builtin_bit_cast(bf16x8, b), c, 0, 0, 0)
__device__ __forceinline__ f32x4 rnd2(f32x4 v, float sc) {
  const u32 a = pack2(v[0], v[1]), b = pack2(v[2], v[3]);
  const u32 c = pack2(__uint_as_float(a << 16) * sc, __uint_as_float(a & 0xffff0000u) * sc), d = pack2(__uint_as_float(b << 16) * sc, __uint_as_float(b & 0xffff0000u) * sc);
  return (f32x4){__uint_as_float(c << 16), __uint_as_float(c & 0xffff0000u), __uint_as_float(d << 16), __uint_as_float(d & 0xffff0000u)};
}
struct XaOps { u32x4 q[2][4], k[2][4], v[8]; };
__device__ __forceinline__ void xa_load(XaOps& o, __amdgpu_buffer_rsrc_t rsf, int sh, int lane) {
#pragma unroll
  for (int i = 0; i < 8; ++i) {
    o.q[i >> 2][i & 3] = __builtin_amdgcn_raw_buffer_load_b128(rsf, lane * 16, FO_Q + (sh * 8 + i) * 1024, 16);
    o.k[i >> 2][i & 3] = __builtin_amdgcn_raw_buffer_load_b128(rsf, lane * 16, FO_K + (sh * 8 + i) * 1024, 16);
  }
#pragma unroll
  for (int dt = 0; dt < 8; ++dt) o.v[dt] = __builtin_amdgcn_raw_buffer_load_b128(rsf, lane * 16, FO_V + (sh * 8 + dt) * 1024, 16);
}
__device__ __forceinline__ void xa_compute(const XaOps& o, const u32x4 (&km)[4], const u32x2 (&vm)[8], __amdgpu_buffer_rsrc_t rsf, int sh, int lane) {
  const int lr = lane & 15, lg = lane >> 4;
  const float scale = 0.08838834764831845f, L2E = 1.4426950408889634f, NEG = -1000000000.0f;
  const f32x4 z = (f32x4){0, 0, 0, 0};
  f32x4 s00 = z, s0m = z, s10 = z, s11 = z, s1m = z;
#pragma unroll
  for (int ks = 0; ks < 4; ++ks) { s00 = MF16(o.k[0][ks], o.q[0][ks], s00); s10 = MF16(o.k[0][ks], o.q[1][ks], s10); s11 = MF16(o.k[1][ks], o.q[1][ks], s11); }
#pragma unroll
  for (int ks = 0; ks < 4; ++ks) { s0m = MF16(km[ks], o.q[0][ks], s0m); s1m = MF16(km[ks], o.q[1][ks], s1m); }
  s00 = rnd2(s00, scale); s10 = rnd2(s10, scale); s11 = rnd2(s11, scale); s0m = rnd2(s0m, scale); s1m = rnd2(s1m, scale);
  asm volatile("" : "+v"(s00), "+v"(s11), "+v"(s0m), "+v"(s1m));
#pragma unroll
  for (int e = 0; e < 4; ++e) {
    const bool msk = 4 * lg + e > lr;
    s00[e] = msk ? NEG : s00[e]; s11[e] = msk ? NEG : s11[e];
    s0m[e] = lg >= 2 ? -INFINITY : s0m[e]; s1m[e] = lg >= 2 ? -INFINITY : s1m[e];
  }
  float m0 = NEG, m1 = -INFINITY;
#pragma unroll
  for (int e = 0; e < 4; ++e) { m0 = __builtin_fmaxf(m0, __builtin_fmaxf(s00[e], s0m[e])); m1 = __builtin_fmaxf(__builtin_fmaxf(m1, s10[e]), __builtin_fmaxf(s11[e], s1m[e])); }
  m0 = xmax4(m0); m1 = xmax4(m1);
  float t0 = 0.f, t1 = 0.f;
#pragma unroll
  for (int e = 0; e < 4; ++e) {
    s00[e] = __builtin_amdgcn_exp2f((s00[e] - m0) * L2E); s0m[e] = __builtin_amdgcn_exp2f((s0m[e] - m0) * L2E);
    s10[e] = __builtin_amdgcn_exp2f((s10[e] - m1) * L2E); s11[e] = __builtin_amdgcn_exp2f((s11[e] - m1) * L2E); s1m[e] = __builtin_amdgcn_exp2f((s1m[e] - m1) * L2E);
    t0 += s00[e] + s0m[e]; t1 += (s10[e] + s11[e]) + s1m[e];
  }
  const float em = __builtin_amdgcn_exp2f((NEG - m0) * L2E);
  t0 += 4.f * em;
  const float r0 = __builtin_amdgcn_rcpf(xsum4(t0)), r1 = __builtin_amdgcn_rcpf(xsum4(t1));
  s00 *= r0; s0m *= r0; s10 *= r1; s11 *= r1; s1m *= r1;
  const u32x4 p0 = (u32x4){pack2(s00[0], s00[1]), pack2(s00[2], s00[3]), pack2(s0m[0], s0m[1]), pack2(s0m[2], s0m[3])};
  const u32x4 p1 = (u32x4){pack2(s10[0], s10[1]), pack2(s10[2], s10[3]), pack2(s11[0], s11[1]), pack2(s11[2], s11[3])};
  const u32x4 pm = (u32x4){0u, 0u, pack2(s1m[0], s1m[1]), pack2(s1m[2], s1m[3])};
  const u32 pe = pack2(em * r0, em * r0);
  const u32x4 px = (u32x4){0u, 0u, pe, pe};
#pragma unroll
  for (int pr = 0; pr < 4; ++pr) {
    f32x4 o0[2], o1[2];
#pragma unroll
    for (int t = 0; t < 2; ++t) {
      const int dt = 2 * pr + t;
      const u32x4 a0 = (u32x4){o.v[dt][0], o.v[dt][1], vm[dt][0], vm[dt][1]};
      o0[t] = MF16(a0, p0, z);
      o1[t] = MF16(o.v[dt], p1, z);
      o0[t] = MF16(o.v[dt], px, o0[t]);
      o1[t] = MF16(a0, pm, o1[t]);
    }
    const int ob = FO_O + ((sh >> 5) * 256 + (sh & 31) * 4 + pr) * 1024;
    __builtin_amdgcn_raw_buffer_store_b128((u32x4){pack2(o0[0][0], o0[0][1]), pack2(o0[0][2], o0[0][3]), pack2(o0[1][0], o0[1][1]), pack2(o0[1][2], o0[1][3])}, rsf, lane * 16, ob, 16);
    __builtin_amdgcn_raw_buffer_store_b128((u32x4){pack2(o1[0][0], o1[0][1]), pack2(o1[0][2], o1[0][3]), pack2(o1[1][0], o1[1][1]), pack2(o1[1][2], o1[1][3])}, rsf, lane * 16, ob + 131072, 16);
  }
}
#define NAW 64
__device__ __forceinline__ void a_role(__amdgpu_buffer_rsrc_t rsx, __amdgpu_buffer_rsrc_t rsf, int sh, int lim, int wi, int done, int lane, int fb, u32 tag) {
  const int lr = lane & 15, lg = lane >> 4;
  const int okm = (lg * 8 + (lr & 7)) * 16, ovm = lg < 2 ? (lg * 16 + lr) * 8 : 0x7ffffff0;
  int rp = 0, xp = -1, spin = 0, pub = done, miss = 0, tp = 200;
  u32 tseen = 0, tq0 = 0, tq1 = 0, tq2 = 0, tq3 = 0; (void)tq0; (void)tq1; (void)tq2; (void)tq3;
#define XA_WAITX(s_)                                                                  \
  while (xp <= ((s_) >> 5)) {                                                                                          \
    asm volatile("" ::: "memory");                                                                                     \
    int pv = min(xprog(__builtin_amdgcn_raw_buffer_load_b32(rsx, lane * 128, PGO_X, 16), tag), xprog(__builtin_amdgcn_raw_buffer_load_b32(rsx, (64 + (lane & 31)) * 128, PGO_X, 16), tag)); \
    _Pragma("unroll") for (int o_ = 1; o_ < 64; o_ *= 2) pv = min(pv, __shfl_xor(pv, o_));                             \
    xp = __builtin_amdgcn_readfirstlane(pv);                                                                           \
    if (xp > ((s_) >> 5) || ++spin > SPIN_CAP) break;                                                                  \
    asm volatile("s_sleep 16" ::: "memory");                                                                           \
  }
#define XA_PUB() if (pub != done) { pub = done; asm volatile("s_nop 4\n\tbuffer_store_dword %0, %1, %2, %3 offen sc1" :: "v"(done), "v"(lane ? 0x7ffffff0 : 0), "s"(rsx), "s"(PGO_A + wi * 128) : "memory"); }
#define XA_STEP(CUR, NXT)                                                                                              \
  {                                                                                                                    \
    const int seg = sh >> 5, nx = sh + NAW;                                                                            \
    XA_WAITX(min(nx, lim - 1))                                                                                         \
    if (rp <= seg) {                         \
      while (true) {                                                                                                   \
        int want = min(seg + 1, NSEG - 1);                                                                             \
        if (tseen) {                                                                                                   \
          const u32 due = tseen + (u32)(want - rp) * tp - (want + 1 < NSEG ? 0 : XA_LAST) + (seg + 1 < NSEG ? 0 : XA_END); \
          if ((int)(due - (u32)__builtin_amdgcn_s_memrealtime()) > 0 || miss) { asm volatile("s_waitcnt vmcnt(0)" ::: "memory"); XA_PUB() } \
          for (int i_ = 0; i_ < 4000 && (int)(due - (u32)__builtin_amdgcn_s_memrealtime()) > 0; ++i_) asm volatile("s_sleep 2" ::: "memory"); \
          if (!miss) want = min(max(want, rp + max((int)((u32)__builtin_amdgcn_s_memrealtime() - tseen), 0) / tp), NSEG - 1); \
        }                                                                                                              \
        XA_DBG4                                                                                                        \
        asm volatile("" ::: "memory");                                                                                 \
        if (seg + 1 < NSEG) {                                                                                          \
          const int r_ = __builtin_amdgcn_readfirstlane(__builtin_amdgcn_raw_buffer_load_b32(rsx, 0, pgr + want * 128, 16)); \
          if (r_ >= 0) { const u32 n_ = (u32)__builtin_amdgcn_s_memrealtime(); tseen = n_ - ((n_ - (r_ >> 16)) & 0x7fffu) + XA_LAG; tp = max(r_ & 1023, 1); rp = want; miss = 0; break; } \
        } else if (__ballot(__builtin_amdgcn_raw_buffer_load_b32(rsx, ((sh & 31) * 4 + (lane & 3)) * 128, PGO_D, 16) == SENT) == 0) break; \
        if (++spin > SPIN_CAP) break;                                                                                  \
        if (miss++ && seg + 2 < NSEG) asm volatile("s_sleep 20" ::: "memory");                                                         \
      }                                                                                                                \
    }                                                                                                                  \
    asm volatile("" ::: "memory");                                                                                     \
    XA_DBG2                                                                                                            \
    u32x4 km[4]; u32x2 vm[8];                                                                                          \
    _Pragma("unroll") for (int i = 0; i < 4; ++i) km[i] = __builtin_amdgcn_raw_buffer_load_b128(rsf, okm, FO_KM + (sh * 4 + i) * 512, 16); \
    _Pragma("unroll") for (int i = 0; i < 8; ++i) vm[i] = __builtin_amdgcn_raw_buffer_load_b64(rsf, ovm, FO_VM + (sh * 8 + i) * 256, 16);  \
                              \
    const int pk_ = min(rp + max((int)((u32)__builtin_amdgcn_s_memrealtime() - tseen), 0) / tp, NSEG - 1);              \
    const u32 pv_ = __builtin_amdgcn_raw_buffer_load_b32(rsx, 0, pgr + pk_ * 128, 16);                                 \
    asm volatile("" ::: "memory");                                                                                     \
    __builtin_amdgcn_s_waitcnt(0x0F70);                                                                   \
    asm volatile("" ::: "memory");                                                                                     \
    { const int r_ = __builtin_amdgcn_readfirstlane(pv_);                                                              \
      if (r_ >= 0) { const u32 n_ = (u32)__builtin_amdgcn_s_memrealtime(); tseen = n_ - ((n_ - (r_ >> 16)) & 0x7fffu) + XA_LAG; tp = max(r_ & 1023, 1); rp = pk_; } } \
    XA_PUB()                                   \
    if (nx < lim) xa_load(NXT, rsf, nx, lane);                                                                         \
    XA_DBG3                                                                                                            \
    __builtin_amdgcn_sched_barrier(0);                                                                                 \
    xa_compute(CUR, km, vm, rsf, sh, lane);                                                                            \
    ++done;                                                                                                            \
    XA_DBG                                                                                                             \
    sh = nx;                                                                                                           \
    if (sh >= lim) break;                                                                                              \
  }
#define XA_LAST 24
#define XA_END 0
#define XA_LAG 30
#define XA_DBG
#define XA_DBG2
#define XA_DBG3
#define XA_DBG4
  const int pgr = PGO_R + (__builtin_amdgcn_readfirstlane(wait_start(rsx, fb, tag, true)[2]) & 8192);
  XaOps A, B;
  if (sh >= NAW) {
    const int r_ = __builtin_amdgcn_readfirstlane(__builtin_amdgcn_raw_buffer_load_b32(rsx, 0, pgr + 40 * 128, 16));
    if (r_ >= 0) { const u32 n_ = (u32)__builtin_amdgcn_s_memrealtime(); tseen = n_ - ((n_ - (r_ >> 16)) & 0x7fffu) + XA_LAG; tp = max(r_ & 1023, 1); rp = 40; }
  }
  XA_WAITX(sh)
  asm volatile("" ::: "memory");
  xa_load(A, rsf, sh, lane);
  while (true) { XA_STEP(A, B) XA_STEP(B, A) }
  asm volatile("s_waitcnt vmcnt(0)" ::: "memory");
  XA_PUB()
}

typedef __attribute__((ext_vector_type(32))) unsigned int u32x32;
typedef __attribute__((ext_vector_type(16))) unsigned int u32x16;
typedef __attribute__((ext_vector_type(8))) unsigned int u32x8;
#define STR2(x) #x
#define STR(x) STR2(x)
#define RNG(f, b, n) f "[" STR(b) ":" STR((b) + (n)-1) "]"
#define REG(b) "v[" STR(b) "]"
#define LR_RED 16384
#define LR_FRG 24576
#define LR_CTL LR_RED
#define LR_STG 32768
#define L_RSTAMP LR_STG
#define STAMPV(i, v)
#define EXF (NH * 8 * 1024)
#define EXK 65536
#define OOR 0x7ffffff0

__device__ __forceinline__ u32 lds_word(int addr) {
  u32 v;
  asm volatile("ds_read_b32 %0, %1\n\ts_waitcnt lgkmcnt(0)" : "=v"(v) : "v"(addr) : "memory");
  return __builtin_amdgcn_readfirstlane(v);
}
__device__ __forceinline__ int fresh_lane() {
  int l;
  asm volatile("v_mbcnt_lo_u32_b32 %0, -1, 0\n\tv_mbcnt_hi_u32_b32 %0, -1, %0" : "=v"(l));
  return l;
}
__device__ __forceinline__ u32 rt32() { return (u32)__builtin_amdgcn_s_memrealtime(); }
#define SYO(s) (XSYN + flip + (s) * 64)
#define TKS 0.85f
#define TLB 1
#define TLC 1
#define TLP 2
#define KG0 0
#define CLS0 0u
#define APC 5
#define ADL 12
#define CALN 2
#define TE1 4
#define TE2 3
#define TE3 2
#define XTH 3
#define TS3 42
#define TE0 60
#define TD0 100
#define TD 120
#define PHASE 1
#define XSK 0
#define CLKS(R, X) WLG(2) "s_sub_u32 s49, s50, " R "\n\t" "s_cmp_le_i32 s49, " STR(3 * PHASE) "\n\t" "s_cbranch_scc1 " X "\n\t"
#define CLKW(L, OFF) WLG(0) "s_memrealtime s[72:73]\n\t" "s_add_u32 s50, s43, s44\n\t" "s_add_u32 s50, s50, " OFF "\n\t" "s_nop 1\n\t" "s_memrealtime s[74:75]\n\t" "s_nop 3\n" \
                     L ":\n\t" "s_memrealtime s[76:77]\n\t" CLKS("s72", L "x") "s_memrealtime s[72:73]\n\t" CLKS("s74", L "x")     \
                     "s_memrealtime s[74:75]\n\t" CLKS("s76", L "x") "s_branch " L "\n" \
                     L "x:\n\t" CLKP(L)
#define CLKP(L) "s_cmp_lt_i32 s49, 1\n\t" "s_cbranch_scc1 " L "y\n\t" "s_nop " STR(PHN) "\n\t" "s_cmp_lt_i32 s49, 2\n\t" "s_cbranch_scc1 " L "y\n\t" "s_nop " STR(PHN) "\n\t" \
                "s_cmp_lt_i32 s49, 3\n\t" "s_cbranch_scc1 " L "y\n\t" "s_nop " STR(PHN) "\n\t" "s_nop 0\n\t" "s_branch " L "y\n\t" "s_nop 0\n" L "y:\n\t"
#define PHN 3
#define CLKL(L, OFF) L ":\n\t" "s_memrealtime vcc\n\t" "ds_read_b32 v62, v38\n\t" WLG(0) "v_readfirstlane_b32 s49, v62\n\t" "s_max_u32 s44, s44, s49\n\t" \
                     "s_add_u32 s50, s43, s44\n\t" "s_add_u32 s50, s50, " OFF "\n\t" "s_sub_u32 s49, s50, vcc_lo\n\t" "s_cmp_le_i32 s49, 16\n\t" "s_cbranch_scc1 " L "z\n\t" \
                     CLKSL "s_branch " L "\n" L "z:\n\t"
#define CLKSL "s_sleep 1\n\t"
#define TS_(n) "s_memtime s[80+2*" #n ":81+2*" #n "]\n\t"
#define TSA(n)
#define TSB(n)
#define TSW(n)
#define TVM 16
template <int NTL, int KS>
__device__ __forceinline__ void tr_chunk(const u16* S, int lr, int lg, u32 (&r)[NTL][KS][8]) {
  const int q = lr >> 2, p = lr & 3;
#pragma unroll
  for (int t = 0; t < NTL; ++t)
#pragma unroll
    for (int i = 0; i < KS; ++i)
#pragma unroll
      for (int kb = 0; kb < 2; ++kb) {
        const u16* base = S + (64 * i + 32 * kb + 16 * (lg >> 1) + 4 * (lg & 1) + (q & 1) + 8 * (q >> 1)) * (16 * NTL) + 16 * t + 4 * p;
        const u32x2 t0 = __builtin_bit_cast(u32x2, __builtin_amdgcn_ds_read_tr16_b64_v4i16((LAS v4i16_t*)(base)));
        const u32x2 t1 = __builtin_bit_cast(u32x2, __builtin_amdgcn_ds_read_tr16_b64_v4i16((LAS v4i16_t*)(base + 2 * 16 * NTL)));
        r[t][i][4 * kb + 0] = t0[0]; r[t][i][4 * kb + 1] = t0[1]; r[t][i][4 * kb + 2] = t1[0]; r[t][i][4 * kb + 3] = t1[1];
      }
}
#define SMF(acc, a, bf, bi) "v_smfmac_f32_16x16x64_bf16 " RNG("v", acc, 4) ", " RNG("v", a, 4) ", " RNG(bf, bi, 8) ", v34\n\t"
#define ZER(r) "v_mov_b64 " RNG("v", r, 2) ", 0\n\t" "v_mov_b64 " RNG("v", (r) + 2, 2) ", 0\n\t"
#define GLD(r, o) "s_xor_b32 s49, s50, " STR(o) "\n\t" "buffer_load_dwordx4 " RNG("v", r, 4) ", v32, %[rs], s49 offen sc1\n\t"
#define GL1(r, o) "s_xor_b32 s49, s50, " STR(o) "\n\t" "buffer_load_dwordx4 " RNG("v", r, 4) ", v33, %[rs], s49 offen sc1\n\t"
#define GL2(r, o) GL1(r, 4096 + (o))
#define MXA0(r) "v_max3_u32 v63, " REG(r) ", " REG((r) + 1) ", " REG((r) + 1) "\n\t"
#define MXA(r) "v_max3_u32 v63, v63, " REG(r) ", " REG((r) + 1) "\n\t"
#define MXB(r) "v_max3_u32 v63, v63, " REG((r) + 2) ", " REG((r) + 3) "\n\t"
#define CMPB "v_cmp_eq_u32 vcc, s54, v63\n\t"
#define PSA(r) "v_pk_add_f32 " RNG("v", r, 2) ", " RNG("v", r, 2) ", " RNG("v", (r) + 2, 2) "\n\t"
#define PSW(r, o) "ds_write_addtid_b32 " REG(r) " offset:" STR(o) "\n\t"
#define RDQ(r, o) "ds_read_b128 " RNG("v", r, 4) ", v37 offset:" STR(o) "\n\t"
#define SQ(t, i) SMF(64 + 4 * (t), 96 + 4 * (i), "a", ((i) * 4 + (t)) * 8)
#define SV(t, i) SMF(24 + 4 * (t), 96 + 4 * (i), "a", 128 + ((i) * 2 + (t)) * 8)
#define SW(q, i) SMF(64 + 4 * (q), 96 + 4 * (i), "v", 128 + ((i) * 2 + (q)) * 8)
#define WVM(n) "s_waitcnt vmcnt(" STR(n) ")\n\t"
#define WLG(n) "s_waitcnt lgkmcnt(" STR(n) ")\n\t"
#define WLGF(n) WLG(n)
#define PACE 7
#define PC "s_nop " STR(PACE) "\n\t"
#define QBLK(i, M0) WVM(5 - (i)) SQ(0, i) M0(96 + 4 * (i)) SQ(1, i) MXB(96 + 4 * (i)) SQ(2, i) SQ(3, i)
#define WBLK(i, M0) WVM(8 - (i)) SW(0, i) M0(96 + 4 * (i)) SW(1, i) MXB(96 + 4 * (i))
#define CL8(a, b, c, d, e, f, g, h) "v" #a, "v" #b, "v" #c, "v" #d, "v" #e, "v" #f, "v" #g, "v" #h
#define CL64 CL8(64, 65, 66, 67, 68, 69, 70, 71)
#define CL72 CL8(72, 73, 74, 75, 76, 77, 78, 79)
#define CL80 CL8(80, 81, 82, 83, 84, 85, 86, 87)
#define CL88 CL8(88, 89, 90, 91, 92, 93, 94, 95)
#define CL96 CL8(96, 97, 98, 99, 100, 101, 102, 103), CL8(104, 105, 106, 107, 108, 109, 110, 111)
#define CL112 CL8(112, 113, 114, 115, 116, 117, 118, 119)
#define CL120 CL8(120, 121, 122, 123, 124, 125, 126, 127)
#define PKA(d, a, b) "v_pk_add_f32 " RNG("v", d, 2) ", " RNG("v", a, 2) ", " RNG("v", b, 2) "\n\t"
#define ARRIVE(o) "s_mov_b64 exec, 1\n\t" "ds_add_rtn_u32 v62, v38, v39 offset:" STR(o) "\n\t" "s_mov_b64 exec, -1\n\t"

#define EXTRA "v_readlane_b32 s69, v35, s45\n\t" "s_add_u32 s66, s56, s69\n\t" "s_add_u32 s67, s57, s69\n\t" "s_add_u32 s67, s67, s69\n\t" "s_add_u32 s68, s66, " STR(TVM) "\n\t"
#define TEX(s) ((s) == 0 ? TE0 : ((s) < 4 ? TE1 : ((s) < 12 ? TE2 : ((s) < TS3 ? TE3 : 0))))
#define R2BASE "s_bitcmp1_b32 s45, 0\n\t" "s_cselect_b32 s71, " STR(XR2S) ", 0\n\t" "s_add_u32 s71, s71, %[r2]\n\t"
#define X4B 0
#define X4C 0
#define STB "buffer_store_dwordx2 v[116:117], v11, %[rs], s36 offen sc1\n"
#define STC "buffer_store_dwordx2 v[80:81], v17, %[rs], s36 offen sc1\n\t"
#define R2TEXT                                                                                                     \
              TSB(0)                                                                                               \
              "buffer_load_dwordx4 v[64:67], v43, %[rs], s71 offen sc1\n\t"                                      \
              "buffer_load_dwordx4 v[68:71], v8, %[rs], s71 offen sc1\n\t"                                       \
              "buffer_load_dwordx4 v[76:79], v9, %[rs], s71 offen sc1\n\t"                                       \
              "buffer_load_dwordx4 v[60:63], v10, %[rs], s71 offen sc1\n\t"                                      \
              "buffer_store_dwordx4 v[72:75], v47, %[rf], s41 offen sc1\n"                                       \
              ".Lvw%=:\n\t"                                                                                        \
              "ds_read_b32 v59, v38 offset:12\n\t"                                                                 \
              "ds_read2_b32 v[112:113], v12 offset0:0 offset1:4\n\t" "ds_read2_b32 v[114:115], v12 offset0:32 offset1:36\n\t" \
              "ds_read2_b32 v[116:117], v13 offset0:0 offset1:4\n\t" "ds_read2_b32 v[118:119], v13 offset0:32 offset1:36\n\t" \
              "ds_read2_b32 v[120:121], v14 offset0:0 offset1:4\n\t" "ds_read2_b32 v[122:123], v14 offset0:32 offset1:36\n\t" \
              "ds_read2_b32 v[124:125], v15 offset0:0 offset1:4\n\t" "ds_read2_b32 v[126:127], v15 offset0:32 offset1:36\n\t" \
              "ds_read_b64 v[96:97], v18 offset:2048\n\t" "ds_read_b64 v[100:101], v18 offset:2056\n\t"      \
              "ds_read_b64 v[104:105], v18 offset:3072\n\t" "ds_read_b64 v[108:109], v18 offset:3080\n\t"    \
              WLG(0)                                                                                               \
              "v_readfirstlane_b32 s49, v59\n\t"                                                                 \
              "s_sub_u32 s49, s49, 4\n\t"                                                                      \
              "s_cmp_lt_i32 s49, s38\n\t"                                                                      \
              "s_cbranch_scc1 .Lvw%=\n\t"                                                                          \
              PKA(112, 112, 116) PKA(114, 114, 118) PKA(120, 120, 124) PKA(122, 122, 126) PKA(112, 112, 120) PKA(114, 114, 122) \
              "v_mov_b32 v59, 0\n\t" "v_mov_b32 v99, 0\n\t" "v_mov_b32 v103, 0\n\t" "v_mov_b32 v107, 0\n\t" "v_mov_b32 v111, 0\n\t" \
              "v_cvt_pk_bf16_f32 v98, v112, v59\n\t" "v_cvt_pk_bf16_f32 v106, v113, v59\n\t" "v_cvt_pk_bf16_f32 v102, v114, v59\n\t" "v_cvt_pk_bf16_f32 v110, v115, v59\n\t" \
              "v_mov_b64 v[112:113], v[80:81]\n\t" "v_mov_b64 v[114:115], v[82:83]\n\t"                            \
              "v_mov_b32_dpp v112, v84 row_ror:8 row_mask:0xf bank_mask:0xc\n\t" "v_mov_b32_dpp v113, v85 row_ror:8 row_mask:0xf bank_mask:0xc\n\t" \
              "v_mov_b32_dpp v114, v86 row_ror:8 row_mask:0xf bank_mask:0xc\n\t" "v_mov_b32_dpp v115, v87 row_ror:8 row_mask:0xf bank_mask:0xc\n\t" \
              "v_mov_b32 v52, 0x3f803f80\n\t" "v_mov_b32 v53, 0x3f803f80\n\t" "v_mov_b32 v54, 0x3f803f80\n\t" "v_mov_b32 v55, 0x3f803f80\n\t" \
              "v_mov_b32 v95, 0x3e0293ee\n\t" "v_mov_b32 v123, 0\n\t" "s_mov_b32 s65, 0\n\t" \
              "v_pk_mul_f32 v[112:113], v[112:113], v[94:95] op_sel:[0,1]\n\t" "v_pk_mul_f32 v[114:115], v[114:115], v[94:95] op_sel:[0,1]\n\t" \
              TSB(1) WVM(4) TSB(2) \
              "v_permlane32_swap_b32 v64, v66\n\t" "v_permlane32_swap_b32 v65, v67\n\t" \
              "v_pk_add_f32 v[92:93], v[64:65], v[66:67]\n\t" \
              WVM(3) SFM(68) \
              "v_permlane16_swap_b32 v92, v93\n\t" \
              "v_add_f32 v94, v92, v93\n\t" \
              WVM(2) "v_mul_f32 v94, v95, v94\n\t" SFM(76) \
              "v_exp_f32 v92, v94\n\t" \
              WVM(1) TSB(3) SFM(60) \
                \
              ".Lex%=:\n\t" \
              "v_exp_f32 v88, v112\n\t" "v_exp_f32 v89, v113\n\t" "v_cvt_pk_bf16_f32 v58, v92, v59\n\t" "v_exp_f32 v90, v114\n\t" "v_exp_f32 v91, v115\n\t" \
              "v_cvt_pk_bf16_f32 v56, v88, v89\n\t" "v_cvt_pk_bf16_f32 v57, v90, v91\n\t" \
              TSB(4) ROR8(122, 58) ROR8(120, 56) \
              "v_mfma_f32_16x16x32_bf16 v[88:91], v[52:55], v[56:59], 0\n\t" \
              ROR8(121, 57) \
              "v_mfma_f32_16x16x32_bf16 v[116:119], v[96:99], v[56:59], 0\n\t" \
              "s_nop 0\n\t" \
              "v_mfma_f32_16x16x32_bf16 v[116:119], v[100:103], v[120:123], v[116:119]\n\t" \
              "v_add_f32_dpp v92, v88, v88 row_ror:8 row_mask:0xf bank_mask:0xf\n\t" "v_subrev_u32 v89, 0x33800000, v92\n\t" \
              "v_mfma_f32_16x16x32_bf16 v[124:127], v[108:111], v[56:59], 0\n\t" \
              "v_rcp_f32 v93, v92\n\t" \
              "v_mfma_f32_16x16x32_bf16 v[124:127], v[104:107], v[120:123], v[124:127]\n\t" \
              TSB(5) "v_cmp_le_u32 vcc, 0x20000000, v89\n\t" "s_nop 0\n\t" \
              "s_cbranch_vccnz .Lmx%=\n\t" \
              OTAIL STB TSB(6)
#define SFM(r) "v_pk_fma_f32 v[112:113], " RNG("v", r, 2) ", v[94:95], v[112:113] op_sel:[0,1,0]\n\t" "v_pk_fma_f32 v[114:115], " RNG("v", (r) + 2, 2) ", v[94:95], v[114:115] op_sel:[0,1,0]\n\t"
#define ROR8(d, s) "v_mov_b32_dpp " REG(d) ", " REG(s) " row_ror:8 row_mask:0xf bank_mask:0xf\n\t"
#define OTAIL "v_pk_mul_f32 v[116:117], v[116:117], v[92:93] op_sel:[0,1]\n\t" "v_pk_mul_f32 v[118:119], v[118:119], v[92:93] op_sel:[0,1]\n\t" \
              "v_cvt_pk_bf16_f32 v116, v116, v117\n\t" "v_cvt_pk_bf16_f32 v117, v118, v119\n\t" \
              "s_mov_b64 exec, s[78:79]\n\t" \
              "v_pk_mul_f32 v[124:125], v[124:125], v[92:93] op_sel:[0,1]\n\t" "v_pk_mul_f32 v[126:127], v[126:127], v[92:93] op_sel:[0,1]\n\t" \
              "v_cvt_pk_bf16_f32 v116, v124, v125\n\t" "v_cvt_pk_bf16_f32 v117, v126, v127\n\t" \
              "s_mov_b64 exec, -1\n\t"
#define R2MAX ".Lmx%=:\n\t" OTAIL \
              "s_cmp_lg_u32 s65, 0\n\t" "s_cbranch_scc1 .Lend%=\n\t" "s_mov_b32 s65, 1\n\t" \
              "v_max3_f32 v93, v112, v113, v94\n\t" \
              "v_max3_f32 v93, v93, v114, v115\n\t" \
              "s_nop 1\n\t" \
              "v_max_f32_dpp v92, v93, v93 row_ror:8 row_mask:0xf bank_mask:0xf\n\t" \
              "v_max_f32_dpp v88, v93, v93 row_ror:8 row_mask:0xf bank_mask:0xf\n\t" \
              "s_nop 1\n\t" \
              "v_permlane16_swap_b32 v92, v88\n\t" \
              "v_max_f32 v93, v92, v88\n\t" "v_max_f32 v92, v92, v88\n\t" \
              "s_nop 1\n\t" \
              "v_permlane32_swap_b32 v93, v92\n\t" \
              "v_max_f32 v93, v93, v92\n\t" \
              "v_sub_f32 v94, v94, v93\n\t" "v_sub_f32 v112, v112, v93\n\t" "v_exp_f32 v92, v94\n\t" "v_sub_f32 v113, v113, v93\n\t" "v_sub_f32 v114, v114, v93\n\t" "v_sub_f32 v115, v115, v93\n\t" \
              "s_branch .Lex%=\n"
#define AV 0
#define VREST
#define PS1(r) "v_add_f32 " REG(r) ", " REG(r) ", " REG((r) + 2) "\n\t"
#define VPOST VREST PSA(24) PSW(24, 2560) PSW(25, 2832) PSA(28) PSW(28, 3200) PSW(29, 3472) ARRIVE(12)
#define R2OUT "=&{v[60:79]}"(pr), "=&{v[116:117]}"(ov)
#define FINEO
#define STEPCHK "2"
#define ROLESV
#define ROLEOUT

__device__ __forceinline__ void clk_edge(u32& cyc, u32& tick) {
  const u32 t = rt32();
  for (int i = 0; i < 64; ++i) { tick = rt32(); if (tick != t) break; }
  cyc = (u32)__builtin_amdgcn_s_memtime();
}
__device__ __forceinline__ float clk_rate(u32 cyc0, u32 tk0) { u32 c, t; clk_edge(c, t); return (float)(c - cyc0) / (float)max(t - tk0, 1u); }
__device__ __forceinline__ void dma16(__amdgpu_buffer_rsrc_t rs, int voff, int soff, int sstep, int ldsa) {
#pragma unroll
  for (int i = 0; i < 16; ++i)
    asm volatile("s_mov_b32 m0, %0\n\ts_nop 3\n\tbuffer_load_dwordx4 %1, %2, %3 offen lds" :: "s"(ldsa + i * 1024), "v"(voff), "s"(rs), "s"(soff ^ (i * sstep)) : "memory");
}

#define KH(t) (192 + 8 * (t))
#define KMF(t, a, b) "v_mfma_f32_16x16x32_bf16 " RNG("v", 80 + 4 * (t), 4) ", " RNG("v", KH(t) + 4 * (a), 4) ", " RNG("v", KH(3) + 4 * (b), 4) ", " RNG("v", 80 + 4 * (t), 4) "\n\t"
#define KX(j) (92 + 4 * ((j) & 1))
#define KA(t, j) "v_pk_mul_f32 " RNG("v", KX(j), 2) ", " RNG("v", 64 + 4 * (j), 2) ", " RNG("v", 112 + ((t) & 2), 2) " op_sel:[0," STR((t) & 1) "] op_sel_hi:[1," STR((t) & 1) "]\n\t"
#define KF(j) "v_fract_f32 " REG(KX(j) + 1) ", " REG(KX(j) + 1) "\n\t"
#define KE(j, N) "v_exp_f32 " REG(KX(j)) ", " REG(KX(j)) "\n\t" "v_cos_f32 " REG(KX(j) + 2) ", " REG(KX(j) + 1) "\n\t" "v_sin_f32 " REG(KX(j) + 3) ", " N REG(KX(j) + 1) "\n\t"
#define KM(j) "v_pk_mul_f32 " RNG("v", KX(j) + 2, 2) ", " RNG("v", KX(j) + 2, 2) ", " RNG("v", KX(j), 2) " op_sel_hi:[1,0]\n\t"
#define KC(j) "v_pk_mul_f32 " RNG("v", KX(j), 2) ", " RNG("v", KX(j) + 2, 2) ", " RNG("v", 66 + 4 * (j), 2) " op_sel_hi:[1,0]\n\t" \
  "v_pk_fma_f32 " RNG("v", KX(j), 2) ", " RNG("v", KX(j) + 2, 2) ", " RNG("v", 66 + 4 * (j), 2) ", " RNG("v", KX(j), 2) " op_sel:[1,1,0] op_sel_hi:[0,1,1] neg_hi:[1,0,0]\n\t"
#define KD(t, j, r) "v_cvt_pk_bf16_f32 " REG(KH(t) + (j)) ", " REG(r) ", " REG((r) + 1) "\n\t" \
  "v_dot2c_f32_bf16 " REG(r) ", s47, " REG(KH(t) + (j)) "\n\t" "v_dot2c_f32_bf16 " REG((r) + 1) ", s48, " REG(KH(t) + (j)) "\n\t"
#define KL(t, j, r) "v_cvt_pk_bf16_f32 " REG(KH(t) + 4 + (j)) ", " REG(r) ", " REG((r) + 1) "\n\t"
#define KDM "s_add_u32 m0, m0, 1024\n\t" "s_add_u32 s43, s43, s42\n\t" "buffer_load_dwordx4 v110, s[36:39], s43 offen lds\n\t"
#define KP(j) "ds_read_b128 " RNG("v", 64 + 4 * (j), 4) ", v111 offset:" STR(16 * (j)) "\n\t"
#define KV(j, L, A) KE(j, "-") L KM(j) KC(j) A KD(3, j, KX(j))
#define KU(t, j, L, A) KE(j, "") L KM(j) A KD(t, j, KX(j) + 2)
#define KSUB                                                                                                        \
  KV(0, WLG(8) "s_add_u32 m0, m0, 1024\n\t", KA(3, 1)) KV(1, KL(3, 0, KX(0)), KA(3, 2))                              \
  "s_xor_b32 s43, s40, s41\n\t" "buffer_load_dwordx4 v110, s[36:39], s43 offen lds\n\t" KV(2, KL(3, 1, KX(1)), KA(3, 3)) \
  KV(3, KL(3, 2, KX(2)), KA(0, 0))                                                                                  \
  KU(0, 0, KL(3, 3, KX(3)), KA(0, 1)) KU(0, 1, KL(0, 0, KX(0) + 2), KA(0, 2)) KDM KU(0, 2, KL(0, 1, KX(1) + 2), KA(0, 3)) \
  KU(0, 3, KL(0, 2, KX(2) + 2), KA(1, 0)) KF(0)                                                                     \
  KU(1, 0, KL(0, 3, KX(3) + 2), KA(1, 1)) KF(1) KMF(0, 1, 0) KU(1, 1, KL(1, 0, KX(0) + 2), KA(1, 2)) KF(2) KDM      \
  KMF(0, 0, 1) KU(1, 2, KL(1, 1, KX(1) + 2), KA(1, 3)) KF(3) KMF(0, 0, 0) KU(1, 3, KL(1, 2, KX(2) + 2), KA(2, 0)) KF(0) KP(0) \
  KU(2, 0, KL(1, 3, KX(3) + 2), KA(2, 1)) KF(1) KP(1) KMF(1, 1, 0) KU(2, 1, KL(2, 0, KX(0) + 2), KA(2, 2)) KF(2) KP(2) KDM \
  KMF(1, 0, 1) KU(2, 2, KL(2, 1, KX(1) + 2), KA(2, 3)) KF(3) KP(3) KMF(1, 0, 0) KU(2, 3, KL(2, 2, KX(2) + 2), WLG(3) KA(3, 0)) \
  KMF(2, 0, 1) "v_add_u32 v111, 256, v111\n\t" "s_lshl2_add_u32 s41, s42, s41\n\t" KL(2, 3, KX(3) + 2)
#define KZERO ZER(80) ZER(84) ZER(88)
#define KCHUNK(PTR, VO, SO, ST, BUF, PF) "s_mov_b64 s[36:37], " PTR "\n\t" "s_mov_b32 m0, %[sc]\n\t" "v_mov_b32 v110, " VO "\n\t" SO "s_mov_b32 s41, 0\n\t" "s_mov_b32 s42, " STR(ST) "\n\t" \
  "buffer_load_dword " PF ", s[36:39], 0 offen lds\n\t" "s_add_u32 m0, %[sl], " STR((BUF) * 16384 - 1024) "\n\t"
#define KCALL "s_call_b64 s[44:45], .Lkg%=\n\t" WVM(16) KMF(2, 0, 0)
#define KTR(F, d, A, o) "ds_read_b64_tr_b16 " RNG(F, d, 2) ", " A " offset:" STR(o) "\n\t"
#define KTQ1(d, o) KTR("a", d, "%[aq]", o) KTR("a", (d) + 2, "%[aq]", (o) + 128) KTR("a", (d) + 4, "%[aq]", (o) + 2048) KTR("a", (d) + 6, "%[aq]", (o) + 2176)
#define KTQ(d, o) KCALL KTQ1(d, o) KMF(2, 1, 0) KTQ1((d) + 8, (o) + 32)
#define KTO1(d, o) KTR("v", d, "%[ao]", o) KTR("v", (d) + 2, "%[ao]", (o) + 64) KTR("v", (d) + 4, "%[ao]", (o) + 1024) KTR("v", (d) + 6, "%[ao]", (o) + 1088)
#define KTO(d, o) KCALL KTO1(d, o) KMF(2, 1, 0) KTO1((d) + 8, (o) + 2048)
#define KOUT(o) "s_add_u32 s43, %[ko], " STR(o) "\n\t"                                                              \
  "buffer_store_dwordx4 v[80:83], %[l16], %[rt], s43 offen sc1\n\t" "buffer_store_dwordx4 v[84:87], %[l16], %[rt], s43 offen offset:1024 sc1\n\t" "s_nop 3\n\t" \
  "buffer_store_dwordx4 v[88:91], %[l16], %[rt], s43 offen offset:2048 sc1\n\t"
struct SsmPtrs { const u16 *A_re, *A_im, *B_re, *B_im, *C_re, *C_im, *log_dt; };
__device__ __forceinline__ void r_role(unsigned char* lds, int b, const u16* __restrict__ mem0, const u16* __restrict__ Wq, const u16* __restrict__ Wk, const u16* __restrict__ Wv,
                                       const u16* __restrict__ Wo, int fb, u32 tag, u64 pc0, int clen, __amdgpu_buffer_rsrc_t rsx, __amdgpu_buffer_rsrc_t rsf,
                                       __amdgpu_buffer_rsrc_t rst, SsmPtrs sp, int tid, int lane, int w
                                       ) {
  const int lr = lane & 15, lg = lane >> 4;
  const int ldsb = (int)(size_t)(LAS unsigned char*)lds;
  const int idx = (lr & 2) ? 0xEEEE : 0x4444;
  int tries = 0;
  u32 tcnt = 0;
  u32 cyc0 = 0, tk0 = 0;
  if (tid < 8) *(u32*)(lds + LR_CTL + tid * 4) = tid == 1 ? rt32() : 0u;
  const int xcd = b & 3, li = b >> 2;
  const int head = xcd * 8 + (li >> 2), ks = li & 3;
  const int vcu = head * 4 + ks;
  const int cg = (xcd >> 1) * 32 + li, rg = xcd & 1;
  const int rot1 = ((li >> 1) + w) & 3, rot3 = ((li >> 2) + 2 * w) & 7;
  const int pz1 = lane < 32 ? XR1A(lane >> 3, cg * 256 + rg * 128 + (lane & 7) * 16) : XR3A(lg & 1, vcu * 256 + lr * 16);
  const int pgo = PGO_X + ((w >> 1) * 32 + head) * 128;
  const int calo = XCAL + (li & 3) * 8192 + ((li >> 2) & 1) * 4096 + ((li >> 3) * 4 + xcd) * 128;
  u32 xp0;
  u32x32 wa[6], wo[4];
  {
    const __amdgpu_buffer_rsrc_t rwq = __builtin_amdgcn_make_buffer_rsrc((void*)Wq, 0, DM * HD * 2, 0x00020000);
    const int vq = (lane >> 2) * (HD * 2) + vcu * 64 + (lane & 3) * 16, sq = (256 * w + (rot1 << 6)) * (HD * 2);
    const int vo = (lane >> 1) * (DM * 2) + cg * 32 + (lane & 1) * 16, so = (1024 * w + (rot3 << 7)) * (DM * 2);
    const int sl = ldsb + LR_STG + w * 32768;
    const int ch0 = (b * 4 + w) * 2;
    u32x2 pin[6];
    u32 ldt;
    {
      const int po = (ch0 * SSM_N + 4 * lane) * 2;
      asm volatile("global_load_dwordx2 %0, %7, %9\n\tglobal_load_dwordx2 %1, %7, %10\n\tglobal_load_dwordx2 %2, %7, %11\n\tglobal_load_dwordx2 %3, %7, %12\n\t"
                   "global_load_dwordx2 %4, %7, %13\n\tglobal_load_dwordx2 %5, %7, %14\n\tglobal_load_ushort %6, %8, %15"
                   : "=&v"(pin[0]), "=&v"(pin[1]), "=&v"(pin[2]), "=&v"(pin[3]), "=&v"(pin[4]), "=&v"(pin[5]), "=&v"(ldt)
                   : "v"(po), "v"((ch0 + (lane >> 5)) * 2), "s"(sp.A_re), "s"(sp.A_im), "s"(sp.B_re), "s"(sp.B_im), "s"(sp.C_re), "s"(sp.C_im), "s"(sp.log_dt) : "memory");
    }
    u32x4 m0v;
    asm volatile("global_load_dwordx4 %0, %1, %2" : "=&v"(m0v) : "v"(((4 * rg + ((lane >> 1) & 3)) * DM + 16 * cg + 8 * (lane & 1)) * 2), "s"(mem0) : "memory");
    const u16* const wp[4] = {Wq, Wk, Wv, Wo};
    int pfo[5];
#pragma unroll
    for (int c = 0; c < 5; ++c) {
      const int lo_ = (int)(size_t)wp[c < 3 ? c : 3], s3 = (384 - lo_) & 1023, s63 = (8064 - lo_) & 8191;
      pfo[c] = c < 3 ? (lane < 16 ? (256 * w + li * 8 + (lane & 7)) * (HD * 2) + xcd * 2048 + (lane >> 3) * 1024 + s3
                                  : (lane < 24 && (s63 >> 11) == xcd ? (256 * w + li * 8 + (lane & 7)) * (HD * 2) + s63 : OOR))
                     : (lane < 16 ? (1024 * w + li * 16 + lane + (((c - 3) * 512) ^ ((((s3 >> 7) + 2 * w) & 4) << 7))) * (DM * 2) + (xcd >> 1) * 1024 + s3 : OOR);
    }
    const int scr = ldsb + LR_RED + 1024 + w * 256;
    asm volatile("s_mov_b32 m0, %0\n\ts_nop 3\n\tbuffer_load_dword %1, %2, 0 offen lds" :: "s"(scr), "v"(pfo[0]), "s"(rwq) : "memory");
#define KP0 16
#pragma unroll
    for (int i = 0; i < KP0; ++i)
      asm volatile("s_mov_b32 m0, %0\n\ts_nop 3\n\tbuffer_load_dwordx4 %1, %2, %3 offen lds" :: "s"(sl + i * 1024), "v"(vq), "s"(rwq), "s"(sq ^ (i * 16 * HD * 2)) : "memory");
    asm volatile("s_waitcnt vmcnt(1+" STR(KP0) ")" : "+v"(pin[0]), "+v"(pin[1]), "+v"(pin[2]), "+v"(pin[3]), "+v"(pin[4]), "+v"(pin[5]), "+v"(ldt), "+v"(m0v)::"memory");
    f32x4* prm = (f32x4*)(lds + w * 4096);
    {
      const float dt = rbf(expf(bf2f((u16)ldt)));
#pragma unroll
      for (int i = 0; i < 4; ++i) {
        float v[6];
#pragma unroll
        for (int j = 0; j < 6; ++j) v[j] = __uint_as_float((i & 1) ? (pin[j][i >> 1] & 0xffff0000u) : (pin[j][i >> 1] << 16));
        const float are = dt * v[0], aim = dt * v[1], br = dt * v[2], bi = dt * v[3];
        const float cr = v[4] * br - v[5] * bi, ci = v[4] * bi + v[5] * br;
        const float rev = aim * 0.15915494309189535f;
        prm[4 * lane + i] = (f32x4){are * 1.4426950408889634f, rev - __builtin_rintf(rev), cr, ci};
      }
    }
#pragma unroll
    for (int i = KP0; i < 16; ++i)
      asm volatile("s_mov_b32 m0, %0\n\ts_nop 3\n\tbuffer_load_dwordx4 %1, %2, %3 offen lds" :: "s"(sl + i * 1024), "v"(vq), "s"(rwq), "s"(sq ^ (i * 16 * HD * 2)) : "memory");
    const f32x4 lv = {(float)(128 * (lr >> 3) + (lr & 7)), (float)(lr < 8 ? 256 + 128 * (lr >> 2) + 2 * (lr & 3) : 512 + 128 * ((lr - 8) >> 1) + 4 * (lr & 1)),
                         (float)(lr <= 8 ? 1024 + 128 * lr : 0), (float)(8 * lr)};
    const int trow = 16 * (lg >> 1) + 4 * (lg & 1) + ((lr >> 2) & 1) + 8 * (lr >> 3);
    asm volatile(
        "s_nop 4\n\t" "s_mov_b32 s46, m0\n\t" "s_mov_b32 s38, " STR(DM * HD * 2) "\n\t" "s_mov_b32 s39, 0x20000\n\t" "s_mov_b32 s47, 0xbf80\n\t" "s_mov_b32 s48, 0xbf800000\n\t"
        WLG(0)
        "ds_read_b128 v[64:67], %[pa]\n\t" "ds_read_b128 v[68:71], %[pa] offset:16\n\t" "ds_read_b128 v[72:75], %[pa] offset:32\n\t" "ds_read_b128 v[76:79], %[pa] offset:48\n\t"
        "v_add_u32 v111, 256, %[pa]\n\t" KZERO
        KCHUNK("%[wk]", "%[vq]", "s_mov_b32 s40, %[sq]\n\t", 16 * HD * 2, 1, "%[p1]")
        WLG(0) KA(3, 0)
        KTQ(0, 0) KTQ(32, 4096) KTQ(64, 8192) KTQ(96, 12288)
        KCHUNK("%[wv]", "%[vq]", "", 16 * HD * 2, 0, "%[p2]")
        KTQ(16, 16384) KTQ(48, 16384 + 4096) KTQ(80, 16384 + 8192) KTQ(112, 16384 + 12288)
        KOUT(0)
        KCHUNK("%[wo]", "%[vo]", "s_mov_b32 s40, %[so]\n\t", 32 * DM * 2, 1, "%[p3]")
        KZERO
        KTQ(128, 0) KTQ(144, 4096) KTQ(160, 8192) KTQ(176, 12288)
        KCHUNK("%[wo]", "%[vo]", "s_xor_b32 s40, s40, " STR(512 * DM * 2) "\n\t", 32 * DM * 2, 0, "%[p4]")
        KTO(128, 16384) KTO(144, 16384 + 4096) KTO(160, 16384 + 8192) KTO(176, 16384 + 12288)
        KOUT(3072)
        "s_branch .Lke%=\n"
        ".Lkg%=:\n\t"
        KSUB
        "s_setpc_b64 s[44:45]\n"
        ".Lke%=:\n\t"
        WLG(0) "s_mov_b32 m0, s46"
        : "=&{a[0:31]}"(wa[0]), "=&{a[32:63]}"(wa[1]), "=&{a[64:95]}"(wa[2]), "=&{a[96:127]}"(wa[3]), "=&{a[128:159]}"(wa[4]), "=&{a[160:191]}"(wa[5]),
          "=&{v[128:159]}"(wo[0]), "=&{v[160:191]}"(wo[1])
        : "{v[112:115]}"(lv), [pa] "v"(ldsb + w * 4096 + lg * 64), [vq] "v"(vq), [vo] "v"(vo),
          [p1] "v"(pfo[1]), [p2] "v"(pfo[2]), [p3] "v"(pfo[3]), [p4] "v"(pfo[4]), [aq] "v"(sl + trow * 64 + (lr & 3) * 8), [ao] "v"(sl + trow * 32 + (lr & 3) * 8), [l16] "v"(lane * 16),
          [wk] "s"(Wk), [wv] "s"(Wv), [wo] "s"(Wo), [sq] "s"(sq), [so] "s"(so), [sl] "s"(sl), [sc] "s"(scr), [rt] "s"(rst), [ko] "s"(2 * SSM_L * SSM_D * 2 + ch0 * 3072)
        : CL64, CL72, CL80, CL88, CL96, CL8(192, 193, 194, 195, 196, 197, 198, 199), CL8(200, 201, 202, 203, 204, 205, 206, 207), CL8(208, 209, 210, 211, 212, 213, 214, 215),
          CL8(216, 217, 218, 219, 220, 221, 222, 223), "s36", "s37", "s38", "s39", "s40", "s41", "s42", "s43", "s44", "s45", "s46", "s47", "s48", "scc", "memory");
    {
      const u32x4 pz = (u32x4){SENT, SENT, SENT, SENT};
      if (w < 3) {
        u32x4 v = pz;
        if (w == 0 && lane < 32) v = (u32x4){nosent(m0v[0]), nosent(m0v[1]), nosent(m0v[2]), nosent(m0v[3])};
#pragma unroll
        for (int hf = 0; hf < 2; ++hf) {
          __builtin_amdgcn_raw_buffer_store_b128(v, rsx, pz1, XSB(w) + hf * 8192, 16);
          __builtin_amdgcn_raw_buffer_store_b128(pz, rsx, XR2(vcu) + lane * 16, XR2B + (w & 1) * XR2S + hf * 8192, 16);
          __builtin_amdgcn_raw_buffer_store_b128(pz, rsx, lane < 16 ? XR2(vcu) + 1024 + lane * 16 : OOR, XR2B + (w & 1) * XR2S + hf * 8192, 16);
        }
      } else {
        __builtin_amdgcn_raw_buffer_store_b128(pz, rsx, lane < 8 ? lane * 16 : OOR, calo, 16);
        if (b < 32) {
          __builtin_amdgcn_raw_buffer_store_b128(pz, rsx, lane * 16, XSYN + b * 2048, 16);
          __builtin_amdgcn_raw_buffer_store_b128(pz, rsx, lane * 16, XSYN + b * 2048 + 1024, 16);
        } else if (b >= 48 && b < 48 + 41) {
          __builtin_amdgcn_raw_buffer_store_b128(pz, rsx, lane * 16, PGO_D + (b - 48) * 1024, 16);
        }
      }
      asm volatile("" ::: "memory");
    }
    xp0 = __builtin_amdgcn_raw_buffer_load_b32(rsx, 0, pgo, 16);
    clk_edge(cyc0, tk0);
    asm volatile("s_waitcnt vmcnt(0)\n\ts_barrier" : "+v"(xp0)::"memory");
#define CLKF() clk_rate(cyc0, tk0)
    if (w == 0) {
      const u64 c0 = __builtin_amdgcn_s_memtime();
      asm volatile("s_waitcnt lgkmcnt(0)" ::: "memory");
      u32 v = __builtin_amdgcn_raw_buffer_load_b32(rsx, 0, calo, 16);
      asm volatile("s_waitcnt vmcnt(0)" : "+v"(v)::"memory");
      const u32 lat = (u32)(__builtin_amdgcn_s_memtime() - c0);
      const float f = CLKF();
      if (lane == 0) __builtin_amdgcn_raw_buffer_store_b128((u32x4){tag, __float_as_uint(f), lat, 0u}, rsx, 0, PGO_Y + b * 128, 16);
      asm volatile("" ::: "memory");
    }
    {
      u32 r[1][8][8];
      tr_chunk<1, 8>((const u16*)(lds + LR_STG + w * 32768), lr, lg, r);
#pragma unroll
      for (int i = 0; i < 8; ++i)
#pragma unroll
        for (int e = 0; e < 8; ++e) { const int o = (8 + i) * 8 + e; wo[o >> 5][o & 31] = r[0][i][e]; }
      asm volatile("s_waitcnt lgkmcnt(0)" ::: "memory");
    }
  }
  const int hf = (lr >> 1) & 1;
  const int voff1 = lg * 256 + (lr >> 3) * 128 + (((lr >> 2) & 1) * 2 + (lr & 1)) * 32 + hf * 16;
  const int voff3 = (lr >> 3) * 512 + (lg >> 1) * 256 + (((lr >> 2) & 1) * 2 + (lr & 1)) * 16 + (lg & 1) * 128 + hf * 64;
  const int ard = ldsb + (2 * (lr >> 3) + (lg >> 1)) * 640 + (lr & 1) * 272 + ((lr >> 1) & 3) * 64 + (lg & 1) * 32;
  const int ctl = ldsb + LR_CTL, one = 1;
  const int r8 = lane & 7, hk = (lane >> 3) & 1;
#define COA 1
#define COB 1
  const int st01 = lr < 8 ? XR2(vcu) + lr * 16 + lg * 128 : OOR, st2 = (lr < 8 && lg >= 2) ? XR2(vcu) + 1024 + lr * 16 + (lg - 2) * 128 : OOR;
  const int ld01 = hk * 512 + r8 * 16 + lg * 128, ld2 = XR2(head * 4 + lg) + 1024 + r8 * 16 + hk * 128;
  int sto = XR3A(r8 >> 2, vcu * 256 + (r8 & 3) * 16 + lg * 64 + hk * 8);
  const int stk = lr >= 8 ? (lg * 8 + (lr - 8)) * 16 : OOR;
  const int lo = X4C ? 2 * (lr & 7) + (lr >> 3) : lr;
  const int arw = 0, arr = ldsb + 3840 + ((lo >> 3) + 2 * ((lo >> 2) & 1)) * 64 + (lo & 3) * 16;
  const int st1 = XR1A(lg, cg * 256 + rg * 128 + lo * 8);
  const int avw = 0;
  const int avc = ldsb + 2560 + (lr >> 3) * 640 + (8 * ((lr >> 2) & 1) + (lr & 3)) * 4;
  const int avr = avc + (lg & 1) * 272 + (lg >> 1) * 64, avf = avc + (lg & 1) * 128 + (lg >> 1) * 16;
  u32x16 kc = {(u32)voff1, (u32)voff3, (u32)idx, (u32)TEX(lane), (u32)(lane < 16 ? XR2(vcu) + 1024 + lane * 16 : OOR), (u32)ard, (u32)ctl, (u32)one, (u32)st01, (u32)st2, (u32)(XR2(vcu) + lane * 16), (u32)ld2, (u32)(lane * 16), (u32)arw, (u32)arr, (u32)stk};
  int o1c = XR1A(xcd, w * 4096) + rot1 * 1024, o3c = XR3A(rg, w * 8192) + rot3 * 1024;
  const int fo = w < 2 ? FO_K + ((head * 2 + w) * 4 + ks) * 1024 : FO_V + (head * 8 + 2 * ks + (w - 2)) * 1024;
  int xp = xprog(__builtin_amdgcn_readfirstlane(xp0), tag);
#define XWAIT(n) for (int it_ = 0; it_ < SPIN_CAP && xp < (n); ++it_) {                                               \
    if (it_) asm volatile("s_sleep 4" ::: "memory");                                                                  \
    xp = xprog(__builtin_amdgcn_readfirstlane(__builtin_amdgcn_raw_buffer_load_b32(rsx, 0, pgo, 16)), tag); asm volatile("" ::: "memory"); }
  XWAIT(1)
  *(u32x4*)(lds + LR_FRG + w * 1024 + lane * 16) = __builtin_amdgcn_raw_buffer_load_b128(rsf, lane * 16, fo, 16);
  const u32 tbase = __builtin_amdgcn_readfirstlane(*(const u32*)(lds + LR_CTL + 4));
  const u32 tb0 = tbase - (1u << 20);
#define TABLE(l) ({ const float f_ = CLKF() * *(const float*)(lds + LR_CTL + 28);          \
    const float k_ = 1.f + TKS * (fminf(fmaxf(23.9f / f_, 1.f), 1.5f) - 1.f);                                          \
    const int nb_ = (int)(TDB * k_ + 0.5f) + TLB * (l), nc_ = nb_ + (int)((TDC - TDB) * k_ + 0.5f) + TLC * (l), np_ = nc_ + (int)((TP - TDC) * k_ + 0.5f) + TLP * (l); \
    (u32)(nb_ | (nc_ << 10) | (np_ << 20)); })
#define XPN 1
  if (b == 0 && w == 1) {
    u32 tt = 0, lat = 0;
    bool xok = false;
    for (int it = 0; it < SPIN_CAP; ++it) {
      asm volatile("" ::: "memory");
      const u32x4 r0 = __builtin_amdgcn_raw_buffer_load_b128(rsx, lane * 128, PGO_Y, 16), r1 = __builtin_amdgcn_raw_buffer_load_b128(rsx, lane * 128 + 8192, PGO_Y, 16);
      if (!xok) xok = __ballot(xprog(__builtin_amdgcn_raw_buffer_load_b32(rsx, lane * 128, PGO_X, 16), tag) < XPN) == 0;
      const u64 miss = __ballot(r0[0] != tag || r1[0] != tag);
      if (tt == 0 && __popcll(miss) <= 24) {
        float fm = (r0[0] == tag ? __uint_as_float(r0[1]) : 0.f) + (r1[0] == tag ? __uint_as_float(r1[1]) : 0.f), fn = (r0[0] == tag ? 1.f : 0.f) + (r1[0] == tag ? 1.f : 0.f);
#pragma unroll
        for (int o = 4; o < 64; o *= 2) { fm += __shfl_xor(fm, o); fn += __shfl_xor(fn, o); }
        fm = fn > 0.f ? fm / fn : 100.f;
        const float f0 = __uint_as_float(__builtin_amdgcn_readfirstlane(__float_as_uint(fm)));
        fm = fminf(fm, __shfl_xor(fm, 1)); fm = fminf(fm, __shfl_xor(fm, 2));
        if (lane == 0) *(float*)(lds + LR_CTL + 28) = fminf(fmaxf(fm / f0, 0.8f), 1.f);
        asm volatile("s_waitcnt lgkmcnt(0)" ::: "memory");
        tt = TABLE(0);
      }
      lat = min(r0[2], r1[2]);
      if (miss == 0 && xok) break;
    }
    { auto r = __builtin_amdgcn_permlane16_swap(lat, lat, false, false); lat = min(r[0], r[1]); }
    { auto r = __builtin_amdgcn_permlane32_swap(lat, lat, false, false); lat = min(r[0], r[1]); }
    u32 mm[4];
#pragma unroll
    for (int k = 0; k < 4; ++k)
      mm[k] = __builtin_amdgcn_readlane(lat, 4 * k) + __builtin_amdgcn_readlane(lat, 4 * k + 2) + __builtin_amdgcn_readlane(lat, 4 * (k ^ 2) + 1) + __builtin_amdgcn_readlane(lat, 4 * (k ^ 2) + 3);
    const u32 hb = mm[0] + mm[2] <= mm[1] + mm[3] ? 0u : 1u;
    const u32 best = (hb << 13) | ((hb ? mm[1] <= mm[3] : mm[0] <= mm[2]) ? CLS0 : 16384u - CLS0);
    if (lane == 0) __builtin_amdgcn_raw_buffer_store_b128((u32x4){rt32() + TD0, tt, best, tag}, rsx, 0, XSTA, 16);
  }
  XWAIT(2)
  LBAR();
  {
    u64 pc; asm volatile("s_getpc_b64 %0" : "=s"(pc));
    const u32 cv = __builtin_amdgcn_raw_buffer_load_b32(__builtin_amdgcn_make_buffer_rsrc((void*)pc0, 0, clen, 0x00020000), (int)(pc - pc0) + tid * 64, 0, 0);
    asm volatile("" :: "v"(cv));
  }
  xp = max(xp, xprog(__builtin_amdgcn_readfirstlane(__builtin_amdgcn_raw_buffer_load_b32(rsx, 0, pgo, 16)), tag));
  u32x4 st = wait_start(rsx, fb, tag, false);
  const int flip = __builtin_amdgcn_readfirstlane(st[2]) & 8192, cls = __builtin_amdgcn_readfirstlane(st[2]) & 16384;
  o1c ^= cls; o3c ^= cls; sto ^= cls; kc[4] ^= cls; kc[8] ^= cls; kc[9] ^= cls; kc[10] ^= cls; kc[11] ^= cls;
  { const u32 now = rt32(); if (st[0] - now + 100000u > 200000u) st[0] = now; }
  u32x16 kd = {(u32)((ld01 + XR2(vcu ^ 1)) ^ cls), (u32)((ld01 + XR2(vcu ^ 2)) ^ cls), (u32)((ld01 + XR2(vcu ^ 3)) ^ cls), (u32)(X4B && hk ? OOR : sto), (u32)avr, (u32)(avr + 4096), (u32)(avr + 8192),
               (u32)(avr + 12288), (u32)avw, (u32)(X4C && hk ? OOR : st1 ^ cls), (u32)(ldsb + LR_FRG + lane * 16), (u32)(XSB(lane + 1) + flip), (u32)(lane * 8), (u32)avf,
               (u32)(b == 0 && lane == 0 ? PGO_R + flip : OOR), (u32)(pz1 ^ cls)};
  u32x16 sa = {(u32)flip, (u32)(XSB(1) + flip), 0u, (u32)(fo + NH * 8 * 1024), (u32)(ldsb + LR_FRG + 4096 + w * 1024), (u32)(FO_KM + (head * 4 + ks) * 512),
               (u32)(FO_VM + (head * 8 + 2 * ks) * 256), tb0, st[0] - tb0 - (xcd & 1) * XSK, 0u, (u32)SYO(1), 0u, 0u, 0u, 0u, 0u};
  int snt = -1, synced = 0;
  int cb = __builtin_amdgcn_readfirstlane(st[1]), cc = (cb >> 10) & 1023, cp = (cb >> 20) & 1023;
  cb &= 1023;
#define RESYNC()                                                                                                      \
  {                                                                                                                   \
    for (int it = 0; it < 100; ++it) {                                                                                \
      const u32x2 tw = __builtin_amdgcn_raw_buffer_load_b64(rsx, 0, SYO(seg) + 8, 16);                                \
      const u32 tv = __builtin_amdgcn_readfirstlane(tw[0]), to = __builtin_amdgcn_readfirstlane(tw[1]);               \
      if (tv != SENT && to != SENT) {                                                                                 \
        cb = to & 1023; cc = (to >> 10) & 1023; cp = to >> 20; sa[7] = tb0 + seg * cp; sa[8] = tv - sa[7] - (xcd & 1) * XSK;            \
        if (lane == 0) *(u32*)(lds + LR_CTL + 24) = to;                                                               \
        break;                                                                                                        \
      }                                                                                                               \
      asm volatile("s_sleep 4" ::: "memory");                                                                         \
    }                                                                                                                 \
    if (lane == 0) *(u32*)(lds + LR_CTL) = sa[8];                                                                     \
    synced = seg;                                                                                                     \
  }
  int nev = 0; (void)nev;
  bool rsn = false;
  u32x32 keep;
  u32x2 ov;
  asm volatile("" : "={v[60:91]}"(keep));
  while (true) {
    u32x8 sc = {(u32)o1c, (u32)o3c, (u32)snt, (u32)cp, (u32)cb, (u32)cc, 0u, (u32)pgo};
    sa[15] = rsn ? 1u : 0u;
    sa[14] = xp;
#pragma unroll
    for (int i = 0; i < 16; ++i) sa[i] = __builtin_amdgcn_readfirstlane(sa[i]);
#pragma unroll
    for (int i = 0; i < 8; ++i) sc[i] = __builtin_amdgcn_readfirstlane(sc[i]);
    asm volatile(
        "s_nop 4\n\t" "s_mov_b32 s60, m0\n\t"
        "s_mov_b32 m0, %[mb]\n\t" "s_mov_b32 s61, s51\n\t" "v_mov_b32 v48, %[tg]\n\t" "s_mov_b32 s64, s50\n\t"
        EXTRA "s_mov_b32 s70, 0\n\t" "s_mov_b32 s78, 0xff00ff00\n\t" "s_mov_b32 s79, 0xff00ff00\n\t"
        R2BASE
        "s_cmp_eq_u32 s47, 1\n\t" "s_cbranch_scc1 .LB%=\n\t"
        "s_cmp_eq_u32 s47, 2\n\t" "s_cbranch_scc1 .LC%=\n"
        ".LA%=:\n\t"
        CLKW(".Lta%=", "0")
        "s_add_u32 s50, s36, s52\n\t"
        TSA(0) GLD(96, 0)
        "ds_read_b32 v62, v38 offset:16\n\t" "ds_read_b128 v[52:55], v18\n\t" "ds_read_b128 v[56:59], v18 offset:1024\n\t" ZER(64) ZER(68)
        GLD(100, 1024) ZER(72) ZER(76) ZER(24) ZER(28) R2BASE
        GLD(104, 2048)
        "s_mov_b32 m0, s40\n\t" "s_mov_b32 s47, 0\n\t"
        "s_sub_i32 s63, s64, s45\n\t" "s_cmp_lt_i32 s63, " STR(XTH) "\n\t" "s_cselect_b32 s63, s59, %[dm]\n\t" "s_nop " STR(APC) "\n\t"
        GLD(108, 3072)
        ".Lfw%=:\n\t"
        WLG(0)
        "v_readfirstlane_b32 s49, v62\n\t"
        "s_cmp_ge_u32 s49, s38\n\t"
        "s_cbranch_scc1 .Lfx%=\n\t"
        "ds_read_b32 v62, v38 offset:16\n\t" "ds_read_b128 v[52:55], v18\n\t" "ds_read_b128 v[56:59], v18 offset:1024\n\t"
        "s_branch .Lfw%=\n"
        ".Lfx%=:\n\t"
        WVM(3) TSA(1) SQ(0, 0) MXA0(96) SQ(1, 0) MXB(96) SQ(2, 0) SQ(3, 0)
        WVM(2) SQ(0, 1) "buffer_load_dwordx4 v44, %[rf], s39 offen sc1 lds\n\t" SQ(1, 1) "buffer_load_dword v48, off, %[rs], s63 sc1\n\t"
        SQ(2, 1) MXA(100) MXB(100) SQ(3, 1) "s_mov_b32 m0, %[mb]\n\t"
        QBLK(2, MXA)
        WVM(2) SQ(0, 3) MXA(108) MXB(108) SQ(1, 3) CMPB SQ(2, 3) SQ(3, 3) TSA(2)
        "s_cbranch_vccnz .Lfail%=\n\t"
        "s_nop 1\n\t" PSA(64) PSW(64, 0) PSW(65, 272) PSA(68) SV(0, 0) PSW(68, 640) PSW(69, 912) PSA(72) SV(1, 0) PSW(72, 1280) PSW(73, 1552) PSA(76) SV(0, 1) PSW(76, 1920) PSW(77, 2192)
        ARRIVE(8) TSA(3)
        RDQ(80, 0) SV(1, 1) RDQ(84, 16) RDQ(88, 4096) SV(0, 2) RDQ(92, 4112) RDQ(112, 8192) SV(1, 2) RDQ(116, 8208) WLG(12) RDQ(120, 12288) SV(0, 3) RDQ(124, 12304) SV(1, 3)
        WLG(8) TSA(4)
        "v_readfirstlane_b32 s48, v62\n\t"
        "s_and_b32 s48, s48, 3\n\t"
        ROLESV "s_cmp_lg_u32 s48, 3\n\t"
        "s_cbranch_scc1 .Lnl%=\n\t"
        WLG(4) PKA(80, 80, 88) PKA(82, 82, 90) PKA(84, 84, 92) PKA(86, 86, 94)
        WLG(0) PKA(112, 112, 120) PKA(114, 114, 122) PKA(116, 116, 124) PKA(118, 118, 126)
        TSA(5) PKA(80, 80, 112) PKA(82, 82, 114) PKA(84, 84, 116) PKA(86, 86, 118)
        "v_cvt_pk_bf16_f32 v72, v80, v81\n\t" "v_cvt_pk_bf16_f32 v73, v82, v83\n\t" "v_cvt_pk_bf16_f32 v74, v84, v85\n\t" "v_cvt_pk_bf16_f32 v75, v86, v87\n\t"
        "s_nop 1\n\t"
        "v_mfma_f32_16x16x32_bf16 v[88:91], v[72:75], v[72:75], 0\n\t"
        "v_mfma_f32_16x16x32_bf16 v[80:83], v[52:55], v[72:75], 0\n\t"
        "v_mfma_f32_16x16x32_bf16 v[84:87], v[56:59], v[72:75], 0\n\t"
        "s_nop 5\n\t"
        "buffer_store_dwordx4 v[88:91], v41, %[rs], s71 offen sc1\n\t"
        "buffer_store_dwordx4 v[80:83], v40, %[rs], s71 offen sc1\n\t"
        "buffer_store_dwordx4 v[84:87], v40, %[rs], s71 offen offset:512 sc1\n\t" TSA(6) TSB(7)
        VPOST TSA(7)
        "s_cmp_eq_u32 s45, 63\n\t"
        "s_cbranch_scc0 .LB%=\n\t"
        "buffer_store_dwordx4 v[72:75], v47, %[rf], s41 offen sc1\n\t"
        "s_branch .LC%=\n"
        ".LB%=:\n\t"
        CLKW(".Ltb%=", "s66")
        "s_mov_b32 s47, 1\n\t"
        R2TEXT
        "s_branch .LC2%=\n"
        R2MAX
        ".Lnl%=:\n\t"
        "s_nop 2\n\t" WLG(4) VPOST
        "s_cmp_lg_u32 s48, 0\n\t"
        "s_cbranch_scc1 .LC%=\n"
        ".Lv0%=:\n\t"
        "v_add_u32 v80, 4096, v21\n\t" "v_add_u32 v81, 8192, v21\n\t" "v_add_u32 v82, 12288, v21\n\t"
        "ds_read_b32 v59, v38 offset:12\n\t"
        "ds_read2_b32 v[112:113], v21 offset0:0 offset1:68\n\t" "ds_read2_b32 v[114:115], v21 offset0:16 offset1:84\n\t"
        "ds_read2_b32 v[116:117], v80 offset0:0 offset1:68\n\t" "ds_read2_b32 v[118:119], v80 offset0:16 offset1:84\n\t"
        "ds_read2_b32 v[120:121], v81 offset0:0 offset1:68\n\t" "ds_read2_b32 v[122:123], v81 offset0:16 offset1:84\n\t"
        "ds_read2_b32 v[124:125], v82 offset0:0 offset1:68\n\t" "ds_read2_b32 v[126:127], v82 offset0:16 offset1:84\n\t"
        WLG(0)
        "v_readfirstlane_b32 s49, v59\n\t"
        "s_sub_u32 s49, s49, 4\n\t"
        "s_cmp_lt_i32 s49, s38\n\t"
        "s_cbranch_scc1 .Lv0%=\n\t"
        PKA(112, 112, 116) PKA(114, 114, 118) PKA(120, 120, 124) PKA(122, 122, 126) PKA(112, 112, 120) PKA(114, 114, 122)
        "v_cvt_pk_bf16_f32 v112, v112, v113\n\t" "v_cvt_pk_bf16_f32 v113, v114, v115\n\t"
        "s_add_u32 s49, s45, 1\n\t" "v_mov_b64 v[116:117], -1\n\t" "v_mov_b64 v[118:119], -1\n\t" "v_readlane_b32 s63, v19, s49\n\t"
        "s_cmp_eq_u32 s45, 63\n\t" "s_cselect_b32 s65, 0, s68\n\t"
        CLKL(".Ltv%=", "s65") CLKW(".Ltw%=", "s65")
        "s_lshl_b32 s50, s50, 17\n\t" "s_lshr_b32 s50, s50, 1\n\t" "s_or_b32 s50, s50, s55\n\t" "v_mov_b32 v114, s50\n\t" "s_lshl_b32 s50, s45, 7\n\t"
        "buffer_store_dwordx4 v[116:119], v23, %[rs], s63 offen sc1\n\t"
        "s_xor_b32 s63, s71, " STR(XR2S) "\n\t"
        "buffer_store_dwordx4 v[116:119], v42, %[rs], s63 offen sc1\n\t"
        "buffer_store_dwordx4 v[116:119], v36, %[rs], s63 offen sc1\n\t"
        "buffer_store_dwordx2 v[112:113], v20, %[rf], s42 offen sc1\n\t"
        "buffer_store_dword v114, v22, %[rs], s50 offen sc1\n"
        ".LC%=:\n\t"
        "s_mov_b32 s47, 3\n\t"
        "s_cmp_eq_u32 s45, 63\n\t"
        "s_cbranch_scc1 .Lend%=\n\t"
        CLKL(".Ltl%=", "s67")
        ".LC2%=:\n\t"
        CLKW(".Ltc%=", "s67")
        "s_add_u32 s50, s36, s53\n\t"
        TSW(0)
        GL1(96, 0) PC GL1(100, 1024) PC GL1(104, 2048) PC GL1(108, 3072) PC GL2(112, 0) PC GL2(116, 1024)
        "s_mov_b32 s48, s36\n\t" "s_mov_b32 s36, s37\n\t" "s_add_u32 s45, s45, 1\n\t" "s_add_u32 s38, s38, 4\n\t" "v_readlane_b32 s37, v19, s45\n\t"
        "s_add_u32 s39, s39, " STR(EXF) "\n\t" "s_xor_b32 s40, s40, 4096\n\t" "v_xor_b32 v18, 4096, v18\n\t"
        "s_add_u32 s41, s41, " STR(EXK) "\n\t" "s_add_u32 s42, s42, " STR(EXK) "\n\t" "s_add_u32 s43, s43, s55\n\t"
        GL2(120, 2048) ZER(64) ZER(68) "s_mov_b32 s47, 2\n\t"
        "s_mul_i32 s70, s69, 3\n\t" EXTRA
        GL2(124, 3072)
        "buffer_load_dword v61, off, %[rs], s46 sc1\n\t"
        "s_add_u32 s46, s46, 64\n\t"
        WVM(8) TSW(1) WBLK(0, MXA0) WBLK(1, MXA) WBLK(2, MXA) WBLK(3, MXA) WBLK(4, MXA) WBLK(5, MXA) WBLK(6, MXA)
        WVM(1) TSW(2) SW(0, 7) MXA(124) MXB(124) SW(1, 7) CMPB TSW(3)
        "s_cbranch_vccnz .Lfail%=\n\t"
        "s_nop 3\n\t"
        "v_add_f32_dpp v80, v64, v66 quad_perm:[0,1,2,3] row_mask:0x3 bank_mask:0xf\n\t" "v_add_f32_dpp v81, v65, v67 quad_perm:[0,1,2,3] row_mask:0x3 bank_mask:0xf\n\t"
        "v_add_f32_dpp v80, v68, v70 quad_perm:[0,1,2,3] row_mask:0xc bank_mask:0xf\n\t" "v_add_f32_dpp v81, v69, v71 quad_perm:[0,1,2,3] row_mask:0xc bank_mask:0xf\n\t"
        "s_nop 1\n\t"
        "v_permlane32_swap_b32 v80, v81\n\t"
        "v_add_f32 v80, v80, v81\n\t"
        PSW(80, 3840)
        ARRIVE(16) TSW(4)
        "ds_read_b128 v[80:83], v46\n\t" "ds_read_b128 v[84:87], v46 offset:4096\n\t"
        "ds_read_b128 v[88:91], v46 offset:8192\n\t" "ds_read_b128 v[92:95], v46 offset:12288\n\t"
        "v_readfirstlane_b32 s50, v48\n\t"
        WLG(4) TSW(5)
        "v_readfirstlane_b32 s51, v62\n\t"
        WLG(2) PKA(80, 80, 84) "s_and_b32 s51, s51, 3\n\t" PKA(82, 82, 86) "s_cmp_lg_u32 s51, 3\n\t" WLG(0) PKA(88, 88, 92) PKA(90, 90, 94) PKA(80, 80, 88) PKA(82, 82, 90)
        "v_cvt_pk_bf16_f32 v80, v80, v81\n\t" "v_cvt_pk_bf16_f32 v81, v82, v83\n\t"
        "v_min_u32 v80, -2, v80\n\t" "v_min_u32 v81, -2, v81\n\t"
        "s_cbranch_scc1 .Lnv%=\n\t"
        STC TSW(6)
        WVM(1) "s_branch .Lnw%=\n"
        ".Lnv%=:\n\t" WVM(0)
        ".Lnw%=:\n\t"
        "v_readfirstlane_b32 s49, v61\n\t"
        "s_sub_u32 s50, s50, %[tg]\n\t" "s_cmp_le_u32 s50, 80\n\t" "s_cselect_b32 s50, s50, 0\n\t"
        "s_max_i32 s64, s64, s50\n\t" "s_add_u32 s44, s44, s70\n\t"
        "s_not_b32 s49, s49\n\t" "s_or_b32 s49, s49, s61\n\t"
        "s_sub_i32 s50, s64, s45\n\t" "s_cmp_lt_i32 s50, " STEPCHK "\n\t" "s_cselect_b32 s50, 1, 0\n\t"
        "s_or_b32 s50, s50, s49\n\t"
        "s_cbranch_scc0 .LA%=\n\t"
        "s_mov_b32 s47, 4\n\t"
        "s_branch .Lend%=\n" ".Lfail%=:\n\t" "s_nop 7\n" ".Lend%=:\n\t"
        "s_waitcnt vmcnt(0) lgkmcnt(0)\n\t"
        ROLEOUT "s_mov_b32 s50, s64\n\t" "s_mov_b32 m0, s60"
        : "+{s[36:51]}"(sa), "+{v[8:23]}"(kd), "+{v[60:91]}"(keep), "=&{v[116:117]}"(ov) FINEO
        : "{v[32:47]}"(kc), "{s[52:59]}"(sc), [rs] "s"(rsx), [rf] "s"(rsf), [dm] "s"(__builtin_amdgcn_readfirstlane(PGO_Y + b * 128)), [tg] "s"(__builtin_amdgcn_readfirstlane(tag)), [r2] "s"(__builtin_amdgcn_readfirstlane(XR2B + flip)), [mb] "s"(__builtin_amdgcn_readfirstlane(ldsb + w * 4096)),
          "{a[0:31]}"(wa[0]), "{a[32:63]}"(wa[1]), "{a[64:95]}"(wa[2]), "{a[96:127]}"(wa[3]), "{a[128:159]}"(wa[4]), "{a[160:191]}"(wa[5]),
          "{v[128:159]}"(wo[0]), "{v[160:191]}"(wo[1]), "{v[192:223]}"(wo[2]), "{v[224:255]}"(wo[3])
        : CL8(24, 25, 26, 27, 28, 29, 30, 31), "v48", CL8(52, 53, 54, 55, 56, 57, 58, 59), "v92", "v93", "v94", "v95", CL96, "v112", "v113", "v114", "v115", "v118", "v119", CL120,
          "s60", "s61", "s62", "s63", "s64", "s65", "s66", "s67", "s68", "s69", "s70", "s71", "s72", "s73", "s74", "s75", "s76", "s77", "s78", "s79", "vcc", "scc", "memory");
    const int code = __builtin_amdgcn_readfirstlane(sa[11]);
    xp = __builtin_amdgcn_readfirstlane(sa[14]);
    if (code == 2) {
      sa[1] = sa[0]; sa[0] = sa[12]; sa[9] -= 1; sa[2] -= 4; sa[3] -= EXF; sa[4] ^= 4096; kd[10] ^= 4096;
      sa[5] -= EXK; sa[6] -= EXK; sa[7] -= cp; sa[10] -= 64;
    }
    const int seg = __builtin_amdgcn_readfirstlane(sa[9]);
    if (code == 3) break;
    if (code == 4) {
      XWAIT(seg + 2)
      if (sa[13] != 0) {
        rsn = false;
        if (b == 0 && sa[15] == 3) {
          const int l = min((int)*(const u32*)(lds + LR_CTL + 20), 40);
          const u32 tt = TABLE(l);
          if (lane == 0) {
            __builtin_amdgcn_raw_buffer_store_b64((u32x2){nosent(rt32() + TD), tt}, rsx, 0, SYO(seg) + 8, 16);
            if (seg > 2) *(u32*)(lds + LR_CTL + 20) = l + 1;
          }
        }
        RESYNC()
      }
      sa[11] = 0;
      continue;
    }
    if (code == 1) {
      u32 mx = 0;
#pragma unroll
      for (int i = 0; i < 20; ++i) if (i < 12 || i >= 16) mx = max(mx, keep[i]);
      if (__ballot(mx == SENT) == 0 || tries > TRY_CAP) { __builtin_amdgcn_raw_buffer_store_b64((u32x2){nosent(ov[0]), nosent(ov[1])}, rsx, sto, sa[0], 16); sa[11] = 2; continue; }
    }
    ++tries; tcnt += 1 << (10 * code);
    {
      const u32 to = lds_word(ctl + 24);
      if (to != 0 && (int)(to >> 20) != cp) { cb = to & 1023; cc = (to >> 10) & 1023; cp = to >> 20; sa[7] = tb0 + seg * cp; sa[8] = 0; }
    }
    if (tries > TRY_CAP) snt = snt == -1 ? 0 : snt + 1;
    if (code == 0 && synced != seg && __builtin_amdgcn_readfirstlane(__builtin_amdgcn_raw_buffer_load_b32(rsx, 0, SYO(seg), 16)) != SENT) {
      RESYNC()
      continue;
    }
    if (b == 0 && !rsn) {
      if (lane == 0) __builtin_amdgcn_raw_buffer_store_b32(1u, rsx, 0, SYO(seg + 1), 16);
      rsn = true;
    }
    sa[8] = max(sa[8], rt32() + TR - sa[7] - (code == 0 ? 0 : (code == 1 ? cb : cc) + code * TEX(seg)));
    if (lane == 0) atomicMax((u32*)(lds + LR_CTL), sa[8]);
  }
  LBAR();
  if (tid == 0) __builtin_amdgcn_raw_buffer_store_b32(1u, rsx, 0, PGO_D + vcu * 128, 16);
  if (b == 0 && w == 1) {
    for (int it = 0; it < SPIN_CAP; ++it) {
      asm volatile("" ::: "memory");
      if (__ballot(__builtin_amdgcn_raw_buffer_load_b32(rsx, lane * 128, PGO_S, 16) != tag || __builtin_amdgcn_raw_buffer_load_b32(rsx, lane * 128 + 8192, PGO_S, 16) != tag) == 0) break;
    }
    if (lane == 0) __builtin_amdgcn_raw_buffer_store_b32(tag, rsx, 0, XEPO, 16);
  }
}

#define XSTAT 3
#define XVM 8
#define XP0 -1
#define XSC " sc1"
#define XPITCH 1056
#define XSLOT (32 * XPITCH)
#define XW(HB, t) "a[" STR(HB) "+8*xk+4*" #t ":" STR(HB) "+8*xk+4*" #t "+3]"
#define XF(rt) "v[96+8*(xk&3)+4*" #rt ":96+8*(xk&3)+4*" #rt "+3]"
#define XFN(rt) "v[96+8*((xk+3)&3)+4*" #rt ":96+8*((xk+3)&3)+4*" #rt "+3]"
#define XA(AC, o) "v[" STR(AC) "+" #o ":" STR(AC) "+" #o "+3]"
#define XM1(d, a, b, FIRST) ".if (xk == 0) && " STR(FIRST) "\n\tv_mfma_f32_16x16x32_bf16 " d ", " a ", " b ", 0\n\t.else\n\tv_mfma_f32_16x16x32_bf16 " d ", " a ", " b ", " d "\n\t.endif\n\t"
#define XMF_QK(i, HB, AC, F) XMF_QK##i(HB, AC, F)
#define XMF_QK0(HB, AC, F) XM1(XA(AC, 0), XW(HB, 0), XF(0), F)
#define XMF_QK1(HB, AC, F) XM1(XA(AC, 4), XW(HB, 1), XF(0), F)
#define XMF_QK2(HB, AC, F) XM1(XA(AC, 8), XW(HB, 0), XF(1), F)
#define XMF_QK3(HB, AC, F) XM1(XA(AC, 12), XW(HB, 1), XF(1), F)
#define XMF_V(i, HB, AC, F) XMF_V##i(HB, AC, F)
#define XMF_V0(HB, AC, F) XM1(XA(AC, 0), XF(0), XW(HB, 0), F)
#define XMF_V1(HB, AC, F) XM1(XA(AC, 8), XF(0), XW(HB, 1), F)
#define XMF_V2(HB, AC, F) XM1(XA(AC, 4), XF(1), XW(HB, 0), F)
#define XMF_V3(HB, AC, F) XM1(XA(AC, 12), XF(1), XW(HB, 1), F)
#define XRD(rt, CUR, NXT) ".if xk < 13\n\tds_read_b128 " XFN(rt) ", " CUR " offset:64*(xk+3)+" STR(rt * 16 * XPITCH) "\n\t.else\n\t" \
                          "ds_read_b128 " XFN(rt) ", " NXT " offset:64*(xk-13)+" STR(rt * 16 * XPITCH) "\n\t.endif\n\t"
#define XCV(EPI, PV, i) ".if " STR(EPI) " && (xk >= 1) && (xk < 3)\n\tv_cvt_pk_bf16_f32 v[128+4*(xk-1)+" #i "], v[" STR(PV) "+8*(xk-1)+2*" #i "], v[" STR(PV) "+8*(xk-1)+2*" #i "+1]\n\t.endif\n\t"
#define XHALF(MF, HB, AC, PV, EPI, CUR, NXT, DSLOT, GOFF)                                                   \
  ".set xk, 0\n\t"                                                                                          \
  ".rept 16\n\t"                                                                                            \
  "s_waitcnt lgkmcnt(4)\n\t"                                                                                \
  MF(0, HB, AC, EPI) XRD(0, CUR, NXT) XCV(EPI, PV, 0)                                                       \
  ".if (xk >= 5) && (xk < 13)\n\t"                                                                          \
  "s_add_u32 m0, m0, " STR(XPITCH) "\n\t"                                                                   \
  ".endif\n\t"                                                                                              \
  MF(1, HB, AC, EPI) XRD(1, CUR, NXT) XCV(EPI, PV, 1)                                                       \
  ".if (xk >= 5) && (xk < 13)\n\t"                                                                          \
  "s_add_u32 %[dg], %[dg], 2048\n\t"                                                                        \
  ".endif\n\t"                                                                                              \
  MF(2, HB, AC, EPI) XCV(EPI, PV, 2)                                                                        \
  ".if (xk >= 4) && (xk < 12)\n\t"                                                                          \
  "buffer_load_dwordx4 %[l16], %[rx], %[dg] offen lds\n\t"                                                  \
  ".endif\n\t"                                                                                              \
  ".if xk == 0\n\t"                                                                                         \
  "s_add_u32 m0, %[dl], " STR((DSLOT) * XSLOT) "\n\t" "s_add_u32 %[dg], %[gb], " STR(GOFF) "\n\t"           \
  ".endif\n\t"                                                                                              \
  ".if " STR(EPI) " && (xk == " STR(XSTAT) ")\n\t"                                                                       \
  "buffer_store_dwordx4 v[128:131], v137, %[rf], %[sg] offen" XSC "\n\t"                                       \
  ".endif\n\t"                                                                                              \
  ".if " STR(EPI) " && (xk == 13)\n\t"                                                                      \
  "v_mov_b32 v137, %[l16]\n\t" "v_mov_b32 v138, %[lp2]\n\t"                                                 \
  ".endif\n\t"                                                                                              \
  ".if " STR(EPI) " && (xk == 14)\n\t"                                                                      \
  "s_add_u32 %[sg], %[sg], " STR(NH * 8 * 1024) "\n\t"                                                      \
  "v_add_u32 v136, 1, v136\n\t"                                                                             \
  ".endif\n\t"                                                                                              \
  ".if !" STR(EPI) " && (xk == 1)\n\t"                                                                      \
  "buffer_store_dword v136, %[fo], %[rp], 0 offen sc1\n\t"                                                  \
  ".endif\n\t"                                                                                              \
  MF(3, HB, AC, EPI) XCV(EPI, PV, 3)                                                                        \
  ".if xk == 0\n\t"                                                                                         \
  "s_waitcnt vmcnt(" STR(XVM) ")\n\t" "s_barrier\n\t"                                                               \
  ".endif\n\t"                                                                                              \
  ".if xk == 2\n\t"                                                                                         \
  "s_min_u32 %[dg], %[dg], " STR(S_LEN * DM * 2 - 16384 + 1024) "\n\t"                                      \
  ".endif\n\t"                                                                                              \
  ".if " STR(EPI) " && (xk == " STR(XSTAT) ")\n\t"                                                                       \
  "buffer_store_dwordx4 v[132:135], v138, %[rf], %[sg] offen" XSC "\n\t"                                       \
  ".endif\n\t"                                                                                              \
  ".set xk, xk+1\n\t"                                                                                       \
  ".endr\n\t"
#define XLOOP(MF)                                                                                           \
  "s_mov_b32 %[kp], m0\n\t"                                                                                 \
  "v_mov_b32 v136, %[p0]\n\t" "v_mov_b32 v137, " STR(OOR) "\n\t" "v_mov_b32 v138, " STR(OOR) "\n\t"                                                                                \
  "ds_read_b128 v[96:99], %[r0]\n\tds_read_b128 v[100:103], %[r0] offset:" STR(16 * XPITCH) "\n\t"          \
  "ds_read_b128 v[104:107], %[r0] offset:64\n\tds_read_b128 v[108:111], %[r0] offset:64+" STR(16 * XPITCH) "\n\t"   \
  "ds_read_b128 v[112:115], %[r0] offset:128\n\tds_read_b128 v[116:119], %[r0] offset:128+" STR(16 * XPITCH) "\n"   \
  ".Lx%=:\n\t"                                                                                              \
  XHALF(MF, 0, 64, 80, 1, "%[r0]", "%[r1]", 3, 66560) XHALF(MF, 128, 64, 80, 0, "%[r1]", "%[r2]", 0, 131072) \
  XHALF(MF, 0, 80, 64, 1, "%[r2]", "%[r3]", 1, 132096) XHALF(MF, 128, 80, 64, 0, "%[r3]", "%[r0]", 2, 196608) \
  "s_add_u32 %[gb], %[gb], 131072\n\t"                                                                      \
  "s_sub_u32 %[cnt], %[cnt], 1\n\t"                                                                         \
  "s_cmp_lg_u32 %[cnt], 0\n\t"                                                                              \
  "s_cbranch_scc1 .Lx%=\n\t"                                                                                \
  "s_mov_b32 m0, %[kp]\n\t"                                                                                 \
  "s_nop 7\n\t" "s_waitcnt lgkmcnt(0)"

template <bool ISV>
__device__ __forceinline__ void x_role(unsigned char* lds, const u16* __restrict__ x, const u16* __restrict__ Wm, int fo0, int head,
                                       __amdgpu_buffer_rsrc_t rsf, u32* prog, u32 tag, int lane, int w) {
  const int ldsb = (int)(size_t)(LAS unsigned char*)lds;
  const __amdgpu_buffer_rsrc_t rsxin = __builtin_amdgcn_make_buffer_rsrc((void*)x, 0, S_LEN * DM * 2, 0x00020000);
  const __amdgpu_buffer_rsrc_t rsp = __builtin_amdgcn_make_buffer_rsrc((void*)prog, 0, 128, 0x00020000);
  const int lr = lane & 15, lg = lane >> 4;
  const int xrd = ldsb + lr * XPITCH + lg * 16;
  const int fob = fo0 + (ISV ? (head * 8 + 2 * w) : (head * 8 + w)) * 1024;
  const int p2 = ISV ? 1024 : 4096;
  u32x32 xw[8];
  {
    const __amdgpu_buffer_rsrc_t rw = __builtin_amdgcn_make_buffer_rsrc((void*)Wm, 0, DM * HD * 2, 0x00020000);
    const int vq = (lane >> 4) * (HD * 2) + head * 256 + (lane & 15) * 16;
#define XWISSUE(c) _Pragma("unroll") for (int j = 0; j < 8; ++j)                                                       \
      asm volatile("s_mov_b32 m0, %0\n\ts_nop 3\n\tbuffer_load_dwordx4 %1, %2, %3 offen lds"                           \
                   :: "s"(ldsb + ((c) & 3) * XSLOT + (8 * w + j) * XPITCH), "v"(vq), "s"(rw), "s"((128 * (c) + 32 * w + 4 * j) * (HD * 2)) : "memory");
#define XXISSUE(h) _Pragma("unroll") for (int j = 0; j < 8; ++j)                                                       \
      asm volatile("s_mov_b32 m0, %0\n\ts_nop 3\n\tbuffer_load_dwordx4 %1, %2, %3 offen lds"                           \
                   :: "s"(ldsb + (h) * XSLOT + (8 * w + j) * XPITCH), "v"(lane * 16), "s"(rsxin), "s"((((h) >> 1) * 32 + 8 * w + j) * 2048 + ((h) & 1) * 1024) : "memory");
    XWISSUE(0) XWISSUE(1) XWISSUE(2) XWISSUE(3)
    const int q = lr >> 2, p = lr & 3;
#pragma unroll
    for (int c = 0; c < 8; ++c) {
      asm volatile("s_waitcnt vmcnt(24)\n\ts_barrier" ::: "memory");
#pragma unroll
      for (int s = 0; s < 4; ++s)
#pragma unroll
        for (int t = 0; t < 2; ++t) {
          const unsigned char* base = lds + (c & 3) * XSLOT + (8 * s + 2 * lg) * XPITCH + q * 256 + w * 64 + p * 16 + t * 8;
          const u32x2 t0 = __builtin_bit_cast(u32x2, __builtin_amdgcn_ds_read_tr16_b64_v4i16((LAS v4i16_t*)(base)));
          const u32x2 t1 = __builtin_bit_cast(u32x2, __builtin_amdgcn_ds_read_tr16_b64_v4i16((LAS v4i16_t*)(base + XPITCH)));
          const int o = ((4 * c + s) * 2 + t) * 4;
          xw[o >> 5][(o & 31) + 0] = t0[0]; xw[o >> 5][(o & 31) + 1] = t0[1]; xw[o >> 5][(o & 31) + 2] = t1[0]; xw[o >> 5][(o & 31) + 3] = t1[1];
        }
      LBAR();
      if (c < 4) { XWISSUE(c + 4) } else if (c < 7) { XXISSUE(c - 4) }
    }
  }
  asm volatile("s_waitcnt vmcnt(8)\n\ts_barrier" ::: "memory");
  u32x16 xacc;
  {
    u32 kp, dg; int gb = 8 * w * 2048, cnt = NSEG / 2, sg = fob - NH * 8 * 1024;
#define XASM(MF)                                                                                            \
    asm volatile(XLOOP(MF)                                                                                  \
                 : "=&{v[80:95]}"(xacc), [kp] "=&s"(kp), [dg] "=&s"(dg), [gb] "+s"(gb), [cnt] "+s"(cnt), [sg] "+s"(sg) \
                 : [r0] "v"(xrd), [r1] "v"(xrd + XSLOT), [r2] "v"(xrd + 2 * XSLOT), [r3] "v"(xrd + 3 * XSLOT), [l16] "v"(lane * 16), [lp2] "v"(lane * 16 + p2), \
                   [rx] "s"(rsxin), [rf] "s"(rsf), [rp] "s"(rsp), [fo] "v"(lane == 0 && w == 0 ? 0 : OOR), [dl] "s"(ldsb + 8 * w * XPITCH), [p0] "s"(tag + XP0), \
                   "{a[0:31]}"(xw[0]), "{a[32:63]}"(xw[1]), "{a[64:95]}"(xw[2]), "{a[96:127]}"(xw[3]),      \
                   "{a[128:159]}"(xw[4]), "{a[160:191]}"(xw[5]), "{a[192:223]}"(xw[6]), "{a[224:255]}"(xw[7]) \
                 : CL64, CL72, CL96, CL112, CL120, CL8(128, 129, 130, 131, 132, 133, 134, 135), "v136", "v137", "v138", "scc", "memory");
    if (ISV) { XASM(XMF_V) } else { XASM(XMF_QK) }
  }
#pragma unroll
  for (int jj = 0; jj < 2; ++jj) {
    u32x4 pk;
#pragma unroll
    for (int e = 0; e < 4; ++e) pk[e] = pack2(__uint_as_float(xacc[8 * jj + 2 * e]), __uint_as_float(xacc[8 * jj + 2 * e + 1]));
    __builtin_amdgcn_raw_buffer_store_b128(pk, rsf, lane * 16 + p2 * jj, fob + (NSEG - 1) * (NH * 8 * 1024), 16);
  }
  asm volatile("s_waitcnt vmcnt(0)\n\ts_barrier" ::: "memory");
  if (lane == 0 && w == 0) __hip_atomic_store(prog, tag + NSEG + 8, RLX_AGENT);
}

#define SSM_LO 1
#define SU_CP 4416
#define SU_U 0
#define SU_K (4 * SU_CP)
#define SU_P (SU_K + 10400)
#define SU_D (SU_P + 2048)
#define SU_RAW (SU_D + 2592)
#define SU_CH (SU_RAW + 4128 + 32)
#define SST(i)
__device__ __forceinline__ void ssm_unit(unsigned char* sm, int d0, __amdgpu_buffer_rsrc_t rst, __amdgpu_buffer_rsrc_t rsx, unsigned char* xb, int fb, bool& uok,
                                         const u16* __restrict__ Dskip, int tid, int lane, int w) {
  unsigned char* cb = sm + w * SU_CH;
  const int dd = d0 + w;
  SST(0)
  if (!uok) {
    for (int it = 0; it < SPIN_CAP; ++it) {
      asm volatile("" ::: "memory");
      if ((int)__builtin_amdgcn_readfirstlane(__builtin_amdgcn_raw_buffer_load_b32(rsx, 0, XUDN, 16)) >= 15) break;
      asm volatile("s_sleep 8" ::: "memory");
    }
    asm volatile("" ::: "memory");
    uok = true;
  }
  f32x4 acc[3];
#pragma unroll
  for (int t = 0; t < 3; ++t) acc[t] = __builtin_bit_cast(f32x4, __builtin_amdgcn_raw_buffer_load_b128(rst, lane * 16, 2 * SSM_L * SSM_D * 2 + dd * 3072 + t * 1024, 16));
  const float dsk = bf2f(Dskip[dd]);
  u32x4 uv[4];
#pragma unroll
  for (int i = 0; i < 4; ++i) uv[i] = __builtin_amdgcn_raw_buffer_load_b128(rst, (tid + 256 * i) * 16, d0 * 4096, 16);
  const int r = lane & 31, h = lane >> 5;
  {
    const int lr = lane & 15, lg = lane >> 4;
    SST(2)
    u32* Kd = (u32*)(cb + SU_D);
#pragma unroll
    for (int e = 0; e < 4; ++e) {
      Kd[128 * (lg >> 1) + 8 * lr + 4 * (lg & 1) + e] = split_hl(acc[0][e]);
      Kd[lg < 2 ? 256 + 64 * lg + 4 * lr + e : 384 + 32 * (2 * (lg - 2) + (e >> 1)) + 2 * lr + (e & 1)] = split_hl(acc[1][e]);
      if (lg < 2 || (lg == 2 && e == 0 && lr == 0)) Kd[512 + 16 * (4 * lg + e) + lr] = split_hl(acc[2][e]);
    }
    asm volatile("s_waitcnt lgkmcnt(0)" ::: "memory");
    unsigned char* kr = cb + SU_K + (lane + 1) * 80;
#pragma unroll
    for (int c8 = 0; c8 < 4; ++c8) {
      u32 kv[8];
#pragma unroll
      for (int e = 0; e < 8; ++e) kv[e] = Kd[kidx(32 * lane + 31 - 8 * c8 - e)];
      u32x4 hi, lo;
#pragma unroll
      for (int e = 0; e < 4; ++e) {
        hi[e] = __builtin_amdgcn_perm(kv[2 * e + 1], kv[2 * e], 0x05040100u);
        lo[e] = __builtin_amdgcn_perm(kv[2 * e + 1], kv[2 * e], 0x07060302u);
      }
      *(u32x4*)(kr + 16 * c8) = hi;
      if (SSM_LO) *(u32x4*)(kr + 5200 + 16 * c8) = lo;
    }
    if (lane < 8) *(u32x4*)(cb + SU_K + (lane >> 2) * 5200 + (lane & 3) * 16) = (u32x4){0u, 0u, 0u, 0u};
  }
  SST(3)
#pragma unroll
  for (int i = 0; i < 4; ++i) {
    const int l = 2 * (tid + 256 * i) + 8;
    *(u32*)(sm + 0 * SU_CH + SU_RAW + l * 2) = __builtin_amdgcn_perm(uv[i][2], uv[i][0], 0x05040100u);
    *(u32*)(sm + 1 * SU_CH + SU_RAW + l * 2) = __builtin_amdgcn_perm(uv[i][2], uv[i][0], 0x07060302u);
    *(u32*)(sm + 2 * SU_CH + SU_RAW + l * 2) = __builtin_amdgcn_perm(uv[i][3], uv[i][1], 0x05040100u);
    *(u32*)(sm + 3 * SU_CH + SU_RAW + l * 2) = __builtin_amdgcn_perm(uv[i][3], uv[i][1], 0x07060302u);
  }
  if (lane < 2) *(u32x4*)(cb + SU_RAW + lane * (4096 + 16)) = (u32x4){0u, 0u, 0u, 0u};
  LBAR();
  {
    if (lane < 20) *(u32x4*)(cb + SU_U + (lane / 5) * SU_CP + (lane % 5) * 16) = (u32x4){0u, 0u, 0u, 0u};
#pragma unroll
    for (int i = 0; i < 5; ++i) {
      const int g = lane + 64 * i;
      if (g < 257) {
        const u32x4 a = *(const u32x4*)(cb + SU_RAW + g * 16);
        const u32x2 c = *(const u32x2*)(cb + SU_RAW + g * 16 + 16);
        unsigned char* dst = cb + SU_U + 80 + g * 16;
        *(u32x4*)(dst) = a;
        *(u32x4*)(dst + SU_CP) = (u32x4){__builtin_amdgcn_alignbyte(a[1], a[0], 2), __builtin_amdgcn_alignbyte(a[2], a[1], 2), __builtin_amdgcn_alignbyte(a[3], a[2], 2), __builtin_amdgcn_alignbyte(c[0], a[3], 2)};
        *(u32x4*)(dst + 2 * SU_CP) = (u32x4){a[1], a[2], a[3], c[0]};
        *(u32x4*)(dst + 3 * SU_CP) = (u32x4){__builtin_amdgcn_alignbyte(a[2], a[1], 2), __builtin_amdgcn_alignbyte(a[3], a[2], 2), __builtin_amdgcn_alignbyte(c[0], a[3], 2), __builtin_amdgcn_alignbyte(c[1], c[0], 2)};
      }
    }
  }
  asm volatile("s_waitcnt lgkmcnt(0)" ::: "memory");
  SST(4)
  f32x16 cv0, cv1;
  {
    const int m0 = 48 - 15 + 8 * h + r;
    const int cbo = (int)(size_t)(LAS unsigned char*)cb;
    int ua = cbo + SU_U + (m0 & 3) * SU_CP + (m0 & ~3) * 2 - 32;
    const int kb = cbo + SU_K + 16 * h;
    int a1 = kb + (33 + r) * 80, a0 = kb + (1 + r) * 80, cnt = 32;
#define CRA(S, O) "ds_read_b64 " RNG("v", S, 2) ", %[ua] offset:" STR(O) "\n\t" "ds_read_b64 " RNG("v", (S) + 2, 2) ", %[ua] offset:" STR((O) + 8) "\n\t"
#define CRB(S, ad, O) "ds_read_b128 " RNG("v", S, 4) ", " ad " offset:" STR(O) "\n\t"
#define CMF(C, A, B) "v_mfma_f32_32x32x16_bf16 " RNG("v", C, 16) ", " RNG("v", A, 4) ", " RNG("v", B, 4) ", " RNG("v", C, 16) "\n\t"
#define CUPD "v_add_u32 %[ua], 64, %[ua]\n\t" "v_subrev_u32 %[a1], 80, %[a1]\n\t" "v_subrev_u32 %[a0], 80, %[a0]\n\t" "v_max_i32 %[a0], %[a0], %[kb]\n\t"
#define CHALF(S, N, O, UPD)                                                                       \
    WLG(5) CMF(64, S, (S) + 8) UPD CRA(N, O)                                                      \
    WLG(6) CMF(80, S, (S) + 16) CRB((N) + 8, "%[a0]", O) CRB((N) + 16, "%[a1]", O)                \
    WLG(6) CMF(80, (S) + 4, (S) + 8) CRA((N) + 4, (O) + 2048)                                     \
    WLG(7) CMF(64, S, (S) + 12) CRB((N) + 12, "%[a0]", (O) + 5200)                                \
    WLG(7) CMF(80, S, (S) + 20) CRB((N) + 20, "%[a1]", (O) + 5200)                                \
    CMF(80, (S) + 4, (S) + 12)
    asm volatile(
        CRA(96, 32) CRB(104, "%[a0]", 32) CRB(112, "%[a1]", 32) CRA(100, 32 + 2048) CRB(108, "%[a0]", 32 + 5200) CRB(116, "%[a1]", 32 + 5200)
        ZER(64) ZER(68) ZER(72) ZER(76) ZER(80) ZER(84) ZER(88) ZER(92)
        ".Lc%=:\n\t"
        CHALF(96, 120, 0, "")
        CHALF(120, 96, 32, CUPD)
        "s_sub_u32 %[cnt], %[cnt], 1\n\t"
        "s_cmp_lg_u32 %[cnt], 0\n\t"
        "s_cbranch_scc1 .Lc%=\n\t"
        "s_nop 7\n\t" "s_nop 3\n\t" WLG(0)
        : "=&{v[64:79]}"(cv0), "=&{v[80:95]}"(cv1), [ua] "+v"(ua), [a0] "+v"(a0), [a1] "+v"(a1), [cnt] "+s"(cnt)
        : [kb] "v"(kb)
        : CL96, CL112, CL120, CL8(128, 129, 130, 131, 132, 133, 134, 135), CL8(136, 137, 138, 139, 140, 141, 142, 143),
          CL8(144, 145, 146, 147, 148, 149, 150, 151), CL8(152, 153, 154, 155, 156, 157, 158, 159), "scc", "memory");
  }
  SST(5)
  {
    u16* yo = (u16*)(cb + SU_K);
#pragma unroll
    for (int g = 0; g < 2; ++g)
#pragma unroll
      for (int i = 0; i < 16; i += 4) {
        const int rr = 2 * i + 4 * h, a = 32 * g + r;
        const u32x2 uu = *(const u32x2*)(cb + SU_U + (48 + 32 * a + rr) * 2);
        float y[4];
#pragma unroll
        for (int e = 0; e < 4; ++e) {
          const float uf = __uint_as_float((e & 1) ? (uu[e >> 1] & 0xffff0000u) : (uu[e >> 1] << 16));
          y[e] = rbf_hw(g ? cv1[i + e] : cv0[i + e]) + rbf_hw(uf * dsk);
        }
        *(u32x2*)(yo + a * 40 + rr) = (u32x2){pack2(y[0], y[1]), pack2(y[2], y[3])};
      }
  }
  LBAR();
  SST(6)
#pragma unroll
  for (int i = 0; i < 4; ++i) {
    const int l = 2 * (tid + 256 * i);
    u32 c[4];
#pragma unroll
    for (int j = 0; j < 4; ++j) c[j] = *(const u32*)(sm + j * SU_CH + SU_K + ((l >> 5) * 40 + (l & 31)) * 2);
    __builtin_amdgcn_raw_buffer_store_b128((u32x4){__builtin_amdgcn_perm(c[1], c[0], 0x05040100u), __builtin_amdgcn_perm(c[3], c[2], 0x05040100u),
                                                   __builtin_amdgcn_perm(c[1], c[0], 0x07060302u), __builtin_amdgcn_perm(c[3], c[2], 0x07060302u)},
                                           rst, l * 8, SSM_L * SSM_D * 2 + d0 * 4096, 16);
  }
  asm volatile("s_waitcnt vmcnt(0)" ::: "memory");
  LBAR();
  if (tid == 0) __hip_atomic_fetch_add((u32*)(xb + XYDN), 1u, RLX_AGENT);
  SST(7)
}

#define L_TOTAL 163840
__device__ __forceinline__ int ldw(__amdgpu_buffer_rsrc_t rs, int off) {
  asm volatile("" ::: "memory");
  return __builtin_amdgcn_readfirstlane(__builtin_amdgcn_raw_buffer_load_b32(rs, 0, off, 16));
}
#define GF(rt) "v[64+8*(xk&7)+4*" #rt ":64+8*(xk&7)+4*" #rt "+3]"
#define GW2 "v[128+4*xk:128+4*xk+3]"
#define GW3 "v[24+4*(xk&1):24+4*(xk&1)+3]"
#define GLOAD(S, K) "s_xor_b32 %[tq], " S ", 1024*(" K ")\n\t"
#define GTR(K) "ds_read_b128 v[24+4*((" K ")&1):24+4*((" K ")&1)+3], %[wl] offset:1024*((" K ")&31)\n\t"
#define GPACE 0
#define YAUX 16
#define UDELAY 100
#define GNOSSM 1
#define GNG 3
#define GS0 22
__device__ __forceinline__ void g_role(unsigned char* lds, const u16* __restrict__ Wo, u16* __restrict__ out, __amdgpu_buffer_rsrc_t rsx,
                                       __amdgpu_buffer_rsrc_t rsf, int fb, u32 tag, int grp, int sl, int w) {
  const int lane = fresh_lane();
  const int lr = lane & 15, lg = lane >> 4;
  const int rot = (2 * sl + 8 * w) & 31;
  const int ldsb = (int)(size_t)(LAS unsigned char*)lds;
  u32x32 xw[8], w2[4];
  LBAR();
  {
    const __amdgpu_buffer_rsrc_t rw = __builtin_amdgcn_make_buffer_rsrc((void*)Wo, 0, DM * HD * 2, 0x00020000);
    const int vq = (lane >> 3) * (DM * 2) + sl * 128 + (lane & 7) * 16;
    const int sq = (1024 * w + rot * 32) * (DM * 2);
#define GSLOT(c) ((c) < 8 ? (((c) & 3) == 3 ? 131072 + w * 8192 : w * 32768 + 8192 * (((c) & 3) + 1)) : (((c) - 8) % 3 == 2 ? 131072 + w * 8192 : w * 32768 + 8192 * (((c) - 8) % 3 + 2)))
#define GISSUE(c) _Pragma("unroll") for (int j = 0; j < 8; ++j)                                                        \
      asm volatile("s_mov_b32 m0, %0\n\ts_nop 3\n\tbuffer_load_dwordx4 %1, %2, %3 offen lds"                           \
                   :: "s"(ldsb + GSLOT(c) + j * 1024), "v"(vq), "s"(rw), "s"(sq ^ ((64 * (c) + 8 * j) * DM * 2)) : "memory");
    GISSUE(0) GISSUE(1) GISSUE(2) GISSUE(3)
    const int q = lr >> 2, p = lr & 3;
    u32x4 w3[16];
#pragma unroll
    for (int c = 0; c < 16; ++c) {
      if (c < 5) waitvm<24>(); else if (c < 14) waitvm<16>(); else if (c == 14) waitvm<8>(); else waitvm<0>();
      u32x4 f3[2];
#pragma unroll
      for (int s = 0; s < 2; ++s)
#pragma unroll
        for (int t = 0; t < 4; ++t) {
          const unsigned char* base = lds + GSLOT(c) + (32 * s + 8 * lg + q) * 128 + (t < 2 ? p * 16 + t * 8 : t * 32 + p * 8);
          const u32x2 t0 = __builtin_bit_cast(u32x2, __builtin_amdgcn_ds_read_tr16_b64_v4i16((LAS v4i16_t*)(base)));
          const u32x2 t1 = __builtin_bit_cast(u32x2, __builtin_amdgcn_ds_read_tr16_b64_v4i16((LAS v4i16_t*)(base + 512)));
          const int o = t < 2 ? ((2 * c + s) * 2 + t) * 4 : (2 * c + s) * 4;
          if (t < 2) { xw[o >> 5][(o & 31) + 0] = t0[0]; xw[o >> 5][(o & 31) + 1] = t0[1]; xw[o >> 5][(o & 31) + 2] = t1[0]; xw[o >> 5][(o & 31) + 3] = t1[1]; }
          else if (t == 2) { w2[o >> 5][(o & 31) + 0] = t0[0]; w2[o >> 5][(o & 31) + 1] = t0[1]; w2[o >> 5][(o & 31) + 2] = t1[0]; w2[o >> 5][(o & 31) + 3] = t1[1]; }
          else f3[s] = (u32x4){t0[0], t0[1], t1[0], t1[1]};
        }
      asm volatile("s_waitcnt lgkmcnt(0)" ::: "memory");
      if (c < 8) { *(u32x4*)(lds + w * 32768 + (2 * c) * 1024 + lane * 16) = f3[0]; *(u32x4*)(lds + w * 32768 + (2 * c + 1) * 1024 + lane * 16) = f3[1]; }
      else { w3[2 * (c - 8)] = f3[0]; w3[2 * (c - 8) + 1] = f3[1]; }
      if (GPACE) __builtin_amdgcn_s_sleep(GPACE);
      if (c < 4) { GISSUE(c + 4) } else if (c >= 5 && c < 13) { GISSUE(c + 3) }
    }
#pragma unroll
    for (int i = 0; i < 16; ++i) *(u32x4*)(lds + w * 32768 + (16 + i) * 1024 + lane * 16) = w3[i];
    LBAR();
  }
  wait_start(rsx, fb, tag, true);
  const int wl = ldsb + w * 32768 + lane * 16;
  const __amdgpu_buffer_rsrc_t rso = __builtin_amdgcn_make_buffer_rsrc((void*)out, 0, S_LEN * OUTLD * 2, 0x00020000);
  const int rw_ = ldsb + 131072 + w * 8192 + lane * 16, rr_ = ldsb + 131072 + w * 2048 + lane * 16;
  const int oa = ((16 * (w & 1) + lr) * OUTLD + 64 * sl + (w < 2 ? 8 * lg : 32 + 4 * lg)) * 2, ob = oa + (w < 2 ? 8 : 32);
  const int hq = (grp + GNG - (62 - GS0) % GNG) % GNG - 1, hf = hq < 2 ? hq : -1;
#define GRW(i) "ds_write_b128 %[rw], " RNG("v", 32 + 4 * (i), 4) " offset:" STR(1024 * (i)) "\n\t"
#define GRR(i) "ds_read_b128 " RNG("v", 32 + 4 * (i), 4) ", %[rr] offset:" STR(8192 * ((i) >> 1) + 1024 * ((i) & 1)) "\n\t"
#define GNXT(D, S) "s_cmp_lt_u32 " S ", " STR(GS0) "\n\t" "s_cselect_b32 " D ", 1, " STR(GNG) "\n\t" "s_add_u32 " D ", " D ", " S "\n\t"
#define GDN(S) "s_min_u32 s41, " S ", 63\n\t" "s_lshl_b32 s41, s41, 5\n\t" "v_add_u32 v23, s41, %[u0]\n\t" "v_lshrrev_b32 v22, 6, v23\n\t" \
               "v_and_b32 v23, 63, v23\n\t" "v_lshlrev_b32 v23, 7, v23\n\t" "s_lshr_b32 s41, s41, 6\n\t" "s_max_u32 s41, s41, 29\n\t" "s_lshl_b32 s41, s41, 13\n\t" "s_add_u32 s41, s41, %[pa]\n\t" \
               "buffer_load_dword v23, v23, %[rs], s41 offen sc1\n\t"
#define GOK(D) "v_cmp_le_i32 vcc, v23, v22\n\t" "s_cmp_eq_u64 vcc, 0\n\t" "s_cselect_b32 " D ", 1, 0\n\t"
static_assert(NAW == 64, "shifts above");
#define GEPI(MID, OA, OB) \
      GRW(0) GRW(1) GRW(2) GRW(3) GRW(4) GRW(5) GRW(6) GRW(7) \
      MID \
      WLG(0) "s_barrier\n\t" \
      GRR(0) GRR(1) GRR(2) GRR(3) GRR(4) GRR(5) GRR(6) GRR(7) \
      WLG(4) PKA(32, 32, 40) PKA(34, 34, 42) PKA(36, 36, 44) PKA(38, 38, 46) \
      WLG(0) PKA(48, 48, 56) PKA(50, 50, 58) PKA(52, 52, 60) PKA(54, 54, 62) \
      PKA(32, 32, 48) PKA(34, 34, 50) PKA(36, 36, 52) PKA(38, 38, 54) \
      "v_cvt_pk_bf16_f32 v20, v32, v33\n\t" "v_cvt_pk_bf16_f32 v21, v34, v35\n\t" "v_cvt_pk_bf16_f32 v18, v36, v37\n\t" "v_cvt_pk_bf16_f32 v19, v38, v39\n\t" \
      "buffer_store_dwordx2 v[20:21], " OA ", %[ro], s42 offen\n\t" \
      "buffer_store_dwordx2 v[18:19], " OB ", %[ro], s42 offen\n\t"
#define GH "v[64+4*(xk&15):64+4*(xk&15)+3]"
  asm volatile(
      "s_nop 4\n\t"
      "v_add_u32 v16, 131072, %[va]\n\t" "v_sub_u32 v15, %[ob], %[oa]\n\t" "v_add_u32 v15, v15, %[oc]\n\t"
      "s_mov_b32 s36, %[s0]\n\t" "s_mov_b32 s43, " STR(SPIN_CAP) "\n"
      ".Lp%=:\n\t"
      GDN("s36") WVM(0) GOK("s40")
      "s_cbranch_scc1 .Lr%=\n\t"
      "s_sleep 2\n\t" "s_sub_u32 s43, s43, 1\n\t" "s_cmp_gt_i32 s43, 0\n\t" "s_cbranch_scc1 .Lp%=\n"
      ".Lr%=:\n\t"
      GNXT("s37", "s36") GDN("s37")
      "s_lshl_b32 s38, s36, 18\n\t" "s_add_u32 s38, s38, %[gb]\n\t"
      ".set xk, 0\n\t.rept 8\n\t" GLOAD("s38", "xk")
      "buffer_load_dwordx4 " GF(0) ", %[va], %[rf], %[tq] offen sc1\n\t" "buffer_load_dwordx4 " GF(1) ", v16, %[rf], %[tq] offen sc1\n\t"
      ".set xk, xk+1\n\t.endr\n\t"
      WVM(16) GOK("s40")
      ".Lb%=:\n\t"
      GNXT("s37", "s36") GNXT("s41", "s37") GDN("s41") GTR("0")
      "s_cmp_lt_u32 s37, 63\n\t" "s_cselect_b32 s40, s40, 0\n\t"
      "s_cmp_lg_u32 s40, 0\n\t" "s_cselect_b32 s39, s37, s36\n\t"
      "s_lshl_b32 s39, s39, 18\n\t" "s_add_u32 s39, s39, %[gb]\n\t" "s_lshl_b32 s38, s36, 18\n\t" "s_add_u32 s38, s38, %[gb]\n\t"
      "s_lshl_b32 s42, s36, 17\n\t"
      "buffer_load_dword v17, %[vt], %[rf], s39 offen sc1\n\t"
      ".set xk, 0\n\t.rept 32\n\t"
      "s_waitcnt vmcnt(14)\n\t"
      XM1(XA(32, 0), XW(0, 0), GF(0), 1) GTR("xk+1") XM1(XA(32, 4), XW(0, 1), GF(0), 1)
      ".if xk < 24\n\t" GLOAD("s38", "xk+8") ".else\n\t" GLOAD("s39", "xk-24") ".endif\n\t"
      XM1(XA(32, 8), XW(0, 0), GF(1), 1) XM1(XA(32, 12), XW(0, 1), GF(1), 1)
      XM1(XA(32, 16), GW2, GF(0), 1) "s_waitcnt lgkmcnt(1)\n\t" XM1(XA(32, 20), GW3, GF(0), 1)
      "buffer_load_dwordx4 " GF(0) ", %[va], %[rf], %[tq] offen sc1\n\t"
      XM1(XA(32, 24), GW2, GF(1), 1) XM1(XA(32, 28), GW3, GF(1), 1)
      "buffer_load_dwordx4 " GF(1) ", v16, %[rf], %[tq] offen sc1\n\t"
      ".if xk == 16\n\t" "s_barrier\n\t" ".endif\n\t"
      ".set xk, xk+1\n\t.endr\n\t"
      GEPI(GOK("s41"), "%[oa]", "%[ob]")
      "s_mov_b32 s36, s37\n\t"
      "s_cmp_lg_u32 s40, 0\n\t" "s_mov_b32 s40, s41\n\t" "s_cbranch_scc1 .Lb%=\n\t"
      "s_cmp_lt_u32 s36, 63\n\t" "s_cbranch_scc1 .Lp%=\n\t"
      "s_cmp_lt_i32 %[hf], 0\n\t" "s_cbranch_scc1 .Le%=\n\t"
      "s_mov_b32 s36, 63\n"
      ".Lh%=:\n\t"
      GDN("s36") WVM(0) GOK("s40")
      "s_cbranch_scc1 .Li%=\n\t"
      "s_sleep 2\n\t" "s_sub_u32 s43, s43, 1\n\t" "s_cmp_gt_i32 s43, 0\n\t" "s_cbranch_scc1 .Lh%=\n"
      ".Li%=:\n\t"
      "s_lshl_b32 s38, %[hf], 17\n\t" "s_add_u32 s38, s38, 63*262144\n\t" "s_add_u32 s38, s38, %[gb]\n\t"
      ".set xk, 0\n\t.rept 16\n\t" GLOAD("s38", "xk") "buffer_load_dwordx4 " GH ", %[va], %[rf], %[tq] offen sc1\n\t" ".set xk, xk+1\n\t.endr\n\t"
      GTR("0")
      "s_lshl_b32 s42, %[hf], 16\n\t" "s_add_u32 s42, s42, 63*131072\n\t"
      ".set xk, 0\n\t.rept 32\n\t"
      ".if xk < 17\n\t" "s_waitcnt vmcnt(15)\n\t" ".else\n\t" "s_waitcnt vmcnt(31-xk)\n\t" ".endif\n\t"
      XM1(XA(32, 0), XW(0, 0), GH, 1) GTR("xk+1") XM1(XA(32, 4), XW(0, 1), GH, 1)
      XM1(XA(32, 16), GW2, GH, 1) "s_waitcnt lgkmcnt(1)\n\t" XM1(XA(32, 20), GW3, GH, 1)
      ".if xk < 16\n\t" GLOAD("s38", "xk+16") "buffer_load_dwordx4 " GH ", %[va], %[rf], %[tq] offen sc1\n\t" ".endif\n\t"
      ".if xk == 16\n\t" "s_barrier\n\t" ".endif\n\t"
      ".set xk, xk+1\n\t.endr\n\t"
      "s_nop 7\n\t"
      GEPI("", "%[oc]", "v15")
      ".Le%=:\n\t"
      WVM(0)
      : [tq] "=&s"(sl)
      : [rs] "s"(rsx), [s0] "s"(__builtin_amdgcn_readfirstlane(grp ? GS0 + grp : 0)), [gb] "s"(FO_O - 32768 + (32 * w + rot) * 1024), [va] "v"(lane * 16 + 32768),
        [rf] "s"(rsf), [wl] "v"(wl), [rw] "v"(rw_), [rr] "v"(rr_), [oa] "v"(oa), [ob] "v"(ob), [ro] "s"(rso), [u0] "v"(8 * w + (lane & 7)), [pa] "s"(PGO_A - 29 * 8192), [hf] "s"(__builtin_amdgcn_readfirstlane(hf)), [oc] "v"((w & 1) ? OOR - 64 : oa), [vt] "v"((sl >> 3) * 131072 + 32768 + (sl & 7) * 4096 + lane * 64 - rot * 1024),
        "{a[0:31]}"(xw[0]), "{a[32:63]}"(xw[1]), "{a[64:95]}"(xw[2]), "{a[96:127]}"(xw[3]),
        "{a[128:159]}"(xw[4]), "{a[160:191]}"(xw[5]), "{a[192:223]}"(xw[6]), "{a[224:255]}"(xw[7]),
        "{v[128:159]}"(w2[0]), "{v[160:191]}"(w2[1]), "{v[192:223]}"(w2[2]), "{v[224:255]}"(w2[3])
      : "v15", "v16", "v17", "v18", "v19", "v20", "v21", "v22", "v23", "vcc", CL8(24, 25, 26, 27, 28, 29, 30, 31), CL8(32, 33, 34, 35, 36, 37, 38, 39), CL8(40, 41, 42, 43, 44, 45, 46, 47),
        CL8(48, 49, 50, 51, 52, 53, 54, 55), CL8(56, 57, 58, 59, 60, 61, 62, 63), CL64, CL72, CL80, CL88, CL96, CL112, CL120,
        "s36", "s37", "s38", "s39", "s40", "s41", "s42", "s43", "scc", "memory");
}
static_assert(4 * SU_CH <= L_TOTAL && 4 * XSLOT <= L_TOTAL, "lds");
__global__ void __launch_bounds__(256, 1)
main_kernel(const u16* __restrict__ x, const u16* __restrict__ mem0, const u16* __restrict__ Wq, const u16* __restrict__ Wk, const u16* __restrict__ Wv, const u16* __restrict__ Wo,
            unsigned char* ws, const u16* __restrict__ u, const u16* __restrict__ A_re, const u16* __restrict__ A_im, const u16* __restrict__ B_re,
            const u16* __restrict__ B_im, const u16* __restrict__ C_re, const u16* __restrict__ C_im,
            const u16* __restrict__ log_dt, const u16* __restrict__ Dskip, u16* __restrict__ out
            ) {
  __shared__ __attribute__((aligned(8192))) unsigned char lds[L_TOTAL];
  const int w = __builtin_amdgcn_readfirstlane(threadIdx.x >> 6);
  u64 pc0;
  int clen;
  {
    asm volatile("s_getpc_b64 %0\n.Lkpc0:\n\ts_mov_b32 %1, .Lkpc1-.Lkpc0" : "=s"(pc0), "=s"(clen));
  }
#define LOCAL_IDS const int lane = fresh_lane(), tid = w * 64 + lane; (void)tid;
  const int si = (blockIdx.x >> 3) * 4 + (blockIdx.x & 3);
  unsigned char* xb = (unsigned char*)(((size_t)ws + XPAGE - 1) & ~(size_t)(XPAGE - 1));
  const int fb = (int)(ws + WS_F - xb);
  u32 tag;
  asm volatile("s_load_dword %0, %1, 0x0\n\ts_waitcnt lgkmcnt(0)\n\ts_add_u32 %0, %0, 256" : "=s"(tag) : "s"(ws + WS_F) : "scc", "memory");
  const __amdgpu_buffer_rsrc_t rsx = __builtin_amdgcn_make_buffer_rsrc(xb, 0, fb + (int)WS_F_SZ, 0x00020000);
  const __amdgpu_buffer_rsrc_t rsf = __builtin_amdgcn_make_buffer_rsrc(ws + WS_QX, 0, (int)(4 * WS_QKV_SZ + 2 * WS_KM_SZ), 0x00020000);
  const __amdgpu_buffer_rsrc_t rst = __builtin_amdgcn_make_buffer_rsrc(ws + WS_UT, 0, 2 * SSM_L * SSM_D * 2 + SSM_D * 3072, 0x00020000);
  if ((blockIdx.x & 7) < 4) {
    { LOCAL_IDS
    r_role(lds, si, mem0, Wq, Wk, Wv, Wo, fb, tag, pc0, clen, rsx, rsf, rst, SsmPtrs{A_re, A_im, B_re, B_im, C_re, C_im, log_dt}, tid, lane, w
           ); }
    LOCAL_IDS
    {
      for (int it = 0; it < SPIN_CAP; ++it) {
        if (ldw(rsx, XYDN) >= 255) break;
        asm volatile("s_sleep 8" ::: "memory");
      }
      asm volatile("" ::: "memory");
      asm volatile(".Lkpc1:" ::: "memory");
      unsigned char* im = lds + 65536;
      const __amdgpu_buffer_rsrc_t rso = __builtin_amdgcn_make_buffer_rsrc((void*)out, 0, S_LEN * OUTLD * 2, 0x00020000);
      u32x4 t[8];
#pragma unroll
      for (int i = 0; i < 8; ++i) { const int p = i * 256 + tid; t[i] = __builtin_amdgcn_raw_buffer_load_b128(rst, (p >> 3) * 16384 + (p & 7) * 16, SSM_L * SSM_D * 2 + si * 128, 16); }
#pragma unroll
      for (int i = 0; i < 8; ++i) {
        const int p = i * 256 + tid, un = p >> 3, rp = p & 7;
        *(u32x2*)(im + (2 * rp) * 2064 + un * 8) = (u32x2){t[i][0], t[i][1]};
        *(u32x2*)(im + (2 * rp + 1) * 2064 + un * 8) = (u32x2){t[i][2], t[i][3]};
      }
      LBAR();
#pragma unroll
      for (int i = 0; i < 8; ++i) {
        const int p = i * 256 + tid;
        __builtin_amdgcn_raw_buffer_store_b128(*(const u32x4*)(im + (p >> 7) * 2064 + (p & 127) * 16), rso, ((p >> 7) * OUTLD + DM + (p & 127) * 8) * 2, si * 16 * OUTLD * 2, YAUX);
      }
    }
  } else {
    const int g = blockIdx.x & 3, li = blockIdx.x >> 3;
    const int xi = g == 0 ? (li < 16 ? li : -1) : (li < 26 + (g < 3) ? 16 + 27 * (g - 1) + li : -1);
    const int ei = g == 0 ? li - 16 : 16 + 5 * (g - 1) + (31 - li);
    if (xi >= 0) {
      LOCAL_IDS
      const int head = xi & 31, mi = xi >> 5;
      u32* prog = (u32*)(xb + PGO_X + xi * 128);
      if (mi == 1) x_role<true>(lds, x, Wv, FO_V, head, rsf, prog, tag, lane, w);
      else x_role<false>(lds, x, mi == 0 ? Wk : Wq, mi == 0 ? FO_K : FO_Q, head, rsf, prog, tag, lane, w);
    } else {
      LOCAL_IDS
      u32 acc = 0;
#pragma unroll
      for (int i = 0; i < 4; ++i) acc += *(const u32*)(x + (size_t)((i * 32 + ei) * 256 + tid) * 64);
#define UV 0
#define WWARM(n, first) {    \
        const __amdgpu_buffer_rsrc_t rwo = __builtin_amdgcn_make_buffer_rsrc((void*)Wo, 0, DM * HD * 2, 0x00020000); \
        const int ldsb = (int)(size_t)(LAS unsigned char*)lds; \
        _Pragma("unroll 1") for (int c = 0; c < (n); ++c) { dma16(rwo, lane * 16, ((first) + c) * 16384, 1024, ldsb + w * 32768 + (c & 1) * 16384); waitvm<32>(); } }
      if (UV == 0 || (UV == 1 && ei < 16)) WWARM(4, (ei * 4 + w) * 4)
      if (UV == 2 && ei < 16) WWARM(8, (ei * 4 + w) * 8)
      if (ei == 0 && tid < 16) acc += *(const u32*)(Dskip + tid * 64);
      asm volatile("" :: "v"(acc));
      if (ei >= 16) {
      { const u32 t0 = rt32(); for (int i = 0; i < 1000 && (int)(rt32() - t0) < UDELAY; ++i) asm volatile("s_sleep 16" ::: "memory"); }
      asm volatile("s_waitcnt vmcnt(0)\n\ts_barrier" ::: "memory");
      for (int ps = 0; ps < 2; ++ps) {
      const int r0 = ((ei - 16) * 2 + ps) * 64;
#pragma unroll
      for (int hb = 0; hb < 2; ++hb) {
        u32x4 t[16];
#pragma unroll
        for (int i = 0; i < 16; ++i) t[i] = *(const u32x4*)(u + (size_t)r0 * SSM_D + ((hb * 16 + i) * 256 + tid) * 8);
#pragma unroll
        for (int i = 0; i < 16; ++i) { const int p = (hb * 16 + i) * 256 + tid; *(u32x4*)(lds + (p >> 7) * 2064 + (p & 127) * 16) = t[i]; }
      }
      LBAR();
      for (int i = 0; i < 32; ++i) {
        const int p = i * 256 + tid, un = p >> 5, rp = p & 31;
        const u32x2 a = *(const u32x2*)(lds + (2 * rp) * 2064 + un * 8), c = *(const u32x2*)(lds + (2 * rp + 1) * 2064 + un * 8);
        __builtin_amdgcn_raw_buffer_store_b128((u32x4){a[0], a[1], c[0], c[1]}, rst, un * 16384 + rp * 16, r0 * 8, 16);
      }
      asm volatile("s_waitcnt vmcnt(0) lgkmcnt(0)\n\ts_barrier" ::: "memory");
      }
      if (UV == 1) { WWARM(4, (ei * 4 + w) * 4) waitvm<0>(); }
      wait_start(rsx, fb, tag, true);
      if (tid == 0) __hip_atomic_fetch_add((u32*)(xb + XUDN), 1u, RLX_AGENT);
      }
    }
    asm volatile("s_waitcnt lgkmcnt(0)\n\ts_barrier" :: "s"(tag) : "memory");
    if (w == 0 && fresh_lane() == 0) __builtin_amdgcn_raw_buffer_store_b32(tag, rsx, 0, PGO_S + si * 128, 16);
    const bool isg = g == 0 ? li >= 16 : (g >= 5 - GNG && li < 16);
    if (xi >= 0 && !(GNOSSM && isg)) {
      LOCAL_IDS
      bool uok = false;
      wait_start(rsx, fb, tag, true);
      while (true) {
        LBAR();
        if (tid == 0) *(u32*)(lds + L_TOTAL - 16) = __hip_atomic_fetch_add((u32*)(xb + XTKT), 1u, RLX_AGENT) + 1u;
        LBAR();
        const int un = __builtin_amdgcn_readfirstlane(*(const u32*)(lds + L_TOTAL - 16));
        if (un >= 256) break;
        ssm_unit(lds, 4 * un, rst, rsx, xb, fb, uok, Dskip, tid, lane, w);
        if (isg) break;
      }
    }
    if (isg) {
      g_role(lds, Wo, out, rsx, rsf, fb, tag, g ? g + GNG - 4 : 0, li & 15, w);
    }
    else {
      const int lane = fresh_lane();
      int sh = (ei - 16) * 4 + w, lim = (NSEG - 4) * NH, wi = sh, dn = 0;
      bool go = xi < 0;
      while (true) {
        if (go) a_role(rsx, rsf, sh, lim, wi, dn, lane, fb, tag);
        if (xi >= 0 && (w >= 2 || go)) break;
        const int t = __builtin_amdgcn_readfirstlane(lane == 0 ? __hip_atomic_fetch_add((u32*)(xb + XTK2), 1u, RLX_AGENT) + 1u : 0u);
        if (t >= 128) break;
        wi = 64 + t; sh = (NSEG - 4) * NH + t; lim = sh + 1; dn = 31; go = true;
      }
    }
  }
}

extern "C" void kernel_launch(void* const* d_in, const int* in_sizes, int n_in, void* d_out, int out_size,
                              void* d_ws, size_t ws_size, hipStream_t stream) {
  const u16* x = (const u16*)d_in[0];
  const u16* mem0 = (const u16*)d_in[1];
  const u16* Wq = (const u16*)d_in[2];
  const u16* Wk = (const u16*)d_in[3];
  const u16* Wv = (const u16*)d_in[4];
  const u16* Wo = (const u16*)d_in[5];
  const u16* u = (const u16*)d_in[7];
  const u16* A_re = (const u16*)d_in[8];
  const u16* A_im = (const u16*)d_in[9];
  const u16* B_re = (const u16*)d_in[10];
  const u16* B_im = (const u16*)d_in[11];
  const u16* C_re = (const u16*)d_in[12];
  const u16* C_im = (const u16*)d_in[13];
  const u16* log_dt = (const u16*)d_in[14];
  const u16* Dskip = (const u16*)d_in[15];
  u16* out = (u16*)d_out;
  char* ws = (char*)d_ws;

  main_kernel<<<256, 256, 0, stream>>>(x, mem0, Wq, Wk, Wv, Wo, (unsigned char*)ws, u, A_re, A_im, B_re, B_im, C_re, C_im, log_dt, Dskip, out
      );
}
```

```cpp
#include <hip/hip_runtime.h>
#include <hip/hip_bf16.h>
#include <stdint.h>

#define S_LEN 2048
#define DM 1024
#define NH 32
#define DH 128
#define HD 4096
#define MEM 8
#define SEG 32
#define NSEG 64
#define LTOK 40
#define SSM_L 2048
#define SSM_D 1024
#define SSM_N 128
#define OUTLD 2048

typedef unsigned short u16;
typedef unsigned int u32;
typedef unsigned long long u64;
typedef __attribute__((ext_vector_type(8))) short bf16x8;
typedef __attribute__((ext_vector_type(4))) float f32x4;
typedef __attribute__((ext_vector_type(4))) unsigned int u32x4;
typedef __attribute__((ext_vector_type(2))) unsigned int u32x2;
typedef __bf16 bf16x2_t __attribute__((ext_vector_type(2)));
typedef short v4i16_t __attribute__((ext_vector_type(4)));

#define SENT 0xFFFFFFFFu
#define RLX_AGENT __ATOMIC_RELAXED, __HIP_MEMORY_SCOPE_AGENT

__device__ __forceinline__ float bf2f(u16 v) { return __uint_as_float(((u32)v) << 16); }
__device__ __forceinline__ u16 f2bf(float f) {
  u32 u = __float_as_uint(f);
  u32 r = (u + 0x7fffu + ((u >> 16) & 1u)) >> 16;
  if ((u & 0x7fffffffu) > 0x7f800000u) r = 0x7fc0u;
  return (u16)r;
}
__device__ __forceinline__ float rbf(float f) { return bf2f(f2bf(f)); }

#define TR 8
#define TP 178
#define TDB 61
#define TDC 109
#define NR1 4
#define XPAGE (2 << 20)
#define XRING 3
#define XLP(p, L) ((((L) >> 13) << 15) + ((p) << 14) + ((L) & 8191))
#define XO_R3 131072
#define XR1A(x, L) XLP((x) & 1, ((x) >> 1) * 16384 + (L))
#define XR3A(rg, L) (XO_R3 + XLP(rg, L))
#define XSB(s) (((s) % XRING) == 2 ? 1048576 : ((s) % XRING) * 262144)
#define XR2B 524288
#define XR2S 1048576
#define XR2(j) (((((j) & 31) + (((j) >> 6) << 5)) / 6 << 15) + (((((j) >> 5) & 1) ^ 1) << 14) + ((((j) & 31) + (((j) >> 6) << 5)) % 6) * 1280)
#define XSYN (1048576 + 262144)
#define PGO_R (XSYN + 32768)
#define XCAL (PGO_R + 32768)
static_assert(XCAL + 16384 <= XPAGE, "exchange page");
#define WS_X 0ull
#define WS_X_SZ ((u64)2 * XPAGE)
#define WS_F (WS_X + WS_X_SZ)
#define XEPO (fb)
#define XSTA (fb + 128)
#define PGO_X (fb + 256)
#define PGO_Y (PGO_X + 96 * 128)
#define PGO_S (PGO_Y + 128 * 128)
#define PGO_D (PGO_S + 128 * 128)
#define PGO_A (PGO_D + 128 * 128)
#define XTKT (PGO_A + 192 * 128)
#define XUDN (XTKT + 128)
#define XYDN (XUDN + 128)
#define XTK2 (XYDN + 128)
#define WS_F_SZ ((u64)(2 + 96 + 128 + 128 + 128 + 192 + 8) * 128)
#define WS_XCH_END (WS_F + 131072)
#define WS_QX (WS_XCH_END)
#define WS_QKV_SZ ((u64)S_LEN * HD * 2)
#define WS_KX (WS_QX + WS_QKV_SZ)
#define WS_VX (WS_KX + WS_QKV_SZ)
#define WS_OX (WS_VX + WS_QKV_SZ)
#define WS_KM (WS_OX + WS_QKV_SZ)
#define WS_KM_SZ ((u64)NSEG * 8 * HD * 2)
#define WS_VM (WS_KM + WS_KM_SZ)
#define WS_UT (WS_VM + WS_KM_SZ)
#define WS_YT (WS_UT + (u64)SSM_L * SSM_D * 2)
#define WS_KD (WS_YT + (u64)SSM_L * SSM_D * 2)
#define WS_TOTAL (WS_KD + (u64)SSM_D * 3072)

#define SPIN_CAP 400000
#define TRY_CAP 20000
#define LAS __attribute__((address_space(3)))
#define LBAR() asm volatile("s_waitcnt lgkmcnt(0)\n\ts_barrier" ::: "memory")
__device__ __forceinline__ u32 max4u(u32x4 v) { return max(max(v[0], v[1]), max(v[2], v[3])); }
typedef float f32x2_t __attribute__((ext_vector_type(2)));
__device__ __forceinline__ u32 pack2(float a, float b) {
  f32x2_t v = {a, b};
  bf16x2_t r = __builtin_convertvector(v, bf16x2_t);
  return __builtin_bit_cast(u32, r);
}
__device__ __forceinline__ float rbf_hw(float a) { return __uint_as_float(pack2(a, a) & 0xffff0000u); }
__device__ __forceinline__ u32 nosent(u32 v) { return min(v, 0xFFFFFFFEu); }
template <int N> __device__ __forceinline__ void waitvm() { asm volatile("s_waitcnt vmcnt(%0)" ::"n"(N) : "memory"); }
typedef __attribute__((ext_vector_type(16))) float f32x16;
__device__ __forceinline__ u32 split_hl(float kv) {
  const u32 h = pack2(kv, kv) & 0xffffu;
  const float r1 = kv - __uint_as_float(h << 16);
  return h | (pack2(r1, r1) & 0xffff0000u);
}
__device__ __forceinline__ int kidx(int idx) {
  const int sh = idx < 256 ? 0 : (idx < 512 ? 1 : (idx < 1024 ? 2 : 3));
  const int q = sh ? ((idx + (1 << (sh - 1)) - 1 + ((idx >> sh) & 1)) >> sh) : idx;
  return q + 128 * sh;
}

#define FO_Q 0
#define FO_K ((int)WS_QKV_SZ)
#define FO_V ((int)(2 * WS_QKV_SZ))
#define FO_O ((int)(3 * WS_QKV_SZ))
#define FO_KM ((int)(4 * WS_QKV_SZ))
#define FO_VM ((int)(4 * WS_QKV_SZ + WS_KM_SZ))
__device__ __forceinline__ u32x4 wait_start(__amdgpu_buffer_rsrc_t rsx, int fb, u32 tag, bool slow) {
  u32x4 st;
  for (int it = 0; it < SPIN_CAP; ++it) {
    asm volatile("" ::: "memory");
    st = __builtin_amdgcn_raw_buffer_load_b128(rsx, 0, XSTA, 16);
    if ((u32)__builtin_amdgcn_readfirstlane(st[3]) == tag) break;
    if (slow) asm volatile("s_sleep 20" ::: "memory"); else asm volatile("s_sleep 3" ::: "memory");
  }
  asm volatile("" ::: "memory");
  return st;
}
__device__ __forceinline__ int xprog(u32 v, u32 tag) { return v - tag <= 80u ? (int)(v - tag) : -1; }
__device__ __forceinline__ float xmax4(float m) {
  { auto r = __builtin_amdgcn_permlane16_swap(__float_as_uint(m), __float_as_uint(m), false, false); m = __builtin_fmaxf(__uint_as_float(r[0]), __uint_as_float(r[1])); }
  { auto r = __builtin_amdgcn_permlane32_swap(__float_as_uint(m), __float_as_uint(m), false, false); m = __builtin_fmaxf(__uint_as_float(r[0]), __uint_as_float(r[1])); }
  return m;
}
__device__ __forceinline__ float xsum4(float m) {
  { auto r = __builtin_amdgcn_permlane16_swap(__float_as_uint(m), __float_as_uint(m), false, false); m = __uint_as_float(r[0]) + __uint_as_float(r[1]); }
  { auto r = __builtin_amdgcn_permlane32_swap(__float_as_uint(m), __float_as_uint(m), false, false); m = __uint_as_float(r[0]) + __uint_as_float(r[1]); }
  return m;
}
#define MF16(a, b, c) __builtin_amdgcn_mfma_f32_16x16x32_bf16(__builtin_bit_cast(bf16x8, a), __builtin_bit_cast(bf16x8, b), c, 0, 0, 0)
__device__ __forceinline__ f32x4 rnd2(f32x4 v, float sc) {
  const u32 a = pack2(v[0], v[1]), b = pack2(v[2], v[3]);
  const u32 c = pack2(__uint_as_float(a << 16) * sc, __uint_as_float(a & 0xffff0000u) * sc), d = pack2(__uint_as_float(b << 16) * sc, __uint_as_float(b & 0xffff0000u) * sc);
  return (f32x4){__uint_as_float(c << 16), __uint_as_float(c & 0xffff0000u), __uint_as_float(d << 16), __uint_as_float(d & 0xffff0000u)};
}
struct XaOps { u32x4 q[2][4], k[2][4], v[8]; };
__device__ __forceinline__ void xa_load(XaOps& o, __amdgpu_buffer_rsrc_t rsf, int sh, int lane) {
#pragma unroll
  for (int i = 0; i < 8; ++i) {
    o.q[i >> 2][i & 3] = __builtin_amdgcn_raw_buffer_load_b128(rsf, lane * 16, FO_Q + (sh * 8 + i) * 1024, 16);
    o.k[i >> 2][i & 3] = __builtin_amdgcn_raw_buffer_load_b128(rsf, lane * 16, FO_K + (sh * 8 + i) * 1024, 16);
  }
#pragma unroll
  for (int dt = 0; dt < 8; ++dt) o.v[dt] = __builtin_amdgcn_raw_buffer_load_b128(rsf, lane * 16, FO_V + (sh * 8 + dt) * 1024, 16);
}
__device__ __forceinline__ void xa_compute(const XaOps& o, const u32x4 (&km)[4], const u32x2 (&vm)[8], __amdgpu_buffer_rsrc_t rsf, int sh, int lane) {
  const int lr = lane & 15, lg = lane >> 4;
  const float scale = 0.08838834764831845f, L2E = 1.4426950408889634f, NEG = -1000000000.0f;
  const f32x4 z = (f32x4){0, 0, 0, 0};
  f32x4 s00 = z, s0m = z, s10 = z, s11 = z, s1m = z;
#pragma unroll
  for (int ks = 0; ks < 4; ++ks) { s00 = MF16(o.k[0][ks], o.q[0][ks], s00); s10 = MF16(o.k[0][ks], o.q[1][ks], s10); s11 = MF16(o.k[1][ks], o.q[1][ks], s11); }
#pragma unroll
  for (int ks = 0; ks < 4; ++ks) { s0m = MF16(km[ks], o.q[0][ks], s0m); s1m = MF16(km[ks], o.q[1][ks], s1m); }
  s00 = rnd2(s00, scale); s10 = rnd2(s10, scale); s11 = rnd2(s11, scale); s0m = rnd2(s0m, scale); s1m = rnd2(s1m, scale);
  asm volatile("" : "+v"(s00), "+v"(s11), "+v"(s0m), "+v"(s1m));
#pragma unroll
  for (int e = 0; e < 4; ++e) {
    const bool msk = 4 * lg + e > lr;
    s00[e] = msk ? NEG : s00[e]; s11[e] = msk ? NEG : s11[e];
    s0m[e] = lg >= 2 ? -INFINITY : s0m[e]; s1m[e] = lg >= 2 ? -INFINITY : s1m[e];
  }
  float m0 = NEG, m1 = -INFINITY;
#pragma unroll
  for (int e = 0; e < 4; ++e) { m0 = __builtin_fmaxf(m0, __builtin_fmaxf(s00[e], s0m[e])); m1 = __builtin_fmaxf(__builtin_fmaxf(m1, s10[e]), __builtin_fmaxf(s11[e], s1m[e])); }
  m0 = xmax4(m0); m1 = xmax4(m1);
  float t0 = 0.f, t1 = 0.f;
#pragma unroll
  for (int e = 0; e < 4; ++e) {
    s00[e] = __builtin_amdgcn_exp2f((s00[e] - m0) * L2E); s0m[e] = __builtin_amdgcn_exp2f((s0m[e] - m0) * L2E);
    s10[e] = __builtin_amdgcn_exp2f((s10[e] - m1) * L2E); s11[e] = __builtin_amdgcn_exp2f((s11[e] - m1) * L2E); s1m[e] = __builtin_amdgcn_exp2f((s1m[e] - m1) * L2E);
    t0 += s00[e] + s0m[e]; t1 += (s10[e] + s11[e]) + s1m[e];
  }
  const float em = __builtin_amdgcn_exp2f((NEG - m0) * L2E);
  t0 += 4.f * em;
  const float r0 = __builtin_amdgcn_rcpf(xsum4(t0)), r1 = __builtin_amdgcn_rcpf(xsum4(t1));
  s00 *= r0; s0m *= r0; s10 *= r1; s11 *= r1; s1m *= r1;
  const u32x4 p0 = (u32x4){pack2(s00[0], s00[1]), pack2(s00[2], s00[3]), pack2(s0m[0], s0m[1]), pack2(s0m[2], s0m[3])};
  const u32x4 p1 = (u32x4){pack2(s10[0], s10[1]), pack2(s10[2], s10[3]), pack2(s11[0], s11[1]), pack2(s11[2], s11[3])};
  const u32x4 pm = (u32x4){0u, 0u, pack2(s1m[0], s1m[1]), pack2(s1m[2], s1m[3])};
  const u32 pe = pack2(em * r0, em * r0);
  const u32x4 px = (u32x4){0u, 0u, pe, pe};
#pragma unroll
  for (int pr = 0; pr < 4; ++pr) {
    f32x4 o0[2], o1[2];
#pragma unroll
    for (int t = 0; t < 2; ++t) {
      const int dt = 2 * pr + t;
      const u32x4 a0 = (u32x4){o.v[dt][0], o.v[dt][1], vm[dt][0], vm[dt][1]};
      o0[t] = MF16(a0, p0, z);
      o1[t] = MF16(o.v[dt], p1, z);
      o0[t] = MF16(o.v[dt], px, o0[t]);
      o1[t] = MF16(a0, pm, o1[t]);
    }
    const int ob = FO_O + ((sh >> 5) * 256 + (sh & 31) * 4 + pr) * 1024;
    __builtin_amdgcn_raw_buffer_store_b128((u32x4){pack2(o0[0][0], o0[0][1]), pack2(o0[0][2], o0[0][3]), pack2(o0[1][0], o0[1][1]), pack2(o0[1][2], o0[1][3])}, rsf, lane * 16, ob, 16);
    __builtin_amdgcn_raw_buffer_store_b128((u32x4){pack2(o1[0][0], o1[0][1]), pack2(o1[0][2], o1[0][3]), pack2(o1[1][0], o1[1][1]), pack2(o1[1][2], o1[1][3])}, rsf, lane * 16, ob + 131072, 16);
  }
}
#define NAW 64
__device__ __forceinline__ void a_role(__amdgpu_buffer_rsrc_t rsx, __amdgpu_buffer_rsrc_t rsf, int sh, int lim, int wi, int done, int lane, int fb, u32 tag) {
  const int lr = lane & 15, lg = lane >> 4;
  const int okm = (lg * 8 + (lr & 7)) * 16, ovm = lg < 2 ? (lg * 16 + lr) * 8 : 0x7ffffff0;
  int rp = 0, xp = -1, spin = 0, pub = done, miss = 0, tp = 200;
  u32 tseen = 0, tq0 = 0, tq1 = 0, tq2 = 0, tq3 = 0; (void)tq0; (void)tq1; (void)tq2; (void)tq3;
#define XA_WAITX(s_)                                                                  \
  while (xp <= ((s_) >> 5)) {                                                                                          \
    asm volatile("" ::: "memory");                                                                                     \
    int pv = min(xprog(__builtin_amdgcn_raw_buffer_load_b32(rsx, lane * 128, PGO_X, 16), tag), xprog(__builtin_amdgcn_raw_buffer_load_b32(rsx, (64 + (lane & 31)) * 128, PGO_X, 16), tag)); \
    _Pragma("unroll") for (int o_ = 1; o_ < 64; o_ *= 2) pv = min(pv, __shfl_xor(pv, o_));                             \
    xp = __builtin_amdgcn_readfirstlane(pv);                                                                           \
    if (xp > ((s_) >> 5) || ++spin > SPIN_CAP) break;                                                                  \
    asm volatile("s_sleep 16" ::: "memory");                                                                           \
  }
#define XA_PUB() if (pub != done) { pub = done; asm volatile("s_nop 4\n\tbuffer_store_dword %0, %1, %2, %3 offen sc1" :: "v"(done), "v"(lane ? 0x7ffffff0 : 0), "s"(rsx), "s"(PGO_A + wi * 128) : "memory"); }
#define XA_STEP(CUR, NXT)                                                                                              \
  {                                                                                                                    \
    const int seg = sh >> 5, nx = sh + NAW;                                                                            \
    XA_WAITX(min(nx, lim - 1))                                                                                         \
    if (rp <= seg) {                         \
      while (true) {                                                                                                   \
        int want = min(seg + 1, NSEG - 1);                                                                             \
        if (tseen) {                                                                                                   \
          const u32 due = tseen + (u32)(want - rp) * tp - (want + 1 < NSEG ? 0 : XA_LAST) + (seg + 1 < NSEG ? 0 : XA_END); \
          if ((int)(due - (u32)__builtin_amdgcn_s_memrealtime()) > 0 || miss) { asm volatile("s_waitcnt vmcnt(0)" ::: "memory"); XA_PUB() } \
          for (int i_ = 0; i_ < 4000 && (int)(due - (u32)__builtin_amdgcn_s_memrealtime()) > 0; ++i_) asm volatile("s_sleep 2" ::: "memory"); \
          if (!miss) want = min(max(want, rp + max((int)((u32)__builtin_amdgcn_s_memrealtime() - tseen), 0) / tp), NSEG - 1); \
        }                                                                                                              \
        XA_DBG4                                                                                                        \
        asm volatile("" ::: "memory");                                                                                 \
        if (seg + 1 < NSEG) {                                                                                          \
          const int r_ = __builtin_amdgcn_readfirstlane(__builtin_amdgcn_raw_buffer_load_b32(rsx, 0, pgr + want * 128, 16)); \
          if (r_ >= 0) { const u32 n_ = (u32)__builtin_amdgcn_s_memrealtime(); tseen = n_ - ((n_ - (r_ >> 16)) & 0x7fffu) + XA_LAG; tp = max(r_ & 1023, 1); rp = want; miss = 0; break; } \
        } else if (__ballot(__builtin_amdgcn_raw_buffer_load_b32(rsx, ((sh & 31) * 4 + (lane & 3)) * 128, PGO_D, 16) == SENT) == 0) break; \
        if (++spin > SPIN_CAP) break;                                                                                  \
        if (miss++ && seg + 2 < NSEG) asm volatile("s_sleep 20" ::: "memory");                                                         \
      }                                                                                                                \
    }                                                                                                                  \
    asm volatile("" ::: "memory");                                                                                     \
    XA_DBG2                                                                                                            \
    u32x4 km[4]; u32x2 vm[8];                                                                                          \
    _Pragma("unroll") for (int i = 0; i < 4; ++i) km[i] = __builtin_amdgcn_raw_buffer_load_b128(rsf, okm, FO_KM + (sh * 4 + i) * 512, 16); \
    _Pragma("unroll") for (int i = 0; i < 8; ++i) vm[i] = __builtin_amdgcn_raw_buffer_load_b64(rsf, ovm, FO_VM + (sh * 8 + i) * 256, 16);  \
                              \
    const int pk_ = min(rp + max((int)((u32)__builtin_amdgcn_s_memrealtime() - tseen), 0) / tp, NSEG - 1);              \
    const u32 pv_ = __builtin_amdgcn_raw_buffer_load_b32(rsx, 0, pgr + pk_ * 128, 16);                                 \
    asm volatile("" ::: "memory");                                                                                     \
    __builtin_amdgcn_s_waitcnt(0x0F70);                                                                   \
    asm volatile("" ::: "memory");                                                                                     \
    { const int r_ = __builtin_amdgcn_readfirstlane(pv_);                                                              \
      if (r_ >= 0) { const u32 n_ = (u32)__builtin_amdgcn_s_memrealtime(); tseen = n_ - ((n_ - (r_ >> 16)) & 0x7fffu) + XA_LAG; tp = max(r_ & 1023, 1); rp = pk_; } } \
    XA_PUB()                                   \
    if (nx < lim) xa_load(NXT, rsf, nx, lane);                                                                         \
    XA_DBG3                                                                                                            \
    __builtin_amdgcn_sched_barrier(0);                                                                                 \
    xa_compute(CUR, km, vm, rsf, sh, lane);                                                                            \
    ++done;                                                                                                            \
    XA_DBG                                                                                                             \
    sh = nx;                                                                                                           \
    if (sh >= lim) break;                                                                                              \
  }
#define XA_LAST 24
#define XA_END 0
#define XA_LAG 30
#define XA_DBG
#define XA_DBG2
#define XA_DBG3
#define XA_DBG4
  const int pgr = PGO_R + (__builtin_amdgcn_readfirstlane(wait_start(rsx, fb, tag, true)[2]) & 8192);
  XaOps A, B;
  if (sh >= NAW) {
    const int r_ = __builtin_amdgcn_readfirstlane(__builtin_amdgcn_raw_buffer_load_b32(rsx, 0, pgr + 40 * 128, 16));
    if (r_ >= 0) { const u32 n_ = (u32)__builtin_amdgcn_s_memrealtime(); tseen = n_ - ((n_ - (r_ >> 16)) & 0x7fffu) + XA_LAG; tp = max(r_ & 1023, 1); rp = 40; }
  }
  XA_WAITX(sh)
  asm volatile("" ::: "memory");
  xa_load(A, rsf, sh, lane);
  while (true) { XA_STEP(A, B) XA_STEP(B, A) }
  asm volatile("s_waitcnt vmcnt(0)" ::: "memory");
  XA_PUB()
}

typedef __attribute__((ext_vector_type(32))) unsigned int u32x32;
typedef __attribute__((ext_vector_type(16))) unsigned int u32x16;
typedef __attribute__((ext_vector_type(8))) unsigned int u32x8;
#define STR2(x) #x
#define STR(x) STR2(x)
#define RNG(f, b, n) f "[" STR(b) ":" STR((b) + (n)-1) "]"
#define REG(b) "v[" STR(b) "]"
#define LR_RED 16384
#define LR_CTL LR_RED
#define LR_STG 32768
#define L_RSTAMP LR_STG
#define STAMPV(i, v)
#define EXF (NH * 8 * 1024)
#define EXK 65536
#define OOR 0x7ffffff0

__device__ __forceinline__ u32 lds_word(int addr) {
  u32 v;
  asm volatile("ds_read_b32 %0, %1\n\ts_waitcnt lgkmcnt(0)" : "=v"(v) : "v"(addr) : "memory");
  return __builtin_amdgcn_readfirstlane(v);
}
__device__ __forceinline__ int fresh_lane() {
  int l;
  asm volatile("v_mbcnt_lo_u32_b32 %0, -1, 0\n\tv_mbcnt_hi_u32_b32 %0, -1, %0" : "=v"(l));
  return l;
}
__device__ __forceinline__ u32 rt32() { return (u32)__builtin_amdgcn_s_memrealtime(); }
#define SYO(s) (XSYN + flip + (s) * 64)
#define TKS 0.85f
#define TLB 1
#define TLC 1
#define TLP 2
#define KG0 0
#define CLS0 0u
#define APC 5
#define ADL 12
#define CALN 2
#define TE1 4
#define TE2 3
#define TE3 2
#define XTH 4
#define TS3 42
#define TE0 60
#define TD0 100
#define TD 120
#define PHASE 1
#define XSK 0
#define CLKS(R, X) WLG(2) "s_sub_u32 s49, s50, " R "\n\t" "s_cmp_le_i32 s49, " STR(3 * PHASE) "\n\t" "s_cbranch_scc1 " X "\n\t"
#define CLKH(OFF) WLG(0) "s_memrealtime s[72:73]\n\t" "s_add_u32 s50, s43, s44\n\t" "s_add_u32 s50, s50, " OFF "\n\t" "s_nop 1\n\t" "s_memrealtime s[74:75]\n\t"
#define CLKW(L, OFF) CLKH(OFF) "s_nop 3\n" \
                     L ":\n\t" "s_memrealtime s[76:77]\n\t" CLKS("s72", L "x") "s_memrealtime s[72:73]\n\t" CLKS("s74", L "x")     \
                     "s_memrealtime s[74:75]\n\t" CLKS("s76", L "x") "s_branch " L "\n" \
                     L "x:\n\t" CLKP(L)
#define CLKP(L) "s_cmp_lt_i32 s49, 1\n\t" "s_cbranch_scc1 " L "y\n\t" "s_nop " STR(PHN) "\n\t" "s_cmp_lt_i32 s49, 2\n\t" "s_cbranch_scc1 " L "y\n\t" "s_nop " STR(PHN) "\n\t" \
                "s_cmp_lt_i32 s49, 3\n\t" "s_cbranch_scc1 " L "y\n\t" "s_nop " STR(PHN) "\n\t" "s_nop 0\n\t" "s_branch " L "y\n\t" "s_nop 0\n" L "y:\n\t"
#define PHN 3
#define REL(L) "s_cbranch_vccnz " L "\n\t" "s_barrier\n" L ":\n\t"
#define RELN(L) "s_cbranch_vccnz " L "\n\t" "s_cmp_eq_u32 %[wv], 0\n\t" "s_cbranch_scc1 " L "a\n\t" "s_cmp_eq_u32 %[wv], 1\n\t" "s_cbranch_scc1 " L "b\n\t" \
                "s_barrier\n\t" "s_nop " STR(PD2) "\n\t" "s_branch " L "\n" L "b:\n\t" "s_barrier\n\t" "s_branch " L "\n" L "a:\n\t" "s_barrier\n" L ":\n\t"
#define PD2 2
#define TS_(n) "s_memtime s[80+2*" #n ":81+2*" #n "]\n\t"
#define TSA(n)
#define TSB(n)
#define TSW(n)
#define TVM 12
template <int NTL, int KS>
__device__ __forceinline__ void tr_chunk(const u16* S, int lr, int lg, u32 (&r)[NTL][KS][8]) {
  const int q = lr >> 2, p = lr & 3;
#pragma unroll
  for (int t = 0; t < NTL; ++t)
#pragma unroll
    for (int i = 0; i < KS; ++i)
#pragma unroll
      for (int kb = 0; kb < 2; ++kb) {
        const u16* base = S + (64 * i + 32 * kb + 16 * (lg >> 1) + 4 * (lg & 1) + (q & 1) + 8 * (q >> 1)) * (16 * NTL) + 16 * t + 4 * p;
        const u32x2 t0 = __builtin_bit_cast(u32x2, __builtin_amdgcn_ds_read_tr16_b64_v4i16((LAS v4i16_t*)(base)));
        const u32x2 t1 = __builtin_bit_cast(u32x2, __builtin_amdgcn_ds_read_tr16_b64_v4i16((LAS v4i16_t*)(base + 2 * 16 * NTL)));
        r[t][i][4 * kb + 0] = t0[0]; r[t][i][4 * kb + 1] = t0[1]; r[t][i][4 * kb + 2] = t1[0]; r[t][i][4 * kb + 3] = t1[1];
      }
}
#define SMF(acc, a, bf, bi) "v_smfmac_f32_16x16x64_bf16 " RNG("v", acc, 4) ", " RNG("v", a, 4) ", " RNG(bf, bi, 8) ", v34\n\t"
#define ZER(r) "v_mov_b64 " RNG("v", r, 2) ", 0\n\t" "v_mov_b64 " RNG("v", (r) + 2, 2) ", 0\n\t"
#define GLD(r, o) "s_xor_b32 s49, s50, " STR(o) "\n\t" "buffer_load_dwordx4 " RNG("v", r, 4) ", v32, %[rs], s49 offen sc1\n\t"
#define GL1(r, o) "s_xor_b32 s49, s50, " STR(o) "\n\t" "buffer_load_dwordx4 " RNG("v", r, 4) ", v33, %[rs], s49 offen sc1\n\t"
#define GL2(r, o) GL1(r, 4096 + (o))
#define MXA0(r) "v_max3_u32 v63, " REG(r) ", " REG((r) + 1) ", " REG((r) + 1) "\n\t"
#define MXA(r) "v_max3_u32 v63, v63, " REG(r) ", " REG((r) + 1) "\n\t"
#define MXB(r) "v_max3_u32 v63, v63, " REG((r) + 2) ", " REG((r) + 3) "\n\t"
#define CMPB "v_cmp_eq_u32 vcc, s54, v63\n\t"
#define PSA(r) "v_pk_add_f32 " RNG("v", r, 2) ", " RNG("v", r, 2) ", " RNG("v", (r) + 2, 2) "\n\t"
#define PSW(r, o) "ds_write_addtid_b32 " REG(r) " offset:" STR(o) "\n\t"
#define RDQ(r, o) "ds_read_b128 " RNG("v", r, 4) ", v37 offset:" STR(o) "\n\t"
#define SQ(t, i) SMF(64 + 4 * (t), 96 + 4 * (i), "a", ((i) * 4 + (t)) * 8)
#define SV(t, i) SMF(24 + 4 * (t), 96 + 4 * (i), "a", 128 + ((i) * 2 + (t)) * 8)
#define SVP(t, i) SV(t, i)
#define SW(a, q, i) SMF((a) + 4 * (q), 96 + 4 * (i), "v", 128 + ((i) * 2 + (q)) * 8)
#define WVM(n) "s_waitcnt vmcnt(" STR(n) ")\n\t"
#define WLG(n) "s_waitcnt lgkmcnt(" STR(n) ")\n\t"
#define WLGF(n) WLG(n)
#define PACE 7
#define PC "s_nop " STR(PACE) "\n\t"
#define PC6
#define PC7
#define WBLK(a, n, i, M0) WVM((n) + 7 - (i)) SW(a, 0, i) M0(96 + 4 * (i)) SW(a, 1, i) MXB(96 + 4 * (i))
#define CL8(a, b, c, d, e, f, g, h) "v" #a, "v" #b, "v" #c, "v" #d, "v" #e, "v" #f, "v" #g, "v" #h
#define CL64 CL8(64, 65, 66, 67, 68, 69, 70, 71)
#define CL72 CL8(72, 73, 74, 75, 76, 77, 78, 79)
#define CL80 CL8(80, 81, 82, 83, 84, 85, 86, 87)
#define CL88 CL8(88, 89, 90, 91, 92, 93, 94, 95)
#define CL96 CL8(96, 97, 98, 99, 100, 101, 102, 103), CL8(104, 105, 106, 107, 108, 109, 110, 111)
#define CL112 CL8(112, 113, 114, 115, 116, 117, 118, 119)
#define CL120 CL8(120, 121, 122, 123, 124, 125, 126, 127)
#define PKA(d, a, b) "v_pk_add_f32 " RNG("v", d, 2) ", " RNG("v", a, 2) ", " RNG("v", b, 2) "\n\t"
#define CNT(o) STR(4096 + 64 * (o))
#define ARRIVE(o) "ds_add_u32 v45, v39 offset:" CNT(o) "\n\t"

#define EXTRA "v_readlane_b32 s69, v35, s45\n\t" "s_add_u32 s66, s56, s69\n\t" "s_add_u32 s67, s57, s69\n\t" "s_add_u32 s67, s67, s69\n\t"
#define TEX(s) ((s) == 0 ? TE0 : ((s) < 4 ? TE1 : ((s) < 12 ? TE2 : ((s) < TS3 ? TE3 : 0))))
#define R2BASE "s_bitcmp1_b32 s45, 0\n\t" "s_cselect_b32 s71, " STR(XR2S) ", 0\n\t" "s_add_u32 s71, s71, %[r2]\n\t"
#define X4B 0
#define X4C 0
#define STB "buffer_store_dwordx2 v[116:117], v11, %[rs], s36 offen sc1\n"
#define STC "buffer_store_dwordx2 v[80:81], v17, %[rs], s36 offen sc1\n\t"
#define R2TEXT                                                                                                     \
              TSB(0)                                                                                               \
              "buffer_load_dwordx4 v[64:67], v43, %[rs], s71 offen sc1\n\t"                                      \
              "buffer_load_dwordx4 v[68:71], v8, %[rs], s71 offen sc1\n\t"                                       \
              "buffer_load_dwordx4 v[76:79], v9, %[rs], s71 offen sc1\n\t"                                       \
              "buffer_load_dwordx4 v[60:63], v10, %[rs], s71 offen sc1\n\t"                                      \
              "buffer_store_dwordx4 v[72:75], v47, %[rf], s41 offen sc1\n\t"                                     \
              "s_cbranch_vccnz .Lvw%=\n\t" VPOST ARRIVE(12) "s_barrier\n" ".Lvw%=:\n\t"                             \
              "ds_read2_b32 v[96:97], v12 offset0:0 offset1:4\n\t" "ds_read2_b32 v[98:99], v12 offset0:32 offset1:36\n\t" \
              "ds_read2_b32 v[100:101], v13 offset0:0 offset1:4\n\t" "ds_read2_b32 v[102:103], v13 offset0:32 offset1:36\n\t" \
              "ds_read2_b32 v[104:105], v14 offset0:0 offset1:4\n\t" "ds_read2_b32 v[106:107], v14 offset0:32 offset1:36\n\t" \
              "ds_read2_b32 v[108:109], v15 offset0:0 offset1:4\n\t" "ds_read2_b32 v[110:111], v15 offset0:32 offset1:36\n\t" \
              "v_mov_b64 v[112:113], v[80:81]\n\t" "v_mov_b64 v[114:115], v[82:83]\n\t" "s_mov_b32 s65, 0\n\t"     \
              "v_mov_b32_dpp v112, v84 row_ror:8 row_mask:0xf bank_mask:0xc\n\t" "v_mov_b32_dpp v113, v85 row_ror:8 row_mask:0xf bank_mask:0xc\n\t" \
              "v_mov_b32_dpp v114, v86 row_ror:8 row_mask:0xf bank_mask:0xc\n\t" "v_mov_b32_dpp v115, v87 row_ror:8 row_mask:0xf bank_mask:0xc\n\t" \
              "v_pk_mul_f32 v[112:113], v[112:113], s[62:63] op_sel_hi:[1,0]\n\t" "v_pk_mul_f32 v[114:115], v[114:115], s[62:63] op_sel_hi:[1,0]\n\t" \
              WLG(0)                                                                                               \
              PKA(96, 96, 100) PKA(98, 98, 102) PKA(104, 104, 108) PKA(106, 106, 110) PKA(96, 96, 104) PKA(98, 98, 106) \
              "v_cvt_pk_bf16_f32 v96, v96, 0\n\t" "v_cvt_pk_bf16_f32 v97, v97, 0\n\t" "v_cvt_pk_bf16_f32 v98, v98, 0\n\t" "v_cvt_pk_bf16_f32 v99, v99, 0\n\t" \
              "v_accvgpr_write_b32 a194, v96\n\t" "v_accvgpr_write_b32 a202, v97\n\t" "v_accvgpr_write_b32 a198, v98\n\t" "v_accvgpr_write_b32 a206, v99\n\t" \
              TSB(1) WVM(4) TSB(2) \
              "v_permlane32_swap_b32 v64, v66\n\t" "v_permlane32_swap_b32 v65, v67\n\t" \
              "v_pk_add_f32 v[92:93], v[64:65], v[66:67]\n\t" \
              WVM(3) SFM(68) \
              "v_permlane16_swap_b32 v92, v93\n\t" \
              "v_add_f32 v94, v92, v93\n\t" \
              WVM(2) "v_mul_f32 v94, s62, v94\n\t" SFM(76) \
              "v_exp_f32 v92, v94\n\t" \
              WVM(1) TSB(3) SFM(60) \
                \
              ".Lex%=:\n\t" \
              "v_exp_f32 v88, v112\n\t" "v_exp_f32 v89, v113\n\t" "v_cvt_pk_bf16_f32 v58, v92, 0\n\t" "v_exp_f32 v90, v114\n\t" "v_exp_f32 v91, v115\n\t" \
              "v_cvt_pk_bf16_f32 v56, v88, v89\n\t" "v_cvt_pk_bf16_f32 v57, v90, v91\n\t" \
              TSB(4) ROR8(50, 58) ROR8(48, 56) \
              "v_mfma_f32_16x16x32_bf16 v[88:91], v[52:55], v[56:59], 0\n\t" \
              ROR8(49, 57) "s_nop 1\n\t" \
              "v_mfma_f32_16x16x32_bf16 v[116:119], a[192:195], v[56:59], 0\n\t" \
              "s_nop 2\n\t" \
              "v_mfma_f32_16x16x32_bf16 v[116:119], a[196:199], v[48:51], v[116:119]\n\t" \
              "v_add_f32_dpp v92, v88, v88 row_ror:8 row_mask:0xf bank_mask:0xf\n\t" "v_subrev_u32 v89, 0x33800000, v92\n\t" \
              "v_mfma_f32_16x16x32_bf16 v[124:127], a[204:207], v[56:59], 0\n\t" \
              "v_rcp_f32 v93, v92\n\t" \
              "v_mfma_f32_16x16x32_bf16 v[124:127], a[200:203], v[48:51], v[124:127]\n\t" \
              TSB(5) "v_cmp_le_u32 vcc, 0x20000000, v89\n\t" "s_nop 0\n\t" \
              "s_cbranch_vccnz .Lmx%=\n\t" \
              OTAIL STB TSB(6)
#define SFM(r) "v_pk_fma_f32 v[112:113], " RNG("v", r, 2) ", s[62:63], v[112:113] op_sel_hi:[1,0,1]\n\t" "v_pk_fma_f32 v[114:115], " RNG("v", (r) + 2, 2) ", s[62:63], v[114:115] op_sel_hi:[1,0,1]\n\t"
#define ROR8(d, s) "v_mov_b32_dpp " REG(d) ", " REG(s) " row_ror:8 row_mask:0xf bank_mask:0xf\n\t"
#define OTAIL "v_pk_mul_f32 v[116:117], v[116:117], v[92:93] op_sel:[0,1]\n\t" "v_pk_mul_f32 v[118:119], v[118:119], v[92:93] op_sel:[0,1]\n\t" \
              "v_cvt_pk_bf16_f32 v116, v116, v117\n\t" "v_cvt_pk_bf16_f32 v117, v118, v119\n\t" \
              "s_mov_b64 exec, s[78:79]\n\t" \
              "v_pk_mul_f32 v[124:125], v[124:125], v[92:93] op_sel:[0,1]\n\t" "v_pk_mul_f32 v[126:127], v[126:127], v[92:93] op_sel:[0,1]\n\t" \
              "v_cvt_pk_bf16_f32 v116, v124, v125\n\t" "v_cvt_pk_bf16_f32 v117, v126, v127\n\t" \
              "s_mov_b64 exec, -1\n\t"
#define R2MAX ".Lmx%=:\n\t" OTAIL \
              "s_cmp_lg_u32 s65, 0\n\t" "s_cbranch_scc1 .Lend%=\n\t" "s_mov_b32 s65, 1\n\t" \
              "v_max3_f32 v93, v112, v113, v94\n\t" \
              "v_max3_f32 v93, v93, v114, v115\n\t" \
              "s_nop 1\n\t" \
              "v_max_f32_dpp v92, v93, v93 row_ror:8 row_mask:0xf bank_mask:0xf\n\t" \
              "v_max_f32_dpp v88, v93, v93 row_ror:8 row_mask:0xf bank_mask:0xf\n\t" \
              "s_nop 1\n\t" \
              "v_permlane16_swap_b32 v92, v88\n\t" \
              "v_max_f32 v93, v92, v88\n\t" "v_max_f32 v92, v92, v88\n\t" \
              "s_nop 1\n\t" \
              "v_permlane32_swap_b32 v93, v92\n\t" \
              "v_max_f32 v93, v93, v92\n\t" \
              "v_sub_f32 v94, v94, v93\n\t" "v_sub_f32 v112, v112, v93\n\t" "v_exp_f32 v92, v94\n\t" "v_sub_f32 v113, v113, v93\n\t" "v_sub_f32 v114, v114, v93\n\t" "v_sub_f32 v115, v115, v93\n\t" \
              "s_branch .Lex%=\n"
#define PS1(r) "v_add_f32 " REG(r) ", " REG(r) ", " REG((r) + 2) "\n\t"
#define NOP(n) "s_nop " STR(n) "\n\t"
#define PCA 13
#define PDA 8
#define PDC 8
#define PDF 7
#define SLV 0
#define QKRED(X) RDQ(80, 12288) RDQ(84, 12304) X RDQ(88, 0) RDQ(92, 16) RDQ(112, 4096) RDQ(116, 4112) RDQ(120, 8192) RDQ(124, 8208) \
        WLG(4) TSA(4) PKA(80, 80, 88) PKA(82, 82, 90) PKA(84, 84, 92) PKA(86, 86, 94) \
        WLG(2) PKA(80, 80, 112) PKA(82, 82, 114) PKA(84, 84, 116) PKA(86, 86, 118) \
        WLG(0) PKA(80, 80, 120) PKA(82, 82, 122) PKA(84, 84, 124) PKA(86, 86, 126) \
        "v_cvt_pk_bf16_f32 v72, v80, v81\n\t" "v_cvt_pk_bf16_f32 v73, v82, v83\n\t" "v_cvt_pk_bf16_f32 v74, v84, v85\n\t" "v_cvt_pk_bf16_f32 v75, v86, v87\n\t" \
        NOP(1) TSA(5) \
        "v_mfma_f32_16x16x32_bf16 v[88:91], v[72:75], v[72:75], 0\n\t" NOP(2) \
        "v_mfma_f32_16x16x32_bf16 v[80:83], a[208:211], v[72:75], 0\n\t" NOP(2) \
        "v_mfma_f32_16x16x32_bf16 v[84:87], a[212:215], v[72:75], 0\n\t"
#define QKPUB "buffer_store_dwordx4 v[88:91], v41, %[rs], s71 offen sc1\n\t" NOP(0) \
        "buffer_store_dwordx4 v[80:83], v40, %[rs], s71 offen sc1\n\t" NOP(2) \
        "buffer_store_dwordx4 v[84:87], v40, %[rs], s71 offen offset:512 sc1\n\t"
#define MRED(W, X) "ds_read_b128 v[80:83], v46 offset:12288\n\t" W "ds_read_b128 v[84:87], v46\n\t" "ds_read_b128 v[88:91], v46 offset:4096\n\t" "ds_read_b128 v[92:95], v46 offset:8192\n\t" X TSW(4) \
        WLG(2) TSW(5) PKA(80, 80, 84) PKA(82, 82, 86) WLG(0) PKA(88, 88, 92) PKA(90, 90, 94) PKA(84, 80, 88) PKA(86, 82, 90) \
        "v_cvt_pk_bf16_f32 v80, v84, v85\n\t" "v_cvt_pk_bf16_f32 v81, v86, v87\n\t"
#define ORD(a, b) "v_cmpx_o_f32 s[50:51], " REG(a) ", " REG(b) "\n\t"
#define REDO(L) "s_cmp_lg_u64 exec, -1\n\t" "s_mov_b64 exec, -1\n\t" "s_cbranch_scc1 " L "\n" L "r:\n\t"
#define CWAIT(L, o, S) L ":\n\t" "ds_read_b32 v62, v38 offset:" CNT(o) "\n\t" WLG(0) "v_readfirstlane_b32 s49, v62\n\t" "s_cmp_lt_u32 s49, " S "\n\t" "s_cbranch_scc1 " L "\n\t"
#define KRA
#define KRC
#define KR0
#define KRX
#define VPOST PSA(24) PSA(28) PSW(24, 2560) PSW(25, 2832) PSW(28, 3200) PSW(29, 3472)
#define NBAR(L) "s_cbranch_scc1 " L "\n\t" "s_barrier\n" L ":\n\t" "s_mov_b32 s65, 0\n\t"
#define BARA "s_barrier\n\t"
#define NBA(L) NBAR(L)
#define FAILA ".Lfn%="
#define BARC "s_barrier\n\t"
#define NBC(L) NBAR(L)
#define FAILC ".Lfn%="
#define PZADR(SB, M) "s_add_u32 s49, s45, 1\n\t" "s_xor_b32 s62, s71, " STR(XR2S) "\n\t" "v_readlane_b32 s63, v19, s49\n\t" "s_mov_b32 s49, " SB "\n\t" \
        ".if !((" STR(M) ") & 1)\n\t" "s_mov_b32 s63, 0x7ff00000\n\t" ".endif\n\t" ".if !((" STR(M) ") & 2)\n\t" "s_mov_b32 s49, 0x7ff00000\n\t" ".endif\n\t" \
        ".if !((" STR(M) ") & 4)\n\t" "s_mov_b32 s62, 0x7ff00000\n\t" ".endif\n\t" "s_cmp_eq_u32 %[wv], 0\n\t" \
        "s_cselect_b32 s49, s49, s62\n\t" "s_cselect_b32 s63, s63, s62\n\t" "v_add_u32 v48, s49, v23\n\t" "s_mov_b32 exec_hi, 0\n\t" "v_add_u32 v48, s63, v23\n\t" "s_mov_b32 exec_hi, -1\n\t"
#define PZST "buffer_store_dwordx4 v[92:95], v48, %[rs], 0 offen sc1\n\t"
#define MA 0
#define MC 0
#define MB (7 - MA - MC)
#define PZD 1
#define PZB0 PZADR("s63", MB)
#define PZB PZST
#define PZC0
#define PZC ""
#define PZNC 0
#define PZA0
#define PZA
#define PZNA 0
#define LASTC(L) REL(L)
#define LASTA(L) ""
#define PCL PCA
#define ABODY(W, X, F, n) TSA(0) GLD(96, 0) NOP(PCA) GLD(100, 1024) NOP(PCA) GLD(104, 2048) W GLD(108, 3072) "s_mov_b32 s47, 0\n\t" X \
        WVM((n) + 3) TSA(1) SQ(0, 0) SQ(1, 0) SQ(2, 0) SQ(3, 0) \
        WVM((n) + 2) SQ(0, 1) SQ(1, 1) SQ(2, 1) SQ(3, 1) \
        WVM((n) + 1) SQ(0, 2) MXA0(96) MXB(96) SQ(1, 2) MXA(100) MXB(100) SQ(2, 2) MXA(104) MXB(104) SQ(3, 2) \
        WVM(n) TSA(2) SQ(0, 3) MXA(108) MXB(108) CMPB SQ(1, 3) "s_add_u32 s48, s38, 4\n\t" "s_cmp_lg_u32 s65, 0\n\t" NOP(0) SQ(2, 3) "s_cbranch_vccnz " F "\n\t" PS1(64) PS1(65) SQ(3, 3) PS1(68) PS1(69) \
        NOP(0) PSW(64, 0) PSW(65, 272) PSW(68, 640) PSW(69, 912) PSA(72) PSA(76) PSW(72, 1280) PSW(73, 1552) PSW(76, 1920) PSW(77, 2192)
#define CISSUE(X, Y) TSW(0) GL1(96, 0) PC GL1(100, 1024) PC GL1(104, 2048) PC GL1(108, 3072) PC GL2(112, 0) PC GL2(116, 1024) \
        "s_mov_b32 s48, s36\n\t" "s_mov_b32 s36, s37\n\t" "s_add_u32 s45, s45, 1\n\t" "s_add_u32 s38, s38, 4\n\t" "v_readlane_b32 s37, v19, s45\n\t" \
        "s_add_u32 s39, s39, " STR(EXF) "\n\t" "s_add_u32 s40, s40, " STR(EXF) "\n\t" \
        "s_add_u32 s41, s41, " STR(EXK) "\n\t" "s_add_u32 s42, s42, " STR(EXK) "\n\t" "s_add_u32 s43, s43, s55\n\t" PC6 \
        GL2(120, 2048) "s_mov_b32 s47, 2\n\t" "s_mul_i32 s70, s69, 3\n\t" EXTRA R2BASE PC7 X \
        GL2(124, 3072) Y
#define ZALL ZER(64) ZER(68) ZER(72) ZER(76) ZER(80) ZER(84) ZER(24) ZER(28)
#define SLIP(X) "ds_read2_b32 v[62:63], v38 offset0:0 offset1:2\n\t" WLG(0) "v_readfirstlane_b32 s50, v62\n\t" "v_readfirstlane_b32 s51, v63\n\t" \
        "s_sub_u32 s50, s50, s43\n\t" X "s_max_i32 s44, s44, s50\n\t" "s_cmp_eq_u32 s51, s45\n\t" "s_cmov_b32 s61, 0\n\t"
#define DPA(d, r, m) "v_add_f32_dpp " REG(d) ", " REG(r) ", " REG((r) + 2) " quad_perm:[0,1,2,3] row_mask:" m " bank_mask:0xf\n\t"
#define CBODY(m, X, n, F) CBODY_(80, m, X, n, F)
#define CBODY_(a, m, X, n, F) \
        WVM((m) + 7) TSW(1) WBLK(a, m, 0, MXA0) WBLK(a, m, 1, MXA) X WBLK(a, n, 2, MXA) WBLK(a, n, 3, MXA) WBLK(a, n, 4, MXA) WBLK(a, n, 5, MXA) WBLK(a, n, 6, MXA) \
        WVM(n) TSW(2) SW(a, 0, 7) MXA(124) MXB(124) CMPB SW(a, 1, 7) "s_cmp_lg_u32 s65, 0\n\t" NOP(1) "s_cbranch_vccnz " F "\n\t" \
        DPA(88, a, "0x3") DPA(89, (a) + 1, "0x3") NOP(1) DPA(88, (a) + 4, "0xc") DPA(89, (a) + 5, "0xc") NOP(1) \
        "v_permlane32_swap_b32 v88, v89\n\t" "v_add_f32 v88, v88, v89\n\t" TSW(3) PSW(88, 3840)
#define FINEO
#define NEXTP "s_cmp_eq_u32 s61, -1\n\t" "s_cbranch_scc1 .Lta%=\n\t"
#define NEXTN "s_branch .LNA%=\n"

__device__ __forceinline__ void clk_edge(u32& cyc, u32& tick) {
  const u32 t = rt32();
  for (int i = 0; i < 64; ++i) { tick = rt32(); if (tick != t) break; }
  cyc = (u32)__builtin_amdgcn_s_memtime();
}
__device__ __forceinline__ float clk_rate(u32 cyc0, u32 tk0) { u32 c, t; clk_edge(c, t); return (float)(c - cyc0) / (float)max(t - tk0, 1u); }
__device__ __forceinline__ void dma16(__amdgpu_buffer_rsrc_t rs, int voff, int soff, int sstep, int ldsa) {
#pragma unroll
  for (int i = 0; i < 16; ++i)
    asm volatile("s_mov_b32 m0, %0\n\ts_nop 3\n\tbuffer_load_dwordx4 %1, %2, %3 offen lds" :: "s"(ldsa + i * 1024), "v"(voff), "s"(rs), "s"(soff ^ (i * sstep)) : "memory");
}

#define KH(t) (192 + 8 * (t))
#define KMF(t, a, b) "v_mfma_f32_16x16x32_bf16 " RNG("v", 80 + 4 * (t), 4) ", " RNG("v", KH(t) + 4 * (a), 4) ", " RNG("v", KH(3) + 4 * (b), 4) ", " RNG("v", 80 + 4 * (t), 4) "\n\t"
#define KX(j) (92 + 4 * ((j) & 1))
#define KA(t, j) "v_pk_mul_f32 " RNG("v", KX(j), 2) ", " RNG("v", 64 + 4 * (j), 2) ", " RNG("v", 112 + ((t) & 2), 2) " op_sel:[0," STR((t) & 1) "] op_sel_hi:[1," STR((t) & 1) "]\n\t"
#define KF(j) "v_fract_f32 " REG(KX(j) + 1) ", " REG(KX(j) + 1) "\n\t"
#define KE(j, N) "v_exp_f32 " REG(KX(j)) ", " REG(KX(j)) "\n\t" "v_cos_f32 " REG(KX(j) + 2) ", " REG(KX(j) + 1) "\n\t" "v_sin_f32 " REG(KX(j) + 3) ", " N REG(KX(j) + 1) "\n\t"
#define KM(j) "v_pk_mul_f32 " RNG("v", KX(j) + 2, 2) ", " RNG("v", KX(j) + 2, 2) ", " RNG("v", KX(j), 2) " op_sel_hi:[1,0]\n\t"
#define KC(j) "v_pk_mul_f32 " RNG("v", KX(j), 2) ", " RNG("v", KX(j) + 2, 2) ", " RNG("v", 66 + 4 * (j), 2) " op_sel_hi:[1,0]\n\t" \
  "v_pk_fma_f32 " RNG("v", KX(j), 2) ", " RNG("v", KX(j) + 2, 2) ", " RNG("v", 66 + 4 * (j), 2) ", " RNG("v", KX(j), 2) " op_sel:[1,1,0] op_sel_hi:[0,1,1] neg_hi:[1,0,0]\n\t"
#define KD(t, j, r) "v_cvt_pk_bf16_f32 " REG(KH(t) + (j)) ", " REG(r) ", " REG((r) + 1) "\n\t" \
  "v_dot2c_f32_bf16 " REG(r) ", s47, " REG(KH(t) + (j)) "\n\t" "v_dot2c_f32_bf16 " REG((r) + 1) ", s48, " REG(KH(t) + (j)) "\n\t"
#define KL(t, j, r) "v_cvt_pk_bf16_f32 " REG(KH(t) + 4 + (j)) ", " REG(r) ", " REG((r) + 1) "\n\t"
#define KDM "s_add_u32 m0, m0, 1024\n\t" "s_add_u32 s43, s43, s42\n\t" "buffer_load_dwordx4 v110, s[36:39], s43 offen lds\n\t"
#define KP(j) "ds_read_b128 " RNG("v", 64 + 4 * (j), 4) ", v111 offset:" STR(16 * (j)) "\n\t"
#define KV(j, L, A) KE(j, "-") L KM(j) KC(j) A KD(3, j, KX(j))
#define KU(t, j, L, A) KE(j, "") L KM(j) A KD(t, j, KX(j) + 2)
#define KSUB                                                                                                        \
  KV(0, WLG(8) "s_add_u32 m0, m0, 1024\n\t", KA(3, 1)) KV(1, KL(3, 0, KX(0)), KA(3, 2))                              \
  "s_xor_b32 s43, s40, s41\n\t" "buffer_load_dwordx4 v110, s[36:39], s43 offen lds\n\t" KV(2, KL(3, 1, KX(1)), KA(3, 3)) \
  KV(3, KL(3, 2, KX(2)), KA(0, 0))                                                                                  \
  KU(0, 0, KL(3, 3, KX(3)), KA(0, 1)) KU(0, 1, KL(0, 0, KX(0) + 2), KA(0, 2)) KDM KU(0, 2, KL(0, 1, KX(1) + 2), KA(0, 3)) \
  KU(0, 3, KL(0, 2, KX(2) + 2), KA(1, 0)) KF(0)                                                                     \
  KU(1, 0, KL(0, 3, KX(3) + 2), KA(1, 1)) KF(1) KMF(0, 1, 0) KU(1, 1, KL(1, 0, KX(0) + 2), KA(1, 2)) KF(2) KDM      \
  KMF(0, 0, 1) KU(1, 2, KL(1, 1, KX(1) + 2), KA(1, 3)) KF(3) KMF(0, 0, 0) KU(1, 3, KL(1, 2, KX(2) + 2), KA(2, 0)) KF(0) KP(0) \
  KU(2, 0, KL(1, 3, KX(3) + 2), KA(2, 1)) KF(1) KP(1) KMF(1, 1, 0) KU(2, 1, KL(2, 0, KX(0) + 2), KA(2, 2)) KF(2) KP(2) KDM \
  KMF(1, 0, 1) KU(2, 2, KL(2, 1, KX(1) + 2), KA(2, 3)) KF(3) KP(3) KMF(1, 0, 0) KU(2, 3, KL(2, 2, KX(2) + 2), WLG(3) KA(3, 0)) \
  KMF(2, 0, 1) "v_add_u32 v111, 256, v111\n\t" "s_lshl2_add_u32 s41, s42, s41\n\t" KL(2, 3, KX(3) + 2)
#define KZERO ZER(80) ZER(84) ZER(88)
#define KCHUNK(PTR, VO, SO, ST, BUF, PF) "s_mov_b64 s[36:37], " PTR "\n\t" "s_mov_b32 m0, %[sc]\n\t" "v_mov_b32 v110, " VO "\n\t" SO "s_mov_b32 s41, 0\n\t" "s_mov_b32 s42, " STR(ST) "\n\t" \
  "buffer_load_dword " PF ", s[36:39], 0 offen lds\n\t" "s_add_u32 m0, %[sl], " STR((BUF) * 16384 - 1024) "\n\t"
#define KCALL "s_call_b64 s[44:45], .Lkg%=\n\t" WVM(16) KMF(2, 0, 0)
#define KTR(F, d, A, o) "ds_read_b64_tr_b16 " RNG(F, d, 2) ", " A " offset:" STR(o) "\n\t"
#define KTQ1(d, o) KTR("a", d, "%[aq]", o) KTR("a", (d) + 2, "%[aq]", (o) + 128) KTR("a", (d) + 4, "%[aq]", (o) + 2048) KTR("a", (d) + 6, "%[aq]", (o) + 2176)
#define KTQ(d, o) KCALL KTQ1(d, o) KMF(2, 1, 0) KTQ1((d) + 8, (o) + 32)
#define KTO1(d, o) KTR("v", d, "%[ao]", o) KTR("v", (d) + 2, "%[ao]", (o) + 64) KTR("v", (d) + 4, "%[ao]", (o) + 1024) KTR("v", (d) + 6, "%[ao]", (o) + 1088)
#define KTO(d, o) KCALL KTO1(d, o) KMF(2, 1, 0) KTO1((d) + 8, (o) + 2048)
#define KOUT(o) "s_add_u32 s43, %[ko], " STR(o) "\n\t"                                                              \
  "buffer_store_dwordx4 v[80:83], %[l16], %[rt], s43 offen sc1\n\t" "buffer_store_dwordx4 v[84:87], %[l16], %[rt], s43 offen offset:1024 sc1\n\t" "s_nop 3\n\t" \
  "buffer_store_dwordx4 v[88:91], %[l16], %[rt], s43 offen offset:2048 sc1\n\t"
struct SsmPtrs { const u16 *A_re, *A_im, *B_re, *B_im, *C_re, *C_im, *log_dt; };
__device__ __forceinline__ void r_role(unsigned char* lds, int b, const u16* __restrict__ mem0, const u16* __restrict__ Wq, const u16* __restrict__ Wk, const u16* __restrict__ Wv,
                                       const u16* __restrict__ Wo, int fb, u32 tag, u64 pc0, int clen, __amdgpu_buffer_rsrc_t rsx, __amdgpu_buffer_rsrc_t rsf,
                                       __amdgpu_buffer_rsrc_t rst, SsmPtrs sp, int tid, int lane, int w
                                       ) {
  const int lr = lane & 15, lg = lane >> 4;
  const int ldsb = (int)(size_t)(LAS unsigned char*)lds;
  const int idx = (lr & 2) ? 0xEEEE : 0x4444;
  int tries = 0;
  u32 tcnt = 0;
  u32 cyc0 = 0, tk0 = 0;
  if (tid < 8) *(u32*)(lds + LR_CTL + tid * 4) = tid < 2 ? rt32() : 0u;
  *(u32*)(lds + LR_CTL + 4096 + 256 + tid * 4) = 0u;
  const int xcd = b & 3, li = b >> 2;
  const int head = xcd * 8 + (li >> 2), ks = li & 3;
  const int vcu = head * 4 + ks;
  const int cg = (xcd >> 1) * 32 + li, rg = xcd & 1;
  const int rot1 = ((li >> 1) + w) & 3, rot3 = ((li >> 2) + 2 * w) & 7;
  const int pz1 = lane < 32 ? XR1A(lane >> 3, cg * 256 + rg * 128 + (lane & 7) * 16) : XR3A(lg & 1, vcu * 256 + lr * 16);
  const int pgo = PGO_X + head * 128;
  const int calo = XCAL + (li & 3) * 8192 + ((li >> 2) & 1) * 4096 + ((li >> 3) * 4 + xcd) * 128;
  u32 xp0;
  u32x32 wa[6], wo[4];
  {
    const __amdgpu_buffer_rsrc_t rwq = __builtin_amdgcn_make_buffer_rsrc((void*)Wq, 0, DM * HD * 2, 0x00020000);
    const int vq = (lane >> 2) * (HD * 2) + vcu * 64 + (lane & 3) * 16, sq = (256 * w + (rot1 << 6)) * (HD * 2);
    const int vo = (lane >> 1) * (DM * 2) + cg * 32 + (lane & 1) * 16, so = (1024 * w + (rot3 << 7)) * (DM * 2);
    const int sl = ldsb + LR_STG + w * 32768;
    const int ch0 = (b * 4 + w) * 2;
    u32x2 pin[6];
    u32 ldt;
    {
      const int po = (ch0 * SSM_N + 4 * lane) * 2;
      asm volatile("global_load_dwordx2 %0, %7, %9\n\tglobal_load_dwordx2 %1, %7, %10\n\tglobal_load_dwordx2 %2, %7, %11\n\tglobal_load_dwordx2 %3, %7, %12\n\t"
                   "global_load_dwordx2 %4, %7, %13\n\tglobal_load_dwordx2 %5, %7, %14\n\tglobal_load_ushort %6, %8, %15"
                   : "=&v"(pin[0]), "=&v"(pin[1]), "=&v"(pin[2]), "=&v"(pin[3]), "=&v"(pin[4]), "=&v"(pin[5]), "=&v"(ldt)
                   : "v"(po), "v"((ch0 + (lane >> 5)) * 2), "s"(sp.A_re), "s"(sp.A_im), "s"(sp.B_re), "s"(sp.B_im), "s"(sp.C_re), "s"(sp.C_im), "s"(sp.log_dt) : "memory");
    }
    u32x4 m0v;
    asm volatile("global_load_dwordx4 %0, %1, %2" : "=&v"(m0v) : "v"(((4 * rg + ((lane >> 1) & 3)) * DM + 16 * cg + 8 * (lane & 1)) * 2), "s"(mem0) : "memory");
    const u16* const wp[4] = {Wq, Wk, Wv, Wo};
    int pfo[5];
#pragma unroll
    for (int c = 0; c < 5; ++c) {
      const int lo_ = (int)(size_t)wp[c < 3 ? c : 3], s3 = (384 - lo_) & 1023, s63 = (8064 - lo_) & 8191;
      pfo[c] = c < 3 ? (lane < 16 ? (256 * w + li * 8 + (lane & 7)) * (HD * 2) + xcd * 2048 + (lane >> 3) * 1024 + s3
                                  : (lane < 24 && (s63 >> 11) == xcd ? (256 * w + li * 8 + (lane & 7)) * (HD * 2) + s63 : OOR))
                     : (lane < 16 ? (1024 * w + li * 16 + lane + (((c - 3) * 512) ^ ((((s3 >> 7) + 2 * w) & 4) << 7))) * (DM * 2) + (xcd >> 1) * 1024 + s3 : OOR);
    }
    const int scr = ldsb + LR_RED + 1024 + w * 256;
    asm volatile("s_mov_b32 m0, %0\n\ts_nop 3\n\tbuffer_load_dword %1, %2, 0 offen lds" :: "s"(scr), "v"(pfo[0]), "s"(rwq) : "memory");
#define KP0 16
#pragma unroll
    for (int i = 0; i < KP0; ++i)
      asm volatile("s_mov_b32 m0, %0\n\ts_nop 3\n\tbuffer_load_dwordx4 %1, %2, %3 offen lds" :: "s"(sl + i * 1024), "v"(vq), "s"(rwq), "s"(sq ^ (i * 16 * HD * 2)) : "memory");
    asm volatile("s_waitcnt vmcnt(1+" STR(KP0) ")" : "+v"(pin[0]), "+v"(pin[1]), "+v"(pin[2]), "+v"(pin[3]), "+v"(pin[4]), "+v"(pin[5]), "+v"(ldt), "+v"(m0v)::"memory");
    f32x4* prm = (f32x4*)(lds + w * 4096);
    {
      const float dt = rbf(expf(bf2f((u16)ldt)));
#pragma unroll
      for (int i = 0; i < 4; ++i) {
        float v[6];
#pragma unroll
        for (int j = 0; j < 6; ++j) v[j] = __uint_as_float((i & 1) ? (pin[j][i >> 1] & 0xffff0000u) : (pin[j][i >> 1] << 16));
        const float are = dt * v[0], aim = dt * v[1], br = dt * v[2], bi = dt * v[3];
        const float cr = v[4] * br - v[5] * bi, ci = v[4] * bi + v[5] * br;
        const float rev = aim * 0.15915494309189535f;
        prm[4 * lane + i] = (f32x4){are * 1.4426950408889634f, rev - __builtin_rintf(rev), cr, ci};
      }
    }
#pragma unroll
    for (int i = KP0; i < 16; ++i)
      asm volatile("s_mov_b32 m0, %0\n\ts_nop 3\n\tbuffer_load_dwordx4 %1, %2, %3 offen lds" :: "s"(sl + i * 1024), "v"(vq), "s"(rwq), "s"(sq ^ (i * 16 * HD * 2)) : "memory");
    const f32x4 lv = {(float)(128 * (lr >> 3) + (lr & 7)), (float)(lr < 8 ? 256 + 128 * (lr >> 2) + 2 * (lr & 3) : 512 + 128 * ((lr - 8) >> 1) + 4 * (lr & 1)),
                         (float)(lr <= 8 ? 1024 + 128 * lr : 0), (float)(8 * lr)};
    const int trow = 16 * (lg >> 1) + 4 * (lg & 1) + ((lr >> 2) & 1) + 8 * (lr >> 3);
    asm volatile(
        "s_nop 4\n\t" "s_mov_b32 s46, m0\n\t" "s_mov_b32 s38, " STR(DM * HD * 2) "\n\t" "s_mov_b32 s39, 0x20000\n\t" "s_mov_b32 s47, 0xbf80\n\t" "s_mov_b32 s48, 0xbf800000\n\t"
        WLG(0)
        "ds_read_b128 v[64:67], %[pa]\n\t" "ds_read_b128 v[68:71], %[pa] offset:16\n\t" "ds_read_b128 v[72:75], %[pa] offset:32\n\t" "ds_read_b128 v[76:79], %[pa] offset:48\n\t"
        "v_add_u32 v111, 256, %[pa]\n\t" KZERO
        KCHUNK("%[wk]", "%[vq]", "s_mov_b32 s40, %[sq]\n\t", 16 * HD * 2, 1, "%[p1]")
        WLG(0) KA(3, 0)
        KTQ(0, 0) KTQ(32, 4096) KTQ(64, 8192) KTQ(96, 12288)
        KCHUNK("%[wv]", "%[vq]", "", 16 * HD * 2, 0, "%[p2]")
        KTQ(16, 16384) KTQ(48, 16384 + 4096) KTQ(80, 16384 + 8192) KTQ(112, 16384 + 12288)
        KOUT(0)
        KCHUNK("%[wo]", "%[vo]", "s_mov_b32 s40, %[so]\n\t", 32 * DM * 2, 1, "%[p3]")
        KZERO
        KTQ(128, 0) KTQ(144, 4096) KTQ(160, 8192) KTQ(176, 12288)
        KCHUNK("%[wo]", "%[vo]", "s_xor_b32 s40, s40, " STR(512 * DM * 2) "\n\t", 32 * DM * 2, 0, "%[p4]")
        KTO(128, 16384) KTO(144, 16384 + 4096) KTO(160, 16384 + 8192) KTO(176, 16384 + 12288)
        KOUT(3072)
        "s_branch .Lke%=\n"
        ".Lkg%=:\n\t"
        KSUB
        "s_setpc_b64 s[44:45]\n"
        ".Lke%=:\n\t"
        WLG(0) "s_mov_b32 m0, s46"
        : "=&{a[0:31]}"(wa[0]), "=&{a[32:63]}"(wa[1]), "=&{a[64:95]}"(wa[2]), "=&{a[96:127]}"(wa[3]), "=&{a[128:159]}"(wa[4]), "=&{a[160:191]}"(wa[5]),
          "=&{v[128:159]}"(wo[0]), "=&{v[160:191]}"(wo[1])
        : "{v[112:115]}"(lv), [pa] "v"(ldsb + w * 4096 + lg * 64), [vq] "v"(vq), [vo] "v"(vo),
          [p1] "v"(pfo[1]), [p2] "v"(pfo[2]), [p3] "v"(pfo[3]), [p4] "v"(pfo[4]), [aq] "v"(sl + trow * 64 + (lr & 3) * 8), [ao] "v"(sl + trow * 32 + (lr & 3) * 8), [l16] "v"(lane * 16),
          [wk] "s"(Wk), [wv] "s"(Wv), [wo] "s"(Wo), [sq] "s"(sq), [so] "s"(so), [sl] "s"(sl), [sc] "s"(scr), [rt] "s"(rst), [ko] "s"(2 * SSM_L * SSM_D * 2 + ch0 * 3072)
        : CL64, CL72, CL80, CL88, CL96, CL8(192, 193, 194, 195, 196, 197, 198, 199), CL8(200, 201, 202, 203, 204, 205, 206, 207), CL8(208, 209, 210, 211, 212, 213, 214, 215),
          CL8(216, 217, 218, 219, 220, 221, 222, 223), "s36", "s37", "s38", "s39", "s40", "s41", "s42", "s43", "s44", "s45", "s46", "s47", "s48", "scc", "memory");
    {
      const u32x4 pz = (u32x4){SENT, SENT, SENT, SENT};
      if (w < 3) {
        u32x4 v = pz;
        if (w == 0 && lane < 32) v = (u32x4){nosent(m0v[0]), nosent(m0v[1]), nosent(m0v[2]), nosent(m0v[3])};
#pragma unroll
        for (int hf = 0; hf < 2; ++hf) {
          __builtin_amdgcn_raw_buffer_store_b128(v, rsx, pz1, XSB(w) + hf * 8192, 16);
          __builtin_amdgcn_raw_buffer_store_b128(pz, rsx, XR2(vcu) + lane * 16, XR2B + (w & 1) * XR2S + hf * 8192, 16);
          __builtin_amdgcn_raw_buffer_store_b128(pz, rsx, lane < 16 ? XR2(vcu) + 1024 + lane * 16 : OOR, XR2B + (w & 1) * XR2S + hf * 8192, 16);
        }
      } else {
        __builtin_amdgcn_raw_buffer_store_b128(pz, rsx, lane < 8 ? lane * 16 : OOR, calo, 16);
        if (b < 32) {
          __builtin_amdgcn_raw_buffer_store_b128(pz, rsx, lane * 16, XSYN + b * 2048, 16);
          __builtin_amdgcn_raw_buffer_store_b128(pz, rsx, lane * 16, XSYN + b * 2048 + 1024, 16);
        } else if (b >= 48 && b < 48 + 41) {
          __builtin_amdgcn_raw_buffer_store_b128(pz, rsx, lane * 16, PGO_D + (b - 48) * 1024, 16);
        }
      }
      asm volatile("" ::: "memory");
    }
    xp0 = __builtin_amdgcn_raw_buffer_load_b32(rsx, (lane & 1) * 4096, pgo, 16);
    clk_edge(cyc0, tk0);
    asm volatile("s_waitcnt vmcnt(0)\n\ts_barrier" : "+v"(xp0)::"memory");
#define CLKF() clk_rate(cyc0, tk0)
    if (w == 0) {
      const u64 c0 = __builtin_amdgcn_s_memtime();
      asm volatile("s_waitcnt lgkmcnt(0)" ::: "memory");
      u32 v = __builtin_amdgcn_raw_buffer_load_b32(rsx, 0, calo, 16);
      asm volatile("s_waitcnt vmcnt(0)" : "+v"(v)::"memory");
      const u32 lat = (u32)(__builtin_amdgcn_s_memtime() - c0);
      const float f = CLKF();
      if (lane == 0) __builtin_amdgcn_raw_buffer_store_b128((u32x4){tag, __float_as_uint(f), lat, 0u}, rsx, 0, PGO_Y + b * 128, 16);
      asm volatile("" ::: "memory");
    }
    {
      u32 r[1][8][8];
      tr_chunk<1, 8>((const u16*)(lds + LR_STG + w * 32768), lr, lg, r);
#pragma unroll
      for (int i = 0; i < 8; ++i)
#pragma unroll
        for (int e = 0; e < 8; ++e) { const int o = (8 + i) * 8 + e; wo[o >> 5][o & 31] = r[0][i][e]; }
      asm volatile("s_waitcnt lgkmcnt(0)" ::: "memory");
    }
  }
  const int hf = (lr >> 1) & 1;
  const int voff1 = lg * 256 + (lr >> 3) * 128 + (((lr >> 2) & 1) * 2 + (lr & 1)) * 32 + hf * 16;
  const int voff3 = (lr >> 3) * 512 + (lg >> 1) * 256 + (((lr >> 2) & 1) * 2 + (lr & 1)) * 16 + (lg & 1) * 128 + hf * 64;
  const int ard = ldsb + (2 * (lr >> 3) + (lg >> 1)) * 640 + (lr & 1) * 272 + ((lr >> 1) & 3) * 64 + (lg & 1) * 32;
  const int ctl = ldsb + LR_CTL, one = w == 0 ? 2 : (w == 3 ? 4 : 1);
  const int r8 = lane & 7, hk = (lane >> 3) & 1;
#define COA 1
#define COB 1
  const int st01 = lr < 8 ? XR2(vcu) + lr * 16 + lg * 128 : OOR, st2 = (lr < 8 && lg >= 2) ? XR2(vcu) + 1024 + lr * 16 + (lg - 2) * 128 : OOR;
  const int ld01 = hk * 512 + r8 * 16 + lg * 128, ld2 = XR2(head * 4 + lg) + 1024 + r8 * 16 + hk * 128;
  int sto = XR3A(r8 >> 2, vcu * 256 + (r8 & 3) * 16 + lg * 64 + hk * 8);
  const int stk = lr >= 8 ? (lg * 8 + (lr - 8)) * 16 : OOR;
  const int lo = X4C ? 2 * (lr & 7) + (lr >> 3) : lr;
  const int arw = ldsb + LR_CTL + lane * 4, arr = ldsb + 3840 + ((lo >> 3) + 2 * ((lo >> 2) & 1)) * 64 + (lo & 3) * 16;
  const int st1 = XR1A(lg, cg * 256 + rg * 128 + lo * 8);
  const int avw = 0;
  const int avc = ldsb + 2560 + (lr >> 3) * 640 + (8 * ((lr >> 2) & 1) + (lr & 3)) * 4;
  const int avr = avc + (lg & 1) * 272 + (lg >> 1) * 64, avf = avc + (lg & 1) * 128 + (lg >> 1) * 16;
  u32x16 kc = {(u32)voff1, (u32)voff3, (u32)idx, (u32)TEX(lane), (u32)(lane < 16 ? XR2(vcu) + 1024 + lane * 16 : OOR), (u32)ard, (u32)ctl, (u32)one, (u32)st01, (u32)st2, (u32)(XR2(vcu) + lane * 16), (u32)ld2, (u32)(lane * 16), (u32)arw, (u32)arr, (u32)stk};
  int o1c = XR1A(xcd, w * 4096) + rot1 * 1024, o3c = XR3A(rg, w * 8192) + rot3 * 1024;
  const int fok = FO_K + (head * 8 + ks) * 1024, fov = FO_V + (head * 8 + 2 * ks) * 1024;
#define XPROG(v) min(xprog(__builtin_amdgcn_readlane(v, 0), tag), xprog(__builtin_amdgcn_readlane(v, 1), tag))
  int xp = w == 3 ? XPROG(xp0) : 1000;
#define XWAIT(n) for (int it_ = 0; it_ < SPIN_CAP && xp < (n); ++it_) {                                               \
    if (it_) asm volatile("s_sleep 4" ::: "memory");                                                                  \
    const u32 v_ = __builtin_amdgcn_raw_buffer_load_b32(rsx, (lane & 1) * 4096, pgo, 16); xp = XPROG(v_); asm volatile("" ::: "memory"); }
  XWAIT(1)
  u32x16 fv;
  u32x8 fk;
  {
    const int lo_ = w == 3 ? lane * 16 : OOR;
    const u32x4 v0 = __builtin_amdgcn_raw_buffer_load_b128(rsf, lo_, fov, 16), v1 = __builtin_amdgcn_raw_buffer_load_b128(rsf, lo_, fov + 1024, 16);
    const u32x4 k0 = __builtin_amdgcn_raw_buffer_load_b128(rsf, lo_, fok, 16), k1 = __builtin_amdgcn_raw_buffer_load_b128(rsf, lo_, fok + 4096, 16);
    fv = (u32x16){v0[0], v0[1], 0u, 0u, v0[2], v0[3], 0u, 0u, v1[0], v1[1], 0u, 0u, v1[2], v1[3], 0u, 0u};
    fk = (u32x8){k0[0], k0[1], k0[2], k0[3], k1[0], k1[1], k1[2], k1[3]};
    asm volatile("" : "+{a[192:207]}"(fv), "+{a[208:215]}"(fk));
  }
  const u32 tbase = __builtin_amdgcn_readfirstlane(*(const u32*)(lds + LR_CTL + 4));
  const u32 tb0 = tbase - (1u << 20);
#define TABLE(l) ({ const float f_ = CLKF() * *(const float*)(lds + LR_CTL + 28);          \
    const float k_ = 1.f + TKS * (fminf(fmaxf(23.9f / f_, 1.f), 1.5f) - 1.f);                                          \
    const int nb_ = (int)(TDB * k_ + 0.5f) + TLB * (l), nc_ = nb_ + (int)((TDC - TDB) * k_ + 0.5f) + TLC * (l), np_ = nc_ + (int)((TP - TDC) * k_ + 0.5f) + TLP * (l); \
    (u32)(nb_ | (nc_ << 10) | (np_ << 20)); })
#define XPN 1
  if (b == 0 && w == 1) {
    u32 tt = 0, lat = 0;
    bool xok = false;
    for (int it = 0; it < SPIN_CAP; ++it) {
      asm volatile("" ::: "memory");
      const u32x4 r0 = __builtin_amdgcn_raw_buffer_load_b128(rsx, lane * 128, PGO_Y, 16), r1 = __builtin_amdgcn_raw_buffer_load_b128(rsx, lane * 128 + 8192, PGO_Y, 16);
      if (!xok) xok = __ballot(xprog(__builtin_amdgcn_raw_buffer_load_b32(rsx, lane * 128, PGO_X, 16), tag) < XPN) == 0;
      const u64 miss = __ballot(r0[0] != tag || r1[0] != tag);
      if (tt == 0 && __popcll(miss) <= 24) {
        float fm = (r0[0] == tag ? __uint_as_float(r0[1]) : 0.f) + (r1[0] == tag ? __uint_as_float(r1[1]) : 0.f), fn = (r0[0] == tag ? 1.f : 0.f) + (r1[0] == tag ? 1.f : 0.f);
#pragma unroll
        for (int o = 4; o < 64; o *= 2) { fm += __shfl_xor(fm, o); fn += __shfl_xor(fn, o); }
        fm = fn > 0.f ? fm / fn : 100.f;
        const float f0 = __uint_as_float(__builtin_amdgcn_readfirstlane(__float_as_uint(fm)));
        fm = fminf(fm, __shfl_xor(fm, 1)); fm = fminf(fm, __shfl_xor(fm, 2));
        if (lane == 0) *(float*)(lds + LR_CTL + 28) = fminf(fmaxf(fm / f0, 0.8f), 1.f);
        asm volatile("s_waitcnt lgkmcnt(0)" ::: "memory");
        tt = TABLE(0);
      }
      lat = min(r0[2], r1[2]);
      if (miss == 0 && xok) break;
    }
    { auto r = __builtin_amdgcn_permlane16_swap(lat, lat, false, false); lat = min(r[0], r[1]); }
    { auto r = __builtin_amdgcn_permlane32_swap(lat, lat, false, false); lat = min(r[0], r[1]); }
    u32 mm[4];
#pragma unroll
    for (int k = 0; k < 4; ++k)
      mm[k] = __builtin_amdgcn_readlane(lat, 4 * k) + __builtin_amdgcn_readlane(lat, 4 * k + 2) + __builtin_amdgcn_readlane(lat, 4 * (k ^ 2) + 1) + __builtin_amdgcn_readlane(lat, 4 * (k ^ 2) + 3);
    const u32 hb = mm[0] + mm[2] <= mm[1] + mm[3] ? 0u : 1u;
    const u32 best = (hb << 13) | ((hb ? mm[1] <= mm[3] : mm[0] <= mm[2]) ? CLS0 : 16384u - CLS0);
    if (lane == 0) __builtin_amdgcn_raw_buffer_store_b128((u32x4){rt32() + TD0, tt, best, tag}, rsx, 0, XSTA, 16);
  }
  XWAIT(2)
#pragma unroll
  for (int i = 0; i < 3; ++i) *(u32x4*)(lds + w * 4096 + i * 1024 + lane * 16) = (u32x4){SENT, SENT, SENT, SENT};
  LBAR();
  {
    u64 pc; asm volatile("s_getpc_b64 %0" : "=s"(pc));
    const u32 cv = __builtin_amdgcn_raw_buffer_load_b32(__builtin_amdgcn_make_buffer_rsrc((void*)pc0, 0, clen, 0x00020000), (int)(pc - pc0) + tid * 64, 0, 0);
    asm volatile("" :: "v"(cv));
  }
  if (w == 3) { const u32 v_ = __builtin_amdgcn_raw_buffer_load_b32(rsx, (lane & 1) * 4096, pgo, 16); xp = max(xp, XPROG(v_)); }
  u32x4 st = wait_start(rsx, fb, tag, false);
  const int flip = __builtin_amdgcn_readfirstlane(st[2]) & 8192, cls = __builtin_amdgcn_readfirstlane(st[2]) & 16384;
  o1c ^= cls; o3c ^= cls; sto ^= cls; kc[4] ^= cls; kc[8] ^= cls; kc[9] ^= cls; kc[10] ^= cls; kc[11] ^= cls;
  { const u32 now = rt32(); if (st[0] - now + 100000u > 200000u) st[0] = now; }
  u32x16 kd = {(u32)((ld01 + XR2(vcu ^ 1)) ^ cls), (u32)((ld01 + XR2(vcu ^ 2)) ^ cls), (u32)((ld01 + XR2(vcu ^ 3)) ^ cls), (u32)(X4B && hk ? OOR : sto), (u32)avr, (u32)(avr + 4096), (u32)(avr + 8192),
               (u32)(avr + 12288), (u32)avw, (u32)(X4C && hk ? OOR : st1 ^ cls), (u32)((lane & 1) * 4096), (u32)(XSB(lane + 1) + flip), (u32)(lane * 8), (u32)avf,
               (u32)(b == 0 && lane == 0 ? PGO_R + flip : OOR), (u32)(w == 0 ? pz1 ^ cls : (w == 1 ? kc[10] : kc[4]))};
  u32x16 sa = {(u32)flip, (u32)(XSB(1) + flip), 0u, (u32)fok, (u32)fov, (u32)(FO_KM + (head * 4 + ks) * 512),
               (u32)(FO_VM + (head * 8 + 2 * ks) * 256), tb0, st[0] - tb0 - (xcd & 1) * XSK, 0u, (u32)SYO(1), 0u, 0u, 0u, 0u, 0u};
  int snt = -1, synced = 0;
  int cb = __builtin_amdgcn_readfirstlane(st[1]), cc = (cb >> 10) & 1023, cp = (cb >> 20) & 1023;
  cb &= 1023;
#define RESYNC()                                                                                                      \
  {                                                                                                                   \
    for (int it = 0; it < 100; ++it) {                                                                                \
      const u32x2 tw = __builtin_amdgcn_raw_buffer_load_b64(rsx, 0, SYO(seg) + 8, 16);                                \
      const u32 tv = __builtin_amdgcn_readfirstlane(tw[0]), to = __builtin_amdgcn_readfirstlane(tw[1]);               \
      if (tv != SENT && to != SENT) {                                                                                 \
        cb = to & 1023; cc = (to >> 10) & 1023; cp = to >> 20; sa[7] = tb0 + seg * cp; sa[8] = tv - sa[7] - (xcd & 1) * XSK;            \
        if (lane == 0) *(u32*)(lds + LR_CTL + 24) = to;                                                               \
        break;                                                                                                        \
      }                                                                                                               \
      asm volatile("s_sleep 4" ::: "memory");                                                                         \
    }                                                                                                                 \
    synced = seg;                                                                                                     \
  }
  int nev = 0; (void)nev;
  bool again = false;
  if (tid == 192) *(u32*)(lds + LR_CTL + 24) = st[1];
  u32x32 keep;
  u32x2 ov;
  asm volatile("" : "={v[60:91]}"(keep));
  while (true) {
    u32x8 sc = {(u32)o1c, (u32)o3c, (u32)snt, (u32)cp, (u32)cb, (u32)cc, 0u, (u32)pgo};
    sa[15] = again ? 1u : 0u;
    sa[14] = xp;
    sa[13] = lds_word(ctl + 8) == sa[9] + 1 ? 0u : SENT;
#pragma unroll
    for (int i = 0; i < 16; ++i) sa[i] = __builtin_amdgcn_readfirstlane(sa[i]);
#pragma unroll
    for (int i = 0; i < 8; ++i) sc[i] = __builtin_amdgcn_readfirstlane(sc[i]);
    asm volatile(
        "s_nop 4\n\t" "s_mov_b32 s60, m0\n\t"
        "s_mov_b32 m0, %[mb]\n\t" "s_cmp_lg_u32 s51, 0\n\t" "s_cselect_b64 vcc, -1, 0\n\t" "s_mov_b32 s64, s50\n\t" "s_mov_b32 s61, s49\n\t"
        EXTRA "s_mov_b32 s70, 0\n\t" KR0
        R2BASE
        "s_cmp_lg_u32 %[wv], 3\n\t" "s_cbranch_scc1 .LN%=\n\t"
        "s_mov_b32 s78, 0xff00ff00\n\t" "s_mov_b32 s79, 0xff00ff00\n\t" "s_mov_b32 s62, 0x3e0293ee\n\t"
        "v_mov_b32 v52, 0x3f803f80\n\t" "v_mov_b32 v53, 0x3f803f80\n\t" "v_mov_b32 v54, 0x3f803f80\n\t" "v_mov_b32 v55, 0x3f803f80\n\t" "v_mov_b32 v51, 0\n\t" "v_mov_b32 v59, 0\n\t"
        "v_mov_b32 v60, %[tg]\n\t"
        "s_cmp_eq_u32 s47, 1\n\t" "s_cbranch_scc1 .LB%=\n\t"
        "s_cmp_eq_u32 s47, 2\n\t" "s_cbranch_scc1 .LC%=\n\t"
        ZALL
        ".LA%=:\n\t"
        CLKW(".Lta%=", "0") REL(".Lua%=")
        "s_add_u32 s50, s36, s52\n\t" NOP(PDA)
        ABODY(NOP(PCL) LASTA(".Lwa%="), WVM(5) "v_readlane_b32 s50, v60, 0\n\t" "v_readlane_b32 s51, v60, 1\n\t"
              "s_sub_u32 s50, s50, %[tg]\n\t" "s_sub_u32 s51, s51, %[tg]\n\t" "s_min_u32 s63, s50, s51\n\t" "s_max_u32 s50, s50, s51\n\t" "s_cmp_le_u32 s50, 80\n\t"
              "s_cselect_b32 s50, s63, 0\n\t" "s_max_i32 s64, s64, s50\n\t", ".Lfail%=", 0) TSA(3)
        QKRED(BARA) ORD(88, 89) ORD(90, 91) QKPUB TSA(6) TSB(7) REDO(".Lqk%=")
        SVP(0, 0) "s_cmp_eq_u32 s45, 63\n\t" SVP(1, 0) SVP(0, 1) SVP(1, 1) SVP(0, 2) SVP(1, 2) SVP(0, 3) SVP(1, 3) TSA(7)
        "s_cbranch_scc1 .Llast%=\n"
        ".LB%=:\n\t"
        CLKW(".Ltb%=", "s66")
        "s_mov_b32 s47, 1\n\t"
        R2TEXT
        ".LC%=:\n\t"
        ZALL
        "s_mov_b32 s47, 5\n\t" "s_sub_i32 s63, s64, s45\n\t" "s_cmp_lt_i32 s63, 2\n\t" "s_cbranch_scc1 .Lend%=\n\t"
        "s_cmp_lt_i32 s63, " STR(XTH) "\n\t" "s_cselect_b32 s63, s59, %[dm]\n\t"
        CLKW(".Ltc%=", "s67") REL(".Luc%=")
        "s_add_u32 s50, s36, s53\n\t" NOP(PDC)
        CISSUE(LASTC(".Lwc%="), "")
        "buffer_load_dword v61, off, %[rs], s46 sc1\n\t" "s_add_u32 s46, s46, 64\n\t"
        "s_add_u32 s49, s39, 4096\n\t" NOP(PDF)
        "buffer_load_dwordx4 a[208:211], v44, %[rf], s39 offen sc1\n\t" "buffer_load_dwordx4 a[212:215], v44, %[rf], s49 offen sc1\n\t"
        "buffer_load_dwordx2 a[192:193], v44, %[rf], s40 offen sc1\n\t" "buffer_load_dwordx2 a[196:197], v44, %[rf], s40 offen offset:8 sc1\n\t"
        "buffer_load_dwordx2 a[200:201], v44, %[rf], s40 offen offset:1024 sc1\n\t" "buffer_load_dwordx2 a[204:205], v44, %[rf], s40 offen offset:1032 sc1\n\t"
#define NXC 8
        "buffer_load_dword v60, v18, %[rs], s63 offen sc1\n\t"
        CBODY(NXC, "", NXC, ".Lfail%=")
        MRED(BARC, "s_add_u32 s44, s44, s70\n\t") ORD(84, 85) ORD(86, 87) STC TSW(6) REDO(".Lmr%=")
        CLKH("0") WVM(NXC) "v_readfirstlane_b32 s49, v61\n\t" "s_and_b32 s61, s61, s49\n\t" TSW(7)
        NEXTP
        "s_not_b32 s49, s61\n\t" "s_mov_b32 s47, 4\n\t" "s_branch .Lend%=\n"
        CWAIT(".Lqk%=", 8, "s48") KRA QKRED("") NOP(1) QKPUB SLIP("s_sub_u32 s51, s51, 1\n\t") "s_branch .Lqk%=r\n"
        CWAIT(".Lmr%=", 16, "s38") KRC WVM(0) MRED("", "") "v_min_u32 v80, -2, v80\n\t" "v_min_u32 v81, -2, v81\n\t" STC
        SLIP("s_add_u32 s50, s50, s55\n\t" "s_sub_u32 s50, s50, s57\n\t") "s_branch .Lmr%=r\n"
        ".Llast%=:\n\t"
        "buffer_store_dwordx4 v[72:75], v47, %[rf], s41 offen sc1\n\t" "s_mov_b32 s47, 3\n\t" NOP(4) VPOST ARRIVE(12) "s_barrier\n\t"
        "s_branch .Lend%=\n"
        R2MAX
        ".LN%=:\n\t"
        ZALL "s_mov_b32 s65, s51\n\t" "v_mov_b64 v[92:93], -1\n\t" "v_mov_b64 v[94:95], -1\n\t"
        "s_cmp_eq_u32 s47, 2\n\t" "s_cbranch_scc1 .LNC%=\n"
        ".LNA%=:\n\t" PZA0
        "s_add_u32 s50, s36, s52\n\t"
        RELN(".Lna%=")
        ABODY(NOP(PCA), LASTA(".Lxa%=") PZA, FAILA, PZNA) NBA(".Lya%=") ARRIVE(8) TSA(3)
        SV(0, 0) SV(1, 0) SV(0, 1) SV(1, 1) SV(0, 2) SV(1, 2) SV(0, 3) SV(1, 3) ZER(64) ZER(68) ZER(72) ZER(76)
        VPOST TSA(7) ZER(80) ZER(84) ZER(24) ZER(28)
        "v_add_u32 v88, 4096, v21\n\t" "v_add_u32 v89, 8192, v21\n\t" "v_add_u32 v90, 12288, v21\n\t"
        PZB0
        "s_lshl_b32 s61, s45, 7\n\t" "s_cmp_lg_u32 %[wv], 0\n\t"
        WLG(0) "s_barrier\n\t" PZB
        PSW(92, 0) PSW(92, 272) PSW(92, 640) PSW(92, 912) PSW(92, 1280) PSW(92, 1552) PSW(92, 1920) PSW(92, 2192) PSW(92, 3840)
        "s_cbranch_scc1 .LNB%=\n\t"
        "s_memrealtime s[72:73]\n\t"
        "ds_read_b32 v91, v38 offset:24\n"
        ".Lv0%=:\n\t"
        "ds_read_b32 v62, v38 offset:" CNT(12) "\n\t"
        "ds_read2_b32 v[112:113], v21 offset0:0 offset1:68\n\t" "ds_read2_b32 v[114:115], v21 offset0:16 offset1:84\n\t"
        "ds_read2_b32 v[116:117], v88 offset0:0 offset1:68\n\t" "ds_read2_b32 v[118:119], v88 offset0:16 offset1:84\n\t"
        "ds_read2_b32 v[120:121], v89 offset0:0 offset1:68\n\t" "ds_read2_b32 v[122:123], v89 offset0:16 offset1:84\n\t"
        "ds_read2_b32 v[124:125], v90 offset0:0 offset1:68\n\t" "ds_read2_b32 v[126:127], v90 offset0:16 offset1:84\n\t"
        WLG(0) "v_cmp_gt_u32 vcc, s48, v62\n\t" "s_cbranch_vccnz .Lv0%=\n\t"
        "s_add_u32 s50, s72, " STR(TVM) "\n\t" "s_lshl_b32 s50, s50, 17\n\t" "s_lshr_b32 s50, s50, 1\n\t"
        PKA(112, 112, 116) PKA(114, 114, 118) PKA(120, 120, 124) PKA(122, 122, 126) PKA(112, 112, 120) PKA(114, 114, 122)
        "v_lshrrev_b32 v91, 20, v91\n\t"
        "v_cvt_pk_bf16_f32 v112, v112, v113\n\t" "v_cvt_pk_bf16_f32 v113, v114, v115\n\t" "v_or_b32 v114, s50, v91\n\t"
        "s_cmp_eq_u32 s45, 63\n\t" "s_cbranch_scc1 .Lv2%=\n\t"
        "s_sleep " STR(SLV) "\n"
        ".Lv2%=:\n\t"
        "buffer_store_dwordx2 v[112:113], v20, %[rf], s42 offen sc1\n\t"
        "buffer_store_dword v114, v22, %[rs], s61 offen sc1\n"
        ".LNB%=:\n\t"
        "s_mov_b32 s47, 3\n\t"
        "s_cmp_eq_u32 s45, 63\n\t"
        "s_cbranch_scc1 .Lend%=\n"
        ".LNC%=:\n\t" PZC0
        "s_add_u32 s50, s36, s53\n\t"
        WLG(0) RELN(".Lnc%=")
        CISSUE("", LASTC(".Lxc%="))
        CBODY(0, PZC, PZNC, FAILC) NBC(".Lyc%=")
        ARRIVE(16) TSW(4)
        NEXTN
        ".Lfn%=:\n\t" "s_cbranch_scc1 .Lfail%=\n\t" "s_barrier\n"
        ".Lfail%=:\n\t" "s_nop 7\n" ".Lend%=:\n\t"
        "s_waitcnt vmcnt(0) lgkmcnt(0)\n\t"
        "s_mov_b32 s50, s64\n\t" KRX "s_mov_b32 m0, s60"
        : "+{s[36:51]}"(sa), "+{v[60:91]}"(keep), "=&{v[116:117]}"(ov), "+{a[192:207]}"(fv), "+{a[208:215]}"(fk) FINEO
        : "{v[8:23]}"(kd), "{v[32:47]}"(kc), "{s[52:59]}"(sc), [rs] "s"(rsx), [rf] "s"(rsf), [dm] "s"(__builtin_amdgcn_readfirstlane(PGO_Y + b * 128)), [tg] "s"(__builtin_amdgcn_readfirstlane(tag)), [r2] "s"(__builtin_amdgcn_readfirstlane(XR2B + flip)), [mb] "s"(__builtin_amdgcn_readfirstlane(ldsb + w * 4096)), [wv] "s"(__builtin_amdgcn_readfirstlane(w)),
          "{a[0:31]}"(wa[0]), "{a[32:63]}"(wa[1]), "{a[64:95]}"(wa[2]), "{a[96:127]}"(wa[3]), "{a[128:159]}"(wa[4]), "{a[160:191]}"(wa[5]),
          "{v[128:159]}"(wo[0]), "{v[160:191]}"(wo[1]), "{v[192:223]}"(wo[2]), "{v[224:255]}"(wo[3])
        : CL8(24, 25, 26, 27, 28, 29, 30, 31), "v48", "v49", "v50", "v51", CL8(52, 53, 54, 55, 56, 57, 58, 59), "v92", "v93", "v94", "v95", CL96, "v112", "v113", "v114", "v115", "v118", "v119", CL120,
          "s60", "s61", "s62", "s63", "s64", "s65", "s66", "s67", "s68", "s69", "s70", "s71", "s72", "s73", "s74", "s75", "s76", "s77", "s78", "s79", "vcc", "scc", "memory");
    const int code = __builtin_amdgcn_readfirstlane(sa[11]);
    xp = __builtin_amdgcn_readfirstlane(sa[14]);
    if (w == 3 && (code == 4 || code == 2)) xp = max(xp, XPROG(keep[0]));
    if (code == 2) {
      sa[1] = sa[0]; sa[0] = sa[12]; sa[9] -= 1; sa[2] -= 4; sa[3] -= EXF; sa[4] -= EXF;
      sa[5] -= EXK; sa[6] -= EXK; sa[7] -= cp; sa[10] -= 64;
    }
    const int seg = __builtin_amdgcn_readfirstlane(sa[9]);
    if (code == 3) break;
    if (code == 4) {
      if (w == 3 && sa[13] != 0) {
        if (b == 0) {
          const int l = min((int)*(const u32*)(lds + LR_CTL + 20), 40);
          const u32 tt = TABLE(l);
          if (lane == 0) {
            __builtin_amdgcn_raw_buffer_store_b64((u32x2){nosent(rt32() + TD), tt}, rsx, 0, SYO(seg) + 8, 16);
            if (seg > 2) *(u32*)(lds + LR_CTL + 20) = l + 1;
          }
        }
        RESYNC()
      }
      sa[11] = 0; again = false;
      continue;
    }
    if (code == 5) { XWAIT(seg + 2) xp = max(xp, seg + 2); sa[11] = 2; again = false; continue; }
    if (code == 1) {
      u32 mx = 0;
#pragma unroll
      for (int i = 0; i < 20; ++i) if (i < 12 || i >= 16) mx = max(mx, keep[i]);
      if (__ballot(mx == SENT) == 0 || tries > TRY_CAP) { __builtin_amdgcn_raw_buffer_store_b64((u32x2){nosent(ov[0]), nosent(ov[1])}, rsx, sto, sa[0], 16); sa[11] = 2; again = false; continue; }
    }
    ++tries; tcnt += 1 << (10 * code);
    again = true;
    if (tries > TRY_CAP) snt = snt == -1 ? 0 : snt + 1;
    if (w == 3 && synced != seg && __builtin_amdgcn_readfirstlane(__builtin_amdgcn_raw_buffer_load_b32(rsx, 0, SYO(seg), 16)) != SENT) {
      RESYNC()
      continue;
    }
    if (b == 0 && (int)lds_word(ctl + 8) != seg + 1 && lane == 0) {
      __builtin_amdgcn_raw_buffer_store_b32(1u, rsx, 0, SYO(seg + 1), 16);
      *(u32*)(lds + LR_CTL + 8) = seg + 1;
    }
    const u32 tr = rt32() + TR;
    if (w == 3) sa[8] = max(sa[8], tr - sa[7] - (code == 0 ? 0 : (code == 1 ? cb : cc) + code * TEX(seg)));
    else {
      if (lane == 0) *(u32*)(lds + LR_CTL) = tr;
      for (int i = 0; i < 1000 && (int)(tr - rt32()) > 0; ++i) {}
    }
  }
  LBAR();
  if (tid == 0) __builtin_amdgcn_raw_buffer_store_b32(1u, rsx, 0, PGO_D + vcu * 128, 16);
  if (b == 0 && w == 1) {
    for (int it = 0; it < SPIN_CAP; ++it) {
      asm volatile("" ::: "memory");
      if (__ballot(__builtin_amdgcn_raw_buffer_load_b32(rsx, lane * 128, PGO_S, 16) != tag || __builtin_amdgcn_raw_buffer_load_b32(rsx, lane * 128 + 8192, PGO_S, 16) != tag) == 0) break;
    }
    if (lane == 0) __builtin_amdgcn_raw_buffer_store_b32(tag, rsx, 0, XEPO, 16);
  }
}

#define XSTAT 3
#define XVM 8
#define XP0 -1
#define XSC " sc1"
#define XPITCH 1056
#define XSLOT (32 * XPITCH)
#define XW(HB, t) "a[" STR(HB) "+8*xk+4*" #t ":" STR(HB) "+8*xk+4*" #t "+3]"
#define XF(rt) "v[96+8*(xk&3)+4*" #rt ":96+8*(xk&3)+4*" #rt "+3]"
#define XFN(rt) "v[96+8*((xk+3)&3)+4*" #rt ":96+8*((xk+3)&3)+4*" #rt "+3]"
#define XA(AC, o) "v[" STR(AC) "+" #o ":" STR(AC) "+" #o "+3]"
#define XM1(d, a, b, FIRST) ".if (xk == 0) && " STR(FIRST) "\n\tv_mfma_f32_16x16x32_bf16 " d ", " a ", " b ", 0\n\t.else\n\tv_mfma_f32_16x16x32_bf16 " d ", " a ", " b ", " d "\n\t.endif\n\t"
#define XMF_QK(i, HB, AC, F) XMF_QK##i(HB, AC, F)
#define XMF_QK0(HB, AC, F) XM1(XA(AC, 0), XW(HB, 0), XF(0), F)
#define XMF_QK1(HB, AC, F) XM1(XA(AC, 4), XW(HB, 1), XF(0), F)
#define XMF_QK2(HB, AC, F) XM1(XA(AC, 8), XW(HB, 0), XF(1), F)
#define XMF_QK3(HB, AC, F) XM1(XA(AC, 12), XW(HB, 1), XF(1), F)
#define XMF_V(i, HB, AC, F) XMF_V##i(HB, AC, F)
#define XMF_V0(HB, AC, F) XM1(XA(AC, 0), XF(0), XW(HB, 0), F)
#define XMF_V1(HB, AC, F) XM1(XA(AC, 8), XF(0), XW(HB, 1), F)
#define XMF_V2(HB, AC, F) XM1(XA(AC, 4), XF(1), XW(HB, 0), F)
#define XMF_V3(HB, AC, F) XM1(XA(AC, 12), XF(1), XW(HB, 1), F)
#define XRD(rt, CUR, NXT) ".if xk < 13\n\tds_read_b128 " XFN(rt) ", " CUR " offset:64*(xk+3)+" STR(rt * 16 * XPITCH) "\n\t.else\n\t" \
                          "ds_read_b128 " XFN(rt) ", " NXT " offset:64*(xk-13)+" STR(rt * 16 * XPITCH) "\n\t.endif\n\t"
#define XCV(EPI, PV, i) ".if " STR(EPI) " && (xk >= 1) && (xk < 3)\n\tv_cvt_pk_bf16_f32 v[128+4*(xk-1)+" #i "], v[" STR(PV) "+8*(xk-1)+2*" #i "], v[" STR(PV) "+8*(xk-1)+2*" #i "+1]\n\t.endif\n\t"
#define XHALF(MF, HB, AC, PV, EPI, CUR, NXT, DSLOT, GOFF)                                                   \
  ".set xk, 0\n\t"                                                                                          \
  ".rept 16\n\t"                                                                                            \
  "s_waitcnt lgkmcnt(4)\n\t"                                                                                \
  MF(0, HB, AC, EPI) XRD(0, CUR, NXT) XCV(EPI, PV, 0)                                                       \
  ".if (xk >= 5) && (xk < 13)\n\t"                                                                          \
  "s_add_u32 m0, m0, " STR(XPITCH) "\n\t"                                                                   \
  ".endif\n\t"                                                                                              \
  MF(1, HB, AC, EPI) XRD(1, CUR, NXT) XCV(EPI, PV, 1)                                                       \
  ".if (xk >= 5) && (xk < 13)\n\t"                                                                          \
  "s_add_u32 %[dg], %[dg], 2048\n\t"                                                                        \
  ".endif\n\t"                                                                                              \
  MF(2, HB, AC, EPI) XCV(EPI, PV, 2)                                                                        \
  ".if (xk >= 4) && (xk < 12)\n\t"                                                                          \
  "buffer_load_dwordx4 %[l16], %[rx], %[dg] offen lds\n\t"                                                  \
  ".endif\n\t"                                                                                              \
  ".if xk == 0\n\t"                                                                                         \
  "s_add_u32 m0, %[dl], " STR((DSLOT) * XSLOT) "\n\t" "s_add_u32 %[dg], %[gb], " STR(GOFF) "\n\t"           \
  ".endif\n\t"                                                                                              \
  ".if " STR(EPI) " && (xk == " STR(XSTAT) ")\n\t"                                                                       \
  "buffer_store_dwordx4 v[128:131], v137, %[rf], %[sg] offen" XSC "\n\t"                                       \
  ".endif\n\t"                                                                                              \
  ".if " STR(EPI) " && (xk == 13)\n\t"                                                                      \
  "v_mov_b32 v137, %[l16]\n\t" "v_mov_b32 v138, %[lp2]\n\t"                                                 \
  ".endif\n\t"                                                                                              \
  ".if " STR(EPI) " && (xk == 14)\n\t"                                                                      \
  "s_add_u32 %[sg], %[sg], " STR(NH * 8 * 1024) "\n\t"                                                      \
  "v_add_u32 v136, 1, v136\n\t"                                                                             \
  ".endif\n\t"                                                                                              \
  ".if !" STR(EPI) " && (xk == 1)\n\t"                                                                      \
  "buffer_store_dword v136, %[fo], %[rp], 0 offen sc1\n\t"                                                  \
  ".endif\n\t"                                                                                              \
  MF(3, HB, AC, EPI) XCV(EPI, PV, 3)                                                                        \
  ".if xk == 0\n\t"                                                                                         \
  "s_waitcnt vmcnt(" STR(XVM) ")\n\t" "s_barrier\n\t"                                                               \
  ".endif\n\t"                                                                                              \
  ".if xk == 2\n\t"                                                                                         \
  "s_min_u32 %[dg], %[dg], " STR(S_LEN * DM * 2 - 16384 + 1024) "\n\t"                                      \
  ".endif\n\t"                                                                                              \
  ".if " STR(EPI) " && (xk == " STR(XSTAT) ")\n\t"                                                                       \
  "buffer_store_dwordx4 v[132:135], v138, %[rf], %[sg] offen" XSC "\n\t"                                       \
  ".endif\n\t"                                                                                              \
  ".set xk, xk+1\n\t"                                                                                       \
  ".endr\n\t"
#define XLOOP(MF)                                                                                           \
  "s_mov_b32 %[kp], m0\n\t"                                                                                 \
  "v_mov_b32 v136, %[p0]\n\t" "v_mov_b32 v137, " STR(OOR) "\n\t" "v_mov_b32 v138, " STR(OOR) "\n\t"                                                                                \
  "ds_read_b128 v[96:99], %[r0]\n\tds_read_b128 v[100:103], %[r0] offset:" STR(16 * XPITCH) "\n\t"          \
  "ds_read_b128 v[104:107], %[r0] offset:64\n\tds_read_b128 v[108:111], %[r0] offset:64+" STR(16 * XPITCH) "\n\t"   \
  "ds_read_b128 v[112:115], %[r0] offset:128\n\tds_read_b128 v[116:119], %[r0] offset:128+" STR(16 * XPITCH) "\n"   \
  ".Lx%=:\n\t"                                                                                              \
  XHALF(MF, 0, 64, 80, 1, "%[r0]", "%[r1]", 3, 66560) XHALF(MF, 128, 64, 80, 0, "%[r1]", "%[r2]", 0, 131072) \
  XHALF(MF, 0, 80, 64, 1, "%[r2]", "%[r3]", 1, 132096) XHALF(MF, 128, 80, 64, 0, "%[r3]", "%[r0]", 2, 196608) \
  "s_add_u32 %[gb], %[gb], 131072\n\t"                                                                      \
  "s_sub_u32 %[cnt], %[cnt], 1\n\t"                                                                         \
  "s_cmp_lg_u32 %[cnt], 0\n\t"                                                                              \
  "s_cbranch_scc1 .Lx%=\n\t"                                                                                \
  "s_mov_b32 m0, %[kp]\n\t"                                                                                 \
  "s_nop 7\n\t" "s_waitcnt lgkmcnt(0)"

template <bool ISV>
__device__ __forceinline__ void x_role(unsigned char* lds, const u16* __restrict__ x, const u16* __restrict__ Wm, int fo0, int head,
                                       __amdgpu_buffer_rsrc_t rsf, u32* prog, u32 tag, int lane, int w) {
  const int ldsb = (int)(size_t)(LAS unsigned char*)lds;
  const __amdgpu_buffer_rsrc_t rsxin = __builtin_amdgcn_make_buffer_rsrc((void*)x, 0, S_LEN * DM * 2, 0x00020000);
  const __amdgpu_buffer_rsrc_t rsp = __builtin_amdgcn_make_buffer_rsrc((void*)prog, 0, 128, 0x00020000);
  const int lr = lane & 15, lg = lane >> 4;
  const int xrd = ldsb + lr * XPITCH + lg * 16;
  const int fob = fo0 + (ISV ? (head * 8 + 2 * w) : (head * 8 + w)) * 1024;
  const int p2 = ISV ? 1024 : 4096;
  u32x32 xw[8];
  {
    const __amdgpu_buffer_rsrc_t rw = __builtin_amdgcn_make_buffer_rsrc((void*)Wm, 0, DM * HD * 2, 0x00020000);
    const int vq = (lane >> 4) * (HD * 2) + head * 256 + (lane & 15) * 16;
#define XWISSUE(c) _Pragma("unroll") for (int j = 0; j < 8; ++j)                                                       \
      asm volatile("s_mov_b32 m0, %0\n\ts_nop 3\n\tbuffer_load_dwordx4 %1, %2, %3 offen lds"                           \
                   :: "s"(ldsb + ((c) & 3) * XSLOT + (8 * w + j) * XPITCH), "v"(vq), "s"(rw), "s"((128 * (c) + 32 * w + 4 * j) * (HD * 2)) : "memory");
#define XXISSUE(h) _Pragma("unroll") for (int j = 0; j < 8; ++j)                                                       \
      asm volatile("s_mov_b32 m0, %0\n\ts_nop 3\n\tbuffer_load_dwordx4 %1, %2, %3 offen lds"                           \
                   :: "s"(ldsb + (h) * XSLOT + (8 * w + j) * XPITCH), "v"(lane * 16), "s"(rsxin), "s"((((h) >> 1) * 32 + 8 * w + j) * 2048 + ((h) & 1) * 1024) : "memory");
    XWISSUE(0) XWISSUE(1) XWISSUE(2) XWISSUE(3)
    const int q = lr >> 2, p = lr & 3;
#pragma unroll
    for (int c = 0; c < 8; ++c) {
      asm volatile("s_waitcnt vmcnt(24)\n\ts_barrier" ::: "memory");
#pragma unroll
      for (int s = 0; s < 4; ++s)
#pragma unroll
        for (int t = 0; t < 2; ++t) {
          const unsigned char* base = lds + (c & 3) * XSLOT + (8 * s + 2 * lg) * XPITCH + q * 256 + w * 64 + p * 16 + t * 8;
          const u32x2 t0 = __builtin_bit_cast(u32x2, __builtin_amdgcn_ds_read_tr16_b64_v4i16((LAS v4i16_t*)(base)));
          const u32x2 t1 = __builtin_bit_cast(u32x2, __builtin_amdgcn_ds_read_tr16_b64_v4i16((LAS v4i16_t*)(base + XPITCH)));
          const int o = ((4 * c + s) * 2 + t) * 4;
          xw[o >> 5][(o & 31) + 0] = t0[0]; xw[o >> 5][(o & 31) + 1] = t0[1]; xw[o >> 5][(o & 31) + 2] = t1[0]; xw[o >> 5][(o & 31) + 3] = t1[1];
        }
      LBAR();
      if (c < 4) { XWISSUE(c + 4) } else if (c < 7) { XXISSUE(c - 4) }
    }
  }
  asm volatile("s_waitcnt vmcnt(8)\n\ts_barrier" ::: "memory");
  u32x16 xacc;
  {
    u32 kp, dg; int gb = 8 * w * 2048, cnt = NSEG / 2, sg = fob - NH * 8 * 1024;
#define XASM(MF)                                                                                            \
    asm volatile(XLOOP(MF)                                                                                  \
                 : "=&{v[80:95]}"(xacc), [kp] "=&s"(kp), [dg] "=&s"(dg), [gb] "+s"(gb), [cnt] "+s"(cnt), [sg] "+s"(sg) \
                 : [r0] "v"(xrd), [r1] "v"(xrd + XSLOT), [r2] "v"(xrd + 2 * XSLOT), [r3] "v"(xrd + 3 * XSLOT), [l16] "v"(lane * 16), [lp2] "v"(lane * 16 + p2), \
                   [rx] "s"(rsxin), [rf] "s"(rsf), [rp] "s"(rsp), [fo] "v"(lane == 0 && w == 0 ? 0 : OOR), [dl] "s"(ldsb + 8 * w * XPITCH), [p0] "s"(tag + XP0), \
                   "{a[0:31]}"(xw[0]), "{a[32:63]}"(xw[1]), "{a[64:95]}"(xw[2]), "{a[96:127]}"(xw[3]),      \
                   "{a[128:159]}"(xw[4]), "{a[160:191]}"(xw[5]), "{a[192:223]}"(xw[6]), "{a[224:255]}"(xw[7]) \
                 : CL64, CL72, CL96, CL112, CL120, CL8(128, 129, 130, 131, 132, 133, 134, 135), "v136", "v137", "v138", "scc", "memory");
    if (ISV) { XASM(XMF_V) } else { XASM(XMF_QK) }
  }
#pragma unroll
  for (int jj = 0; jj < 2; ++jj) {
    u32x4 pk;
#pragma unroll
    for (int e = 0; e < 4; ++e) pk[e] = pack2(__uint_as_float(xacc[8 * jj + 2 * e]), __uint_as_float(xacc[8 * jj + 2 * e + 1]));
    __builtin_amdgcn_raw_buffer_store_b128(pk, rsf, lane * 16 + p2 * jj, fob + (NSEG - 1) * (NH * 8 * 1024), 16);
  }
  asm volatile("s_waitcnt vmcnt(0)\n\ts_barrier" ::: "memory");
  if (lane == 0 && w == 0) __hip_atomic_store(prog, tag + NSEG + 8, RLX_AGENT);
}

#define SSM_LO 1
#define SU_CP 4416
#define SU_U 0
#define SU_K (4 * SU_CP)
#define SU_P (SU_K + 10400)
#define SU_D (SU_P + 2048)
#define SU_RAW (SU_D + 2592)
#define SU_CH (SU_RAW + 4128 + 32)
#define SST(i)
__device__ __forceinline__ void ssm_unit(unsigned char* sm, int d0, __amdgpu_buffer_rsrc_t rst, __amdgpu_buffer_rsrc_t rsx, unsigned char* xb, int fb, bool& uok,
                                         const u16* __restrict__ Dskip, int tid, int lane, int w) {
  unsigned char* cb = sm + w * SU_CH;
  const int dd = d0 + w;
  SST(0)
  if (!uok) {
    for (int it = 0; it < SPIN_CAP; ++it) {
      asm volatile("" ::: "memory");
      if ((int)__builtin_amdgcn_readfirstlane(__builtin_amdgcn_raw_buffer_load_b32(rsx, 0, XUDN, 16)) >= 15) break;
      asm volatile("s_sleep 8" ::: "memory");
    }
    asm volatile("" ::: "memory");
    uok = true;
  }
  f32x4 acc[3];
#pragma unroll
  for (int t = 0; t < 3; ++t) acc[t] = __builtin_bit_cast(f32x4, __builtin_amdgcn_raw_buffer_load_b128(rst, lane * 16, 2 * SSM_L * SSM_D * 2 + dd * 3072 + t * 1024, 16));
  const float dsk = bf2f(Dskip[dd]);
  u32x4 uv[4];
#pragma unroll
  for (int i = 0; i < 4; ++i) uv[i] = __builtin_amdgcn_raw_buffer_load_b128(rst, (tid + 256 * i) * 16, d0 * 4096, 16);
  const int r = lane & 31, h = lane >> 5;
  {
    const int lr = lane & 15, lg = lane >> 4;
    SST(2)
    u32* Kd = (u32*)(cb + SU_D);
#pragma unroll
    for (int e = 0; e < 4; ++e) {
      Kd[128 * (lg >> 1) + 8 * lr + 4 * (lg & 1) + e] = split_hl(acc[0][e]);
      Kd[lg < 2 ? 256 + 64 * lg + 4 * lr + e : 384 + 32 * (2 * (lg - 2) + (e >> 1)) + 2 * lr + (e & 1)] = split_hl(acc[1][e]);
      if (lg < 2 || (lg == 2 && e == 0 && lr == 0)) Kd[512 + 16 * (4 * lg + e) + lr] = split_hl(acc[2][e]);
    }
    asm volatile("s_waitcnt lgkmcnt(0)" ::: "memory");
    unsigned char* kr = cb + SU_K + (lane + 1) * 80;
#pragma unroll
    for (int c8 = 0; c8 < 4; ++c8) {
      u32 kv[8];
#pragma unroll
      for (int e = 0; e < 8; ++e) kv[e] = Kd[kidx(32 * lane + 31 - 8 * c8 - e)];
      u32x4 hi, lo;
#pragma unroll
      for (int e = 0; e < 4; ++e) {
        hi[e] = __builtin_amdgcn_perm(kv[2 * e + 1], kv[2 * e], 0x05040100u);
        lo[e] = __builtin_amdgcn_perm(kv[2 * e + 1], kv[2 * e], 0x07060302u);
      }
      *(u32x4*)(kr + 16 * c8) = hi;
      if (SSM_LO) *(u32x4*)(kr + 5200 + 16 * c8) = lo;
    }
    if (lane < 8) *(u32x4*)(cb + SU_K + (lane >> 2) * 5200 + (lane & 3) * 16) = (u32x4){0u, 0u, 0u, 0u};
  }
  SST(3)
#pragma unroll
  for (int i = 0; i < 4; ++i) {
    const int l = 2 * (tid + 256 * i) + 8;
    *(u32*)(sm + 0 * SU_CH + SU_RAW + l * 2) = __builtin_amdgcn_perm(uv[i][2], uv[i][0], 0x05040100u);
    *(u32*)(sm + 1 * SU_CH + SU_RAW + l * 2) = __builtin_amdgcn_perm(uv[i][2], uv[i][0], 0x07060302u);
    *(u32*)(sm + 2 * SU_CH + SU_RAW + l * 2) = __builtin_amdgcn_perm(uv[i][3], uv[i][1], 0x05040100u);
    *(u32*)(sm + 3 * SU_CH + SU_RAW + l * 2) = __builtin_amdgcn_perm(uv[i][3], uv[i][1], 0x07060302u);
  }
  if (lane < 2) *(u32x4*)(cb + SU_RAW + lane * (4096 + 16)) = (u32x4){0u, 0u, 0u, 0u};
  LBAR();
  {
    if (lane < 20) *(u32x4*)(cb + SU_U + (lane / 5) * SU_CP + (lane % 5) * 16) = (u32x4){0u, 0u, 0u, 0u};
#pragma unroll
    for (int i = 0; i < 5; ++i) {
      const int g = lane + 64 * i;
      if (g < 257) {
        const u32x4 a = *(const u32x4*)(cb + SU_RAW + g * 16);
        const u32x2 c = *(const u32x2*)(cb + SU_RAW + g * 16 + 16);
        unsigned char* dst = cb + SU_U + 80 + g * 16;
        *(u32x4*)(dst) = a;
        *(u32x4*)(dst + SU_CP) = (u32x4){__builtin_amdgcn_alignbyte(a[1], a[0], 2), __builtin_amdgcn_alignbyte(a[2], a[1], 2), __builtin_amdgcn_alignbyte(a[3], a[2], 2), __builtin_amdgcn_alignbyte(c[0], a[3], 2)};
        *(u32x4*)(dst + 2 * SU_CP) = (u32x4){a[1], a[2], a[3], c[0]};
        *(u32x4*)(dst + 3 * SU_CP) = (u32x4){__builtin_amdgcn_alignbyte(a[2], a[1], 2), __builtin_amdgcn_alignbyte(a[3], a[2], 2), __builtin_amdgcn_alignbyte(c[0], a[3], 2), __builtin_amdgcn_alignbyte(c[1], c[0], 2)};
      }
    }
  }
  asm volatile("s_waitcnt lgkmcnt(0)" ::: "memory");
  SST(4)
  f32x16 cv0, cv1;
  {
    const int m0 = 48 - 15 + 8 * h + r;
    const int cbo = (int)(size_t)(LAS unsigned char*)cb;
    int ua = cbo + SU_U + (m0 & 3) * SU_CP + (m0 & ~3) * 2 - 32;
    const int kb = cbo + SU_K + 16 * h;
    int a1 = kb + (33 + r) * 80, a0 = kb + (1 + r) * 80, cnt = 32;
#define CRA(S, O) "ds_read_b64 " RNG("v", S, 2) ", %[ua] offset:" STR(O) "\n\t" "ds_read_b64 " RNG("v", (S) + 2, 2) ", %[ua] offset:" STR((O) + 8) "\n\t"
#define CRB(S, ad, O) "ds_read_b128 " RNG("v", S, 4) ", " ad " offset:" STR(O) "\n\t"
#define CMF(C, A, B) "v_mfma_f32_32x32x16_bf16 " RNG("v", C, 16) ", " RNG("v", A, 4) ", " RNG("v", B, 4) ", " RNG("v", C, 16) "\n\t"
#define CUPD "v_add_u32 %[ua], 64, %[ua]\n\t" "v_subrev_u32 %[a1], 80, %[a1]\n\t" "v_subrev_u32 %[a0], 80, %[a0]\n\t" "v_max_i32 %[a0], %[a0], %[kb]\n\t"
#define CHALF(S, N, O, UPD)                                                                       \
    WLG(5) CMF(64, S, (S) + 8) UPD CRA(N, O)                                                      \
    WLG(6) CMF(80, S, (S) + 16) CRB((N) + 8, "%[a0]", O) CRB((N) + 16, "%[a1]", O)                \
    WLG(6) CMF(80, (S) + 4, (S) + 8) CRA((N) + 4, (O) + 2048)                                     \
    WLG(7) CMF(64, S, (S) + 12) CRB((N) + 12, "%[a0]", (O) + 5200)                                \
    WLG(7) CMF(80, S, (S) + 20) CRB((N) + 20, "%[a1]", (O) + 5200)                                \
    CMF(80, (S) + 4, (S) + 12)
    asm volatile(
        CRA(96, 32) CRB(104, "%[a0]", 32) CRB(112, "%[a1]", 32) CRA(100, 32 + 2048) CRB(108, "%[a0]", 32 + 5200) CRB(116, "%[a1]", 32 + 5200)
        ZER(64) ZER(68) ZER(72) ZER(76) ZER(80) ZER(84) ZER(88) ZER(92)
        ".Lc%=:\n\t"
        CHALF(96, 120, 0, "")
        CHALF(120, 96, 32, CUPD)
        "s_sub_u32 %[cnt], %[cnt], 1\n\t"
        "s_cmp_lg_u32 %[cnt], 0\n\t"
        "s_cbranch_scc1 .Lc%=\n\t"
        "s_nop 7\n\t" "s_nop 3\n\t" WLG(0)
        : "=&{v[64:79]}"(cv0), "=&{v[80:95]}"(cv1), [ua] "+v"(ua), [a0] "+v"(a0), [a1] "+v"(a1), [cnt] "+s"(cnt)
        : [kb] "v"(kb)
        : CL96, CL112, CL120, CL8(128, 129, 130, 131, 132, 133, 134, 135), CL8(136, 137, 138, 139, 140, 141, 142, 143),
          CL8(144, 145, 146, 147, 148, 149, 150, 151), CL8(152, 153, 154, 155, 156, 157, 158, 159), "scc", "memory");
  }
  SST(5)
  {
    u16* yo = (u16*)(cb + SU_K);
#pragma unroll
    for (int g = 0; g < 2; ++g)
#pragma unroll
      for (int i = 0; i < 16; i += 4) {
        const int rr = 2 * i + 4 * h, a = 32 * g + r;
        const u32x2 uu = *(const u32x2*)(cb + SU_U + (48 + 32 * a + rr) * 2);
        float y[4];
#pragma unroll
        for (int e = 0; e < 4; ++e) {
          const float uf = __uint_as_float((e & 1) ? (uu[e >> 1] & 0xffff0000u) : (uu[e >> 1] << 16));
          y[e] = rbf_hw(g ? cv1[i + e] : cv0[i + e]) + rbf_hw(uf * dsk);
        }
        *(u32x2*)(yo + a * 40 + rr) = (u32x2){pack2(y[0], y[1]), pack2(y[2], y[3])};
      }
  }
  LBAR();
  SST(6)
#pragma unroll
  for (int i = 0; i < 4; ++i) {
    const int l = 2 * (tid + 256 * i);
    u32 c[4];
#pragma unroll
    for (int j = 0; j < 4; ++j) c[j] = *(const u32*)(sm + j * SU_CH + SU_K + ((l >> 5) * 40 + (l & 31)) * 2);
    __builtin_amdgcn_raw_buffer_store_b128((u32x4){__builtin_amdgcn_perm(c[1], c[0], 0x05040100u), __builtin_amdgcn_perm(c[3], c[2], 0x05040100u),
                                                   __builtin_amdgcn_perm(c[1], c[0], 0x07060302u), __builtin_amdgcn_perm(c[3], c[2], 0x07060302u)},
                                           rst, l * 8, SSM_L * SSM_D * 2 + d0 * 4096, 16);
  }
  asm volatile("s_waitcnt vmcnt(0)" ::: "memory");
  LBAR();
  if (tid == 0) __hip_atomic_fetch_add((u32*)(xb + XYDN), 1u, RLX_AGENT);
  SST(7)
}

#define L_TOTAL 163840
__device__ __forceinline__ int ldw(__amdgpu_buffer_rsrc_t rs, int off) {
  asm volatile("" ::: "memory");
  return __builtin_amdgcn_readfirstlane(__builtin_amdgcn_raw_buffer_load_b32(rs, 0, off, 16));
}
#define GF(rt) "v[64+8*(xk&7)+4*" #rt ":64+8*(xk&7)+4*" #rt "+3]"
#define GW2 "v[128+4*xk:128+4*xk+3]"
#define GW3 "v[24+4*(xk&1):24+4*(xk&1)+3]"
#define GLOAD(S, K) "s_xor_b32 %[tq], " S ", 1024*(" K ")\n\t"
#define GTR(K) "ds_read_b128 v[24+4*((" K ")&1):24+4*((" K ")&1)+3], %[wl] offset:1024*((" K ")&31)\n\t"
#define GPACE 0
#define YAUX 16
#define UDELAY 100
#define GNOSSM 1
#define GNG 3
#define GS0 22
__device__ __forceinline__ void g_role(unsigned char* lds, const u16* __restrict__ Wo, u16* __restrict__ out, __amdgpu_buffer_rsrc_t rsx,
                                       __amdgpu_buffer_rsrc_t rsf, int fb, u32 tag, int grp, int sl, int w) {
  const int lane = fresh_lane();
  const int lr = lane & 15, lg = lane >> 4;
  const int rot = (2 * sl + 8 * w) & 31;
  const int ldsb = (int)(size_t)(LAS unsigned char*)lds;
  u32x32 xw[8], w2[4];
  LBAR();
  {
    const __amdgpu_buffer_rsrc_t rw = __builtin_amdgcn_make_buffer_rsrc((void*)Wo, 0, DM * HD * 2, 0x00020000);
    const int vq = (lane >> 3) * (DM * 2) + sl * 128 + (lane & 7) * 16;
    const int sq = (1024 * w + rot * 32) * (DM * 2);
#define GSLOT(c) ((c) < 8 ? (((c) & 3) == 3 ? 131072 + w * 8192 : w * 32768 + 8192 * (((c) & 3) + 1)) : (((c) - 8) % 3 == 2 ? 131072 + w * 8192 : w * 32768 + 8192 * (((c) - 8) % 3 + 2)))
#define GISSUE(c) _Pragma("unroll") for (int j = 0; j < 8; ++j)                                                        \
      asm volatile("s_mov_b32 m0, %0\n\ts_nop 3\n\tbuffer_load_dwordx4 %1, %2, %3 offen lds"                           \
                   :: "s"(ldsb + GSLOT(c) + j * 1024), "v"(vq), "s"(rw), "s"(sq ^ ((64 * (c) + 8 * j) * DM * 2)) : "memory");
    GISSUE(0) GISSUE(1) GISSUE(2) GISSUE(3)
    const int q = lr >> 2, p = lr & 3;
    u32x4 w3[16];
#pragma unroll
    for (int c = 0; c < 16; ++c) {
      if (c < 5) waitvm<24>(); else if (c < 14) waitvm<16>(); else if (c == 14) waitvm<8>(); else waitvm<0>();
      u32x4 f3[2];
#pragma unroll
      for (int s = 0; s < 2; ++s)
#pragma unroll
        for (int t = 0; t < 4; ++t) {
          const unsigned char* base = lds + GSLOT(c) + (32 * s + 8 * lg + q) * 128 + (t < 2 ? p * 16 + t * 8 : t * 32 + p * 8);
          const u32x2 t0 = __builtin_bit_cast(u32x2, __builtin_amdgcn_ds_read_tr16_b64_v4i16((LAS v4i16_t*)(base)));
          const u32x2 t1 = __builtin_bit_cast(u32x2, __builtin_amdgcn_ds_read_tr16_b64_v4i16((LAS v4i16_t*)(base + 512)));
          const int o = t < 2 ? ((2 * c + s) * 2 + t) * 4 : (2 * c + s) * 4;
          if (t < 2) { xw[o >> 5][(o & 31) + 0] = t0[0]; xw[o >> 5][(o & 31) + 1] = t0[1]; xw[o >> 5][(o & 31) + 2] = t1[0]; xw[o >> 5][(o & 31) + 3] = t1[1]; }
          else if (t == 2) { w2[o >> 5][(o & 31) + 0] = t0[0]; w2[o >> 5][(o & 31) + 1] = t0[1]; w2[o >> 5][(o & 31) + 2] = t1[0]; w2[o >> 5][(o & 31) + 3] = t1[1]; }
          else f3[s] = (u32x4){t0[0], t0[1], t1[0], t1[1]};
        }
      asm volatile("s_waitcnt lgkmcnt(0)" ::: "memory");
      if (c < 8) { *(u32x4*)(lds + w * 32768 + (2 * c) * 1024 + lane * 16) = f3[0]; *(u32x4*)(lds + w * 32768 + (2 * c + 1) * 1024 + lane * 16) = f3[1]; }
      else { w3[2 * (c - 8)] = f3[0]; w3[2 * (c - 8) + 1] = f3[1]; }
      if (GPACE) __builtin_amdgcn_s_sleep(GPACE);
      if (c < 4) { GISSUE(c + 4) } else if (c >= 5 && c < 13) { GISSUE(c + 3) }
    }
#pragma unroll
    for (int i = 0; i < 16; ++i) *(u32x4*)(lds + w * 32768 + (16 + i) * 1024 + lane * 16) = w3[i];
    LBAR();
  }
  wait_start(rsx, fb, tag, true);
  const int wl = ldsb + w * 32768 + lane * 16;
  const __amdgpu_buffer_rsrc_t rso = __builtin_amdgcn_make_buffer_rsrc((void*)out, 0, S_LEN * OUTLD * 2, 0x00020000);
  const int rw_ = ldsb + 131072 + w * 8192 + lane * 16, rr_ = ldsb + 131072 + w * 2048 + lane * 16;
  const int oa = ((16 * (w & 1) + lr) * OUTLD + 64 * sl + (w < 2 ? 8 * lg : 32 + 4 * lg)) * 2, ob = oa + (w < 2 ? 8 : 32);
  const int hq = (grp + GNG - (62 - GS0) % GNG) % GNG - 1, hf = hq < 2 ? hq : -1;
#define GRW(i) "ds_write_b128 %[rw], " RNG("v", 32 + 4 * (i), 4) " offset:" STR(1024 * (i)) "\n\t"
#define GRR(i) "ds_read_b128 " RNG("v", 32 + 4 * (i), 4) ", %[rr] offset:" STR(8192 * ((i) >> 1) + 1024 * ((i) & 1)) "\n\t"
#define GNXT(D, S) "s_cmp_lt_u32 " S ", " STR(GS0) "\n\t" "s_cselect_b32 " D ", 1, " STR(GNG) "\n\t" "s_add_u32 " D ", " D ", " S "\n\t"
#define GDN(S) "s_min_u32 s41, " S ", 63\n\t" "s_lshl_b32 s41, s41, 5\n\t" "v_add_u32 v23, s41, %[u0]\n\t" "v_lshrrev_b32 v22, 6, v23\n\t" \
               "v_and_b32 v23, 63, v23\n\t" "v_lshlrev_b32 v23, 7, v23\n\t" "s_lshr_b32 s41, s41, 6\n\t" "s_max_u32 s41, s41, 29\n\t" "s_lshl_b32 s41, s41, 13\n\t" "s_add_u32 s41, s41, %[pa]\n\t" \
               "buffer_load_dword v23, v23, %[rs], s41 offen sc1\n\t"
#define GOK(D) "v_cmp_le_i32 vcc, v23, v22\n\t" "s_cmp_eq_u64 vcc, 0\n\t" "s_cselect_b32 " D ", 1, 0\n\t"
static_assert(NAW == 64, "shifts above");
#define GEPI(MID, OA, OB) \
      GRW(0) GRW(1) GRW(2) GRW(3) GRW(4) GRW(5) GRW(6) GRW(7) \
      MID \
      WLG(0) "s_barrier\n\t" \
      GRR(0) GRR(1) GRR(2) GRR(3) GRR(4) GRR(5) GRR(6) GRR(7) \
      WLG(4) PKA(32, 32, 40) PKA(34, 34, 42) PKA(36, 36, 44) PKA(38, 38, 46) \
      WLG(0) PKA(48, 48, 56) PKA(50, 50, 58) PKA(52, 52, 60) PKA(54, 54, 62) \
      PKA(32, 32, 48) PKA(34, 34, 50) PKA(36, 36, 52) PKA(38, 38, 54) \
      "v_cvt_pk_bf16_f32 v20, v32, v33\n\t" "v_cvt_pk_bf16_f32 v21, v34, v35\n\t" "v_cvt_pk_bf16_f32 v18, v36, v37\n\t" "v_cvt_pk_bf16_f32 v19, v38, v39\n\t" \
      "buffer_store_dwordx2 v[20:21], " OA ", %[ro], s42 offen\n\t" \
      "buffer_store_dwordx2 v[18:19], " OB ", %[ro], s42 offen\n\t"
#define GH "v[64+4*(xk&15):64+4*(xk&15)+3]"
  asm volatile(
      "s_nop 4\n\t"
      "v_add_u32 v16, 131072, %[va]\n\t" "v_sub_u32 v15, %[ob], %[oa]\n\t" "v_add_u32 v15, v15, %[oc]\n\t"
      "s_mov_b32 s36, %[s0]\n\t" "s_mov_b32 s43, " STR(SPIN_CAP) "\n"
      ".Lp%=:\n\t"
      GDN("s36") WVM(0) GOK("s40")
      "s_cbranch_scc1 .Lr%=\n\t"
      "s_sleep 2\n\t" "s_sub_u32 s43, s43, 1\n\t" "s_cmp_gt_i32 s43, 0\n\t" "s_cbranch_scc1 .Lp%=\n"
      ".Lr%=:\n\t"
      GNXT("s37", "s36") GDN("s37")
      "s_lshl_b32 s38, s36, 18\n\t" "s_add_u32 s38, s38, %[gb]\n\t"
      ".set xk, 0\n\t.rept 8\n\t" GLOAD("s38", "xk")
      "buffer_load_dwordx4 " GF(0) ", %[va], %[rf], %[tq] offen sc1\n\t" "buffer_load_dwordx4 " GF(1) ", v16, %[rf], %[tq] offen sc1\n\t"
      ".set xk, xk+1\n\t.endr\n\t"
      WVM(16) GOK("s40")
      ".Lb%=:\n\t"
      GNXT("s37", "s36") GNXT("s41", "s37") GDN("s41") GTR("0")
      "s_cmp_lt_u32 s37, 63\n\t" "s_cselect_b32 s40, s40, 0\n\t"
      "s_cmp_lg_u32 s40, 0\n\t" "s_cselect_b32 s39, s37, s36\n\t"
      "s_lshl_b32 s39, s39, 18\n\t" "s_add_u32 s39, s39, %[gb]\n\t" "s_lshl_b32 s38, s36, 18\n\t" "s_add_u32 s38, s38, %[gb]\n\t"
      "s_lshl_b32 s42, s36, 17\n\t"
      "buffer_load_dword v17, %[vt], %[rf], s39 offen sc1\n\t"
      ".set xk, 0\n\t.rept 32\n\t"
      "s_waitcnt vmcnt(14)\n\t"
      XM1(XA(32, 0), XW(0, 0), GF(0), 1) GTR("xk+1") XM1(XA(32, 4), XW(0, 1), GF(0), 1)
      ".if xk < 24\n\t" GLOAD("s38", "xk+8") ".else\n\t" GLOAD("s39", "xk-24") ".endif\n\t"
      XM1(XA(32, 8), XW(0, 0), GF(1), 1) XM1(XA(32, 12), XW(0, 1), GF(1), 1)
      XM1(XA(32, 16), GW2, GF(0), 1) "s_waitcnt lgkmcnt(1)\n\t" XM1(XA(32, 20), GW3, GF(0), 1)
      "buffer_load_dwordx4 " GF(0) ", %[va], %[rf], %[tq] offen sc1\n\t"
      XM1(XA(32, 24), GW2, GF(1), 1) XM1(XA(32, 28), GW3, GF(1), 1)
      "buffer_load_dwordx4 " GF(1) ", v16, %[rf], %[tq] offen sc1\n\t"
      ".if xk == 16\n\t" "s_barrier\n\t" ".endif\n\t"
      ".set xk, xk+1\n\t.endr\n\t"
      GEPI(GOK("s41"), "%[oa]", "%[ob]")
      "s_mov_b32 s36, s37\n\t"
      "s_cmp_lg_u32 s40, 0\n\t" "s_mov_b32 s40, s41\n\t" "s_cbranch_scc1 .Lb%=\n\t"
      "s_cmp_lt_u32 s36, 63\n\t" "s_cbranch_scc1 .Lp%=\n\t"
      "s_cmp_lt_i32 %[hf], 0\n\t" "s_cbranch_scc1 .Le%=\n\t"
      "s_mov_b32 s36, 63\n"
      ".Lh%=:\n\t"
      GDN("s36") WVM(0) GOK("s40")
      "s_cbranch_scc1 .Li%=\n\t"
      "s_sleep 2\n\t" "s_sub_u32 s43, s43, 1\n\t" "s_cmp_gt_i32 s43, 0\n\t" "s_cbranch_scc1 .Lh%=\n"
      ".Li%=:\n\t"
      "s_lshl_b32 s38, %[hf], 17\n\t" "s_add_u32 s38, s38, 63*262144\n\t" "s_add_u32 s38, s38, %[gb]\n\t"
      ".set xk, 0\n\t.rept 16\n\t" GLOAD("s38", "xk") "buffer_load_dwordx4 " GH ", %[va], %[rf], %[tq] offen sc1\n\t" ".set xk, xk+1\n\t.endr\n\t"
      GTR("0")
      "s_lshl_b32 s42, %[hf], 16\n\t" "s_add_u32 s42, s42, 63*131072\n\t"
      ".set xk, 0\n\t.rept 32\n\t"
      ".if xk < 17\n\t" "s_waitcnt vmcnt(15)\n\t" ".else\n\t" "s_waitcnt vmcnt(31-xk)\n\t" ".endif\n\t"
      XM1(XA(32, 0), XW(0, 0), GH, 1) GTR("xk+1") XM1(XA(32, 4), XW(0, 1), GH, 1)
      XM1(XA(32, 16), GW2, GH, 1) "s_waitcnt lgkmcnt(1)\n\t" XM1(XA(32, 20), GW3, GH, 1)
      ".if xk < 16\n\t" GLOAD("s38", "xk+16") "buffer_load_dwordx4 " GH ", %[va], %[rf], %[tq] offen sc1\n\t" ".endif\n\t"
      ".if xk == 16\n\t" "s_barrier\n\t" ".endif\n\t"
      ".set xk, xk+1\n\t.endr\n\t"
      "s_nop 7\n\t"
      GEPI("", "%[oc]", "v15")
      ".Le%=:\n\t"
      WVM(0)
      : [tq] "=&s"(sl)
      : [rs] "s"(rsx), [s0] "s"(__builtin_amdgcn_readfirstlane(grp ? GS0 + grp : 0)), [gb] "s"(FO_O - 32768 + (32 * w + rot) * 1024), [va] "v"(lane * 16 + 32768),
        [rf] "s"(rsf), [wl] "v"(wl), [rw] "v"(rw_), [rr] "v"(rr_), [oa] "v"(oa), [ob] "v"(ob), [ro] "s"(rso), [u0] "v"(8 * w + (lane & 7)), [pa] "s"(PGO_A - 29 * 8192), [hf] "s"(__builtin_amdgcn_readfirstlane(hf)), [oc] "v"((w & 1) ? OOR - 64 : oa), [vt] "v"((sl >> 3) * 131072 + 32768 + (sl & 7) * 4096 + lane * 64 - rot * 1024),
        "{a[0:31]}"(xw[0]), "{a[32:63]}"(xw[1]), "{a[64:95]}"(xw[2]), "{a[96:127]}"(xw[3]),
        "{a[128:159]}"(xw[4]), "{a[160:191]}"(xw[5]), "{a[192:223]}"(xw[6]), "{a[224:255]}"(xw[7]),
        "{v[128:159]}"(w2[0]), "{v[160:191]}"(w2[1]), "{v[192:223]}"(w2[2]), "{v[224:255]}"(w2[3])
      : "v15", "v16", "v17", "v18", "v19", "v20", "v21", "v22", "v23", "vcc", CL8(24, 25, 26, 27, 28, 29, 30, 31), CL8(32, 33, 34, 35, 36, 37, 38, 39), CL8(40, 41, 42, 43, 44, 45, 46, 47),
        CL8(48, 49, 50, 51, 52, 53, 54, 55), CL8(56, 57, 58, 59, 60, 61, 62, 63), CL64, CL72, CL80, CL88, CL96, CL112, CL120,
        "s36", "s37", "s38", "s39", "s40", "s41", "s42", "s43", "scc", "memory");
}
static_assert(4 * SU_CH <= L_TOTAL && 4 * XSLOT <= L_TOTAL, "lds");
__global__ void __launch_bounds__(256, 1)
main_kernel(const u16* __restrict__ x, const u16* __restrict__ mem0, const u16* __restrict__ Wq, const u16* __restrict__ Wk, const u16* __restrict__ Wv, const u16* __restrict__ Wo,
            unsigned char* ws, const u16* __restrict__ u, const u16* __restrict__ A_re, const u16* __restrict__ A_im, const u16* __restrict__ B_re,
            const u16* __restrict__ B_im, const u16* __restrict__ C_re, const u16* __restrict__ C_im,
            const u16* __restrict__ log_dt, const u16* __restrict__ Dskip, u16* __restrict__ out
            ) {
  __shared__ __attribute__((aligned(8192))) unsigned char lds[L_TOTAL];
  const int w = __builtin_amdgcn_readfirstlane(threadIdx.x >> 6);
  u64 pc0;
  int clen;
  {
    asm volatile("s_getpc_b64 %0\n.Lkpc0:\n\ts_mov_b32 %1, .Lkpc1-.Lkpc0" : "=s"(pc0), "=s"(clen));
  }
#define LOCAL_IDS const int lane = fresh_lane(), tid = w * 64 + lane; (void)tid;
  const int si = (blockIdx.x >> 3) * 4 + (blockIdx.x & 3);
  unsigned char* xb = (unsigned char*)(((size_t)ws + XPAGE - 1) & ~(size_t)(XPAGE - 1));
  const int fb = (int)(ws + WS_F - xb);
  u32 tag;
  asm volatile("s_load_dword %0, %1, 0x0\n\ts_waitcnt lgkmcnt(0)\n\ts_add_u32 %0, %0, 256" : "=s"(tag) : "s"(ws + WS_F) : "scc", "memory");
  const __amdgpu_buffer_rsrc_t rsx = __builtin_amdgcn_make_buffer_rsrc(xb, 0, fb + (int)WS_F_SZ, 0x00020000);
  const __amdgpu_buffer_rsrc_t rsf = __builtin_amdgcn_make_buffer_rsrc(ws + WS_QX, 0, (int)(4 * WS_QKV_SZ + 2 * WS_KM_SZ), 0x00020000);
  const __amdgpu_buffer_rsrc_t rst = __builtin_amdgcn_make_buffer_rsrc(ws + WS_UT, 0, 2 * SSM_L * SSM_D * 2 + SSM_D * 3072, 0x00020000);
  if ((blockIdx.x & 7) < 4) {
    { LOCAL_IDS
    r_role(lds, si, mem0, Wq, Wk, Wv, Wo, fb, tag, pc0, clen, rsx, rsf, rst, SsmPtrs{A_re, A_im, B_re, B_im, C_re, C_im, log_dt}, tid, lane, w
           ); }
    LOCAL_IDS
    {
      for (int it = 0; it < SPIN_CAP; ++it) {
        if (ldw(rsx, XYDN) >= 255) break;
        asm volatile("s_sleep 8" ::: "memory");
      }
      asm volatile("" ::: "memory");
      asm volatile(".Lkpc1:" ::: "memory");
      unsigned char* im = lds + 65536;
      const __amdgpu_buffer_rsrc_t rso = __builtin_amdgcn_make_buffer_rsrc((void*)out, 0, S_LEN * OUTLD * 2, 0x00020000);
      u32x4 t[8];
#pragma unroll
      for (int i = 0; i < 8; ++i) { const int p = i * 256 + tid; t[i] = __builtin_amdgcn_raw_buffer_load_b128(rst, (p >> 3) * 16384 + (p & 7) * 16, SSM_L * SSM_D * 2 + si * 128, 16); }
#pragma unroll
      for (int i = 0; i < 8; ++i) {
        const int p = i * 256 + tid, un = p >> 3, rp = p & 7;
        *(u32x2*)(im + (2 * rp) * 2064 + un * 8) = (u32x2){t[i][0], t[i][1]};
        *(u32x2*)(im + (2 * rp + 1) * 2064 + un * 8) = (u32x2){t[i][2], t[i][3]};
      }
      LBAR();
#pragma unroll
      for (int i = 0; i < 8; ++i) {
        const int p = i * 256 + tid;
        __builtin_amdgcn_raw_buffer_store_b128(*(const u32x4*)(im + (p >> 7) * 2064 + (p & 127) * 16), rso, ((p >> 7) * OUTLD + DM + (p & 127) * 8) * 2, si * 16 * OUTLD * 2, YAUX);
      }
    }
  } else {
    const int g = blockIdx.x & 3, li = blockIdx.x >> 3;
    const int xi = g == 0 ? (li < 16 ? li : -1) : (li < 26 + (g < 3) ? 16 + 27 * (g - 1) + li : -1);
    const int ei = g == 0 ? li - 16 : 16 + 5 * (g - 1) + (31 - li);
    if (xi >= 0) {
      LOCAL_IDS
      const int head = xi & 31, mi = xi >> 5;
      u32* prog = (u32*)(xb + PGO_X + xi * 128);
      if (mi == 1) x_role<true>(lds, x, Wv, FO_V, head, rsf, prog, tag, lane, w);
      else x_role<false>(lds, x, mi == 0 ? Wk : Wq, mi == 0 ? FO_K : FO_Q, head, rsf, prog, tag, lane, w);
    } else {
      LOCAL_IDS
      u32 acc = 0;
#pragma unroll
      for (int i = 0; i < 4; ++i) acc += *(const u32*)(x + (size_t)((i * 32 + ei) * 256 + tid) * 64);
#define UV 0
#define WWARM(n, first) {    \
        const __amdgpu_buffer_rsrc_t rwo = __builtin_amdgcn_make_buffer_rsrc((void*)Wo, 0, DM * HD * 2, 0x00020000); \
        const int ldsb = (int)(size_t)(LAS unsigned char*)lds; \
        _Pragma("unroll 1") for (int c = 0; c < (n); ++c) { dma16(rwo, lane * 16, ((first) + c) * 16384, 1024, ldsb + w * 32768 + (c & 1) * 16384); waitvm<32>(); } }
      if (UV == 0 || (UV == 1 && ei < 16)) WWARM(4, (ei * 4 + w) * 4)
      if (UV == 2 && ei < 16) WWARM(8, (ei * 4 + w) * 8)
      if (ei == 0 && tid < 16) acc += *(const u32*)(Dskip + tid * 64);
      asm volatile("" :: "v"(acc));
      if (ei >= 16) {
      { const u32 t0 = rt32(); for (int i = 0; i < 1000 && (int)(rt32() - t0) < UDELAY; ++i) asm volatile("s_sleep 16" ::: "memory"); }
      asm volatile("s_waitcnt vmcnt(0)\n\ts_barrier" ::: "memory");
      for (int ps = 0; ps < 2; ++ps) {
      const int r0 = ((ei - 16) * 2 + ps) * 64;
#pragma unroll
      for (int hb = 0; hb < 2; ++hb) {
        u32x4 t[16];
#pragma unroll
        for (int i = 0; i < 16; ++i) t[i] = *(const u32x4*)(u + (size_t)r0 * SSM_D + ((hb * 16 + i) * 256 + tid) * 8);
#pragma unroll
        for (int i = 0; i < 16; ++i) { const int p = (hb * 16 + i) * 256 + tid; *(u32x4*)(lds + (p >> 7) * 2064 + (p & 127) * 16) = t[i]; }
      }
      LBAR();
      for (int i = 0; i < 32; ++i) {
        const int p = i * 256 + tid, un = p >> 5, rp = p & 31;
        const u32x2 a = *(const u32x2*)(lds + (2 * rp) * 2064 + un * 8), c = *(const u32x2*)(lds + (2 * rp + 1) * 2064 + un * 8);
        __builtin_amdgcn_raw_buffer_store_b128((u32x4){a[0], a[1], c[0], c[1]}, rst, un * 16384 + rp * 16, r0 * 8, 16);
      }
      asm volatile("s_waitcnt vmcnt(0) lgkmcnt(0)\n\ts_barrier" ::: "memory");
      }
      if (UV == 1) { WWARM(4, (ei * 4 + w) * 4) waitvm<0>(); }
      wait_start(rsx, fb, tag, true);
      if (tid == 0) __hip_atomic_fetch_add((u32*)(xb + XUDN), 1u, RLX_AGENT);
      }
    }
    asm volatile("s_waitcnt lgkmcnt(0)\n\ts_barrier" :: "s"(tag) : "memory");
    if (w == 0 && fresh_lane() == 0) __builtin_amdgcn_raw_buffer_store_b32(tag, rsx, 0, PGO_S + si * 128, 16);
    const bool isg = g == 0 ? li >= 16 : (g >= 5 - GNG && li < 16);
    if (xi >= 0 && !(GNOSSM && isg)) {
      LOCAL_IDS
      bool uok = false;
      wait_start(rsx, fb, tag, true);
      while (true) {
        LBAR();
        if (tid == 0) *(u32*)(lds + L_TOTAL - 16) = __hip_atomic_fetch_add((u32*)(xb + XTKT), 1u, RLX_AGENT) + 1u;
        LBAR();
        const int un = __builtin_amdgcn_readfirstlane(*(const u32*)(lds + L_TOTAL - 16));
        if (un >= 256) break;
        ssm_unit(lds, 4 * un, rst, rsx, xb, fb, uok, Dskip, tid, lane, w);
        if (isg) break;
      }
    }
    if (isg) {
      g_role(lds, Wo, out, rsx, rsf, fb, tag, g ? g + GNG - 4 : 0, li & 15, w);
    }
    else {
      const int lane = fresh_lane();
      int sh = (ei - 16) * 4 + w, lim = (NSEG - 4) * NH, wi = sh, dn = 0;
      bool go = xi < 0;
      while (true) {
        if (go) a_role(rsx, rsf, sh, lim, wi, dn, lane, fb, tag);
        if (xi >= 0 && (w >= 2 || go)) break;
        const int t = __builtin_amdgcn_readfirstlane(lane == 0 ? __hip_atomic_fetch_add((u32*)(xb + XTK2), 1u, RLX_AGENT) + 1u : 0u);
        if (t >= 128) break;
        wi = 64 + t; sh = (NSEG - 4) * NH + t; lim = sh + 1; dn = 31; go = true;
      }
    }
  }
}

extern "C" void kernel_launch(void* const* d_in, const int* in_sizes, int n_in, void* d_out, int out_size,
                              void* d_ws, size_t ws_size, hipStream_t stream) {
  const u16* x = (const u16*)d_in[0];
  const u16* mem0 = (const u16*)d_in[1];
  const u16* Wq = (const u16*)d_in[2];
  const u16* Wk = (const u16*)d_in[3];
  const u16* Wv = (const u16*)d_in[4];
  const u16* Wo = (const u16*)d_in[5];
  const u16* u = (const u16*)d_in[7];
  const u16* A_re = (const u16*)d_in[8];
  const u16* A_im = (const u16*)d_in[9];
  const u16* B_re = (const u16*)d_in[10];
  const u16* B_im = (const u16*)d_in[11];
  const u16* C_re = (const u16*)d_in[12];
  const u16* C_im = (const u16*)d_in[13];
  const u16* log_dt = (const u16*)d_in[14];
  const u16* Dskip = (const u16*)d_in[15];
  u16* out = (u16*)d_out;
  char* ws = (char*)d_ws;

  main_kernel<<<256, 256, 0, stream>>>(x, mem0, Wq, Wk, Wv, Wo, (unsigned char*)ws, u, A_re, A_im, B_re, B_im, C_re, C_im, log_dt, Dskip, out
      );
}
```

```cpp
#include <hip/hip_runtime.h>
#include <hip/hip_bf16.h>
#include <stdint.h>

#define S_LEN 2048
#define DM 1024
#define NH 32
#define DH 128
#define HD 4096
#define MEM 8
#define SEG 32
#define NSEG 64
#define LTOK 40
#define SSM_L 2048
#define SSM_D 1024
#define SSM_N 128
#define OUTLD 2048

typedef unsigned short u16;
typedef unsigned int u32;
typedef unsigned long long u64;
typedef __attribute__((ext_vector_type(8))) short bf16x8;
typedef __attribute__((ext_vector_type(4))) float f32x4;
typedef __attribute__((ext_vector_type(4))) unsigned int u32x4;
typedef __attribute__((ext_vector_type(2))) unsigned int u32x2;
typedef __bf16 bf16x2_t __attribute__((ext_vector_type(2)));
typedef short v4i16_t __attribute__((ext_vector_type(4)));

#define SENT 0xFFFFFFFFu
#define RLX_AGENT __ATOMIC_RELAXED, __HIP_MEMORY_SCOPE_AGENT

__device__ __forceinline__ float bf2f(u16 v) { return __uint_as_float(((u32)v) << 16); }
__device__ __forceinline__ u16 f2bf(float f) {
  u32 u = __float_as_uint(f);
  u32 r = (u + 0x7fffu + ((u >> 16) & 1u)) >> 16;
  if ((u & 0x7fffffffu) > 0x7f800000u) r = 0x7fc0u;
  return (u16)r;
}
__device__ __forceinline__ float rbf(float f) { return bf2f(f2bf(f)); }

#define TR 92
#define TP 176
#define TDB 60
#define TDC 108
#define NR1 4
#define XPAGE (2 << 20)
#define XRING 3
#define XLP(p, L) ((((L) >> 13) << 15) + ((p) << 14) + ((L) & 8191))
#define XO_R3 131072
#define XR1A(x, L) XLP((x) & 1, ((x) >> 1) * 16384 + (L))
#define XR3A(rg, L) (XO_R3 + XLP(rg, L))
#define XSB(s) (((s) % XRING) == 2 ? 1048576 : ((s) % XRING) * 262144)
#define XR2B 524288
#define XR2S 1048576
#define XR2(j) (((((j) & 31) + (((j) >> 6) << 5)) / 6 << 15) + (((((j) >> 5) & 1) ^ 1) << 14) + ((((j) & 31) + (((j) >> 6) << 5)) % 6) * 1280)
#define XSYN (1048576 + 262144)
#define PGO_R (XSYN + 32768)
#define XCAL (PGO_R + 32768)
static_assert(XCAL + 16384 <= XPAGE, "exchange page");
#define WS_X 0ull
#define NPG 2
#define WS_X_SZ ((u64)NPG * XPAGE)
#define WS_F (WS_X + WS_X_SZ)
#define XEPO (fb)
#define XSTA (fb + 128)
#define PGO_X (fb + 256)
#define PGO_Y (PGO_X + 96 * 128)
#define PGO_S (PGO_Y + 128 * 128)
#define PGO_D (PGO_S + 128 * 128)
#define PGO_A (PGO_D + 128 * 128)
#define XTKT (PGO_A + 192 * 128)
#define XUDN (XTKT + 128)
#define XYDN (XUDN + 128)
#define XTK2 (XYDN + 128)
#define XSTR(x) (XTK2 + 128 + (x) * 128)
#define WS_F_SZ ((u64)(2 + 96 + 128 + 128 + 128 + 192 + 8) * 128)
#define WS_XCH_END (WS_F + 131072)
#define WS_QX (WS_XCH_END)
#define WS_QKV_SZ ((u64)S_LEN * HD * 2)
#define WS_KX (WS_QX + WS_QKV_SZ)
#define WS_VX (WS_KX + WS_QKV_SZ)
#define WS_OX (WS_VX + WS_QKV_SZ)
#define WS_KM (WS_OX + WS_QKV_SZ)
#define WS_KM_SZ ((u64)NSEG * 8 * HD * 2)
#define WS_VM (WS_KM + WS_KM_SZ)
#define WS_UT (WS_VM + WS_KM_SZ)
#define WS_YT (WS_UT + (u64)SSM_L * SSM_D * 2)
#define WS_KD (WS_YT + (u64)SSM_L * SSM_D * 2)
#define WS_TOTAL (WS_KD + (u64)SSM_D * 3072)

#define SPIN_CAP 400000
#define TRY_CAP 20000
#define LAS __attribute__((address_space(3)))
#define LBAR() asm volatile("s_waitcnt lgkmcnt(0)\n\ts_barrier" ::: "memory")
__device__ __forceinline__ u32 max4u(u32x4 v) { return max(max(v[0], v[1]), max(v[2], v[3])); }
typedef float f32x2_t __attribute__((ext_vector_type(2)));
__device__ __forceinline__ u32 pack2(float a, float b) {
  f32x2_t v = {a, b};
  bf16x2_t r = __builtin_convertvector(v, bf16x2_t);
  return __builtin_bit_cast(u32, r);
}
__device__ __forceinline__ float rbf_hw(float a) { return __uint_as_float(pack2(a, a) & 0xffff0000u); }
__device__ __forceinline__ u32 nosent(u32 v) { return min(v, 0xFFFFFFFEu); }
template <int N> __device__ __forceinline__ void waitvm() { asm volatile("s_waitcnt vmcnt(%0)" ::"n"(N) : "memory"); }
typedef __attribute__((ext_vector_type(16))) float f32x16;
__device__ __forceinline__ u32 split_hl(float kv) {
  const u32 h = pack2(kv, kv) & 0xffffu;
  const float r1 = kv - __uint_as_float(h << 16);
  return h | (pack2(r1, r1) & 0xffff0000u);
}
__device__ __forceinline__ int kidx(int idx) {
  const int sh = idx < 256 ? 0 : (idx < 512 ? 1 : (idx < 1024 ? 2 : 3));
  const int q = sh ? ((idx + (1 << (sh - 1)) - 1 + ((idx >> sh) & 1)) >> sh) : idx;
  return q + 128 * sh;
}

#define FO_Q 0
#define FO_K ((int)WS_QKV_SZ)
#define FO_V ((int)(2 * WS_QKV_SZ))
#define FO_O ((int)(3 * WS_QKV_SZ))
#define FO_KM ((int)(4 * WS_QKV_SZ))
#define FO_VM ((int)(4 * WS_QKV_SZ + WS_KM_SZ))
__device__ __forceinline__ u32x4 wait_start(__amdgpu_buffer_rsrc_t rsx, int fb, u32 tag, bool slow, int xcd = 0) {
  u32x4 st;
  for (int it = 0; it < SPIN_CAP; ++it) {
    asm volatile("" ::: "memory");
    st = __builtin_amdgcn_raw_buffer_load_b128(rsx, 0, slow ? XSTA : XSTR(xcd), 16);
    if ((u32)__builtin_amdgcn_readfirstlane(st[3]) == tag) break;
    if (slow) asm volatile("s_sleep 20" ::: "memory"); else asm volatile("s_sleep 1" ::: "memory");
  }
  asm volatile("" ::: "memory");
  return st;
}
__device__ __forceinline__ int xprog(u32 v, u32 tag) { return v - tag <= 80u ? (int)(v - tag) : -1; }
__device__ __forceinline__ float xmax4(float m) {
  { auto r = __builtin_amdgcn_permlane16_swap(__float_as_uint(m), __float_as_uint(m), false, false); m = __builtin_fmaxf(__uint_as_float(r[0]), __uint_as_float(r[1])); }
  { auto r = __builtin_amdgcn_permlane32_swap(__float_as_uint(m), __float_as_uint(m), false, false); m = __builtin_fmaxf(__uint_as_float(r[0]), __uint_as_float(r[1])); }
  return m;
}
__device__ __forceinline__ float xsum4(float m) {
  { auto r = __builtin_amdgcn_permlane16_swap(__float_as_uint(m), __float_as_uint(m), false, false); m = __uint_as_float(r[0]) + __uint_as_float(r[1]); }
  { auto r = __builtin_amdgcn_permlane32_swap(__float_as_uint(m), __float_as_uint(m), false, false); m = __uint_as_float(r[0]) + __uint_as_float(r[1]); }
  return m;
}
#define MF16(a, b, c) __builtin_amdgcn_mfma_f32_16x16x32_bf16(__builtin_bit_cast(bf16x8, a), __builtin_bit_cast(bf16x8, b), c, 0, 0, 0)
__device__ __forceinline__ f32x4 rnd2(f32x4 v, float sc) {
  const u32 a = pack2(v[0], v[1]), b = pack2(v[2], v[3]);
  const u32 c = pack2(__uint_as_float(a << 16) * sc, __uint_as_float(a & 0xffff0000u) * sc), d = pack2(__uint_as_float(b << 16) * sc, __uint_as_float(b & 0xffff0000u) * sc);
  return (f32x4){__uint_as_float(c << 16), __uint_as_float(c & 0xffff0000u), __uint_as_float(d << 16), __uint_as_float(d & 0xffff0000u)};
}
struct XaOps { u32x4 q[2][4], k[2][4], v[8]; };
__device__ __forceinline__ void xa_load(XaOps& o, __amdgpu_buffer_rsrc_t rsf, int sh, int lane) {
#pragma unroll
  for (int i = 0; i < 8; ++i) {
    o.q[i >> 2][i & 3] = __builtin_amdgcn_raw_buffer_load_b128(rsf, lane * 16, FO_Q + (sh * 8 + i) * 1024, 16);
    o.k[i >> 2][i & 3] = __builtin_amdgcn_raw_buffer_load_b128(rsf, lane * 16, FO_K + (sh * 8 + i) * 1024, 16);
  }
#pragma unroll
  for (int dt = 0; dt < 8; ++dt) o.v[dt] = __builtin_amdgcn_raw_buffer_load_b128(rsf, lane * 16, FO_V + (sh * 8 + dt) * 1024, 16);
}
__device__ __forceinline__ void xa_compute(const XaOps& o, const u32x4 (&km)[4], const u32x2 (&vm)[8], __amdgpu_buffer_rsrc_t rsf, int sh, int lane) {
  const int lr = lane & 15, lg = lane >> 4;
  const float scale = 0.08838834764831845f, L2E = 1.4426950408889634f, NEG = -1000000000.0f;
  const f32x4 z = (f32x4){0, 0, 0, 0};
  f32x4 s00 = z, s0m = z, s10 = z, s11 = z, s1m = z;
#pragma unroll
  for (int ks = 0; ks < 4; ++ks) { s00 = MF16(o.k[0][ks], o.q[0][ks], s00); s10 = MF16(o.k[0][ks], o.q[1][ks], s10); s11 = MF16(o.k[1][ks], o.q[1][ks], s11); }
#pragma unroll
  for (int ks = 0; ks < 4; ++ks) { s0m = MF16(km[ks], o.q[0][ks], s0m); s1m = MF16(km[ks], o.q[1][ks], s1m); }
  s00 = rnd2(s00, scale); s10 = rnd2(s10, scale); s11 = rnd2(s11, scale); s0m = rnd2(s0m, scale); s1m = rnd2(s1m, scale);
  asm volatile("" : "+v"(s00), "+v"(s11), "+v"(s0m), "+v"(s1m));
#pragma unroll
  for (int e = 0; e < 4; ++e) {
    const bool msk = 4 * lg + e > lr;
    s00[e] = msk ? NEG : s00[e]; s11[e] = msk ? NEG : s11[e];
    s0m[e] = lg >= 2 ? -INFINITY : s0m[e]; s1m[e] = lg >= 2 ? -INFINITY : s1m[e];
  }
  float m0 = NEG, m1 = -INFINITY;
#pragma unroll
  for (int e = 0; e < 4; ++e) { m0 = __builtin_fmaxf(m0, __builtin_fmaxf(s00[e], s0m[e])); m1 = __builtin_fmaxf(__builtin_fmaxf(m1, s10[e]), __builtin_fmaxf(s11[e], s1m[e])); }
  m0 = xmax4(m0); m1 = xmax4(m1);
  float t0 = 0.f, t1 = 0.f;
#pragma unroll
  for (int e = 0; e < 4; ++e) {
    s00[e] = __builtin_amdgcn_exp2f((s00[e] - m0) * L2E); s0m[e] = __builtin_amdgcn_exp2f((s0m[e] - m0) * L2E);
    s10[e] = __builtin_amdgcn_exp2f((s10[e] - m1) * L2E); s11[e] = __builtin_amdgcn_exp2f((s11[e] - m1) * L2E); s1m[e] = __builtin_amdgcn_exp2f((s1m[e] - m1) * L2E);
    t0 += s00[e] + s0m[e]; t1 += (s10[e] + s11[e]) + s1m[e];
  }
  const float em = __builtin_amdgcn_exp2f((NEG - m0) * L2E);
  t0 += 4.f * em;
  const float r0 = __builtin_amdgcn_rcpf(xsum4(t0)), r1 = __builtin_amdgcn_rcpf(xsum4(t1));
  s00 *= r0; s0m *= r0; s10 *= r1; s11 *= r1; s1m *= r1;
  const u32x4 p0 = (u32x4){pack2(s00[0], s00[1]), pack2(s00[2], s00[3]), pack2(s0m[0], s0m[1]), pack2(s0m[2], s0m[3])};
  const u32x4 p1 = (u32x4){pack2(s10[0], s10[1]), pack2(s10[2], s10[3]), pack2(s11[0], s11[1]), pack2(s11[2], s11[3])};
  const u32x4 pm = (u32x4){0u, 0u, pack2(s1m[0], s1m[1]), pack2(s1m[2], s1m[3])};
  const u32 pe = pack2(em * r0, em * r0);
  const u32x4 px = (u32x4){0u, 0u, pe, pe};
#pragma unroll
  for (int pr = 0; pr < 4; ++pr) {
    f32x4 o0[2], o1[2];
#pragma unroll
    for (int t = 0; t < 2; ++t) {
      const int dt = 2 * pr + t;
      const u32x4 a0 = (u32x4){o.v[dt][0], o.v[dt][1], vm[dt][0], vm[dt][1]};
      o0[t] = MF16(a0, p0, z);
      o1[t] = MF16(o.v[dt], p1, z);
      o0[t] = MF16(o.v[dt], px, o0[t]);
      o1[t] = MF16(a0, pm, o1[t]);
    }
    const int ob = FO_O + ((sh >> 5) * 256 + (sh & 31) * 4 + pr) * 1024;
    __builtin_amdgcn_raw_buffer_store_b128((u32x4){pack2(o0[0][0], o0[0][1]), pack2(o0[0][2], o0[0][3]), pack2(o0[1][0], o0[1][1]), pack2(o0[1][2], o0[1][3])}, rsf, lane * 16, ob, 16);
    __builtin_amdgcn_raw_buffer_store_b128((u32x4){pack2(o1[0][0], o1[0][1]), pack2(o1[0][2], o1[0][3]), pack2(o1[1][0], o1[1][1]), pack2(o1[1][2], o1[1][3])}, rsf, lane * 16, ob + 131072, 16);
  }
}
#define NAW 64
__device__ __forceinline__ void a_role(__amdgpu_buffer_rsrc_t rsx, __amdgpu_buffer_rsrc_t rsf, int sh, int lim, int wi, int done, int lane, int fb, u32 tag) {
  const int lr = lane & 15, lg = lane >> 4;
  const int okm = (lg * 8 + (lr & 7)) * 16, ovm = lg < 2 ? (lg * 16 + lr) * 8 : 0x7ffffff0;
  int rp = 0, xp = -1, spin = 0, pub = done, miss = 0, tp = 200;
  u32 tseen = 0, tq0 = 0, tq1 = 0, tq2 = 0, tq3 = 0; (void)tq0; (void)tq1; (void)tq2; (void)tq3;
#define XA_WAITX(s_)                                                                  \
  while (xp <= ((s_) >> 5)) {                                                                                          \
    asm volatile("" ::: "memory");                                                                                     \
    int pv = min(xprog(__builtin_amdgcn_raw_buffer_load_b32(rsx, lane * 128, PGO_X, 16), tag), xprog(__builtin_amdgcn_raw_buffer_load_b32(rsx, (64 + (lane & 31)) * 128, PGO_X, 16), tag)); \
    _Pragma("unroll") for (int o_ = 1; o_ < 64; o_ *= 2) pv = min(pv, __shfl_xor(pv, o_));                             \
    xp = __builtin_amdgcn_readfirstlane(pv);                                                                           \
    if (xp > ((s_) >> 5) || ++spin > SPIN_CAP) break;                                                                  \
    asm volatile("s_sleep 16" ::: "memory");                                                                           \
  }
#define XA_PUB() if (pub != done) { pub = done; asm volatile("s_nop 4\n\tbuffer_store_dword %0, %1, %2, %3 offen sc1" :: "v"(done), "v"(lane ? 0x7ffffff0 : 0), "s"(rsx), "s"(PGO_A + wi * 128) : "memory"); }
#define XA_STEP(CUR, NXT)                                                                                              \
  {                                                                                                                    \
    const int seg = sh >> 5, nx = sh + NAW;                                                                            \
    XA_WAITX(min(nx, lim - 1))                                                                                         \
    if (rp <= seg) {                         \
      while (true) {                                                                                                   \
        int want = min(seg + 1, NSEG - 1);                                                                             \
        if (tseen) {                                                                                                   \
          const u32 due = tseen + (u32)(want - rp) * tp - (want + 1 < NSEG ? 0 : XA_LAST) + (seg + 1 < NSEG ? 0 : XA_END); \
          if ((int)(due - (u32)__builtin_amdgcn_s_memrealtime()) > 0 || miss) { asm volatile("s_waitcnt vmcnt(0)" ::: "memory"); XA_PUB() } \
          for (int i_ = 0; i_ < 4000 && (int)(due - (u32)__builtin_amdgcn_s_memrealtime()) > 0; ++i_) asm volatile("s_sleep 2" ::: "memory"); \
          if (!miss) want = min(max(want, rp + max((int)((u32)__builtin_amdgcn_s_memrealtime() - tseen), 0) / tp), NSEG - 1); \
        }                                                                                                              \
        XA_DBG4                                                                                                        \
        asm volatile("" ::: "memory");                                                                                 \
        if (seg + 1 < NSEG) {                                                                                          \
          const int r_ = __builtin_amdgcn_readfirstlane(__builtin_amdgcn_raw_buffer_load_b32(rsx, 0, pgr + want * 128, 16)); \
          if (r_ >= 0) { const u32 n_ = (u32)__builtin_amdgcn_s_memrealtime(); tseen = n_ - ((n_ - (r_ >> 16)) & 0x7fffu) + XA_LAG; tp = max(r_ & 1023, 1); rp = want; miss = 0; break; } \
        } else if (__ballot(__builtin_amdgcn_raw_buffer_load_b32(rsx, ((sh & 31) * 4 + (lane & 3)) * 128, PGO_D, 16) == SENT) == 0) break; \
        if (++spin > SPIN_CAP) break;                                                                                  \
        if (miss++ && seg + 2 < NSEG) asm volatile("s_sleep 20" ::: "memory");                                                         \
      }                                                                                                                \
    }                                                                                                                  \
    asm volatile("" ::: "memory");                                                                                     \
    XA_DBG2                                                                                                            \
    u32x4 km[4]; u32x2 vm[8];                                                                                          \
    _Pragma("unroll") for (int i = 0; i < 4; ++i) km[i] = __builtin_amdgcn_raw_buffer_load_b128(rsf, okm, FO_KM + (sh * 4 + i) * 512, 16); \
    _Pragma("unroll") for (int i = 0; i < 8; ++i) vm[i] = __builtin_amdgcn_raw_buffer_load_b64(rsf, ovm, FO_VM + (sh * 8 + i) * 256, 16);  \
                              \
    const int pk_ = min(rp + max((int)((u32)__builtin_amdgcn_s_memrealtime() - tseen), 0) / tp, NSEG - 1);              \
    const u32 pv_ = __builtin_amdgcn_raw_buffer_load_b32(rsx, 0, pgr + pk_ * 128, 16);                                 \
    asm volatile("" ::: "memory");                                                                                     \
    __builtin_amdgcn_s_waitcnt(0x0F70);                                                                   \
    asm volatile("" ::: "memory");                                                                                     \
    { const int r_ = __builtin_amdgcn_readfirstlane(pv_);                                                              \
      if (r_ >= 0) { const u32 n_ = (u32)__builtin_amdgcn_s_memrealtime(); tseen = n_ - ((n_ - (r_ >> 16)) & 0x7fffu) + XA_LAG; tp = max(r_ & 1023, 1); rp = pk_; } } \
    XA_PUB()                                   \
    if (nx < lim) xa_load(NXT, rsf, nx, lane);                                                                         \
    XA_DBG3                                                                                                            \
    __builtin_amdgcn_sched_barrier(0);                                                                                 \
    xa_compute(CUR, km, vm, rsf, sh, lane);                                                                            \
    ++done;                                                                                                            \
    XA_DBG                                                                                                             \
    sh = nx;                                                                                                           \
    if (sh >= lim) break;                                                                                              \
  }
#define XA_LAST 24
#define XA_END 0
#define XA_LAG 30
#define XA_DBG
#define XA_DBG2
#define XA_DBG3
#define XA_DBG4
  const int pgr = PGO_R + (__builtin_amdgcn_readfirstlane(wait_start(rsx, fb, tag, true)[2]) & 8192);
  XaOps A, B;
  if (sh >= NAW) {
    const int r_ = __builtin_amdgcn_readfirstlane(__builtin_amdgcn_raw_buffer_load_b32(rsx, 0, pgr + 40 * 128, 16));
    if (r_ >= 0) { const u32 n_ = (u32)__builtin_amdgcn_s_memrealtime(); tseen = n_ - ((n_ - (r_ >> 16)) & 0x7fffu) + XA_LAG; tp = max(r_ & 1023, 1); rp = 40; }
  }
  XA_WAITX(sh)
  asm volatile("" ::: "memory");
  xa_load(A, rsf, sh, lane);
  while (true) { XA_STEP(A, B) XA_STEP(B, A) }
  asm volatile("s_waitcnt vmcnt(0)" ::: "memory");
  XA_PUB()
}

typedef __attribute__((ext_vector_type(32))) unsigned int u32x32;
typedef __attribute__((ext_vector_type(16))) unsigned int u32x16;
typedef __attribute__((ext_vector_type(8))) unsigned int u32x8;
#define STR2(x) #x
#define STR(x) STR2(x)
#define RNG(f, b, n) f "[" STR(b) ":" STR((b) + (n)-1) "]"
#define REG(b) "v[" STR(b) "]"
#define LR_RED 16384
#define LR_CTL LR_RED
#define LR_STG 32768
#define L_RSTAMP LR_STG
#define STAMPV(i, v)
#define EXF (NH * 8 * 1024)
#define EXK 65536
#define OOR 0x7ffffff0

__device__ __forceinline__ u32 lds_word(int addr) {
  u32 v;
  asm volatile("ds_read_b32 %0, %1\n\ts_waitcnt lgkmcnt(0)" : "=v"(v) : "v"(addr) : "memory");
  return __builtin_amdgcn_readfirstlane(v);
}
__device__ __forceinline__ int fresh_lane() {
  int l;
  asm volatile("v_mbcnt_lo_u32_b32 %0, -1, 0\n\tv_mbcnt_hi_u32_b32 %0, -1, %0" : "=v"(l));
  return l;
}
__device__ __forceinline__ u32 rt32() { return (u32)__builtin_amdgcn_s_memrealtime(); }
#define SYO(s) (XSYN + flip + (s) * 64)
#define TKS 0.85f
#define TLB 1
#define TLC 1
#define TLP 2
#define KG0 0
#define CLS0 0u
#define APC 5
#define ADL 12
#define CALN 2
#define TE1 4
#define TE2 3
#define TE3 2
#define XTH 4
#define TS3 42
#define TE0 30
#define TD0 170
#define TD 120
#define PHASE 1
#define XSK 0
#define CLKS(T, R, X) WLG(2) "s_sub_u32 s49, " T ", " R "\n\t" "s_cmp_le_i32 s49, " STR(3 * PHASE) "\n\t" "s_cbranch_scc1 " X "\n\t"
#define CLKH(OFF) WLG(0) "s_memrealtime s[72:73]\n\t" "s_add_u32 s50, s43, s44\n\t" "s_add_u32 s50, s50, " OFF "\n\t" "s_nop 1\n\t" "s_memrealtime s[74:75]\n\t"
#define CLKL(L, T)   L ":\n\t" "s_memrealtime s[76:77]\n\t" CLKS(T, "s72", L "x") "s_memrealtime s[72:73]\n\t" CLKS(T, "s74", L "x")     \
                     "s_memrealtime s[74:75]\n\t" CLKS(T, "s76", L "x") "s_branch " L "\n" \
                     L "x:\n\t" CLKP(L)
#define CLKW(L, OFF) CLKH(OFF) "s_nop 3\n" CLKL(L, "s50")
#define CLKP(L) "s_cmp_lt_i32 s49, 1\n\t" "s_cbranch_scc1 " L "y\n\t" "s_nop " STR(PHN) "\n\t" "s_cmp_lt_i32 s49, 2\n\t" "s_cbranch_scc1 " L "y\n\t" "s_nop " STR(PHN) "\n\t" \
                "s_cmp_lt_i32 s49, 3\n\t" "s_cbranch_scc1 " L "y\n\t" "s_nop " STR(PHN) "\n\t" "s_nop 0\n\t" "s_branch " L "y\n\t" "s_nop 0\n" L "y:\n\t"
#define PHN 3
#define REL(L) "s_barrier\n" L ":\n\t"
#define RELV(L) "s_cbranch_vccnz " L "\n\t" REL(L)
#define RELN(L) "s_cmp_eq_u32 %[wv], 0\n\t" "s_cbranch_scc1 " L "a\n\t" "s_cmp_eq_u32 %[wv], 1\n\t" "s_cbranch_scc1 " L "b\n\t" \
                "s_barrier\n\t" "s_nop " STR(PD2) "\n\t" "s_branch " L "\n" L "b:\n\t" "s_barrier\n\t" "s_branch " L "\n" L "a:\n\t" "s_barrier\n" L ":\n\t"
#define PD2 2
#define TS_(n) "s_memtime s[80+2*" #n ":81+2*" #n "]\n\t"
#define TSA(n)
#define TSB(n)
#define TSW(n)
#define TVM 12
template <int NTL, int KS>
__device__ __forceinline__ void tr_chunk(const u16* S, int lr, int lg, u32 (&r)[NTL][KS][8]) {
  const int q = lr >> 2, p = lr & 3;
#pragma unroll
  for (int t = 0; t < NTL; ++t)
#pragma unroll
    for (int i = 0; i < KS; ++i)
#pragma unroll
      for (int kb = 0; kb < 2; ++kb) {
        const u16* base = S + (64 * i + 32 * kb + 16 * (lg >> 1) + 4 * (lg & 1) + (q & 1) + 8 * (q >> 1)) * (16 * NTL) + 16 * t + 4 * p;
        const u32x2 t0 = __builtin_bit_cast(u32x2, __builtin_amdgcn_ds_read_tr16_b64_v4i16((LAS v4i16_t*)(base)));
        const u32x2 t1 = __builtin_bit_cast(u32x2, __builtin_amdgcn_ds_read_tr16_b64_v4i16((LAS v4i16_t*)(base + 2 * 16 * NTL)));
        r[t][i][4 * kb + 0] = t0[0]; r[t][i][4 * kb + 1] = t0[1]; r[t][i][4 * kb + 2] = t1[0]; r[t][i][4 * kb + 3] = t1[1];
      }
}
#define SMF(acc, a, bf, bi) "v_smfmac_f32_16x16x64_bf16 " RNG("v", acc, 4) ", " RNG("v", a, 4) ", " RNG(bf, bi, 8) ", v34\n\t"
#define ZER(r) "v_mov_b64 " RNG("v", r, 2) ", 0\n\t" "v_mov_b64 " RNG("v", (r) + 2, 2) ", 0\n\t"
#define GLD(r, o) "s_xor_b32 s49, s50, " STR(o) "\n\t" "buffer_load_dwordx4 " RNG("v", r, 4) ", v32, %[rs], s49 offen sc1\n\t"
#define GL1(r, o) "s_xor_b32 s49, s50, " STR(o) "\n\t" "buffer_load_dwordx4 " RNG("v", r, 4) ", v33, %[rs], s49 offen sc1\n\t"
#define GL2(r, o) GL1(r, 4096 + (o))
#define MXA0(r) "v_max3_u32 v63, " REG(r) ", " REG((r) + 1) ", " REG((r) + 1) "\n\t"
#define MXA(r) "v_max3_u32 v63, v63, " REG(r) ", " REG((r) + 1) "\n\t"
#define MXB(r) "v_max3_u32 v63, v63, " REG((r) + 2) ", " REG((r) + 3) "\n\t"
#define CMPB "v_cmp_eq_u32 vcc, s54, v63\n\t"
#define PSA(r) "v_pk_add_f32 " RNG("v", r, 2) ", " RNG("v", r, 2) ", " RNG("v", (r) + 2, 2) "\n\t"
#define PSW(r, o) "ds_write_addtid_b32 " REG(r) " offset:" STR(o) "\n\t"
#define RDQ(r, o) "ds_read_b128 " RNG("v", r, 4) ", v37 offset:" STR(o) "\n\t"
#define SQ(t, i) SMF(64 + 4 * (t), 96 + 4 * (i), "a", ((i) * 4 + (t)) * 8)
#define SV(t, i) SMF(24 + 4 * (t), 96 + 4 * (i), "a", 128 + ((i) * 2 + (t)) * 8)
#define SVP(t, i) SV(t, i)
#define SW(a, q, i) SMF((a) + 4 * (q), 96 + 4 * (i), "v", 128 + ((i) * 2 + (q)) * 8)
#define WVM(n) "s_waitcnt vmcnt(" STR(n) ")\n\t"
#define WLG(n) "s_waitcnt lgkmcnt(" STR(n) ")\n\t"
#define WLGF(n) WLG(n)
#define PACE 7
#define PC "s_nop " STR(PACE) "\n\t"
#define PC6
#define PC7
#define WBLK(a, n, i, M0) WVM((n) + 7 - (i)) SW(a, 0, i) M0(96 + 4 * (i)) SW(a, 1, i) MXB(96 + 4 * (i))
#define CL8(a, b, c, d, e, f, g, h) "v" #a, "v" #b, "v" #c, "v" #d, "v" #e, "v" #f, "v" #g, "v" #h
#define CL64 CL8(64, 65, 66, 67, 68, 69, 70, 71)
#define CL72 CL8(72, 73, 74, 75, 76, 77, 78, 79)
#define CL80 CL8(80, 81, 82, 83, 84, 85, 86, 87)
#define CL88 CL8(88, 89, 90, 91, 92, 93, 94, 95)
#define CL96 CL8(96, 97, 98, 99, 100, 101, 102, 103), CL8(104, 105, 106, 107, 108, 109, 110, 111)
#define CL112 CL8(112, 113, 114, 115, 116, 117, 118, 119)
#define CL120 CL8(120, 121, 122, 123, 124, 125, 126, 127)
#define PKA(d, a, b) "v_pk_add_f32 " RNG("v", d, 2) ", " RNG("v", a, 2) ", " RNG("v", b, 2) "\n\t"
#define CNT(o) STR(4096 + 64 * (o))
#define ARRIVE(o) "ds_add_u32 v45, v39 offset:" CNT(o) "\n\t"

#define EXTRA "v_readlane_b32 s69, v35, s45\n\t" "s_and_b32 s56, s58, 1023\n\t" "s_bfe_u32 s57, s58, 0xa000a\n\t" "s_lshr_b32 s55, s58, 20\n\t" "s_add_u32 s66, s56, s69\n\t" "s_add_u32 s67, s57, s69\n\t" "s_add_u32 s67, s67, s69\n\t"
#define TEX(s) ((s) == 0 ? TE0 : ((s) < 4 ? TE1 : ((s) < 12 ? TE2 : ((s) < TS3 ? TE3 : 0))))
#define R2BASE "s_bitcmp1_b32 s45, 0\n\t" "s_cselect_b32 s71, " STR(XR2S) ", 0\n\t" "s_add_u32 s71, s71, %[r2]\n\t"
#define X4B 0
#define X4C 0
#define STB "buffer_store_dwordx2 v[116:117], v11, %[rs], s36 offen sc1\n"
#define STC "buffer_store_dwordx2 v[80:81], v17, %[rs], s36 offen sc1\n\t"
#define R2TEXT                                                                                                     \
              TSB(0)                                                                                               \
              "buffer_load_dwordx4 v[64:67], v43, %[rs], s71 offen sc1\n\t"                                      \
              "buffer_load_dwordx4 v[68:71], v8, %[rs], s71 offen sc1\n\t"                                       \
              "buffer_load_dwordx4 v[76:79], v9, %[rs], s71 offen sc1\n\t"                                       \
              "buffer_load_dwordx4 v[60:63], v10, %[rs], s71 offen sc1\n\t"                                      \
              "buffer_store_dwordx4 v[72:75], v47, %[rf], s41 offen sc1\n\t"                                     \
              "s_cbranch_vccnz .Lvw%=\n\t" VPOST ARRIVE(12) "s_barrier\n" ".Lvw%=:\n\t"                             \
              "ds_read2_b32 v[96:97], v12 offset0:0 offset1:4\n\t" "ds_read2_b32 v[98:99], v12 offset0:32 offset1:36\n\t" \
              "ds_read2_b32 v[100:101], v13 offset0:0 offset1:4\n\t" "ds_read2_b32 v[102:103], v13 offset0:32 offset1:36\n\t" \
              "ds_read2_b32 v[104:105], v14 offset0:0 offset1:4\n\t" "ds_read2_b32 v[106:107], v14 offset0:32 offset1:36\n\t" \
              "ds_read2_b32 v[108:109], v15 offset0:0 offset1:4\n\t" "ds_read2_b32 v[110:111], v15 offset0:32 offset1:36\n\t" \
              "v_mov_b64 v[112:113], v[80:81]\n\t" "v_mov_b64 v[114:115], v[82:83]\n\t" "s_mov_b32 s65, 0\n\t"     \
              "v_mov_b32_dpp v112, v84 row_ror:8 row_mask:0xf bank_mask:0xc\n\t" "v_mov_b32_dpp v113, v85 row_ror:8 row_mask:0xf bank_mask:0xc\n\t" \
              "v_mov_b32_dpp v114, v86 row_ror:8 row_mask:0xf bank_mask:0xc\n\t" "v_mov_b32_dpp v115, v87 row_ror:8 row_mask:0xf bank_mask:0xc\n\t" \
              "v_pk_mul_f32 v[112:113], v[112:113], s[62:63] op_sel_hi:[1,0]\n\t" "v_pk_mul_f32 v[114:115], v[114:115], s[62:63] op_sel_hi:[1,0]\n\t" \
              WLG(0)                                                                                               \
              PKA(96, 96, 100) PKA(98, 98, 102) PKA(104, 104, 108) PKA(106, 106, 110) PKA(96, 96, 104) PKA(98, 98, 106) \
              "v_cvt_pk_bf16_f32 v96, v96, 0\n\t" "v_cvt_pk_bf16_f32 v97, v97, 0\n\t" "v_cvt_pk_bf16_f32 v98, v98, 0\n\t" "v_cvt_pk_bf16_f32 v99, v99, 0\n\t" \
              "v_accvgpr_write_b32 a194, v96\n\t" "v_accvgpr_write_b32 a202, v97\n\t" "v_accvgpr_write_b32 a198, v98\n\t" "v_accvgpr_write_b32 a206, v99\n\t" \
              TSB(1) WVM(4) TSB(2) \
              "v_permlane32_swap_b32 v64, v66\n\t" "v_permlane32_swap_b32 v65, v67\n\t" \
              "v_pk_add_f32 v[92:93], v[64:65], v[66:67]\n\t" \
              WVM(3) SFM(68) \
              "v_permlane16_swap_b32 v92, v93\n\t" \
              "v_add_f32 v94, v92, v93\n\t" \
              WVM(2) "v_mul_f32 v94, s62, v94\n\t" SFM(76) \
              "v_exp_f32 v92, v94\n\t" \
              WVM(1) TSB(3) SFM(60) \
                \
              ".Lex%=:\n\t" \
              "v_exp_f32 v88, v112\n\t" "v_exp_f32 v89, v113\n\t" "v_cvt_pk_bf16_f32 v58, v92, 0\n\t" "v_exp_f32 v90, v114\n\t" "v_exp_f32 v91, v115\n\t" \
              "v_cvt_pk_bf16_f32 v56, v88, v89\n\t" "v_cvt_pk_bf16_f32 v57, v90, v91\n\t" \
              TSB(4) ROR8(50, 58) ROR8(48, 56) \
              "v_mfma_f32_16x16x32_bf16 v[88:91], v[52:55], v[56:59], 0\n\t" \
              ROR8(49, 57) "s_nop 1\n\t" \
              "v_mfma_f32_16x16x32_bf16 v[116:119], a[192:195], v[56:59], 0\n\t" \
              "s_nop 2\n\t" \
              "v_mfma_f32_16x16x32_bf16 v[116:119], a[196:199], v[48:51], v[116:119]\n\t" \
              "v_add_f32_dpp v92, v88, v88 row_ror:8 row_mask:0xf bank_mask:0xf\n\t" "v_subrev_u32 v89, 0x33800000, v92\n\t" \
              "v_mfma_f32_16x16x32_bf16 v[124:127], a[204:207], v[56:59], 0\n\t" \
              "v_rcp_f32 v93, v92\n\t" \
              "v_mfma_f32_16x16x32_bf16 v[124:127], a[200:203], v[48:51], v[124:127]\n\t" \
              TSB(5) "v_cmp_le_u32 vcc, 0x20000000, v89\n\t" "s_nop 0\n\t" \
              "s_cbranch_vccnz .Lmx%=\n\t" \
              OTAIL STB TSB(6)
#define SFM(r) "v_pk_fma_f32 v[112:113], " RNG("v", r, 2) ", s[62:63], v[112:113] op_sel_hi:[1,0,1]\n\t" "v_pk_fma_f32 v[114:115], " RNG("v", (r) + 2, 2) ", s[62:63], v[114:115] op_sel_hi:[1,0,1]\n\t"
#define ROR8(d, s) "v_mov_b32_dpp " REG(d) ", " REG(s) " row_ror:8 row_mask:0xf bank_mask:0xf\n\t"
#define OTAIL "v_pk_mul_f32 v[116:117], v[116:117], v[92:93] op_sel:[0,1]\n\t" "v_pk_mul_f32 v[118:119], v[118:119], v[92:93] op_sel:[0,1]\n\t" \
              "v_cvt_pk_bf16_f32 v116, v116, v117\n\t" "v_cvt_pk_bf16_f32 v117, v118, v119\n\t" \
              "s_mov_b64 exec, s[78:79]\n\t" \
              "v_pk_mul_f32 v[124:125], v[124:125], v[92:93] op_sel:[0,1]\n\t" "v_pk_mul_f32 v[126:127], v[126:127], v[92:93] op_sel:[0,1]\n\t" \
              "v_cvt_pk_bf16_f32 v116, v124, v125\n\t" "v_cvt_pk_bf16_f32 v117, v126, v127\n\t" \
              "s_mov_b64 exec, -1\n\t"
#define MX3(a, b) "v_max3_u32 v95, v95, " REG(a) ", " REG(b) "\n\t"
#define R2MAX ".Lmx%=:\n\t" "v_max_u32 v95, v60, v61\n\t" MX3(62, 63) MX3(64, 65) MX3(66, 67) MX3(68, 69) MX3(70, 71) MX3(76, 77) MX3(78, 79) \
              "v_cmp_eq_u32 vcc, s54, v95\n\t" "s_cbranch_vccnz .Lfail%=\n\t" OTAIL \
              "s_cmp_lg_u32 s65, 0\n\t" "s_cbranch_scc1 .Lend%=\n\t" "s_mov_b32 s65, 1\n\t" \
              "v_max3_f32 v93, v112, v113, v94\n\t" \
              "v_max3_f32 v93, v93, v114, v115\n\t" \
              "s_nop 1\n\t" \
              "v_max_f32_dpp v92, v93, v93 row_ror:8 row_mask:0xf bank_mask:0xf\n\t" \
              "v_max_f32_dpp v88, v93, v93 row_ror:8 row_mask:0xf bank_mask:0xf\n\t" \
              "s_nop 1\n\t" \
              "v_permlane16_swap_b32 v92, v88\n\t" \
              "v_max_f32 v93, v92, v88\n\t" "v_max_f32 v92, v92, v88\n\t" \
              "s_nop 1\n\t" \
              "v_permlane32_swap_b32 v93, v92\n\t" \
              "v_max_f32 v93, v93, v92\n\t" \
              "v_sub_f32 v94, v94, v93\n\t" "v_sub_f32 v112, v112, v93\n\t" "v_exp_f32 v92, v94\n\t" "v_sub_f32 v113, v113, v93\n\t" "v_sub_f32 v114, v114, v93\n\t" "v_sub_f32 v115, v115, v93\n\t" \
              "s_branch .Lex%=\n"
#define PS1(r) "v_add_f32 " REG(r) ", " REG(r) ", " REG((r) + 2) "\n\t"
#define NOP(n) "s_nop " STR(n) "\n\t"
#define PCA 13
#define PDA 8
#define PDC 8
#define PDF 7
#define SLV 0
#define QKRED(X) RDQ(80, 12288) RDQ(84, 12304) X RDQ(88, 0) RDQ(92, 16) RDQ(112, 4096) RDQ(116, 4112) RDQ(120, 8192) RDQ(124, 8208) \
        WLG(4) TSA(4) PKA(80, 80, 88) PKA(82, 82, 90) PKA(84, 84, 92) PKA(86, 86, 94) \
        WLG(2) PKA(80, 80, 112) PKA(82, 82, 114) PKA(84, 84, 116) PKA(86, 86, 118) \
        WLG(0) PKA(80, 80, 120) PKA(82, 82, 122) PKA(84, 84, 124) PKA(86, 86, 126) \
        "v_cvt_pk_bf16_f32 v72, v80, v81\n\t" "v_cvt_pk_bf16_f32 v73, v82, v83\n\t" "v_cvt_pk_bf16_f32 v74, v84, v85\n\t" "v_cvt_pk_bf16_f32 v75, v86, v87\n\t" \
        NOP(1) TSA(5) \
        "v_mfma_f32_16x16x32_bf16 v[88:91], v[72:75], v[72:75], 0\n\t" NOP(2) \
        "v_mfma_f32_16x16x32_bf16 v[80:83], a[208:211], v[72:75], 0\n\t" NOP(2) \
        "v_mfma_f32_16x16x32_bf16 v[84:87], a[212:215], v[72:75], 0\n\t"
#define QKPUB "buffer_store_dwordx4 v[88:91], v41, %[rs], s71 offen sc1\n\t" NOP(0) \
        "buffer_store_dwordx4 v[80:83], v40, %[rs], s71 offen sc1\n\t" NOP(2) \
        "buffer_store_dwordx4 v[84:87], v40, %[rs], s71 offen offset:512 sc1\n\t"
#define MRED(W, X) "ds_read_b128 v[80:83], v46 offset:12288\n\t" W "ds_read_b128 v[84:87], v46\n\t" "ds_read_b128 v[88:91], v46 offset:4096\n\t" "ds_read_b128 v[92:95], v46 offset:8192\n\t" X TSW(4) \
        WLG(2) TSW(5) PKA(80, 80, 84) PKA(82, 82, 86) WLG(0) PKA(88, 88, 92) PKA(90, 90, 94) PKA(84, 80, 88) PKA(86, 82, 90) \
        "v_cvt_pk_bf16_f32 v80, v84, v85\n\t" "v_cvt_pk_bf16_f32 v81, v86, v87\n\t"
#define ORD(a, b) "v_cmpx_o_f32 s[50:51], " REG(a) ", " REG(b) "\n\t"
#define REDO(L) "s_cmp_lg_u64 exec, -1\n\t" "s_mov_b64 exec, -1\n\t" "s_cbranch_scc1 " L "\n" L "r:\n\t"
#define CWAIT(L, o, S) L ":\n\t" "ds_read_b32 v62, v38 offset:" CNT(o) "\n\t" WLG(0) "v_readfirstlane_b32 s49, v62\n\t" "s_cmp_lt_u32 s49, " S "\n\t" "s_cbranch_scc1 " L "\n\t"
#define VPOST PSA(24) PSA(28) PSW(24, 2560) PSW(25, 2832) PSW(28, 3200) PSW(29, 3472)
#define BARA "s_barrier\n\t"
#define BARC "s_barrier\n\t"
#define PZADR(SB, M) "s_add_u32 s49, s45, 1\n\t" "s_xor_b32 s62, s71, " STR(XR2S) "\n\t" "v_readlane_b32 s63, v19, s49\n\t" "s_mov_b32 s49, " SB "\n\t" \
        ".if !((" STR(M) ") & 1)\n\t" "s_mov_b32 s63, 0x7ff00000\n\t" ".endif\n\t" ".if !((" STR(M) ") & 2)\n\t" "s_mov_b32 s49, 0x7ff00000\n\t" ".endif\n\t" \
        ".if !((" STR(M) ") & 4)\n\t" "s_mov_b32 s62, 0x7ff00000\n\t" ".endif\n\t" "s_cmp_eq_u32 %[wv], 0\n\t" \
        "s_cselect_b32 s49, s49, s62\n\t" "s_cselect_b32 s63, s63, s62\n\t" "v_add_u32 v48, s49, v23\n\t" "s_mov_b32 exec_hi, 0\n\t" "v_add_u32 v48, s63, v23\n\t" "s_mov_b32 exec_hi, -1\n\t"
#define PZST "buffer_store_dwordx4 v[92:95], v48, %[rs], 0 offen sc1\n\t"
#define MA 0
#define MC 0
#define MB (7 - MA - MC)
#define PZD 1
#define PZB0 PZADR("s63", MB)
#define PZB PZST
#define PZC0
#define PZC ""
#define PZNC 0
#define PZA0
#define PZA
#define PZNA 0
#define LASTC(L) RELV(L)
#define LASTA(L) ""
#define PCL PCA
#define ABODY(W, X, F, n) TSA(0) GLD(96, 0) NOP(PCA) GLD(100, 1024) NOP(PCA) GLD(104, 2048) W GLD(108, 3072) "s_mov_b32 s47, 0\n\t" X \
        WVM((n) + 3) TSA(1) SQ(0, 0) SQ(1, 0) SQ(2, 0) SQ(3, 0) \
        WVM((n) + 2) SQ(0, 1) SQ(1, 1) SQ(2, 1) SQ(3, 1) \
        WVM((n) + 1) SQ(0, 2) MXA0(96) MXB(96) SQ(1, 2) MXA(100) MXB(100) SQ(2, 2) MXA(104) MXB(104) SQ(3, 2) \
        WVM(n) TSA(2) SQ(0, 3) MXA(108) MXB(108) CMPB SQ(1, 3) "s_add_u32 s48, s38, 4\n\t" NOP(0) NOP(0) SQ(2, 3) "s_cbranch_vccnz " F "\n\t" PS1(64) PS1(65) SQ(3, 3) PS1(68) PS1(69) \
        NOP(0) PSW(64, 0) PSW(65, 272) PSW(68, 640) PSW(69, 912) PSA(72) PSA(76) PSW(72, 1280) PSW(73, 1552) PSW(76, 1920) PSW(77, 2192)
#define CISSUE(X, Y) TSW(0) GL1(96, 0) PC GL1(100, 1024) PC GL1(104, 2048) PC GL1(108, 3072) PC GL2(112, 0) PC GL2(116, 1024) \
        "s_mov_b32 s48, s36\n\t" "s_mov_b32 s36, s37\n\t" "s_add_u32 s45, s45, 1\n\t" "s_add_u32 s38, s38, 4\n\t" "v_readlane_b32 s37, v19, s45\n\t" \
        "s_add_u32 s39, s39, " STR(EXF) "\n\t" "s_add_u32 s40, s40, " STR(EXF) "\n\t" \
        "s_add_u32 s41, s41, " STR(EXK) "\n\t" "s_add_u32 s42, s42, " STR(EXK) "\n\t" "s_add_u32 s43, s43, s55\n\t" PC6 \
        GL2(120, 2048) "s_mov_b32 s47, 2\n\t" "s_mul_i32 s70, s69, 3\n\t" R2BASE "s_add_u32 s43, s43, s70\n\t" NOP(2) PC7 X \
        GL2(124, 3072) Y
#define ZALL ZER(64) ZER(68) ZER(72) ZER(76) ZER(80) ZER(84) ZER(24) ZER(28)
#define SHRD "ds_read_b32 v62, v38\n\t"
#define SHUP WLG(0) "v_readfirstlane_b32 s49, v62\n\t" "s_max_u32 s44, s44, s49\n\t"
#define DPA(d, r, m) "v_add_f32_dpp " REG(d) ", " REG(r) ", " REG((r) + 2) " quad_perm:[0,1,2,3] row_mask:" m " bank_mask:0xf\n\t"
#define CBODY(m, X, n, F) CBODY_(80, m, X, n, F)
#define CBODY_(a, m, X, n, F) \
        WVM((m) + 7) TSW(1) WBLK(a, m, 0, MXA0) WBLK(a, m, 1, MXA) X WBLK(a, n, 2, MXA) WBLK(a, n, 3, MXA) WBLK(a, n, 4, MXA) WBLK(a, n, 5, MXA) WBLK(a, n, 6, MXA) \
        WVM(n) TSW(2) SW(a, 0, 7) MXA(124) MXB(124) CMPB SW(a, 1, 7) NOP(0) NOP(1) "s_cbranch_vccnz " F "\n\t" \
        DPA(88, a, "0x3") DPA(89, (a) + 1, "0x3") NOP(1) DPA(88, (a) + 4, "0xc") DPA(89, (a) + 5, "0xc") NOP(1) \
        "v_permlane32_swap_b32 v88, v89\n\t" "v_add_f32 v88, v88, v89\n\t" TSW(3) PSW(88, 3840)
#define FINEO
#define NEXTP "s_branch .LA%=\n"
#define NEXTN "s_branch .LNA%=\n"

__device__ __forceinline__ void clk_edge(u32& cyc, u32& tick) {
  const u32 t = rt32();
  for (int i = 0; i < 64; ++i) { tick = rt32(); if (tick != t) break; }
  cyc = (u32)__builtin_amdgcn_s_memtime();
}
__device__ __forceinline__ float clk_rate(u32 cyc0, u32 tk0) { u32 c, t; clk_edge(c, t); return (float)(c - cyc0) / (float)max(t - tk0, 1u); }
__device__ __forceinline__ void dma16(__amdgpu_buffer_rsrc_t rs, int voff, int soff, int sstep, int ldsa) {
#pragma unroll
  for (int i = 0; i < 16; ++i)
    asm volatile("s_mov_b32 m0, %0\n\ts_nop 3\n\tbuffer_load_dwordx4 %1, %2, %3 offen lds" :: "s"(ldsa + i * 1024), "v"(voff), "s"(rs), "s"(soff ^ (i * sstep)) : "memory");
}

#define KH(t) (192 + 8 * (t))
#define KMF(t, a, b) "v_mfma_f32_16x16x32_bf16 " RNG("v", 80 + 4 * (t), 4) ", " RNG("v", KH(t) + 4 * (a), 4) ", " RNG("v", KH(3) + 4 * (b), 4) ", " RNG("v", 80 + 4 * (t), 4) "\n\t"
#define KX(j) (92 + 4 * ((j) & 1))
#define KA(t, j) "v_pk_mul_f32 " RNG("v", KX(j), 2) ", " RNG("v", 64 + 4 * (j), 2) ", " RNG("v", 112 + ((t) & 2), 2) " op_sel:[0," STR((t) & 1) "] op_sel_hi:[1," STR((t) & 1) "]\n\t"
#define KF(j) "v_fract_f32 " REG(KX(j) + 1) ", " REG(KX(j) + 1) "\n\t"
#define KE(j, N) "v_exp_f32 " REG(KX(j)) ", " REG(KX(j)) "\n\t" "v_cos_f32 " REG(KX(j) + 2) ", " REG(KX(j) + 1) "\n\t" "v_sin_f32 " REG(KX(j) + 3) ", " N REG(KX(j) + 1) "\n\t"
#define KM(j) "v_pk_mul_f32 " RNG("v", KX(j) + 2, 2) ", " RNG("v", KX(j) + 2, 2) ", " RNG("v", KX(j), 2) " op_sel_hi:[1,0]\n\t"
#define KC(j) "v_pk_mul_f32 " RNG("v", KX(j), 2) ", " RNG("v", KX(j) + 2, 2) ", " RNG("v", 66 + 4 * (j), 2) " op_sel_hi:[1,0]\n\t" \
  "v_pk_fma_f32 " RNG("v", KX(j), 2) ", " RNG("v", KX(j) + 2, 2) ", " RNG("v", 66 + 4 * (j), 2) ", " RNG("v", KX(j), 2) " op_sel:[1,1,0] op_sel_hi:[0,1,1] neg_hi:[1,0,0]\n\t"
#define KD(t, j, r) "v_cvt_pk_bf16_f32 " REG(KH(t) + (j)) ", " REG(r) ", " REG((r) + 1) "\n\t" \
  "v_dot2c_f32_bf16 " REG(r) ", s47, " REG(KH(t) + (j)) "\n\t" "v_dot2c_f32_bf16 " REG((r) + 1) ", s48, " REG(KH(t) + (j)) "\n\t"
#define KL(t, j, r) "v_cvt_pk_bf16_f32 " REG(KH(t) + 4 + (j)) ", " REG(r) ", " REG((r) + 1) "\n\t"
#define KDM "s_add_u32 m0, m0, 1024\n\t" "s_add_u32 s43, s43, s42\n\t" "buffer_load_dwordx4 v110, s[36:39], s43 offen lds\n\t"
#define KP(j) "ds_read_b128 " RNG("v", 64 + 4 * (j), 4) ", v111 offset:" STR(16 * (j)) "\n\t"
#define KV(j, L, A) KE(j, "-") L KM(j) KC(j) A KD(3, j, KX(j))
#define KU(t, j, L, A) KE(j, "") L KM(j) A KD(t, j, KX(j) + 2)
#define KSUB                                                                                                        \
  KV(0, WLG(8) "s_add_u32 m0, m0, 1024\n\t", KA(3, 1)) KV(1, KL(3, 0, KX(0)), KA(3, 2))                              \
  "s_xor_b32 s43, s40, s41\n\t" "buffer_load_dwordx4 v110, s[36:39], s43 offen lds\n\t" KV(2, KL(3, 1, KX(1)), KA(3, 3)) \
  KV(3, KL(3, 2, KX(2)), KA(0, 0))                                                                                  \
  KU(0, 0, KL(3, 3, KX(3)), KA(0, 1)) KU(0, 1, KL(0, 0, KX(0) + 2), KA(0, 2)) KDM KU(0, 2, KL(0, 1, KX(1) + 2), KA(0, 3)) \
  KU(0, 3, KL(0, 2, KX(2) + 2), KA(1, 0)) KF(0)                                                                     \
  KU(1, 0, KL(0, 3, KX(3) + 2), KA(1, 1)) KF(1) KMF(0, 1, 0) KU(1, 1, KL(1, 0, KX(0) + 2), KA(1, 2)) KF(2) KDM      \
  KMF(0, 0, 1) KU(1, 2, KL(1, 1, KX(1) + 2), KA(1, 3)) KF(3) KMF(0, 0, 0) KU(1, 3, KL(1, 2, KX(2) + 2), KA(2, 0)) KF(0) KP(0) \
  KU(2, 0, KL(1, 3, KX(3) + 2), KA(2, 1)) KF(1) KP(1) KMF(1, 1, 0) KU(2, 1, KL(2, 0, KX(0) + 2), KA(2, 2)) KF(2) KP(2) KDM \
  KMF(1, 0, 1) KU(2, 2, KL(2, 1, KX(1) + 2), KA(2, 3)) KF(3) KP(3) KMF(1, 0, 0) KU(2, 3, KL(2, 2, KX(2) + 2), WLG(3) KA(3, 0)) \
  KMF(2, 0, 1) "v_add_u32 v111, 256, v111\n\t" "s_lshl2_add_u32 s41, s42, s41\n\t" KL(2, 3, KX(3) + 2)
#define KZERO ZER(80) ZER(84) ZER(88)
#define KCHUNK(PTR, VO, SO, ST, BUF, PF) "s_mov_b64 s[36:37], " PTR "\n\t" "s_mov_b32 m0, %[sc]\n\t" "v_mov_b32 v110, " VO "\n\t" SO "s_mov_b32 s41, 0\n\t" "s_mov_b32 s42, " STR(ST) "\n\t" \
  "buffer_load_dword " PF ", s[36:39], 0 offen lds\n\t" "s_add_u32 m0, %[sl], " STR((BUF) * 16384 - 1024) "\n\t"
#define KCALL "s_call_b64 s[44:45], .Lkg%=\n\t" WVM(16) KMF(2, 0, 0)
#define KTR(F, d, A, o) "ds_read_b64_tr_b16 " RNG(F, d, 2) ", " A " offset:" STR(o) "\n\t"
#define KTQ1(d, o) KTR("a", d, "%[aq]", o) KTR("a", (d) + 2, "%[aq]", (o) + 128) KTR("a", (d) + 4, "%[aq]", (o) + 2048) KTR("a", (d) + 6, "%[aq]", (o) + 2176)
#define KTQ(d, o) KCALL KTQ1(d, o) KMF(2, 1, 0) KTQ1((d) + 8, (o) + 32)
#define KTO1(d, o) KTR("v", d, "%[ao]", o) KTR("v", (d) + 2, "%[ao]", (o) + 64) KTR("v", (d) + 4, "%[ao]", (o) + 1024) KTR("v", (d) + 6, "%[ao]", (o) + 1088)
#define KTO(d, o) KCALL KTO1(d, o) KMF(2, 1, 0) KTO1((d) + 8, (o) + 2048)
#define KOUT(o) "s_add_u32 s43, %[ko], " STR(o) "\n\t"                                                              \
  "buffer_store_dwordx4 v[80:83], %[l16], %[rt], s43 offen sc1\n\t" "buffer_store_dwordx4 v[84:87], %[l16], %[rt], s43 offen offset:1024 sc1\n\t" "s_nop 3\n\t" \
  "buffer_store_dwordx4 v[88:91], %[l16], %[rt], s43 offen offset:2048 sc1\n\t"
struct SsmPtrs { const u16 *A_re, *A_im, *B_re, *B_im, *C_re, *C_im, *log_dt; };
__device__ __forceinline__ void r_role(unsigned char* lds, int b, const u16* __restrict__ mem0, const u16* __restrict__ Wq, const u16* __restrict__ Wk, const u16* __restrict__ Wv,
                                       const u16* __restrict__ Wo, int fb, u32 tag, u64 pc0, int clen, __amdgpu_buffer_rsrc_t rsx, __amdgpu_buffer_rsrc_t rsf,
                                       __amdgpu_buffer_rsrc_t rst, SsmPtrs sp, int tid, int lane, int w
                                       ) {
  const int lr = lane & 15, lg = lane >> 4;
  const int ldsb = (int)(size_t)(LAS unsigned char*)lds;
  const int idx = (lr & 2) ? 0xEEEE : 0x4444;
  u32 tcnt = 0; (void)tcnt;
  u32 cyc0 = 0, tk0 = 0;
  if (tid < 8) *(u32*)(lds + LR_CTL + tid * 4) = 0u;
  *(u32*)(lds + LR_CTL + 4096 + 256 + tid * 4) = 0u;
  const int xcd = b & 3, li = b >> 2;
  const int head = xcd * 8 + (li >> 2), ks = li & 3;
  const int vcu = head * 4 + ks;
  const int cg = (xcd >> 1) * 32 + li, rg = xcd & 1;
  const int rot1 = ((li >> 1) + w) & 3, rot3 = ((li >> 2) + 2 * w) & 7;
  const int pz1 = lane < 32 ? XR1A(lane >> 3, cg * 256 + rg * 128 + (lane & 7) * 16) : XR3A(lg & 1, vcu * 256 + lr * 16);
  const int pgo = PGO_X + head * 128;
  const int calo = XCAL + (li & 3) * 8192 + ((li >> 2) & 1) * 4096 + ((li >> 3) * 4 + xcd) * 128;
  u32 xp0;
  u32x32 wa[6], wo[4];
  {
    const __amdgpu_buffer_rsrc_t rwq = __builtin_amdgcn_make_buffer_rsrc((void*)Wq, 0, DM * HD * 2, 0x00020000);
    const int vq = (lane >> 2) * (HD * 2) + vcu * 64 + (lane & 3) * 16, sq = (256 * w + (rot1 << 6)) * (HD * 2);
    const int vo = (lane >> 1) * (DM * 2) + cg * 32 + (lane & 1) * 16, so = (1024 * w + (rot3 << 7)) * (DM * 2);
    const int sl = ldsb + LR_STG + w * 32768;
    const int ch0 = (b * 4 + w) * 2;
    u32x2 pin[6];
    u32 ldt;
    {
      const int po = (ch0 * SSM_N + 4 * lane) * 2;
      asm volatile("global_load_dwordx2 %0, %7, %9\n\tglobal_load_dwordx2 %1, %7, %10\n\tglobal_load_dwordx2 %2, %7, %11\n\tglobal_load_dwordx2 %3, %7, %12\n\t"
                   "global_load_dwordx2 %4, %7, %13\n\tglobal_load_dwordx2 %5, %7, %14\n\tglobal_load_ushort %6, %8, %15"
                   : "=&v"(pin[0]), "=&v"(pin[1]), "=&v"(pin[2]), "=&v"(pin[3]), "=&v"(pin[4]), "=&v"(pin[5]), "=&v"(ldt)
                   : "v"(po), "v"((ch0 + (lane >> 5)) * 2), "s"(sp.A_re), "s"(sp.A_im), "s"(sp.B_re), "s"(sp.B_im), "s"(sp.C_re), "s"(sp.C_im), "s"(sp.log_dt) : "memory");
    }
    u32x4 m0v;
    asm volatile("global_load_dwordx4 %0, %1, %2" : "=&v"(m0v) : "v"(((4 * rg + ((lane >> 1) & 3)) * DM + 16 * cg + 8 * (lane & 1)) * 2), "s"(mem0) : "memory");
    const u16* const wp[4] = {Wq, Wk, Wv, Wo};
    int pfo[5];
#pragma unroll
    for (int c = 0; c < 5; ++c) {
      const int lo_ = (int)(size_t)wp[c < 3 ? c : 3], s3 = (384 - lo_) & 1023, s63 = (8064 - lo_) & 8191;
      pfo[c] = c < 3 ? (lane < 16 ? (256 * w + li * 8 + (lane & 7)) * (HD * 2) + xcd * 2048 + (lane >> 3) * 1024 + s3
                                  : (lane < 24 && (s63 >> 11) == xcd ? (256 * w + li * 8 + (lane & 7)) * (HD * 2) + s63 : OOR))
                     : (lane < 16 ? (1024 * w + li * 16 + lane + (((c - 3) * 512) ^ ((((s3 >> 7) + 2 * w) & 4) << 7))) * (DM * 2) + (xcd >> 1) * 1024 + s3 : OOR);
    }
    const int scr = ldsb + LR_RED + 1024 + w * 256;
    asm volatile("s_mov_b32 m0, %0\n\ts_nop 3\n\tbuffer_load_dword %1, %2, 0 offen lds" :: "s"(scr), "v"(pfo[0]), "s"(rwq) : "memory");
#define KP0 16
#pragma unroll
    for (int i = 0; i < KP0; ++i)
      asm volatile("s_mov_b32 m0, %0\n\ts_nop 3\n\tbuffer_load_dwordx4 %1, %2, %3 offen lds" :: "s"(sl + i * 1024), "v"(vq), "s"(rwq), "s"(sq ^ (i * 16 * HD * 2)) : "memory");
    asm volatile("s_waitcnt vmcnt(1+" STR(KP0) ")" : "+v"(pin[0]), "+v"(pin[1]), "+v"(pin[2]), "+v"(pin[3]), "+v"(pin[4]), "+v"(pin[5]), "+v"(ldt), "+v"(m0v)::"memory");
    f32x4* prm = (f32x4*)(lds + w * 4096);
    {
      const float dt = rbf(expf(bf2f((u16)ldt)));
#pragma unroll
      for (int i = 0; i < 4; ++i) {
        float v[6];
#pragma unroll
        for (int j = 0; j < 6; ++j) v[j] = __uint_as_float((i & 1) ? (pin[j][i >> 1] & 0xffff0000u) : (pin[j][i >> 1] << 16));
        const float are = dt * v[0], aim = dt * v[1], br = dt * v[2], bi = dt * v[3];
        const float cr = v[4] * br - v[5] * bi, ci = v[4] * bi + v[5] * br;
        const float rev = aim * 0.15915494309189535f;
        prm[4 * lane + i] = (f32x4){are * 1.4426950408889634f, rev - __builtin_rintf(rev), cr, ci};
      }
    }
#pragma unroll
    for (int i = KP0; i < 16; ++i)
      asm volatile("s_mov_b32 m0, %0\n\ts_nop 3\n\tbuffer_load_dwordx4 %1, %2, %3 offen lds" :: "s"(sl + i * 1024), "v"(vq), "s"(rwq), "s"(sq ^ (i * 16 * HD * 2)) : "memory");
    const f32x4 lv = {(float)(128 * (lr >> 3) + (lr & 7)), (float)(lr < 8 ? 256 + 128 * (lr >> 2) + 2 * (lr & 3) : 512 + 128 * ((lr - 8) >> 1) + 4 * (lr & 1)),
                         (float)(lr <= 8 ? 1024 + 128 * lr : 0), (float)(8 * lr)};
    const int trow = 16 * (lg >> 1) + 4 * (lg & 1) + ((lr >> 2) & 1) + 8 * (lr >> 3);
    asm volatile(
        "s_nop 4\n\t" "s_mov_b32 s46, m0\n\t" "s_mov_b32 s38, " STR(DM * HD * 2) "\n\t" "s_mov_b32 s39, 0x20000\n\t" "s_mov_b32 s47, 0xbf80\n\t" "s_mov_b32 s48, 0xbf800000\n\t"
        WLG(0)
        "ds_read_b128 v[64:67], %[pa]\n\t" "ds_read_b128 v[68:71], %[pa] offset:16\n\t" "ds_read_b128 v[72:75], %[pa] offset:32\n\t" "ds_read_b128 v[76:79], %[pa] offset:48\n\t"
        "v_add_u32 v111, 256, %[pa]\n\t" KZERO
        KCHUNK("%[wk]", "%[vq]", "s_mov_b32 s40, %[sq]\n\t", 16 * HD * 2, 1, "%[p1]")
        WLG(0) KA(3, 0)
        KTQ(0, 0) KTQ(32, 4096) KTQ(64, 8192) KTQ(96, 12288)
        KCHUNK("%[wv]", "%[vq]", "", 16 * HD * 2, 0, "%[p2]")
        KTQ(16, 16384) KTQ(48, 16384 + 4096) KTQ(80, 16384 + 8192) KTQ(112, 16384 + 12288)
        KOUT(0)
        KCHUNK("%[wo]", "%[vo]", "s_mov_b32 s40, %[so]\n\t", 32 * DM * 2, 1, "%[p3]")
        KZERO
        KTQ(128, 0) KTQ(144, 4096) KTQ(160, 8192) KTQ(176, 12288)
        KCHUNK("%[wo]", "%[vo]", "s_xor_b32 s40, s40, " STR(512 * DM * 2) "\n\t", 32 * DM * 2, 0, "%[p4]")
        KTO(128, 16384) KTO(144, 16384 + 4096) KTO(160, 16384 + 8192) KTO(176, 16384 + 12288)
        KOUT(3072)
        "s_branch .Lke%=\n"
        ".Lkg%=:\n\t"
        KSUB
        "s_setpc_b64 s[44:45]\n"
        ".Lke%=:\n\t"
        WLG(0) "s_mov_b32 m0, s46"
        : "=&{a[0:31]}"(wa[0]), "=&{a[32:63]}"(wa[1]), "=&{a[64:95]}"(wa[2]), "=&{a[96:127]}"(wa[3]), "=&{a[128:159]}"(wa[4]), "=&{a[160:191]}"(wa[5]),
          "=&{v[128:159]}"(wo[0]), "=&{v[160:191]}"(wo[1])
        : "{v[112:115]}"(lv), [pa] "v"(ldsb + w * 4096 + lg * 64), [vq] "v"(vq), [vo] "v"(vo),
          [p1] "v"(pfo[1]), [p2] "v"(pfo[2]), [p3] "v"(pfo[3]), [p4] "v"(pfo[4]), [aq] "v"(sl + trow * 64 + (lr & 3) * 8), [ao] "v"(sl + trow * 32 + (lr & 3) * 8), [l16] "v"(lane * 16),
          [wk] "s"(Wk), [wv] "s"(Wv), [wo] "s"(Wo), [sq] "s"(sq), [so] "s"(so), [sl] "s"(sl), [sc] "s"(scr), [rt] "s"(rst), [ko] "s"(2 * SSM_L * SSM_D * 2 + ch0 * 3072)
        : CL64, CL72, CL80, CL88, CL96, CL8(192, 193, 194, 195, 196, 197, 198, 199), CL8(200, 201, 202, 203, 204, 205, 206, 207), CL8(208, 209, 210, 211, 212, 213, 214, 215),
          CL8(216, 217, 218, 219, 220, 221, 222, 223), "s36", "s37", "s38", "s39", "s40", "s41", "s42", "s43", "s44", "s45", "s46", "s47", "s48", "scc", "memory");
    {
      const u32x4 pz = (u32x4){SENT, SENT, SENT, SENT};
      if (w < 3) {
        u32x4 v = pz;
        if (w == 0 && lane < 32) v = (u32x4){nosent(m0v[0]), nosent(m0v[1]), nosent(m0v[2]), nosent(m0v[3])};
#pragma unroll
        for (int hf = 0; hf < 2; ++hf) {
          __builtin_amdgcn_raw_buffer_store_b128(v, rsx, pz1, XSB(w) + hf * 8192, 16);
          __builtin_amdgcn_raw_buffer_store_b128(pz, rsx, XR2(vcu) + lane * 16, XR2B + (w & 1) * XR2S + hf * 8192, 16);
          __builtin_amdgcn_raw_buffer_store_b128(pz, rsx, lane < 16 ? XR2(vcu) + 1024 + lane * 16 : OOR, XR2B + (w & 1) * XR2S + hf * 8192, 16);
        }
      } else {
        __builtin_amdgcn_raw_buffer_store_b128(pz, rsx, lane < 8 ? lane * 16 : OOR, calo, 16);
        if (b < 32) {
          __builtin_amdgcn_raw_buffer_store_b128(pz, rsx, lane * 16, XSYN + b * 2048, 16);
          __builtin_amdgcn_raw_buffer_store_b128(pz, rsx, lane * 16, XSYN + b * 2048 + 1024, 16);
        } else if (b >= 48 && b < 48 + 41) {
          __builtin_amdgcn_raw_buffer_store_b128(pz, rsx, lane * 16, PGO_D + (b - 48) * 1024, 16);
        }
      }
      asm volatile("" ::: "memory");
    }
    xp0 = __builtin_amdgcn_raw_buffer_load_b32(rsx, (lane & 1) * 4096, pgo, 16);
    clk_edge(cyc0, tk0);
    asm volatile("s_waitcnt vmcnt(0)\n\ts_barrier" : "+v"(xp0)::"memory");
#define CLKF() clk_rate(cyc0, tk0)
    if (w == 0) {
      const int l0 = lane ? OOR : 0;
      const u64 c0 = __builtin_amdgcn_s_memtime();
      u32 v;
      asm volatile("s_waitcnt lgkmcnt(0)\n\tbuffer_load_dword %0, off, %1, %2 sc1\n\tbuffer_store_dword %3, %4, %1, %5 offen sc1\n\ts_waitcnt vmcnt(1)"
                   : "=&v"(v) : "s"(rsx), "s"(calo), "v"(tag), "v"(l0), "s"(PGO_Y + b * 128), "s"(c0) : "memory");
      const u32 lat = (u32)(__builtin_amdgcn_s_memtime() - c0);
      const float f = CLKF();
      typedef __attribute__((ext_vector_type(3))) unsigned int u32x3;
      __builtin_amdgcn_raw_buffer_store_b96((u32x3){__float_as_uint(f), lat, tag}, rsx, l0, PGO_Y + b * 128 + 4, 16);
      asm volatile("" ::: "memory");
    }
    {
      u32 r[1][8][8];
      tr_chunk<1, 8>((const u16*)(lds + LR_STG + w * 32768), lr, lg, r);
#pragma unroll
      for (int i = 0; i < 8; ++i)
#pragma unroll
        for (int e = 0; e < 8; ++e) { const int o = (8 + i) * 8 + e; wo[o >> 5][o & 31] = r[0][i][e]; }
      asm volatile("s_waitcnt lgkmcnt(0)" ::: "memory");
    }
  }
  const int hf = (lr >> 1) & 1;
  const int voff1 = lg * 256 + (lr >> 3) * 128 + (((lr >> 2) & 1) * 2 + (lr & 1)) * 32 + hf * 16;
  const int voff3 = (lr >> 3) * 512 + (lg >> 1) * 256 + (((lr >> 2) & 1) * 2 + (lr & 1)) * 16 + (lg & 1) * 128 + hf * 64;
  const int ard = ldsb + (2 * (lr >> 3) + (lg >> 1)) * 640 + (lr & 1) * 272 + ((lr >> 1) & 3) * 64 + (lg & 1) * 32;
  const int ctl = ldsb + LR_CTL, one = w == 0 ? 2 : (w == 3 ? 4 : 1);
  const int r8 = lane & 7, hk = (lane >> 3) & 1;
#define COA 1
#define COB 1
  const int st01 = lr < 8 ? XR2(vcu) + lr * 16 + lg * 128 : OOR, st2 = (lr < 8 && lg >= 2) ? XR2(vcu) + 1024 + lr * 16 + (lg - 2) * 128 : OOR;
  const int ld01 = hk * 512 + r8 * 16 + lg * 128, ld2 = XR2(head * 4 + lg) + 1024 + r8 * 16 + hk * 128;
  int sto = XR3A(r8 >> 2, vcu * 256 + (r8 & 3) * 16 + lg * 64 + hk * 8);
  const int stk = lr >= 8 ? (lg * 8 + (lr - 8)) * 16 : OOR;
  const int lo = X4C ? 2 * (lr & 7) + (lr >> 3) : lr;
  const int arw = ldsb + LR_CTL + lane * 4, arr = ldsb + 3840 + ((lo >> 3) + 2 * ((lo >> 2) & 1)) * 64 + (lo & 3) * 16;
  const int st1 = XR1A(lg, cg * 256 + rg * 128 + lo * 8);
  const int avw = 0;
  const int avc = ldsb + 2560 + (lr >> 3) * 640 + (8 * ((lr >> 2) & 1) + (lr & 3)) * 4;
  const int avr = avc + (lg & 1) * 272 + (lg >> 1) * 64, avf = avc + (lg & 1) * 128 + (lg >> 1) * 16;
  u32x16 kc = {(u32)voff1, (u32)voff3, (u32)idx, (u32)TEX(lane), (u32)(lane < 16 ? XR2(vcu) + 1024 + lane * 16 : OOR), (u32)ard, (u32)ctl, (u32)one, (u32)st01, (u32)st2, (u32)(XR2(vcu) + lane * 16), (u32)ld2, (u32)(lane * 16), (u32)arw, (u32)arr, (u32)stk};
  int o1c = XR1A(xcd, w * 4096) + rot1 * 1024, o3c = XR3A(rg, w * 8192) + rot3 * 1024;
  const int fok = FO_K + (head * 8 + ks) * 1024, fov = FO_V + (head * 8 + 2 * ks) * 1024;
#define XPROG(v) min(xprog(__builtin_amdgcn_readlane(v, 0), tag), xprog(__builtin_amdgcn_readlane(v, 1), tag))
  int xp = w == 3 ? XPROG(xp0) : 1000;
#define XWAIT(n) for (int it_ = 0; it_ < SPIN_CAP && xp < (n); ++it_) {                                               \
    if (it_) asm volatile("s_sleep 4" ::: "memory");                                                                  \
    const u32 v_ = __builtin_amdgcn_raw_buffer_load_b32(rsx, (lane & 1) * 4096, pgo, 16); xp = XPROG(v_); asm volatile("" ::: "memory"); }
  XWAIT(1)
  u32x16 fv;
  u32x8 fk;
  {
    const int lo_ = w == 3 ? lane * 16 : OOR;
    const u32x4 v0 = __builtin_amdgcn_raw_buffer_load_b128(rsf, lo_, fov, 16), v1 = __builtin_amdgcn_raw_buffer_load_b128(rsf, lo_, fov + 1024, 16);
    const u32x4 k0 = __builtin_amdgcn_raw_buffer_load_b128(rsf, lo_, fok, 16), k1 = __builtin_amdgcn_raw_buffer_load_b128(rsf, lo_, fok + 4096, 16);
    fv = (u32x16){v0[0], v0[1], 0u, 0u, v0[2], v0[3], 0u, 0u, v1[0], v1[1], 0u, 0u, v1[2], v1[3], 0u, 0u};
    fk = (u32x8){k0[0], k0[1], k0[2], k0[3], k1[0], k1[1], k1[2], k1[3]};
    asm volatile("" : "+{a[192:207]}"(fv), "+{a[208:215]}"(fk));
  }
#define XPN 1
#define CALM 40
  XWAIT(2)
#pragma unroll
  for (int i = 0; i < 3; ++i) *(u32x4*)(lds + w * 4096 + i * 1024 + lane * 16) = (u32x4){SENT, SENT, SENT, SENT};
  {
    u64 pc; asm volatile("s_getpc_b64 %0" : "=s"(pc));
    const u32 cv = __builtin_amdgcn_raw_buffer_load_b32(__builtin_amdgcn_make_buffer_rsrc((void*)pc0, 0, clen, 0x00020000), (int)(pc - pc0) + tid * 64, 0, 0);
    asm volatile("" :: "v"(cv));
  }
  if (w == 3) { const u32 v_ = __builtin_amdgcn_raw_buffer_load_b32(rsx, (lane & 1) * 4096, pgo, 16); xp = max(xp, XPROG(v_)); }
  u32x4 st;
  if (w == 1) {
    if (b == 0) {
      u32 tt = 0, best = 0;
      bool xok = false;
      const int sto_ = lane < 5 ? (lane ? XSTR(lane - 1) : XSTA) : OOR;
      for (int it = 0; it < SPIN_CAP; ++it) {
        asm volatile("" ::: "memory");
        const u32x4 r0 = __builtin_amdgcn_raw_buffer_load_b128(rsx, lane * 128, PGO_Y, 16), r1 = __builtin_amdgcn_raw_buffer_load_b128(rsx, lane * 128 + 8192, PGO_Y, 16);
        if (!xok) xok = __ballot(xprog(__builtin_amdgcn_raw_buffer_load_b32(rsx, lane * 128, PGO_X, 16), tag) < XPN) == 0;
        const bool c0 = r0[3] == tag, c1 = r1[3] == tag;
        if (tt == 0 && __popcll(__ballot(!c0 || !c1)) <= CALM) {
          float fm = (c0 ? __uint_as_float(r0[1]) : 0.f) + (c1 ? __uint_as_float(r1[1]) : 0.f), fn = (c0 ? 1.f : 0.f) + (c1 ? 1.f : 0.f);
#pragma unroll
          for (int o = 4; o < 64; o *= 2) { fm += __shfl_xor(fm, o); fn += __shfl_xor(fn, o); }
          fm = fn > 0.f ? fm / fn : 100.f;
          const float f0 = __uint_as_float(__builtin_amdgcn_readfirstlane(__float_as_uint(fm)));
          fm = fminf(fm, __shfl_xor(fm, 1)); fm = fminf(fm, __shfl_xor(fm, 2));
          const float f_ = CLKF() * fminf(fmaxf(fm / f0, 0.8f), 1.f);
          const float k_ = 1.f + TKS * (fminf(fmaxf(23.9f / f_, 1.f), 1.5f) - 1.f);
          const int nb_ = (int)(TDB * k_ + 0.5f), nc_ = nb_ + (int)((TDC - TDB) * k_ + 0.5f), np_ = nc_ + (int)((TP - TDC) * k_ + 0.5f);
          u32 lat = min(c0 ? r0[2] : 65535u, c1 ? r1[2] : 65535u);
          { auto r = __builtin_amdgcn_permlane16_swap(lat, lat, false, false); lat = min(r[0], r[1]); }
          { auto r = __builtin_amdgcn_permlane32_swap(lat, lat, false, false); lat = min(r[0], r[1]); }
          u32 mm[4];
#pragma unroll
          for (int k = 0; k < 4; ++k)
            mm[k] = __builtin_amdgcn_readlane(lat, 4 * k) + __builtin_amdgcn_readlane(lat, 4 * k + 2) + __builtin_amdgcn_readlane(lat, 4 * (k ^ 2) + 1) + __builtin_amdgcn_readlane(lat, 4 * (k ^ 2) + 3);
          const u32 hb = mm[0] + mm[2] <= mm[1] + mm[3] ? 0u : 1u;
          best = (hb << 13) | ((hb ? mm[1] <= mm[3] : mm[0] <= mm[2]) ? CLS0 : 16384u - CLS0);
          if (__ballot(lat == 65535u) == 0) tt = __builtin_amdgcn_readfirstlane(nb_ | (nc_ << 10) | (np_ << 20));
        }
        if (__ballot(r0[0] != tag || r1[0] != tag) == 0 && xok && tt) break;
      }
      st = (u32x4){rt32() + TD0, tt, best, tag};
      __builtin_amdgcn_raw_buffer_store_b128(st, rsx, sto_, 0, 16);
    } else st = wait_start(rsx, fb, tag, false, xcd);
    if (lane == 0) *(u32x4*)(lds + LR_CTL + 32) = st;
  }
  LBAR();
  st = *(const u32x4*)(lds + LR_CTL + 32);
  const int flip = __builtin_amdgcn_readfirstlane(st[2]) & 8192, cls = __builtin_amdgcn_readfirstlane(st[2]) & 16384;
  o1c ^= cls; o3c ^= cls; sto ^= cls; kc[4] ^= cls; kc[8] ^= cls; kc[9] ^= cls; kc[10] ^= cls; kc[11] ^= cls;
  { const u32 now = rt32(); if (st[0] - now + 100000u > 200000u) st[0] = now; }
  u32x16 kd = {(u32)((ld01 + XR2(vcu ^ 1)) ^ cls), (u32)((ld01 + XR2(vcu ^ 2)) ^ cls), (u32)((ld01 + XR2(vcu ^ 3)) ^ cls), (u32)(X4B && hk ? OOR : sto), (u32)avr, (u32)(avr + 4096), (u32)(avr + 8192),
               (u32)(avr + 12288), (u32)avw, (u32)(X4C && hk ? OOR : st1 ^ cls), (u32)((lane & 1) * 4096), (u32)(XSB(lane + 1) + flip), (u32)(lane * 8), (u32)avf,
               (u32)(b == 0 && lane == 0 ? XSYN + flip : OOR), (u32)(w == 0 ? pz1 ^ cls : (w == 1 ? kc[10] : kc[4]))};
  u32x16 sa = {(u32)flip, (u32)(XSB(1) + flip), 0u, (u32)fok, (u32)fov, (u32)(FO_KM + (head * 4 + ks) * 512),
               (u32)(FO_VM + (head * 8 + 2 * ks) * 256), st[0], 0u, 0u, (u32)SYO(1), 0u, 0u, (u32)TRY_CAP, 0u, 0u};
  u32x8 sc = {(u32)o1c, (u32)o3c, SENT, 0u, 0u, 0u, st[1], (u32)pgo};
#define TUNEO
#define KCNT
#define KHST
#define KTRC
#define TRCI
#define PREP 16
#define GAP 12
#define RETA(n, L) "s_cmp_eq_u32 s49, " #n "\n\t" "s_cmov_b32 s48, .Lpc%=-" L "\n\t"
  u32x32 keep;
  u32x2 ov;
  asm volatile("" : "={v[60:91]}"(keep));
  while (true) {
    sa[14] = xp;
#pragma unroll
    for (int i = 0; i < 16; ++i) sa[i] = __builtin_amdgcn_readfirstlane(sa[i]);
#pragma unroll
    for (int i = 0; i < 8; ++i) sc[i] = __builtin_amdgcn_readfirstlane(sc[i]);
    asm volatile(
        "s_nop 4\n\t" "s_mov_b32 s60, m0\n\t"
        "s_mov_b32 m0, %[mb]\n\t" "s_mov_b32 s64, s50\n\t" "s_mov_b32 s98, s49\n\t" "s_mov_b32 s68, 0\n\t"
        EXTRA R2BASE
        "s_cmp_lg_u32 %[wv], 3\n\t" "s_cbranch_scc1 .LN%=\n\t"
        "s_mov_b32 s78, 0xff00ff00\n\t" "s_mov_b32 s79, 0xff00ff00\n\t" "s_mov_b32 s62, 0x3e0293ee\n\t"
        "v_mov_b32 v52, 0x3f803f80\n\t" "v_mov_b32 v53, 0x3f803f80\n\t" "v_mov_b32 v54, 0x3f803f80\n\t" "v_mov_b32 v55, 0x3f803f80\n\t" "v_mov_b32 v51, 0\n\t" "v_mov_b32 v59, 0\n\t"
        "v_mov_b32 v60, %[tg]\n\t" "v_mov_b32 v61, -1\n\t" "s_mov_b32 s63, %[dm]\n\t"
        "s_branch .Lent%=\n"
        ".LA%=:\n\t"
        CLKW(".Lta%=", "0") REL(".Lua%=")
        "s_add_u32 s50, s36, s52\n\t" NOP(PDA)
        ABODY(NOP(PCL) LASTA(".Lwa%="), WVM(5) "v_readlane_b32 s50, v60, 0\n\t" "v_readlane_b32 s51, v60, 1\n\t"
              "s_sub_u32 s50, s50, %[tg]\n\t" "s_sub_u32 s51, s51, %[tg]\n\t" "s_min_u32 s63, s50, s51\n\t" "s_max_u32 s50, s50, s51\n\t" "s_cmp_le_u32 s50, 80\n\t"
              "s_cselect_b32 s50, s63, 0\n\t" "v_readfirstlane_b32 s49, v61\n\t" "s_max_i32 s64, s64, s50\n\t"
              "s_cmp_lg_u32 s49, -1\n\t" "s_cmov_b32 s58, s49\n\t" EXTRA "v_mov_b32 v62, s58\n\t" "ds_write_b32 v38, v62 offset:24\n\t", ".Lfail%=", 0) TSA(3)
        QKRED(BARA) ORD(88, 89) ORD(90, 91) QKPUB TSA(6) TSB(7) REDO(".Lqk%=")
        SVP(0, 0) SHRD SVP(1, 0) SVP(0, 1) SVP(1, 1) SVP(0, 2) SVP(1, 2) WLG(0) "v_readfirstlane_b32 s49, v62\n\t" SVP(0, 3) SVP(1, 3) "s_max_u32 s44, s44, s49\n\t" "s_cmp_eq_u32 s45, 63\n\t" TSA(7)
        "s_cbranch_scc1 .Llast%=\n"
        ".LB%=:\n\t"
        CLKW(".Ltb%=", "s66")
        "s_mov_b32 s47, 1\n\t"
        R2TEXT
        ".LC%=:\n\t"
        ZALL
        "s_mov_b32 s47, 5\n\t" "s_sub_i32 s63, s64, s45\n\t" "s_cmp_lt_i32 s63, 2\n\t" "s_cbranch_scc1 .Lend%=\n\t"
        "s_cmp_lt_i32 s63, " STR(XTH) "\n\t" "s_cselect_b32 s63, s59, %[dm]\n\t"
        CLKW(".Ltc%=", "s67") REL(".Luc%=")
        "s_add_u32 s50, s36, s53\n\t" NOP(PDC)
        CISSUE(LASTC(".Lwc%="), "")
        "buffer_load_dword v61, off, %[rs], s46 sc1\n\t" "s_add_u32 s46, s46, 64\n\t"
        "s_add_u32 s49, s39, 4096\n\t" NOP(PDF)
        "buffer_load_dwordx4 a[208:211], v44, %[rf], s39 offen sc1\n\t" "buffer_load_dwordx4 a[212:215], v44, %[rf], s49 offen sc1\n\t"
        "buffer_load_dwordx2 a[192:193], v44, %[rf], s40 offen sc1\n\t" "buffer_load_dwordx2 a[196:197], v44, %[rf], s40 offen offset:8 sc1\n\t"
        "buffer_load_dwordx2 a[200:201], v44, %[rf], s40 offen offset:1024 sc1\n\t" "buffer_load_dwordx2 a[204:205], v44, %[rf], s40 offen offset:1032 sc1\n\t"
#define NXC 8
        "buffer_load_dword v60, v18, %[rs], s63 offen sc1\n\t"
        CBODY(NXC, "", NXC, ".Lfail%=")
        MRED(BARC, "") ORD(84, 85) ORD(86, 87) STC TSW(6) REDO(".Lmr%=")
        SHRD SHUP TSW(7)
        NEXTP
        CWAIT(".Lqk%=", 8, "s48") QKRED("") NOP(1) QKPUB "s_branch .Lqk%=r\n"
        CWAIT(".Lmr%=", 16, "s38") WVM(0) MRED("", "") "v_min_u32 v80, -2, v80\n\t" "v_min_u32 v81, -2, v81\n\t" STC
        "s_branch .Lmr%=r\n"
        ".Llast%=:\n\t"
        "buffer_store_dwordx4 v[72:75], v47, %[rf], s41 offen sc1\n\t" "s_mov_b32 s47, 3\n\t" NOP(4) VPOST ARRIVE(12) "s_barrier\n\t"
        "s_branch .Lend%=\n"
        R2MAX
        ".Lnorm%=:\n\t"
        "s_cmp_lg_u32 %[wv], 3\n\t" "s_cbranch_scc1 .LNA%=\n\t" "s_cmp_eq_u32 s47, 0\n\t" "s_cbranch_scc1 .LA%=\n\t" "s_branch .LC%=\n"
        ".LN%=:\n\t"
        "v_mov_b64 v[92:93], -1\n\t" "v_mov_b64 v[94:95], -1\n\t" "s_mov_b32 s65, s44\n\t" "s_mov_b32 s78, -100\n\t" "s_mov_b32 s79, 4\n\t"
        "s_branch .Lent%=\n"
        ".LNA%=:\n\t" PZA0
        "s_add_u32 s50, s36, s52\n\t"
        RELN(".Lna%=")
        ABODY(NOP(PCA), LASTA(".Lxa%=") PZA, ".Lfail%=", PZNA) BARA ARRIVE(8) TSA(3)
        SV(0, 0) SV(1, 0) SV(0, 1) SV(1, 1) SV(0, 2) SV(1, 2) SV(0, 3) SV(1, 3) ZER(64) ZER(68) ZER(72) ZER(76)
        VPOST TSA(7) ZER(80) ZER(84) ZER(24) ZER(28)
        "v_add_u32 v88, 4096, v21\n\t" "v_add_u32 v89, 8192, v21\n\t" "v_add_u32 v90, 12288, v21\n\t"
        PZB0
        "s_lshl_b32 s61, s45, 7\n\t" "s_add_u32 s61, s61, " STR(PGO_R - XSYN) "\n\t" "s_cmp_lg_u32 %[wv], 0\n\t"
        WLG(0) "s_barrier\n\t" PZB
        PSW(92, 0) PSW(92, 272) PSW(92, 640) PSW(92, 912) PSW(92, 1280) PSW(92, 1552) PSW(92, 1920) PSW(92, 2192) PSW(92, 3840)
        "ds_read2_b32 v[50:51], v38 offset0:0 offset1:6\n\t"
        "s_cbranch_scc1 .LNB%=\n\t"
        "s_memrealtime s[72:73]\n"
        ".Lv0%=:\n\t"
        "ds_read_b32 v62, v38 offset:" CNT(12) "\n\t"
        "ds_read2_b32 v[112:113], v21 offset0:0 offset1:68\n\t" "ds_read2_b32 v[114:115], v21 offset0:16 offset1:84\n\t"
        "ds_read2_b32 v[116:117], v88 offset0:0 offset1:68\n\t" "ds_read2_b32 v[118:119], v88 offset0:16 offset1:84\n\t"
        "ds_read2_b32 v[120:121], v89 offset0:0 offset1:68\n\t" "ds_read2_b32 v[122:123], v89 offset0:16 offset1:84\n\t"
        "ds_read2_b32 v[124:125], v90 offset0:0 offset1:68\n\t" "ds_read2_b32 v[126:127], v90 offset0:16 offset1:84\n\t"
        WLG(0) "v_cmp_gt_u32 vcc, s48, v62\n\t" "s_cbranch_vccnz .Lv0%=\n\t"
        "s_add_u32 s50, s72, " STR(TVM) "\n\t" "s_lshl_b32 s50, s50, 17\n\t" "s_lshr_b32 s50, s50, 1\n\t"
        PKA(112, 112, 116) PKA(114, 114, 118) PKA(120, 120, 124) PKA(122, 122, 126) PKA(112, 112, 120) PKA(114, 114, 122)
        "v_lshrrev_b32 v91, 20, v51\n\t" "v_readfirstlane_b32 s49, v50\n\t"
        "v_cvt_pk_bf16_f32 v112, v112, v113\n\t" "v_cvt_pk_bf16_f32 v113, v114, v115\n\t" "v_or_b32 v114, s50, v91\n\t"
        "s_cmp_eq_u32 s45, 63\n\t" "s_cbranch_scc1 .Lv2%=\n\t"
        "s_sleep " STR(SLV) "\n"
        ".Lv2%=:\n\t"
        "buffer_store_dwordx2 v[112:113], v20, %[rf], s42 offen sc1\n\t"
        "buffer_store_dword v114, v22, %[rs], s61 offen sc1\n\t"
        "s_cmp_eq_u32 s49, s65\n\t" "s_cbranch_scc1 .LNB%=\n\t"
        "s_mov_b32 s65, s49\n\t" KHST "s_sub_u32 s49, s45, s78\n\t" "s_mov_b32 s78, s45\n\t"
        "s_cmp_gt_i32 s49, " STR(GAP) "\n\t" "s_cbranch_scc1 .LNB%=\n\t" "s_cmp_lt_u32 s45, s79\n\t" "s_cbranch_scc1 .LNB%=\n\t"
        "s_add_u32 s79, s45, 3\n\t" "s_lshl_b32 s49, s45, 6\n\t" "v_add_u32 v50, " STR(TLB + ((TLB + TLC) << 10) + ((TLB + TLC + TLP) << 20)) ", v51\n\t"
        "buffer_store_dword v50, v22, %[rs], s49 offen offset:128 sc1\n"
        ".LNB%=:\n\t"
        WLG(0) "v_readfirstlane_b32 s58, v51\n\t" EXTRA
        "s_mov_b32 s47, 3\n\t"
        "s_cmp_eq_u32 s45, 63\n\t"
        "s_cbranch_scc1 .Lend%=\n"
        ".LNC%=:\n\t" PZC0
        "s_add_u32 s50, s36, s53\n\t"
        WLG(0) RELN(".Lnc%=")
        CISSUE("", LASTC(".Lxc%="))
        CBODY(0, PZC, PZNC, ".Lfail%=") BARC
        ARRIVE(16) TSW(4)
        NEXTN
        ".Lfail%=:\n\t" KCNT
        "s_cmp_lg_u32 s47, 2\n\t" "s_cbranch_scc1 .Lnu%=\n\t"
        "s_mov_b32 s37, s36\n\t" "s_mov_b32 s36, s48\n\t" "s_sub_u32 s45, s45, 1\n\t" "s_sub_u32 s38, s38, 4\n\t" "s_sub_u32 s39, s39, " STR(EXF) "\n\t" "s_sub_u32 s40, s40, " STR(EXF) "\n\t"
        "s_sub_u32 s41, s41, " STR(EXK) "\n\t" "s_sub_u32 s42, s42, " STR(EXK) "\n\t" "s_sub_u32 s43, s43, s55\n\t" "s_sub_u32 s43, s43, s70\n\t" "s_sub_u32 s46, s46, 64\n"
        ".Lnu%=:\n\t"
        "s_mov_b32 s51, 1\n\t" "s_nop 7\n\t" WVM(0) "s_sub_u32 s98, s98, 1\n\t" "s_cbranch_scc1 .Lcap%=\n"
        ".Lent%=:\n\t"
        "s_cmp_eq_u32 s47, 1\n\t" "s_cselect_b32 s99, s66, s67\n\t" "s_cbranch_scc1 .Lnz%=\n\t" ZALL ".Lnz%=:\n\t"
        "s_cmp_eq_u32 s47, 0\n\t" "s_cselect_b32 s99, 0, s99\n\t" "s_cselect_b32 s50, s52, s53\n\t" "s_add_u32 s99, s99, s43\n\t" "s_add_u32 s50, s50, s36\n\t"
        SHRD "s_memrealtime s[72:73]\n\t"
        "s_cmp_eq_u32 %[wv], 3\n\t" "s_cselect_b32 s49, 0, 3\n\t" "s_add_u32 s49, s49, s47\n\t" "s_cmp_lg_u32 s51, 0\n\t" "s_cselect_b32 s49, s49, 7\n\t" "s_cselect_b64 vcc, -1, 0\n\t"
        "s_cselect_b32 s51, 0, 14\n\t" "s_mov_b32 s48, .Lpc%=-.Lnorm%=\n\t"
        RETA(0, ".Lua%=") RETA(1, ".Ltb%=y") RETA(2, ".Luc%=") RETA(3, ".Lna%=") RETA(5, ".Lnc%=")
        "s_getpc_b64 s[96:97]\n" ".Lpc%=:\n\t" "s_sub_u32 s96, s96, s48\n\t" "s_subb_u32 s97, s97, 0\n\t"
        SHUP "s_sub_u32 s49, s99, s72\n\t" "s_add_u32 s49, s49, s44\n"
        ".Lbump%=:\n\t"
        "s_cmp_ge_i32 s49, " STR(PREP) "\n\t" "s_cbranch_scc1 .Lok%=\n\t" "s_add_u32 s44, s44, %[tr]\n\t" "s_add_u32 s49, s49, %[tr]\n\t" "s_branch .Lbump%=\n"
        ".Lok%=:\n\t"
        "s_mov_b64 exec, 1\n\t" "v_mov_b32 v62, s44\n\t" "ds_max_u32 v38, v62\n\t" KTRC "s_mov_b64 exec, -1\n\t"
        "s_add_u32 s99, s99, s44\n\t" "s_sub_u32 s99, s99, s51\n\t"
        WLG(0) "s_memrealtime s[72:73]\n\t" "s_nop 3\n\t" "s_memrealtime s[74:75]\n\t" "s_nop 3\n"
        CLKL(".Lth%=", "s99")
        "s_setpc_b64 s[96:97]\n"
        ".Lcap%=:\n\t" "s_add_u32 s47, s47, 8\n"
        ".Lend%=:\n\t"
        "s_waitcnt vmcnt(0) lgkmcnt(0)\n\t"
        "s_mov_b32 s50, s64\n\t" "s_mov_b32 s49, s98\n\t" "s_mov_b32 s51, s68\n\t" "s_mov_b32 m0, s60"
        : "+{s[36:51]}"(sa), "+{s[52:59]}"(sc), "+{v[60:91]}"(keep), "=&{v[116:117]}"(ov), "+{a[192:207]}"(fv), "+{a[208:215]}"(fk), "+{v[8:23]}"(kd) FINEO TUNEO
        : "{v[32:47]}"(kc), [rs] "s"(rsx), [rf] "s"(rsf), [dm] "s"(__builtin_amdgcn_readfirstlane(PGO_Y + b * 128)), [tg] "s"(__builtin_amdgcn_readfirstlane(tag)), [r2] "s"(__builtin_amdgcn_readfirstlane(XR2B + flip)), [mb] "s"(__builtin_amdgcn_readfirstlane(ldsb + w * 4096)), [wv] "s"(__builtin_amdgcn_readfirstlane(w)), [tr] "s"(TR) TRCI,
          "{a[0:31]}"(wa[0]), "{a[32:63]}"(wa[1]), "{a[64:95]}"(wa[2]), "{a[96:127]}"(wa[3]), "{a[128:159]}"(wa[4]), "{a[160:191]}"(wa[5]),
          "{v[128:159]}"(wo[0]), "{v[160:191]}"(wo[1]), "{v[192:223]}"(wo[2]), "{v[224:255]}"(wo[3])
        : CL8(24, 25, 26, 27, 28, 29, 30, 31), "v48", "v49", "v50", "v51", CL8(52, 53, 54, 55, 56, 57, 58, 59), "v92", "v93", "v94", "v95", CL96, "v112", "v113", "v114", "v115", "v118", "v119", CL120,
          "s60", "s61", "s62", "s63", "s64", "s65", "s66", "s67", "s68", "s69", "s70", "s71", "s72", "s73", "s74", "s75", "s76", "s77", "s78", "s79", "s96", "s97", "s98", "s99", "vcc", "scc", "memory");
    const int code = __builtin_amdgcn_readfirstlane(sa[11]);
    xp = __builtin_amdgcn_readfirstlane(sa[14]);
    tcnt += sa[15];
    sa[15] = 0;
    if (code == 3) break;
    const int seg = __builtin_amdgcn_readfirstlane(sa[9]);
    if (code == 5) { XWAIT(seg + 2) xp = max(xp, seg + 2); sa[11] = 2; }
    else if (code == 1) { __builtin_amdgcn_raw_buffer_store_b64((u32x2){nosent(ov[0]), nosent(ov[1])}, rsx, sto, sa[0], 16); sa[11] = 2; }
    else { sc[2] += 1; sa[13] = 0; sa[11] = code - 8; sa[15] = 1; }
  }
  LBAR();
  if (tid == 0) __builtin_amdgcn_raw_buffer_store_b32(1u, rsx, 0, PGO_D + vcu * 128, 16);
  if (b == 0 && w == 1) {
    for (int it = 0; it < SPIN_CAP; ++it) {
      asm volatile("" ::: "memory");
      if (__ballot(__builtin_amdgcn_raw_buffer_load_b32(rsx, lane * 128, PGO_S, 16) != tag || __builtin_amdgcn_raw_buffer_load_b32(rsx, lane * 128 + 8192, PGO_S, 16) != tag) == 0) break;
    }
    if (lane == 0) __builtin_amdgcn_raw_buffer_store_b32(tag, rsx, 0, XEPO, 16);
  }
}

#define XSTAT 3
#define XVM 8
#define XP0 -1
#define XSC " sc1"
#define XPITCH 1056
#define XSLOT (32 * XPITCH)
#define XW(HB, t) "a[" STR(HB) "+8*xk+4*" #t ":" STR(HB) "+8*xk+4*" #t "+3]"
#define XF(rt) "v[96+8*(xk&3)+4*" #rt ":96+8*(xk&3)+4*" #rt "+3]"
#define XFN(rt) "v[96+8*((xk+3)&3)+4*" #rt ":96+8*((xk+3)&3)+4*" #rt "+3]"
#define XA(AC, o) "v[" STR(AC) "+" #o ":" STR(AC) "+" #o "+3]"
#define XM1(d, a, b, FIRST) ".if (xk == 0) && " STR(FIRST) "\n\tv_mfma_f32_16x16x32_bf16 " d ", " a ", " b ", 0\n\t.else\n\tv_mfma_f32_16x16x32_bf16 " d ", " a ", " b ", " d "\n\t.endif\n\t"
#define XMF_QK(i, HB, AC, F) XMF_QK##i(HB, AC, F)
#define XMF_QK0(HB, AC, F) XM1(XA(AC, 0), XW(HB, 0), XF(0), F)
#define XMF_QK1(HB, AC, F) XM1(XA(AC, 4), XW(HB, 1), XF(0), F)
#define XMF_QK2(HB, AC, F) XM1(XA(AC, 8), XW(HB, 0), XF(1), F)
#define XMF_QK3(HB, AC, F) XM1(XA(AC, 12), XW(HB, 1), XF(1), F)
#define XMF_V(i, HB, AC, F) XMF_V##i(HB, AC, F)
#define XMF_V0(HB, AC, F) XM1(XA(AC, 0), XF(0), XW(HB, 0), F)
#define XMF_V1(HB, AC, F) XM1(XA(AC, 8), XF(0), XW(HB, 1), F)
#define XMF_V2(HB, AC, F) XM1(XA(AC, 4), XF(1), XW(HB, 0), F)
#define XMF_V3(HB, AC, F) XM1(XA(AC, 12), XF(1), XW(HB, 1), F)
#define XRD(rt, CUR, NXT) ".if xk < 13\n\tds_read_b128 " XFN(rt) ", " CUR " offset:64*(xk+3)+" STR(rt * 16 * XPITCH) "\n\t.else\n\t" \
                          "ds_read_b128 " XFN(rt) ", " NXT " offset:64*(xk-13)+" STR(rt * 16 * XPITCH) "\n\t.endif\n\t"
#define XCV(EPI, PV, i) ".if " STR(EPI) " && (xk >= 1) && (xk < 3)\n\tv_cvt_pk_bf16_f32 v[128+4*(xk-1)+" #i "], v[" STR(PV) "+8*(xk-1)+2*" #i "], v[" STR(PV) "+8*(xk-1)+2*" #i "+1]\n\t.endif\n\t"
#define XHALF(MF, HB, AC, PV, EPI, CUR, NXT, DSLOT, GOFF)                                                   \
  ".set xk, 0\n\t"                                                                                          \
  ".rept 16\n\t"                                                                                            \
  "s_waitcnt lgkmcnt(4)\n\t"                                                                                \
  MF(0, HB, AC, EPI) XRD(0, CUR, NXT) XCV(EPI, PV, 0)                                                       \
  ".if (xk >= 5) && (xk < 13)\n\t"                                                                          \
  "s_add_u32 m0, m0, " STR(XPITCH) "\n\t"                                                                   \
  ".endif\n\t"                                                                                              \
  MF(1, HB, AC, EPI) XRD(1, CUR, NXT) XCV(EPI, PV, 1)                                                       \
  ".if (xk >= 5) && (xk < 13)\n\t"                                                                          \
  "s_add_u32 %[dg], %[dg], 2048\n\t"                                                                        \
  ".endif\n\t"                                                                                              \
  MF(2, HB, AC, EPI) XCV(EPI, PV, 2)                                                                        \
  ".if (xk >= 4) && (xk < 12)\n\t"                                                                          \
  "buffer_load_dwordx4 %[l16], %[rx], %[dg] offen lds\n\t"                                                  \
  ".endif\n\t"                                                                                              \
  ".if xk == 0\n\t"                                                                                         \
  "s_add_u32 m0, %[dl], " STR((DSLOT) * XSLOT) "\n\t" "s_add_u32 %[dg], %[gb], " STR(GOFF) "\n\t"           \
  ".endif\n\t"                                                                                              \
  ".if " STR(EPI) " && (xk == " STR(XSTAT) ")\n\t"                                                                       \
  "buffer_store_dwordx4 v[128:131], v137, %[rf], %[sg] offen" XSC "\n\t"                                       \
  ".endif\n\t"                                                                                              \
  ".if " STR(EPI) " && (xk == 13)\n\t"                                                                      \
  "v_mov_b32 v137, %[l16]\n\t" "v_mov_b32 v138, %[lp2]\n\t"                                                 \
  ".endif\n\t"                                                                                              \
  ".if " STR(EPI) " && (xk == 14)\n\t"                                                                      \
  "s_add_u32 %[sg], %[sg], " STR(NH * 8 * 1024) "\n\t"                                                      \
  "v_add_u32 v136, 1, v136\n\t"                                                                             \
  ".endif\n\t"                                                                                              \
  ".if !" STR(EPI) " && (xk == 1)\n\t"                                                                      \
  "buffer_store_dword v136, %[fo], %[rp], 0 offen sc1\n\t"                                                  \
  ".endif\n\t"                                                                                              \
  MF(3, HB, AC, EPI) XCV(EPI, PV, 3)                                                                        \
  ".if xk == 0\n\t"                                                                                         \
  "s_waitcnt vmcnt(" STR(XVM) ")\n\t" "s_barrier\n\t"                                                               \
  ".endif\n\t"                                                                                              \
  ".if xk == 2\n\t"                                                                                         \
  "s_min_u32 %[dg], %[dg], " STR(S_LEN * DM * 2 - 16384 + 1024) "\n\t"                                      \
  ".endif\n\t"                                                                                              \
  ".if " STR(EPI) " && (xk == " STR(XSTAT) ")\n\t"                                                                       \
  "buffer_store_dwordx4 v[132:135], v138, %[rf], %[sg] offen" XSC "\n\t"                                       \
  ".endif\n\t"                                                                                              \
  ".set xk, xk+1\n\t"                                                                                       \
  ".endr\n\t"
#define XLOOP(MF)                                                                                           \
  "s_mov_b32 %[kp], m0\n\t"                                                                                 \
  "v_mov_b32 v136, %[p0]\n\t" "v_mov_b32 v137, " STR(OOR) "\n\t" "v_mov_b32 v138, " STR(OOR) "\n\t"                                                                                \
  "ds_read_b128 v[96:99], %[r0]\n\tds_read_b128 v[100:103], %[r0] offset:" STR(16 * XPITCH) "\n\t"          \
  "ds_read_b128 v[104:107], %[r0] offset:64\n\tds_read_b128 v[108:111], %[r0] offset:64+" STR(16 * XPITCH) "\n\t"   \
  "ds_read_b128 v[112:115], %[r0] offset:128\n\tds_read_b128 v[116:119], %[r0] offset:128+" STR(16 * XPITCH) "\n"   \
  ".Lx%=:\n\t"                                                                                              \
  XHALF(MF, 0, 64, 80, 1, "%[r0]", "%[r1]", 3, 66560) XHALF(MF, 128, 64, 80, 0, "%[r1]", "%[r2]", 0, 131072) \
  XHALF(MF, 0, 80, 64, 1, "%[r2]", "%[r3]", 1, 132096) XHALF(MF, 128, 80, 64, 0, "%[r3]", "%[r0]", 2, 196608) \
  "s_add_u32 %[gb], %[gb], 131072\n\t"                                                                      \
  "s_sub_u32 %[cnt], %[cnt], 1\n\t"                                                                         \
  "s_cmp_lg_u32 %[cnt], 0\n\t"                                                                              \
  "s_cbranch_scc1 .Lx%=\n\t"                                                                                \
  "s_mov_b32 m0, %[kp]\n\t"                                                                                 \
  "s_nop 7\n\t" "s_waitcnt lgkmcnt(0)"

template <bool ISV>
__device__ __forceinline__ void x_role(unsigned char* lds, const u16* __restrict__ x, const u16* __restrict__ Wm, int fo0, int head,
                                       __amdgpu_buffer_rsrc_t rsf, u32* prog, u32 tag, int lane, int w) {
  const int ldsb = (int)(size_t)(LAS unsigned char*)lds;
  const __amdgpu_buffer_rsrc_t rsxin = __builtin_amdgcn_make_buffer_rsrc((void*)x, 0, S_LEN * DM * 2, 0x00020000);
  const __amdgpu_buffer_rsrc_t rsp = __builtin_amdgcn_make_buffer_rsrc((void*)prog, 0, 128, 0x00020000);
  const int lr = lane & 15, lg = lane >> 4;
  const int xrd = ldsb + lr * XPITCH + lg * 16;
  const int fob = fo0 + (ISV ? (head * 8 + 2 * w) : (head * 8 + w)) * 1024;
  const int p2 = ISV ? 1024 : 4096;
  u32x32 xw[8];
  {
    const __amdgpu_buffer_rsrc_t rw = __builtin_amdgcn_make_buffer_rsrc((void*)Wm, 0, DM * HD * 2, 0x00020000);
    const int vq = (lane >> 4) * (HD * 2) + head * 256 + (lane & 15) * 16;
#define XWISSUE(c) _Pragma("unroll") for (int j = 0; j < 8; ++j)                                                       \
      asm volatile("s_mov_b32 m0, %0\n\ts_nop 3\n\tbuffer_load_dwordx4 %1, %2, %3 offen lds"                           \
                   :: "s"(ldsb + ((c) & 3) * XSLOT + (8 * w + j) * XPITCH), "v"(vq), "s"(rw), "s"((128 * (c) + 32 * w + 4 * j) * (HD * 2)) : "memory");
#define XXISSUE(h) _Pragma("unroll") for (int j = 0; j < 8; ++j)                                                       \
      asm volatile("s_mov_b32 m0, %0\n\ts_nop 3\n\tbuffer_load_dwordx4 %1, %2, %3 offen lds"                           \
                   :: "s"(ldsb + (h) * XSLOT + (8 * w + j) * XPITCH), "v"(lane * 16), "s"(rsxin), "s"((((h) >> 1) * 32 + 8 * w + j) * 2048 + ((h) & 1) * 1024) : "memory");
    XWISSUE(0) XWISSUE(1) XWISSUE(2) XWISSUE(3)
    const int q = lr >> 2, p = lr & 3;
#pragma unroll
    for (int c = 0; c < 8; ++c) {
      asm volatile("s_waitcnt vmcnt(24)\n\ts_barrier" ::: "memory");
#pragma unroll
      for (int s = 0; s < 4; ++s)
#pragma unroll
        for (int t = 0; t < 2; ++t) {
          const unsigned char* base = lds + (c & 3) * XSLOT + (8 * s + 2 * lg) * XPITCH + q * 256 + w * 64 + p * 16 + t * 8;
          const u32x2 t0 = __builtin_bit_cast(u32x2, __builtin_amdgcn_ds_read_tr16_b64_v4i16((LAS v4i16_t*)(base)));
          const u32x2 t1 = __builtin_bit_cast(u32x2, __builtin_amdgcn_ds_read_tr16_b64_v4i16((LAS v4i16_t*)(base + XPITCH)));
          const int o = ((4 * c + s) * 2 + t) * 4;
          xw[o >> 5][(o & 31) + 0] = t0[0]; xw[o >> 5][(o & 31) + 1] = t0[1]; xw[o >> 5][(o & 31) + 2] = t1[0]; xw[o >> 5][(o & 31) + 3] = t1[1];
        }
      LBAR();
      if (c < 4) { XWISSUE(c + 4) } else if (c < 7) { XXISSUE(c - 4) }
    }
  }
  asm volatile("s_waitcnt vmcnt(8)\n\ts_barrier" ::: "memory");
  u32x16 xacc;
  {
    u32 kp, dg; int gb = 8 * w * 2048, cnt = NSEG / 2, sg = fob - NH * 8 * 1024;
#define XASM(MF)                                                                                            \
    asm volatile(XLOOP(MF)                                                                                  \
                 : "=&{v[80:95]}"(xacc), [kp] "=&s"(kp), [dg] "=&s"(dg), [gb] "+s"(gb), [cnt] "+s"(cnt), [sg] "+s"(sg) \
                 : [r0] "v"(xrd), [r1] "v"(xrd + XSLOT), [r2] "v"(xrd + 2 * XSLOT), [r3] "v"(xrd + 3 * XSLOT), [l16] "v"(lane * 16), [lp2] "v"(lane * 16 + p2), \
                   [rx] "s"(rsxin), [rf] "s"(rsf), [rp] "s"(rsp), [fo] "v"(lane == 0 && w == 0 ? 0 : OOR), [dl] "s"(ldsb + 8 * w * XPITCH), [p0] "s"(tag + XP0), \
                   "{a[0:31]}"(xw[0]), "{a[32:63]}"(xw[1]), "{a[64:95]}"(xw[2]), "{a[96:127]}"(xw[3]),      \
                   "{a[128:159]}"(xw[4]), "{a[160:191]}"(xw[5]), "{a[192:223]}"(xw[6]), "{a[224:255]}"(xw[7]) \
                 : CL64, CL72, CL96, CL112, CL120, CL8(128, 129, 130, 131, 132, 133, 134, 135), "v136", "v137", "v138", "scc", "memory");
    if (ISV) { XASM(XMF_V) } else { XASM(XMF_QK) }
  }
#pragma unroll
  for (int jj = 0; jj < 2; ++jj) {
    u32x4 pk;
#pragma unroll
    for (int e = 0; e < 4; ++e) pk[e] = pack2(__uint_as_float(xacc[8 * jj + 2 * e]), __uint_as_float(xacc[8 * jj + 2 * e + 1]));
    __builtin_amdgcn_raw_buffer_store_b128(pk, rsf, lane * 16 + p2 * jj, fob + (NSEG - 1) * (NH * 8 * 1024), 16);
  }
  asm volatile("s_waitcnt vmcnt(0)\n\ts_barrier" ::: "memory");
  if (lane == 0 && w == 0) __hip_atomic_store(prog, tag + NSEG + 8, RLX_AGENT);
}

#define SSM_LO 1
#define SU_CP 4416
#define SU_U 0
#define SU_K (4 * SU_CP)
#define SU_P (SU_K + 10400)
#define SU_D (SU_P + 2048)
#define SU_RAW (SU_D + 2592)
#define SU_CH (SU_RAW + 4128 + 32)
#define SST(i)
__device__ __forceinline__ void ssm_unit(unsigned char* sm, int d0, __amdgpu_buffer_rsrc_t rst, __amdgpu_buffer_rsrc_t rsx, unsigned char* xb, int fb, bool& uok,
                                         const u16* __restrict__ Dskip, int tid, int lane, int w) {
  unsigned char* cb = sm + w * SU_CH;
  const int dd = d0 + w;
  SST(0)
  if (!uok) {
    for (int it = 0; it < SPIN_CAP; ++it) {
      asm volatile("" ::: "memory");
      if ((int)__builtin_amdgcn_readfirstlane(__builtin_amdgcn_raw_buffer_load_b32(rsx, 0, XUDN, 16)) >= 15) break;
      asm volatile("s_sleep 8" ::: "memory");
    }
    asm volatile("" ::: "memory");
    uok = true;
  }
  f32x4 acc[3];
#pragma unroll
  for (int t = 0; t < 3; ++t) acc[t] = __builtin_bit_cast(f32x4, __builtin_amdgcn_raw_buffer_load_b128(rst, lane * 16, 2 * SSM_L * SSM_D * 2 + dd * 3072 + t * 1024, 16));
  const float dsk = bf2f(Dskip[dd]);
  u32x4 uv[4];
#pragma unroll
  for (int i = 0; i < 4; ++i) uv[i] = __builtin_amdgcn_raw_buffer_load_b128(rst, (tid + 256 * i) * 16, d0 * 4096, 16);
  const int r = lane & 31, h = lane >> 5;
  {
    const int lr = lane & 15, lg = lane >> 4;
    SST(2)
    u32* Kd = (u32*)(cb + SU_D);
#pragma unroll
    for (int e = 0; e < 4; ++e) {
      Kd[128 * (lg >> 1) + 8 * lr + 4 * (lg & 1) + e] = split_hl(acc[0][e]);
      Kd[lg < 2 ? 256 + 64 * lg + 4 * lr + e : 384 + 32 * (2 * (lg - 2) + (e >> 1)) + 2 * lr + (e & 1)] = split_hl(acc[1][e]);
      if (lg < 2 || (lg == 2 && e == 0 && lr == 0)) Kd[512 + 16 * (4 * lg + e) + lr] = split_hl(acc[2][e]);
    }
    asm volatile("s_waitcnt lgkmcnt(0)" ::: "memory");
    unsigned char* kr = cb + SU_K + (lane + 1) * 80;
#pragma unroll
    for (int c8 = 0; c8 < 4; ++c8) {
      u32 kv[8];
#pragma unroll
      for (int e = 0; e < 8; ++e) kv[e] = Kd[kidx(32 * lane + 31 - 8 * c8 - e)];
      u32x4 hi, lo;
#pragma unroll
      for (int e = 0; e < 4; ++e) {
        hi[e] = __builtin_amdgcn_perm(kv[2 * e + 1], kv[2 * e], 0x05040100u);
        lo[e] = __builtin_amdgcn_perm(kv[2 * e + 1], kv[2 * e], 0x07060302u);
      }
      *(u32x4*)(kr + 16 * c8) = hi;
      if (SSM_LO) *(u32x4*)(kr + 5200 + 16 * c8) = lo;
    }
    if (lane < 8) *(u32x4*)(cb + SU_K + (lane >> 2) * 5200 + (lane & 3) * 16) = (u32x4){0u, 0u, 0u, 0u};
  }
  SST(3)
#pragma unroll
  for (int i = 0; i < 4; ++i) {
    const int l = 2 * (tid + 256 * i) + 8;
    *(u32*)(sm + 0 * SU_CH + SU_RAW + l * 2) = __builtin_amdgcn_perm(uv[i][2], uv[i][0], 0x05040100u);
    *(u32*)(sm + 1 * SU_CH + SU_RAW + l * 2) = __builtin_amdgcn_perm(uv[i][2], uv[i][0], 0x07060302u);
    *(u32*)(sm + 2 * SU_CH + SU_RAW + l * 2) = __builtin_amdgcn_perm(uv[i][3], uv[i][1], 0x05040100u);
    *(u32*)(sm + 3 * SU_CH + SU_RAW + l * 2) = __builtin_amdgcn_perm(uv[i][3], uv[i][1], 0x07060302u);
  }
  if (lane < 2) *(u32x4*)(cb + SU_RAW + lane * (4096 + 16)) = (u32x4){0u, 0u, 0u, 0u};
  LBAR();
  {
    if (lane < 20) *(u32x4*)(cb + SU_U + (lane / 5) * SU_CP + (lane % 5) * 16) = (u32x4){0u, 0u, 0u, 0u};
#pragma unroll
    for (int i = 0; i < 5; ++i) {
      const int g = lane + 64 * i;
      if (g < 257) {
        const u32x4 a = *(const u32x4*)(cb + SU_RAW + g * 16);
        const u32x2 c = *(const u32x2*)(cb + SU_RAW + g * 16 + 16);
        unsigned char* dst = cb + SU_U + 80 + g * 16;
        *(u32x4*)(dst) = a;
        *(u32x4*)(dst + SU_CP) = (u32x4){__builtin_amdgcn_alignbyte(a[1], a[0], 2), __builtin_amdgcn_alignbyte(a[2], a[1], 2), __builtin_amdgcn_alignbyte(a[3], a[2], 2), __builtin_amdgcn_alignbyte(c[0], a[3], 2)};
        *(u32x4*)(dst + 2 * SU_CP) = (u32x4){a[1], a[2], a[3], c[0]};
        *(u32x4*)(dst + 3 * SU_CP) = (u32x4){__builtin_amdgcn_alignbyte(a[2], a[1], 2), __builtin_amdgcn_alignbyte(a[3], a[2], 2), __builtin_amdgcn_alignbyte(c[0], a[3], 2), __builtin_amdgcn_alignbyte(c[1], c[0], 2)};
      }
    }
  }
  asm volatile("s_waitcnt lgkmcnt(0)" ::: "memory");
  SST(4)
  f32x16 cv0, cv1;
  {
    const int m0 = 48 - 15 + 8 * h + r;
    const int cbo = (int)(size_t)(LAS unsigned char*)cb;
    int ua = cbo + SU_U + (m0 & 3) * SU_CP + (m0 & ~3) * 2 - 32;
    const int kb = cbo + SU_K + 16 * h;
    int a1 = kb + (33 + r) * 80, a0 = kb + (1 + r) * 80, cnt = 32;
#define CRA(S, O) "ds_read_b64 " RNG("v", S, 2) ", %[ua] offset:" STR(O) "\n\t" "ds_read_b64 " RNG("v", (S) + 2, 2) ", %[ua] offset:" STR((O) + 8) "\n\t"
#define CRB(S, ad, O) "ds_read_b128 " RNG("v", S, 4) ", " ad " offset:" STR(O) "\n\t"
#define CMF(C, A, B) "v_mfma_f32_32x32x16_bf16 " RNG("v", C, 16) ", " RNG("v", A, 4) ", " RNG("v", B, 4) ", " RNG("v", C, 16) "\n\t"
#define CUPD "v_add_u32 %[ua], 64, %[ua]\n\t" "v_subrev_u32 %[a1], 80, %[a1]\n\t" "v_subrev_u32 %[a0], 80, %[a0]\n\t" "v_max_i32 %[a0], %[a0], %[kb]\n\t"
#define CHALF(S, N, O, UPD)                                                                       \
    WLG(5) CMF(64, S, (S) + 8) UPD CRA(N, O)                                                      \
    WLG(6) CMF(80, S, (S) + 16) CRB((N) + 8, "%[a0]", O) CRB((N) + 16, "%[a1]", O)                \
    WLG(6) CMF(80, (S) + 4, (S) + 8) CRA((N) + 4, (O) + 2048)                                     \
    WLG(7) CMF(64, S, (S) + 12) CRB((N) + 12, "%[a0]", (O) + 5200)                                \
    WLG(7) CMF(80, S, (S) + 20) CRB((N) + 20, "%[a1]", (O) + 5200)                                \
    CMF(80, (S) + 4, (S) + 12)
    asm volatile(
        CRA(96, 32) CRB(104, "%[a0]", 32) CRB(112, "%[a1]", 32) CRA(100, 32 + 2048) CRB(108, "%[a0]", 32 + 5200) CRB(116, "%[a1]", 32 + 5200)
        ZER(64) ZER(68) ZER(72) ZER(76) ZER(80) ZER(84) ZER(88) ZER(92)
        ".Lc%=:\n\t"
        CHALF(96, 120, 0, "")
        CHALF(120, 96, 32, CUPD)
        "s_sub_u32 %[cnt], %[cnt], 1\n\t"
        "s_cmp_lg_u32 %[cnt], 0\n\t"
        "s_cbranch_scc1 .Lc%=\n\t"
        "s_nop 7\n\t" "s_nop 3\n\t" WLG(0)
        : "=&{v[64:79]}"(cv0), "=&{v[80:95]}"(cv1), [ua] "+v"(ua), [a0] "+v"(a0), [a1] "+v"(a1), [cnt] "+s"(cnt)
        : [kb] "v"(kb)
        : CL96, CL112, CL120, CL8(128, 129, 130, 131, 132, 133, 134, 135), CL8(136, 137, 138, 139, 140, 141, 142, 143),
          CL8(144, 145, 146, 147, 148, 149, 150, 151), CL8(152, 153, 154, 155, 156, 157, 158, 159), "scc", "memory");
  }
  SST(5)
  {
    u16* yo = (u16*)(cb + SU_K);
#pragma unroll
    for (int g = 0; g < 2; ++g)
#pragma unroll
      for (int i = 0; i < 16; i += 4) {
        const int rr = 2 * i + 4 * h, a = 32 * g + r;
        const u32x2 uu = *(const u32x2*)(cb + SU_U + (48 + 32 * a + rr) * 2);
        float y[4];
#pragma unroll
        for (int e = 0; e < 4; ++e) {
          const float uf = __uint_as_float((e & 1) ? (uu[e >> 1] & 0xffff0000u) : (uu[e >> 1] << 16));
          y[e] = rbf_hw(g ? cv1[i + e] : cv0[i + e]) + rbf_hw(uf * dsk);
        }
        *(u32x2*)(yo + a * 40 + rr) = (u32x2){pack2(y[0], y[1]), pack2(y[2], y[3])};
      }
  }
  LBAR();
  SST(6)
#pragma unroll
  for (int i = 0; i < 4; ++i) {
    const int l = 2 * (tid + 256 * i);
    u32 c[4];
#pragma unroll
    for (int j = 0; j < 4; ++j) c[j] = *(const u32*)(sm + j * SU_CH + SU_K + ((l >> 5) * 40 + (l & 31)) * 2);
    __builtin_amdgcn_raw_buffer_store_b128((u32x4){__builtin_amdgcn_perm(c[1], c[0], 0x05040100u), __builtin_amdgcn_perm(c[3], c[2], 0x05040100u),
                                                   __builtin_amdgcn_perm(c[1], c[0], 0x07060302u), __builtin_amdgcn_perm(c[3], c[2], 0x07060302u)},
                                           rst, l * 8, SSM_L * SSM_D * 2 + d0 * 4096, 16);
  }
  asm volatile("s_waitcnt vmcnt(0)" ::: "memory");
  LBAR();
  if (tid == 0) __hip_atomic_fetch_add((u32*)(xb + XYDN), 1u, RLX_AGENT);
  SST(7)
}

#define L_TOTAL 163840
__device__ __forceinline__ int ldw(__amdgpu_buffer_rsrc_t rs, int off) {
  asm volatile("" ::: "memory");
  return __builtin_amdgcn_readfirstlane(__builtin_amdgcn_raw_buffer_load_b32(rs, 0, off, 16));
}
#define GF(rt) "v[64+8*(xk&7)+4*" #rt ":64+8*(xk&7)+4*" #rt "+3]"
#define GW2 "v[128+4*xk:128+4*xk+3]"
#define GW3 "v[24+4*(xk&1):24+4*(xk&1)+3]"
#define GLOAD(S, K) "s_xor_b32 %[tq], " S ", 1024*(" K ")\n\t"
#define GTR(K) "ds_read_b128 v[24+4*((" K ")&1):24+4*((" K ")&1)+3], %[wl] offset:1024*((" K ")&31)\n\t"
#define GPACE 0
#define YAUX 16
#define UDELAY 100
#define GNOSSM 1
#define GNG 3
#define GS0 22
__device__ __forceinline__ void g_role(unsigned char* lds, const u16* __restrict__ Wo, u16* __restrict__ out, __amdgpu_buffer_rsrc_t rsx,
                                       __amdgpu_buffer_rsrc_t rsf, int fb, u32 tag, int grp, int sl, int w) {
  const int lane = fresh_lane();
  const int lr = lane & 15, lg = lane >> 4;
  const int rot = (2 * sl + 8 * w) & 31;
  const int ldsb = (int)(size_t)(LAS unsigned char*)lds;
  u32x32 xw[8], w2[4];
  LBAR();
  {
    const __amdgpu_buffer_rsrc_t rw = __builtin_amdgcn_make_buffer_rsrc((void*)Wo, 0, DM * HD * 2, 0x00020000);
    const int vq = (lane >> 3) * (DM * 2) + sl * 128 + (lane & 7) * 16;
    const int sq = (1024 * w + rot * 32) * (DM * 2);
#define GSLOT(c) ((c) < 8 ? (((c) & 3) == 3 ? 131072 + w * 8192 : w * 32768 + 8192 * (((c) & 3) + 1)) : (((c) - 8) % 3 == 2 ? 131072 + w * 8192 : w * 32768 + 8192 * (((c) - 8) % 3 + 2)))
#define GISSUE(c) _Pragma("unroll") for (int j = 0; j < 8; ++j)                                                        \
      asm volatile("s_mov_b32 m0, %0\n\ts_nop 3\n\tbuffer_load_dwordx4 %1, %2, %3 offen lds"                           \
                   :: "s"(ldsb + GSLOT(c) + j * 1024), "v"(vq), "s"(rw), "s"(sq ^ ((64 * (c) + 8 * j) * DM * 2)) : "memory");
    GISSUE(0) GISSUE(1) GISSUE(2) GISSUE(3)
    const int q = lr >> 2, p = lr & 3;
    u32x4 w3[16];
#pragma unroll
    for (int c = 0; c < 16; ++c) {
      if (c < 5) waitvm<24>(); else if (c < 14) waitvm<16>(); else if (c == 14) waitvm<8>(); else waitvm<0>();
      u32x4 f3[2];
#pragma unroll
      for (int s = 0; s < 2; ++s)
#pragma unroll
        for (int t = 0; t < 4; ++t) {
          const unsigned char* base = lds + GSLOT(c) + (32 * s + 8 * lg + q) * 128 + (t < 2 ? p * 16 + t * 8 : t * 32 + p * 8);
          const u32x2 t0 = __builtin_bit_cast(u32x2, __builtin_amdgcn_ds_read_tr16_b64_v4i16((LAS v4i16_t*)(base)));
          const u32x2 t1 = __builtin_bit_cast(u32x2, __builtin_amdgcn_ds_read_tr16_b64_v4i16((LAS v4i16_t*)(base + 512)));
          const int o = t < 2 ? ((2 * c + s) * 2 + t) * 4 : (2 * c + s) * 4;
          if (t < 2) { xw[o >> 5][(o & 31) + 0] = t0[0]; xw[o >> 5][(o & 31) + 1] = t0[1]; xw[o >> 5][(o & 31) + 2] = t1[0]; xw[o >> 5][(o & 31) + 3] = t1[1]; }
          else if (t == 2) { w2[o >> 5][(o & 31) + 0] = t0[0]; w2[o >> 5][(o & 31) + 1] = t0[1]; w2[o >> 5][(o & 31) + 2] = t1[0]; w2[o >> 5][(o & 31) + 3] = t1[1]; }
          else f3[s] = (u32x4){t0[0], t0[1], t1[0], t1[1]};
        }
      asm volatile("s_waitcnt lgkmcnt(0)" ::: "memory");
      if (c < 8) { *(u32x4*)(lds + w * 32768 + (2 * c) * 1024 + lane * 16) = f3[0]; *(u32x4*)(lds + w * 32768 + (2 * c + 1) * 1024 + lane * 16) = f3[1]; }
      else { w3[2 * (c - 8)] = f3[0]; w3[2 * (c - 8) + 1] = f3[1]; }
      if (GPACE) __builtin_amdgcn_s_sleep(GPACE);
      if (c < 4) { GISSUE(c + 4) } else if (c >= 5 && c < 13) { GISSUE(c + 3) }
    }
#pragma unroll
    for (int i = 0; i < 16; ++i) *(u32x4*)(lds + w * 32768 + (16 + i) * 1024 + lane * 16) = w3[i];
    LBAR();
  }
  wait_start(rsx, fb, tag, true);
  const int wl = ldsb + w * 32768 + lane * 16;
  const __amdgpu_buffer_rsrc_t rso = __builtin_amdgcn_make_buffer_rsrc((void*)out, 0, S_LEN * OUTLD * 2, 0x00020000);
  const int rw_ = ldsb + 131072 + w * 8192 + lane * 16, rr_ = ldsb + 131072 + w * 2048 + lane * 16;
  const int oa = ((16 * (w & 1) + lr) * OUTLD + 64 * sl + (w < 2 ? 8 * lg : 32 + 4 * lg)) * 2, ob = oa + (w < 2 ? 8 : 32);
  const int hq = (grp + GNG - (62 - GS0) % GNG) % GNG - 1, hf = hq < 2 ? hq : -1;
#define GRW(i) "ds_write_b128 %[rw], " RNG("v", 32 + 4 * (i), 4) " offset:" STR(1024 * (i)) "\n\t"
#define GRR(i) "ds_read_b128 " RNG("v", 32 + 4 * (i), 4) ", %[rr] offset:" STR(8192 * ((i) >> 1) + 1024 * ((i) & 1)) "\n\t"
#define GNXT(D, S) "s_cmp_lt_u32 " S ", " STR(GS0) "\n\t" "s_cselect_b32 " D ", 1, " STR(GNG) "\n\t" "s_add_u32 " D ", " D ", " S "\n\t"
#define GDN(S) "s_min_u32 s41, " S ", 63\n\t" "s_lshl_b32 s41, s41, 5\n\t" "v_add_u32 v23, s41, %[u0]\n\t" "v_lshrrev_b32 v22, 6, v23\n\t" \
               "v_and_b32 v23, 63, v23\n\t" "v_lshlrev_b32 v23, 7, v23\n\t" "s_lshr_b32 s41, s41, 6\n\t" "s_max_u32 s41, s41, 29\n\t" "s_lshl_b32 s41, s41, 13\n\t" "s_add_u32 s41, s41, %[pa]\n\t" \
               "buffer_load_dword v23, v23, %[rs], s41 offen sc1\n\t"
#define GOK(D) "v_cmp_le_i32 vcc, v23, v22\n\t" "s_cmp_eq_u64 vcc, 0\n\t" "s_cselect_b32 " D ", 1, 0\n\t"
static_assert(NAW == 64, "shifts above");
#define GEPI(MID, OA, OB) \
      GRW(0) GRW(1) GRW(2) GRW(3) GRW(4) GRW(5) GRW(6) GRW(7) \
      MID \
      WLG(0) "s_barrier\n\t" \
      GRR(0) GRR(1) GRR(2) GRR(3) GRR(4) GRR(5) GRR(6) GRR(7) \
      WLG(4) PKA(32, 32, 40) PKA(34, 34, 42) PKA(36, 36, 44) PKA(38, 38, 46) \
      WLG(0) PKA(48, 48, 56) PKA(50, 50, 58) PKA(52, 52, 60) PKA(54, 54, 62) \
      PKA(32, 32, 48) PKA(34, 34, 50) PKA(36, 36, 52) PKA(38, 38, 54) \
      "v_cvt_pk_bf16_f32 v20, v32, v33\n\t" "v_cvt_pk_bf16_f32 v21, v34, v35\n\t" "v_cvt_pk_bf16_f32 v18, v36, v37\n\t" "v_cvt_pk_bf16_f32 v19, v38, v39\n\t" \
      "buffer_store_dwordx2 v[20:21], " OA ", %[ro], s42 offen\n\t" \
      "buffer_store_dwordx2 v[18:19], " OB ", %[ro], s42 offen\n\t"
#define GH "v[64+4*(xk&15):64+4*(xk&15)+3]"
  asm volatile(
      "s_nop 4\n\t"
      "v_add_u32 v16, 131072, %[va]\n\t" "v_sub_u32 v15, %[ob], %[oa]\n\t" "v_add_u32 v15, v15, %[oc]\n\t"
      "s_mov_b32 s36, %[s0]\n\t" "s_mov_b32 s43, " STR(SPIN_CAP) "\n"
      ".Lp%=:\n\t"
      GDN("s36") WVM(0) GOK("s40")
      "s_cbranch_scc1 .Lr%=\n\t"
      "s_sleep 2\n\t" "s_sub_u32 s43, s43, 1\n\t" "s_cmp_gt_i32 s43, 0\n\t" "s_cbranch_scc1 .Lp%=\n"
      ".Lr%=:\n\t"
      GNXT("s37", "s36") GDN("s37")
      "s_lshl_b32 s38, s36, 18\n\t" "s_add_u32 s38, s38, %[gb]\n\t"
      ".set xk, 0\n\t.rept 8\n\t" GLOAD("s38", "xk")
      "buffer_load_dwordx4 " GF(0) ", %[va], %[rf], %[tq] offen sc1\n\t" "buffer_load_dwordx4 " GF(1) ", v16, %[rf], %[tq] offen sc1\n\t"
      ".set xk, xk+1\n\t.endr\n\t"
      WVM(16) GOK("s40")
      ".Lb%=:\n\t"
      GNXT("s37", "s36") GNXT("s41", "s37") GDN("s41") GTR("0")
      "s_cmp_lt_u32 s37, 63\n\t" "s_cselect_b32 s40, s40, 0\n\t"
      "s_cmp_lg_u32 s40, 0\n\t" "s_cselect_b32 s39, s37, s36\n\t"
      "s_lshl_b32 s39, s39, 18\n\t" "s_add_u32 s39, s39, %[gb]\n\t" "s_lshl_b32 s38, s36, 18\n\t" "s_add_u32 s38, s38, %[gb]\n\t"
      "s_lshl_b32 s42, s36, 17\n\t"
      "buffer_load_dword v17, %[vt], %[rf], s39 offen sc1\n\t"
      ".set xk, 0\n\t.rept 32\n\t"
      "s_waitcnt vmcnt(14)\n\t"
      XM1(XA(32, 0), XW(0, 0), GF(0), 1) GTR("xk+1") XM1(XA(32, 4), XW(0, 1), GF(0), 1)
      ".if xk < 24\n\t" GLOAD("s38", "xk+8") ".else\n\t" GLOAD("s39", "xk-24") ".endif\n\t"
      XM1(XA(32, 8), XW(0, 0), GF(1), 1) XM1(XA(32, 12), XW(0, 1), GF(1), 1)
      XM1(XA(32, 16), GW2, GF(0), 1) "s_waitcnt lgkmcnt(1)\n\t" XM1(XA(32, 20), GW3, GF(0), 1)
      "buffer_load_dwordx4 " GF(0) ", %[va], %[rf], %[tq] offen sc1\n\t"
      XM1(XA(32, 24), GW2, GF(1), 1) XM1(XA(32, 28), GW3, GF(1), 1)
      "buffer_load_dwordx4 " GF(1) ", v16, %[rf], %[tq] offen sc1\n\t"
      ".if xk == 16\n\t" "s_barrier\n\t" ".endif\n\t"
      ".set xk, xk+1\n\t.endr\n\t"
      GEPI(GOK("s41"), "%[oa]", "%[ob]")
      "s_mov_b32 s36, s37\n\t"
      "s_cmp_lg_u32 s40, 0\n\t" "s_mov_b32 s40, s41\n\t" "s_cbranch_scc1 .Lb%=\n\t"
      "s_cmp_lt_u32 s36, 63\n\t" "s_cbranch_scc1 .Lp%=\n\t"
      "s_cmp_lt_i32 %[hf], 0\n\t" "s_cbranch_scc1 .Le%=\n\t"
      "s_mov_b32 s36, 63\n"
      ".Lh%=:\n\t"
      GDN("s36") WVM(0) GOK("s40")
      "s_cbranch_scc1 .Li%=\n\t"
      "s_sleep 2\n\t" "s_sub_u32 s43, s43, 1\n\t" "s_cmp_gt_i32 s43, 0\n\t" "s_cbranch_scc1 .Lh%=\n"
      ".Li%=:\n\t"
      "s_lshl_b32 s38, %[hf], 17\n\t" "s_add_u32 s38, s38, 63*262144\n\t" "s_add_u32 s38, s38, %[gb]\n\t"
      ".set xk, 0\n\t.rept 16\n\t" GLOAD("s38", "xk") "buffer_load_dwordx4 " GH ", %[va], %[rf], %[tq] offen sc1\n\t" ".set xk, xk+1\n\t.endr\n\t"
      GTR("0")
      "s_lshl_b32 s42, %[hf], 16\n\t" "s_add_u32 s42, s42, 63*131072\n\t"
      ".set xk, 0\n\t.rept 32\n\t"
      ".if xk < 17\n\t" "s_waitcnt vmcnt(15)\n\t" ".else\n\t" "s_waitcnt vmcnt(31-xk)\n\t" ".endif\n\t"
      XM1(XA(32, 0), XW(0, 0), GH, 1) GTR("xk+1") XM1(XA(32, 4), XW(0, 1), GH, 1)
      XM1(XA(32, 16), GW2, GH, 1) "s_waitcnt lgkmcnt(1)\n\t" XM1(XA(32, 20), GW3, GH, 1)
      ".if xk < 16\n\t" GLOAD("s38", "xk+16") "buffer_load_dwordx4 " GH ", %[va], %[rf], %[tq] offen sc1\n\t" ".endif\n\t"
      ".if xk == 16\n\t" "s_barrier\n\t" ".endif\n\t"
      ".set xk, xk+1\n\t.endr\n\t"
      "s_nop 7\n\t"
      GEPI("", "%[oc]", "v15")
      ".Le%=:\n\t"
      WVM(0)
      : [tq] "=&s"(sl)
      : [rs] "s"(rsx), [s0] "s"(__builtin_amdgcn_readfirstlane(grp ? GS0 + grp : 0)), [gb] "s"(FO_O - 32768 + (32 * w + rot) * 1024), [va] "v"(lane * 16 + 32768),
        [rf] "s"(rsf), [wl] "v"(wl), [rw] "v"(rw_), [rr] "v"(rr_), [oa] "v"(oa), [ob] "v"(ob), [ro] "s"(rso), [u0] "v"(8 * w + (lane & 7)), [pa] "s"(PGO_A - 29 * 8192), [hf] "s"(__builtin_amdgcn_readfirstlane(hf)), [oc] "v"((w & 1) ? OOR - 64 : oa), [vt] "v"((sl >> 3) * 131072 + 32768 + (sl & 7) * 4096 + lane * 64 - rot * 1024),
        "{a[0:31]}"(xw[0]), "{a[32:63]}"(xw[1]), "{a[64:95]}"(xw[2]), "{a[96:127]}"(xw[3]),
        "{a[128:159]}"(xw[4]), "{a[160:191]}"(xw[5]), "{a[192:223]}"(xw[6]), "{a[224:255]}"(xw[7]),
        "{v[128:159]}"(w2[0]), "{v[160:191]}"(w2[1]), "{v[192:223]}"(w2[2]), "{v[224:255]}"(w2[3])
      : "v15", "v16", "v17", "v18", "v19", "v20", "v21", "v22", "v23", "vcc", CL8(24, 25, 26, 27, 28, 29, 30, 31), CL8(32, 33, 34, 35, 36, 37, 38, 39), CL8(40, 41, 42, 43, 44, 45, 46, 47),
        CL8(48, 49, 50, 51, 52, 53, 54, 55), CL8(56, 57, 58, 59, 60, 61, 62, 63), CL64, CL72, CL80, CL88, CL96, CL112, CL120,
        "s36", "s37", "s38", "s39", "s40", "s41", "s42", "s43", "scc", "memory");
}
static_assert(4 * SU_CH <= L_TOTAL && 4 * XSLOT <= L_TOTAL, "lds");
__global__ void __launch_bounds__(256, 1)
main_kernel(const u16* __restrict__ x, const u16* __restrict__ mem0, const u16* __restrict__ Wq, const u16* __restrict__ Wk, const u16* __restrict__ Wv, const u16* __restrict__ Wo,
            unsigned char* ws, const u16* __restrict__ u, const u16* __restrict__ A_re, const u16* __restrict__ A_im, const u16* __restrict__ B_re,
            const u16* __restrict__ B_im, const u16* __restrict__ C_re, const u16* __restrict__ C_im,
            const u16* __restrict__ log_dt, const u16* __restrict__ Dskip, u16* __restrict__ out
            ) {
  __shared__ __attribute__((aligned(8192))) unsigned char lds[L_TOTAL];
  const int w = __builtin_amdgcn_readfirstlane(threadIdx.x >> 6);
  u64 pc0;
  int clen;
  {
    asm volatile("s_getpc_b64 %0\n.Lkpc0:\n\ts_mov_b32 %1, .Lkpc1-.Lkpc0" : "=s"(pc0), "=s"(clen));
  }
#define LOCAL_IDS const int lane = fresh_lane(), tid = w * 64 + lane; (void)tid;
  const int si = (blockIdx.x >> 3) * 4 + (blockIdx.x & 3);
  unsigned char* xb = (unsigned char*)(((size_t)ws + XPAGE - 1) & ~(size_t)(XPAGE - 1));
  const int fb = (int)(ws + WS_F - xb);
  u32 tag;
  asm volatile("s_load_dword %0, %1, 0x0\n\ts_waitcnt lgkmcnt(0)\n\ts_add_u32 %0, %0, 256" : "=s"(tag) : "s"(ws + WS_F) : "scc", "memory");
  const __amdgpu_buffer_rsrc_t rsx = __builtin_amdgcn_make_buffer_rsrc(xb, 0, fb + (int)WS_F_SZ, 0x00020000);
  const __amdgpu_buffer_rsrc_t rsf = __builtin_amdgcn_make_buffer_rsrc(ws + WS_QX, 0, (int)(4 * WS_QKV_SZ + 2 * WS_KM_SZ), 0x00020000);
  const __amdgpu_buffer_rsrc_t rst = __builtin_amdgcn_make_buffer_rsrc(ws + WS_UT, 0, 2 * SSM_L * SSM_D * 2 + SSM_D * 3072, 0x00020000);
  if ((blockIdx.x & 7) < 4) {
    { LOCAL_IDS
    r_role(lds, si, mem0, Wq, Wk, Wv, Wo, fb, tag, pc0, clen, rsx, rsf, rst, SsmPtrs{A_re, A_im, B_re, B_im, C_re, C_im, log_dt}, tid, lane, w
           ); }
    LOCAL_IDS
    {
      for (int it = 0; it < SPIN_CAP; ++it) {
        if (ldw(rsx, XYDN) >= 255) break;
        asm volatile("s_sleep 8" ::: "memory");
      }
      asm volatile("" ::: "memory");
      asm volatile(".Lkpc1:" ::: "memory");
      unsigned char* im = lds + 65536;
      const __amdgpu_buffer_rsrc_t rso = __builtin_amdgcn_make_buffer_rsrc((void*)out, 0, S_LEN * OUTLD * 2, 0x00020000);
      u32x4 t[8];
#pragma unroll
      for (int i = 0; i < 8; ++i) { const int p = i * 256 + tid; t[i] = __builtin_amdgcn_raw_buffer_load_b128(rst, (p >> 3) * 16384 + (p & 7) * 16, SSM_L * SSM_D * 2 + si * 128, 16); }
#pragma unroll
      for (int i = 0; i < 8; ++i) {
        const int p = i * 256 + tid, un = p >> 3, rp = p & 7;
        *(u32x2*)(im + (2 * rp) * 2064 + un * 8) = (u32x2){t[i][0], t[i][1]};
        *(u32x2*)(im + (2 * rp + 1) * 2064 + un * 8) = (u32x2){t[i][2], t[i][3]};
      }
      LBAR();
#pragma unroll
      for (int i = 0; i < 8; ++i) {
        const int p = i * 256 + tid;
        __builtin_amdgcn_raw_buffer_store_b128(*(const u32x4*)(im + (p >> 7) * 2064 + (p & 127) * 16), rso, ((p >> 7) * OUTLD + DM + (p & 127) * 8) * 2, si * 16 * OUTLD * 2, YAUX);
      }
    }
  } else {
    const int g = blockIdx.x & 3, li = blockIdx.x >> 3;
    const int xi = g == 0 ? (li < 16 ? li : -1) : (li < 26 + (g < 3) ? 16 + 27 * (g - 1) + li : -1);
    const int ei = g == 0 ? li - 16 : 16 + 5 * (g - 1) + (31 - li);
    if (xi >= 0) {
      LOCAL_IDS
      const int head = xi & 31, mi = xi >> 5;
      u32* prog = (u32*)(xb + PGO_X + xi * 128);
      if (mi == 1) x_role<true>(lds, x, Wv, FO_V, head, rsf, prog, tag, lane, w);
      else x_role<false>(lds, x, mi == 0 ? Wk : Wq, mi == 0 ? FO_K : FO_Q, head, rsf, prog, tag, lane, w);
    } else {
      LOCAL_IDS
      u32 acc = 0;
#pragma unroll
      for (int i = 0; i < 4; ++i) acc += *(const u32*)(x + (size_t)((i * 32 + ei) * 256 + tid) * 64);
#define UV 0
#define WWARM(n, first) {    \
        const __amdgpu_buffer_rsrc_t rwo = __builtin_amdgcn_make_buffer_rsrc((void*)Wo, 0, DM * HD * 2, 0x00020000); \
        const int ldsb = (int)(size_t)(LAS unsigned char*)lds; \
        _Pragma("unroll 1") for (int c = 0; c < (n); ++c) { dma16(rwo, lane * 16, ((first) + c) * 16384, 1024, ldsb + w * 32768 + (c & 1) * 16384); waitvm<32>(); } }
      if (UV == 0 || (UV == 1 && ei < 16)) WWARM(4, (ei * 4 + w) * 4)
      if (UV == 2 && ei < 16) WWARM(8, (ei * 4 + w) * 8)
      if (ei == 0 && tid < 16) acc += *(const u32*)(Dskip + tid * 64);
      asm volatile("" :: "v"(acc));
      if (ei >= 16) {
      { const u32 t0 = rt32(); for (int i = 0; i < 1000 && (int)(rt32() - t0) < UDELAY; ++i) asm volatile("s_sleep 16" ::: "memory"); }
      asm volatile("s_waitcnt vmcnt(0)\n\ts_barrier" ::: "memory");
      for (int ps = 0; ps < 2; ++ps) {
      const int r0 = ((ei - 16) * 2 + ps) * 64;
#pragma unroll
      for (int hb = 0; hb < 2; ++hb) {
        u32x4 t[16];
#pragma unroll
        for (int i = 0; i < 16; ++i) t[i] = *(const u32x4*)(u + (size_t)r0 * SSM_D + ((hb * 16 + i) * 256 + tid) * 8);
#pragma unroll
        for (int i = 0; i < 16; ++i) { const int p = (hb * 16 + i) * 256 + tid; *(u32x4*)(lds + (p >> 7) * 2064 + (p & 127) * 16) = t[i]; }
      }
      LBAR();
      for (int i = 0; i < 32; ++i) {
        const int p = i * 256 + tid, un = p >> 5, rp = p & 31;
        const u32x2 a = *(const u32x2*)(lds + (2 * rp) * 2064 + un * 8), c = *(const u32x2*)(lds + (2 * rp + 1) * 2064 + un * 8);
        __builtin_amdgcn_raw_buffer_store_b128((u32x4){a[0], a[1], c[0], c[1]}, rst, un * 16384 + rp * 16, r0 * 8, 16);
      }
      asm volatile("s_waitcnt vmcnt(0) lgkmcnt(0)\n\ts_barrier" ::: "memory");
      }
      if (UV == 1) { WWARM(4, (ei * 4 + w) * 4) waitvm<0>(); }
      wait_start(rsx, fb, tag, true);
      if (tid == 0) __hip_atomic_fetch_add((u32*)(xb + XUDN), 1u, RLX_AGENT);
      }
    }
    asm volatile("s_waitcnt lgkmcnt(0)\n\ts_barrier" :: "s"(tag) : "memory");
    if (w == 0 && fresh_lane() == 0) __builtin_amdgcn_raw_buffer_store_b32(tag, rsx, 0, PGO_S + si * 128, 16);
    const bool isg = g == 0 ? li >= 16 : (g >= 5 - GNG && li < 16);
    if (xi >= 0 && !(GNOSSM && isg)) {
      LOCAL_IDS
      bool uok = false;
      wait_start(rsx, fb, tag, true);
      while (true) {
        LBAR();
        if (tid == 0) *(u32*)(lds + L_TOTAL - 16) = __hip_atomic_fetch_add((u32*)(xb + XTKT), 1u, RLX_AGENT) + 1u;
        LBAR();
        const int un = __builtin_amdgcn_readfirstlane(*(const u32*)(lds + L_TOTAL - 16));
        if (un >= 256) break;
        ssm_unit(lds, 4 * un, rst, rsx, xb, fb, uok, Dskip, tid, lane, w);
        if (isg) break;
      }
    }
    if (isg) {
      g_role(lds, Wo, out, rsx, rsf, fb, tag, g ? g + GNG - 4 : 0, li & 15, w);
    }
    else {
      const int lane = fresh_lane();
      int sh = (ei - 16) * 4 + w, lim = (NSEG - 4) * NH, wi = sh, dn = 0;
      bool go = xi < 0;
      while (true) {
        if (go) a_role(rsx, rsf, sh, lim, wi, dn, lane, fb, tag);
        if (xi >= 0 && (w >= 2 || go)) break;
        const int t = __builtin_amdgcn_readfirstlane(lane == 0 ? __hip_atomic_fetch_add((u32*)(xb + XTK2), 1u, RLX_AGENT) + 1u : 0u);
        if (t >= 128) break;
        wi = 64 + t; sh = (NSEG - 4) * NH + t; lim = sh + 1; dn = 31; go = true;
      }
    }
  }
}

extern "C" void kernel_launch(void* const* d_in, const int* in_sizes, int n_in, void* d_out, int out_size,
                              void* d_ws, size_t ws_size, hipStream_t stream) {
  const u16* x = (const u16*)d_in[0];
  const u16* mem0 = (const u16*)d_in[1];
  const u16* Wq = (const u16*)d_in[2];
  const u16* Wk = (const u16*)d_in[3];
  const u16* Wv = (const u16*)d_in[4];
  const u16* Wo = (const u16*)d_in[5];
  const u16* u = (const u16*)d_in[7];
  const u16* A_re = (const u16*)d_in[8];
  const u16* A_im = (const u16*)d_in[9];
  const u16* B_re = (const u16*)d_in[10];
  const u16* B_im = (const u16*)d_in[11];
  const u16* C_re = (const u16*)d_in[12];
  const u16* C_im = (const u16*)d_in[13];
  const u16* log_dt = (const u16*)d_in[14];
  const u16* Dskip = (const u16*)d_in[15];
  u16* out = (u16*)d_out;
  char* ws = (char*)d_ws;

  main_kernel<<<256, 256, 0, stream>>>(x, mem0, Wq, Wk, Wv, Wo, (unsigned char*)ws, u, A_re, A_im, B_re, B_im, C_re, C_im, log_dt, Dskip, out
      );
}
```

```cpp
#include <hip/hip_runtime.h>
#include <hip/hip_bf16.h>
#include <stdint.h>

#define S_LEN 2048
#define DM 1024
#define NH 32
#define DH 128
#define HD 4096
#define MEM 8
#define SEG 32
#define NSEG 64
#define LTOK 40
#define SSM_L 2048
#define SSM_D 1024
#define SSM_N 128
#define OUTLD 2048

typedef unsigned short u16;
typedef unsigned int u32;
typedef unsigned long long u64;
typedef __attribute__((ext_vector_type(8))) short bf16x8;
typedef __attribute__((ext_vector_type(4))) float f32x4;
typedef __attribute__((ext_vector_type(4))) unsigned int u32x4;
typedef __attribute__((ext_vector_type(2))) unsigned int u32x2;
typedef __bf16 bf16x2_t __attribute__((ext_vector_type(2)));
typedef short v4i16_t __attribute__((ext_vector_type(4)));

#define SENT 0xFFFFFFFFu
#define RLX_AGENT __ATOMIC_RELAXED, __HIP_MEMORY_SCOPE_AGENT

__device__ __forceinline__ float bf2f(u16 v) { return __uint_as_float(((u32)v) << 16); }
__device__ __forceinline__ u16 f2bf(float f) {
  u32 u = __float_as_uint(f);
  u32 r = (u + 0x7fffu + ((u >> 16) & 1u)) >> 16;
  if ((u & 0x7fffffffu) > 0x7f800000u) r = 0x7fc0u;
  return (u16)r;
}
__device__ __forceinline__ float rbf(float f) { return bf2f(f2bf(f)); }

#define TR 92
#define TP 180
#define TDB 62
#define TDC 110
#define NR1 4
#define XPAGE (2 << 20)
#define XRING 3
#define XLP(p, L) ((((L) >> 13) << 15) + ((p) << 14) + ((L) & 8191))
#define XO_R3 131072
#define XR1A(x, L) XLP((x) & 1, ((x) >> 1) * 16384 + (L))
#define XR3A(rg, L) (XO_R3 + XLP(rg, L))
#define XSB(s) (((s) % XRING) == 2 ? 1048576 : ((s) % XRING) * 262144)
#define XR2B 524288
#define XR2S 1048576
#define XR2(j) (((((j) & 31) + (((j) >> 6) << 5)) / 6 << 15) + (((((j) >> 5) & 1) ^ 1) << 14) + ((((j) & 31) + (((j) >> 6) << 5)) % 6) * 1280)
#define XSYN (1048576 + 262144)
#define PGO_R (XSYN + 32768)
#define XCAL (PGO_R + 32768)
static_assert(XCAL + 16384 <= XPAGE, "exchange page");
#define WS_X 0ull
#define NPG 2
#define WS_X_SZ ((u64)NPG * XPAGE)
#define WS_F (WS_X + WS_X_SZ)
#define XEPO (fb)
#define XSTA (fb + 128)
#define PGO_X (fb + 256)
#define PGO_Y (PGO_X + 96 * 128)
#define PGO_S (PGO_Y + 128 * 128)
#define PGO_D (PGO_S + 128 * 128)
#define PGO_A (PGO_D + 128 * 128)
#define XTKT (PGO_A + 192 * 128)
#define XUDN (XTKT + 128)
#define XYDN (XUDN + 128)
#define XTK2 (XYDN + 128)
#define XSTR(x) (XTK2 + 128 + (x) * 128)
#define WS_F_SZ ((u64)(2 + 96 + 128 + 128 + 128 + 192 + 8) * 128)
#define WS_XCH_END (WS_F + 131072)
#define WS_QX (WS_XCH_END)
#define WS_QKV_SZ ((u64)S_LEN * HD * 2)
#define WS_KX (WS_QX + WS_QKV_SZ)
#define WS_VX (WS_KX + WS_QKV_SZ)
#define WS_OX (WS_VX + WS_QKV_SZ)
#define WS_KM (WS_OX + WS_QKV_SZ)
#define WS_KM_SZ ((u64)NSEG * 8 * HD * 2)
#define WS_VM (WS_KM + WS_KM_SZ)
#define WS_UT (WS_VM + WS_KM_SZ)
#define WS_YT (WS_UT + (u64)SSM_L * SSM_D * 2)
#define WS_KD (WS_YT + (u64)SSM_L * SSM_D * 2)
#define WS_TOTAL (WS_KD + (u64)SSM_D * 3072)

#define SPIN_CAP 400000
#define TRY_CAP 20000
#define LAS __attribute__((address_space(3)))
#define LBAR() asm volatile("s_waitcnt lgkmcnt(0)\n\ts_barrier" ::: "memory")
__device__ __forceinline__ u32 max4u(u32x4 v) { return max(max(v[0], v[1]), max(v[2], v[3])); }
typedef float f32x2_t __attribute__((ext_vector_type(2)));
__device__ __forceinline__ u32 pack2(float a, float b) {
  f32x2_t v = {a, b};
  bf16x2_t r = __builtin_convertvector(v, bf16x2_t);
  return __builtin_bit_cast(u32, r);
}
__device__ __forceinline__ float rbf_hw(float a) { return __uint_as_float(pack2(a, a) & 0xffff0000u); }
__device__ __forceinline__ u32 nosent(u32 v) { return min(v, 0xFFFFFFFEu); }
template <int N> __device__ __forceinline__ void waitvm() { asm volatile("s_waitcnt vmcnt(%0)" ::"n"(N) : "memory"); }
typedef __attribute__((ext_vector_type(16))) float f32x16;
__device__ __forceinline__ u32 split_hl(float kv) {
  const u32 h = pack2(kv, kv) & 0xffffu;
  const float r1 = kv - __uint_as_float(h << 16);
  return h | (pack2(r1, r1) & 0xffff0000u);
}
__device__ __forceinline__ int kidx(int idx) {
  const int sh = idx < 256 ? 0 : (idx < 512 ? 1 : (idx < 1024 ? 2 : 3));
  const int q = sh ? ((idx + (1 << (sh - 1)) - 1 + ((idx >> sh) & 1)) >> sh) : idx;
  return q + 128 * sh;
}

#define FO_Q 0
#define FO_K ((int)WS_QKV_SZ)
#define FO_V ((int)(2 * WS_QKV_SZ))
#define FO_O ((int)(3 * WS_QKV_SZ))
#define FO_KM ((int)(4 * WS_QKV_SZ))
#define FO_VM ((int)(4 * WS_QKV_SZ + WS_KM_SZ))
__device__ __forceinline__ u32x4 wait_start(__amdgpu_buffer_rsrc_t rsx, int fb, u32 tag, bool slow, int xcd = 0) {
  u32x4 st;
  for (int it = 0; it < SPIN_CAP; ++it) {
    asm volatile("" ::: "memory");
    st = __builtin_amdgcn_raw_buffer_load_b128(rsx, 0, slow ? XSTA : XSTR(xcd), 16);
    if ((u32)__builtin_amdgcn_readfirstlane(st[3]) == tag) break;
    if (slow) asm volatile("s_sleep 20" ::: "memory"); else asm volatile("s_sleep 1" ::: "memory");
  }
  asm volatile("" ::: "memory");
  return st;
}
__device__ __forceinline__ int xprog(u32 v, u32 tag) { return v - tag <= 80u ? (int)(v - tag) : -1; }
__device__ __forceinline__ float xmax4(float m) {
  { auto r = __builtin_amdgcn_permlane16_swap(__float_as_uint(m), __float_as_uint(m), false, false); m = __builtin_fmaxf(__uint_as_float(r[0]), __uint_as_float(r[1])); }
  { auto r = __builtin_amdgcn_permlane32_swap(__float_as_uint(m), __float_as_uint(m), false, false); m = __builtin_fmaxf(__uint_as_float(r[0]), __uint_as_float(r[1])); }
  return m;
}
__device__ __forceinline__ float xsum4(float m) {
  { auto r = __builtin_amdgcn_permlane16_swap(__float_as_uint(m), __float_as_uint(m), false, false); m = __uint_as_float(r[0]) + __uint_as_float(r[1]); }
  { auto r = __builtin_amdgcn_permlane32_swap(__float_as_uint(m), __float_as_uint(m), false, false); m = __uint_as_float(r[0]) + __uint_as_float(r[1]); }
  return m;
}
#define MF16(a, b, c) __builtin_amdgcn_mfma_f32_16x16x32_bf16(__builtin_bit_cast(bf16x8, a), __builtin_bit_cast(bf16x8, b), c, 0, 0, 0)
__device__ __forceinline__ f32x4 rnd2(f32x4 v, float sc) {
  const u32 a = pack2(v[0], v[1]), b = pack2(v[2], v[3]);
  const u32 c = pack2(__uint_as_float(a << 16) * sc, __uint_as_float(a & 0xffff0000u) * sc), d = pack2(__uint_as_float(b << 16) * sc, __uint_as_float(b & 0xffff0000u) * sc);
  return (f32x4){__uint_as_float(c << 16), __uint_as_float(c & 0xffff0000u), __uint_as_float(d << 16), __uint_as_float(d & 0xffff0000u)};
}
struct XaOps { u32x4 q[2][4], k[2][4], v[8]; };
__device__ __forceinline__ void xa_load(XaOps& o, __amdgpu_buffer_rsrc_t rsf, int sh, int lane) {
#pragma unroll
  for (int i = 0; i < 8; ++i) {
    o.q[i >> 2][i & 3] = __builtin_amdgcn_raw_buffer_load_b128(rsf, lane * 16, FO_Q + (sh * 8 + i) * 1024, 16);
    o.k[i >> 2][i & 3] = __builtin_amdgcn_raw_buffer_load_b128(rsf, lane * 16, FO_K + (sh * 8 + i) * 1024, 16);
  }
#pragma unroll
  for (int dt = 0; dt < 8; ++dt) o.v[dt] = __builtin_amdgcn_raw_buffer_load_b128(rsf, lane * 16, FO_V + (sh * 8 + dt) * 1024, 16);
}
__device__ __forceinline__ void xa_compute(const XaOps& o, const u32x4 (&km)[4], const u32x2 (&vm)[8], __amdgpu_buffer_rsrc_t rsf, int sh, int lane) {
  const int lr = lane & 15, lg = lane >> 4;
  const float scale = 0.08838834764831845f, L2E = 1.4426950408889634f, NEG = -1000000000.0f;
  const f32x4 z = (f32x4){0, 0, 0, 0};
  f32x4 s00 = z, s0m = z, s10 = z, s11 = z, s1m = z;
#pragma unroll
  for (int ks = 0; ks < 4; ++ks) { s00 = MF16(o.k[0][ks], o.q[0][ks], s00); s10 = MF16(o.k[0][ks], o.q[1][ks], s10); s11 = MF16(o.k[1][ks], o.q[1][ks], s11); }
#pragma unroll
  for (int ks = 0; ks < 4; ++ks) { s0m = MF16(km[ks], o.q[0][ks], s0m); s1m = MF16(km[ks], o.q[1][ks], s1m); }
  s00 = rnd2(s00, scale); s10 = rnd2(s10, scale); s11 = rnd2(s11, scale); s0m = rnd2(s0m, scale); s1m = rnd2(s1m, scale);
  asm volatile("" : "+v"(s00), "+v"(s11), "+v"(s0m), "+v"(s1m));
#pragma unroll
  for (int e = 0; e < 4; ++e) {
    const bool msk = 4 * lg + e > lr;
    s00[e] = msk ? NEG : s00[e]; s11[e] = msk ? NEG : s11[e];
    s0m[e] = lg >= 2 ? -INFINITY : s0m[e]; s1m[e] = lg >= 2 ? -INFINITY : s1m[e];
  }
  float m0 = NEG, m1 = -INFINITY;
#pragma unroll
  for (int e = 0; e < 4; ++e) { m0 = __builtin_fmaxf(m0, __builtin_fmaxf(s00[e], s0m[e])); m1 = __builtin_fmaxf(__builtin_fmaxf(m1, s10[e]), __builtin_fmaxf(s11[e], s1m[e])); }
  m0 = xmax4(m0); m1 = xmax4(m1);
  float t0 = 0.f, t1 = 0.f;
#pragma unroll
  for (int e = 0; e < 4; ++e) {
    s00[e] = __builtin_amdgcn_exp2f((s00[e] - m0) * L2E); s0m[e] = __builtin_amdgcn_exp2f((s0m[e] - m0) * L2E);
    s10[e] = __builtin_amdgcn_exp2f((s10[e] - m1) * L2E); s11[e] = __builtin_amdgcn_exp2f((s11[e] - m1) * L2E); s1m[e] = __builtin_amdgcn_exp2f((s1m[e] - m1) * L2E);
    t0 += s00[e] + s0m[e]; t1 += (s10[e] + s11[e]) + s1m[e];
  }
  const float em = __builtin_amdgcn_exp2f((NEG - m0) * L2E);
  t0 += 4.f * em;
  const float r0 = __builtin_amdgcn_rcpf(xsum4(t0)), r1 = __builtin_amdgcn_rcpf(xsum4(t1));
  s00 *= r0; s0m *= r0; s10 *= r1; s11 *= r1; s1m *= r1;
  const u32x4 p0 = (u32x4){pack2(s00[0], s00[1]), pack2(s00[2], s00[3]), pack2(s0m[0], s0m[1]), pack2(s0m[2], s0m[3])};
  const u32x4 p1 = (u32x4){pack2(s10[0], s10[1]), pack2(s10[2], s10[3]), pack2(s11[0], s11[1]), pack2(s11[2], s11[3])};
  const u32x4 pm = (u32x4){0u, 0u, pack2(s1m[0], s1m[1]), pack2(s1m[2], s1m[3])};
  const u32 pe = pack2(em * r0, em * r0);
  const u32x4 px = (u32x4){0u, 0u, pe, pe};
#pragma unroll
  for (int pr = 0; pr < 4; ++pr) {
    f32x4 o0[2], o1[2];
#pragma unroll
    for (int t = 0; t < 2; ++t) {
      const int dt = 2 * pr + t;
      const u32x4 a0 = (u32x4){o.v[dt][0], o.v[dt][1], vm[dt][0], vm[dt][1]};
      o0[t] = MF16(a0, p0, z);
      o1[t] = MF16(o.v[dt], p1, z);
      o0[t] = MF16(o.v[dt], px, o0[t]);
      o1[t] = MF16(a0, pm, o1[t]);
    }
    const int ob = FO_O + ((sh >> 5) * 256 + (sh & 31) * 4 + pr) * 1024;
    __builtin_amdgcn_raw_buffer_store_b128((u32x4){pack2(o0[0][0], o0[0][1]), pack2(o0[0][2], o0[0][3]), pack2(o0[1][0], o0[1][1]), pack2(o0[1][2], o0[1][3])}, rsf, lane * 16, ob, 16);
    __builtin_amdgcn_raw_buffer_store_b128((u32x4){pack2(o1[0][0], o1[0][1]), pack2(o1[0][2], o1[0][3]), pack2(o1[1][0], o1[1][1]), pack2(o1[1][2], o1[1][3])}, rsf, lane * 16, ob + 131072, 16);
  }
}
#define NAW 64
__device__ __forceinline__ void a_role(__amdgpu_buffer_rsrc_t rsx, __amdgpu_buffer_rsrc_t rsf, int sh, int lim, int wi, int done, int lane, int fb, u32 tag) {
  const int lr = lane & 15, lg = lane >> 4;
  const int okm = (lg * 8 + (lr & 7)) * 16, ovm = lg < 2 ? (lg * 16 + lr) * 8 : 0x7ffffff0;
  int rp = 0, xp = -1, spin = 0, pub = done, miss = 0, tp = 200;
  u32 tseen = 0, tq0 = 0, tq1 = 0, tq2 = 0, tq3 = 0; (void)tq0; (void)tq1; (void)tq2; (void)tq3;
#define XA_WAITX(s_)                                                                  \
  while (xp <= ((s_) >> 5)) {                                                                                          \
    asm volatile("" ::: "memory");                                                                                     \
    int pv = min(xprog(__builtin_amdgcn_raw_buffer_load_b32(rsx, lane * 128, PGO_X, 16), tag), xprog(__builtin_amdgcn_raw_buffer_load_b32(rsx, (64 + (lane & 31)) * 128, PGO_X, 16), tag)); \
    _Pragma("unroll") for (int o_ = 1; o_ < 64; o_ *= 2) pv = min(pv, __shfl_xor(pv, o_));                             \
    xp = __builtin_amdgcn_readfirstlane(pv);                                                                           \
    if (xp > ((s_) >> 5) || ++spin > SPIN_CAP) break;                                                                  \
    asm volatile("s_sleep 16" ::: "memory");                                                                           \
  }
#define XA_PUB() if (pub != done) { pub = done; asm volatile("s_nop 4\n\tbuffer_store_dword %0, %1, %2, %3 offen sc1" :: "v"(done), "v"(lane ? 0x7ffffff0 : 0), "s"(rsx), "s"(PGO_A + wi * 128) : "memory"); }
#define XA_STEP(CUR, NXT)                                                                                              \
  {                                                                                                                    \
    const int seg = sh >> 5, nx = sh + NAW;                                                                            \
    XA_WAITX(min(nx, lim - 1))                                                                                         \
    if (rp <= seg) {                         \
      while (true) {                                                                                                   \
        int want = min(seg + 1, NSEG - 1);                                                                             \
        if (tseen) {                                                                                                   \
          const u32 due = tseen + (u32)(want - rp) * tp - (want + 1 < NSEG ? 0 : XA_LAST) + (seg + 1 < NSEG ? 0 : XA_END); \
          if ((int)(due - (u32)__builtin_amdgcn_s_memrealtime()) > 0 || miss) { asm volatile("s_waitcnt vmcnt(0)" ::: "memory"); XA_PUB() } \
          for (int i_ = 0; i_ < 4000 && (int)(due - (u32)__builtin_amdgcn_s_memrealtime()) > 0; ++i_) asm volatile("s_sleep 2" ::: "memory"); \
          if (!miss) want = min(max(want, rp + max((int)((u32)__builtin_amdgcn_s_memrealtime() - tseen), 0) / tp), NSEG - 1); \
        }                                                                                                              \
        XA_DBG4                                                                                                        \
        asm volatile("" ::: "memory");                                                                                 \
        if (seg + 1 < NSEG) {                                                                                          \
          const int r_ = __builtin_amdgcn_readfirstlane(__builtin_amdgcn_raw_buffer_load_b32(rsx, 0, pgr + want * 128, 16)); \
          if (r_ >= 0) { const u32 n_ = (u32)__builtin_amdgcn_s_memrealtime(); tseen = n_ - ((n_ - (r_ >> 16)) & 0x7fffu) + XA_LAG; tp = max(r_ & 1023, 1); rp = want; miss = 0; break; } \
        } else if (__ballot(__builtin_amdgcn_raw_buffer_load_b32(rsx, ((sh & 31) * 4 + (lane & 3)) * 128, PGO_D, 16) == SENT) == 0) break; \
        if (++spin > SPIN_CAP) break;                                                                                  \
        if (miss++ && seg + 2 < NSEG) asm volatile("s_sleep 20" ::: "memory");                                                         \
      }                                                                                                                \
    }                                                                                                                  \
    asm volatile("" ::: "memory");                                                                                     \
    XA_DBG2                                                                                                            \
    u32x4 km[4]; u32x2 vm[8];                                                                                          \
    _Pragma("unroll") for (int i = 0; i < 4; ++i) km[i] = __builtin_amdgcn_raw_buffer_load_b128(rsf, okm, FO_KM + (sh * 4 + i) * 512, 16); \
    _Pragma("unroll") for (int i = 0; i < 8; ++i) vm[i] = __builtin_amdgcn_raw_buffer_load_b64(rsf, ovm, FO_VM + (sh * 8 + i) * 256, 16);  \
                              \
    const int pk_ = min(rp + max((int)((u32)__builtin_amdgcn_s_memrealtime() - tseen), 0) / tp, NSEG - 1);              \
    const u32 pv_ = __builtin_amdgcn_raw_buffer_load_b32(rsx, 0, pgr + pk_ * 128, 16);                                 \
    asm volatile("" ::: "memory");                                                                                     \
    __builtin_amdgcn_s_waitcnt(0x0F70);                                                                   \
    asm volatile("" ::: "memory");                                                                                     \
    { const int r_ = __builtin_amdgcn_readfirstlane(pv_);                                                              \
      if (r_ >= 0) { const u32 n_ = (u32)__builtin_amdgcn_s_memrealtime(); tseen = n_ - ((n_ - (r_ >> 16)) & 0x7fffu) + XA_LAG; tp = max(r_ & 1023, 1); rp = pk_; } } \
    XA_PUB()                                   \
    if (nx < lim) xa_load(NXT, rsf, nx, lane);                                                                         \
    XA_DBG3                                                                                                            \
    __builtin_amdgcn_sched_barrier(0);                                                                                 \
    xa_compute(CUR, km, vm, rsf, sh, lane);                                                                            \
    ++done;                                                                                                            \
    XA_DBG                                                                                                             \
    sh = nx;                                                                                                           \
    if (sh >= lim) break;                                                                                              \
  }
#define XA_LAST 24
#define XA_END 0
#define XA_LAG 30
#define XA_DBG
#define XA_DBG2
#define XA_DBG3
#define XA_DBG4
  const int pgr = PGO_R + (__builtin_amdgcn_readfirstlane(wait_start(rsx, fb, tag, true)[2]) & 8192);
  XaOps A, B;
  if (sh >= NAW) {
    const int r_ = __builtin_amdgcn_readfirstlane(__builtin_amdgcn_raw_buffer_load_b32(rsx, 0, pgr + 40 * 128, 16));
    if (r_ >= 0) { const u32 n_ = (u32)__builtin_amdgcn_s_memrealtime(); tseen = n_ - ((n_ - (r_ >> 16)) & 0x7fffu) + XA_LAG; tp = max(r_ & 1023, 1); rp = 40; }
  }
  XA_WAITX(sh)
  asm volatile("" ::: "memory");
  xa_load(A, rsf, sh, lane);
  while (true) { XA_STEP(A, B) XA_STEP(B, A) }
  asm volatile("s_waitcnt vmcnt(0)" ::: "memory");
  XA_PUB()
}

typedef __attribute__((ext_vector_type(32))) unsigned int u32x32;
typedef __attribute__((ext_vector_type(16))) unsigned int u32x16;
typedef __attribute__((ext_vector_type(8))) unsigned int u32x8;
#define STR2(x) #x
#define STR(x) STR2(x)
#define RNG(f, b, n) f "[" STR(b) ":" STR((b) + (n)-1) "]"
#define REG(b) "v[" STR(b) "]"
#define LR_RED 16384
#define LR_CTL LR_RED
#define LR_STG 32768
#define L_RSTAMP LR_STG
#define STAMPV(i, v)
#define EXF (NH * 8 * 1024)
#define EXK 65536
#define OOR 0x7ffffff0

__device__ __forceinline__ u32 lds_word(int addr) {
  u32 v;
  asm volatile("ds_read_b32 %0, %1\n\ts_waitcnt lgkmcnt(0)" : "=v"(v) : "v"(addr) : "memory");
  return __builtin_amdgcn_readfirstlane(v);
}
__device__ __forceinline__ int fresh_lane() {
  int l;
  asm volatile("v_mbcnt_lo_u32_b32 %0, -1, 0\n\tv_mbcnt_hi_u32_b32 %0, -1, %0" : "=v"(l));
  return l;
}
__device__ __forceinline__ u32 rt32() { return (u32)__builtin_amdgcn_s_memrealtime(); }
#define SYO(s) (XSYN + flip + (s) * 64)
#define TKS 0.85f
#define TLB 1
#define TLC 1
#define TLP 2
#define KG0 0
#define CLS0 0u
#define APC 5
#define ADL 12
#define CALN 2
#define TE1 4
#define TE2 3
#define TE3 2
#define XTH 4
#define TS3 42
#define TE0 24
#define TD0 100
#define TD 120
#define PHASE 1
#define XSK 0
#define CLKS(T, R, X) WLG(2) "s_sub_u32 s49, " T ", " R "\n\t" "s_cmp_le_i32 s49, " STR(3 * PHASE) "\n\t" "s_cbranch_scc1 " X "\n\t"
#define CLKH(OFF) WLG(0) "s_memrealtime s[72:73]\n\t" "s_add_u32 s50, s43, s44\n\t" "s_add_u32 s50, s50, " OFF "\n\t" "s_nop 1\n\t" "s_memrealtime s[74:75]\n\t"
#define CLKL(L, T)   L ":\n\t" "s_memrealtime s[76:77]\n\t" CLKS(T, "s72", L "x") "s_memrealtime s[72:73]\n\t" CLKS(T, "s74", L "x")     \
                     "s_memrealtime s[74:75]\n\t" CLKS(T, "s76", L "x") "s_branch " L "\n" \
                     L "x:\n\t" CLKP(L)
#define CLKW(L, OFF) CLKH(OFF) "s_nop 3\n" CLKL(L, "s50")
#define CLKP(L) "s_cmp_lt_i32 s49, 1\n\t" "s_cbranch_scc1 " L "y\n\t" "s_nop " STR(PHN) "\n\t" "s_cmp_lt_i32 s49, 2\n\t" "s_cbranch_scc1 " L "y\n\t" "s_nop " STR(PHN) "\n\t" \
                "s_cmp_lt_i32 s49, 3\n\t" "s_cbranch_scc1 " L "y\n\t" "s_nop " STR(PHN) "\n\t" "s_nop 0\n\t" "s_branch " L "y\n\t" "s_nop 0\n" L "y:\n\t"
#define PHN 3
#define REL(L) "s_barrier\n" L ":\n\t"
#define RELV(L) "s_cbranch_vccnz " L "\n\t" REL(L)
#define RELN(L) "s_cmp_eq_u32 %[wv], 0\n\t" "s_cbranch_scc1 " L "a\n\t" "s_cmp_eq_u32 %[wv], 1\n\t" "s_cbranch_scc1 " L "b\n\t" \
                "s_barrier\n\t" "s_nop " STR(PD2) "\n\t" "s_branch " L "\n" L "b:\n\t" "s_barrier\n\t" "s_branch " L "\n" L "a:\n\t" "s_barrier\n" L ":\n\t"
#define PD2 2
#define TS_(n) "s_memtime s[80+2*" #n ":81+2*" #n "]\n\t"
#define TSA(n)
#define TSB(n)
#define TSW(n)
#define TVM 12
template <int NTL, int KS>
__device__ __forceinline__ void tr_chunk(const u16* S, int lr, int lg, u32 (&r)[NTL][KS][8]) {
  const int q = lr >> 2, p = lr & 3;
#pragma unroll
  for (int t = 0; t < NTL; ++t)
#pragma unroll
    for (int i = 0; i < KS; ++i)
#pragma unroll
      for (int kb = 0; kb < 2; ++kb) {
        const u16* base = S + (64 * i + 32 * kb + 16 * (lg >> 1) + 4 * (lg & 1) + (q & 1) + 8 * (q >> 1)) * (16 * NTL) + 16 * t + 4 * p;
        const u32x2 t0 = __builtin_bit_cast(u32x2, __builtin_amdgcn_ds_read_tr16_b64_v4i16((LAS v4i16_t*)(base)));
        const u32x2 t1 = __builtin_bit_cast(u32x2, __builtin_amdgcn_ds_read_tr16_b64_v4i16((LAS v4i16_t*)(base + 2 * 16 * NTL)));
        r[t][i][4 * kb + 0] = t0[0]; r[t][i][4 * kb + 1] = t0[1]; r[t][i][4 * kb + 2] = t1[0]; r[t][i][4 * kb + 3] = t1[1];
      }
}
#define SMF(acc, a, bf, bi) "v_smfmac_f32_16x16x64_bf16 " RNG("v", acc, 4) ", " RNG("v", a, 4) ", " RNG(bf, bi, 8) ", v34\n\t"
#define ZER(r) "v_mov_b64 " RNG("v", r, 2) ", 0\n\t" "v_mov_b64 " RNG("v", (r) + 2, 2) ", 0\n\t"
#define GLD(r, o) "s_xor_b32 s49, s50, " STR(o) "\n\t" "buffer_load_dwordx4 " RNG("v", r, 4) ", v32, %[rs], s49 offen sc1\n\t"
#define GL1(r, o) "s_xor_b32 s49, s50, " STR(o) "\n\t" "buffer_load_dwordx4 " RNG("v", r, 4) ", v33, %[rs], s49 offen sc1\n\t"
#define GL2(r, o) GL1(r, 4096 + (o))
#define MXA0(r) "v_max3_u32 v63, " REG(r) ", " REG((r) + 1) ", " REG((r) + 1) "\n\t"
#define MXA(r) "v_max3_u32 v63, v63, " REG(r) ", " REG((r) + 1) "\n\t"
#define MXB(r) "v_max3_u32 v63, v63, " REG((r) + 2) ", " REG((r) + 3) "\n\t"
#define CMPB "v_cmp_eq_u32 vcc, s54, v63\n\t"
#define PSA(r) "v_pk_add_f32 " RNG("v", r, 2) ", " RNG("v", r, 2) ", " RNG("v", (r) + 2, 2) "\n\t"
#define PSW(r, o) "ds_write_addtid_b32 " REG(r) " offset:" STR(o) "\n\t"
#define RDQ(r, o) "ds_read_b128 " RNG("v", r, 4) ", v37 offset:" STR(o) "\n\t"
#define SQ(t, i) SMF(64 + 4 * (t), 96 + 4 * (i), "a", ((i) * 4 + (t)) * 8)
#define SV(t, i) SMF(24 + 4 * (t), 96 + 4 * (i), "a", 128 + ((i) * 2 + (t)) * 8)
#define SVP(t, i) SV(t, i)
#define SW(a, q, i) SMF((a) + 4 * (q), 96 + 4 * (i), "v", 128 + ((i) * 2 + (q)) * 8)
#define WVM(n) "s_waitcnt vmcnt(" STR(n) ")\n\t"
#define WLG(n) "s_waitcnt lgkmcnt(" STR(n) ")\n\t"
#define WLGF(n) WLG(n)
#define PACE 7
#define PC "s_nop " STR(PACE) "\n\t"
#define PC6
#define PC7
#define WBLK(a, n, i, M0) WVM((n) + 7 - (i)) SW(a, 0, i) M0(96 + 4 * (i)) SW(a, 1, i) MXB(96 + 4 * (i))
#define CL8(a, b, c, d, e, f, g, h) "v" #a, "v" #b, "v" #c, "v" #d, "v" #e, "v" #f, "v" #g, "v" #h
#define CL64 CL8(64, 65, 66, 67, 68, 69, 70, 71)
#define CL72 CL8(72, 73, 74, 75, 76, 77, 78, 79)
#define CL80 CL8(80, 81, 82, 83, 84, 85, 86, 87)
#define CL88 CL8(88, 89, 90, 91, 92, 93, 94, 95)
#define CL96 CL8(96, 97, 98, 99, 100, 101, 102, 103), CL8(104, 105, 106, 107, 108, 109, 110, 111)
#define CL112 CL8(112, 113, 114, 115, 116, 117, 118, 119)
#define CL120 CL8(120, 121, 122, 123, 124, 125, 126, 127)
#define PKA(d, a, b) "v_pk_add_f32 " RNG("v", d, 2) ", " RNG("v", a, 2) ", " RNG("v", b, 2) "\n\t"
#define CNT(o) STR(4096 + 64 * (o))
#define ARRIVE(o) "ds_add_u32 v45, v39 offset:" CNT(o) "\n\t"

#define EXTRA "v_readlane_b32 s69, v35, s45\n\t" "s_and_b32 s56, s58, 1023\n\t" "s_bfe_u32 s57, s58, 0xa000a\n\t" "s_lshr_b32 s55, s58, 20\n\t" "s_add_u32 s66, s56, s69\n\t" "s_add_u32 s67, s57, s69\n\t" "s_add_u32 s67, s67, s69\n\t"
#define TEX(s) ((s) == 0 ? TE0 : ((s) < 4 ? TE1 : ((s) < 12 ? TE2 : ((s) < TS3 ? TE3 : 0))))
#define R2BASE "s_bitcmp1_b32 s45, 0\n\t" "s_cselect_b32 s71, " STR(XR2S) ", 0\n\t" "s_add_u32 s71, s71, %[r2]\n\t"
#define X4B 0
#define X4C 0
#define STB "buffer_store_dwordx2 v[116:117], v11, %[rs], s36 offen sc1\n"
#define STC "buffer_store_dwordx2 v[80:81], v17, %[rs], s36 offen sc1\n\t"
#define R2TEXT                                                                                                     \
              TSB(0)                                                                                               \
              "buffer_load_dwordx4 v[64:67], v43, %[rs], s71 offen sc1\n\t"                                      \
              "buffer_load_dwordx4 v[68:71], v8, %[rs], s71 offen sc1\n\t"                                       \
              "buffer_load_dwordx4 v[76:79], v9, %[rs], s71 offen sc1\n\t"                                       \
              "buffer_load_dwordx4 v[60:63], v10, %[rs], s71 offen sc1\n\t"                                      \
              "buffer_store_dwordx4 v[72:75], v47, %[rf], s41 offen sc1\n\t"                                     \
              "s_cbranch_vccnz .Lvw%=\n\t" VPOST ARRIVE(12) "s_barrier\n" ".Lvw%=:\n\t"                             \
              "ds_read2_b32 v[96:97], v12 offset0:0 offset1:4\n\t" "ds_read2_b32 v[98:99], v12 offset0:32 offset1:36\n\t" \
              "ds_read2_b32 v[100:101], v13 offset0:0 offset1:4\n\t" "ds_read2_b32 v[102:103], v13 offset0:32 offset1:36\n\t" \
              "ds_read2_b32 v[104:105], v14 offset0:0 offset1:4\n\t" "ds_read2_b32 v[106:107], v14 offset0:32 offset1:36\n\t" \
              "ds_read2_b32 v[108:109], v15 offset0:0 offset1:4\n\t" "ds_read2_b32 v[110:111], v15 offset0:32 offset1:36\n\t" \
              "v_mov_b64 v[112:113], v[80:81]\n\t" "v_mov_b64 v[114:115], v[82:83]\n\t" "s_mov_b32 s65, 0\n\t"     \
              "v_mov_b32_dpp v112, v84 row_ror:8 row_mask:0xf bank_mask:0xc\n\t" "v_mov_b32_dpp v113, v85 row_ror:8 row_mask:0xf bank_mask:0xc\n\t" \
              "v_mov_b32_dpp v114, v86 row_ror:8 row_mask:0xf bank_mask:0xc\n\t" "v_mov_b32_dpp v115, v87 row_ror:8 row_mask:0xf bank_mask:0xc\n\t" \
              "v_pk_mul_f32 v[112:113], v[112:113], s[62:63] op_sel_hi:[1,0]\n\t" "v_pk_mul_f32 v[114:115], v[114:115], s[62:63] op_sel_hi:[1,0]\n\t" \
              WLG(0)                                                                                               \
              PKA(96, 96, 100) PKA(98, 98, 102) PKA(104, 104, 108) PKA(106, 106, 110) PKA(96, 96, 104) PKA(98, 98, 106) \
              "v_cvt_pk_bf16_f32 v96, v96, 0\n\t" "v_cvt_pk_bf16_f32 v97, v97, 0\n\t" "v_cvt_pk_bf16_f32 v98, v98, 0\n\t" "v_cvt_pk_bf16_f32 v99, v99, 0\n\t" \
              "v_accvgpr_write_b32 a194, v96\n\t" "v_accvgpr_write_b32 a202, v97\n\t" "v_accvgpr_write_b32 a198, v98\n\t" "v_accvgpr_write_b32 a206, v99\n\t" \
              TSB(1) WVM(4) TSB(2) \
              "v_permlane32_swap_b32 v64, v66\n\t" "v_permlane32_swap_b32 v65, v67\n\t" \
              "v_pk_add_f32 v[92:93], v[64:65], v[66:67]\n\t" \
              WVM(3) SFM(68) \
              "v_permlane16_swap_b32 v92, v93\n\t" \
              "v_add_f32 v94, v92, v93\n\t" \
              WVM(2) "v_mul_f32 v94, s62, v94\n\t" SFM(76) \
              "v_exp_f32 v92, v94\n\t" \
              WVM(1) TSB(3) SFM(60) \
                \
              ".Lex%=:\n\t" \
              "v_exp_f32 v88, v112\n\t" "v_exp_f32 v89, v113\n\t" "v_cvt_pk_bf16_f32 v58, v92, 0\n\t" "v_exp_f32 v90, v114\n\t" "v_exp_f32 v91, v115\n\t" \
              "v_cvt_pk_bf16_f32 v56, v88, v89\n\t" "v_cvt_pk_bf16_f32 v57, v90, v91\n\t" \
              TSB(4) ROR8(50, 58) ROR8(48, 56) \
              "v_mfma_f32_16x16x32_bf16 v[88:91], v[52:55], v[56:59], 0\n\t" \
              ROR8(49, 57) "s_nop 1\n\t" \
              "v_mfma_f32_16x16x32_bf16 v[116:119], a[192:195], v[56:59], 0\n\t" \
              "s_nop 2\n\t" \
              "v_mfma_f32_16x16x32_bf16 v[116:119], a[196:199], v[48:51], v[116:119]\n\t" \
              "v_add_f32_dpp v92, v88, v88 row_ror:8 row_mask:0xf bank_mask:0xf\n\t" "v_subrev_u32 v89, 0x33800000, v92\n\t" \
              "v_mfma_f32_16x16x32_bf16 v[124:127], a[204:207], v[56:59], 0\n\t" \
              "v_rcp_f32 v93, v92\n\t" \
              "v_mfma_f32_16x16x32_bf16 v[124:127], a[200:203], v[48:51], v[124:127]\n\t" \
              TSB(5) "v_cmp_le_u32 vcc, 0x20000000, v89\n\t" "s_nop 0\n\t" \
              "s_cbranch_vccnz .Lmx%=\n\t" \
              OTAIL STB TSB(6)
#define SFM(r) "v_pk_fma_f32 v[112:113], " RNG("v", r, 2) ", s[62:63], v[112:113] op_sel_hi:[1,0,1]\n\t" "v_pk_fma_f32 v[114:115], " RNG("v", (r) + 2, 2) ", s[62:63], v[114:115] op_sel_hi:[1,0,1]\n\t"
#define ROR8(d, s) "v_mov_b32_dpp " REG(d) ", " REG(s) " row_ror:8 row_mask:0xf bank_mask:0xf\n\t"
#define OTAIL "v_pk_mul_f32 v[116:117], v[116:117], v[92:93] op_sel:[0,1]\n\t" "v_pk_mul_f32 v[118:119], v[118:119], v[92:93] op_sel:[0,1]\n\t" \
              "v_cvt_pk_bf16_f32 v116, v116, v117\n\t" "v_cvt_pk_bf16_f32 v117, v118, v119\n\t" \
              "s_mov_b64 exec, s[78:79]\n\t" \
              "v_pk_mul_f32 v[124:125], v[124:125], v[92:93] op_sel:[0,1]\n\t" "v_pk_mul_f32 v[126:127], v[126:127], v[92:93] op_sel:[0,1]\n\t" \
              "v_cvt_pk_bf16_f32 v116, v124, v125\n\t" "v_cvt_pk_bf16_f32 v117, v126, v127\n\t" \
              "s_mov_b64 exec, -1\n\t"
#define MX3(a, b) "v_max3_u32 v95, v95, " REG(a) ", " REG(b) "\n\t"
#define R2MAX ".Lmx%=:\n\t" "v_max_u32 v95, v60, v61\n\t" MX3(62, 63) MX3(64, 65) MX3(66, 67) MX3(68, 69) MX3(70, 71) MX3(76, 77) MX3(78, 79) \
              "v_cmp_eq_u32 vcc, s54, v95\n\t" "s_cbranch_vccnz .Lfail%=\n\t" OTAIL \
              "s_cmp_lg_u32 s65, 0\n\t" "s_cbranch_scc1 .Lend%=\n\t" "s_mov_b32 s65, 1\n\t" \
              "v_max3_f32 v93, v112, v113, v94\n\t" \
              "v_max3_f32 v93, v93, v114, v115\n\t" \
              "s_nop 1\n\t" \
              "v_max_f32_dpp v92, v93, v93 row_ror:8 row_mask:0xf bank_mask:0xf\n\t" \
              "v_max_f32_dpp v88, v93, v93 row_ror:8 row_mask:0xf bank_mask:0xf\n\t" \
              "s_nop 1\n\t" \
              "v_permlane16_swap_b32 v92, v88\n\t" \
              "v_max_f32 v93, v92, v88\n\t" "v_max_f32 v92, v92, v88\n\t" \
              "s_nop 1\n\t" \
              "v_permlane32_swap_b32 v93, v92\n\t" \
              "v_max_f32 v93, v93, v92\n\t" \
              "v_sub_f32 v94, v94, v93\n\t" "v_sub_f32 v112, v112, v93\n\t" "v_exp_f32 v92, v94\n\t" "v_sub_f32 v113, v113, v93\n\t" "v_sub_f32 v114, v114, v93\n\t" "v_sub_f32 v115, v115, v93\n\t" \
              "s_branch .Lex%=\n"
#define PS1(r) "v_add_f32 " REG(r) ", " REG(r) ", " REG((r) + 2) "\n\t"
#define NOP(n) "s_nop " STR(n) "\n\t"
#define PCA 13
#define PDA 8
#define PDC 8
#define PDF 7
#define SLV 0
#define QKRED(X) RDQ(80, 12288) RDQ(84, 12304) X RDQ(88, 0) RDQ(92, 16) RDQ(112, 4096) RDQ(116, 4112) RDQ(120, 8192) RDQ(124, 8208) \
        WLG(4) TSA(4) PKA(80, 80, 88) PKA(82, 82, 90) PKA(84, 84, 92) PKA(86, 86, 94) \
        WLG(2) PKA(80, 80, 112) PKA(82, 82, 114) PKA(84, 84, 116) PKA(86, 86, 118) \
        WLG(0) PKA(80, 80, 120) PKA(82, 82, 122) PKA(84, 84, 124) PKA(86, 86, 126) \
        "v_cvt_pk_bf16_f32 v72, v80, v81\n\t" "v_cvt_pk_bf16_f32 v73, v82, v83\n\t" "v_cvt_pk_bf16_f32 v74, v84, v85\n\t" "v_cvt_pk_bf16_f32 v75, v86, v87\n\t" \
        NOP(1) TSA(5) \
        "v_mfma_f32_16x16x32_bf16 v[88:91], v[72:75], v[72:75], 0\n\t" NOP(2) \
        "v_mfma_f32_16x16x32_bf16 v[80:83], a[208:211], v[72:75], 0\n\t" NOP(2) \
        "v_mfma_f32_16x16x32_bf16 v[84:87], a[212:215], v[72:75], 0\n\t"
#define QKPUB "buffer_store_dwordx4 v[88:91], v41, %[rs], s71 offen sc1\n\t" NOP(0) \
        "buffer_store_dwordx4 v[80:83], v40, %[rs], s71 offen sc1\n\t" NOP(2) \
        "buffer_store_dwordx4 v[84:87], v40, %[rs], s71 offen offset:512 sc1\n\t"
#define MRED(W, X) "ds_read_b128 v[80:83], v46 offset:12288\n\t" W "ds_read_b128 v[84:87], v46\n\t" "ds_read_b128 v[88:91], v46 offset:4096\n\t" "ds_read_b128 v[92:95], v46 offset:8192\n\t" X TSW(4) \
        WLG(2) TSW(5) PKA(80, 80, 84) PKA(82, 82, 86) WLG(0) PKA(88, 88, 92) PKA(90, 90, 94) PKA(84, 80, 88) PKA(86, 82, 90) \
        "v_cvt_pk_bf16_f32 v80, v84, v85\n\t" "v_cvt_pk_bf16_f32 v81, v86, v87\n\t"
#define ORD(a, b) "v_cmpx_o_f32 s[50:51], " REG(a) ", " REG(b) "\n\t"
#define REDO(L) "s_cmp_lg_u64 exec, -1\n\t" "s_mov_b64 exec, -1\n\t" "s_cbranch_scc1 " L "\n" L "r:\n\t"
#define CWAIT(L, o, S) L ":\n\t" "ds_read_b32 v62, v38 offset:" CNT(o) "\n\t" WLG(0) "v_readfirstlane_b32 s49, v62\n\t" "s_cmp_lt_u32 s49, " S "\n\t" "s_cbranch_scc1 " L "\n\t"
#define VPOST PSA(24) PSA(28) PSW(24, 2560) PSW(25, 2832) PSW(28, 3200) PSW(29, 3472)
#define BARA "s_barrier\n\t"
#define BARC "s_barrier\n\t"
#define PZADR(SB, M) "s_add_u32 s49, s45, 1\n\t" "s_xor_b32 s62, s71, " STR(XR2S) "\n\t" "v_readlane_b32 s63, v19, s49\n\t" "s_mov_b32 s49, " SB "\n\t" \
        ".if !((" STR(M) ") & 1)\n\t" "s_mov_b32 s63, 0x7ff00000\n\t" ".endif\n\t" ".if !((" STR(M) ") & 2)\n\t" "s_mov_b32 s49, 0x7ff00000\n\t" ".endif\n\t" \
        ".if !((" STR(M) ") & 4)\n\t" "s_mov_b32 s62, 0x7ff00000\n\t" ".endif\n\t" "s_cmp_eq_u32 %[wv], 0\n\t" \
        "s_cselect_b32 s49, s49, s62\n\t" "s_cselect_b32 s63, s63, s62\n\t" "v_add_u32 v48, s49, v23\n\t" "s_mov_b32 exec_hi, 0\n\t" "v_add_u32 v48, s63, v23\n\t" "s_mov_b32 exec_hi, -1\n\t"
#define PZST "buffer_store_dwordx4 v[92:95], v48, %[rs], 0 offen sc1\n\t"
#define MA 0
#define MC 0
#define MB (7 - MA - MC)
#define PZD 1
#define PZB0 PZADR("s63", MB)
#define PZB PZST
#define PZC0
#define PZC ""
#define PZNC 0
#define PZA0
#define PZA
#define PZNA 0
#define LASTC(L) RELV(L)
#define LASTA(L) ""
#define PCL PCA
#define ABODY(W, X, F, n) TSA(0) GLD(96, 0) NOP(PCA) GLD(100, 1024) NOP(PCA) GLD(104, 2048) W GLD(108, 3072) "s_mov_b32 s47, 0\n\t" X \
        WVM((n) + 3) TSA(1) SQ(0, 0) SQ(1, 0) SQ(2, 0) SQ(3, 0) \
        WVM((n) + 2) SQ(0, 1) SQ(1, 1) SQ(2, 1) SQ(3, 1) \
        WVM((n) + 1) SQ(0, 2) MXA0(96) MXB(96) SQ(1, 2) MXA(100) MXB(100) SQ(2, 2) MXA(104) MXB(104) SQ(3, 2) \
        WVM(n) TSA(2) SQ(0, 3) MXA(108) MXB(108) CMPB SQ(1, 3) "s_add_u32 s48, s38, 4\n\t" NOP(0) NOP(0) SQ(2, 3) "s_cbranch_vccnz " F "\n\t" PS1(64) PS1(65) SQ(3, 3) PS1(68) PS1(69) \
        NOP(0) PSW(64, 0) PSW(65, 272) PSW(68, 640) PSW(69, 912) PSA(72) PSA(76) PSW(72, 1280) PSW(73, 1552) PSW(76, 1920) PSW(77, 2192)
#define CISSUE(X, Y) TSW(0) GL1(96, 0) PC GL1(100, 1024) PC GL1(104, 2048) PC GL1(108, 3072) PC GL2(112, 0) PC GL2(116, 1024) \
        "s_mov_b32 s48, s36\n\t" "s_mov_b32 s36, s37\n\t" "s_add_u32 s45, s45, 1\n\t" "s_add_u32 s38, s38, 4\n\t" "v_readlane_b32 s37, v19, s45\n\t" \
        "s_add_u32 s39, s39, " STR(EXF) "\n\t" "s_add_u32 s40, s40, " STR(EXF) "\n\t" \
        "s_add_u32 s41, s41, " STR(EXK) "\n\t" "s_add_u32 s42, s42, " STR(EXK) "\n\t" "s_add_u32 s43, s43, s55\n\t" PC6 \
        GL2(120, 2048) "s_mov_b32 s47, 2\n\t" "s_mul_i32 s70, s69, 3\n\t" R2BASE "s_add_u32 s43, s43, s70\n\t" NOP(2) PC7 X \
        GL2(124, 3072) Y
#define ZALL ZER(64) ZER(68) ZER(72) ZER(76) ZER(80) ZER(84) ZER(24) ZER(28)
#define SHRD "ds_read_b32 v62, v38\n\t"
#define SHUP WLG(0) "v_readfirstlane_b32 s49, v62\n\t" "s_max_u32 s44, s44, s49\n\t"
#define DPA(d, r, m) "v_add_f32_dpp " REG(d) ", " REG(r) ", " REG((r) + 2) " quad_perm:[0,1,2,3] row_mask:" m " bank_mask:0xf\n\t"
#define CBODY(m, X, n, F) CBODY_(80, m, X, n, F)
#define CBODY_(a, m, X, n, F) \
        WVM((m) + 7) TSW(1) WBLK(a, m, 0, MXA0) WBLK(a, m, 1, MXA) X WBLK(a, n, 2, MXA) WBLK(a, n, 3, MXA) WBLK(a, n, 4, MXA) WBLK(a, n, 5, MXA) WBLK(a, n, 6, MXA) \
        WVM(n) TSW(2) SW(a, 0, 7) MXA(124) MXB(124) CMPB SW(a, 1, 7) NOP(0) NOP(1) "s_cbranch_vccnz " F "\n\t" \
        DPA(88, a, "0x3") DPA(89, (a) + 1, "0x3") NOP(1) DPA(88, (a) + 4, "0xc") DPA(89, (a) + 5, "0xc") NOP(1) \
        "v_permlane32_swap_b32 v88, v89\n\t" "v_add_f32 v88, v88, v89\n\t" TSW(3) PSW(88, 3840)
#define FINEO
#define NEXTP "s_branch .LA%=\n"
#define NEXTN "s_branch .LNA%=\n"

__device__ __forceinline__ void clk_edge(u32& cyc, u32& tick) {
  const u32 t = rt32();
  for (int i = 0; i < 64; ++i) { tick = rt32(); if (tick != t) break; }
  cyc = (u32)__builtin_amdgcn_s_memtime();
}
__device__ __forceinline__ float clk_rate(u32 cyc0, u32 tk0) { u32 c, t; clk_edge(c, t); return (float)(c - cyc0) / (float)max(t - tk0, 1u); }
__device__ __forceinline__ void dma16(__amdgpu_buffer_rsrc_t rs, int voff, int soff, int sstep, int ldsa) {
#pragma unroll
  for (int i = 0; i < 16; ++i)
    asm volatile("s_mov_b32 m0, %0\n\ts_nop 3\n\tbuffer_load_dwordx4 %1, %2, %3 offen lds" :: "s"(ldsa + i * 1024), "v"(voff), "s"(rs), "s"(soff ^ (i * sstep)) : "memory");
}

#define KH(t) (192 + 8 * (t))
#define KMF(t, a, b) "v_mfma_f32_16x16x32_bf16 " RNG("v", 80 + 4 * (t), 4) ", " RNG("v", KH(t) + 4 * (a), 4) ", " RNG("v", KH(3) + 4 * (b), 4) ", " RNG("v", 80 + 4 * (t), 4) "\n\t"
#define KX(j) (92 + 4 * ((j) & 1))
#define KA(t, j) "v_pk_mul_f32 " RNG("v", KX(j), 2) ", " RNG("v", 64 + 4 * (j), 2) ", " RNG("v", 112 + ((t) & 2), 2) " op_sel:[0," STR((t) & 1) "] op_sel_hi:[1," STR((t) & 1) "]\n\t"
#define KF(j) "v_fract_f32 " REG(KX(j) + 1) ", " REG(KX(j) + 1) "\n\t"
#define KE(j, N) "v_exp_f32 " REG(KX(j)) ", " REG(KX(j)) "\n\t" "v_cos_f32 " REG(KX(j) + 2) ", " REG(KX(j) + 1) "\n\t" "v_sin_f32 " REG(KX(j) + 3) ", " N REG(KX(j) + 1) "\n\t"
#define KM(j) "v_pk_mul_f32 " RNG("v", KX(j) + 2, 2) ", " RNG("v", KX(j) + 2, 2) ", " RNG("v", KX(j), 2) " op_sel_hi:[1,0]\n\t"
#define KC(j) "v_pk_mul_f32 " RNG("v", KX(j), 2) ", " RNG("v", KX(j) + 2, 2) ", " RNG("v", 66 + 4 * (j), 2) " op_sel_hi:[1,0]\n\t" \
  "v_pk_fma_f32 " RNG("v", KX(j), 2) ", " RNG("v", KX(j) + 2, 2) ", " RNG("v", 66 + 4 * (j), 2) ", " RNG("v", KX(j), 2) " op_sel:[1,1,0] op_sel_hi:[0,1,1] neg_hi:[1,0,0]\n\t"
#define KD(t, j, r) "v_cvt_pk_bf16_f32 " REG(KH(t) + (j)) ", " REG(r) ", " REG((r) + 1) "\n\t" \
  "v_dot2c_f32_bf16 " REG(r) ", s47, " REG(KH(t) + (j)) "\n\t" "v_dot2c_f32_bf16 " REG((r) + 1) ", s48, " REG(KH(t) + (j)) "\n\t"
#define KL(t, j, r) "v_cvt_pk_bf16_f32 " REG(KH(t) + 4 + (j)) ", " REG(r) ", " REG((r) + 1) "\n\t"
#define KDM "s_add_u32 m0, m0, 1024\n\t" "s_add_u32 s43, s43, s42\n\t" "buffer_load_dwordx4 v110, s[36:39], s43 offen lds\n\t"
#define KP(j) "ds_read_b128 " RNG("v", 64 + 4 * (j), 4) ", v111 offset:" STR(16 * (j)) "\n\t"
#define KV(j, L, A) KE(j, "-") L KM(j) KC(j) A KD(3, j, KX(j))
#define KU(t, j, L, A) KE(j, "") L KM(j) A KD(t, j, KX(j) + 2)
#define KSUB                                                                                                        \
  KV(0, WLG(8) "s_add_u32 m0, m0, 1024\n\t", KA(3, 1)) KV(1, KL(3, 0, KX(0)), KA(3, 2))                              \
  "s_xor_b32 s43, s40, s41\n\t" "buffer_load_dwordx4 v110, s[36:39], s43 offen lds\n\t" KV(2, KL(3, 1, KX(1)), KA(3, 3)) \
  KV(3, KL(3, 2, KX(2)), KA(0, 0))                                                                                  \
  KU(0, 0, KL(3, 3, KX(3)), KA(0, 1)) KU(0, 1, KL(0, 0, KX(0) + 2), KA(0, 2)) KDM KU(0, 2, KL(0, 1, KX(1) + 2), KA(0, 3)) \
  KU(0, 3, KL(0, 2, KX(2) + 2), KA(1, 0)) KF(0)                                                                     \
  KU(1, 0, KL(0, 3, KX(3) + 2), KA(1, 1)) KF(1) KMF(0, 1, 0) KU(1, 1, KL(1, 0, KX(0) + 2), KA(1, 2)) KF(2) KDM      \
  KMF(0, 0, 1) KU(1, 2, KL(1, 1, KX(1) + 2), KA(1, 3)) KF(3) KMF(0, 0, 0) KU(1, 3, KL(1, 2, KX(2) + 2), KA(2, 0)) KF(0) KP(0) \
  KU(2, 0, KL(1, 3, KX(3) + 2), KA(2, 1)) KF(1) KP(1) KMF(1, 1, 0) KU(2, 1, KL(2, 0, KX(0) + 2), KA(2, 2)) KF(2) KP(2) KDM \
  KMF(1, 0, 1) KU(2, 2, KL(2, 1, KX(1) + 2), KA(2, 3)) KF(3) KP(3) KMF(1, 0, 0) KU(2, 3, KL(2, 2, KX(2) + 2), WLG(3) KA(3, 0)) \
  KMF(2, 0, 1) "v_add_u32 v111, 256, v111\n\t" "s_lshl2_add_u32 s41, s42, s41\n\t" KL(2, 3, KX(3) + 2)
#define KZERO ZER(80) ZER(84) ZER(88)
#define KCHUNK(PTR, VO, SO, ST, BUF, PF) "s_mov_b64 s[36:37], " PTR "\n\t" "s_mov_b32 m0, %[sc]\n\t" "v_mov_b32 v110, " VO "\n\t" SO "s_mov_b32 s41, 0\n\t" "s_mov_b32 s42, " STR(ST) "\n\t" \
  "buffer_load_dword " PF ", s[36:39], 0 offen lds\n\t" "s_add_u32 m0, %[sl], " STR((BUF) * 16384 - 1024) "\n\t"
#define KCALL "s_call_b64 s[44:45], .Lkg%=\n\t" WVM(16) KMF(2, 0, 0)
#define KTR(F, d, A, o) "ds_read_b64_tr_b16 " RNG(F, d, 2) ", " A " offset:" STR(o) "\n\t"
#define KTQ1(d, o) KTR("a", d, "%[aq]", o) KTR("a", (d) + 2, "%[aq]", (o) + 128) KTR("a", (d) + 4, "%[aq]", (o) + 2048) KTR("a", (d) + 6, "%[aq]", (o) + 2176)
#define KTQ(d, o) KCALL KTQ1(d, o) KMF(2, 1, 0) KTQ1((d) + 8, (o) + 32)
#define KTO1(d, o) KTR("v", d, "%[ao]", o) KTR("v", (d) + 2, "%[ao]", (o) + 64) KTR("v", (d) + 4, "%[ao]", (o) + 1024) KTR("v", (d) + 6, "%[ao]", (o) + 1088)
#define KTO(d, o) KCALL KTO1(d, o) KMF(2, 1, 0) KTO1((d) + 8, (o) + 2048)
#define KOUT(o) "s_add_u32 s43, %[ko], " STR(o) "\n\t"                                                              \
  "buffer_store_dwordx4 v[80:83], %[l16], %[rt], s43 offen sc1\n\t" "buffer_store_dwordx4 v[84:87], %[l16], %[rt], s43 offen offset:1024 sc1\n\t" "s_nop 3\n\t" \
  "buffer_store_dwordx4 v[88:91], %[l16], %[rt], s43 offen offset:2048 sc1\n\t"
struct SsmPtrs { const u16 *A_re, *A_im, *B_re, *B_im, *C_re, *C_im, *log_dt; };
__device__ __forceinline__ void r_role(unsigned char* lds, int b, const u16* __restrict__ mem0, const u16* __restrict__ Wq, const u16* __restrict__ Wk, const u16* __restrict__ Wv,
                                       const u16* __restrict__ Wo, int fb, u32 tag, u64 pc0, int clen, __amdgpu_buffer_rsrc_t rsx, __amdgpu_buffer_rsrc_t rsf,
                                       __amdgpu_buffer_rsrc_t rst, SsmPtrs sp, int tid, int lane, int w
                                       ) {
  const int lr = lane & 15, lg = lane >> 4;
  const int ldsb = (int)(size_t)(LAS unsigned char*)lds;
  const int idx = (lr & 2) ? 0xEEEE : 0x4444;
  u32 tcnt = 0; (void)tcnt;
  u32 cyc0 = 0, tk0 = 0;
  if (tid < 8) *(u32*)(lds + LR_CTL + tid * 4) = 0u;
  *(u32*)(lds + LR_CTL + 4096 + 256 + tid * 4) = 0u;
  const int xcd = b & 3, li = b >> 2;
  const int head = xcd * 8 + (li >> 2), ks = li & 3;
  const int vcu = head * 4 + ks;
  const int cg = (xcd >> 1) * 32 + li, rg = xcd & 1;
  const int rot1 = ((li >> 1) + w) & 3, rot3 = ((li >> 2) + 2 * w) & 7;
  const int pz1 = lane < 32 ? XR1A(lane >> 3, cg * 256 + rg * 128 + (lane & 7) * 16) : XR3A(lg & 1, vcu * 256 + lr * 16);
  const int pgo = PGO_X + head * 128;
  const int calo = XCAL + (li & 3) * 8192 + ((li >> 2) & 1) * 4096 + ((li >> 3) * 4 + xcd) * 128;
  u32 xp0;
  u32x32 wa[6], wo[4];
  {
    const __amdgpu_buffer_rsrc_t rwq = __builtin_amdgcn_make_buffer_rsrc((void*)Wq, 0, DM * HD * 2, 0x00020000);
    const int vq = (lane >> 2) * (HD * 2) + vcu * 64 + (lane & 3) * 16, sq = (256 * w + (rot1 << 6)) * (HD * 2);
    const int vo = (lane >> 1) * (DM * 2) + cg * 32 + (lane & 1) * 16, so = (1024 * w + (rot3 << 7)) * (DM * 2);
    const int sl = ldsb + LR_STG + w * 32768;
    const int ch0 = (b * 4 + w) * 2;
    u32x2 pin[6];
    u32 ldt;
    {
      const int po = (ch0 * SSM_N + 4 * lane) * 2;
      asm volatile("global_load_dwordx2 %0, %7, %9\n\tglobal_load_dwordx2 %1, %7, %10\n\tglobal_load_dwordx2 %2, %7, %11\n\tglobal_load_dwordx2 %3, %7, %12\n\t"
                   "global_load_dwordx2 %4, %7, %13\n\tglobal_load_dwordx2 %5, %7, %14\n\tglobal_load_ushort %6, %8, %15"
                   : "=&v"(pin[0]), "=&v"(pin[1]), "=&v"(pin[2]), "=&v"(pin[3]), "=&v"(pin[4]), "=&v"(pin[5]), "=&v"(ldt)
                   : "v"(po), "v"((ch0 + (lane >> 5)) * 2), "s"(sp.A_re), "s"(sp.A_im), "s"(sp.B_re), "s"(sp.B_im), "s"(sp.C_re), "s"(sp.C_im), "s"(sp.log_dt) : "memory");
    }
    u32x4 m0v;
    asm volatile("global_load_dwordx4 %0, %1, %2" : "=&v"(m0v) : "v"(((4 * rg + ((lane >> 1) & 3)) * DM + 16 * cg + 8 * (lane & 1)) * 2), "s"(mem0) : "memory");
    const u16* const wp[4] = {Wq, Wk, Wv, Wo};
    int pfo[5];
#pragma unroll
    for (int c = 0; c < 5; ++c) {
      const int lo_ = (int)(size_t)wp[c < 3 ? c : 3], s3 = (384 - lo_) & 1023, s63 = (8064 - lo_) & 8191;
      pfo[c] = c < 3 ? (lane < 16 ? (256 * w + li * 8 + (lane & 7)) * (HD * 2) + xcd * 2048 + (lane >> 3) * 1024 + s3
                                  : (lane < 24 && (s63 >> 11) == xcd ? (256 * w + li * 8 + (lane & 7)) * (HD * 2) + s63 : OOR))
                     : (lane < 16 ? (1024 * w + li * 16 + lane + (((c - 3) * 512) ^ ((((s3 >> 7) + 2 * w) & 4) << 7))) * (DM * 2) + (xcd >> 1) * 1024 + s3 : OOR);
    }
    const int scr = ldsb + LR_RED + 1024 + w * 256;
    asm volatile("s_mov_b32 m0, %0\n\ts_nop 3\n\tbuffer_load_dword %1, %2, 0 offen lds" :: "s"(scr), "v"(pfo[0]), "s"(rwq) : "memory");
#define KP0 16
#pragma unroll
    for (int i = 0; i < KP0; ++i)
      asm volatile("s_mov_b32 m0, %0\n\ts_nop 3\n\tbuffer_load_dwordx4 %1, %2, %3 offen lds" :: "s"(sl + i * 1024), "v"(vq), "s"(rwq), "s"(sq ^ (i * 16 * HD * 2)) : "memory");
    asm volatile("s_waitcnt vmcnt(1+" STR(KP0) ")" : "+v"(pin[0]), "+v"(pin[1]), "+v"(pin[2]), "+v"(pin[3]), "+v"(pin[4]), "+v"(pin[5]), "+v"(ldt), "+v"(m0v)::"memory");
    f32x4* prm = (f32x4*)(lds + w * 4096);
    {
      const float dt = rbf(expf(bf2f((u16)ldt)));
#pragma unroll
      for (int i = 0; i < 4; ++i) {
        float v[6];
#pragma unroll
        for (int j = 0; j < 6; ++j) v[j] = __uint_as_float((i & 1) ? (pin[j][i >> 1] & 0xffff0000u) : (pin[j][i >> 1] << 16));
        const float are = dt * v[0], aim = dt * v[1], br = dt * v[2], bi = dt * v[3];
        const float cr = v[4] * br - v[5] * bi, ci = v[4] * bi + v[5] * br;
        const float rev = aim * 0.15915494309189535f;
        prm[4 * lane + i] = (f32x4){are * 1.4426950408889634f, rev - __builtin_rintf(rev), cr, ci};
      }
    }
#pragma unroll
    for (int i = KP0; i < 16; ++i)
      asm volatile("s_mov_b32 m0, %0\n\ts_nop 3\n\tbuffer_load_dwordx4 %1, %2, %3 offen lds" :: "s"(sl + i * 1024), "v"(vq), "s"(rwq), "s"(sq ^ (i * 16 * HD * 2)) : "memory");
    const f32x4 lv = {(float)(128 * (lr >> 3) + (lr & 7)), (float)(lr < 8 ? 256 + 128 * (lr >> 2) + 2 * (lr & 3) : 512 + 128 * ((lr - 8) >> 1) + 4 * (lr & 1)),
                         (float)(lr <= 8 ? 1024 + 128 * lr : 0), (float)(8 * lr)};
    const int trow = 16 * (lg >> 1) + 4 * (lg & 1) + ((lr >> 2) & 1) + 8 * (lr >> 3);
    asm volatile(
        "s_nop 4\n\t" "s_mov_b32 s46, m0\n\t" "s_mov_b32 s38, " STR(DM * HD * 2) "\n\t" "s_mov_b32 s39, 0x20000\n\t" "s_mov_b32 s47, 0xbf80\n\t" "s_mov_b32 s48, 0xbf800000\n\t"
        WLG(0)
        "ds_read_b128 v[64:67], %[pa]\n\t" "ds_read_b128 v[68:71], %[pa] offset:16\n\t" "ds_read_b128 v[72:75], %[pa] offset:32\n\t" "ds_read_b128 v[76:79], %[pa] offset:48\n\t"
        "v_add_u32 v111, 256, %[pa]\n\t" KZERO
        KCHUNK("%[wk]", "%[vq]", "s_mov_b32 s40, %[sq]\n\t", 16 * HD * 2, 1, "%[p1]")
        WLG(0) KA(3, 0)
        KTQ(0, 0) KTQ(32, 4096) KTQ(64, 8192) KTQ(96, 12288)
        KCHUNK("%[wv]", "%[vq]", "", 16 * HD * 2, 0, "%[p2]")
        KTQ(16, 16384) KTQ(48, 16384 + 4096) KTQ(80, 16384 + 8192) KTQ(112, 16384 + 12288)
        KOUT(0)
        KCHUNK("%[wo]", "%[vo]", "s_mov_b32 s40, %[so]\n\t", 32 * DM * 2, 1, "%[p3]")
        KZERO
        KTQ(128, 0) KTQ(144, 4096) KTQ(160, 8192) KTQ(176, 12288)
        KCHUNK("%[wo]", "%[vo]", "s_xor_b32 s40, s40, " STR(512 * DM * 2) "\n\t", 32 * DM * 2, 0, "%[p4]")
        KTO(128, 16384) KTO(144, 16384 + 4096) KTO(160, 16384 + 8192) KTO(176, 16384 + 12288)
        KOUT(3072)
        "s_branch .Lke%=\n"
        ".Lkg%=:\n\t"
        KSUB
        "s_setpc_b64 s[44:45]\n"
        ".Lke%=:\n\t"
        WLG(0) "s_mov_b32 m0, s46"
        : "=&{a[0:31]}"(wa[0]), "=&{a[32:63]}"(wa[1]), "=&{a[64:95]}"(wa[2]), "=&{a[96:127]}"(wa[3]), "=&{a[128:159]}"(wa[4]), "=&{a[160:191]}"(wa[5]),
          "=&{v[128:159]}"(wo[0]), "=&{v[160:191]}"(wo[1])
        : "{v[112:115]}"(lv), [pa] "v"(ldsb + w * 4096 + lg * 64), [vq] "v"(vq), [vo] "v"(vo),
          [p1] "v"(pfo[1]), [p2] "v"(pfo[2]), [p3] "v"(pfo[3]), [p4] "v"(pfo[4]), [aq] "v"(sl + trow * 64 + (lr & 3) * 8), [ao] "v"(sl + trow * 32 + (lr & 3) * 8), [l16] "v"(lane * 16),
          [wk] "s"(Wk), [wv] "s"(Wv), [wo] "s"(Wo), [sq] "s"(sq), [so] "s"(so), [sl] "s"(sl), [sc] "s"(scr), [rt] "s"(rst), [ko] "s"(2 * SSM_L * SSM_D * 2 + ch0 * 3072)
        : CL64, CL72, CL80, CL88, CL96, CL8(192, 193, 194, 195, 196, 197, 198, 199), CL8(200, 201, 202, 203, 204, 205, 206, 207), CL8(208, 209, 210, 211, 212, 213, 214, 215),
          CL8(216, 217, 218, 219, 220, 221, 222, 223), "s36", "s37", "s38", "s39", "s40", "s41", "s42", "s43", "s44", "s45", "s46", "s47", "s48", "scc", "memory");
    {
      const u32x4 pz = (u32x4){SENT, SENT, SENT, SENT};
      if (w < 3) {
        u32x4 v = pz;
        if (w == 0 && lane < 32) v = (u32x4){nosent(m0v[0]), nosent(m0v[1]), nosent(m0v[2]), nosent(m0v[3])};
#pragma unroll
        for (int hf = 0; hf < 2; ++hf) {
          __builtin_amdgcn_raw_buffer_store_b128(v, rsx, pz1, XSB(w) + hf * 8192, 16);
          __builtin_amdgcn_raw_buffer_store_b128(pz, rsx, XR2(vcu) + lane * 16, XR2B + (w & 1) * XR2S + hf * 8192, 16);
          __builtin_amdgcn_raw_buffer_store_b128(pz, rsx, lane < 16 ? XR2(vcu) + 1024 + lane * 16 : OOR, XR2B + (w & 1) * XR2S + hf * 8192, 16);
        }
      } else {
        __builtin_amdgcn_raw_buffer_store_b128(pz, rsx, lane < 8 ? lane * 16 : OOR, calo, 16);
        if (b < 32) {
          __builtin_amdgcn_raw_buffer_store_b128(pz, rsx, lane * 16, XSYN + b * 2048, 16);
          __builtin_amdgcn_raw_buffer_store_b128(pz, rsx, lane * 16, XSYN + b * 2048 + 1024, 16);
        } else if (b >= 48 && b < 48 + 41) {
          __builtin_amdgcn_raw_buffer_store_b128(pz, rsx, lane * 16, PGO_D + (b - 48) * 1024, 16);
        }
      }
      asm volatile("" ::: "memory");
    }
    xp0 = __builtin_amdgcn_raw_buffer_load_b32(rsx, (lane & 1) * 4096, pgo, 16);
    clk_edge(cyc0, tk0);
    asm volatile("s_waitcnt vmcnt(0)\n\ts_barrier" : "+v"(xp0)::"memory");
#define CLKF() clk_rate(cyc0, tk0)
    if (w == 0) {
      const int l0 = lane ? OOR : 0;
      const u64 c0 = __builtin_amdgcn_s_memtime();
      u32 v;
      asm volatile("s_waitcnt lgkmcnt(0)\n\tbuffer_load_dword %0, off, %1, %2 sc1\n\tbuffer_store_dword %3, %4, %1, %5 offen sc1\n\ts_waitcnt vmcnt(1)"
                   : "=&v"(v) : "s"(rsx), "s"(calo), "v"(tag), "v"(l0), "s"(PGO_Y + b * 128), "s"(c0) : "memory");
      const u32 lat = (u32)(__builtin_amdgcn_s_memtime() - c0);
      const float f = CLKF();
      typedef __attribute__((ext_vector_type(3))) unsigned int u32x3;
      __builtin_amdgcn_raw_buffer_store_b96((u32x3){__float_as_uint(f), lat, tag}, rsx, l0, PGO_Y + b * 128 + 4, 16);
      asm volatile("" ::: "memory");
    }
    {
      u32 r[1][8][8];
      tr_chunk<1, 8>((const u16*)(lds + LR_STG + w * 32768), lr, lg, r);
#pragma unroll
      for (int i = 0; i < 8; ++i)
#pragma unroll
        for (int e = 0; e < 8; ++e) { const int o = (8 + i) * 8 + e; wo[o >> 5][o & 31] = r[0][i][e]; }
      asm volatile("s_waitcnt lgkmcnt(0)" ::: "memory");
    }
  }
  const int hf = (lr >> 1) & 1;
  const int voff1 = lg * 256 + (lr >> 3) * 128 + (((lr >> 2) & 1) * 2 + (lr & 1)) * 32 + hf * 16;
  const int voff3 = (lr >> 3) * 512 + (lg >> 1) * 256 + (((lr >> 2) & 1) * 2 + (lr & 1)) * 16 + (lg & 1) * 128 + hf * 64;
  const int ard = ldsb + (2 * (lr >> 3) + (lg >> 1)) * 640 + (lr & 1) * 272 + ((lr >> 1) & 3) * 64 + (lg & 1) * 32;
  const int ctl = ldsb + LR_CTL, one = w == 0 ? 2 : (w == 3 ? 4 : 1);
  const int r8 = lane & 7, hk = (lane >> 3) & 1;
#define COA 1
#define COB 1
  const int st01 = lr < 8 ? XR2(vcu) + lr * 16 + lg * 128 : OOR, st2 = (lr < 8 && lg >= 2) ? XR2(vcu) + 1024 + lr * 16 + (lg - 2) * 128 : OOR;
  const int ld01 = hk * 512 + r8 * 16 + lg * 128, ld2 = XR2(head * 4 + lg) + 1024 + r8 * 16 + hk * 128;
  int sto = XR3A(r8 >> 2, vcu * 256 + (r8 & 3) * 16 + lg * 64 + hk * 8);
  const int stk = lr >= 8 ? (lg * 8 + (lr - 8)) * 16 : OOR;
  const int lo = X4C ? 2 * (lr & 7) + (lr >> 3) : lr;
  const int arw = ldsb + LR_CTL + lane * 4, arr = ldsb + 3840 + ((lo >> 3) + 2 * ((lo >> 2) & 1)) * 64 + (lo & 3) * 16;
  const int st1 = XR1A(lg, cg * 256 + rg * 128 + lo * 8);
  const int avw = 0;
  const int avc = ldsb + 2560 + (lr >> 3) * 640 + (8 * ((lr >> 2) & 1) + (lr & 3)) * 4;
  const int avr = avc + (lg & 1) * 272 + (lg >> 1) * 64, avf = avc + (lg & 1) * 128 + (lg >> 1) * 16;
  u32x16 kc = {(u32)voff1, (u32)voff3, (u32)idx, (u32)TEX(lane), (u32)(lane < 16 ? XR2(vcu) + 1024 + lane * 16 : OOR), (u32)ard, (u32)ctl, (u32)one, (u32)st01, (u32)st2, (u32)(XR2(vcu) + lane * 16), (u32)ld2, (u32)(lane * 16), (u32)arw, (u32)arr, (u32)stk};
  int o1c = XR1A(xcd, w * 4096) + rot1 * 1024, o3c = XR3A(rg, w * 8192) + rot3 * 1024;
  const int fok = FO_K + (head * 8 + ks) * 1024, fov = FO_V + (head * 8 + 2 * ks) * 1024;
#define XPROG(v) min(xprog(__builtin_amdgcn_readlane(v, 0), tag), xprog(__builtin_amdgcn_readlane(v, 1), tag))
  int xp = w == 3 ? XPROG(xp0) : 1000;
#define XWAIT(n) for (int it_ = 0; it_ < SPIN_CAP && xp < (n); ++it_) {                                               \
    if (it_) asm volatile("s_sleep 4" ::: "memory");                                                                  \
    const u32 v_ = __builtin_amdgcn_raw_buffer_load_b32(rsx, (lane & 1) * 4096, pgo, 16); xp = XPROG(v_); asm volatile("" ::: "memory"); }
  XWAIT(1)
  u32x16 fv;
  u32x8 fk;
  {
    const int lo_ = w == 3 ? lane * 16 : OOR;
    const u32x4 v0 = __builtin_amdgcn_raw_buffer_load_b128(rsf, lo_, fov, 16), v1 = __builtin_amdgcn_raw_buffer_load_b128(rsf, lo_, fov + 1024, 16);
    const u32x4 k0 = __builtin_amdgcn_raw_buffer_load_b128(rsf, lo_, fok, 16), k1 = __builtin_amdgcn_raw_buffer_load_b128(rsf, lo_, fok + 4096, 16);
    fv = (u32x16){v0[0], v0[1], 0u, 0u, v0[2], v0[3], 0u, 0u, v1[0], v1[1], 0u, 0u, v1[2], v1[3], 0u, 0u};
    fk = (u32x8){k0[0], k0[1], k0[2], k0[3], k1[0], k1[1], k1[2], k1[3]};
    asm volatile("" : "+{a[192:207]}"(fv), "+{a[208:215]}"(fk));
  }
#define XPN 1
#define CALM 40
  XWAIT(2)
#pragma unroll
  for (int i = 0; i < 3; ++i) *(u32x4*)(lds + w * 4096 + i * 1024 + lane * 16) = (u32x4){SENT, SENT, SENT, SENT};
  {
    u64 pc; asm volatile("s_getpc_b64 %0" : "=s"(pc));
    const u32 cv = __builtin_amdgcn_raw_buffer_load_b32(__builtin_amdgcn_make_buffer_rsrc((void*)pc0, 0, clen, 0x00020000), (int)(pc - pc0) + tid * 64, 0, 0);
    asm volatile("" :: "v"(cv));
  }
  if (w == 3) { const u32 v_ = __builtin_amdgcn_raw_buffer_load_b32(rsx, (lane & 1) * 4096, pgo, 16); xp = max(xp, XPROG(v_)); }
  u32x4 st;
  if (w == 1) {
    if (b == 0) {
      u32 tt = 0, best = 0;
      bool xok = false;
      const int sto_ = lane < 5 ? (lane ? XSTR(lane - 1) : XSTA) : OOR;
      for (int it = 0; it < SPIN_CAP; ++it) {
        asm volatile("" ::: "memory");
        const u32x4 r0 = __builtin_amdgcn_raw_buffer_load_b128(rsx, lane * 128, PGO_Y, 16), r1 = __builtin_amdgcn_raw_buffer_load_b128(rsx, lane * 128 + 8192, PGO_Y, 16);
        if (!xok) xok = __ballot(xprog(__builtin_amdgcn_raw_buffer_load_b32(rsx, lane * 128, PGO_X, 16), tag) < XPN) == 0;
        const bool c0 = r0[3] == tag, c1 = r1[3] == tag;
        if (tt == 0 && __popcll(__ballot(!c0 || !c1)) <= CALM) {
          float fm = (c0 ? __uint_as_float(r0[1]) : 0.f) + (c1 ? __uint_as_float(r1[1]) : 0.f), fn = (c0 ? 1.f : 0.f) + (c1 ? 1.f : 0.f);
#pragma unroll
          for (int o = 4; o < 64; o *= 2) { fm += __shfl_xor(fm, o); fn += __shfl_xor(fn, o); }
          fm = fn > 0.f ? fm / fn : 100.f;
          const float f0 = __uint_as_float(__builtin_amdgcn_readfirstlane(__float_as_uint(fm)));
          fm = fminf(fm, __shfl_xor(fm, 1)); fm = fminf(fm, __shfl_xor(fm, 2));
          const float f_ = CLKF() * fminf(fmaxf(fm / f0, 0.8f), 1.f);
          const float k_ = 1.f + TKS * (fminf(fmaxf(23.9f / f_, 1.f), 1.5f) - 1.f);
          const int nb_ = (int)(TDB * k_ + 0.5f), nc_ = nb_ + (int)((TDC - TDB) * k_ + 0.5f), np_ = nc_ + (int)((TP - TDC) * k_ + 0.5f);
          u32 lat = min(c0 ? r0[2] : 65535u, c1 ? r1[2] : 65535u);
          { auto r = __builtin_amdgcn_permlane16_swap(lat, lat, false, false); lat = min(r[0], r[1]); }
          { auto r = __builtin_amdgcn_permlane32_swap(lat, lat, false, false); lat = min(r[0], r[1]); }
          u32 mm[4];
#pragma unroll
          for (int k = 0; k < 4; ++k)
            mm[k] = __builtin_amdgcn_readlane(lat, 4 * k) + __builtin_amdgcn_readlane(lat, 4 * k + 2) + __builtin_amdgcn_readlane(lat, 4 * (k ^ 2) + 1) + __builtin_amdgcn_readlane(lat, 4 * (k ^ 2) + 3);
          const u32 hb = mm[0] + mm[2] <= mm[1] + mm[3] ? 0u : 1u;
          best = (hb << 13) | ((hb ? mm[1] <= mm[3] : mm[0] <= mm[2]) ? CLS0 : 16384u - CLS0);
          if (__ballot(lat == 65535u) == 0) tt = __builtin_amdgcn_readfirstlane(nb_ | (nc_ << 10) | (np_ << 20));
        }
        if (__ballot(r0[0] != tag || r1[0] != tag) == 0 && xok && tt) break;
      }
      st = (u32x4){rt32() + TD0, tt, best, tag};
      __builtin_amdgcn_raw_buffer_store_b128(st, rsx, sto_, 0, 16);
    } else st = wait_start(rsx, fb, tag, false, xcd);
    if (lane == 0) *(u32x4*)(lds + LR_CTL + 32) = st;
  }
  LBAR();
  st = *(const u32x4*)(lds + LR_CTL + 32);
  const int flip = __builtin_amdgcn_readfirstlane(st[2]) & 8192, cls = __builtin_amdgcn_readfirstlane(st[2]) & 16384;
  o1c ^= cls; o3c ^= cls; sto ^= cls; kc[4] ^= cls; kc[8] ^= cls; kc[9] ^= cls; kc[10] ^= cls; kc[11] ^= cls;
  { const u32 now = rt32(); if (st[0] - now + 100000u > 200000u) st[0] = now; }
  u32x16 kd = {(u32)((ld01 + XR2(vcu ^ 1)) ^ cls), (u32)((ld01 + XR2(vcu ^ 2)) ^ cls), (u32)((ld01 + XR2(vcu ^ 3)) ^ cls), (u32)(X4B && hk ? OOR : sto), (u32)avr, (u32)(avr + 4096), (u32)(avr + 8192),
               (u32)(avr + 12288), (u32)avw, (u32)(X4C && hk ? OOR : st1 ^ cls), (u32)((lane & 1) * 4096), (u32)(XSB(lane + 1) + flip), (u32)(lane * 8), (u32)avf,
               (u32)(b == 0 && lane == 0 ? XSYN + flip : OOR), (u32)(w == 0 ? pz1 ^ cls : (w == 1 ? kc[10] : kc[4]))};
  u32x16 sa = {(u32)flip, (u32)(XSB(1) + flip), 0u, (u32)fok, (u32)fov, (u32)(FO_KM + (head * 4 + ks) * 512),
               (u32)(FO_VM + (head * 8 + 2 * ks) * 256), st[0], 0u, 0u, (u32)SYO(1), 0u, 0u, (u32)TRY_CAP, 0u, 0u};
  u32x8 sc = {(u32)o1c, (u32)o3c, SENT, 0u, 0u, 0u, st[1], (u32)pgo};
#define TUNEO
#define KCNT
#define KHST
#define KTRC
#define TRCI
#define PREP 16
#define GAP 12
#define RETA(n, L) "s_cmp_eq_u32 s49, " #n "\n\t" "s_cmov_b32 s48, .Lpc%=-" L "\n\t"
  u32x32 keep;
  u32x2 ov;
  asm volatile("" : "={v[60:91]}"(keep));
  while (true) {
    sa[14] = xp;
#pragma unroll
    for (int i = 0; i < 16; ++i) sa[i] = __builtin_amdgcn_readfirstlane(sa[i]);
#pragma unroll
    for (int i = 0; i < 8; ++i) sc[i] = __builtin_amdgcn_readfirstlane(sc[i]);
    asm volatile(
        "s_nop 4\n\t" "s_mov_b32 s60, m0\n\t"
        "s_mov_b32 m0, %[mb]\n\t" "s_mov_b32 s64, s50\n\t" "s_mov_b32 s98, s49\n\t" "s_mov_b32 s68, 0\n\t"
        EXTRA R2BASE
        "s_cmp_lg_u32 %[wv], 3\n\t" "s_cbranch_scc1 .LN%=\n\t"
        "s_mov_b32 s78, 0xff00ff00\n\t" "s_mov_b32 s79, 0xff00ff00\n\t" "s_mov_b32 s62, 0x3e0293ee\n\t"
        "v_mov_b32 v52, 0x3f803f80\n\t" "v_mov_b32 v53, 0x3f803f80\n\t" "v_mov_b32 v54, 0x3f803f80\n\t" "v_mov_b32 v55, 0x3f803f80\n\t" "v_mov_b32 v51, 0\n\t" "v_mov_b32 v59, 0\n\t"
        "v_mov_b32 v60, %[tg]\n\t" "v_mov_b32 v61, -1\n\t" "s_mov_b32 s63, %[dm]\n\t"
        "s_branch .Lent%=\n"
        ".LA%=:\n\t"
        CLKW(".Lta%=", "0") REL(".Lua%=")
        "s_add_u32 s50, s36, s52\n\t" NOP(PDA)
        ABODY(NOP(PCL) LASTA(".Lwa%="), WVM(5) "v_readlane_b32 s50, v60, 0\n\t" "v_readlane_b32 s51, v60, 1\n\t"
              "s_sub_u32 s50, s50, %[tg]\n\t" "s_sub_u32 s51, s51, %[tg]\n\t" "s_min_u32 s63, s50, s51\n\t" "s_max_u32 s50, s50, s51\n\t" "s_cmp_le_u32 s50, 80\n\t"
              "s_cselect_b32 s50, s63, 0\n\t" "v_readfirstlane_b32 s49, v61\n\t" "s_max_i32 s64, s64, s50\n\t"
              "s_cmp_lg_u32 s49, -1\n\t" "s_cmov_b32 s58, s49\n\t" EXTRA "v_mov_b32 v62, s58\n\t" "ds_write_b32 v38, v62 offset:24\n\t", ".Lfail%=", 0) TSA(3)
        QKRED(BARA) ORD(88, 89) ORD(90, 91) QKPUB TSA(6) TSB(7) REDO(".Lqk%=")
        SVP(0, 0) SHRD SVP(1, 0) SVP(0, 1) SVP(1, 1) SVP(0, 2) SVP(1, 2) WLG(0) "v_readfirstlane_b32 s49, v62\n\t" SVP(0, 3) SVP(1, 3) "s_max_u32 s44, s44, s49\n\t" "s_cmp_eq_u32 s45, 63\n\t" TSA(7)
        "s_cbranch_scc1 .Llast%=\n"
        ".LB%=:\n\t"
        CLKW(".Ltb%=", "s66")
        "s_mov_b32 s47, 1\n\t"
        R2TEXT
        ".LC%=:\n\t"
        ZALL
        "s_mov_b32 s47, 5\n\t" "s_sub_i32 s63, s64, s45\n\t" "s_cmp_lt_i32 s63, 2\n\t" "s_cbranch_scc1 .Lend%=\n\t"
        "s_cmp_lt_i32 s63, " STR(XTH) "\n\t" "s_cselect_b32 s63, s59, %[dm]\n\t"
        CLKW(".Ltc%=", "s67") REL(".Luc%=")
        "s_add_u32 s50, s36, s53\n\t" NOP(PDC)
        CISSUE(LASTC(".Lwc%="), "")
        "buffer_load_dword v61, off, %[rs], s46 sc1\n\t" "s_add_u32 s46, s46, 64\n\t"
        "s_add_u32 s49, s39, 4096\n\t" NOP(PDF)
        "buffer_load_dwordx4 a[208:211], v44, %[rf], s39 offen sc1\n\t" "buffer_load_dwordx4 a[212:215], v44, %[rf], s49 offen sc1\n\t"
        "buffer_load_dwordx2 a[192:193], v44, %[rf], s40 offen sc1\n\t" "buffer_load_dwordx2 a[196:197], v44, %[rf], s40 offen offset:8 sc1\n\t"
        "buffer_load_dwordx2 a[200:201], v44, %[rf], s40 offen offset:1024 sc1\n\t" "buffer_load_dwordx2 a[204:205], v44, %[rf], s40 offen offset:1032 sc1\n\t"
#define NXC 8
        "buffer_load_dword v60, v18, %[rs], s63 offen sc1\n\t"
        CBODY(NXC, "", NXC, ".Lfail%=")
        MRED(BARC, "") ORD(84, 85) ORD(86, 87) STC TSW(6) REDO(".Lmr%=")
        SHRD SHUP TSW(7)
        NEXTP
        CWAIT(".Lqk%=", 8, "s48") QKRED("") NOP(1) QKPUB "s_branch .Lqk%=r\n"
        CWAIT(".Lmr%=", 16, "s38") WVM(0) MRED("", "") "v_min_u32 v80, -2, v80\n\t" "v_min_u32 v81, -2, v81\n\t" STC
        "s_branch .Lmr%=r\n"
        ".Llast%=:\n\t"
        "buffer_store_dwordx4 v[72:75], v47, %[rf], s41 offen sc1\n\t" "s_mov_b32 s47, 3\n\t" NOP(4) VPOST ARRIVE(12) "s_barrier\n\t"
        "s_branch .Lend%=\n"
        R2MAX
        ".Lnorm%=:\n\t"
        "s_cmp_lg_u32 %[wv], 3\n\t" "s_cbranch_scc1 .LNA%=\n\t" "s_cmp_eq_u32 s47, 0\n\t" "s_cbranch_scc1 .LA%=\n\t" "s_branch .LC%=\n"
        ".LN%=:\n\t"
        "v_mov_b64 v[92:93], -1\n\t" "v_mov_b64 v[94:95], -1\n\t" "s_mov_b32 s65, s44\n\t" "s_mov_b32 s78, -100\n\t" "s_mov_b32 s79, 4\n\t"
        "s_branch .Lent%=\n"
        ".LNA%=:\n\t" PZA0
        "s_add_u32 s50, s36, s52\n\t"
        RELN(".Lna%=")
        ABODY(NOP(PCA), LASTA(".Lxa%=") PZA, ".Lfail%=", PZNA) BARA ARRIVE(8) TSA(3)
        SV(0, 0) SV(1, 0) SV(0, 1) SV(1, 1) SV(0, 2) SV(1, 2) SV(0, 3) SV(1, 3) ZER(64) ZER(68) ZER(72) ZER(76)
        VPOST TSA(7) ZER(80) ZER(84) ZER(24) ZER(28)
        "v_add_u32 v88, 4096, v21\n\t" "v_add_u32 v89, 8192, v21\n\t" "v_add_u32 v90, 12288, v21\n\t"
        PZB0
        "s_lshl_b32 s61, s45, 7\n\t" "s_add_u32 s61, s61, " STR(PGO_R - XSYN) "\n\t" "s_cmp_lg_u32 %[wv], 0\n\t"
        WLG(0) "s_barrier\n\t" PZB
        PSW(92, 0) PSW(92, 272) PSW(92, 640) PSW(92, 912) PSW(92, 1280) PSW(92, 1552) PSW(92, 1920) PSW(92, 2192) PSW(92, 3840)
        "ds_read2_b32 v[50:51], v38 offset0:0 offset1:6\n\t"
        "s_cbranch_scc1 .LNB%=\n\t"
        "s_memrealtime s[72:73]\n"
        ".Lv0%=:\n\t"
        "ds_read_b32 v62, v38 offset:" CNT(12) "\n\t"
        "ds_read2_b32 v[112:113], v21 offset0:0 offset1:68\n\t" "ds_read2_b32 v[114:115], v21 offset0:16 offset1:84\n\t"
        "ds_read2_b32 v[116:117], v88 offset0:0 offset1:68\n\t" "ds_read2_b32 v[118:119], v88 offset0:16 offset1:84\n\t"
        "ds_read2_b32 v[120:121], v89 offset0:0 offset1:68\n\t" "ds_read2_b32 v[122:123], v89 offset0:16 offset1:84\n\t"
        "ds_read2_b32 v[124:125], v90 offset0:0 offset1:68\n\t" "ds_read2_b32 v[126:127], v90 offset0:16 offset1:84\n\t"
        WLG(0) "v_cmp_gt_u32 vcc, s48, v62\n\t" "s_cbranch_vccnz .Lv0%=\n\t"
        "s_add_u32 s50, s72, " STR(TVM) "\n\t" "s_lshl_b32 s50, s50, 17\n\t" "s_lshr_b32 s50, s50, 1\n\t"
        PKA(112, 112, 116) PKA(114, 114, 118) PKA(120, 120, 124) PKA(122, 122, 126) PKA(112, 112, 120) PKA(114, 114, 122)
        "v_lshrrev_b32 v91, 20, v51\n\t" "v_readfirstlane_b32 s49, v50\n\t"
        "v_cvt_pk_bf16_f32 v112, v112, v113\n\t" "v_cvt_pk_bf16_f32 v113, v114, v115\n\t" "v_or_b32 v114, s50, v91\n\t"
        "s_cmp_eq_u32 s45, 63\n\t" "s_cbranch_scc1 .Lv2%=\n\t"
        "s_sleep " STR(SLV) "\n"
        ".Lv2%=:\n\t"
        "buffer_store_dwordx2 v[112:113], v20, %[rf], s42 offen sc1\n\t"
        "buffer_store_dword v114, v22, %[rs], s61 offen sc1\n\t"
        "s_cmp_eq_u32 s49, s65\n\t" "s_cbranch_scc1 .LNB%=\n\t"
        "s_mov_b32 s65, s49\n\t" KHST "s_sub_u32 s49, s45, s78\n\t" "s_mov_b32 s78, s45\n\t"
        "s_cmp_gt_i32 s49, " STR(GAP) "\n\t" "s_cbranch_scc1 .LNB%=\n\t" "s_cmp_lt_u32 s45, s79\n\t" "s_cbranch_scc1 .LNB%=\n\t"
        "s_add_u32 s79, s45, 3\n\t" "s_lshl_b32 s49, s45, 6\n\t" "v_add_u32 v50, " STR(TLB + ((TLB + TLC) << 10) + ((TLB + TLC + TLP) << 20)) ", v51\n\t"
        "buffer_store_dword v50, v22, %[rs], s49 offen offset:128 sc1\n"
        ".LNB%=:\n\t"
        WLG(0) "v_readfirstlane_b32 s58, v51\n\t" EXTRA
        "s_mov_b32 s47, 3\n\t"
        "s_cmp_eq_u32 s45, 63\n\t"
        "s_cbranch_scc1 .Lend%=\n"
        ".LNC%=:\n\t" PZC0
        "s_add_u32 s50, s36, s53\n\t"
        WLG(0) RELN(".Lnc%=")
        CISSUE("", LASTC(".Lxc%="))
        CBODY(0, PZC, PZNC, ".Lfail%=") BARC
        ARRIVE(16) TSW(4)
        NEXTN
        ".Lfail%=:\n\t" KCNT
        "s_cmp_lg_u32 s47, 2\n\t" "s_cbranch_scc1 .Lnu%=\n\t"
        "s_mov_b32 s37, s36\n\t" "s_mov_b32 s36, s48\n\t" "s_sub_u32 s45, s45, 1\n\t" "s_sub_u32 s38, s38, 4\n\t" "s_sub_u32 s39, s39, " STR(EXF) "\n\t" "s_sub_u32 s40, s40, " STR(EXF) "\n\t"
        "s_sub_u32 s41, s41, " STR(EXK) "\n\t" "s_sub_u32 s42, s42, " STR(EXK) "\n\t" "s_sub_u32 s43, s43, s55\n\t" "s_sub_u32 s43, s43, s70\n\t" "s_sub_u32 s46, s46, 64\n"
        ".Lnu%=:\n\t"
        "s_mov_b32 s51, 1\n\t" "s_nop 7\n\t" WVM(0) "s_sub_u32 s98, s98, 1\n\t" "s_cbranch_scc1 .Lcap%=\n"
        ".Lent%=:\n\t"
        "s_cmp_eq_u32 s47, 1\n\t" "s_cselect_b32 s99, s66, s67\n\t" "s_cbranch_scc1 .Lnz%=\n\t" ZALL ".Lnz%=:\n\t"
        "s_cmp_eq_u32 s47, 0\n\t" "s_cselect_b32 s99, 0, s99\n\t" "s_cselect_b32 s50, s52, s53\n\t" "s_add_u32 s99, s99, s43\n\t" "s_add_u32 s50, s50, s36\n\t"
        SHRD "s_memrealtime s[72:73]\n\t"
        "s_cmp_eq_u32 %[wv], 3\n\t" "s_cselect_b32 s49, 0, 3\n\t" "s_add_u32 s49, s49, s47\n\t" "s_cmp_lg_u32 s51, 0\n\t" "s_cselect_b32 s49, s49, 7\n\t" "s_cselect_b64 vcc, -1, 0\n\t"
        "s_cselect_b32 s51, 0, 14\n\t" "s_mov_b32 s48, .Lpc%=-.Lnorm%=\n\t"
        RETA(0, ".Lua%=") RETA(1, ".Ltb%=y") RETA(2, ".Luc%=") RETA(3, ".Lna%=") RETA(5, ".Lnc%=")
        "s_getpc_b64 s[96:97]\n" ".Lpc%=:\n\t" "s_sub_u32 s96, s96, s48\n\t" "s_subb_u32 s97, s97, 0\n\t"
        SHUP "s_sub_u32 s49, s99, s72\n\t" "s_add_u32 s49, s49, s44\n"
        ".Lbump%=:\n\t"
        "s_cmp_ge_i32 s49, " STR(PREP) "\n\t" "s_cbranch_scc1 .Lok%=\n\t" "s_add_u32 s44, s44, %[tr]\n\t" "s_add_u32 s49, s49, %[tr]\n\t" "s_branch .Lbump%=\n"
        ".Lok%=:\n\t"
        "s_mov_b64 exec, 1\n\t" "v_mov_b32 v62, s44\n\t" "ds_max_u32 v38, v62\n\t" KTRC "s_mov_b64 exec, -1\n\t"
        "s_add_u32 s99, s99, s44\n\t" "s_sub_u32 s99, s99, s51\n\t"
        WLG(0) "s_memrealtime s[72:73]\n\t" "s_nop 3\n\t" "s_memrealtime s[74:75]\n\t" "s_nop 3\n"
        CLKL(".Lth%=", "s99")
        "s_setpc_b64 s[96:97]\n"
        ".Lcap%=:\n\t" "s_add_u32 s47, s47, 8\n"
        ".Lend%=:\n\t"
        "s_waitcnt vmcnt(0) lgkmcnt(0)\n\t"
        "s_mov_b32 s50, s64\n\t" "s_mov_b32 s49, s98\n\t" "s_mov_b32 s51, s68\n\t" "s_mov_b32 m0, s60"
        : "+{s[36:51]}"(sa), "+{s[52:59]}"(sc), "+{v[60:91]}"(keep), "=&{v[116:117]}"(ov), "+{a[192:207]}"(fv), "+{a[208:215]}"(fk), "+{v[8:23]}"(kd) FINEO TUNEO
        : "{v[32:47]}"(kc), [rs] "s"(rsx), [rf] "s"(rsf), [dm] "s"(__builtin_amdgcn_readfirstlane(PGO_Y + b * 128)), [tg] "s"(__builtin_amdgcn_readfirstlane(tag)), [r2] "s"(__builtin_amdgcn_readfirstlane(XR2B + flip)), [mb] "s"(__builtin_amdgcn_readfirstlane(ldsb + w * 4096)), [wv] "s"(__builtin_amdgcn_readfirstlane(w)), [tr] "s"(TR) TRCI,
          "{a[0:31]}"(wa[0]), "{a[32:63]}"(wa[1]), "{a[64:95]}"(wa[2]), "{a[96:127]}"(wa[3]), "{a[128:159]}"(wa[4]), "{a[160:191]}"(wa[5]),
          "{v[128:159]}"(wo[0]), "{v[160:191]}"(wo[1]), "{v[192:223]}"(wo[2]), "{v[224:255]}"(wo[3])
        : CL8(24, 25, 26, 27, 28, 29, 30, 31), "v48", "v49", "v50", "v51", CL8(52, 53, 54, 55, 56, 57, 58, 59), "v92", "v93", "v94", "v95", CL96, "v112", "v113", "v114", "v115", "v118", "v119", CL120,
          "s60", "s61", "s62", "s63", "s64", "s65", "s66", "s67", "s68", "s69", "s70", "s71", "s72", "s73", "s74", "s75", "s76", "s77", "s78", "s79", "s96", "s97", "s98", "s99", "vcc", "scc", "memory");
    const int code = __builtin_amdgcn_readfirstlane(sa[11]);
    xp = __builtin_amdgcn_readfirstlane(sa[14]);
    tcnt += sa[15];
    sa[15] = 0;
    if (code == 3) break;
    const int seg = __builtin_amdgcn_readfirstlane(sa[9]);
    if (code == 5) { XWAIT(seg + 2) xp = max(xp, seg + 2); sa[11] = 2; }
    else if (code == 1) { __builtin_amdgcn_raw_buffer_store_b64((u32x2){nosent(ov[0]), nosent(ov[1])}, rsx, sto, sa[0], 16); sa[11] = 2; }
    else { sc[2] += 1; sa[13] = 0; sa[11] = code - 8; sa[15] = 1; }
  }
  LBAR();
  if (tid == 0) __builtin_amdgcn_raw_buffer_store_b32(1u, rsx, 0, PGO_D + vcu * 128, 16);
  if (b == 0 && w == 1) {
    for (int it = 0; it < SPIN_CAP; ++it) {
      asm volatile("" ::: "memory");
      if (__ballot(__builtin_amdgcn_raw_buffer_load_b32(rsx, lane * 128, PGO_S, 16) != tag || __builtin_amdgcn_raw_buffer_load_b32(rsx, lane * 128 + 8192, PGO_S, 16) != tag) == 0) break;
    }
    if (lane == 0) __builtin_amdgcn_raw_buffer_store_b32(tag, rsx, 0, XEPO, 16);
  }
}

#define XSTAT 3
#define XVM 8
#define XP0 -1
#define XSC " sc1"
#define XPITCH 1056
#define XSLOT (32 * XPITCH)
#define XW(HB, t) "a[" STR(HB) "+8*xk+4*" #t ":" STR(HB) "+8*xk+4*" #t "+3]"
#define XF(rt) "v[96+8*(xk&3)+4*" #rt ":96+8*(xk&3)+4*" #rt "+3]"
#define XFN(rt) "v[96+8*((xk+3)&3)+4*" #rt ":96+8*((xk+3)&3)+4*" #rt "+3]"
#define XA(AC, o) "v[" STR(AC) "+" #o ":" STR(AC) "+" #o "+3]"
#define XM1(d, a, b, FIRST) ".if (xk == 0) && " STR(FIRST) "\n\tv_mfma_f32_16x16x32_bf16 " d ", " a ", " b ", 0\n\t.else\n\tv_mfma_f32_16x16x32_bf16 " d ", " a ", " b ", " d "\n\t.endif\n\t"
#define XMF_QK(i, HB, AC, F) XMF_QK##i(HB, AC, F)
#define XMF_QK0(HB, AC, F) XM1(XA(AC, 0), XW(HB, 0), XF(0), F)
#define XMF_QK1(HB, AC, F) XM1(XA(AC, 4), XW(HB, 1), XF(0), F)
#define XMF_QK2(HB, AC, F) XM1(XA(AC, 8), XW(HB, 0), XF(1), F)
#define XMF_QK3(HB, AC, F) XM1(XA(AC, 12), XW(HB, 1), XF(1), F)
#define XMF_V(i, HB, AC, F) XMF_V##i(HB, AC, F)
#define XMF_V0(HB, AC, F) XM1(XA(AC, 0), XF(0), XW(HB, 0), F)
#define XMF_V1(HB, AC, F) XM1(XA(AC, 8), XF(0), XW(HB, 1), F)
#define XMF_V2(HB, AC, F) XM1(XA(AC, 4), XF(1), XW(HB, 0), F)
#define XMF_V3(HB, AC, F) XM1(XA(AC, 12), XF(1), XW(HB, 1), F)
#define XRD(rt, CUR, NXT) ".if xk < 13\n\tds_read_b128 " XFN(rt) ", " CUR " offset:64*(xk+3)+" STR(rt * 16 * XPITCH) "\n\t.else\n\t" \
                          "ds_read_b128 " XFN(rt) ", " NXT " offset:64*(xk-13)+" STR(rt * 16 * XPITCH) "\n\t.endif\n\t"
#define XCV(EPI, PV, i) ".if " STR(EPI) " && (xk >= 1) && (xk < 3)\n\tv_cvt_pk_bf16_f32 v[128+4*(xk-1)+" #i "], v[" STR(PV) "+8*(xk-1)+2*" #i "], v[" STR(PV) "+8*(xk-1)+2*" #i "+1]\n\t.endif\n\t"
#define XHALF(MF, HB, AC, PV, EPI, CUR, NXT, DSLOT, GOFF)                                                   \
  ".set xk, 0\n\t"                                                                                          \
  ".rept 16\n\t"                                                                                            \
  "s_waitcnt lgkmcnt(4)\n\t"                                                                                \
  MF(0, HB, AC, EPI) XRD(0, CUR, NXT) XCV(EPI, PV, 0)                                                       \
  ".if (xk >= 5) && (xk < 13)\n\t"                                                                          \
  "s_add_u32 m0, m0, " STR(XPITCH) "\n\t"                                                                   \
  ".endif\n\t"                                                                                              \
  MF(1, HB, AC, EPI) XRD(1, CUR, NXT) XCV(EPI, PV, 1)                                                       \
  ".if (xk >= 5) && (xk < 13)\n\t"                                                                          \
  "s_add_u32 %[dg], %[dg], 2048\n\t"                                                                        \
  ".endif\n\t"                                                                                              \
  MF(2, HB, AC, EPI) XCV(EPI, PV, 2)                                                                        \
  ".if (xk >= 4) && (xk < 12)\n\t"                                                                          \
  "buffer_load_dwordx4 %[l16], %[rx], %[dg] offen lds\n\t"                                                  \
  ".endif\n\t"                                                                                              \
  ".if xk == 0\n\t"                                                                                         \
  "s_add_u32 m0, %[dl], " STR((DSLOT) * XSLOT) "\n\t" "s_add_u32 %[dg], %[gb], " STR(GOFF) "\n\t"           \
  ".endif\n\t"                                                                                              \
  ".if " STR(EPI) " && (xk == " STR(XSTAT) ")\n\t"                                                                       \
  "buffer_store_dwordx4 v[128:131], v137, %[rf], %[sg] offen" XSC "\n\t"                                       \
  ".endif\n\t"                                                                                              \
  ".if " STR(EPI) " && (xk == 13)\n\t"                                                                      \
  "v_mov_b32 v137, %[l16]\n\t" "v_mov_b32 v138, %[lp2]\n\t"                                                 \
  ".endif\n\t"                                                                                              \
  ".if " STR(EPI) " && (xk == 14)\n\t"                                                                      \
  "s_add_u32 %[sg], %[sg], " STR(NH * 8 * 1024) "\n\t"                                                      \
  "v_add_u32 v136, 1, v136\n\t"                                                                             \
  ".endif\n\t"                                                                                              \
  ".if !" STR(EPI) " && (xk == 1)\n\t"                                                                      \
  "buffer_store_dword v136, %[fo], %[rp], 0 offen sc1\n\t"                                                  \
  ".endif\n\t"                                                                                              \
  MF(3, HB, AC, EPI) XCV(EPI, PV, 3)                                                                        \
  ".if xk == 0\n\t"                                                                                         \
  "s_waitcnt vmcnt(" STR(XVM) ")\n\t" "s_barrier\n\t"                                                               \
  ".endif\n\t"                                                                                              \
  ".if xk == 2\n\t"                                                                                         \
  "s_min_u32 %[dg], %[dg], " STR(S_LEN * DM * 2 - 16384 + 1024) "\n\t"                                      \
  ".endif\n\t"                                                                                              \
  ".if " STR(EPI) " && (xk == " STR(XSTAT) ")\n\t"                                                                       \
  "buffer_store_dwordx4 v[132:135], v138, %[rf], %[sg] offen" XSC "\n\t"                                       \
  ".endif\n\t"                                                                                              \
  ".set xk, xk+1\n\t"                                                                                       \
  ".endr\n\t"
#define XLOOP(MF)                                                                                           \
  "s_mov_b32 %[kp], m0\n\t"                                                                                 \
  "v_mov_b32 v136, %[p0]\n\t" "v_mov_b32 v137, " STR(OOR) "\n\t" "v_mov_b32 v138, " STR(OOR) "\n\t"                                                                                \
  "ds_read_b128 v[96:99], %[r0]\n\tds_read_b128 v[100:103], %[r0] offset:" STR(16 * XPITCH) "\n\t"          \
  "ds_read_b128 v[104:107], %[r0] offset:64\n\tds_read_b128 v[108:111], %[r0] offset:64+" STR(16 * XPITCH) "\n\t"   \
  "ds_read_b128 v[112:115], %[r0] offset:128\n\tds_read_b128 v[116:119], %[r0] offset:128+" STR(16 * XPITCH) "\n"   \
  ".Lx%=:\n\t"                                                                                              \
  XHALF(MF, 0, 64, 80, 1, "%[r0]", "%[r1]", 3, 66560) XHALF(MF, 128, 64, 80, 0, "%[r1]", "%[r2]", 0, 131072) \
  XHALF(MF, 0, 80, 64, 1, "%[r2]", "%[r3]", 1, 132096) XHALF(MF, 128, 80, 64, 0, "%[r3]", "%[r0]", 2, 196608) \
  "s_add_u32 %[gb], %[gb], 131072\n\t"                                                                      \
  "s_sub_u32 %[cnt], %[cnt], 1\n\t"                                                                         \
  "s_cmp_lg_u32 %[cnt], 0\n\t"                                                                              \
  "s_cbranch_scc1 .Lx%=\n\t"                                                                                \
  "s_mov_b32 m0, %[kp]\n\t"                                                                                 \
  "s_nop 7\n\t" "s_waitcnt lgkmcnt(0)"

template <bool ISV>
__device__ __forceinline__ void x_role(unsigned char* lds, const u16* __restrict__ x, const u16* __restrict__ Wm, int fo0, int head,
                                       __amdgpu_buffer_rsrc_t rsf, u32* prog, u32 tag, int lane, int w) {
  const int ldsb = (int)(size_t)(LAS unsigned char*)lds;
  const __amdgpu_buffer_rsrc_t rsxin = __builtin_amdgcn_make_buffer_rsrc((void*)x, 0, S_LEN * DM * 2, 0x00020000);
  const __amdgpu_buffer_rsrc_t rsp = __builtin_amdgcn_make_buffer_rsrc((void*)prog, 0, 128, 0x00020000);
  const int lr = lane & 15, lg = lane >> 4;
  const int xrd = ldsb + lr * XPITCH + lg * 16;
  const int fob = fo0 + (ISV ? (head * 8 + 2 * w) : (head * 8 + w)) * 1024;
  const int p2 = ISV ? 1024 : 4096;
  u32x32 xw[8];
  {
    const __amdgpu_buffer_rsrc_t rw = __builtin_amdgcn_make_buffer_rsrc((void*)Wm, 0, DM * HD * 2, 0x00020000);
    const int vq = (lane >> 4) * (HD * 2) + head * 256 + (lane & 15) * 16;
#define XWISSUE(c) _Pragma("unroll") for (int j = 0; j < 8; ++j)                                                       \
      asm volatile("s_mov_b32 m0, %0\n\ts_nop 3\n\tbuffer_load_dwordx4 %1, %2, %3 offen lds"                           \
                   :: "s"(ldsb + ((c) & 3) * XSLOT + (8 * w + j) * XPITCH), "v"(vq), "s"(rw), "s"((128 * (c) + 32 * w + 4 * j) * (HD * 2)) : "memory");
#define XXISSUE(h) _Pragma("unroll") for (int j = 0; j < 8; ++j)                                                       \
      asm volatile("s_mov_b32 m0, %0\n\ts_nop 3\n\tbuffer_load_dwordx4 %1, %2, %3 offen lds"                           \
                   :: "s"(ldsb + (h) * XSLOT + (8 * w + j) * XPITCH), "v"(lane * 16), "s"(rsxin), "s"((((h) >> 1) * 32 + 8 * w + j) * 2048 + ((h) & 1) * 1024) : "memory");
    XWISSUE(0) XWISSUE(1) XWISSUE(2) XWISSUE(3)
    const int q = lr >> 2, p = lr & 3;
#pragma unroll
    for (int c = 0; c < 8; ++c) {
      asm volatile("s_waitcnt vmcnt(24)\n\ts_barrier" ::: "memory");
#pragma unroll
      for (int s = 0; s < 4; ++s)
#pragma unroll
        for (int t = 0; t < 2; ++t) {
          const unsigned char* base = lds + (c & 3) * XSLOT + (8 * s + 2 * lg) * XPITCH + q * 256 + w * 64 + p * 16 + t * 8;
          const u32x2 t0 = __builtin_bit_cast(u32x2, __builtin_amdgcn_ds_read_tr16_b64_v4i16((LAS v4i16_t*)(base)));
          const u32x2 t1 = __builtin_bit_cast(u32x2, __builtin_amdgcn_ds_read_tr16_b64_v4i16((LAS v4i16_t*)(base + XPITCH)));
          const int o = ((4 * c + s) * 2 + t) * 4;
          xw[o >> 5][(o & 31) + 0] = t0[0]; xw[o >> 5][(o & 31) + 1] = t0[1]; xw[o >> 5][(o & 31) + 2] = t1[0]; xw[o >> 5][(o & 31) + 3] = t1[1];
        }
      LBAR();
      if (c < 4) { XWISSUE(c + 4) } else if (c < 7) { XXISSUE(c - 4) }
    }
  }
  asm volatile("s_waitcnt vmcnt(8)\n\ts_barrier" ::: "memory");
  u32x16 xacc;
  {
    u32 kp, dg; int gb = 8 * w * 2048, cnt = NSEG / 2, sg = fob - NH * 8 * 1024;
#define XASM(MF)                                                                                            \
    asm volatile(XLOOP(MF)                                                                                  \
                 : "=&{v[80:95]}"(xacc), [kp] "=&s"(kp), [dg] "=&s"(dg), [gb] "+s"(gb), [cnt] "+s"(cnt), [sg] "+s"(sg) \
                 : [r0] "v"(xrd), [r1] "v"(xrd + XSLOT), [r2] "v"(xrd + 2 * XSLOT), [r3] "v"(xrd + 3 * XSLOT), [l16] "v"(lane * 16), [lp2] "v"(lane * 16 + p2), \
                   [rx] "s"(rsxin), [rf] "s"(rsf), [rp] "s"(rsp), [fo] "v"(lane == 0 && w == 0 ? 0 : OOR), [dl] "s"(ldsb + 8 * w * XPITCH), [p0] "s"(tag + XP0), \
                   "{a[0:31]}"(xw[0]), "{a[32:63]}"(xw[1]), "{a[64:95]}"(xw[2]), "{a[96:127]}"(xw[3]),      \
                   "{a[128:159]}"(xw[4]), "{a[160:191]}"(xw[5]), "{a[192:223]}"(xw[6]), "{a[224:255]}"(xw[7]) \
                 : CL64, CL72, CL96, CL112, CL120, CL8(128, 129, 130, 131, 132, 133, 134, 135), "v136", "v137", "v138", "scc", "memory");
    if (ISV) { XASM(XMF_V) } else { XASM(XMF_QK) }
  }
#pragma unroll
  for (int jj = 0; jj < 2; ++jj) {
    u32x4 pk;
#pragma unroll
    for (int e = 0; e < 4; ++e) pk[e] = pack2(__uint_as_float(xacc[8 * jj + 2 * e]), __uint_as_float(xacc[8 * jj + 2 * e + 1]));
    __builtin_amdgcn_raw_buffer_store_b128(pk, rsf, lane * 16 + p2 * jj, fob + (NSEG - 1) * (NH * 8 * 1024), 16);
  }
  asm volatile("s_waitcnt vmcnt(0)\n\ts_barrier" ::: "memory");
  if (lane == 0 && w == 0) __hip_atomic_store(prog, tag + NSEG + 8, RLX_AGENT);
}

#define SSM_LO 1
#define SU_CP 4416
#define SU_U 0
#define SU_K (4 * SU_CP)
#define SU_P (SU_K + 10400)
#define SU_D (SU_P + 2048)
#define SU_RAW (SU_D + 2592)
#define SU_CH (SU_RAW + 4128 + 32)
#define SST(i)
__device__ __forceinline__ void ssm_unit(unsigned char* sm, int d0, __amdgpu_buffer_rsrc_t rst, __amdgpu_buffer_rsrc_t rsx, unsigned char* xb, int fb, bool& uok,
                                         const u16* __restrict__ Dskip, int tid, int lane, int w) {
  unsigned char* cb = sm + w * SU_CH;
  const int dd = d0 + w;
  SST(0)
  if (!uok) {
    for (int it = 0; it < SPIN_CAP; ++it) {
      asm volatile("" ::: "memory");
      if ((int)__builtin_amdgcn_readfirstlane(__builtin_amdgcn_raw_buffer_load_b32(rsx, 0, XUDN, 16)) >= 15) break;
      asm volatile("s_sleep 8" ::: "memory");
    }
    asm volatile("" ::: "memory");
    uok = true;
  }
  f32x4 acc[3];
#pragma unroll
  for (int t = 0; t < 3; ++t) acc[t] = __builtin_bit_cast(f32x4, __builtin_amdgcn_raw_buffer_load_b128(rst, lane * 16, 2 * SSM_L * SSM_D * 2 + dd * 3072 + t * 1024, 16));
  const float dsk = bf2f(Dskip[dd]);
  u32x4 uv[4];
#pragma unroll
  for (int i = 0; i < 4; ++i) uv[i] = __builtin_amdgcn_raw_buffer_load_b128(rst, (tid + 256 * i) * 16, d0 * 4096, 16);
  const int r = lane & 31, h = lane >> 5;
  {
    const int lr = lane & 15, lg = lane >> 4;
    SST(2)
    u32* Kd = (u32*)(cb + SU_D);
#pragma unroll
    for (int e = 0; e < 4; ++e) {
      Kd[128 * (lg >> 1) + 8 * lr + 4 * (lg & 1) + e] = split_hl(acc[0][e]);
      Kd[lg < 2 ? 256 + 64 * lg + 4 * lr + e : 384 + 32 * (2 * (lg - 2) + (e >> 1)) + 2 * lr + (e & 1)] = split_hl(acc[1][e]);
      if (lg < 2 || (lg == 2 && e == 0 && lr == 0)) Kd[512 + 16 * (4 * lg + e) + lr] = split_hl(acc[2][e]);
    }
    asm volatile("s_waitcnt lgkmcnt(0)" ::: "memory");
    unsigned char* kr = cb + SU_K + (lane + 1) * 80;
#pragma unroll
    for (int c8 = 0; c8 < 4; ++c8) {
      u32 kv[8];
#pragma unroll
      for (int e = 0; e < 8; ++e) kv[e] = Kd[kidx(32 * lane + 31 - 8 * c8 - e)];
      u32x4 hi, lo;
#pragma unroll
      for (int e = 0; e < 4; ++e) {
        hi[e] = __builtin_amdgcn_perm(kv[2 * e + 1], kv[2 * e], 0x05040100u);
        lo[e] = __builtin_amdgcn_perm(kv[2 * e + 1], kv[2 * e], 0x07060302u);
      }
      *(u32x4*)(kr + 16 * c8) = hi;
      if (SSM_LO) *(u32x4*)(kr + 5200 + 16 * c8) = lo;
    }
    if (lane < 8) *(u32x4*)(cb + SU_K + (lane >> 2) * 5200 + (lane & 3) * 16) = (u32x4){0u, 0u, 0u, 0u};
  }
  SST(3)
#pragma unroll
  for (int i = 0; i < 4; ++i) {
    const int l = 2 * (tid + 256 * i) + 8;
    *(u32*)(sm + 0 * SU_CH + SU_RAW + l * 2) = __builtin_amdgcn_perm(uv[i][2], uv[i][0], 0x05040100u);
    *(u32*)(sm + 1 * SU_CH + SU_RAW + l * 2) = __builtin_amdgcn_perm(uv[i][2], uv[i][0], 0x07060302u);
    *(u32*)(sm + 2 * SU_CH + SU_RAW + l * 2) = __builtin_amdgcn_perm(uv[i][3], uv[i][1], 0x05040100u);
    *(u32*)(sm + 3 * SU_CH + SU_RAW + l * 2) = __builtin_amdgcn_perm(uv[i][3], uv[i][1], 0x07060302u);
  }
  if (lane < 2) *(u32x4*)(cb + SU_RAW + lane * (4096 + 16)) = (u32x4){0u, 0u, 0u, 0u};
  LBAR();
  {
    if (lane < 20) *(u32x4*)(cb + SU_U + (lane / 5) * SU_CP + (lane % 5) * 16) = (u32x4){0u, 0u, 0u, 0u};
#pragma unroll
    for (int i = 0; i < 5; ++i) {
      const int g = lane + 64 * i;
      if (g < 257) {
        const u32x4 a = *(const u32x4*)(cb + SU_RAW + g * 16);
        const u32x2 c = *(const u32x2*)(cb + SU_RAW + g * 16 + 16);
        unsigned char* dst = cb + SU_U + 80 + g * 16;
        *(u32x4*)(dst) = a;
        *(u32x4*)(dst + SU_CP) = (u32x4){__builtin_amdgcn_alignbyte(a[1], a[0], 2), __builtin_amdgcn_alignbyte(a[2], a[1], 2), __builtin_amdgcn_alignbyte(a[3], a[2], 2), __builtin_amdgcn_alignbyte(c[0], a[3], 2)};
        *(u32x4*)(dst + 2 * SU_CP) = (u32x4){a[1], a[2], a[3], c[0]};
        *(u32x4*)(dst + 3 * SU_CP) = (u32x4){__builtin_amdgcn_alignbyte(a[2], a[1], 2), __builtin_amdgcn_alignbyte(a[3], a[2], 2), __builtin_amdgcn_alignbyte(c[0], a[3], 2), __builtin_amdgcn_alignbyte(c[1], c[0], 2)};
      }
    }
  }
  asm volatile("s_waitcnt lgkmcnt(0)" ::: "memory");
  SST(4)
  f32x16 cv0, cv1;
  {
    const int m0 = 48 - 15 + 8 * h + r;
    const int cbo = (int)(size_t)(LAS unsigned char*)cb;
    int ua = cbo + SU_U + (m0 & 3) * SU_CP + (m0 & ~3) * 2 - 32;
    const int kb = cbo + SU_K + 16 * h;
    int a1 = kb + (33 + r) * 80, a0 = kb + (1 + r) * 80, cnt = 32;
#define CRA(S, O) "ds_read_b64 " RNG("v", S, 2) ", %[ua] offset:" STR(O) "\n\t" "ds_read_b64 " RNG("v", (S) + 2, 2) ", %[ua] offset:" STR((O) + 8) "\n\t"
#define CRB(S, ad, O) "ds_read_b128 " RNG("v", S, 4) ", " ad " offset:" STR(O) "\n\t"
#define CMF(C, A, B) "v_mfma_f32_32x32x16_bf16 " RNG("v", C, 16) ", " RNG("v", A, 4) ", " RNG("v", B, 4) ", " RNG("v", C, 16) "\n\t"
#define CUPD "v_add_u32 %[ua], 64, %[ua]\n\t" "v_subrev_u32 %[a1], 80, %[a1]\n\t" "v_subrev_u32 %[a0], 80, %[a0]\n\t" "v_max_i32 %[a0], %[a0], %[kb]\n\t"
#define CHALF(S, N, O, UPD)                                                                       \
    WLG(5) CMF(64, S, (S) + 8) UPD CRA(N, O)                                                      \
    WLG(6) CMF(80, S, (S) + 16) CRB((N) + 8, "%[a0]", O) CRB((N) + 16, "%[a1]", O)                \
    WLG(6) CMF(80, (S) + 4, (S) + 8) CRA((N) + 4, (O) + 2048)                                     \
    WLG(7) CMF(64, S, (S) + 12) CRB((N) + 12, "%[a0]", (O) + 5200)                                \
    WLG(7) CMF(80, S, (S) + 20) CRB((N) + 20, "%[a1]", (O) + 5200)                                \
    CMF(80, (S) + 4, (S) + 12)
    asm volatile(
        CRA(96, 32) CRB(104, "%[a0]", 32) CRB(112, "%[a1]", 32) CRA(100, 32 + 2048) CRB(108, "%[a0]", 32 + 5200) CRB(116, "%[a1]", 32 + 5200)
        ZER(64) ZER(68) ZER(72) ZER(76) ZER(80) ZER(84) ZER(88) ZER(92)
        ".Lc%=:\n\t"
        CHALF(96, 120, 0, "")
        CHALF(120, 96, 32, CUPD)
        "s_sub_u32 %[cnt], %[cnt], 1\n\t"
        "s_cmp_lg_u32 %[cnt], 0\n\t"
        "s_cbranch_scc1 .Lc%=\n\t"
        "s_nop 7\n\t" "s_nop 3\n\t" WLG(0)
        : "=&{v[64:79]}"(cv0), "=&{v[80:95]}"(cv1), [ua] "+v"(ua), [a0] "+v"(a0), [a1] "+v"(a1), [cnt] "+s"(cnt)
        : [kb] "v"(kb)
        : CL96, CL112, CL120, CL8(128, 129, 130, 131, 132, 133, 134, 135), CL8(136, 137, 138, 139, 140, 141, 142, 143),
          CL8(144, 145, 146, 147, 148, 149, 150, 151), CL8(152, 153, 154, 155, 156, 157, 158, 159), "scc", "memory");
  }
  SST(5)
  {
    u16* yo = (u16*)(cb + SU_K);
#pragma unroll
    for (int g = 0; g < 2; ++g)
#pragma unroll
      for (int i = 0; i < 16; i += 4) {
        const int rr = 2 * i + 4 * h, a = 32 * g + r;
        const u32x2 uu = *(const u32x2*)(cb + SU_U + (48 + 32 * a + rr) * 2);
        float y[4];
#pragma unroll
        for (int e = 0; e < 4; ++e) {
          const float uf = __uint_as_float((e & 1) ? (uu[e >> 1] & 0xffff0000u) : (uu[e >> 1] << 16));
          y[e] = rbf_hw(g ? cv1[i + e] : cv0[i + e]) + rbf_hw(uf * dsk);
        }
        *(u32x2*)(yo + a * 40 + rr) = (u32x2){pack2(y[0], y[1]), pack2(y[2], y[3])};
      }
  }
  LBAR();
  SST(6)
#pragma unroll
  for (int i = 0; i < 4; ++i) {
    const int l = 2 * (tid + 256 * i);
    u32 c[4];
#pragma unroll
    for (int j = 0; j < 4; ++j) c[j] = *(const u32*)(sm + j * SU_CH + SU_K + ((l >> 5) * 40 + (l & 31)) * 2);
    __builtin_amdgcn_raw_buffer_store_b128((u32x4){__builtin_amdgcn_perm(c[1], c[0], 0x05040100u), __builtin_amdgcn_perm(c[3], c[2], 0x05040100u),
                                                   __builtin_amdgcn_perm(c[1], c[0], 0x07060302u), __builtin_amdgcn_perm(c[3], c[2], 0x07060302u)},
                                           rst, l * 8, SSM_L * SSM_D * 2 + d0 * 4096, 16);
  }
  asm volatile("s_waitcnt vmcnt(0)" ::: "memory");
  LBAR();
  if (tid == 0) __hip_atomic_fetch_add((u32*)(xb + XYDN), 1u, RLX_AGENT);
  SST(7)
}

#define L_TOTAL 163840
__device__ __forceinline__ int ldw(__amdgpu_buffer_rsrc_t rs, int off) {
  asm volatile("" ::: "memory");
  return __builtin_amdgcn_readfirstlane(__builtin_amdgcn_raw_buffer_load_b32(rs, 0, off, 16));
}
#define GF(rt) "v[64+8*(xk&7)+4*" #rt ":64+8*(xk&7)+4*" #rt "+3]"
#define GW2 "v[128+4*xk:128+4*xk+3]"
#define GW3 "v[24+4*(xk&1):24+4*(xk&1)+3]"
#define GLOAD(S, K) "s_xor_b32 %[tq], " S ", 1024*(" K ")\n\t"
#define GTR(K) "ds_read_b128 v[24+4*((" K ")&1):24+4*((" K ")&1)+3], %[wl] offset:1024*((" K ")&31)\n\t"
#define GPACE 0
#define YAUX 16
#define UDELAY 100
#define GNOSSM 1
#define GNG 3
#define GS0 22
__device__ __forceinline__ void g_role(unsigned char* lds, const u16* __restrict__ Wo, u16* __restrict__ out, __amdgpu_buffer_rsrc_t rsx,
                                       __amdgpu_buffer_rsrc_t rsf, int fb, u32 tag, int grp, int sl, int w) {
  const int lane = fresh_lane();
  const int lr = lane & 15, lg = lane >> 4;
  const int rot = (2 * sl + 8 * w) & 31;
  const int ldsb = (int)(size_t)(LAS unsigned char*)lds;
  u32x32 xw[8], w2[4];
  LBAR();
  {
    const __amdgpu_buffer_rsrc_t rw = __builtin_amdgcn_make_buffer_rsrc((void*)Wo, 0, DM * HD * 2, 0x00020000);
    const int vq = (lane >> 3) * (DM * 2) + sl * 128 + (lane & 7) * 16;
    const int sq = (1024 * w + rot * 32) * (DM * 2);
#define GSLOT(c) ((c) < 8 ? (((c) & 3) == 3 ? 131072 + w * 8192 : w * 32768 + 8192 * (((c) & 3) + 1)) : (((c) - 8) % 3 == 2 ? 131072 + w * 8192 : w * 32768 + 8192 * (((c) - 8) % 3 + 2)))
#define GISSUE(c) _Pragma("unroll") for (int j = 0; j < 8; ++j)                                                        \
      asm volatile("s_mov_b32 m0, %0\n\ts_nop 3\n\tbuffer_load_dwordx4 %1, %2, %3 offen lds"                           \
                   :: "s"(ldsb + GSLOT(c) + j * 1024), "v"(vq), "s"(rw), "s"(sq ^ ((64 * (c) + 8 * j) * DM * 2)) : "memory");
    GISSUE(0) GISSUE(1) GISSUE(2) GISSUE(3)
    const int q = lr >> 2, p = lr & 3;
    u32x4 w3[16];
#pragma unroll
    for (int c = 0; c < 16; ++c) {
      if (c < 5) waitvm<24>(); else if (c < 14) waitvm<16>(); else if (c == 14) waitvm<8>(); else waitvm<0>();
      u32x4 f3[2];
#pragma unroll
      for (int s = 0; s < 2; ++s)
#pragma unroll
        for (int t = 0; t < 4; ++t) {
          const unsigned char* base = lds + GSLOT(c) + (32 * s + 8 * lg + q) * 128 + (t < 2 ? p * 16 + t * 8 : t * 32 + p * 8);
          const u32x2 t0 = __builtin_bit_cast(u32x2, __builtin_amdgcn_ds_read_tr16_b64_v4i16((LAS v4i16_t*)(base)));
          const u32x2 t1 = __builtin_bit_cast(u32x2, __builtin_amdgcn_ds_read_tr16_b64_v4i16((LAS v4i16_t*)(base + 512)));
          const int o = t < 2 ? ((2 * c + s) * 2 + t) * 4 : (2 * c + s) * 4;
          if (t < 2) { xw[o >> 5][(o & 31) + 0] = t0[0]; xw[o >> 5][(o & 31) + 1] = t0[1]; xw[o >> 5][(o & 31) + 2] = t1[0]; xw[o >> 5][(o & 31) + 3] = t1[1]; }
          else if (t == 2) { w2[o >> 5][(o & 31) + 0] = t0[0]; w2[o >> 5][(o & 31) + 1] = t0[1]; w2[o >> 5][(o & 31) + 2] = t1[0]; w2[o >> 5][(o & 31) + 3] = t1[1]; }
          else f3[s] = (u32x4){t0[0], t0[1], t1[0], t1[1]};
        }
      asm volatile("s_waitcnt lgkmcnt(0)" ::: "memory");
      if (c < 8) { *(u32x4*)(lds + w * 32768 + (2 * c) * 1024 + lane * 16) = f3[0]; *(u32x4*)(lds + w * 32768 + (2 * c + 1) * 1024 + lane * 16) = f3[1]; }
      else { w3[2 * (c - 8)] = f3[0]; w3[2 * (c - 8) + 1] = f3[1]; }
      if (GPACE) __builtin_amdgcn_s_sleep(GPACE);
      if (c < 4) { GISSUE(c + 4) } else if (c >= 5 && c < 13) { GISSUE(c + 3) }
    }
#pragma unroll
    for (int i = 0; i < 16; ++i) *(u32x4*)(lds + w * 32768 + (16 + i) * 1024 + lane * 16) = w3[i];
    LBAR();
  }
  wait_start(rsx, fb, tag, true);
  const int wl = ldsb + w * 32768 + lane * 16;
  const __amdgpu_buffer_rsrc_t rso = __builtin_amdgcn_make_buffer_rsrc((void*)out, 0, S_LEN * OUTLD * 2, 0x00020000);
  const int rw_ = ldsb + 131072 + w * 8192 + lane * 16, rr_ = ldsb + 131072 + w * 2048 + lane * 16;
  const int oa = ((16 * (w & 1) + lr) * OUTLD + 64 * sl + (w < 2 ? 8 * lg : 32 + 4 * lg)) * 2, ob = oa + (w < 2 ? 8 : 32);
  const int hq = (grp + GNG - (62 - GS0) % GNG) % GNG - 1, hf = hq < 2 ? hq : -1;
#define GRW(i) "ds_write_b128 %[rw], " RNG("v", 32 + 4 * (i), 4) " offset:" STR(1024 * (i)) "\n\t"
#define GRR(i) "ds_read_b128 " RNG("v", 32 + 4 * (i), 4) ", %[rr] offset:" STR(8192 * ((i) >> 1) + 1024 * ((i) & 1)) "\n\t"
#define GNXT(D, S) "s_cmp_lt_u32 " S ", " STR(GS0) "\n\t" "s_cselect_b32 " D ", 1, " STR(GNG) "\n\t" "s_add_u32 " D ", " D ", " S "\n\t"
#define GDN(S) "s_min_u32 s41, " S ", 63\n\t" "s_lshl_b32 s41, s41, 5\n\t" "v_add_u32 v23, s41, %[u0]\n\t" "v_lshrrev_b32 v22, 6, v23\n\t" \
               "v_and_b32 v23, 63, v23\n\t" "v_lshlrev_b32 v23, 7, v23\n\t" "s_lshr_b32 s41, s41, 6\n\t" "s_max_u32 s41, s41, 29\n\t" "s_lshl_b32 s41, s41, 13\n\t" "s_add_u32 s41, s41, %[pa]\n\t" \
               "buffer_load_dword v23, v23, %[rs], s41 offen sc1\n\t"
#define GOK(D) "v_cmp_le_i32 vcc, v23, v22\n\t" "s_cmp_eq_u64 vcc, 0\n\t" "s_cselect_b32 " D ", 1, 0\n\t"
static_assert(NAW == 64, "shifts above");
#define GEPI(MID, OA, OB) \
      GRW(0) GRW(1) GRW(2) GRW(3) GRW(4) GRW(5) GRW(6) GRW(7) \
      MID \
      WLG(0) "s_barrier\n\t" \
      GRR(0) GRR(1) GRR(2) GRR(3) GRR(4) GRR(5) GRR(6) GRR(7) \
      WLG(4) PKA(32, 32, 40) PKA(34, 34, 42) PKA(36, 36, 44) PKA(38, 38, 46) \
      WLG(0) PKA(48, 48, 56) PKA(50, 50, 58) PKA(52, 52, 60) PKA(54, 54, 62) \
      PKA(32, 32, 48) PKA(34, 34, 50) PKA(36, 36, 52) PKA(38, 38, 54) \
      "v_cvt_pk_bf16_f32 v20, v32, v33\n\t" "v_cvt_pk_bf16_f32 v21, v34, v35\n\t" "v_cvt_pk_bf16_f32 v18, v36, v37\n\t" "v_cvt_pk_bf16_f32 v19, v38, v39\n\t" \
      "buffer_store_dwordx2 v[20:21], " OA ", %[ro], s42 offen\n\t" \
      "buffer_store_dwordx2 v[18:19], " OB ", %[ro], s42 offen\n\t"
#define GH "v[64+4*(xk&15):64+4*(xk&15)+3]"
  asm volatile(
      "s_nop 4\n\t"
      "v_add_u32 v16, 131072, %[va]\n\t" "v_sub_u32 v15, %[ob], %[oa]\n\t" "v_add_u32 v15, v15, %[oc]\n\t"
      "s_mov_b32 s36, %[s0]\n\t" "s_mov_b32 s43, " STR(SPIN_CAP) "\n"
      ".Lp%=:\n\t"
      GDN("s36") WVM(0) GOK("s40")
      "s_cbranch_scc1 .Lr%=\n\t"
      "s_sleep 2\n\t" "s_sub_u32 s43, s43, 1\n\t" "s_cmp_gt_i32 s43, 0\n\t" "s_cbranch_scc1 .Lp%=\n"
      ".Lr%=:\n\t"
      GNXT("s37", "s36") GDN("s37")
      "s_lshl_b32 s38, s36, 18\n\t" "s_add_u32 s38, s38, %[gb]\n\t"
      ".set xk, 0\n\t.rept 8\n\t" GLOAD("s38", "xk")
      "buffer_load_dwordx4 " GF(0) ", %[va], %[rf], %[tq] offen sc1\n\t" "buffer_load_dwordx4 " GF(1) ", v16, %[rf], %[tq] offen sc1\n\t"
      ".set xk, xk+1\n\t.endr\n\t"
      WVM(16) GOK("s40")
      ".Lb%=:\n\t"
      GNXT("s37", "s36") GNXT("s41", "s37") GDN("s41") GTR("0")
      "s_cmp_lt_u32 s37, 63\n\t" "s_cselect_b32 s40, s40, 0\n\t"
      "s_cmp_lg_u32 s40, 0\n\t" "s_cselect_b32 s39, s37, s36\n\t"
      "s_lshl_b32 s39, s39, 18\n\t" "s_add_u32 s39, s39, %[gb]\n\t" "s_lshl_b32 s38, s36, 18\n\t" "s_add_u32 s38, s38, %[gb]\n\t"
      "s_lshl_b32 s42, s36, 17\n\t"
      "buffer_load_dword v17, %[vt], %[rf], s39 offen sc1\n\t"
      ".set xk, 0\n\t.rept 32\n\t"
      "s_waitcnt vmcnt(14)\n\t"
      XM1(XA(32, 0), XW(0, 0), GF(0), 1) GTR("xk+1") XM1(XA(32, 4), XW(0, 1), GF(0), 1)
      ".if xk < 24\n\t" GLOAD("s38", "xk+8") ".else\n\t" GLOAD("s39", "xk-24") ".endif\n\t"
      XM1(XA(32, 8), XW(0, 0), GF(1), 1) XM1(XA(32, 12), XW(0, 1), GF(1), 1)
      XM1(XA(32, 16), GW2, GF(0), 1) "s_waitcnt lgkmcnt(1)\n\t" XM1(XA(32, 20), GW3, GF(0), 1)
      "buffer_load_dwordx4 " GF(0) ", %[va], %[rf], %[tq] offen sc1\n\t"
      XM1(XA(32, 24), GW2, GF(1), 1) XM1(XA(32, 28), GW3, GF(1), 1)
      "buffer_load_dwordx4 " GF(1) ", v16, %[rf], %[tq] offen sc1\n\t"
      ".if xk == 16\n\t" "s_barrier\n\t" ".endif\n\t"
      ".set xk, xk+1\n\t.endr\n\t"
      GEPI(GOK("s41"), "%[oa]", "%[ob]")
      "s_mov_b32 s36, s37\n\t"
      "s_cmp_lg_u32 s40, 0\n\t" "s_mov_b32 s40, s41\n\t" "s_cbranch_scc1 .Lb%=\n\t"
      "s_cmp_lt_u32 s36, 63\n\t" "s_cbranch_scc1 .Lp%=\n\t"
      "s_cmp_lt_i32 %[hf], 0\n\t" "s_cbranch_scc1 .Le%=\n\t"
      "s_mov_b32 s36, 63\n"
      ".Lh%=:\n\t"
      GDN("s36") WVM(0) GOK("s40")
      "s_cbranch_scc1 .Li%=\n\t"
      "s_sleep 2\n\t" "s_sub_u32 s43, s43, 1\n\t" "s_cmp_gt_i32 s43, 0\n\t" "s_cbranch_scc1 .Lh%=\n"
      ".Li%=:\n\t"
      "s_lshl_b32 s38, %[hf], 17\n\t" "s_add_u32 s38, s38, 63*262144\n\t" "s_add_u32 s38, s38, %[gb]\n\t"
      ".set xk, 0\n\t.rept 16\n\t" GLOAD("s38", "xk") "buffer_load_dwordx4 " GH ", %[va], %[rf], %[tq] offen sc1\n\t" ".set xk, xk+1\n\t.endr\n\t"
      GTR("0")
      "s_lshl_b32 s42, %[hf], 16\n\t" "s_add_u32 s42, s42, 63*131072\n\t"
      ".set xk, 0\n\t.rept 32\n\t"
      ".if xk < 17\n\t" "s_waitcnt vmcnt(15)\n\t" ".else\n\t" "s_waitcnt vmcnt(31-xk)\n\t" ".endif\n\t"
      XM1(XA(32, 0), XW(0, 0), GH, 1) GTR("xk+1") XM1(XA(32, 4), XW(0, 1), GH, 1)
      XM1(XA(32, 16), GW2, GH, 1) "s_waitcnt lgkmcnt(1)\n\t" XM1(XA(32, 20), GW3, GH, 1)
      ".if xk < 16\n\t" GLOAD("s38", "xk+16") "buffer_load_dwordx4 " GH ", %[va], %[rf], %[tq] offen sc1\n\t" ".endif\n\t"
      ".if xk == 16\n\t" "s_barrier\n\t" ".endif\n\t"
      ".set xk, xk+1\n\t.endr\n\t"
      "s_nop 7\n\t"
      GEPI("", "%[oc]", "v15")
      ".Le%=:\n\t"
      WVM(0)
      : [tq] "=&s"(sl)
      : [rs] "s"(rsx), [s0] "s"(__builtin_amdgcn_readfirstlane(grp ? GS0 + grp : 0)), [gb] "s"(FO_O - 32768 + (32 * w + rot) * 1024), [va] "v"(lane * 16 + 32768),
        [rf] "s"(rsf), [wl] "v"(wl), [rw] "v"(rw_), [rr] "v"(rr_), [oa] "v"(oa), [ob] "v"(ob), [ro] "s"(rso), [u0] "v"(8 * w + (lane & 7)), [pa] "s"(PGO_A - 29 * 8192), [hf] "s"(__builtin_amdgcn_readfirstlane(hf)), [oc] "v"((w & 1) ? OOR - 64 : oa), [vt] "v"((sl >> 3) * 131072 + 32768 + (sl & 7) * 4096 + lane * 64 - rot * 1024),
        "{a[0:31]}"(xw[0]), "{a[32:63]}"(xw[1]), "{a[64:95]}"(xw[2]), "{a[96:127]}"(xw[3]),
        "{a[128:159]}"(xw[4]), "{a[160:191]}"(xw[5]), "{a[192:223]}"(xw[6]), "{a[224:255]}"(xw[7]),
        "{v[128:159]}"(w2[0]), "{v[160:191]}"(w2[1]), "{v[192:223]}"(w2[2]), "{v[224:255]}"(w2[3])
      : "v15", "v16", "v17", "v18", "v19", "v20", "v21", "v22", "v23", "vcc", CL8(24, 25, 26, 27, 28, 29, 30, 31), CL8(32, 33, 34, 35, 36, 37, 38, 39), CL8(40, 41, 42, 43, 44, 45, 46, 47),
        CL8(48, 49, 50, 51, 52, 53, 54, 55), CL8(56, 57, 58, 59, 60, 61, 62, 63), CL64, CL72, CL80, CL88, CL96, CL112, CL120,
        "s36", "s37", "s38", "s39", "s40", "s41", "s42", "s43", "scc", "memory");
}
static_assert(4 * SU_CH <= L_TOTAL && 4 * XSLOT <= L_TOTAL, "lds");
__global__ void __launch_bounds__(256, 1)
main_kernel(const u16* __restrict__ x, const u16* __restrict__ mem0, const u16* __restrict__ Wq, const u16* __restrict__ Wk, const u16* __restrict__ Wv, const u16* __restrict__ Wo,
            unsigned char* ws, const u16* __restrict__ u, const u16* __restrict__ A_re, const u16* __restrict__ A_im, const u16* __restrict__ B_re,
            const u16* __restrict__ B_im, const u16* __restrict__ C_re, const u16* __restrict__ C_im,
            const u16* __restrict__ log_dt, const u16* __restrict__ Dskip, u16* __restrict__ out
            ) {
  __shared__ __attribute__((aligned(8192))) unsigned char lds[L_TOTAL];
  const int w = __builtin_amdgcn_readfirstlane(threadIdx.x >> 6);
  u64 pc0;
  int clen;
  {
    asm volatile("s_getpc_b64 %0\n.Lkpc0:\n\ts_mov_b32 %1, .Lkpc1-.Lkpc0" : "=s"(pc0), "=s"(clen));
  }
#define LOCAL_IDS const int lane = fresh_lane(), tid = w * 64 + lane; (void)tid;
  const int si = (blockIdx.x >> 3) * 4 + (blockIdx.x & 3);
  unsigned char* xb = (unsigned char*)(((size_t)ws + XPAGE - 1) & ~(size_t)(XPAGE - 1));
  const int fb = (int)(ws + WS_F - xb);
  u32 tag;
  asm volatile("s_load_dword %0, %1, 0x0\n\ts_waitcnt lgkmcnt(0)\n\ts_add_u32 %0, %0, 256" : "=s"(tag) : "s"(ws + WS_F) : "scc", "memory");
  const __amdgpu_buffer_rsrc_t rsx = __builtin_amdgcn_make_buffer_rsrc(xb, 0, fb + (int)WS_F_SZ, 0x00020000);
  const __amdgpu_buffer_rsrc_t rsf = __builtin_amdgcn_make_buffer_rsrc(ws + WS_QX, 0, (int)(4 * WS_QKV_SZ + 2 * WS_KM_SZ), 0x00020000);
  const __amdgpu_buffer_rsrc_t rst = __builtin_amdgcn_make_buffer_rsrc(ws + WS_UT, 0, 2 * SSM_L * SSM_D * 2 + SSM_D * 3072, 0x00020000);
  if ((blockIdx.x & 7) < 4) {
    { LOCAL_IDS
    r_role(lds, si, mem0, Wq, Wk, Wv, Wo, fb, tag, pc0, clen, rsx, rsf, rst, SsmPtrs{A_re, A_im, B_re, B_im, C_re, C_im, log_dt}, tid, lane, w
           ); }
    LOCAL_IDS
    {
      for (int it = 0; it < SPIN_CAP; ++it) {
        if (ldw(rsx, XYDN) >= 255) break;
        asm volatile("s_sleep 8" ::: "memory");
      }
      asm volatile("" ::: "memory");
      asm volatile(".Lkpc1:" ::: "memory");
      unsigned char* im = lds + 65536;
      const __amdgpu_buffer_rsrc_t rso = __builtin_amdgcn_make_buffer_rsrc((void*)out, 0, S_LEN * OUTLD * 2, 0x00020000);
      u32x4 t[8];
#pragma unroll
      for (int i = 0; i < 8; ++i) { const int p = i * 256 + tid; t[i] = __builtin_amdgcn_raw_buffer_load_b128(rst, (p >> 3) * 16384 + (p & 7) * 16, SSM_L * SSM_D * 2 + si * 128, 16); }
#pragma unroll
      for (int i = 0; i < 8; ++i) {
        const int p = i * 256 + tid, un = p >> 3, rp = p & 7;
        *(u32x2*)(im + (2 * rp) * 2064 + un * 8) = (u32x2){t[i][0], t[i][1]};
        *(u32x2*)(im + (2 * rp + 1) * 2064 + un * 8) = (u32x2){t[i][2], t[i][3]};
      }
      LBAR();
#pragma unroll
      for (int i = 0; i < 8; ++i) {
        const int p = i * 256 + tid;
        __builtin_amdgcn_raw_buffer_store_b128(*(const u32x4*)(im + (p >> 7) * 2064 + (p & 127) * 16), rso, ((p >> 7) * OUTLD + DM + (p & 127) * 8) * 2, si * 16 * OUTLD * 2, YAUX);
      }
    }
  } else {
    const int g = blockIdx.x & 3, li = blockIdx.x >> 3;
    const int xi = g == 0 ? (li < 16 ? li : -1) : (li < 26 + (g < 3) ? 16 + 27 * (g - 1) + li : -1);
    const int ei = g == 0 ? li - 16 : 16 + 5 * (g - 1) + (31 - li);
    if (xi >= 0) {
      LOCAL_IDS
      const int head = xi & 31, mi = xi >> 5;
      u32* prog = (u32*)(xb + PGO_X + xi * 128);
      if (mi == 1) x_role<true>(lds, x, Wv, FO_V, head, rsf, prog, tag, lane, w);
      else x_role<false>(lds, x, mi == 0 ? Wk : Wq, mi == 0 ? FO_K : FO_Q, head, rsf, prog, tag, lane, w);
    } else {
      LOCAL_IDS
      u32 acc = 0;
#pragma unroll
      for (int i = 0; i < 4; ++i) acc += *(const u32*)(x + (size_t)((i * 32 + ei) * 256 + tid) * 64);
#define UV 0
#define WWARM(n, first) {    \
        const __amdgpu_buffer_rsrc_t rwo = __builtin_amdgcn_make_buffer_rsrc((void*)Wo, 0, DM * HD * 2, 0x00020000); \
        const int ldsb = (int)(size_t)(LAS unsigned char*)lds; \
        _Pragma("unroll 1") for (int c = 0; c < (n); ++c) { dma16(rwo, lane * 16, ((first) + c) * 16384, 1024, ldsb + w * 32768 + (c & 1) * 16384); waitvm<32>(); } }
      if (UV == 0 || (UV == 1 && ei < 16)) WWARM(4, (ei * 4 + w) * 4)
      if (UV == 2 && ei < 16) WWARM(8, (ei * 4 + w) * 8)
      if (ei == 0 && tid < 16) acc += *(const u32*)(Dskip + tid * 64);
      asm volatile("" :: "v"(acc));
      if (ei >= 16) {
      { const u32 t0 = rt32(); for (int i = 0; i < 1000 && (int)(rt32() - t0) < UDELAY; ++i) asm volatile("s_sleep 16" ::: "memory"); }
      asm volatile("s_waitcnt vmcnt(0)\n\ts_barrier" ::: "memory");
      for (int ps = 0; ps < 2; ++ps) {
      const int r0 = ((ei - 16) * 2 + ps) * 64;
#pragma unroll
      for (int hb = 0; hb < 2; ++hb) {
        u32x4 t[16];
#pragma unroll
        for (int i = 0; i < 16; ++i) t[i] = *(const u32x4*)(u + (size_t)r0 * SSM_D + ((hb * 16 + i) * 256 + tid) * 8);
#pragma unroll
        for (int i = 0; i < 16; ++i) { const int p = (hb * 16 + i) * 256 + tid; *(u32x4*)(lds + (p >> 7) * 2064 + (p & 127) * 16) = t[i]; }
      }
      LBAR();
      for (int i = 0; i < 32; ++i) {
        const int p = i * 256 + tid, un = p >> 5, rp = p & 31;
        const u32x2 a = *(const u32x2*)(lds + (2 * rp) * 2064 + un * 8), c = *(const u32x2*)(lds + (2 * rp + 1) * 2064 + un * 8);
        __builtin_amdgcn_raw_buffer_store_b128((u32x4){a[0], a[1], c[0], c[1]}, rst, un * 16384 + rp * 16, r0 * 8, 16);
      }
      asm volatile("s_waitcnt vmcnt(0) lgkmcnt(0)\n\ts_barrier" ::: "memory");
      }
      if (UV == 1) { WWARM(4, (ei * 4 + w) * 4) waitvm<0>(); }
      wait_start(rsx, fb, tag, true);
      if (tid == 0) __hip_atomic_fetch_add((u32*)(xb + XUDN), 1u, RLX_AGENT);
      }
    }
    asm volatile("s_waitcnt lgkmcnt(0)\n\ts_barrier" :: "s"(tag) : "memory");
    if (w == 0 && fresh_lane() == 0) __builtin_amdgcn_raw_buffer_store_b32(tag, rsx, 0, PGO_S + si * 128, 16);
    const bool isg = g == 0 ? li >= 16 : (g >= 5 - GNG && li < 16);
    if (xi >= 0 && !(GNOSSM && isg)) {
      LOCAL_IDS
      bool uok = false;
      wait_start(rsx, fb, tag, true);
      while (true) {
        LBAR();
        if (tid == 0) *(u32*)(lds + L_TOTAL - 16) = __hip_atomic_fetch_add((u32*)(xb + XTKT), 1u, RLX_AGENT) + 1u;
        LBAR();
        const int un = __builtin_amdgcn_readfirstlane(*(const u32*)(lds + L_TOTAL - 16));
        if (un >= 256) break;
        ssm_unit(lds, 4 * un, rst, rsx, xb, fb, uok, Dskip, tid, lane, w);
        if (isg) break;
      }
    }
    if (isg) {
      g_role(lds, Wo, out, rsx, rsf, fb, tag, g ? g + GNG - 4 : 0, li & 15, w);
    }
    else {
      const int lane = fresh_lane();
      int sh = (ei - 16) * 4 + w, lim = (NSEG - 4) * NH, wi = sh, dn = 0;
      bool go = xi < 0;
      while (true) {
        if (go) a_role(rsx, rsf, sh, lim, wi, dn, lane, fb, tag);
        if (xi >= 0 && (w >= 2 || go)) break;
        const int t = __builtin_amdgcn_readfirstlane(lane == 0 ? __hip_atomic_fetch_add((u32*)(xb + XTK2), 1u, RLX_AGENT) + 1u : 0u);
        if (t >= 128) break;
        wi = 64 + t; sh = (NSEG - 4) * NH + t; lim = sh + 1; dn = 31; go = true;
      }
    }
  }
}

extern "C" void kernel_launch(void* const* d_in, const int* in_sizes, int n_in, void* d_out, int out_size,
                              void* d_ws, size_t ws_size, hipStream_t stream) {
  const u16* x = (const u16*)d_in[0];
  const u16* mem0 = (const u16*)d_in[1];
  const u16* Wq = (const u16*)d_in[2];
  const u16* Wk = (const u16*)d_in[3];
  const u16* Wv = (const u16*)d_in[4];
  const u16* Wo = (const u16*)d_in[5];
  const u16* u = (const u16*)d_in[7];
  const u16* A_re = (const u16*)d_in[8];
  const u16* A_im = (const u16*)d_in[9];
  const u16* B_re = (const u16*)d_in[10];
  const u16* B_im = (const u16*)d_in[11];
  const u16* C_re = (const u16*)d_in[12];
  const u16* C_im = (const u16*)d_in[13];
  const u16* log_dt = (const u16*)d_in[14];
  const u16* Dskip = (const u16*)d_in[15];
  u16* out = (u16*)d_out;
  char* ws = (char*)d_ws;

  main_kernel<<<256, 256, 0, stream>>>(x, mem0, Wq, Wk, Wv, Wo, (unsigned char*)ws, u, A_re, A_im, B_re, B_im, C_re, C_im, log_dt, Dskip, out
      );
}
```

```cpp
#define TUNE
#define DIAG 0
#include <hip/hip_runtime.h>
#include <hip/hip_bf16.h>
#include <stdint.h>

#define S_LEN 2048
#define DM 1024
#define NH 32
#define DH 128
#define HD 4096
#define MEM 8
#define SEG 32
#define NSEG 64
#define LTOK 40
#define SSM_L 2048
#define SSM_D 1024
#define SSM_N 128
#define OUTLD 2048

typedef unsigned short u16;
typedef unsigned int u32;
typedef unsigned long long u64;
typedef __attribute__((ext_vector_type(8))) short bf16x8;
typedef __attribute__((ext_vector_type(4))) float f32x4;
typedef __attribute__((ext_vector_type(4))) unsigned int u32x4;
typedef __attribute__((ext_vector_type(2))) unsigned int u32x2;
typedef __bf16 bf16x2_t __attribute__((ext_vector_type(2)));
typedef short v4i16_t __attribute__((ext_vector_type(4)));

#define SENT 0xFFFFFFFFu
#define RLX_AGENT __ATOMIC_RELAXED, __HIP_MEMORY_SCOPE_AGENT

__device__ __forceinline__ float bf2f(u16 v) { return __uint_as_float(((u32)v) << 16); }
__device__ __forceinline__ u16 f2bf(float f) {
  u32 u = __float_as_uint(f);
  u32 r = (u + 0x7fffu + ((u >> 16) & 1u)) >> 16;
  if ((u & 0x7fffffffu) > 0x7f800000u) r = 0x7fc0u;
  return (u16)r;
}
__device__ __forceinline__ float rbf(float f) { return bf2f(f2bf(f)); }

#define TR 92
#define TP 176
#define TDB 60
#define TDC 108
#define NR1 4
#define XPAGE (2 << 20)
#define XRING 3
#define XLP(p, L) ((((L) >> 13) << 15) + ((p) << 14) + ((L) & 8191))
#define XO_R3 131072
#define XR1A(x, L) XLP((x) & 1, ((x) >> 1) * 16384 + (L))
#define XR3A(rg, L) (XO_R3 + XLP(rg, L))
#define XSB(s) (((s) % XRING) == 2 ? 1048576 : ((s) % XRING) * 262144)
#define XR2B 524288
#define XR2S 1048576
#define XR2(j) (((((j) & 31) + (((j) >> 6) << 5)) / 6 << 15) + (((((j) >> 5) & 1) ^ 1) << 14) + ((((j) & 31) + (((j) >> 6) << 5)) % 6) * 1280)
#define XSYN (1048576 + 262144)
#define PGO_R (XSYN + 32768)
#define XCAL (PGO_R + 32768)
static_assert(XCAL + 16384 <= XPAGE, "exchange page");
#define WS_X 0ull
#define NPG 2
#define WS_X_SZ ((u64)NPG * XPAGE)
#define WS_F (WS_X + WS_X_SZ)
#define XEPO (fb)
#define XSTA (fb + 128)
#define PGO_X (fb + 256)
#define PGO_Y (PGO_X + 96 * 128)
#define PGO_S (PGO_Y + 128 * 128)
#define PGO_D (PGO_S + 128 * 128)
#define PGO_A (PGO_D + 128 * 128)
#define XTKT (PGO_A + 192 * 128)
#define XUDN (XTKT + 128)
#define XYDN (XUDN + 128)
#define XTK2 (XYDN + 128)
#define XSTR(x) (XTK2 + 128 + (x) * 128)
#define WS_F_SZ ((u64)(2 + 96 + 128 + 128 + 128 + 192 + 8) * 128)
#define WS_XCH_END (WS_F + 131072)
#define WS_QX (WS_XCH_END)
#define WS_QKV_SZ ((u64)S_LEN * HD * 2)
#define WS_KX (WS_QX + WS_QKV_SZ)
#define WS_VX (WS_KX + WS_QKV_SZ)
#define WS_OX (WS_VX + WS_QKV_SZ)
#define WS_KM (WS_OX + WS_QKV_SZ)
#define WS_KM_SZ ((u64)NSEG * 8 * HD * 2)
#define WS_VM (WS_KM + WS_KM_SZ)
#define WS_UT (WS_VM + WS_KM_SZ)
#define WS_YT (WS_UT + (u64)SSM_L * SSM_D * 2)
#define WS_KD (WS_YT + (u64)SSM_L * SSM_D * 2)
#define WS_TOTAL (WS_KD + (u64)SSM_D * 3072)

#define SPIN_CAP 400000
#define TRY_CAP 20000
#define LAS __attribute__((address_space(3)))
#define LBAR() asm volatile("s_waitcnt lgkmcnt(0)\n\ts_barrier" ::: "memory")
__device__ __forceinline__ u32 max4u(u32x4 v) { return max(max(v[0], v[1]), max(v[2], v[3])); }
typedef float f32x2_t __attribute__((ext_vector_type(2)));
__device__ __forceinline__ u32 pack2(float a, float b) {
  f32x2_t v = {a, b};
  bf16x2_t r = __builtin_convertvector(v, bf16x2_t);
  return __builtin_bit_cast(u32, r);
}
__device__ __forceinline__ float rbf_hw(float a) { return __uint_as_float(pack2(a, a) & 0xffff0000u); }
__device__ __forceinline__ u32 nosent(u32 v) { return min(v, 0xFFFFFFFEu); }
template <int N> __device__ __forceinline__ void waitvm() { asm volatile("s_waitcnt vmcnt(%0)" ::"n"(N) : "memory"); }
typedef __attribute__((ext_vector_type(16))) float f32x16;
__device__ __forceinline__ u32 split_hl(float kv) {
  const u32 h = pack2(kv, kv) & 0xffffu;
  const float r1 = kv - __uint_as_float(h << 16);
  return h | (pack2(r1, r1) & 0xffff0000u);
}
__device__ __forceinline__ int kidx(int idx) {
  const int sh = idx < 256 ? 0 : (idx < 512 ? 1 : (idx < 1024 ? 2 : 3));
  const int q = sh ? ((idx + (1 << (sh - 1)) - 1 + ((idx >> sh) & 1)) >> sh) : idx;
  return q + 128 * sh;
}

__device__ u32* g_dbg;
#define FO_Q 0
#define FO_K ((int)WS_QKV_SZ)
#define FO_V ((int)(2 * WS_QKV_SZ))
#define FO_O ((int)(3 * WS_QKV_SZ))
#define FO_KM ((int)(4 * WS_QKV_SZ))
#define FO_VM ((int)(4 * WS_QKV_SZ + WS_KM_SZ))
__device__ __forceinline__ u32x4 wait_start(__amdgpu_buffer_rsrc_t rsx, int fb, u32 tag, bool slow, int xcd = 0) {
  u32x4 st;
  for (int it = 0; it < SPIN_CAP; ++it) {
    asm volatile("" ::: "memory");
    st = __builtin_amdgcn_raw_buffer_load_b128(rsx, 0, slow ? XSTA : XSTR(xcd), 16);
    if ((u32)__builtin_amdgcn_readfirstlane(st[3]) == tag) break;
    if (slow) asm volatile("s_sleep 20" ::: "memory"); else asm volatile("s_sleep 1" ::: "memory");
  }
  asm volatile("" ::: "memory");
  return st;
}
__device__ __forceinline__ int xprog(u32 v, u32 tag) { return v - tag <= 80u ? (int)(v - tag) : -1; }
__device__ __forceinline__ float xmax4(float m) {
  { auto r = __builtin_amdgcn_permlane16_swap(__float_as_uint(m), __float_as_uint(m), false, false); m = __builtin_fmaxf(__uint_as_float(r[0]), __uint_as_float(r[1])); }
  { auto r = __builtin_amdgcn_permlane32_swap(__float_as_uint(m), __float_as_uint(m), false, false); m = __builtin_fmaxf(__uint_as_float(r[0]), __uint_as_float(r[1])); }
  return m;
}
__device__ __forceinline__ float xsum4(float m) {
  { auto r = __builtin_amdgcn_permlane16_swap(__float_as_uint(m), __float_as_uint(m), false, false); m = __uint_as_float(r[0]) + __uint_as_float(r[1]); }
  { auto r = __builtin_amdgcn_permlane32_swap(__float_as_uint(m), __float_as_uint(m), false, false); m = __uint_as_float(r[0]) + __uint_as_float(r[1]); }
  return m;
}
#define MF16(a, b, c) __builtin_amdgcn_mfma_f32_16x16x32_bf16(__builtin_bit_cast(bf16x8, a), __builtin_bit_cast(bf16x8, b), c, 0, 0, 0)
__device__ __forceinline__ f32x4 rnd2(f32x4 v, float sc) {
  const u32 a = pack2(v[0], v[1]), b = pack2(v[2], v[3]);
  const u32 c = pack2(__uint_as_float(a << 16) * sc, __uint_as_float(a & 0xffff0000u) * sc), d = pack2(__uint_as_float(b << 16) * sc, __uint_as_float(b & 0xffff0000u) * sc);
  return (f32x4){__uint_as_float(c << 16), __uint_as_float(c & 0xffff0000u), __uint_as_float(d << 16), __uint_as_float(d & 0xffff0000u)};
}
struct XaOps { u32x4 q[2][4], k[2][4], v[8]; };
__device__ __forceinline__ void xa_load(XaOps& o, __amdgpu_buffer_rsrc_t rsf, int sh, int lane) {
#pragma unroll
  for (int i = 0; i < 8; ++i) {
    o.q[i >> 2][i & 3] = __builtin_amdgcn_raw_buffer_load_b128(rsf, lane * 16, FO_Q + (sh * 8 + i) * 1024, 16);
    o.k[i >> 2][i & 3] = __builtin_amdgcn_raw_buffer_load_b128(rsf, lane * 16, FO_K + (sh * 8 + i) * 1024, 16);
  }
#pragma unroll
  for (int dt = 0; dt < 8; ++dt) o.v[dt] = __builtin_amdgcn_raw_buffer_load_b128(rsf, lane * 16, FO_V + (sh * 8 + dt) * 1024, 16);
}
__device__ __forceinline__ void xa_compute(const XaOps& o, const u32x4 (&km)[4], const u32x2 (&vm)[8], __amdgpu_buffer_rsrc_t rsf, int sh, int lane) {
  const int lr = lane & 15, lg = lane >> 4;
  const float scale = 0.08838834764831845f, L2E = 1.4426950408889634f, NEG = -1000000000.0f;
  const f32x4 z = (f32x4){0, 0, 0, 0};
  f32x4 s00 = z, s0m = z, s10 = z, s11 = z, s1m = z;
#pragma unroll
  for (int ks = 0; ks < 4; ++ks) { s00 = MF16(o.k[0][ks], o.q[0][ks], s00); s10 = MF16(o.k[0][ks], o.q[1][ks], s10); s11 = MF16(o.k[1][ks], o.q[1][ks], s11); }
#pragma unroll
  for (int ks = 0; ks < 4; ++ks) { s0m = MF16(km[ks], o.q[0][ks], s0m); s1m = MF16(km[ks], o.q[1][ks], s1m); }
  s00 = rnd2(s00, scale); s10 = rnd2(s10, scale); s11 = rnd2(s11, scale); s0m = rnd2(s0m, scale); s1m = rnd2(s1m, scale);
  asm volatile("" : "+v"(s00), "+v"(s11), "+v"(s0m), "+v"(s1m));
#pragma unroll
  for (int e = 0; e < 4; ++e) {
    const bool msk = 4 * lg + e > lr;
    s00[e] = msk ? NEG : s00[e]; s11[e] = msk ? NEG : s11[e];
    s0m[e] = lg >= 2 ? -INFINITY : s0m[e]; s1m[e] = lg >= 2 ? -INFINITY : s1m[e];
  }
  float m0 = NEG, m1 = -INFINITY;
#pragma unroll
  for (int e = 0; e < 4; ++e) { m0 = __builtin_fmaxf(m0, __builtin_fmaxf(s00[e], s0m[e])); m1 = __builtin_fmaxf(__builtin_fmaxf(m1, s10[e]), __builtin_fmaxf(s11[e], s1m[e])); }
  m0 = xmax4(m0); m1 = xmax4(m1);
  float t0 = 0.f, t1 = 0.f;
#pragma unroll
  for (int e = 0; e < 4; ++e) {
    s00[e] = __builtin_amdgcn_exp2f((s00[e] - m0) * L2E); s0m[e] = __builtin_amdgcn_exp2f((s0m[e] - m0) * L2E);
    s10[e] = __builtin_amdgcn_exp2f((s10[e] - m1) * L2E); s11[e] = __builtin_amdgcn_exp2f((s11[e] - m1) * L2E); s1m[e] = __builtin_amdgcn_exp2f((s1m[e] - m1) * L2E);
    t0 += s00[e] + s0m[e]; t1 += (s10[e] + s11[e]) + s1m[e];
  }
  const float em = __builtin_amdgcn_exp2f((NEG - m0) * L2E);
  t0 += 4.f * em;
  const float r0 = __builtin_amdgcn_rcpf(xsum4(t0)), r1 = __builtin_amdgcn_rcpf(xsum4(t1));
  s00 *= r0; s0m *= r0; s10 *= r1; s11 *= r1; s1m *= r1;
  const u32x4 p0 = (u32x4){pack2(s00[0], s00[1]), pack2(s00[2], s00[3]), pack2(s0m[0], s0m[1]), pack2(s0m[2], s0m[3])};
  const u32x4 p1 = (u32x4){pack2(s10[0], s10[1]), pack2(s10[2], s10[3]), pack2(s11[0], s11[1]), pack2(s11[2], s11[3])};
  const u32x4 pm = (u32x4){0u, 0u, pack2(s1m[0], s1m[1]), pack2(s1m[2], s1m[3])};
  const u32 pe = pack2(em * r0, em * r0);
  const u32x4 px = (u32x4){0u, 0u, pe, pe};
#pragma unroll
  for (int pr = 0; pr < 4; ++pr) {
    f32x4 o0[2], o1[2];
#pragma unroll
    for (int t = 0; t < 2; ++t) {
      const int dt = 2 * pr + t;
      const u32x4 a0 = (u32x4){o.v[dt][0], o.v[dt][1], vm[dt][0], vm[dt][1]};
      o0[t] = MF16(a0, p0, z);
      o1[t] = MF16(o.v[dt], p1, z);
      o0[t] = MF16(o.v[dt], px, o0[t]);
      o1[t] = MF16(a0, pm, o1[t]);
    }
    const int ob = FO_O + ((sh >> 5) * 256 + (sh & 31) * 4 + pr) * 1024;
    __builtin_amdgcn_raw_buffer_store_b128((u32x4){pack2(o0[0][0], o0[0][1]), pack2(o0[0][2], o0[0][3]), pack2(o0[1][0], o0[1][1]), pack2(o0[1][2], o0[1][3])}, rsf, lane * 16, ob, 16);
    __builtin_amdgcn_raw_buffer_store_b128((u32x4){pack2(o1[0][0], o1[0][1]), pack2(o1[0][2], o1[0][3]), pack2(o1[1][0], o1[1][1]), pack2(o1[1][2], o1[1][3])}, rsf, lane * 16, ob + 131072, 16);
  }
}
#define NAW 64
__device__ __forceinline__ void a_role(__amdgpu_buffer_rsrc_t rsx, __amdgpu_buffer_rsrc_t rsf, int sh, int lim, int wi, int done, int lane, int fb, u32 tag) {
  const int lr = lane & 15, lg = lane >> 4;
  const int okm = (lg * 8 + (lr & 7)) * 16, ovm = lg < 2 ? (lg * 16 + lr) * 8 : 0x7ffffff0;
  int rp = 0, xp = -1, spin = 0, pub = done, miss = 0, tp = 200;
  u32 tseen = 0, tq0 = 0, tq1 = 0, tq2 = 0, tq3 = 0; (void)tq0; (void)tq1; (void)tq2; (void)tq3;
#define XA_WAITX(s_)                                                                  \
  while (xp <= ((s_) >> 5)) {                                                                                          \
    asm volatile("" ::: "memory");                                                                                     \
    int pv = min(xprog(__builtin_amdgcn_raw_buffer_load_b32(rsx, lane * 128, PGO_X, 16), tag), xprog(__builtin_amdgcn_raw_buffer_load_b32(rsx, (64 + (lane & 31)) * 128, PGO_X, 16), tag)); \
    _Pragma("unroll") for (int o_ = 1; o_ < 64; o_ *= 2) pv = min(pv, __shfl_xor(pv, o_));                             \
    xp = __builtin_amdgcn_readfirstlane(pv);                                                                           \
    if (xp > ((s_) >> 5) || ++spin > SPIN_CAP) break;                                                                  \
    asm volatile("s_sleep 16" ::: "memory");                                                                           \
  }
#define XA_PUB() if (pub != done) { pub = done; asm volatile("s_nop 4\n\tbuffer_store_dword %0, %1, %2, %3 offen sc1" :: "v"(done), "v"(lane ? 0x7ffffff0 : 0), "s"(rsx), "s"(PGO_A + wi * 128) : "memory"); }
#define XA_STEP(CUR, NXT)                                                                                              \
  {                                                                                                                    \
    const int seg = sh >> 5, nx = sh + NAW;                                                                            \
    XA_WAITX(min(nx, lim - 1))                                                                                         \
    if (rp <= seg) {                         \
      while (true) {                                                                                                   \
        int want = min(seg + 1, NSEG - 1);                                                                             \
        if (tseen) {                                                                                                   \
          const u32 due = tseen + (u32)(want - rp) * tp - (want + 1 < NSEG ? 0 : XA_LAST) + (seg + 1 < NSEG ? 0 : XA_END); \
          if ((int)(due - (u32)__builtin_amdgcn_s_memrealtime()) > 0 || miss) { asm volatile("s_waitcnt vmcnt(0)" ::: "memory"); XA_PUB() } \
          for (int i_ = 0; i_ < 4000 && (int)(due - (u32)__builtin_amdgcn_s_memrealtime()) > 0; ++i_) asm volatile("s_sleep 2" ::: "memory"); \
          if (!miss) want = min(max(want, rp + max((int)((u32)__builtin_amdgcn_s_memrealtime() - tseen), 0) / tp), NSEG - 1); \
        }                                                                                                              \
        XA_DBG4                                                                                                        \
        asm volatile("" ::: "memory");                                                                                 \
        if (seg + 1 < NSEG) {                                                                                          \
          const int r_ = __builtin_amdgcn_readfirstlane(__builtin_amdgcn_raw_buffer_load_b32(rsx, 0, pgr + want * 128, 16)); \
          if (r_ >= 0) { const u32 n_ = (u32)__builtin_amdgcn_s_memrealtime(); tseen = n_ - ((n_ - (r_ >> 16)) & 0x7fffu) + XA_LAG; tp = max(r_ & 1023, 1); rp = want; miss = 0; break; } \
        } else if (__ballot(__builtin_amdgcn_raw_buffer_load_b32(rsx, ((sh & 31) * 4 + (lane & 3)) * 128, PGO_D, 16) == SENT) == 0) break; \
        if (++spin > SPIN_CAP) break;                                                                                  \
        if (miss++ && seg + 2 < NSEG) asm volatile("s_sleep 20" ::: "memory");                                                         \
      }                                                                                                                \
    }                                                                                                                  \
    asm volatile("" ::: "memory");                                                                                     \
    XA_DBG2                                                                                                            \
    u32x4 km[4]; u32x2 vm[8];                                                                                          \
    _Pragma("unroll") for (int i = 0; i < 4; ++i) km[i] = __builtin_amdgcn_raw_buffer_load_b128(rsf, okm, FO_KM + (sh * 4 + i) * 512, 16); \
    _Pragma("unroll") for (int i = 0; i < 8; ++i) vm[i] = __builtin_amdgcn_raw_buffer_load_b64(rsf, ovm, FO_VM + (sh * 8 + i) * 256, 16);  \
                              \
    const int pk_ = min(rp + max((int)((u32)__builtin_amdgcn_s_memrealtime() - tseen), 0) / tp, NSEG - 1);              \
    const u32 pv_ = __builtin_amdgcn_raw_buffer_load_b32(rsx, 0, pgr + pk_ * 128, 16);                                 \
    asm volatile("" ::: "memory");                                                                                     \
    __builtin_amdgcn_s_waitcnt(0x0F70);                                                                   \
    asm volatile("" ::: "memory");                                                                                     \
    { const int r_ = __builtin_amdgcn_readfirstlane(pv_);                                                              \
      if (r_ >= 0) { const u32 n_ = (u32)__builtin_amdgcn_s_memrealtime(); tseen = n_ - ((n_ - (r_ >> 16)) & 0x7fffu) + XA_LAG; tp = max(r_ & 1023, 1); rp = pk_; } } \
    XA_PUB()                                   \
    if (nx < lim) xa_load(NXT, rsf, nx, lane);                                                                         \
    XA_DBG3                                                                                                            \
    __builtin_amdgcn_sched_barrier(0);                                                                                 \
    xa_compute(CUR, km, vm, rsf, sh, lane);                                                                            \
    ++done;                                                                                                            \
    XA_DBG                                                                                                             \
    sh = nx;                                                                                                           \
    if (sh >= lim) break;                                                                                              \
  }
#define XA_LAST 24
#define XA_END 0
#define XA_LAG 30
#define XA_DBG tq2 = (u32)__builtin_amdgcn_s_memrealtime();
#define XA_DBG2 tq0 = (u32)__builtin_amdgcn_s_memrealtime();
#define XA_DBG3 tq1 = (u32)__builtin_amdgcn_s_memrealtime();
#define XA_DBG4 tq3 = tq3 ? tq3 : (seg == 63 ? (u32)__builtin_amdgcn_s_memrealtime() : 0u);
  const int pgr = PGO_R + (__builtin_amdgcn_readfirstlane(wait_start(rsx, fb, tag, true)[2]) & 8192);
  XaOps A, B;
  if (sh >= NAW) {
    const int r_ = __builtin_amdgcn_readfirstlane(__builtin_amdgcn_raw_buffer_load_b32(rsx, 0, pgr + 40 * 128, 16));
    if (r_ >= 0) { const u32 n_ = (u32)__builtin_amdgcn_s_memrealtime(); tseen = n_ - ((n_ - (r_ >> 16)) & 0x7fffu) + XA_LAG; tp = max(r_ & 1023, 1); rp = 40; }
  }
  XA_WAITX(sh)
  asm volatile("" ::: "memory");
  xa_load(A, rsf, sh, lane);
  while (true) { XA_STEP(A, B) XA_STEP(B, A) }
  asm volatile("s_waitcnt vmcnt(0)" ::: "memory");
  XA_PUB()
  const int ai = wi - 128;
  if (lane == 0 && wi >= 128) { g_dbg[3600 + ai] = (u32)__builtin_amdgcn_s_memrealtime(); g_dbg[3700 + ai] = tq0; g_dbg[3800 + ai] = tq1; g_dbg[3900 + ai] = tq2; g_dbg[4000 + ai] = tq3; }
}

typedef __attribute__((ext_vector_type(32))) unsigned int u32x32;
typedef __attribute__((ext_vector_type(16))) unsigned int u32x16;
typedef __attribute__((ext_vector_type(8))) unsigned int u32x8;
#define STR2(x) #x
#define STR(x) STR2(x)
#define RNG(f, b, n) f "[" STR(b) ":" STR((b) + (n)-1) "]"
#define REG(b) "v[" STR(b) "]"
#define LR_RED 16384
#define LR_CTL LR_RED
#define LR_STG 32768
#define L_RSTAMP LR_STG
#define STAMPV(i, v)
#define EXF (NH * 8 * 1024)
#define EXK 65536
#define OOR 0x7ffffff0

__device__ __forceinline__ u32 lds_word(int addr) {
  u32 v;
  asm volatile("ds_read_b32 %0, %1\n\ts_waitcnt lgkmcnt(0)" : "=v"(v) : "v"(addr) : "memory");
  return __builtin_amdgcn_readfirstlane(v);
}
__device__ __forceinline__ int fresh_lane() {
  int l;
  asm volatile("v_mbcnt_lo_u32_b32 %0, -1, 0\n\tv_mbcnt_hi_u32_b32 %0, -1, %0" : "=v"(l));
  return l;
}
__device__ __forceinline__ u32 rt32() { return (u32)__builtin_amdgcn_s_memrealtime(); }
int g_tune[4] = {TR, TP, TDB, TDC};
#undef TR
#undef TP
#undef TDB
#undef TDC
#define TR tun_r
#define TP tun_p
#define TDB tun_b
#define TDC tun_c
#define SYO(s) (XSYN + flip + (s) * 64)
#define TKS 0.85f
#define TLB 1
#define TLC 1
#define TLP 2
#define KG0 0
#define CLS0 0u
#define APC 5
#define ADL 12
#define CALN 2
#define TE1 4
#define TE2 3
#define TE3 2
#define XTH 4
#define TS3 42
#define TE0 30
#define TD0 170
#define TD 120
#define PHASE 1
#define XSK 0
#define CLKS(T, R, X) WLG(2) "s_sub_u32 s49, " T ", " R "\n\t" "s_cmp_le_i32 s49, " STR(3 * PHASE) "\n\t" "s_cbranch_scc1 " X "\n\t"
#define CLKH(OFF) WLG(0) "s_memrealtime s[72:73]\n\t" "s_add_u32 s50, s43, s44\n\t" "s_add_u32 s50, s50, " OFF "\n\t" "s_nop 1\n\t" "s_memrealtime s[74:75]\n\t"
#define CLKL(L, T)   L ":\n\t" "s_memrealtime s[76:77]\n\t" CLKS(T, "s72", L "x") "s_memrealtime s[72:73]\n\t" CLKS(T, "s74", L "x")     \
                     "s_memrealtime s[74:75]\n\t" CLKS(T, "s76", L "x") "s_branch " L "\n" \
                     L "x:\n\t" CLKP(L)
#define CLKW(L, OFF) CLKH(OFF) "s_nop 3\n" CLKL(L, "s50")
#define CLKP(L) "s_cmp_lt_i32 s49, 1\n\t" "s_cbranch_scc1 " L "y\n\t" "s_nop " STR(PHN) "\n\t" "s_cmp_lt_i32 s49, 2\n\t" "s_cbranch_scc1 " L "y\n\t" "s_nop " STR(PHN) "\n\t" \
                "s_cmp_lt_i32 s49, 3\n\t" "s_cbranch_scc1 " L "y\n\t" "s_nop " STR(PHN) "\n\t" "s_nop 0\n\t" "s_branch " L "y\n\t" "s_nop 0\n" L "y:\n\t"
#define PHN 3
#define REL(L) "s_barrier\n" L ":\n\t"
#define RELV(L) "s_cbranch_vccnz " L "\n\t" REL(L)
#define RELN(L) "s_cmp_eq_u32 %[wv], 0\n\t" "s_cbranch_scc1 " L "a\n\t" "s_cmp_eq_u32 %[wv], 1\n\t" "s_cbranch_scc1 " L "b\n\t" \
                "s_barrier\n\t" "s_nop " STR(PD2) "\n\t" "s_branch " L "\n" L "b:\n\t" "s_barrier\n\t" "s_branch " L "\n" L "a:\n\t" "s_barrier\n" L ":\n\t"
#define PD2 2
#define TS_(n) "s_memtime s[80+2*" #n ":81+2*" #n "]\n\t"
#define TSA(n)
#define TSB(n)
#define TSW(n)
#define TVM 12
template <int NTL, int KS>
__device__ __forceinline__ void tr_chunk(const u16* S, int lr, int lg, u32 (&r)[NTL][KS][8]) {
  const int q = lr >> 2, p = lr & 3;
#pragma unroll
  for (int t = 0; t < NTL; ++t)
#pragma unroll
    for (int i = 0; i < KS; ++i)
#pragma unroll
      for (int kb = 0; kb < 2; ++kb) {
        const u16* base = S + (64 * i + 32 * kb + 16 * (lg >> 1) + 4 * (lg & 1) + (q & 1) + 8 * (q >> 1)) * (16 * NTL) + 16 * t + 4 * p;
        const u32x2 t0 = __builtin_bit_cast(u32x2, __builtin_amdgcn_ds_read_tr16_b64_v4i16((LAS v4i16_t*)(base)));
        const u32x2 t1 = __builtin_bit_cast(u32x2, __builtin_amdgcn_ds_read_tr16_b64_v4i16((LAS v4i16_t*)(base + 2 * 16 * NTL)));
        r[t][i][4 * kb + 0] = t0[0]; r[t][i][4 * kb + 1] = t0[1]; r[t][i][4 * kb + 2] = t1[0]; r[t][i][4 * kb + 3] = t1[1];
      }
}
#define SMF(acc, a, bf, bi) "v_smfmac_f32_16x16x64_bf16 " RNG("v", acc, 4) ", " RNG("v", a, 4) ", " RNG(bf, bi, 8) ", v34\n\t"
#define ZER(r) "v_mov_b64 " RNG("v", r, 2) ", 0\n\t" "v_mov_b64 " RNG("v", (r) + 2, 2) ", 0\n\t"
#define GLD(r, o) "s_xor_b32 s49, s50, " STR(o) "\n\t" "buffer_load_dwordx4 " RNG("v", r, 4) ", v32, %[rs], s49 offen sc1\n\t"
#define GL1(r, o) "s_xor_b32 s49, s50, " STR(o) "\n\t" "buffer_load_dwordx4 " RNG("v", r, 4) ", v33, %[rs], s49 offen sc1\n\t"
#define GL2(r, o) GL1(r, 4096 + (o))
#define MXA0(r) "v_max3_u32 v63, " REG(r) ", " REG((r) + 1) ", " REG((r) + 1) "\n\t"
#define MXA(r) "v_max3_u32 v63, v63, " REG(r) ", " REG((r) + 1) "\n\t"
#define MXB(r) "v_max3_u32 v63, v63, " REG((r) + 2) ", " REG((r) + 3) "\n\t"
#define CMPB "v_cmp_eq_u32 vcc, s54, v63\n\t"
#define PSA(r) "v_pk_add_f32 " RNG("v", r, 2) ", " RNG("v", r, 2) ", " RNG("v", (r) + 2, 2) "\n\t"
#define PSW(r, o) "ds_write_addtid_b32 " REG(r) " offset:" STR(o) "\n\t"
#define RDQ(r, o) "ds_read_b128 " RNG("v", r, 4) ", v37 offset:" STR(o) "\n\t"
#define SQ(t, i) SMF(64 + 4 * (t), 96 + 4 * (i), "a", ((i) * 4 + (t)) * 8)
#define SV(t, i) SMF(24 + 4 * (t), 96 + 4 * (i), "a", 128 + ((i) * 2 + (t)) * 8)
#define SVP(t, i) SV(t, i)
#define SW(a, q, i) SMF((a) + 4 * (q), 96 + 4 * (i), "v", 128 + ((i) * 2 + (q)) * 8)
#define WVM(n) "s_waitcnt vmcnt(" STR(n) ")\n\t"
#define WLG(n) "s_waitcnt lgkmcnt(" STR(n) ")\n\t"
#define WLGF(n) WLG(n)
#define PACE 7
#define PC "s_nop " STR(PACE) "\n\t"
#define PC6
#define PC7
#define WBLK(a, n, i, M0) WVM((n) + 7 - (i)) SW(a, 0, i) M0(96 + 4 * (i)) SW(a, 1, i) MXB(96 + 4 * (i))
#define CL8(a, b, c, d, e, f, g, h) "v" #a, "v" #b, "v" #c, "v" #d, "v" #e, "v" #f, "v" #g, "v" #h
#define CL64 CL8(64, 65, 66, 67, 68, 69, 70, 71)
#define CL72 CL8(72, 73, 74, 75, 76, 77, 78, 79)
#define CL80 CL8(80, 81, 82, 83, 84, 85, 86, 87)
#define CL88 CL8(88, 89, 90, 91, 92, 93, 94, 95)
#define CL96 CL8(96, 97, 98, 99, 100, 101, 102, 103), CL8(104, 105, 106, 107, 108, 109, 110, 111)
#define CL112 CL8(112, 113, 114, 115, 116, 117, 118, 119)
#define CL120 CL8(120, 121, 122, 123, 124, 125, 126, 127)
#define PKA(d, a, b) "v_pk_add_f32 " RNG("v", d, 2) ", " RNG("v", a, 2) ", " RNG("v", b, 2) "\n\t"
#define CNT(o) STR(4096 + 64 * (o))
#define ARRIVE(o) "ds_add_u32 v45, v39 offset:" CNT(o) "\n\t"

#define EXTRA "v_readlane_b32 s69, v35, s45\n\t" "s_and_b32 s56, s58, 1023\n\t" "s_bfe_u32 s57, s58, 0xa000a\n\t" "s_lshr_b32 s55, s58, 20\n\t" "s_add_u32 s66, s56, s69\n\t" "s_add_u32 s67, s57, s69\n\t" "s_add_u32 s67, s67, s69\n\t"
#define TEX(s) ((s) == 0 ? TE0 : ((s) < 4 ? TE1 : ((s) < 12 ? TE2 : ((s) < TS3 ? TE3 : 0))))
#define R2BASE "s_bitcmp1_b32 s45, 0\n\t" "s_cselect_b32 s71, " STR(XR2S) ", 0\n\t" "s_add_u32 s71, s71, %[r2]\n\t"
#define X4B 0
#define X4C 0
#define STB "buffer_store_dwordx2 v[116:117], v11, %[rs], s36 offen sc1\n"
#define STC "buffer_store_dwordx2 v[80:81], v17, %[rs], s36 offen sc1\n\t"
#define R2TEXT                                                                                                     \
              TSB(0)                                                                                               \
              "buffer_load_dwordx4 v[64:67], v43, %[rs], s71 offen sc1\n\t"                                      \
              "buffer_load_dwordx4 v[68:71], v8, %[rs], s71 offen sc1\n\t"                                       \
              "buffer_load_dwordx4 v[76:79], v9, %[rs], s71 offen sc1\n\t"                                       \
              "buffer_load_dwordx4 v[60:63], v10, %[rs], s71 offen sc1\n\t"                                      \
              "buffer_store_dwordx4 v[72:75], v47, %[rf], s41 offen sc1\n\t"                                     \
              "s_cbranch_vccnz .Lvw%=\n\t" VPOST ARRIVE(12) "s_barrier\n" ".Lvw%=:\n\t"                             \
              "ds_read2_b32 v[96:97], v12 offset0:0 offset1:4\n\t" "ds_read2_b32 v[98:99], v12 offset0:32 offset1:36\n\t" \
              "ds_read2_b32 v[100:101], v13 offset0:0 offset1:4\n\t" "ds_read2_b32 v[102:103], v13 offset0:32 offset1:36\n\t" \
              "ds_read2_b32 v[104:105], v14 offset0:0 offset1:4\n\t" "ds_read2_b32 v[106:107], v14 offset0:32 offset1:36\n\t" \
              "ds_read2_b32 v[108:109], v15 offset0:0 offset1:4\n\t" "ds_read2_b32 v[110:111], v15 offset0:32 offset1:36\n\t" \
              "v_mov_b64 v[112:113], v[80:81]\n\t" "v_mov_b64 v[114:115], v[82:83]\n\t" "s_mov_b32 s65, 0\n\t"     \
              "v_mov_b32_dpp v112, v84 row_ror:8 row_mask:0xf bank_mask:0xc\n\t" "v_mov_b32_dpp v113, v85 row_ror:8 row_mask:0xf bank_mask:0xc\n\t" \
              "v_mov_b32_dpp v114, v86 row_ror:8 row_mask:0xf bank_mask:0xc\n\t" "v_mov_b32_dpp v115, v87 row_ror:8 row_mask:0xf bank_mask:0xc\n\t" \
              "v_pk_mul_f32 v[112:113], v[112:113], s[62:63] op_sel_hi:[1,0]\n\t" "v_pk_mul_f32 v[114:115], v[114:115], s[62:63] op_sel_hi:[1,0]\n\t" \
              WLG(0)                                                                                               \
              PKA(96, 96, 100) PKA(98, 98, 102) PKA(104, 104, 108) PKA(106, 106, 110) PKA(96, 96, 104) PKA(98, 98, 106) \
              "v_cvt_pk_bf16_f32 v96, v96, 0\n\t" "v_cvt_pk_bf16_f32 v97, v97, 0\n\t" "v_cvt_pk_bf16_f32 v98, v98, 0\n\t" "v_cvt_pk_bf16_f32 v99, v99, 0\n\t" \
              "v_accvgpr_write_b32 a194, v96\n\t" "v_accvgpr_write_b32 a202, v97\n\t" "v_accvgpr_write_b32 a198, v98\n\t" "v_accvgpr_write_b32 a206, v99\n\t" \
              TSB(1) WVM(4) TSB(2) \
              "v_permlane32_swap_b32 v64, v66\n\t" "v_permlane32_swap_b32 v65, v67\n\t" \
              "v_pk_add_f32 v[92:93], v[64:65], v[66:67]\n\t" \
              WVM(3) SFM(68) \
              "v_permlane16_swap_b32 v92, v93\n\t" \
              "v_add_f32 v94, v92, v93\n\t" \
              WVM(2) "v_mul_f32 v94, s62, v94\n\t" SFM(76) \
              "v_exp_f32 v92, v94\n\t" \
              WVM(1) TSB(3) SFM(60) \
                \
              ".Lex%=:\n\t" \
              "v_exp_f32 v88, v112\n\t" "v_exp_f32 v89, v113\n\t" "v_cvt_pk_bf16_f32 v58, v92, 0\n\t" "v_exp_f32 v90, v114\n\t" "v_exp_f32 v91, v115\n\t" \
              "v_cvt_pk_bf16_f32 v56, v88, v89\n\t" "v_cvt_pk_bf16_f32 v57, v90, v91\n\t" \
              TSB(4) ROR8(50, 58) ROR8(48, 56) \
              "v_mfma_f32_16x16x32_bf16 v[88:91], v[52:55], v[56:59], 0\n\t" \
              ROR8(49, 57) "s_nop 1\n\t" \
              "v_mfma_f32_16x16x32_bf16 v[116:119], a[192:195], v[56:59], 0\n\t" \
              "s_nop 2\n\t" \
              "v_mfma_f32_16x16x32_bf16 v[116:119], a[196:199], v[48:51], v[116:119]\n\t" \
              "v_add_f32_dpp v92, v88, v88 row_ror:8 row_mask:0xf bank_mask:0xf\n\t" "v_subrev_u32 v89, 0x33800000, v92\n\t" \
              "v_mfma_f32_16x16x32_bf16 v[124:127], a[204:207], v[56:59], 0\n\t" \
              "v_rcp_f32 v93, v92\n\t" \
              "v_mfma_f32_16x16x32_bf16 v[124:127], a[200:203], v[48:51], v[124:127]\n\t" \
              TSB(5) "v_cmp_le_u32 vcc, 0x20000000, v89\n\t" "s_nop 0\n\t" \
              "s_cbranch_vccnz .Lmx%=\n\t" \
              OTAIL STB TSB(6)
#define SFM(r) "v_pk_fma_f32 v[112:113], " RNG("v", r, 2) ", s[62:63], v[112:113] op_sel_hi:[1,0,1]\n\t" "v_pk_fma_f32 v[114:115], " RNG("v", (r) + 2, 2) ", s[62:63], v[114:115] op_sel_hi:[1,0,1]\n\t"
#define ROR8(d, s) "v_mov_b32_dpp " REG(d) ", " REG(s) " row_ror:8 row_mask:0xf bank_mask:0xf\n\t"
#define OTAIL "v_pk_mul_f32 v[116:117], v[116:117], v[92:93] op_sel:[0,1]\n\t" "v_pk_mul_f32 v[118:119], v[118:119], v[92:93] op_sel:[0,1]\n\t" \
              "v_cvt_pk_bf16_f32 v116, v116, v117\n\t" "v_cvt_pk_bf16_f32 v117, v118, v119\n\t" \
              "s_mov_b64 exec, s[78:79]\n\t" \
              "v_pk_mul_f32 v[124:125], v[124:125], v[92:93] op_sel:[0,1]\n\t" "v_pk_mul_f32 v[126:127], v[126:127], v[92:93] op_sel:[0,1]\n\t" \
              "v_cvt_pk_bf16_f32 v116, v124, v125\n\t" "v_cvt_pk_bf16_f32 v117, v126, v127\n\t" \
              "s_mov_b64 exec, -1\n\t"
#define MX3(a, b) "v_max3_u32 v95, v95, " REG(a) ", " REG(b) "\n\t"
#define R2MAX ".Lmx%=:\n\t" "v_max_u32 v95, v60, v61\n\t" MX3(62, 63) MX3(64, 65) MX3(66, 67) MX3(68, 69) MX3(70, 71) MX3(76, 77) MX3(78, 79) \
              "v_cmp_eq_u32 vcc, s54, v95\n\t" "s_cbranch_vccnz .Lfail%=\n\t" OTAIL \
              "s_cmp_lg_u32 s65, 0\n\t" "s_cbranch_scc1 .Lend%=\n\t" "s_mov_b32 s65, 1\n\t" \
              "v_max3_f32 v93, v112, v113, v94\n\t" \
              "v_max3_f32 v93, v93, v114, v115\n\t" \
              "s_nop 1\n\t" \
              "v_max_f32_dpp v92, v93, v93 row_ror:8 row_mask:0xf bank_mask:0xf\n\t" \
              "v_max_f32_dpp v88, v93, v93 row_ror:8 row_mask:0xf bank_mask:0xf\n\t" \
              "s_nop 1\n\t" \
              "v_permlane16_swap_b32 v92, v88\n\t" \
              "v_max_f32 v93, v92, v88\n\t" "v_max_f32 v92, v92, v88\n\t" \
              "s_nop 1\n\t" \
              "v_permlane32_swap_b32 v93, v92\n\t" \
              "v_max_f32 v93, v93, v92\n\t" \
              "v_sub_f32 v94, v94, v93\n\t" "v_sub_f32 v112, v112, v93\n\t" "v_exp_f32 v92, v94\n\t" "v_sub_f32 v113, v113, v93\n\t" "v_sub_f32 v114, v114, v93\n\t" "v_sub_f32 v115, v115, v93\n\t" \
              "s_branch .Lex%=\n"
#define PS1(r) "v_add_f32 " REG(r) ", " REG(r) ", " REG((r) + 2) "\n\t"
#define NOP(n) "s_nop " STR(n) "\n\t"
#define PCA 13
#define PDA 8
#define PDC 8
#define PDF 7
#define SLV 0
#define QKRED(X) RDQ(80, 12288) RDQ(84, 12304) X RDQ(88, 0) RDQ(92, 16) RDQ(112, 4096) RDQ(116, 4112) RDQ(120, 8192) RDQ(124, 8208) \
        WLG(4) TSA(4) PKA(80, 80, 88) PKA(82, 82, 90) PKA(84, 84, 92) PKA(86, 86, 94) \
        WLG(2) PKA(80, 80, 112) PKA(82, 82, 114) PKA(84, 84, 116) PKA(86, 86, 118) \
        WLG(0) PKA(80, 80, 120) PKA(82, 82, 122) PKA(84, 84, 124) PKA(86, 86, 126) \
        "v_cvt_pk_bf16_f32 v72, v80, v81\n\t" "v_cvt_pk_bf16_f32 v73, v82, v83\n\t" "v_cvt_pk_bf16_f32 v74, v84, v85\n\t" "v_cvt_pk_bf16_f32 v75, v86, v87\n\t" \
        NOP(1) TSA(5) \
        "v_mfma_f32_16x16x32_bf16 v[88:91], v[72:75], v[72:75], 0\n\t" NOP(2) \
        "v_mfma_f32_16x16x32_bf16 v[80:83], a[208:211], v[72:75], 0\n\t" NOP(2) \
        "v_mfma_f32_16x16x32_bf16 v[84:87], a[212:215], v[72:75], 0\n\t"
#define QKPUB "buffer_store_dwordx4 v[88:91], v41, %[rs], s71 offen sc1\n\t" NOP(0) \
        "buffer_store_dwordx4 v[80:83], v40, %[rs], s71 offen sc1\n\t" NOP(2) \
        "buffer_store_dwordx4 v[84:87], v40, %[rs], s71 offen offset:512 sc1\n\t"
#define MRED(W, X) "ds_read_b128 v[80:83], v46 offset:12288\n\t" W "ds_read_b128 v[84:87], v46\n\t" "ds_read_b128 v[88:91], v46 offset:4096\n\t" "ds_read_b128 v[92:95], v46 offset:8192\n\t" X TSW(4) \
        WLG(2) TSW(5) PKA(80, 80, 84) PKA(82, 82, 86) WLG(0) PKA(88, 88, 92) PKA(90, 90, 94) PKA(84, 80, 88) PKA(86, 82, 90) \
        "v_cvt_pk_bf16_f32 v80, v84, v85\n\t" "v_cvt_pk_bf16_f32 v81, v86, v87\n\t"
#define ORD(a, b) "v_cmpx_o_f32 s[50:51], " REG(a) ", " REG(b) "\n\t"
#define REDO(L) "s_cmp_lg_u64 exec, -1\n\t" "s_mov_b64 exec, -1\n\t" "s_cbranch_scc1 " L "\n" L "r:\n\t"
#define CWAIT(L, o, S) L ":\n\t" "ds_read_b32 v62, v38 offset:" CNT(o) "\n\t" WLG(0) "v_readfirstlane_b32 s49, v62\n\t" "s_cmp_lt_u32 s49, " S "\n\t" "s_cbranch_scc1 " L "\n\t"
#define VPOST PSA(24) PSA(28) PSW(24, 2560) PSW(25, 2832) PSW(28, 3200) PSW(29, 3472)
#define BARA "s_barrier\n\t"
#define BARC "s_barrier\n\t"
#define PZADR(SB, M) "s_add_u32 s49, s45, 1\n\t" "s_xor_b32 s62, s71, " STR(XR2S) "\n\t" "v_readlane_b32 s63, v19, s49\n\t" "s_mov_b32 s49, " SB "\n\t" \
        ".if !((" STR(M) ") & 1)\n\t" "s_mov_b32 s63, 0x7ff00000\n\t" ".endif\n\t" ".if !((" STR(M) ") & 2)\n\t" "s_mov_b32 s49, 0x7ff00000\n\t" ".endif\n\t" \
        ".if !((" STR(M) ") & 4)\n\t" "s_mov_b32 s62, 0x7ff00000\n\t" ".endif\n\t" "s_cmp_eq_u32 %[wv], 0\n\t" \
        "s_cselect_b32 s49, s49, s62\n\t" "s_cselect_b32 s63, s63, s62\n\t" "v_add_u32 v48, s49, v23\n\t" "s_mov_b32 exec_hi, 0\n\t" "v_add_u32 v48, s63, v23\n\t" "s_mov_b32 exec_hi, -1\n\t"
#define PZST "buffer_store_dwordx4 v[92:95], v48, %[rs], 0 offen sc1\n\t"
#define MA 0
#define MC 0
#define MB (7 - MA - MC)
#define PZD 1
#define PZB0 PZADR("s63", MB)
#define PZB PZST
#define PZC0
#define PZC ""
#define PZNC 0
#define PZA0
#define PZA
#define PZNA 0
#define LASTC(L) RELV(L)
#define LASTA(L) ""
#define PCL PCA
#define ABODY(W, X, F, n) TSA(0) GLD(96, 0) NOP(PCA) GLD(100, 1024) NOP(PCA) GLD(104, 2048) W GLD(108, 3072) "s_mov_b32 s47, 0\n\t" X \
        WVM((n) + 3) TSA(1) SQ(0, 0) SQ(1, 0) SQ(2, 0) SQ(3, 0) \
        WVM((n) + 2) SQ(0, 1) SQ(1, 1) SQ(2, 1) SQ(3, 1) \
        WVM((n) + 1) SQ(0, 2) MXA0(96) MXB(96) SQ(1, 2) MXA(100) MXB(100) SQ(2, 2) MXA(104) MXB(104) SQ(3, 2) \
        WVM(n) TSA(2) SQ(0, 3) MXA(108) MXB(108) CMPB SQ(1, 3) "s_add_u32 s48, s38, 4\n\t" NOP(0) NOP(0) SQ(2, 3) "s_cbranch_vccnz " F "\n\t" PS1(64) PS1(65) SQ(3, 3) PS1(68) PS1(69) \
        NOP(0) PSW(64, 0) PSW(65, 272) PSW(68, 640) PSW(69, 912) PSA(72) PSA(76) PSW(72, 1280) PSW(73, 1552) PSW(76, 1920) PSW(77, 2192)
#define CISSUE(X, Y) TSW(0) GL1(96, 0) PC GL1(100, 1024) PC GL1(104, 2048) PC GL1(108, 3072) PC GL2(112, 0) PC GL2(116, 1024) \
        "s_mov_b32 s48, s36\n\t" "s_mov_b32 s36, s37\n\t" "s_add_u32 s45, s45, 1\n\t" "s_add_u32 s38, s38, 4\n\t" "v_readlane_b32 s37, v19, s45\n\t" \
        "s_add_u32 s39, s39, " STR(EXF) "\n\t" "s_add_u32 s40, s40, " STR(EXF) "\n\t" \
        "s_add_u32 s41, s41, " STR(EXK) "\n\t" "s_add_u32 s42, s42, " STR(EXK) "\n\t" "s_add_u32 s43, s43, s55\n\t" PC6 \
        GL2(120, 2048) "s_mov_b32 s47, 2\n\t" "s_mul_i32 s70, s69, 3\n\t" R2BASE "s_add_u32 s43, s43, s70\n\t" NOP(2) PC7 X \
        GL2(124, 3072) Y
#define ZALL ZER(64) ZER(68) ZER(72) ZER(76) ZER(80) ZER(84) ZER(24) ZER(28)
#define SHRD "ds_read_b32 v62, v38\n\t"
#define SHUP WLG(0) "v_readfirstlane_b32 s49, v62\n\t" "s_max_u32 s44, s44, s49\n\t"
#define DPA(d, r, m) "v_add_f32_dpp " REG(d) ", " REG(r) ", " REG((r) + 2) " quad_perm:[0,1,2,3] row_mask:" m " bank_mask:0xf\n\t"
#define CBODY(m, X, n, F) CBODY_(80, m, X, n, F)
#define CBODY_(a, m, X, n, F) \
        WVM((m) + 7) TSW(1) WBLK(a, m, 0, MXA0) WBLK(a, m, 1, MXA) X WBLK(a, n, 2, MXA) WBLK(a, n, 3, MXA) WBLK(a, n, 4, MXA) WBLK(a, n, 5, MXA) WBLK(a, n, 6, MXA) \
        WVM(n) TSW(2) SW(a, 0, 7) MXA(124) MXB(124) CMPB SW(a, 1, 7) NOP(0) NOP(1) "s_cbranch_vccnz " F "\n\t" \
        DPA(88, a, "0x3") DPA(89, (a) + 1, "0x3") NOP(1) DPA(88, (a) + 4, "0xc") DPA(89, (a) + 5, "0xc") NOP(1) \
        "v_permlane32_swap_b32 v88, v89\n\t" "v_add_f32 v88, v88, v89\n\t" TSW(3) PSW(88, 3840)
#define FINEO
#define NEXTP "s_branch .LA%=\n"
#define NEXTN "s_branch .LNA%=\n"

__device__ __forceinline__ void clk_edge(u32& cyc, u32& tick) {
  const u32 t = rt32();
  for (int i = 0; i < 64; ++i) { tick = rt32(); if (tick != t) break; }
  cyc = (u32)__builtin_amdgcn_s_memtime();
}
__device__ __forceinline__ float clk_rate(u32 cyc0, u32 tk0) { u32 c, t; clk_edge(c, t); return (float)(c - cyc0) / (float)max(t - tk0, 1u); }
__device__ __forceinline__ void dma16(__amdgpu_buffer_rsrc_t rs, int voff, int soff, int sstep, int ldsa) {
#pragma unroll
  for (int i = 0; i < 16; ++i)
    asm volatile("s_mov_b32 m0, %0\n\ts_nop 3\n\tbuffer_load_dwordx4 %1, %2, %3 offen lds" :: "s"(ldsa + i * 1024), "v"(voff), "s"(rs), "s"(soff ^ (i * sstep)) : "memory");
}

#define KH(t) (192 + 8 * (t))
#define KMF(t, a, b) "v_mfma_f32_16x16x32_bf16 " RNG("v", 80 + 4 * (t), 4) ", " RNG("v", KH(t) + 4 * (a), 4) ", " RNG("v", KH(3) + 4 * (b), 4) ", " RNG("v", 80 + 4 * (t), 4) "\n\t"
#define KX(j) (92 + 4 * ((j) & 1))
#define KA(t, j) "v_pk_mul_f32 " RNG("v", KX(j), 2) ", " RNG("v", 64 + 4 * (j), 2) ", " RNG("v", 112 + ((t) & 2), 2) " op_sel:[0," STR((t) & 1) "] op_sel_hi:[1," STR((t) & 1) "]\n\t"
#define KF(j) "v_fract_f32 " REG(KX(j) + 1) ", " REG(KX(j) + 1) "\n\t"
#define KE(j, N) "v_exp_f32 " REG(KX(j)) ", " REG(KX(j)) "\n\t" "v_cos_f32 " REG(KX(j) + 2) ", " REG(KX(j) + 1) "\n\t" "v_sin_f32 " REG(KX(j) + 3) ", " N REG(KX(j) + 1) "\n\t"
#define KM(j) "v_pk_mul_f32 " RNG("v", KX(j) + 2, 2) ", " RNG("v", KX(j) + 2, 2) ", " RNG("v", KX(j), 2) " op_sel_hi:[1,0]\n\t"
#define KC(j) "v_pk_mul_f32 " RNG("v", KX(j), 2) ", " RNG("v", KX(j) + 2, 2) ", " RNG("v", 66 + 4 * (j), 2) " op_sel_hi:[1,0]\n\t" \
  "v_pk_fma_f32 " RNG("v", KX(j), 2) ", " RNG("v", KX(j) + 2, 2) ", " RNG("v", 66 + 4 * (j), 2) ", " RNG("v", KX(j), 2) " op_sel:[1,1,0] op_sel_hi:[0,1,1] neg_hi:[1,0,0]\n\t"
#define KD(t, j, r) "v_cvt_pk_bf16_f32 " REG(KH(t) + (j)) ", " REG(r) ", " REG((r) + 1) "\n\t" \
  "v_dot2c_f32_bf16 " REG(r) ", s47, " REG(KH(t) + (j)) "\n\t" "v_dot2c_f32_bf16 " REG((r) + 1) ", s48, " REG(KH(t) + (j)) "\n\t"
#define KL(t, j, r) "v_cvt_pk_bf16_f32 " REG(KH(t) + 4 + (j)) ", " REG(r) ", " REG((r) + 1) "\n\t"
#define KDM "s_add_u32 m0, m0, 1024\n\t" "s_add_u32 s43, s43, s42\n\t" "buffer_load_dwordx4 v110, s[36:39], s43 offen lds\n\t"
#define KP(j) "ds_read_b128 " RNG("v", 64 + 4 * (j), 4) ", v111 offset:" STR(16 * (j)) "\n\t"
#define KV(j, L, A) KE(j, "-") L KM(j) KC(j) A KD(3, j, KX(j))
#define KU(t, j, L, A) KE(j, "") L KM(j) A KD(t, j, KX(j) + 2)
#define KSUB                                                                                                        \
  KV(0, WLG(8) "s_add_u32 m0, m0, 1024\n\t", KA(3, 1)) KV(1, KL(3, 0, KX(0)), KA(3, 2))                              \
  "s_xor_b32 s43, s40, s41\n\t" "buffer_load_dwordx4 v110, s[36:39], s43 offen lds\n\t" KV(2, KL(3, 1, KX(1)), KA(3, 3)) \
  KV(3, KL(3, 2, KX(2)), KA(0, 0))                                                                                  \
  KU(0, 0, KL(3, 3, KX(3)), KA(0, 1)) KU(0, 1, KL(0, 0, KX(0) + 2), KA(0, 2)) KDM KU(0, 2, KL(0, 1, KX(1) + 2), KA(0, 3)) \
  KU(0, 3, KL(0, 2, KX(2) + 2), KA(1, 0)) KF(0)                                                                     \
  KU(1, 0, KL(0, 3, KX(3) + 2), KA(1, 1)) KF(1) KMF(0, 1, 0) KU(1, 1, KL(1, 0, KX(0) + 2), KA(1, 2)) KF(2) KDM      \
  KMF(0, 0, 1) KU(1, 2, KL(1, 1, KX(1) + 2), KA(1, 3)) KF(3) KMF(0, 0, 0) KU(1, 3, KL(1, 2, KX(2) + 2), KA(2, 0)) KF(0) KP(0) \
  KU(2, 0, KL(1, 3, KX(3) + 2), KA(2, 1)) KF(1) KP(1) KMF(1, 1, 0) KU(2, 1, KL(2, 0, KX(0) + 2), KA(2, 2)) KF(2) KP(2) KDM \
  KMF(1, 0, 1) KU(2, 2, KL(2, 1, KX(1) + 2), KA(2, 3)) KF(3) KP(3) KMF(1, 0, 0) KU(2, 3, KL(2, 2, KX(2) + 2), WLG(3) KA(3, 0)) \
  KMF(2, 0, 1) "v_add_u32 v111, 256, v111\n\t" "s_lshl2_add_u32 s41, s42, s41\n\t" KL(2, 3, KX(3) + 2)
#define KZERO ZER(80) ZER(84) ZER(88)
#define KCHUNK(PTR, VO, SO, ST, BUF, PF) "s_mov_b64 s[36:37], " PTR "\n\t" "s_mov_b32 m0, %[sc]\n\t" "v_mov_b32 v110, " VO "\n\t" SO "s_mov_b32 s41, 0\n\t" "s_mov_b32 s42, " STR(ST) "\n\t" \
  "buffer_load_dword " PF ", s[36:39], 0 offen lds\n\t" "s_add_u32 m0, %[sl], " STR((BUF) * 16384 - 1024) "\n\t"
#define KCALL "s_call_b64 s[44:45], .Lkg%=\n\t" WVM(16) KMF(2, 0, 0)
#define KTR(F, d, A, o) "ds_read_b64_tr_b16 " RNG(F, d, 2) ", " A " offset:" STR(o) "\n\t"
#define KTQ1(d, o) KTR("a", d, "%[aq]", o) KTR("a", (d) + 2, "%[aq]", (o) + 128) KTR("a", (d) + 4, "%[aq]", (o) + 2048) KTR("a", (d) + 6, "%[aq]", (o) + 2176)
#define KTQ(d, o) KCALL KTQ1(d, o) KMF(2, 1, 0) KTQ1((d) + 8, (o) + 32)
#define KTO1(d, o) KTR("v", d, "%[ao]", o) KTR("v", (d) + 2, "%[ao]", (o) + 64) KTR("v", (d) + 4, "%[ao]", (o) + 1024) KTR("v", (d) + 6, "%[ao]", (o) + 1088)
#define KTO(d, o) KCALL KTO1(d, o) KMF(2, 1, 0) KTO1((d) + 8, (o) + 2048)
#define KOUT(o) "s_add_u32 s43, %[ko], " STR(o) "\n\t"                                                              \
  "buffer_store_dwordx4 v[80:83], %[l16], %[rt], s43 offen sc1\n\t" "buffer_store_dwordx4 v[84:87], %[l16], %[rt], s43 offen offset:1024 sc1\n\t" "s_nop 3\n\t" \
  "buffer_store_dwordx4 v[88:91], %[l16], %[rt], s43 offen offset:2048 sc1\n\t"
struct SsmPtrs { const u16 *A_re, *A_im, *B_re, *B_im, *C_re, *C_im, *log_dt; };
__device__ __forceinline__ void r_role(unsigned char* lds, int b, const u16* __restrict__ mem0, const u16* __restrict__ Wq, const u16* __restrict__ Wk, const u16* __restrict__ Wv,
                                       const u16* __restrict__ Wo, int fb, u32 tag, u64 pc0, int clen, __amdgpu_buffer_rsrc_t rsx, __amdgpu_buffer_rsrc_t rsf,
                                       __amdgpu_buffer_rsrc_t rst, SsmPtrs sp, int tid, int lane, int w
                                       , u32* dbg, int tun_r, int tun_p, int tun_b, int tun_c
                                       ) {
  const int lr = lane & 15, lg = lane >> 4;
  const int ldsb = (int)(size_t)(LAS unsigned char*)lds;
  const int idx = (lr & 2) ? 0xEEEE : 0x4444;
  u32 tcnt = 0; (void)tcnt;
  u32 cyc0 = 0, tk0 = 0;
  if (tid < 8) *(u32*)(lds + LR_CTL + tid * 4) = 0u;
  *(u32*)(lds + LR_CTL + 4096 + 256 + tid * 4) = 0u;
  const int xcd = b & 3, li = b >> 2;
  const int head = xcd * 8 + (li >> 2), ks = li & 3;
  const int vcu = head * 4 + ks;
  const int cg = (xcd >> 1) * 32 + li, rg = xcd & 1;
  const int rot1 = ((li >> 1) + w) & 3, rot3 = ((li >> 2) + 2 * w) & 7;
  const int pz1 = lane < 32 ? XR1A(lane >> 3, cg * 256 + rg * 128 + (lane & 7) * 16) : XR3A(lg & 1, vcu * 256 + lr * 16);
  const int pgo = PGO_X + head * 128;
  const int calo = XCAL + (li & 3) * 8192 + ((li >> 2) & 1) * 4096 + ((li >> 3) * 4 + xcd) * 128;
  u32 xp0;
  u32x32 wa[6], wo[4];
  {
    const __amdgpu_buffer_rsrc_t rwq = __builtin_amdgcn_make_buffer_rsrc((void*)Wq, 0, DM * HD * 2, 0x00020000);
    const int vq = (lane >> 2) * (HD * 2) + vcu * 64 + (lane & 3) * 16, sq = (256 * w + (rot1 << 6)) * (HD * 2);
    const int vo = (lane >> 1) * (DM * 2) + cg * 32 + (lane & 1) * 16, so = (1024 * w + (rot3 << 7)) * (DM * 2);
    const int sl = ldsb + LR_STG + w * 32768;
    const int ch0 = (b * 4 + w) * 2;
    u32x2 pin[6];
    u32 ldt;
    {
      const int po = (ch0 * SSM_N + 4 * lane) * 2;
      asm volatile("global_load_dwordx2 %0, %7, %9\n\tglobal_load_dwordx2 %1, %7, %10\n\tglobal_load_dwordx2 %2, %7, %11\n\tglobal_load_dwordx2 %3, %7, %12\n\t"
                   "global_load_dwordx2 %4, %7, %13\n\tglobal_load_dwordx2 %5, %7, %14\n\tglobal_load_ushort %6, %8, %15"
                   : "=&v"(pin[0]), "=&v"(pin[1]), "=&v"(pin[2]), "=&v"(pin[3]), "=&v"(pin[4]), "=&v"(pin[5]), "=&v"(ldt)
                   : "v"(po), "v"((ch0 + (lane >> 5)) * 2), "s"(sp.A_re), "s"(sp.A_im), "s"(sp.B_re), "s"(sp.B_im), "s"(sp.C_re), "s"(sp.C_im), "s"(sp.log_dt) : "memory");
    }
    u32x4 m0v;
    asm volatile("global_load_dwordx4 %0, %1, %2" : "=&v"(m0v) : "v"(((4 * rg + ((lane >> 1) & 3)) * DM + 16 * cg + 8 * (lane & 1)) * 2), "s"(mem0) : "memory");
    const u16* const wp[4] = {Wq, Wk, Wv, Wo};
    int pfo[5];
#pragma unroll
    for (int c = 0; c < 5; ++c) {
      const int lo_ = (int)(size_t)wp[c < 3 ? c : 3], s3 = (384 - lo_) & 1023, s63 = (8064 - lo_) & 8191;
      pfo[c] = c < 3 ? (lane < 16 ? (256 * w + li * 8 + (lane & 7)) * (HD * 2) + xcd * 2048 + (lane >> 3) * 1024 + s3
                                  : (lane < 24 && (s63 >> 11) == xcd ? (256 * w + li * 8 + (lane & 7)) * (HD * 2) + s63 : OOR))
                     : (lane < 16 ? (1024 * w + li * 16 + lane + (((c - 3) * 512) ^ ((((s3 >> 7) + 2 * w) & 4) << 7))) * (DM * 2) + (xcd >> 1) * 1024 + s3 : OOR);
    }
    const int scr = ldsb + LR_RED + 1024 + w * 256;
    asm volatile("s_mov_b32 m0, %0\n\ts_nop 3\n\tbuffer_load_dword %1, %2, 0 offen lds" :: "s"(scr), "v"(pfo[0]), "s"(rwq) : "memory");
#define KP0 16
#pragma unroll
    for (int i = 0; i < KP0; ++i)
      asm volatile("s_mov_b32 m0, %0\n\ts_nop 3\n\tbuffer_load_dwordx4 %1, %2, %3 offen lds" :: "s"(sl + i * 1024), "v"(vq), "s"(rwq), "s"(sq ^ (i * 16 * HD * 2)) : "memory");
    asm volatile("s_waitcnt vmcnt(1+" STR(KP0) ")" : "+v"(pin[0]), "+v"(pin[1]), "+v"(pin[2]), "+v"(pin[3]), "+v"(pin[4]), "+v"(pin[5]), "+v"(ldt), "+v"(m0v)::"memory");
    f32x4* prm = (f32x4*)(lds + w * 4096);
    {
      const float dt = rbf(expf(bf2f((u16)ldt)));
#pragma unroll
      for (int i = 0; i < 4; ++i) {
        float v[6];
#pragma unroll
        for (int j = 0; j < 6; ++j) v[j] = __uint_as_float((i & 1) ? (pin[j][i >> 1] & 0xffff0000u) : (pin[j][i >> 1] << 16));
        const float are = dt * v[0], aim = dt * v[1], br = dt * v[2], bi = dt * v[3];
        const float cr = v[4] * br - v[5] * bi, ci = v[4] * bi + v[5] * br;
        const float rev = aim * 0.15915494309189535f;
        prm[4 * lane + i] = (f32x4){are * 1.4426950408889634f, rev - __builtin_rintf(rev), cr, ci};
      }
    }
#pragma unroll
    for (int i = KP0; i < 16; ++i)
      asm volatile("s_mov_b32 m0, %0\n\ts_nop 3\n\tbuffer_load_dwordx4 %1, %2, %3 offen lds" :: "s"(sl + i * 1024), "v"(vq), "s"(rwq), "s"(sq ^ (i * 16 * HD * 2)) : "memory");
    const f32x4 lv = {(float)(128 * (lr >> 3) + (lr & 7)), (float)(lr < 8 ? 256 + 128 * (lr >> 2) + 2 * (lr & 3) : 512 + 128 * ((lr - 8) >> 1) + 4 * (lr & 1)),
                         (float)(lr <= 8 ? 1024 + 128 * lr : 0), (float)(8 * lr)};
    const int trow = 16 * (lg >> 1) + 4 * (lg & 1) + ((lr >> 2) & 1) + 8 * (lr >> 3);
    asm volatile(
        "s_nop 4\n\t" "s_mov_b32 s46, m0\n\t" "s_mov_b32 s38, " STR(DM * HD * 2) "\n\t" "s_mov_b32 s39, 0x20000\n\t" "s_mov_b32 s47, 0xbf80\n\t" "s_mov_b32 s48, 0xbf800000\n\t"
        WLG(0)
        "ds_read_b128 v[64:67], %[pa]\n\t" "ds_read_b128 v[68:71], %[pa] offset:16\n\t" "ds_read_b128 v[72:75], %[pa] offset:32\n\t" "ds_read_b128 v[76:79], %[pa] offset:48\n\t"
        "v_add_u32 v111, 256, %[pa]\n\t" KZERO
        KCHUNK("%[wk]", "%[vq]", "s_mov_b32 s40, %[sq]\n\t", 16 * HD * 2, 1, "%[p1]")
        WLG(0) KA(3, 0)
        KTQ(0, 0) KTQ(32, 4096) KTQ(64, 8192) KTQ(96, 12288)
        KCHUNK("%[wv]", "%[vq]", "", 16 * HD * 2, 0, "%[p2]")
        KTQ(16, 16384) KTQ(48, 16384 + 4096) KTQ(80, 16384 + 8192) KTQ(112, 16384 + 12288)
        KOUT(0)
        KCHUNK("%[wo]", "%[vo]", "s_mov_b32 s40, %[so]\n\t", 32 * DM * 2, 1, "%[p3]")
        KZERO
        KTQ(128, 0) KTQ(144, 4096) KTQ(160, 8192) KTQ(176, 12288)
        KCHUNK("%[wo]", "%[vo]", "s_xor_b32 s40, s40, " STR(512 * DM * 2) "\n\t", 32 * DM * 2, 0, "%[p4]")
        KTO(128, 16384) KTO(144, 16384 + 4096) KTO(160, 16384 + 8192) KTO(176, 16384 + 12288)
        KOUT(3072)
        "s_branch .Lke%=\n"
        ".Lkg%=:\n\t"
        KSUB
        "s_setpc_b64 s[44:45]\n"
        ".Lke%=:\n\t"
        WLG(0) "s_mov_b32 m0, s46"
        : "=&{a[0:31]}"(wa[0]), "=&{a[32:63]}"(wa[1]), "=&{a[64:95]}"(wa[2]), "=&{a[96:127]}"(wa[3]), "=&{a[128:159]}"(wa[4]), "=&{a[160:191]}"(wa[5]),
          "=&{v[128:159]}"(wo[0]), "=&{v[160:191]}"(wo[1])
        : "{v[112:115]}"(lv), [pa] "v"(ldsb + w * 4096 + lg * 64), [vq] "v"(vq), [vo] "v"(vo),
          [p1] "v"(pfo[1]), [p2] "v"(pfo[2]), [p3] "v"(pfo[3]), [p4] "v"(pfo[4]), [aq] "v"(sl + trow * 64 + (lr & 3) * 8), [ao] "v"(sl + trow * 32 + (lr & 3) * 8), [l16] "v"(lane * 16),
          [wk] "s"(Wk), [wv] "s"(Wv), [wo] "s"(Wo), [sq] "s"(sq), [so] "s"(so), [sl] "s"(sl), [sc] "s"(scr), [rt] "s"(rst), [ko] "s"(2 * SSM_L * SSM_D * 2 + ch0 * 3072)
        : CL64, CL72, CL80, CL88, CL96, CL8(192, 193, 194, 195, 196, 197, 198, 199), CL8(200, 201, 202, 203, 204, 205, 206, 207), CL8(208, 209, 210, 211, 212, 213, 214, 215),
          CL8(216, 217, 218, 219, 220, 221, 222, 223), "s36", "s37", "s38", "s39", "s40", "s41", "s42", "s43", "s44", "s45", "s46", "s47", "s48", "scc", "memory");
    {
      const u32x4 pz = (u32x4){SENT, SENT, SENT, SENT};
      if (w < 3) {
        u32x4 v = pz;
        if (w == 0 && lane < 32) v = (u32x4){nosent(m0v[0]), nosent(m0v[1]), nosent(m0v[2]), nosent(m0v[3])};
#pragma unroll
        for (int hf = 0; hf < 2; ++hf) {
          __builtin_amdgcn_raw_buffer_store_b128(v, rsx, pz1, XSB(w) + hf * 8192, 16);
          __builtin_amdgcn_raw_buffer_store_b128(pz, rsx, XR2(vcu) + lane * 16, XR2B + (w & 1) * XR2S + hf * 8192, 16);
          __builtin_amdgcn_raw_buffer_store_b128(pz, rsx, lane < 16 ? XR2(vcu) + 1024 + lane * 16 : OOR, XR2B + (w & 1) * XR2S + hf * 8192, 16);
        }
      } else {
        __builtin_amdgcn_raw_buffer_store_b128(pz, rsx, lane < 8 ? lane * 16 : OOR, calo, 16);
        if (b < 32) {
          __builtin_amdgcn_raw_buffer_store_b128(pz, rsx, lane * 16, XSYN + b * 2048, 16);
          __builtin_amdgcn_raw_buffer_store_b128(pz, rsx, lane * 16, XSYN + b * 2048 + 1024, 16);
        } else if (b >= 48 && b < 48 + 41) {
          __builtin_amdgcn_raw_buffer_store_b128(pz, rsx, lane * 16, PGO_D + (b - 48) * 1024, 16);
        }
      }
      asm volatile("" ::: "memory");
    }
    xp0 = __builtin_amdgcn_raw_buffer_load_b32(rsx, (lane & 1) * 4096, pgo, 16);
    clk_edge(cyc0, tk0);
    asm volatile("s_waitcnt vmcnt(0)\n\ts_barrier" : "+v"(xp0)::"memory");
    if (tid == 0) dbg[3072 + b * 2] = rt32();
#define CLKF() clk_rate(cyc0, tk0)
    if (w == 0) {
      const int l0 = lane ? OOR : 0;
      const u64 c0 = __builtin_amdgcn_s_memtime();
      u32 v;
      asm volatile("s_waitcnt lgkmcnt(0)\n\tbuffer_load_dword %0, off, %1, %2 sc1\n\tbuffer_store_dword %3, %4, %1, %5 offen sc1\n\ts_waitcnt vmcnt(1)"
                   : "=&v"(v) : "s"(rsx), "s"(calo), "v"(tag), "v"(l0), "s"(PGO_Y + b * 128), "s"(c0) : "memory");
      const u32 lat = (u32)(__builtin_amdgcn_s_memtime() - c0);
      const float f = CLKF();
      typedef __attribute__((ext_vector_type(3))) unsigned int u32x3;
      __builtin_amdgcn_raw_buffer_store_b96((u32x3){__float_as_uint(f), lat, tag}, rsx, l0, PGO_Y + b * 128 + 4, 16);
      if (tid == 0) { dbg[3072 + b * 2 + 1] = rt32(); dbg[4200 + b] = lat; }
      asm volatile("" ::: "memory");
    }
    {
      u32 r[1][8][8];
      tr_chunk<1, 8>((const u16*)(lds + LR_STG + w * 32768), lr, lg, r);
#pragma unroll
      for (int i = 0; i < 8; ++i)
#pragma unroll
        for (int e = 0; e < 8; ++e) { const int o = (8 + i) * 8 + e; wo[o >> 5][o & 31] = r[0][i][e]; }
      asm volatile("s_waitcnt lgkmcnt(0)" ::: "memory");
    }
  }
  const int hf = (lr >> 1) & 1;
  const int voff1 = lg * 256 + (lr >> 3) * 128 + (((lr >> 2) & 1) * 2 + (lr & 1)) * 32 + hf * 16;
  const int voff3 = (lr >> 3) * 512 + (lg >> 1) * 256 + (((lr >> 2) & 1) * 2 + (lr & 1)) * 16 + (lg & 1) * 128 + hf * 64;
  const int ard = ldsb + (2 * (lr >> 3) + (lg >> 1)) * 640 + (lr & 1) * 272 + ((lr >> 1) & 3) * 64 + (lg & 1) * 32;
  const int ctl = ldsb + LR_CTL, one = w == 0 ? 2 : (w == 3 ? 4 : 1);
  const int r8 = lane & 7, hk = (lane >> 3) & 1;
#define COA 1
#define COB 1
  const int st01 = lr < 8 ? XR2(vcu) + lr * 16 + lg * 128 : OOR, st2 = (lr < 8 && lg >= 2) ? XR2(vcu) + 1024 + lr * 16 + (lg - 2) * 128 : OOR;
  const int ld01 = hk * 512 + r8 * 16 + lg * 128, ld2 = XR2(head * 4 + lg) + 1024 + r8 * 16 + hk * 128;
  int sto = XR3A(r8 >> 2, vcu * 256 + (r8 & 3) * 16 + lg * 64 + hk * 8);
  const int stk = lr >= 8 ? (lg * 8 + (lr - 8)) * 16 : OOR;
  const int lo = X4C ? 2 * (lr & 7) + (lr >> 3) : lr;
  const int arw = ldsb + LR_CTL + lane * 4, arr = ldsb + 3840 + ((lo >> 3) + 2 * ((lo >> 2) & 1)) * 64 + (lo & 3) * 16;
  const int st1 = XR1A(lg, cg * 256 + rg * 128 + lo * 8);
  const int avw = 0;
  const int avc = ldsb + 2560 + (lr >> 3) * 640 + (8 * ((lr >> 2) & 1) + (lr & 3)) * 4;
  const int avr = avc + (lg & 1) * 272 + (lg >> 1) * 64, avf = avc + (lg & 1) * 128 + (lg >> 1) * 16;
  u32x16 kc = {(u32)voff1, (u32)voff3, (u32)idx, (u32)TEX(lane), (u32)(lane < 16 ? XR2(vcu) + 1024 + lane * 16 : OOR), (u32)ard, (u32)ctl, (u32)one, (u32)st01, (u32)st2, (u32)(XR2(vcu) + lane * 16), (u32)ld2, (u32)(lane * 16), (u32)arw, (u32)arr, (u32)stk};
  int o1c = XR1A(xcd, w * 4096) + rot1 * 1024, o3c = XR3A(rg, w * 8192) + rot3 * 1024;
  const int fok = FO_K + (head * 8 + ks) * 1024, fov = FO_V + (head * 8 + 2 * ks) * 1024;
#define XPROG(v) min(xprog(__builtin_amdgcn_readlane(v, 0), tag), xprog(__builtin_amdgcn_readlane(v, 1), tag))
  int xp = w == 3 ? XPROG(xp0) : 1000;
#define XWAIT(n) for (int it_ = 0; it_ < SPIN_CAP && xp < (n); ++it_) {                                               \
    if (it_) asm volatile("s_sleep 4" ::: "memory");                                                                  \
    const u32 v_ = __builtin_amdgcn_raw_buffer_load_b32(rsx, (lane & 1) * 4096, pgo, 16); xp = XPROG(v_); asm volatile("" ::: "memory"); }
  XWAIT(1)
  u32x16 fv;
  u32x8 fk;
  {
    const int lo_ = w == 3 ? lane * 16 : OOR;
    const u32x4 v0 = __builtin_amdgcn_raw_buffer_load_b128(rsf, lo_, fov, 16), v1 = __builtin_amdgcn_raw_buffer_load_b128(rsf, lo_, fov + 1024, 16);
    const u32x4 k0 = __builtin_amdgcn_raw_buffer_load_b128(rsf, lo_, fok, 16), k1 = __builtin_amdgcn_raw_buffer_load_b128(rsf, lo_, fok + 4096, 16);
    fv = (u32x16){v0[0], v0[1], 0u, 0u, v0[2], v0[3], 0u, 0u, v1[0], v1[1], 0u, 0u, v1[2], v1[3], 0u, 0u};
    fk = (u32x8){k0[0], k0[1], k0[2], k0[3], k1[0], k1[1], k1[2], k1[3]};
    asm volatile("" : "+{a[192:207]}"(fv), "+{a[208:215]}"(fk));
  }
#define XPN 1
#define CALM 40
  XWAIT(2)
#pragma unroll
  for (int i = 0; i < 3; ++i) *(u32x4*)(lds + w * 4096 + i * 1024 + lane * 16) = (u32x4){SENT, SENT, SENT, SENT};
  {
    u64 pc; asm volatile("s_getpc_b64 %0" : "=s"(pc));
    const u32 cv = __builtin_amdgcn_raw_buffer_load_b32(__builtin_amdgcn_make_buffer_rsrc((void*)pc0, 0, clen, 0x00020000), (int)(pc - pc0) + tid * 64, 0, 0);
    asm volatile("" :: "v"(cv));
  }
  if (w == 3) { const u32 v_ = __builtin_amdgcn_raw_buffer_load_b32(rsx, (lane & 1) * 4096, pgo, 16); xp = max(xp, XPROG(v_)); }
  u32x4 st;
  if (w == 1) {
    if (b == 0) {
      u32 tt = 0, best = 0;
      bool xok = false;
      const int sto_ = lane < 5 ? (lane ? XSTR(lane - 1) : XSTA) : OOR;
      for (int it = 0; it < SPIN_CAP; ++it) {
        asm volatile("" ::: "memory");
        const u32x4 r0 = __builtin_amdgcn_raw_buffer_load_b128(rsx, lane * 128, PGO_Y, 16), r1 = __builtin_amdgcn_raw_buffer_load_b128(rsx, lane * 128 + 8192, PGO_Y, 16);
        if (!xok) xok = __ballot(xprog(__builtin_amdgcn_raw_buffer_load_b32(rsx, lane * 128, PGO_X, 16), tag) < XPN) == 0;
        const bool c0 = r0[3] == tag, c1 = r1[3] == tag;
        if (tt == 0 && __popcll(__ballot(!c0 || !c1)) <= CALM) {
          float fm = (c0 ? __uint_as_float(r0[1]) : 0.f) + (c1 ? __uint_as_float(r1[1]) : 0.f), fn = (c0 ? 1.f : 0.f) + (c1 ? 1.f : 0.f);
#pragma unroll
          for (int o = 4; o < 64; o *= 2) { fm += __shfl_xor(fm, o); fn += __shfl_xor(fn, o); }
          fm = fn > 0.f ? fm / fn : 100.f;
          const float f0 = __uint_as_float(__builtin_amdgcn_readfirstlane(__float_as_uint(fm)));
          fm = fminf(fm, __shfl_xor(fm, 1)); fm = fminf(fm, __shfl_xor(fm, 2));
          const float f_ = CLKF() * fminf(fmaxf(fm / f0, 0.8f), 1.f);
          const float k_ = 1.f + TKS * (fminf(fmaxf(23.9f / f_, 1.f), 1.5f) - 1.f);
          const int nb_ = (int)(TDB * k_ + 0.5f), nc_ = nb_ + (int)((TDC - TDB) * k_ + 0.5f), np_ = nc_ + (int)((TP - TDC) * k_ + 0.5f);
          u32 lat = min(c0 ? r0[2] : 65535u, c1 ? r1[2] : 65535u);
          { auto r = __builtin_amdgcn_permlane16_swap(lat, lat, false, false); lat = min(r[0], r[1]); }
          { auto r = __builtin_amdgcn_permlane32_swap(lat, lat, false, false); lat = min(r[0], r[1]); }
          u32 mm[4];
#pragma unroll
          for (int k = 0; k < 4; ++k)
            mm[k] = __builtin_amdgcn_readlane(lat, 4 * k) + __builtin_amdgcn_readlane(lat, 4 * k + 2) + __builtin_amdgcn_readlane(lat, 4 * (k ^ 2) + 1) + __builtin_amdgcn_readlane(lat, 4 * (k ^ 2) + 3);
          const u32 hb = mm[0] + mm[2] <= mm[1] + mm[3] ? 0u : 1u;
          best = (hb << 13) | ((hb ? mm[1] <= mm[3] : mm[0] <= mm[2]) ? CLS0 : 16384u - CLS0);
          if (__ballot(lat == 65535u) == 0) tt = __builtin_amdgcn_readfirstlane(nb_ | (nc_ << 10) | (np_ << 20));
          if (lane == 0) { dbg[3501] = (hb ? mm[1] : mm[0]) | ((hb ? mm[3] : mm[2]) << 16); dbg[3502] = rt32(); }
        }
        if (__ballot(r0[0] != tag || r1[0] != tag) == 0 && xok && tt) break;
      }
      st = (u32x4){rt32() + TD0, tt, best, tag};
      __builtin_amdgcn_raw_buffer_store_b128(st, rsx, sto_, 0, 16);
      if (lane == 0) dbg[3503] = rt32();
    } else st = wait_start(rsx, fb, tag, false, xcd);
    if (lane == 0) *(u32x4*)(lds + LR_CTL + 32) = st;
  }
  LBAR();
  st = *(const u32x4*)(lds + LR_CTL + 32);
  const int flip = __builtin_amdgcn_readfirstlane(st[2]) & 8192, cls = __builtin_amdgcn_readfirstlane(st[2]) & 16384;
  o1c ^= cls; o3c ^= cls; sto ^= cls; kc[4] ^= cls; kc[8] ^= cls; kc[9] ^= cls; kc[10] ^= cls; kc[11] ^= cls;
  { const u32 now = rt32(); if (st[0] - now + 100000u > 200000u) st[0] = now; }
  u32x16 kd = {(u32)((ld01 + XR2(vcu ^ 1)) ^ cls), (u32)((ld01 + XR2(vcu ^ 2)) ^ cls), (u32)((ld01 + XR2(vcu ^ 3)) ^ cls), (u32)(X4B && hk ? OOR : sto), (u32)avr, (u32)(avr + 4096), (u32)(avr + 8192),
               (u32)(avr + 12288), (u32)avw, (u32)(X4C && hk ? OOR : st1 ^ cls), (u32)((lane & 1) * 4096), (u32)(XSB(lane + 1) + flip), (u32)(lane * 8), (u32)avf,
               (u32)(b == 0 && lane == 0 ? XSYN + flip : OOR), (u32)(w == 0 ? pz1 ^ cls : (w == 1 ? kc[10] : kc[4]))};
  u32x16 sa = {(u32)flip, (u32)(XSB(1) + flip), 0u, (u32)fok, (u32)fov, (u32)(FO_KM + (head * 4 + ks) * 512),
               (u32)(FO_VM + (head * 8 + 2 * ks) * 256), st[0], 0u, 0u, (u32)SYO(1), 0u, 0u, (u32)TRY_CAP, 0u, 0u};
  u32x8 sc = {(u32)o1c, (u32)o3c, SENT, 0u, 0u, 0u, st[1], (u32)pgo};
  const u32 org_ = __builtin_amdgcn_readfirstlane(st[0]);
  u32x2 hst = {0u, 0u};
#define TUNEO , "+{s[100:101]}"(hst)
#define KCNT "s_mul_i32 s49, s47, 10\n\t" "s_lshl_b32 s49, 1, s49\n\t" "s_add_u32 s68, s68, s49\n\t"
#define KHST "s_bitset1_b64 s[100:101], s45\n\t"
#define KTRC
#define TRCI
#define PREP 16
#define GAP 12
#define RETA(n, L) "s_cmp_eq_u32 s49, " #n "\n\t" "s_cmov_b32 s48, .Lpc%=-" L "\n\t"
  u32x32 keep;
  u32x2 ov;
  asm volatile("" : "={v[60:91]}"(keep));
  while (true) {
    sa[14] = xp;
#pragma unroll
    for (int i = 0; i < 16; ++i) sa[i] = __builtin_amdgcn_readfirstlane(sa[i]);
#pragma unroll
    for (int i = 0; i < 8; ++i) sc[i] = __builtin_amdgcn_readfirstlane(sc[i]);
    asm volatile(
        "s_nop 4\n\t" "s_mov_b32 s60, m0\n\t"
        "s_mov_b32 m0, %[mb]\n\t" "s_mov_b32 s64, s50\n\t" "s_mov_b32 s98, s49\n\t" "s_mov_b32 s68, 0\n\t"
        EXTRA R2BASE
        "s_cmp_lg_u32 %[wv], 3\n\t" "s_cbranch_scc1 .LN%=\n\t"
        "s_mov_b32 s78, 0xff00ff00\n\t" "s_mov_b32 s79, 0xff00ff00\n\t" "s_mov_b32 s62, 0x3e0293ee\n\t"
        "v_mov_b32 v52, 0x3f803f80\n\t" "v_mov_b32 v53, 0x3f803f80\n\t" "v_mov_b32 v54, 0x3f803f80\n\t" "v_mov_b32 v55, 0x3f803f80\n\t" "v_mov_b32 v51, 0\n\t" "v_mov_b32 v59, 0\n\t"
        "v_mov_b32 v60, %[tg]\n\t" "v_mov_b32 v61, -1\n\t" "s_mov_b32 s63, %[dm]\n\t"
        "s_branch .Lent%=\n"
        ".LA%=:\n\t"
        CLKW(".Lta%=", "0") REL(".Lua%=")
        "s_add_u32 s50, s36, s52\n\t" NOP(PDA)
        ABODY(NOP(PCL) LASTA(".Lwa%="), WVM(5) "v_readlane_b32 s50, v60, 0\n\t" "v_readlane_b32 s51, v60, 1\n\t"
              "s_sub_u32 s50, s50, %[tg]\n\t" "s_sub_u32 s51, s51, %[tg]\n\t" "s_min_u32 s63, s50, s51\n\t" "s_max_u32 s50, s50, s51\n\t" "s_cmp_le_u32 s50, 80\n\t"
              "s_cselect_b32 s50, s63, 0\n\t" "v_readfirstlane_b32 s49, v61\n\t" "s_max_i32 s64, s64, s50\n\t"
              "s_cmp_lg_u32 s49, -1\n\t" "s_cmov_b32 s58, s49\n\t" EXTRA "v_mov_b32 v62, s58\n\t" "ds_write_b32 v38, v62 offset:24\n\t", ".Lfail%=", 0) TSA(3)
        QKRED(BARA) ORD(88, 89) ORD(90, 91) QKPUB TSA(6) TSB(7) REDO(".Lqk%=")
        SVP(0, 0) SHRD SVP(1, 0) SVP(0, 1) SVP(1, 1) SVP(0, 2) SVP(1, 2) WLG(0) "v_readfirstlane_b32 s49, v62\n\t" SVP(0, 3) SVP(1, 3) "s_max_u32 s44, s44, s49\n\t" "s_cmp_eq_u32 s45, 63\n\t" TSA(7)
        "s_cbranch_scc1 .Llast%=\n"
        ".LB%=:\n\t"
        CLKW(".Ltb%=", "s66")
        "s_mov_b32 s47, 1\n\t"
        R2TEXT
        ".LC%=:\n\t"
        ZALL
        "s_mov_b32 s47, 5\n\t" "s_sub_i32 s63, s64, s45\n\t" "s_cmp_lt_i32 s63, 2\n\t" "s_cbranch_scc1 .Lend%=\n\t"
        "s_cmp_lt_i32 s63, " STR(XTH) "\n\t" "s_cselect_b32 s63, s59, %[dm]\n\t"
        CLKW(".Ltc%=", "s67") REL(".Luc%=")
        "s_add_u32 s50, s36, s53\n\t" NOP(PDC)
        CISSUE(LASTC(".Lwc%="), "")
        "buffer_load_dword v61, off, %[rs], s46 sc1\n\t" "s_add_u32 s46, s46, 64\n\t"
        "s_add_u32 s49, s39, 4096\n\t" NOP(PDF)
        "buffer_load_dwordx4 a[208:211], v44, %[rf], s39 offen sc1\n\t" "buffer_load_dwordx4 a[212:215], v44, %[rf], s49 offen sc1\n\t"
        "buffer_load_dwordx2 a[192:193], v44, %[rf], s40 offen sc1\n\t" "buffer_load_dwordx2 a[196:197], v44, %[rf], s40 offen offset:8 sc1\n\t"
        "buffer_load_dwordx2 a[200:201], v44, %[rf], s40 offen offset:1024 sc1\n\t" "buffer_load_dwordx2 a[204:205], v44, %[rf], s40 offen offset:1032 sc1\n\t"
#define NXC 8
        "buffer_load_dword v60, v18, %[rs], s63 offen sc1\n\t"
        CBODY(NXC, "", NXC, ".Lfail%=")
        MRED(BARC, "") ORD(84, 85) ORD(86, 87) STC TSW(6) REDO(".Lmr%=")
        SHRD SHUP TSW(7)
        NEXTP
        CWAIT(".Lqk%=", 8, "s48") QKRED("") NOP(1) QKPUB "s_branch .Lqk%=r\n"
        CWAIT(".Lmr%=", 16, "s38") WVM(0) MRED("", "") "v_min_u32 v80, -2, v80\n\t" "v_min_u32 v81, -2, v81\n\t" STC
        "s_branch .Lmr%=r\n"
        ".Llast%=:\n\t"
        "buffer_store_dwordx4 v[72:75], v47, %[rf], s41 offen sc1\n\t" "s_mov_b32 s47, 3\n\t" NOP(4) VPOST ARRIVE(12) "s_barrier\n\t"
        "s_branch .Lend%=\n"
        R2MAX
        ".Lnorm%=:\n\t"
        "s_cmp_lg_u32 %[wv], 3\n\t" "s_cbranch_scc1 .LNA%=\n\t" "s_cmp_eq_u32 s47, 0\n\t" "s_cbranch_scc1 .LA%=\n\t" "s_branch .LC%=\n"
        ".LN%=:\n\t"
        "v_mov_b64 v[92:93], -1\n\t" "v_mov_b64 v[94:95], -1\n\t" "s_mov_b32 s65, s44\n\t" "s_mov_b32 s78, -100\n\t" "s_mov_b32 s79, 4\n\t"
        "s_branch .Lent%=\n"
        ".LNA%=:\n\t" PZA0
        "s_add_u32 s50, s36, s52\n\t"
        RELN(".Lna%=")
        ABODY(NOP(PCA), LASTA(".Lxa%=") PZA, ".Lfail%=", PZNA) BARA ARRIVE(8) TSA(3)
        SV(0, 0) SV(1, 0) SV(0, 1) SV(1, 1) SV(0, 2) SV(1, 2) SV(0, 3) SV(1, 3) ZER(64) ZER(68) ZER(72) ZER(76)
        VPOST TSA(7) ZER(80) ZER(84) ZER(24) ZER(28)
        "v_add_u32 v88, 4096, v21\n\t" "v_add_u32 v89, 8192, v21\n\t" "v_add_u32 v90, 12288, v21\n\t"
        PZB0
        "s_lshl_b32 s61, s45, 7\n\t" "s_add_u32 s61, s61, " STR(PGO_R - XSYN) "\n\t" "s_cmp_lg_u32 %[wv], 0\n\t"
        WLG(0) "s_barrier\n\t" PZB
        PSW(92, 0) PSW(92, 272) PSW(92, 640) PSW(92, 912) PSW(92, 1280) PSW(92, 1552) PSW(92, 1920) PSW(92, 2192) PSW(92, 3840)
        "ds_read2_b32 v[50:51], v38 offset0:0 offset1:6\n\t"
        "s_cbranch_scc1 .LNB%=\n\t"
        "s_memrealtime s[72:73]\n"
        ".Lv0%=:\n\t"
        "ds_read_b32 v62, v38 offset:" CNT(12) "\n\t"
        "ds_read2_b32 v[112:113], v21 offset0:0 offset1:68\n\t" "ds_read2_b32 v[114:115], v21 offset0:16 offset1:84\n\t"
        "ds_read2_b32 v[116:117], v88 offset0:0 offset1:68\n\t" "ds_read2_b32 v[118:119], v88 offset0:16 offset1:84\n\t"
        "ds_read2_b32 v[120:121], v89 offset0:0 offset1:68\n\t" "ds_read2_b32 v[122:123], v89 offset0:16 offset1:84\n\t"
        "ds_read2_b32 v[124:125], v90 offset0:0 offset1:68\n\t" "ds_read2_b32 v[126:127], v90 offset0:16 offset1:84\n\t"
        WLG(0) "v_cmp_gt_u32 vcc, s48, v62\n\t" "s_cbranch_vccnz .Lv0%=\n\t"
        "s_add_u32 s50, s72, " STR(TVM) "\n\t" "s_lshl_b32 s50, s50, 17\n\t" "s_lshr_b32 s50, s50, 1\n\t"
        PKA(112, 112, 116) PKA(114, 114, 118) PKA(120, 120, 124) PKA(122, 122, 126) PKA(112, 112, 120) PKA(114, 114, 122)
        "v_lshrrev_b32 v91, 20, v51\n\t" "v_readfirstlane_b32 s49, v50\n\t"
        "v_cvt_pk_bf16_f32 v112, v112, v113\n\t" "v_cvt_pk_bf16_f32 v113, v114, v115\n\t" "v_or_b32 v114, s50, v91\n\t"
        "s_cmp_eq_u32 s45, 63\n\t" "s_cbranch_scc1 .Lv2%=\n\t"
        "s_sleep " STR(SLV) "\n"
        ".Lv2%=:\n\t"
        "buffer_store_dwordx2 v[112:113], v20, %[rf], s42 offen sc1\n\t"
        "buffer_store_dword v114, v22, %[rs], s61 offen sc1\n\t"
        "s_cmp_eq_u32 s49, s65\n\t" "s_cbranch_scc1 .LNB%=\n\t"
        "s_mov_b32 s65, s49\n\t" KHST "s_sub_u32 s49, s45, s78\n\t" "s_mov_b32 s78, s45\n\t"
        "s_cmp_gt_i32 s49, " STR(GAP) "\n\t" "s_cbranch_scc1 .LNB%=\n\t" "s_cmp_lt_u32 s45, s79\n\t" "s_cbranch_scc1 .LNB%=\n\t"
        "s_add_u32 s79, s45, 3\n\t" "s_lshl_b32 s49, s45, 6\n\t" "v_add_u32 v50, " STR(TLB + ((TLB + TLC) << 10) + ((TLB + TLC + TLP) << 20)) ", v51\n\t"
        "buffer_store_dword v50, v22, %[rs], s49 offen offset:128 sc1\n"
        ".LNB%=:\n\t"
        WLG(0) "v_readfirstlane_b32 s58, v51\n\t" EXTRA
        "s_mov_b32 s47, 3\n\t"
        "s_cmp_eq_u32 s45, 63\n\t"
        "s_cbranch_scc1 .Lend%=\n"
        ".LNC%=:\n\t" PZC0
        "s_add_u32 s50, s36, s53\n\t"
        WLG(0) RELN(".Lnc%=")
        CISSUE("", LASTC(".Lxc%="))
        CBODY(0, PZC, PZNC, ".Lfail%=") BARC
        ARRIVE(16) TSW(4)
        NEXTN
        ".Lfail%=:\n\t" KCNT
        "s_cmp_lg_u32 s47, 2\n\t" "s_cbranch_scc1 .Lnu%=\n\t"
        "s_mov_b32 s37, s36\n\t" "s_mov_b32 s36, s48\n\t" "s_sub_u32 s45, s45, 1\n\t" "s_sub_u32 s38, s38, 4\n\t" "s_sub_u32 s39, s39, " STR(EXF) "\n\t" "s_sub_u32 s40, s40, " STR(EXF) "\n\t"
        "s_sub_u32 s41, s41, " STR(EXK) "\n\t" "s_sub_u32 s42, s42, " STR(EXK) "\n\t" "s_sub_u32 s43, s43, s55\n\t" "s_sub_u32 s43, s43, s70\n\t" "s_sub_u32 s46, s46, 64\n"
        ".Lnu%=:\n\t"
        "s_mov_b32 s51, 1\n\t" "s_nop 7\n\t" WVM(0) "s_sub_u32 s98, s98, 1\n\t" "s_cbranch_scc1 .Lcap%=\n"
        ".Lent%=:\n\t"
        "s_cmp_eq_u32 s47, 1\n\t" "s_cselect_b32 s99, s66, s67\n\t" "s_cbranch_scc1 .Lnz%=\n\t" ZALL ".Lnz%=:\n\t"
        "s_cmp_eq_u32 s47, 0\n\t" "s_cselect_b32 s99, 0, s99\n\t" "s_cselect_b32 s50, s52, s53\n\t" "s_add_u32 s99, s99, s43\n\t" "s_add_u32 s50, s50, s36\n\t"
        SHRD "s_memrealtime s[72:73]\n\t"
        "s_cmp_eq_u32 %[wv], 3\n\t" "s_cselect_b32 s49, 0, 3\n\t" "s_add_u32 s49, s49, s47\n\t" "s_cmp_lg_u32 s51, 0\n\t" "s_cselect_b32 s49, s49, 7\n\t" "s_cselect_b64 vcc, -1, 0\n\t"
        "s_cselect_b32 s51, 0, 14\n\t" "s_mov_b32 s48, .Lpc%=-.Lnorm%=\n\t"
        RETA(0, ".Lua%=") RETA(1, ".Ltb%=y") RETA(2, ".Luc%=") RETA(3, ".Lna%=") RETA(5, ".Lnc%=")
        "s_getpc_b64 s[96:97]\n" ".Lpc%=:\n\t" "s_sub_u32 s96, s96, s48\n\t" "s_subb_u32 s97, s97, 0\n\t"
        SHUP "s_sub_u32 s49, s99, s72\n\t" "s_add_u32 s49, s49, s44\n"
        ".Lbump%=:\n\t"
        "s_cmp_ge_i32 s49, " STR(PREP) "\n\t" "s_cbranch_scc1 .Lok%=\n\t" "s_add_u32 s44, s44, %[tr]\n\t" "s_add_u32 s49, s49, %[tr]\n\t" "s_branch .Lbump%=\n"
        ".Lok%=:\n\t"
        "s_mov_b64 exec, 1\n\t" "v_mov_b32 v62, s44\n\t" "ds_max_u32 v38, v62\n\t" KTRC "s_mov_b64 exec, -1\n\t"
        "s_add_u32 s99, s99, s44\n\t" "s_sub_u32 s99, s99, s51\n\t"
        WLG(0) "s_memrealtime s[72:73]\n\t" "s_nop 3\n\t" "s_memrealtime s[74:75]\n\t" "s_nop 3\n"
        CLKL(".Lth%=", "s99")
        "s_setpc_b64 s[96:97]\n"
        ".Lcap%=:\n\t" "s_add_u32 s47, s47, 8\n"
        ".Lend%=:\n\t"
        "s_waitcnt vmcnt(0) lgkmcnt(0)\n\t"
        "s_mov_b32 s50, s64\n\t" "s_mov_b32 s49, s98\n\t" "s_mov_b32 s51, s68\n\t" "s_mov_b32 m0, s60"
        : "+{s[36:51]}"(sa), "+{s[52:59]}"(sc), "+{v[60:91]}"(keep), "=&{v[116:117]}"(ov), "+{a[192:207]}"(fv), "+{a[208:215]}"(fk), "+{v[8:23]}"(kd) FINEO TUNEO
        : "{v[32:47]}"(kc), [rs] "s"(rsx), [rf] "s"(rsf), [dm] "s"(__builtin_amdgcn_readfirstlane(PGO_Y + b * 128)), [tg] "s"(__builtin_amdgcn_readfirstlane(tag)), [r2] "s"(__builtin_amdgcn_readfirstlane(XR2B + flip)), [mb] "s"(__builtin_amdgcn_readfirstlane(ldsb + w * 4096)), [wv] "s"(__builtin_amdgcn_readfirstlane(w)), [tr] "s"(TR) TRCI,
          "{a[0:31]}"(wa[0]), "{a[32:63]}"(wa[1]), "{a[64:95]}"(wa[2]), "{a[96:127]}"(wa[3]), "{a[128:159]}"(wa[4]), "{a[160:191]}"(wa[5]),
          "{v[128:159]}"(wo[0]), "{v[160:191]}"(wo[1]), "{v[192:223]}"(wo[2]), "{v[224:255]}"(wo[3])
        : CL8(24, 25, 26, 27, 28, 29, 30, 31), "v48", "v49", "v50", "v51", CL8(52, 53, 54, 55, 56, 57, 58, 59), "v92", "v93", "v94", "v95", CL96, "v112", "v113", "v114", "v115", "v118", "v119", CL120,
          "s60", "s61", "s62", "s63", "s64", "s65", "s66", "s67", "s68", "s69", "s70", "s71", "s72", "s73", "s74", "s75", "s76", "s77", "s78", "s79", "s96", "s97", "s98", "s99", "vcc", "scc", "memory");
    const int code = __builtin_amdgcn_readfirstlane(sa[11]);
    xp = __builtin_amdgcn_readfirstlane(sa[14]);
    tcnt += sa[15];
    sa[15] = 0;
    if (code == 3) break;
    const int seg = __builtin_amdgcn_readfirstlane(sa[9]);
    if (code == 5) { XWAIT(seg + 2) xp = max(xp, seg + 2); sa[11] = 2; }
    else if (code == 1) { __builtin_amdgcn_raw_buffer_store_b64((u32x2){nosent(ov[0]), nosent(ov[1])}, rsx, sto, sa[0], 16); sa[11] = 2; }
    else { sc[2] += 1; sa[13] = 0; sa[11] = code - 8; sa[15] = 1; }
  }
  if (lane == 0) { dbg[(b * 4 + w) * 4] = org_; dbg[(b * 4 + w) * 4 + 2] = 0; }
  if (lane == 0) { dbg[(b * 4 + (w + 1) % 4) * 4 + 1] = sa[7] + sa[8]; dbg[(b * 4 + w) * 4 + 3] = tcnt; if (b == 0 && w == 3) { dbg[3500] = sc[6]; dbg[3504] = sa[8]; } }
  if (b == 0 && w == 0 && ((hst[lane >> 5] >> (lane & 31)) & 1)) dbg[4096 + lane] = 1;
  LBAR();
  if (tid == 0) __builtin_amdgcn_raw_buffer_store_b32(1u, rsx, 0, PGO_D + vcu * 128, 16);
  if (b == 0 && w == 1) {
    for (int it = 0; it < SPIN_CAP; ++it) {
      asm volatile("" ::: "memory");
      if (__ballot(__builtin_amdgcn_raw_buffer_load_b32(rsx, lane * 128, PGO_S, 16) != tag || __builtin_amdgcn_raw_buffer_load_b32(rsx, lane * 128 + 8192, PGO_S, 16) != tag) == 0) break;
    }
    if (lane == 0) __builtin_amdgcn_raw_buffer_store_b32(tag, rsx, 0, XEPO, 16);
  }
}

#define XSTAT 3
#define XVM 8
#define XP0 -1
#define XSC " sc1"
#define XPITCH 1056
#define XSLOT (32 * XPITCH)
#define XW(HB, t) "a[" STR(HB) "+8*xk+4*" #t ":" STR(HB) "+8*xk+4*" #t "+3]"
#define XF(rt) "v[96+8*(xk&3)+4*" #rt ":96+8*(xk&3)+4*" #rt "+3]"
#define XFN(rt) "v[96+8*((xk+3)&3)+4*" #rt ":96+8*((xk+3)&3)+4*" #rt "+3]"
#define XA(AC, o) "v[" STR(AC) "+" #o ":" STR(AC) "+" #o "+3]"
#define XM1(d, a, b, FIRST) ".if (xk == 0) && " STR(FIRST) "\n\tv_mfma_f32_16x16x32_bf16 " d ", " a ", " b ", 0\n\t.else\n\tv_mfma_f32_16x16x32_bf16 " d ", " a ", " b ", " d "\n\t.endif\n\t"
#define XMF_QK(i, HB, AC, F) XMF_QK##i(HB, AC, F)
#define XMF_QK0(HB, AC, F) XM1(XA(AC, 0), XW(HB, 0), XF(0), F)
#define XMF_QK1(HB, AC, F) XM1(XA(AC, 4), XW(HB, 1), XF(0), F)
#define XMF_QK2(HB, AC, F) XM1(XA(AC, 8), XW(HB, 0), XF(1), F)
#define XMF_QK3(HB, AC, F) XM1(XA(AC, 12), XW(HB, 1), XF(1), F)
#define XMF_V(i, HB, AC, F) XMF_V##i(HB, AC, F)
#define XMF_V0(HB, AC, F) XM1(XA(AC, 0), XF(0), XW(HB, 0), F)
#define XMF_V1(HB, AC, F) XM1(XA(AC, 8), XF(0), XW(HB, 1), F)
#define XMF_V2(HB, AC, F) XM1(XA(AC, 4), XF(1), XW(HB, 0), F)
#define XMF_V3(HB, AC, F) XM1(XA(AC, 12), XF(1), XW(HB, 1), F)
#define XRD(rt, CUR, NXT) ".if xk < 13\n\tds_read_b128 " XFN(rt) ", " CUR " offset:64*(xk+3)+" STR(rt * 16 * XPITCH) "\n\t.else\n\t" \
                          "ds_read_b128 " XFN(rt) ", " NXT " offset:64*(xk-13)+" STR(rt * 16 * XPITCH) "\n\t.endif\n\t"
#define XCV(EPI, PV, i) ".if " STR(EPI) " && (xk >= 1) && (xk < 3)\n\tv_cvt_pk_bf16_f32 v[128+4*(xk-1)+" #i "], v[" STR(PV) "+8*(xk-1)+2*" #i "], v[" STR(PV) "+8*(xk-1)+2*" #i "+1]\n\t.endif\n\t"
#define XHALF(MF, HB, AC, PV, EPI, CUR, NXT, DSLOT, GOFF)                                                   \
  ".set xk, 0\n\t"                                                                                          \
  ".rept 16\n\t"                                                                                            \
  "s_waitcnt lgkmcnt(4)\n\t"                                                                                \
  MF(0, HB, AC, EPI) XRD(0, CUR, NXT) XCV(EPI, PV, 0)                                                       \
  ".if (xk >= 5) && (xk < 13)\n\t"                                                                          \
  "s_add_u32 m0, m0, " STR(XPITCH) "\n\t"                                                                   \
  ".endif\n\t"                                                                                              \
  MF(1, HB, AC, EPI) XRD(1, CUR, NXT) XCV(EPI, PV, 1)                                                       \
  ".if (xk >= 5) && (xk < 13)\n\t"                                                                          \
  "s_add_u32 %[dg], %[dg], 2048\n\t"                                                                        \
  ".endif\n\t"                                                                                              \
  MF(2, HB, AC, EPI) XCV(EPI, PV, 2)                                                                        \
  ".if (xk >= 4) && (xk < 12)\n\t"                                                                          \
  "buffer_load_dwordx4 %[l16], %[rx], %[dg] offen lds\n\t"                                                  \
  ".endif\n\t"                                                                                              \
  ".if xk == 0\n\t"                                                                                         \
  "s_add_u32 m0, %[dl], " STR((DSLOT) * XSLOT) "\n\t" "s_add_u32 %[dg], %[gb], " STR(GOFF) "\n\t"           \
  ".endif\n\t"                                                                                              \
  ".if " STR(EPI) " && (xk == " STR(XSTAT) ")\n\t"                                                                       \
  "buffer_store_dwordx4 v[128:131], v137, %[rf], %[sg] offen" XSC "\n\t"                                       \
  ".endif\n\t"                                                                                              \
  ".if " STR(EPI) " && (xk == 13)\n\t"                                                                      \
  "v_mov_b32 v137, %[l16]\n\t" "v_mov_b32 v138, %[lp2]\n\t"                                                 \
  ".endif\n\t"                                                                                              \
  ".if " STR(EPI) " && (xk == 14)\n\t"                                                                      \
  "s_add_u32 %[sg], %[sg], " STR(NH * 8 * 1024) "\n\t"                                                      \
  "v_add_u32 v136, 1, v136\n\t"                                                                             \
  ".endif\n\t"                                                                                              \
  ".if !" STR(EPI) " && (xk == 1)\n\t"                                                                      \
  "buffer_store_dword v136, %[fo], %[rp], 0 offen sc1\n\t"                                                  \
  ".endif\n\t"                                                                                              \
  MF(3, HB, AC, EPI) XCV(EPI, PV, 3)                                                                        \
  ".if xk == 0\n\t"                                                                                         \
  "s_waitcnt vmcnt(" STR(XVM) ")\n\t" "s_barrier\n\t"                                                               \
  ".endif\n\t"                                                                                              \
  ".if xk == 2\n\t"                                                                                         \
  "s_min_u32 %[dg], %[dg], " STR(S_LEN * DM * 2 - 16384 + 1024) "\n\t"                                      \
  ".endif\n\t"                                                                                              \
  ".if " STR(EPI) " && (xk == " STR(XSTAT) ")\n\t"                                                                       \
  "buffer_store_dwordx4 v[132:135], v138, %[rf], %[sg] offen" XSC "\n\t"                                       \
  ".endif\n\t"                                                                                              \
  ".set xk, xk+1\n\t"                                                                                       \
  ".endr\n\t"
#define XLOOP(MF)                                                                                           \
  "s_mov_b32 %[kp], m0\n\t"                                                                                 \
  "v_mov_b32 v136, %[p0]\n\t" "v_mov_b32 v137, " STR(OOR) "\n\t" "v_mov_b32 v138, " STR(OOR) "\n\t"                                                                                \
  "ds_read_b128 v[96:99], %[r0]\n\tds_read_b128 v[100:103], %[r0] offset:" STR(16 * XPITCH) "\n\t"          \
  "ds_read_b128 v[104:107], %[r0] offset:64\n\tds_read_b128 v[108:111], %[r0] offset:64+" STR(16 * XPITCH) "\n\t"   \
  "ds_read_b128 v[112:115], %[r0] offset:128\n\tds_read_b128 v[116:119], %[r0] offset:128+" STR(16 * XPITCH) "\n"   \
  ".Lx%=:\n\t"                                                                                              \
  XHALF(MF, 0, 64, 80, 1, "%[r0]", "%[r1]", 3, 66560) XHALF(MF, 128, 64, 80, 0, "%[r1]", "%[r2]", 0, 131072) \
  XHALF(MF, 0, 80, 64, 1, "%[r2]", "%[r3]", 1, 132096) XHALF(MF, 128, 80, 64, 0, "%[r3]", "%[r0]", 2, 196608) \
  "s_add_u32 %[gb], %[gb], 131072\n\t"                                                                      \
  "s_sub_u32 %[cnt], %[cnt], 1\n\t"                                                                         \
  "s_cmp_lg_u32 %[cnt], 0\n\t"                                                                              \
  "s_cbranch_scc1 .Lx%=\n\t"                                                                                \
  "s_mov_b32 m0, %[kp]\n\t"                                                                                 \
  "s_nop 7\n\t" "s_waitcnt lgkmcnt(0)"

template <bool ISV>
__device__ __forceinline__ void x_role(unsigned char* lds, const u16* __restrict__ x, const u16* __restrict__ Wm, int fo0, int head,
                                       __amdgpu_buffer_rsrc_t rsf, u32* prog, u32 tag, int lane, int w) {
  const int ldsb = (int)(size_t)(LAS unsigned char*)lds;
  const __amdgpu_buffer_rsrc_t rsxin = __builtin_amdgcn_make_buffer_rsrc((void*)x, 0, S_LEN * DM * 2, 0x00020000);
  const __amdgpu_buffer_rsrc_t rsp = __builtin_amdgcn_make_buffer_rsrc((void*)prog, 0, 128, 0x00020000);
  const int lr = lane & 15, lg = lane >> 4;
  const int xrd = ldsb + lr * XPITCH + lg * 16;
  const int fob = fo0 + (ISV ? (head * 8 + 2 * w) : (head * 8 + w)) * 1024;
  const int p2 = ISV ? 1024 : 4096;
  u32x32 xw[8];
  {
    const __amdgpu_buffer_rsrc_t rw = __builtin_amdgcn_make_buffer_rsrc((void*)Wm, 0, DM * HD * 2, 0x00020000);
    const int vq = (lane >> 4) * (HD * 2) + head * 256 + (lane & 15) * 16;
#define XWISSUE(c) _Pragma("unroll") for (int j = 0; j < 8; ++j)                                                       \
      asm volatile("s_mov_b32 m0, %0\n\ts_nop 3\n\tbuffer_load_dwordx4 %1, %2, %3 offen lds"                           \
                   :: "s"(ldsb + ((c) & 3) * XSLOT + (8 * w + j) * XPITCH), "v"(vq), "s"(rw), "s"((128 * (c) + 32 * w + 4 * j) * (HD * 2)) : "memory");
#define XXISSUE(h) _Pragma("unroll") for (int j = 0; j < 8; ++j)                                                       \
      asm volatile("s_mov_b32 m0, %0\n\ts_nop 3\n\tbuffer_load_dwordx4 %1, %2, %3 offen lds"                           \
                   :: "s"(ldsb + (h) * XSLOT + (8 * w + j) * XPITCH), "v"(lane * 16), "s"(rsxin), "s"((((h) >> 1) * 32 + 8 * w + j) * 2048 + ((h) & 1) * 1024) : "memory");
    XWISSUE(0) XWISSUE(1) XWISSUE(2) XWISSUE(3)
    const int q = lr >> 2, p = lr & 3;
#pragma unroll
    for (int c = 0; c < 8; ++c) {
      asm volatile("s_waitcnt vmcnt(24)\n\ts_barrier" ::: "memory");
#pragma unroll
      for (int s = 0; s < 4; ++s)
#pragma unroll
        for (int t = 0; t < 2; ++t) {
          const unsigned char* base = lds + (c & 3) * XSLOT + (8 * s + 2 * lg) * XPITCH + q * 256 + w * 64 + p * 16 + t * 8;
          const u32x2 t0 = __builtin_bit_cast(u32x2, __builtin_amdgcn_ds_read_tr16_b64_v4i16((LAS v4i16_t*)(base)));
          const u32x2 t1 = __builtin_bit_cast(u32x2, __builtin_amdgcn_ds_read_tr16_b64_v4i16((LAS v4i16_t*)(base + XPITCH)));
          const int o = ((4 * c + s) * 2 + t) * 4;
          xw[o >> 5][(o & 31) + 0] = t0[0]; xw[o >> 5][(o & 31) + 1] = t0[1]; xw[o >> 5][(o & 31) + 2] = t1[0]; xw[o >> 5][(o & 31) + 3] = t1[1];
        }
      LBAR();
      if (c < 4) { XWISSUE(c + 4) } else if (c < 7) { XXISSUE(c - 4) }
    }
  }
  asm volatile("s_waitcnt vmcnt(8)\n\ts_barrier" ::: "memory");
  if (lane == 0 && w == 0) g_dbg[4352 + (fo0 == FO_K ? 0 : fo0 == FO_V ? 32 : 64) + head] = rt32();
  u32x16 xacc;
  {
    u32 kp, dg; int gb = 8 * w * 2048, cnt = NSEG / 2, sg = fob - NH * 8 * 1024;
#define XASM(MF)                                                                                            \
    asm volatile(XLOOP(MF)                                                                                  \
                 : "=&{v[80:95]}"(xacc), [kp] "=&s"(kp), [dg] "=&s"(dg), [gb] "+s"(gb), [cnt] "+s"(cnt), [sg] "+s"(sg) \
                 : [r0] "v"(xrd), [r1] "v"(xrd + XSLOT), [r2] "v"(xrd + 2 * XSLOT), [r3] "v"(xrd + 3 * XSLOT), [l16] "v"(lane * 16), [lp2] "v"(lane * 16 + p2), \
                   [rx] "s"(rsxin), [rf] "s"(rsf), [rp] "s"(rsp), [fo] "v"(lane == 0 && w == 0 ? 0 : OOR), [dl] "s"(ldsb + 8 * w * XPITCH), [p0] "s"(tag + XP0), \
                   "{a[0:31]}"(xw[0]), "{a[32:63]}"(xw[1]), "{a[64:95]}"(xw[2]), "{a[96:127]}"(xw[3]),      \
                   "{a[128:159]}"(xw[4]), "{a[160:191]}"(xw[5]), "{a[192:223]}"(xw[6]), "{a[224:255]}"(xw[7]) \
                 : CL64, CL72, CL96, CL112, CL120, CL8(128, 129, 130, 131, 132, 133, 134, 135), "v136", "v137", "v138", "scc", "memory");
    if (ISV) { XASM(XMF_V) } else { XASM(XMF_QK) }
  }
#pragma unroll
  for (int jj = 0; jj < 2; ++jj) {
    u32x4 pk;
#pragma unroll
    for (int e = 0; e < 4; ++e) pk[e] = pack2(__uint_as_float(xacc[8 * jj + 2 * e]), __uint_as_float(xacc[8 * jj + 2 * e + 1]));
    __builtin_amdgcn_raw_buffer_store_b128(pk, rsf, lane * 16 + p2 * jj, fob + (NSEG - 1) * (NH * 8 * 1024), 16);
  }
  asm volatile("s_waitcnt vmcnt(0)\n\ts_barrier" ::: "memory");
  if (lane == 0 && w == 0) __hip_atomic_store(prog, tag + NSEG + 8, RLX_AGENT);
}

#define SSM_LO 1
#define SU_CP 4416
#define SU_U 0
#define SU_K (4 * SU_CP)
#define SU_P (SU_K + 10400)
#define SU_D (SU_P + 2048)
#define SU_RAW (SU_D + 2592)
#define SU_CH (SU_RAW + 4128 + 32)
#define SST(i)
__device__ __forceinline__ void ssm_unit(unsigned char* sm, int d0, __amdgpu_buffer_rsrc_t rst, __amdgpu_buffer_rsrc_t rsx, unsigned char* xb, int fb, bool& uok,
                                         const u16* __restrict__ Dskip, int tid, int lane, int w) {
  unsigned char* cb = sm + w * SU_CH;
  const int dd = d0 + w;
  SST(0)
  if (!uok) {
    for (int it = 0; it < SPIN_CAP; ++it) {
      asm volatile("" ::: "memory");
      if ((int)__builtin_amdgcn_readfirstlane(__builtin_amdgcn_raw_buffer_load_b32(rsx, 0, XUDN, 16)) >= 15) break;
      asm volatile("s_sleep 8" ::: "memory");
    }
    asm volatile("" ::: "memory");
    uok = true;
  }
  f32x4 acc[3];
#pragma unroll
  for (int t = 0; t < 3; ++t) acc[t] = __builtin_bit_cast(f32x4, __builtin_amdgcn_raw_buffer_load_b128(rst, lane * 16, 2 * SSM_L * SSM_D * 2 + dd * 3072 + t * 1024, 16));
  const float dsk = bf2f(Dskip[dd]);
  u32x4 uv[4];
#pragma unroll
  for (int i = 0; i < 4; ++i) uv[i] = __builtin_amdgcn_raw_buffer_load_b128(rst, (tid + 256 * i) * 16, d0 * 4096, 16);
  const int r = lane & 31, h = lane >> 5;
  {
    const int lr = lane & 15, lg = lane >> 4;
    SST(2)
    u32* Kd = (u32*)(cb + SU_D);
#pragma unroll
    for (int e = 0; e < 4; ++e) {
      Kd[128 * (lg >> 1) + 8 * lr + 4 * (lg & 1) + e] = split_hl(acc[0][e]);
      Kd[lg < 2 ? 256 + 64 * lg + 4 * lr + e : 384 + 32 * (2 * (lg - 2) + (e >> 1)) + 2 * lr + (e & 1)] = split_hl(acc[1][e]);
      if (lg < 2 || (lg == 2 && e == 0 && lr == 0)) Kd[512 + 16 * (4 * lg + e) + lr] = split_hl(acc[2][e]);
    }
    asm volatile("s_waitcnt lgkmcnt(0)" ::: "memory");
    unsigned char* kr = cb + SU_K + (lane + 1) * 80;
#pragma unroll
    for (int c8 = 0; c8 < 4; ++c8) {
      u32 kv[8];
#pragma unroll
      for (int e = 0; e < 8; ++e) kv[e] = Kd[kidx(32 * lane + 31 - 8 * c8 - e)];
      u32x4 hi, lo;
#pragma unroll
      for (int e = 0; e < 4; ++e) {
        hi[e] = __builtin_amdgcn_perm(kv[2 * e + 1], kv[2 * e], 0x05040100u);
        lo[e] = __builtin_amdgcn_perm(kv[2 * e + 1], kv[2 * e], 0x07060302u);
      }
      *(u32x4*)(kr + 16 * c8) = hi;
      if (SSM_LO) *(u32x4*)(kr + 5200 + 16 * c8) = lo;
    }
    if (lane < 8) *(u32x4*)(cb + SU_K + (lane >> 2) * 5200 + (lane & 3) * 16) = (u32x4){0u, 0u, 0u, 0u};
  }
  SST(3)
#pragma unroll
  for (int i = 0; i < 4; ++i) {
    const int l = 2 * (tid + 256 * i) + 8;
    *(u32*)(sm + 0 * SU_CH + SU_RAW + l * 2) = __builtin_amdgcn_perm(uv[i][2], uv[i][0], 0x05040100u);
    *(u32*)(sm + 1 * SU_CH + SU_RAW + l * 2) = __builtin_amdgcn_perm(uv[i][2], uv[i][0], 0x07060302u);
    *(u32*)(sm + 2 * SU_CH + SU_RAW + l * 2) = __builtin_amdgcn_perm(uv[i][3], uv[i][1], 0x05040100u);
    *(u32*)(sm + 3 * SU_CH + SU_RAW + l * 2) = __builtin_amdgcn_perm(uv[i][3], uv[i][1], 0x07060302u);
  }
  if (lane < 2) *(u32x4*)(cb + SU_RAW + lane * (4096 + 16)) = (u32x4){0u, 0u, 0u, 0u};
  LBAR();
  {
    if (lane < 20) *(u32x4*)(cb + SU_U + (lane / 5) * SU_CP + (lane % 5) * 16) = (u32x4){0u, 0u, 0u, 0u};
#pragma unroll
    for (int i = 0; i < 5; ++i) {
      const int g = lane + 64 * i;
      if (g < 257) {
        const u32x4 a = *(const u32x4*)(cb + SU_RAW + g * 16);
        const u32x2 c = *(const u32x2*)(cb + SU_RAW + g * 16 + 16);
        unsigned char* dst = cb + SU_U + 80 + g * 16;
        *(u32x4*)(dst) = a;
        *(u32x4*)(dst + SU_CP) = (u32x4){__builtin_amdgcn_alignbyte(a[1], a[0], 2), __builtin_amdgcn_alignbyte(a[2], a[1], 2), __builtin_amdgcn_alignbyte(a[3], a[2], 2), __builtin_amdgcn_alignbyte(c[0], a[3], 2)};
        *(u32x4*)(dst + 2 * SU_CP) = (u32x4){a[1], a[2], a[3], c[0]};
        *(u32x4*)(dst + 3 * SU_CP) = (u32x4){__builtin_amdgcn_alignbyte(a[2], a[1], 2), __builtin_amdgcn_alignbyte(a[3], a[2], 2), __builtin_amdgcn_alignbyte(c[0], a[3], 2), __builtin_amdgcn_alignbyte(c[1], c[0], 2)};
      }
    }
  }
  asm volatile("s_waitcnt lgkmcnt(0)" ::: "memory");
  SST(4)
  f32x16 cv0, cv1;
  {
    const int m0 = 48 - 15 + 8 * h + r;
    const int cbo = (int)(size_t)(LAS unsigned char*)cb;
    int ua = cbo + SU_U + (m0 & 3) * SU_CP + (m0 & ~3) * 2 - 32;
    const int kb = cbo + SU_K + 16 * h;
    int a1 = kb + (33 + r) * 80, a0 = kb + (1 + r) * 80, cnt = 32;
#define CRA(S, O) "ds_read_b64 " RNG("v", S, 2) ", %[ua] offset:" STR(O) "\n\t" "ds_read_b64 " RNG("v", (S) + 2, 2) ", %[ua] offset:" STR((O) + 8) "\n\t"
#define CRB(S, ad, O) "ds_read_b128 " RNG("v", S, 4) ", " ad " offset:" STR(O) "\n\t"
#define CMF(C, A, B) "v_mfma_f32_32x32x16_bf16 " RNG("v", C, 16) ", " RNG("v", A, 4) ", " RNG("v", B, 4) ", " RNG("v", C, 16) "\n\t"
#define CUPD "v_add_u32 %[ua], 64, %[ua]\n\t" "v_subrev_u32 %[a1], 80, %[a1]\n\t" "v_subrev_u32 %[a0], 80, %[a0]\n\t" "v_max_i32 %[a0], %[a0], %[kb]\n\t"
#define CHALF(S, N, O, UPD)                                                                       \
    WLG(5) CMF(64, S, (S) + 8) UPD CRA(N, O)                                                      \
    WLG(6) CMF(80, S, (S) + 16) CRB((N) + 8, "%[a0]", O) CRB((N) + 16, "%[a1]", O)                \
    WLG(6) CMF(80, (S) + 4, (S) + 8) CRA((N) + 4, (O) + 2048)                                     \
    WLG(7) CMF(64, S, (S) + 12) CRB((N) + 12, "%[a0]", (O) + 5200)                                \
    WLG(7) CMF(80, S, (S) + 20) CRB((N) + 20, "%[a1]", (O) + 5200)                                \
    CMF(80, (S) + 4, (S) + 12)
    asm volatile(
        CRA(96, 32) CRB(104, "%[a0]", 32) CRB(112, "%[a1]", 32) CRA(100, 32 + 2048) CRB(108, "%[a0]", 32 + 5200) CRB(116, "%[a1]", 32 + 5200)
        ZER(64) ZER(68) ZER(72) ZER(76) ZER(80) ZER(84) ZER(88) ZER(92)
        ".Lc%=:\n\t"
        CHALF(96, 120, 0, "")
        CHALF(120, 96, 32, CUPD)
        "s_sub_u32 %[cnt], %[cnt], 1\n\t"
        "s_cmp_lg_u32 %[cnt], 0\n\t"
        "s_cbranch_scc1 .Lc%=\n\t"
        "s_nop 7\n\t" "s_nop 3\n\t" WLG(0)
        : "=&{v[64:79]}"(cv0), "=&{v[80:95]}"(cv1), [ua] "+v"(ua), [a0] "+v"(a0), [a1] "+v"(a1), [cnt] "+s"(cnt)
        : [kb] "v"(kb)
        : CL96, CL112, CL120, CL8(128, 129, 130, 131, 132, 133, 134, 135), CL8(136, 137, 138, 139, 140, 141, 142, 143),
          CL8(144, 145, 146, 147, 148, 149, 150, 151), CL8(152, 153, 154, 155, 156, 157, 158, 159), "scc", "memory");
  }
  SST(5)
  {
    u16* yo = (u16*)(cb + SU_K);
#pragma unroll
    for (int g = 0; g < 2; ++g)
#pragma unroll
      for (int i = 0; i < 16; i += 4) {
        const int rr = 2 * i + 4 * h, a = 32 * g + r;
        const u32x2 uu = *(const u32x2*)(cb + SU_U + (48 + 32 * a + rr) * 2);
        float y[4];
#pragma unroll
        for (int e = 0; e < 4; ++e) {
          const float uf = __uint_as_float((e & 1) ? (uu[e >> 1] & 0xffff0000u) : (uu[e >> 1] << 16));
          y[e] = rbf_hw(g ? cv1[i + e] : cv0[i + e]) + rbf_hw(uf * dsk);
        }
        *(u32x2*)(yo + a * 40 + rr) = (u32x2){pack2(y[0], y[1]), pack2(y[2], y[3])};
      }
  }
  LBAR();
  SST(6)
#pragma unroll
  for (int i = 0; i < 4; ++i) {
    const int l = 2 * (tid + 256 * i);
    u32 c[4];
#pragma unroll
    for (int j = 0; j < 4; ++j) c[j] = *(const u32*)(sm + j * SU_CH + SU_K + ((l >> 5) * 40 + (l & 31)) * 2);
    __builtin_amdgcn_raw_buffer_store_b128((u32x4){__builtin_amdgcn_perm(c[1], c[0], 0x05040100u), __builtin_amdgcn_perm(c[3], c[2], 0x05040100u),
                                                   __builtin_amdgcn_perm(c[1], c[0], 0x07060302u), __builtin_amdgcn_perm(c[3], c[2], 0x07060302u)},
                                           rst, l * 8, SSM_L * SSM_D * 2 + d0 * 4096, 16);
  }
  asm volatile("s_waitcnt vmcnt(0)" ::: "memory");
  LBAR();
  if (tid == 0) __hip_atomic_fetch_add((u32*)(xb + XYDN), 1u, RLX_AGENT);
  SST(7)
}

#define L_TOTAL 163840
__device__ __forceinline__ int ldw(__amdgpu_buffer_rsrc_t rs, int off) {
  asm volatile("" ::: "memory");
  return __builtin_amdgcn_readfirstlane(__builtin_amdgcn_raw_buffer_load_b32(rs, 0, off, 16));
}
#define GF(rt) "v[64+8*(xk&7)+4*" #rt ":64+8*(xk&7)+4*" #rt "+3]"
#define GW2 "v[128+4*xk:128+4*xk+3]"
#define GW3 "v[24+4*(xk&1):24+4*(xk&1)+3]"
#define GLOAD(S, K) "s_xor_b32 %[tq], " S ", 1024*(" K ")\n\t"
#define GTR(K) "ds_read_b128 v[24+4*((" K ")&1):24+4*((" K ")&1)+3], %[wl] offset:1024*((" K ")&31)\n\t"
#define GPACE 0
#define YAUX 16
#define UDELAY 100
#define GNOSSM 1
#define GNG 3
#define GS0 22
__device__ __forceinline__ void g_role(unsigned char* lds, const u16* __restrict__ Wo, u16* __restrict__ out, __amdgpu_buffer_rsrc_t rsx,
                                       __amdgpu_buffer_rsrc_t rsf, int fb, u32 tag, int grp, int sl, int w) {
  const int lane = fresh_lane();
  const int lr = lane & 15, lg = lane >> 4;
  const int rot = (2 * sl + 8 * w) & 31;
  const int ldsb = (int)(size_t)(LAS unsigned char*)lds;
  u32x32 xw[8], w2[4];
  LBAR();
  if (lane == 0 && w == 0) g_dbg[4480 + 16 * grp + sl] = rt32();
  {
    const __amdgpu_buffer_rsrc_t rw = __builtin_amdgcn_make_buffer_rsrc((void*)Wo, 0, DM * HD * 2, 0x00020000);
    const int vq = (lane >> 3) * (DM * 2) + sl * 128 + (lane & 7) * 16;
    const int sq = (1024 * w + rot * 32) * (DM * 2);
#define GSLOT(c) ((c) < 8 ? (((c) & 3) == 3 ? 131072 + w * 8192 : w * 32768 + 8192 * (((c) & 3) + 1)) : (((c) - 8) % 3 == 2 ? 131072 + w * 8192 : w * 32768 + 8192 * (((c) - 8) % 3 + 2)))
#define GISSUE(c) _Pragma("unroll") for (int j = 0; j < 8; ++j)                                                        \
      asm volatile("s_mov_b32 m0, %0\n\ts_nop 3\n\tbuffer_load_dwordx4 %1, %2, %3 offen lds"                           \
                   :: "s"(ldsb + GSLOT(c) + j * 1024), "v"(vq), "s"(rw), "s"(sq ^ ((64 * (c) + 8 * j) * DM * 2)) : "memory");
    GISSUE(0) GISSUE(1) GISSUE(2) GISSUE(3)
    const int q = lr >> 2, p = lr & 3;
    u32x4 w3[16];
#pragma unroll
    for (int c = 0; c < 16; ++c) {
      if (c < 5) waitvm<24>(); else if (c < 14) waitvm<16>(); else if (c == 14) waitvm<8>(); else waitvm<0>();
      u32x4 f3[2];
#pragma unroll
      for (int s = 0; s < 2; ++s)
#pragma unroll
        for (int t = 0; t < 4; ++t) {
          const unsigned char* base = lds + GSLOT(c) + (32 * s + 8 * lg + q) * 128 + (t < 2 ? p * 16 + t * 8 : t * 32 + p * 8);
          const u32x2 t0 = __builtin_bit_cast(u32x2, __builtin_amdgcn_ds_read_tr16_b64_v4i16((LAS v4i16_t*)(base)));
          const u32x2 t1 = __builtin_bit_cast(u32x2, __builtin_amdgcn_ds_read_tr16_b64_v4i16((LAS v4i16_t*)(base + 512)));
          const int o = t < 2 ? ((2 * c + s) * 2 + t) * 4 : (2 * c + s) * 4;
          if (t < 2) { xw[o >> 5][(o & 31) + 0] = t0[0]; xw[o >> 5][(o & 31) + 1] = t0[1]; xw[o >> 5][(o & 31) + 2] = t1[0]; xw[o >> 5][(o & 31) + 3] = t1[1]; }
          else if (t == 2) { w2[o >> 5][(o & 31) + 0] = t0[0]; w2[o >> 5][(o & 31) + 1] = t0[1]; w2[o >> 5][(o & 31) + 2] = t1[0]; w2[o >> 5][(o & 31) + 3] = t1[1]; }
          else f3[s] = (u32x4){t0[0], t0[1], t1[0], t1[1]};
        }
      asm volatile("s_waitcnt lgkmcnt(0)" ::: "memory");
      if (c < 8) { *(u32x4*)(lds + w * 32768 + (2 * c) * 1024 + lane * 16) = f3[0]; *(u32x4*)(lds + w * 32768 + (2 * c + 1) * 1024 + lane * 16) = f3[1]; }
      else { w3[2 * (c - 8)] = f3[0]; w3[2 * (c - 8) + 1] = f3[1]; }
      if (GPACE) __builtin_amdgcn_s_sleep(GPACE);
      if (c < 4) { GISSUE(c + 4) } else if (c >= 5 && c < 13) { GISSUE(c + 3) }
    }
#pragma unroll
    for (int i = 0; i < 16; ++i) *(u32x4*)(lds + w * 32768 + (16 + i) * 1024 + lane * 16) = w3[i];
    LBAR();
  }
  if (lane == 0 && w == 0) g_dbg[4544 + 16 * grp + sl] = rt32();
  wait_start(rsx, fb, tag, true);
  const int wl = ldsb + w * 32768 + lane * 16;
  const __amdgpu_buffer_rsrc_t rso = __builtin_amdgcn_make_buffer_rsrc((void*)out, 0, S_LEN * OUTLD * 2, 0x00020000);
  const int rw_ = ldsb + 131072 + w * 8192 + lane * 16, rr_ = ldsb + 131072 + w * 2048 + lane * 16;
  const int oa = ((16 * (w & 1) + lr) * OUTLD + 64 * sl + (w < 2 ? 8 * lg : 32 + 4 * lg)) * 2, ob = oa + (w < 2 ? 8 : 32);
  const int hq = (grp + GNG - (62 - GS0) % GNG) % GNG - 1, hf = hq < 2 ? hq : -1;
#define GRW(i) "ds_write_b128 %[rw], " RNG("v", 32 + 4 * (i), 4) " offset:" STR(1024 * (i)) "\n\t"
#define GRR(i) "ds_read_b128 " RNG("v", 32 + 4 * (i), 4) ", %[rr] offset:" STR(8192 * ((i) >> 1) + 1024 * ((i) & 1)) "\n\t"
#define GNXT(D, S) "s_cmp_lt_u32 " S ", " STR(GS0) "\n\t" "s_cselect_b32 " D ", 1, " STR(GNG) "\n\t" "s_add_u32 " D ", " D ", " S "\n\t"
#define GDN(S) "s_min_u32 s41, " S ", 63\n\t" "s_lshl_b32 s41, s41, 5\n\t" "v_add_u32 v23, s41, %[u0]\n\t" "v_lshrrev_b32 v22, 6, v23\n\t" \
               "v_and_b32 v23, 63, v23\n\t" "v_lshlrev_b32 v23, 7, v23\n\t" "s_lshr_b32 s41, s41, 6\n\t" "s_max_u32 s41, s41, 29\n\t" "s_lshl_b32 s41, s41, 13\n\t" "s_add_u32 s41, s41, %[pa]\n\t" \
               "buffer_load_dword v23, v23, %[rs], s41 offen sc1\n\t"
#define GOK(D) "v_cmp_le_i32 vcc, v23, v22\n\t" "s_cmp_eq_u64 vcc, 0\n\t" "s_cselect_b32 " D ", 1, 0\n\t"
static_assert(NAW == 64, "shifts above");
#define GEPI(MID, OA, OB) \
      GRW(0) GRW(1) GRW(2) GRW(3) GRW(4) GRW(5) GRW(6) GRW(7) \
      MID \
      WLG(0) "s_barrier\n\t" \
      GRR(0) GRR(1) GRR(2) GRR(3) GRR(4) GRR(5) GRR(6) GRR(7) \
      WLG(4) PKA(32, 32, 40) PKA(34, 34, 42) PKA(36, 36, 44) PKA(38, 38, 46) \
      WLG(0) PKA(48, 48, 56) PKA(50, 50, 58) PKA(52, 52, 60) PKA(54, 54, 62) \
      PKA(32, 32, 48) PKA(34, 34, 50) PKA(36, 36, 52) PKA(38, 38, 54) \
      "v_cvt_pk_bf16_f32 v20, v32, v33\n\t" "v_cvt_pk_bf16_f32 v21, v34, v35\n\t" "v_cvt_pk_bf16_f32 v18, v36, v37\n\t" "v_cvt_pk_bf16_f32 v19, v38, v39\n\t" \
      "buffer_store_dwordx2 v[20:21], " OA ", %[ro], s42 offen\n\t" \
      "buffer_store_dwordx2 v[18:19], " OB ", %[ro], s42 offen\n\t"
#define GH "v[64+4*(xk&15):64+4*(xk&15)+3]"
  asm volatile(
      "s_nop 4\n\t"
      "v_add_u32 v16, 131072, %[va]\n\t" "v_sub_u32 v15, %[ob], %[oa]\n\t" "v_add_u32 v15, v15, %[oc]\n\t"
      "s_mov_b32 s36, %[s0]\n\t" "s_mov_b32 s43, " STR(SPIN_CAP) "\n"
      ".Lp%=:\n\t"
      GDN("s36") WVM(0) GOK("s40")
      "s_cbranch_scc1 .Lr%=\n\t"
      "s_sleep 2\n\t" "s_sub_u32 s43, s43, 1\n\t" "s_cmp_gt_i32 s43, 0\n\t" "s_cbranch_scc1 .Lp%=\n"
      ".Lr%=:\n\t"
      GNXT("s37", "s36") GDN("s37")
      "s_lshl_b32 s38, s36, 18\n\t" "s_add_u32 s38, s38, %[gb]\n\t"
      ".set xk, 0\n\t.rept 8\n\t" GLOAD("s38", "xk")
      "buffer_load_dwordx4 " GF(0) ", %[va], %[rf], %[tq] offen sc1\n\t" "buffer_load_dwordx4 " GF(1) ", v16, %[rf], %[tq] offen sc1\n\t"
      ".set xk, xk+1\n\t.endr\n\t"
      WVM(16) GOK("s40")
      ".Lb%=:\n\t"
      GNXT("s37", "s36") GNXT("s41", "s37") GDN("s41") GTR("0")
      "s_cmp_lt_u32 s37, 63\n\t" "s_cselect_b32 s40, s40, 0\n\t"
      "s_cmp_lg_u32 s40, 0\n\t" "s_cselect_b32 s39, s37, s36\n\t"
      "s_lshl_b32 s39, s39, 18\n\t" "s_add_u32 s39, s39, %[gb]\n\t" "s_lshl_b32 s38, s36, 18\n\t" "s_add_u32 s38, s38, %[gb]\n\t"
      "s_lshl_b32 s42, s36, 17\n\t"
      "buffer_load_dword v17, %[vt], %[rf], s39 offen sc1\n\t"
      ".set xk, 0\n\t.rept 32\n\t"
      "s_waitcnt vmcnt(14)\n\t"
      XM1(XA(32, 0), XW(0, 0), GF(0), 1) GTR("xk+1") XM1(XA(32, 4), XW(0, 1), GF(0), 1)
      ".if xk < 24\n\t" GLOAD("s38", "xk+8") ".else\n\t" GLOAD("s39", "xk-24") ".endif\n\t"
      XM1(XA(32, 8), XW(0, 0), GF(1), 1) XM1(XA(32, 12), XW(0, 1), GF(1), 1)
      XM1(XA(32, 16), GW2, GF(0), 1) "s_waitcnt lgkmcnt(1)\n\t" XM1(XA(32, 20), GW3, GF(0), 1)
      "buffer_load_dwordx4 " GF(0) ", %[va], %[rf], %[tq] offen sc1\n\t"
      XM1(XA(32, 24), GW2, GF(1), 1) XM1(XA(32, 28), GW3, GF(1), 1)
      "buffer_load_dwordx4 " GF(1) ", v16, %[rf], %[tq] offen sc1\n\t"
      ".if xk == 16\n\t" "s_barrier\n\t" ".endif\n\t"
      ".set xk, xk+1\n\t.endr\n\t"
      GEPI(GOK("s41"), "%[oa]", "%[ob]")
      "s_mov_b32 s36, s37\n\t"
      "s_cmp_lg_u32 s40, 0\n\t" "s_mov_b32 s40, s41\n\t" "s_cbranch_scc1 .Lb%=\n\t"
      "s_cmp_lt_u32 s36, 63\n\t" "s_cbranch_scc1 .Lp%=\n\t"
      "s_cmp_lt_i32 %[hf], 0\n\t" "s_cbranch_scc1 .Le%=\n\t"
      "s_mov_b32 s36, 63\n"
      ".Lh%=:\n\t"
      GDN("s36") WVM(0) GOK("s40")
      "s_cbranch_scc1 .Li%=\n\t"
      "s_sleep 2\n\t" "s_sub_u32 s43, s43, 1\n\t" "s_cmp_gt_i32 s43, 0\n\t" "s_cbranch_scc1 .Lh%=\n"
      ".Li%=:\n\t"
      "s_lshl_b32 s38, %[hf], 17\n\t" "s_add_u32 s38, s38, 63*262144\n\t" "s_add_u32 s38, s38, %[gb]\n\t"
      ".set xk, 0\n\t.rept 16\n\t" GLOAD("s38", "xk") "buffer_load_dwordx4 " GH ", %[va], %[rf], %[tq] offen sc1\n\t" ".set xk, xk+1\n\t.endr\n\t"
      GTR("0")
      "s_lshl_b32 s42, %[hf], 16\n\t" "s_add_u32 s42, s42, 63*131072\n\t"
      ".set xk, 0\n\t.rept 32\n\t"
      ".if xk < 17\n\t" "s_waitcnt vmcnt(15)\n\t" ".else\n\t" "s_waitcnt vmcnt(31-xk)\n\t" ".endif\n\t"
      XM1(XA(32, 0), XW(0, 0), GH, 1) GTR("xk+1") XM1(XA(32, 4), XW(0, 1), GH, 1)
      XM1(XA(32, 16), GW2, GH, 1) "s_waitcnt lgkmcnt(1)\n\t" XM1(XA(32, 20), GW3, GH, 1)
      ".if xk < 16\n\t" GLOAD("s38", "xk+16") "buffer_load_dwordx4 " GH ", %[va], %[rf], %[tq] offen sc1\n\t" ".endif\n\t"
      ".if xk == 16\n\t" "s_barrier\n\t" ".endif\n\t"
      ".set xk, xk+1\n\t.endr\n\t"
      "s_nop 7\n\t"
      GEPI("", "%[oc]", "v15")
      ".Le%=:\n\t"
      WVM(0)
      : [tq] "=&s"(sl)
      : [rs] "s"(rsx), [s0] "s"(__builtin_amdgcn_readfirstlane(grp ? GS0 + grp : 0)), [gb] "s"(FO_O - 32768 + (32 * w + rot) * 1024), [va] "v"(lane * 16 + 32768),
        [rf] "s"(rsf), [wl] "v"(wl), [rw] "v"(rw_), [rr] "v"(rr_), [oa] "v"(oa), [ob] "v"(ob), [ro] "s"(rso), [u0] "v"(8 * w + (lane & 7)), [pa] "s"(PGO_A - 29 * 8192), [hf] "s"(__builtin_amdgcn_readfirstlane(hf)), [oc] "v"((w & 1) ? OOR - 64 : oa), [vt] "v"((sl >> 3) * 131072 + 32768 + (sl & 7) * 4096 + lane * 64 - rot * 1024),
        "{a[0:31]}"(xw[0]), "{a[32:63]}"(xw[1]), "{a[64:95]}"(xw[2]), "{a[96:127]}"(xw[3]),
        "{a[128:159]}"(xw[4]), "{a[160:191]}"(xw[5]), "{a[192:223]}"(xw[6]), "{a[224:255]}"(xw[7]),
        "{v[128:159]}"(w2[0]), "{v[160:191]}"(w2[1]), "{v[192:223]}"(w2[2]), "{v[224:255]}"(w2[3])
      : "v15", "v16", "v17", "v18", "v19", "v20", "v21", "v22", "v23", "vcc", CL8(24, 25, 26, 27, 28, 29, 30, 31), CL8(32, 33, 34, 35, 36, 37, 38, 39), CL8(40, 41, 42, 43, 44, 45, 46, 47),
        CL8(48, 49, 50, 51, 52, 53, 54, 55), CL8(56, 57, 58, 59, 60, 61, 62, 63), CL64, CL72, CL80, CL88, CL96, CL112, CL120,
        "s36", "s37", "s38", "s39", "s40", "s41", "s42", "s43", "scc", "memory");
}
static_assert(4 * SU_CH <= L_TOTAL && 4 * XSLOT <= L_TOTAL, "lds");
__global__ void __launch_bounds__(256, 1)
main_kernel(const u16* __restrict__ x, const u16* __restrict__ mem0, const u16* __restrict__ Wq, const u16* __restrict__ Wk, const u16* __restrict__ Wv, const u16* __restrict__ Wo,
            unsigned char* ws, const u16* __restrict__ u, const u16* __restrict__ A_re, const u16* __restrict__ A_im, const u16* __restrict__ B_re,
            const u16* __restrict__ B_im, const u16* __restrict__ C_re, const u16* __restrict__ C_im,
            const u16* __restrict__ log_dt, const u16* __restrict__ Dskip, u16* __restrict__ out
            , int tun_r, int tun_p, int tun_b, int tun_c
            ) {
  __shared__ __attribute__((aligned(8192))) unsigned char lds[L_TOTAL];
  const int w = __builtin_amdgcn_readfirstlane(threadIdx.x >> 6);
  u64 pc0;
  int clen;
  {
    asm volatile("s_getpc_b64 %0\n.Lkpc0:\n\ts_mov_b32 %1, .Lkpc1-.Lkpc0" : "=s"(pc0), "=s"(clen));
  }
#define LOCAL_IDS const int lane = fresh_lane(), tid = w * 64 + lane; (void)tid;
  const int si = (blockIdx.x >> 3) * 4 + (blockIdx.x & 3);
  unsigned char* xb = (unsigned char*)(((size_t)ws + XPAGE - 1) & ~(size_t)(XPAGE - 1));
  xb += (size_t)(tun_r >> 8) * XPAGE; tun_r &= 255;
  const int fb = (int)(ws + WS_F - xb);
  u32 tag;
  asm volatile("s_load_dword %0, %1, 0x0\n\ts_waitcnt lgkmcnt(0)\n\ts_add_u32 %0, %0, 256" : "=s"(tag) : "s"(ws + WS_F) : "scc", "memory");
  const __amdgpu_buffer_rsrc_t rsx = __builtin_amdgcn_make_buffer_rsrc(xb, 0, fb + (int)WS_F_SZ, 0x00020000);
  const __amdgpu_buffer_rsrc_t rsf = __builtin_amdgcn_make_buffer_rsrc(ws + WS_QX, 0, (int)(4 * WS_QKV_SZ + 2 * WS_KM_SZ), 0x00020000);
  const __amdgpu_buffer_rsrc_t rst = __builtin_amdgcn_make_buffer_rsrc(ws + WS_UT, 0, 2 * SSM_L * SSM_D * 2 + SSM_D * 3072, 0x00020000);
  u32* dbg = (u32*)(ws + WS_TOTAL + (8 << 20));
  const u32 tstart = rt32();
  g_dbg = dbg;
  if ((blockIdx.x & 7) < 4) {
    { LOCAL_IDS
    r_role(lds, si, mem0, Wq, Wk, Wv, Wo, fb, tag, pc0, clen, rsx, rsf, rst, SsmPtrs{A_re, A_im, B_re, B_im, C_re, C_im, log_dt}, tid, lane, w
           , dbg, tun_r, tun_p, tun_b, tun_c
           ); }
    LOCAL_IDS
    if (tid == 0) dbg[2048 + blockIdx.x * 4 + 2] = rt32();
    {
      for (int it = 0; it < SPIN_CAP; ++it) {
        if (ldw(rsx, XYDN) >= 255) break;
        asm volatile("s_sleep 8" ::: "memory");
      }
      asm volatile("" ::: "memory");
      asm volatile(".Lkpc1:" ::: "memory");
      unsigned char* im = lds + 65536;
      const __amdgpu_buffer_rsrc_t rso = __builtin_amdgcn_make_buffer_rsrc((void*)out, 0, S_LEN * OUTLD * 2, 0x00020000);
      u32x4 t[8];
#pragma unroll
      for (int i = 0; i < 8; ++i) { const int p = i * 256 + tid; t[i] = __builtin_amdgcn_raw_buffer_load_b128(rst, (p >> 3) * 16384 + (p & 7) * 16, SSM_L * SSM_D * 2 + si * 128, 16); }
#pragma unroll
      for (int i = 0; i < 8; ++i) {
        const int p = i * 256 + tid, un = p >> 3, rp = p & 7;
        *(u32x2*)(im + (2 * rp) * 2064 + un * 8) = (u32x2){t[i][0], t[i][1]};
        *(u32x2*)(im + (2 * rp + 1) * 2064 + un * 8) = (u32x2){t[i][2], t[i][3]};
      }
      LBAR();
#pragma unroll
      for (int i = 0; i < 8; ++i) {
        const int p = i * 256 + tid;
        __builtin_amdgcn_raw_buffer_store_b128(*(const u32x4*)(im + (p >> 7) * 2064 + (p & 127) * 16), rso, ((p >> 7) * OUTLD + DM + (p & 127) * 8) * 2, si * 16 * OUTLD * 2, YAUX);
      }
    }
  } else {
    const int g = blockIdx.x & 3, li = blockIdx.x >> 3;
    const int xi = g == 0 ? (li < 16 ? li : -1) : (li < 26 + (g < 3) ? 16 + 27 * (g - 1) + li : -1);
    const int ei = g == 0 ? li - 16 : 16 + 5 * (g - 1) + (31 - li);
    if (xi >= 0) {
      LOCAL_IDS
      const int head = xi & 31, mi = xi >> 5;
      u32* prog = (u32*)(xb + PGO_X + xi * 128);
      if (mi == 1) x_role<true>(lds, x, Wv, FO_V, head, rsf, prog, tag, lane, w);
      else x_role<false>(lds, x, mi == 0 ? Wk : Wq, mi == 0 ? FO_K : FO_Q, head, rsf, prog, tag, lane, w);
    } else {
      LOCAL_IDS
      u32 acc = 0;
#pragma unroll
      for (int i = 0; i < 4; ++i) acc += *(const u32*)(x + (size_t)((i * 32 + ei) * 256 + tid) * 64);
#define UV 0
#define WWARM(n, first) {    \
        const __amdgpu_buffer_rsrc_t rwo = __builtin_amdgcn_make_buffer_rsrc((void*)Wo, 0, DM * HD * 2, 0x00020000); \
        const int ldsb = (int)(size_t)(LAS unsigned char*)lds; \
        _Pragma("unroll 1") for (int c = 0; c < (n); ++c) { dma16(rwo, lane * 16, ((first) + c) * 16384, 1024, ldsb + w * 32768 + (c & 1) * 16384); waitvm<32>(); } }
      if (UV == 0 || (UV == 1 && ei < 16)) WWARM(4, (ei * 4 + w) * 4)
      if (UV == 2 && ei < 16) WWARM(8, (ei * 4 + w) * 8)
      if (ei == 0 && tid < 16) acc += *(const u32*)(Dskip + tid * 64);
      asm volatile("" :: "v"(acc));
      if (ei >= 16) {
      { const u32 t0 = rt32(); for (int i = 0; i < 1000 && (int)(rt32() - t0) < UDELAY; ++i) asm volatile("s_sleep 16" ::: "memory"); }
      asm volatile("s_waitcnt vmcnt(0)\n\ts_barrier" ::: "memory");
      for (int ps = 0; ps < 2; ++ps) {
      const int r0 = ((ei - 16) * 2 + ps) * 64;
#pragma unroll
      for (int hb = 0; hb < 2; ++hb) {
        u32x4 t[16];
#pragma unroll
        for (int i = 0; i < 16; ++i) t[i] = *(const u32x4*)(u + (size_t)r0 * SSM_D + ((hb * 16 + i) * 256 + tid) * 8);
#pragma unroll
        for (int i = 0; i < 16; ++i) { const int p = (hb * 16 + i) * 256 + tid; *(u32x4*)(lds + (p >> 7) * 2064 + (p & 127) * 16) = t[i]; }
      }
      LBAR();
      for (int i = 0; i < 32; ++i) {
        const int p = i * 256 + tid, un = p >> 5, rp = p & 31;
        const u32x2 a = *(const u32x2*)(lds + (2 * rp) * 2064 + un * 8), c = *(const u32x2*)(lds + (2 * rp + 1) * 2064 + un * 8);
        __builtin_amdgcn_raw_buffer_store_b128((u32x4){a[0], a[1], c[0], c[1]}, rst, un * 16384 + rp * 16, r0 * 8, 16);
      }
      asm volatile("s_waitcnt vmcnt(0) lgkmcnt(0)\n\ts_barrier" ::: "memory");
      }
      if (tid == 0) dbg[4672 + ei] = rt32();
      if (UV == 1) { WWARM(4, (ei * 4 + w) * 4) waitvm<0>(); }
      wait_start(rsx, fb, tag, true);
      if (tid == 0) __hip_atomic_fetch_add((u32*)(xb + XUDN), 1u, RLX_AGENT);
      }
    }
    if (w == 0 && fresh_lane() == 0) dbg[2048 + blockIdx.x * 4 + 2] = rt32();
    if (w == 0 && fresh_lane() == 0) dbg[2048 + blockIdx.x * 4 + 3] = rt32();
    asm volatile("s_waitcnt lgkmcnt(0)\n\ts_barrier" :: "s"(tag) : "memory");
    if (w == 0 && fresh_lane() == 0) __builtin_amdgcn_raw_buffer_store_b32(tag, rsx, 0, PGO_S + si * 128, 16);
    const bool isg = g == 0 ? li >= 16 : (g >= 5 - GNG && li < 16);
    if (xi >= 0 && !(GNOSSM && isg)) {
      LOCAL_IDS
      bool uok = false;
      wait_start(rsx, fb, tag, true);
      while (true) {
        LBAR();
        if (tid == 0) *(u32*)(lds + L_TOTAL - 16) = __hip_atomic_fetch_add((u32*)(xb + XTKT), 1u, RLX_AGENT) + 1u;
        LBAR();
        const int un = __builtin_amdgcn_readfirstlane(*(const u32*)(lds + L_TOTAL - 16));
        if (un >= 256) break;
        ssm_unit(lds, 4 * un, rst, rsx, xb, fb, uok, Dskip, tid, lane, w);
        if (isg) break;
      }
      if (tid == 0) dbg[2048 + blockIdx.x * 4 + 3] = rt32();
    }
    if (isg) {
      g_role(lds, Wo, out, rsx, rsf, fb, tag, g ? g + GNG - 4 : 0, li & 15, w);
      if (w == 0 && fresh_lane() == 0) g_dbg[4608 + 16 * (g ? g + GNG - 4 : 0) + (li & 15)] = rt32();
    }
    else {
      const int lane = fresh_lane();
      int sh = (ei - 16) * 4 + w, lim = (NSEG - 4) * NH, wi = sh, dn = 0;
      bool go = xi < 0;
      while (true) {
        if (go) a_role(rsx, rsf, sh, lim, wi, dn, lane, fb, tag);
        if (xi >= 0 && (w >= 2 || go)) break;
        const int t = __builtin_amdgcn_readfirstlane(lane == 0 ? __hip_atomic_fetch_add((u32*)(xb + XTK2), 1u, RLX_AGENT) + 1u : 0u);
        if (t >= 128) break;
        wi = 64 + t; sh = (NSEG - 4) * NH + t; lim = sh + 1; dn = 31; go = true;
      }
    }
  }
  if (w == 0 && fresh_lane() == 0) { dbg[2048 + blockIdx.x * 4] = tstart; dbg[2048 + blockIdx.x * 4 + 1] = rt32(); }
}

__global__ void diag_kernel(unsigned char* ws) {
  const u32* h = (const u32*)(ws + WS_TOTAL + (8 << 20));
  __shared__ int mn, mx;
  const int b = threadIdx.x;
  if (b == 0) { mn = 1 << 30; mx = -(1 << 30); }
  __syncthreads();
  const u32 ref = h[2048];
  atomicMin(&mn, (int)(h[2048 + b * 4] - ref));
  __syncthreads();
  const u32 t0 = ref + mn;
  const bool r = (b & 7) < 4; const int li = b >> 3;
  int v = 0;
  if (DIAG == 1) v = h[0] - t0;
  if (DIAG == 2) v = r ? h[2048 + b * 4 + 2] - t0 : 0;
  if (DIAG == 3) v = h[2048 + b * 4 + 1] - t0;
  if (DIAG == 4) v = h[3504] * 4;
  if (DIAG == 5) v = b < 128 ? h[3072 + b * 2] - t0 : 0;
  if (DIAG == 6) v = (h[3500] >> 20) * 25;
  if (DIAG == 7) v = r ? 0 : h[2048 + b * 4 + 3] - t0;
  if (DIAG == 8) v = (!r && (b & 3) == 0 && li >= 16) ? h[2048 + b * 4 + 1] - t0 : 0;
  if (DIAG == 9) v = h[2048 + b * 4] - t0;
  if (DIAG == 10) v = h[3503] - t0;
  if (DIAG == 11) v = (b >= 16 && b < 32) ? h[4672 + b] - t0 : 0;
  if (DIAG == 12) v = r ? h[2048 + b * 4 + 1] - t0 : 0;
  if (DIAG == 13) v = r ? 0 : h[2048 + b * 4 + 2] - t0;
  if (DIAG == 14) v = rt32() - t0;
  if (DIAG == 15) v = (!r && (b & 3) >= 1 && li < 16) ? h[2048 + b * 4 + 1] - t0 : 0;
  if (DIAG == 16) v = b < 96 ? h[4352 + b] - t0 : 0;
  if (DIAG == 17) v = h[1] - t0;
  atomicMax(&mx, v);
  __syncthreads();
  v = min(max(mx, 0), 100000);
  if (b == 0) for (int i = 0; i < 4000000 && (int)(rt32() - t0) < 30000 + 4 * v; ++i) asm volatile("s_sleep 1" ::: "memory");
}
extern "C" void kernel_launch(void* const* d_in, const int* in_sizes, int n_in, void* d_out, int out_size,
                              void* d_ws, size_t ws_size, hipStream_t stream) {
  const u16* x = (const u16*)d_in[0];
  const u16* mem0 = (const u16*)d_in[1];
  const u16* Wq = (const u16*)d_in[2];
  const u16* Wk = (const u16*)d_in[3];
  const u16* Wv = (const u16*)d_in[4];
  const u16* Wo = (const u16*)d_in[5];
  const u16* u = (const u16*)d_in[7];
  const u16* A_re = (const u16*)d_in[8];
  const u16* A_im = (const u16*)d_in[9];
  const u16* B_re = (const u16*)d_in[10];
  const u16* B_im = (const u16*)d_in[11];
  const u16* C_re = (const u16*)d_in[12];
  const u16* C_im = (const u16*)d_in[13];
  const u16* log_dt = (const u16*)d_in[14];
  const u16* Dskip = (const u16*)d_in[15];
  u16* out = (u16*)d_out;
  char* ws = (char*)d_ws;

  main_kernel<<<256, 256, 0, stream>>>(x, mem0, Wq, Wk, Wv, Wo, (unsigned char*)ws, u, A_re, A_im, B_re, B_im, C_re, C_im, log_dt, Dskip, out
      , g_tune[0], g_tune[1], g_tune[2], g_tune[3]
      );
  diag_kernel<<<1, 256, 0, stream>>>((unsigned char*)ws);
}
```

```cpp
#define TUNE
#define DIAG 52
#define PREG 256
#include <hip/hip_runtime.h>
#include <hip/hip_bf16.h>
#include <stdint.h>

#define S_LEN 2048
#define DM 1024
#define NH 32
#define DH 128
#define HD 4096
#define MEM 8
#define SEG 32
#define NSEG 64
#define LTOK 40
#define SSM_L 2048
#define SSM_D 1024
#define SSM_N 128
#define OUTLD 2048

typedef unsigned short u16;
typedef unsigned int u32;
typedef unsigned long long u64;
typedef __attribute__((ext_vector_type(8))) short bf16x8;
typedef __attribute__((ext_vector_type(4))) float f32x4;
typedef __attribute__((ext_vector_type(4))) unsigned int u32x4;
typedef __attribute__((ext_vector_type(2))) unsigned int u32x2;
typedef __bf16 bf16x2_t __attribute__((ext_vector_type(2)));
typedef short v4i16_t __attribute__((ext_vector_type(4)));

#define SENT 0xFFFFFFFFu
#define RLX_AGENT __ATOMIC_RELAXED, __HIP_MEMORY_SCOPE_AGENT

__device__ __forceinline__ float bf2f(u16 v) { return __uint_as_float(((u32)v) << 16); }
__device__ __forceinline__ u16 f2bf(float f) {
  u32 u = __float_as_uint(f);
  u32 r = (u + 0x7fffu + ((u >> 16) & 1u)) >> 16;
  if ((u & 0x7fffffffu) > 0x7f800000u) r = 0x7fc0u;
  return (u16)r;
}
__device__ __forceinline__ float rbf(float f) { return bf2f(f2bf(f)); }

#define TR 92
#define TP 176
#define TDB 60
#define TDC 108
#define NR1 4
#define XPAGE (2 << 20)
#define XRING 3
#define XLP(p, L) ((((L) >> 13) << 15) + ((p) << 14) + ((L) & 8191))
#define XO_R3 131072
#define XR1A(x, L) XLP((x) & 1, ((x) >> 1) * 16384 + (L))
#define XR3A(rg, L) (XO_R3 + XLP(rg, L))
#define XSB(s) (((s) % XRING) == 2 ? 1048576 : ((s) % XRING) * 262144)
#define XR2B 524288
#define XR2S 1048576
#define XR2(j) (((((j) & 31) + (((j) >> 6) << 5)) / 6 << 15) + (((((j) >> 5) & 1) ^ 1) << 14) + ((((j) & 31) + (((j) >> 6) << 5)) % 6) * 1280)
#define XSYN (1048576 + 262144)
#define PGO_R (XSYN + 32768)
#define XCAL (PGO_R + 32768)
static_assert(XCAL + 16384 <= XPAGE, "exchange page");
#define WS_X 0ull
#define NPG 2
#define WS_X_SZ ((u64)NPG * XPAGE)
#define WS_F (WS_X + WS_X_SZ)
#define XEPO (fb)
#define XSTA (fb + 128)
#define PGO_X (fb + 256)
#define PGO_Y (PGO_X + 96 * 128)
#define PGO_S (PGO_Y + 128 * 128)
#define PGO_D (PGO_S + 128 * 128)
#define PGO_A (PGO_D + 128 * 128)
#define XTKT (PGO_A + 192 * 128)
#define XUDN (XTKT + 128)
#define XYDN (XUDN + 128)
#define XTK2 (XYDN + 128)
#define XSTR(x) (XTK2 + 128 + (x) * 128)
#define WS_F_SZ ((u64)(2 + 96 + 128 + 128 + 128 + 192 + 8) * 128)
#define WS_XCH_END (WS_F + 131072)
#define WS_QX (WS_XCH_END)
#define WS_QKV_SZ ((u64)S_LEN * HD * 2)
#define WS_KX (WS_QX + WS_QKV_SZ)
#define WS_VX (WS_KX + WS_QKV_SZ)
#define WS_OX (WS_VX + WS_QKV_SZ)
#define WS_KM (WS_OX + WS_QKV_SZ)
#define WS_KM_SZ ((u64)NSEG * 8 * HD * 2)
#define WS_VM (WS_KM + WS_KM_SZ)
#define WS_UT (WS_VM + WS_KM_SZ)
#define WS_YT (WS_UT + (u64)SSM_L * SSM_D * 2)
#define WS_KD (WS_YT + (u64)SSM_L * SSM_D * 2)
#define WS_TOTAL (WS_KD + (u64)SSM_D * 3072)

#define SPIN_CAP 400000
#define TRY_CAP 20000
#define LAS __attribute__((address_space(3)))
#define LBAR() asm volatile("s_waitcnt lgkmcnt(0)\n\ts_barrier" ::: "memory")
__device__ __forceinline__ u32 max4u(u32x4 v) { return max(max(v[0], v[1]), max(v[2], v[3])); }
typedef float f32x2_t __attribute__((ext_vector_type(2)));
__device__ __forceinline__ u32 pack2(float a, float b) {
  f32x2_t v = {a, b};
  bf16x2_t r = __builtin_convertvector(v, bf16x2_t);
  return __builtin_bit_cast(u32, r);
}
__device__ __forceinline__ float rbf_hw(float a) { return __uint_as_float(pack2(a, a) & 0xffff0000u); }
__device__ __forceinline__ u32 nosent(u32 v) { return min(v, 0xFFFFFFFEu); }
template <int N> __device__ __forceinline__ void waitvm() { asm volatile("s_waitcnt vmcnt(%0)" ::"n"(N) : "memory"); }
typedef __attribute__((ext_vector_type(16))) float f32x16;
__device__ __forceinline__ u32 split_hl(float kv) {
  const u32 h = pack2(kv, kv) & 0xffffu;
  const float r1 = kv - __uint_as_float(h << 16);
  return h | (pack2(r1, r1) & 0xffff0000u);
}
__device__ __forceinline__ int kidx(int idx) {
  const int sh = idx < 256 ? 0 : (idx < 512 ? 1 : (idx < 1024 ? 2 : 3));
  const int q = sh ? ((idx + (1 << (sh - 1)) - 1 + ((idx >> sh) & 1)) >> sh) : idx;
  return q + 128 * sh;
}

__device__ u32* g_dbg;
#define FO_Q 0
#define FO_K ((int)WS_QKV_SZ)
#define FO_V ((int)(2 * WS_QKV_SZ))
#define FO_O ((int)(3 * WS_QKV_SZ))
#define FO_KM ((int)(4 * WS_QKV_SZ))
#define FO_VM ((int)(4 * WS_QKV_SZ + WS_KM_SZ))
__device__ __forceinline__ u32x4 wait_start(__amdgpu_buffer_rsrc_t rsx, int fb, u32 tag, bool slow, int xcd = 0) {
  u32x4 st;
  for (int it = 0; it < SPIN_CAP; ++it) {
    asm volatile("" ::: "memory");
    st = __builtin_amdgcn_raw_buffer_load_b128(rsx, 0, slow ? XSTA : XSTR(xcd), 16);
    if ((u32)__builtin_amdgcn_readfirstlane(st[3]) == tag) break;
    if (slow) asm volatile("s_sleep 20" ::: "memory"); else asm volatile("s_sleep 1" ::: "memory");
  }
  asm volatile("" ::: "memory");
  return st;
}
__device__ __forceinline__ int xprog(u32 v, u32 tag) { return v - tag <= 80u ? (int)(v - tag) : -1; }
__device__ __forceinline__ float xmax4(float m) {
  { auto r = __builtin_amdgcn_permlane16_swap(__float_as_uint(m), __float_as_uint(m), false, false); m = __builtin_fmaxf(__uint_as_float(r[0]), __uint_as_float(r[1])); }
  { auto r = __builtin_amdgcn_permlane32_swap(__float_as_uint(m), __float_as_uint(m), false, false); m = __builtin_fmaxf(__uint_as_float(r[0]), __uint_as_float(r[1])); }
  return m;
}
__device__ __forceinline__ float xsum4(float m) {
  { auto r = __builtin_amdgcn_permlane16_swap(__float_as_uint(m), __float_as_uint(m), false, false); m = __uint_as_float(r[0]) + __uint_as_float(r[1]); }
  { auto r = __builtin_amdgcn_permlane32_swap(__float_as_uint(m), __float_as_uint(m), false, false); m = __uint_as_float(r[0]) + __uint_as_float(r[1]); }
  return m;
}
#define MF16(a, b, c) __builtin_amdgcn_mfma_f32_16x16x32_bf16(__builtin_bit_cast(bf16x8, a), __builtin_bit_cast(bf16x8, b), c, 0, 0, 0)
__device__ __forceinline__ f32x4 rnd2(f32x4 v, float sc) {
  const u32 a = pack2(v[0], v[1]), b = pack2(v[2], v[3]);
  const u32 c = pack2(__uint_as_float(a << 16) * sc, __uint_as_float(a & 0xffff0000u) * sc), d = pack2(__uint_as_float(b << 16) * sc, __uint_as_float(b & 0xffff0000u) * sc);
  return (f32x4){__uint_as_float(c << 16), __uint_as_float(c & 0xffff0000u), __uint_as_float(d << 16), __uint_as_float(d & 0xffff0000u)};
}
struct XaOps { u32x4 q[2][4], k[2][4], v[8]; };
__device__ __forceinline__ void xa_load(XaOps& o, __amdgpu_buffer_rsrc_t rsf, int sh, int lane) {
#pragma unroll
  for (int i = 0; i < 8; ++i) {
    o.q[i >> 2][i & 3] = __builtin_amdgcn_raw_buffer_load_b128(rsf, lane * 16, FO_Q + (sh * 8 + i) * 1024, 16);
    o.k[i >> 2][i & 3] = __builtin_amdgcn_raw_buffer_load_b128(rsf, lane * 16, FO_K + (sh * 8 + i) * 1024, 16);
  }
#pragma unroll
  for (int dt = 0; dt < 8; ++dt) o.v[dt] = __builtin_amdgcn_raw_buffer_load_b128(rsf, lane * 16, FO_V + (sh * 8 + dt) * 1024, 16);
}
__device__ __forceinline__ void xa_compute(const XaOps& o, const u32x4 (&km)[4], const u32x2 (&vm)[8], __amdgpu_buffer_rsrc_t rsf, int sh, int lane) {
  const int lr = lane & 15, lg = lane >> 4;
  const float scale = 0.08838834764831845f, L2E = 1.4426950408889634f, NEG = -1000000000.0f;
  const f32x4 z = (f32x4){0, 0, 0, 0};
  f32x4 s00 = z, s0m = z, s10 = z, s11 = z, s1m = z;
#pragma unroll
  for (int ks = 0; ks < 4; ++ks) { s00 = MF16(o.k[0][ks], o.q[0][ks], s00); s10 = MF16(o.k[0][ks], o.q[1][ks], s10); s11 = MF16(o.k[1][ks], o.q[1][ks], s11); }
#pragma unroll
  for (int ks = 0; ks < 4; ++ks) { s0m = MF16(km[ks], o.q[0][ks], s0m); s1m = MF16(km[ks], o.q[1][ks], s1m); }
  s00 = rnd2(s00, scale); s10 = rnd2(s10, scale); s11 = rnd2(s11, scale); s0m = rnd2(s0m, scale); s1m = rnd2(s1m, scale);
  asm volatile("" : "+v"(s00), "+v"(s11), "+v"(s0m), "+v"(s1m));
#pragma unroll
  for (int e = 0; e < 4; ++e) {
    const bool msk = 4 * lg + e > lr;
    s00[e] = msk ? NEG : s00[e]; s11[e] = msk ? NEG : s11[e];
    s0m[e] = lg >= 2 ? -INFINITY : s0m[e]; s1m[e] = lg >= 2 ? -INFINITY : s1m[e];
  }
  float m0 = NEG, m1 = -INFINITY;
#pragma unroll
  for (int e = 0; e < 4; ++e) { m0 = __builtin_fmaxf(m0, __builtin_fmaxf(s00[e], s0m[e])); m1 = __builtin_fmaxf(__builtin_fmaxf(m1, s10[e]), __builtin_fmaxf(s11[e], s1m[e])); }
  m0 = xmax4(m0); m1 = xmax4(m1);
  float t0 = 0.f, t1 = 0.f;
#pragma unroll
  for (int e = 0; e < 4; ++e) {
    s00[e] = __builtin_amdgcn_exp2f((s00[e] - m0) * L2E); s0m[e] = __builtin_amdgcn_exp2f((s0m[e] - m0) * L2E);
    s10[e] = __builtin_amdgcn_exp2f((s10[e] - m1) * L2E); s11[e] = __builtin_amdgcn_exp2f((s11[e] - m1) * L2E); s1m[e] = __builtin_amdgcn_exp2f((s1m[e] - m1) * L2E);
    t0 += s00[e] + s0m[e]; t1 += (s10[e] + s11[e]) + s1m[e];
  }
  const float em = __builtin_amdgcn_exp2f((NEG - m0) * L2E);
  t0 += 4.f * em;
  const float r0 = __builtin_amdgcn_rcpf(xsum4(t0)), r1 = __builtin_amdgcn_rcpf(xsum4(t1));
  s00 *= r0; s0m *= r0; s10 *= r1; s11 *= r1; s1m *= r1;
  const u32x4 p0 = (u32x4){pack2(s00[0], s00[1]), pack2(s00[2], s00[3]), pack2(s0m[0], s0m[1]), pack2(s0m[2], s0m[3])};
  const u32x4 p1 = (u32x4){pack2(s10[0], s10[1]), pack2(s10[2], s10[3]), pack2(s11[0], s11[1]), pack2(s11[2], s11[3])};
  const u32x4 pm = (u32x4){0u, 0u, pack2(s1m[0], s1m[1]), pack2(s1m[2], s1m[3])};
  const u32 pe = pack2(em * r0, em * r0);
  const u32x4 px = (u32x4){0u, 0u, pe, pe};
#pragma unroll
  for (int pr = 0; pr < 4; ++pr) {
    f32x4 o0[2], o1[2];
#pragma unroll
    for (int t = 0; t < 2; ++t) {
      const int dt = 2 * pr + t;
      const u32x4 a0 = (u32x4){o.v[dt][0], o.v[dt][1], vm[dt][0], vm[dt][1]};
      o0[t] = MF16(a0, p0, z);
      o1[t] = MF16(o.v[dt], p1, z);
      o0[t] = MF16(o.v[dt], px, o0[t]);
      o1[t] = MF16(a0, pm, o1[t]);
    }
    const int ob = FO_O + ((sh >> 5) * 256 + (sh & 31) * 4 + pr) * 1024;
    __builtin_amdgcn_raw_buffer_store_b128((u32x4){pack2(o0[0][0], o0[0][1]), pack2(o0[0][2], o0[0][3]), pack2(o0[1][0], o0[1][1]), pack2(o0[1][2], o0[1][3])}, rsf, lane * 16, ob, 16);
    __builtin_amdgcn_raw_buffer_store_b128((u32x4){pack2(o1[0][0], o1[0][1]), pack2(o1[0][2], o1[0][3]), pack2(o1[1][0], o1[1][1]), pack2(o1[1][2], o1[1][3])}, rsf, lane * 16, ob + 131072, 16);
  }
}
#define NAW 64
__device__ __forceinline__ void a_role(__amdgpu_buffer_rsrc_t rsx, __amdgpu_buffer_rsrc_t rsf, int sh, int lim, int wi, int done, int lane, int fb, u32 tag) {
  const int lr = lane & 15, lg = lane >> 4;
  const int okm = (lg * 8 + (lr & 7)) * 16, ovm = lg < 2 ? (lg * 16 + lr) * 8 : 0x7ffffff0;
  int rp = 0, xp = -1, spin = 0, pub = done, miss = 0, tp = 200;
  u32 tseen = 0, tq0 = 0, tq1 = 0, tq2 = 0, tq3 = 0; (void)tq0; (void)tq1; (void)tq2; (void)tq3;
#define XA_WAITX(s_)                                                                  \
  while (xp <= ((s_) >> 5)) {                                                                                          \
    asm volatile("" ::: "memory");                                                                                     \
    int pv = min(xprog(__builtin_amdgcn_raw_buffer_load_b32(rsx, lane * 128, PGO_X, 16), tag), xprog(__builtin_amdgcn_raw_buffer_load_b32(rsx, (64 + (lane & 31)) * 128, PGO_X, 16), tag)); \
    _Pragma("unroll") for (int o_ = 1; o_ < 64; o_ *= 2) pv = min(pv, __shfl_xor(pv, o_));                             \
    xp = __builtin_amdgcn_readfirstlane(pv);                                                                           \
    if (xp > ((s_) >> 5) || ++spin > SPIN_CAP) break;                                                                  \
    asm volatile("s_sleep 16" ::: "memory");                                                                           \
  }
#define XA_PUB() if (pub != done) { pub = done; asm volatile("s_nop 4\n\tbuffer_store_dword %0, %1, %2, %3 offen sc1" :: "v"(done), "v"(lane ? 0x7ffffff0 : 0), "s"(rsx), "s"(PGO_A + wi * 128) : "memory"); }
#define XA_STEP(CUR, NXT)                                                                                              \
  {                                                                                                                    \
    const int seg = sh >> 5, nx = sh + NAW;                                                                            \
    XA_WAITX(min(nx, lim - 1))                                                                                         \
    if (rp <= seg) {                         \
      while (true) {                                                                                                   \
        int want = min(seg + 1, NSEG - 1);                                                                             \
        if (tseen) {                                                                                                   \
          const u32 due = tseen + (u32)(want - rp) * tp - (want + 1 < NSEG ? 0 : XA_LAST) + (seg + 1 < NSEG ? 0 : XA_END); \
          if ((int)(due - (u32)__builtin_amdgcn_s_memrealtime()) > 0 || miss) { asm volatile("s_waitcnt vmcnt(0)" ::: "memory"); XA_PUB() } \
          for (int i_ = 0; i_ < 4000 && (int)(due - (u32)__builtin_amdgcn_s_memrealtime()) > 0; ++i_) asm volatile("s_sleep 2" ::: "memory"); \
          if (!miss) want = min(max(want, rp + max((int)((u32)__builtin_amdgcn_s_memrealtime() - tseen), 0) / tp), NSEG - 1); \
        }                                                                                                              \
        XA_DBG4                                                                                                        \
        asm volatile("" ::: "memory");                                                                                 \
        if (seg + 1 < NSEG) {                                                                                          \
          const int r_ = __builtin_amdgcn_readfirstlane(__builtin_amdgcn_raw_buffer_load_b32(rsx, 0, pgr + want * 128, 16)); \
          if (r_ >= 0) { const u32 n_ = (u32)__builtin_amdgcn_s_memrealtime(); tseen = n_ - ((n_ - (r_ >> 16)) & 0x7fffu) + XA_LAG; tp = max(r_ & 1023, 1); rp = want; miss = 0; break; } \
        } else if (__ballot(__builtin_amdgcn_raw_buffer_load_b32(rsx, ((sh & 31) * 4 + (lane & 3)) * 128, PGO_D, 16) == SENT) == 0) break; \
        if (++spin > SPIN_CAP) break;                                                                                  \
        if (miss++ && seg + 2 < NSEG) asm volatile("s_sleep 20" ::: "memory");                                                         \
      }                                                                                                                \
    }                                                                                                                  \
    asm volatile("" ::: "memory");                                                                                     \
    XA_DBG2                                                                                                            \
    u32x4 km[4]; u32x2 vm[8];                                                                                          \
    _Pragma("unroll") for (int i = 0; i < 4; ++i) km[i] = __builtin_amdgcn_raw_buffer_load_b128(rsf, okm, FO_KM + (sh * 4 + i) * 512, 16); \
    _Pragma("unroll") for (int i = 0; i < 8; ++i) vm[i] = __builtin_amdgcn_raw_buffer_load_b64(rsf, ovm, FO_VM + (sh * 8 + i) * 256, 16);  \
                              \
    const int pk_ = min(rp + max((int)((u32)__builtin_amdgcn_s_memrealtime() - tseen), 0) / tp, NSEG - 1);              \
    const u32 pv_ = __builtin_amdgcn_raw_buffer_load_b32(rsx, 0, pgr + pk_ * 128, 16);                                 \
    asm volatile("" ::: "memory");                                                                                     \
    __builtin_amdgcn_s_waitcnt(0x0F70);                                                                   \
    asm volatile("" ::: "memory");                                                                                     \
    { const int r_ = __builtin_amdgcn_readfirstlane(pv_);                                                              \
      if (r_ >= 0) { const u32 n_ = (u32)__builtin_amdgcn_s_memrealtime(); tseen = n_ - ((n_ - (r_ >> 16)) & 0x7fffu) + XA_LAG; tp = max(r_ & 1023, 1); rp = pk_; } } \
    XA_PUB()                                   \
    if (nx < lim) xa_load(NXT, rsf, nx, lane);                                                                         \
    XA_DBG3                                                                                                            \
    __builtin_amdgcn_sched_barrier(0);                                                                                 \
    xa_compute(CUR, km, vm, rsf, sh, lane);                                                                            \
    ++done;                                                                                                            \
    XA_DBG                                                                                                             \
    sh = nx;                                                                                                           \
    if (sh >= lim) break;                                                                                              \
  }
#define XA_LAST 24
#define XA_END 0
#define XA_LAG 30
#define XA_DBG tq2 = (u32)__builtin_amdgcn_s_memrealtime();
#define XA_DBG2 tq0 = (u32)__builtin_amdgcn_s_memrealtime();
#define XA_DBG3 tq1 = (u32)__builtin_amdgcn_s_memrealtime();
#define XA_DBG4 tq3 = tq3 ? tq3 : (seg == 63 ? (u32)__builtin_amdgcn_s_memrealtime() : 0u);
  const int pgr = PGO_R + (__builtin_amdgcn_readfirstlane(wait_start(rsx, fb, tag, true)[2]) & 8192);
  XaOps A, B;
  if (sh >= NAW) {
    const int r_ = __builtin_amdgcn_readfirstlane(__builtin_amdgcn_raw_buffer_load_b32(rsx, 0, pgr + 40 * 128, 16));
    if (r_ >= 0) { const u32 n_ = (u32)__builtin_amdgcn_s_memrealtime(); tseen = n_ - ((n_ - (r_ >> 16)) & 0x7fffu) + XA_LAG; tp = max(r_ & 1023, 1); rp = 40; }
  }
  XA_WAITX(sh)
  asm volatile("" ::: "memory");
  xa_load(A, rsf, sh, lane);
  while (true) { XA_STEP(A, B) XA_STEP(B, A) }
  asm volatile("s_waitcnt vmcnt(0)" ::: "memory");
  XA_PUB()
  const int ai = wi - 128;
  if (lane == 0 && wi >= 128) { g_dbg[3600 + ai] = (u32)__builtin_amdgcn_s_memrealtime(); g_dbg[3700 + ai] = tq0; g_dbg[3800 + ai] = tq1; g_dbg[3900 + ai] = tq2; g_dbg[4000 + ai] = tq3; }
}

typedef __attribute__((ext_vector_type(32))) unsigned int u32x32;
typedef __attribute__((ext_vector_type(16))) unsigned int u32x16;
typedef __attribute__((ext_vector_type(8))) unsigned int u32x8;
#define STR2(x) #x
#define STR(x) STR2(x)
#define RNG(f, b, n) f "[" STR(b) ":" STR((b) + (n)-1) "]"
#define REG(b) "v[" STR(b) "]"
#define LR_RED 16384
#define LR_CTL LR_RED
#define LR_STG 32768
#define L_RSTAMP LR_STG
#define STAMPV(i, v)
#define EXF (NH * 8 * 1024)
#define EXK 65536
#define OOR 0x7ffffff0

__device__ __forceinline__ u32 lds_word(int addr) {
  u32 v;
  asm volatile("ds_read_b32 %0, %1\n\ts_waitcnt lgkmcnt(0)" : "=v"(v) : "v"(addr) : "memory");
  return __builtin_amdgcn_readfirstlane(v);
}
__device__ __forceinline__ int fresh_lane() {
  int l;
  asm volatile("v_mbcnt_lo_u32_b32 %0, -1, 0\n\tv_mbcnt_hi_u32_b32 %0, -1, %0" : "=v"(l));
  return l;
}
__device__ __forceinline__ u32 rt32() { return (u32)__builtin_amdgcn_s_memrealtime(); }
int g_tune[4] = {TR, TP, TDB, TDC};
#undef TR
#undef TP
#undef TDB
#undef TDC
#define TR tun_r
#define TP tun_p
#define TDB tun_b
#define TDC tun_c
#define SYO(s) (XSYN + flip + (s) * 64)
#define TKS 0.85f
#define TLB 1
#define TLC 1
#define TLP 2
#define KG0 0
#define CLS0 0u
#define APC 5
#define ADL 12
#define CALN 2
#define TE1 4
#define TE2 3
#define TE3 2
#define XTH 4
#define TS3 42
#define TE0 30
#define TD0 170
#define TD 120
#define PHASE 1
#define XSK 0
#define CLKS(T, R, X) WLG(2) "s_sub_u32 s49, " T ", " R "\n\t" "s_cmp_le_i32 s49, " STR(3 * PHASE) "\n\t" "s_cbranch_scc1 " X "\n\t"
#define CLKH(OFF) WLG(0) "s_memrealtime s[72:73]\n\t" "s_add_u32 s50, s43, s44\n\t" "s_add_u32 s50, s50, " OFF "\n\t" "s_nop 1\n\t" "s_memrealtime s[74:75]\n\t"
#define CLKL(L, T)   L ":\n\t" "s_memrealtime s[76:77]\n\t" CLKS(T, "s72", L "x") "s_memrealtime s[72:73]\n\t" CLKS(T, "s74", L "x")     \
                     "s_memrealtime s[74:75]\n\t" CLKS(T, "s76", L "x") "s_branch " L "\n" \
                     L "x:\n\t" CLKP(L)
#define CLKW(L, OFF) CLKH(OFF) "s_nop 3\n" CLKL(L, "s50")
#define CLKP(L) "s_cmp_lt_i32 s49, 1\n\t" "s_cbranch_scc1 " L "y\n\t" "s_nop " STR(PHN) "\n\t" "s_cmp_lt_i32 s49, 2\n\t" "s_cbranch_scc1 " L "y\n\t" "s_nop " STR(PHN) "\n\t" \
                "s_cmp_lt_i32 s49, 3\n\t" "s_cbranch_scc1 " L "y\n\t" "s_nop " STR(PHN) "\n\t" "s_nop 0\n\t" "s_branch " L "y\n\t" "s_nop 0\n" L "y:\n\t"
#define PHN 3
#define REL(L) "s_barrier\n" L ":\n\t"
#define RELV(L) "s_cbranch_vccnz " L "\n\t" REL(L)
#define RELN(L) "s_cmp_eq_u32 %[wv], 0\n\t" "s_cbranch_scc1 " L "a\n\t" "s_cmp_eq_u32 %[wv], 1\n\t" "s_cbranch_scc1 " L "b\n\t" \
                "s_barrier\n\t" "s_nop " STR(PD2) "\n\t" "s_branch " L "\n" L "b:\n\t" "s_barrier\n\t" "s_branch " L "\n" L "a:\n\t" "s_barrier\n" L ":\n\t"
#define PD2 2
#define TS_(n) "s_memtime s[80+2*" #n ":81+2*" #n "]\n\t"
#define TSA(n)
#define TSB(n)
#define TSW(n)
#define TVM 12
template <int NTL, int KS>
__device__ __forceinline__ void tr_chunk(const u16* S, int lr, int lg, u32 (&r)[NTL][KS][8]) {
  const int q = lr >> 2, p = lr & 3;
#pragma unroll
  for (int t = 0; t < NTL; ++t)
#pragma unroll
    for (int i = 0; i < KS; ++i)
#pragma unroll
      for (int kb = 0; kb < 2; ++kb) {
        const u16* base = S + (64 * i + 32 * kb + 16 * (lg >> 1) + 4 * (lg & 1) + (q & 1) + 8 * (q >> 1)) * (16 * NTL) + 16 * t + 4 * p;
        const u32x2 t0 = __builtin_bit_cast(u32x2, __builtin_amdgcn_ds_read_tr16_b64_v4i16((LAS v4i16_t*)(base)));
        const u32x2 t1 = __builtin_bit_cast(u32x2, __builtin_amdgcn_ds_read_tr16_b64_v4i16((LAS v4i16_t*)(base + 2 * 16 * NTL)));
        r[t][i][4 * kb + 0] = t0[0]; r[t][i][4 * kb + 1] = t0[1]; r[t][i][4 * kb + 2] = t1[0]; r[t][i][4 * kb + 3] = t1[1];
      }
}
#define SMF(acc, a, bf, bi) "v_smfmac_f32_16x16x64_bf16 " RNG("v", acc, 4) ", " RNG("v", a, 4) ", " RNG(bf, bi, 8) ", v34\n\t"
#define ZER(r) "v_mov_b64 " RNG("v", r, 2) ", 0\n\t" "v_mov_b64 " RNG("v", (r) + 2, 2) ", 0\n\t"
#define GLD(r, o) "s_xor_b32 s49, s50, " STR(o) "\n\t" "buffer_load_dwordx4 " RNG("v", r, 4) ", v32, %[rs], s49 offen sc1\n\t"
#define GL1(r, o) "s_xor_b32 s49, s50, " STR(o) "\n\t" "buffer_load_dwordx4 " RNG("v", r, 4) ", v33, %[rs], s49 offen sc1\n\t"
#define GL2(r, o) GL1(r, 4096 + (o))
#define MXA0(r) "v_max3_u32 v63, " REG(r) ", " REG((r) + 1) ", " REG((r) + 1) "\n\t"
#define MXA(r) "v_max3_u32 v63, v63, " REG(r) ", " REG((r) + 1) "\n\t"
#define MXB(r) "v_max3_u32 v63, v63, " REG((r) + 2) ", " REG((r) + 3) "\n\t"
#define CMPB "v_cmp_eq_u32 vcc, s54, v63\n\t"
#define PSA(r) "v_pk_add_f32 " RNG("v", r, 2) ", " RNG("v", r, 2) ", " RNG("v", (r) + 2, 2) "\n\t"
#define PSW(r, o) "ds_write_addtid_b32 " REG(r) " offset:" STR(o) "\n\t"
#define RDQ(r, o) "ds_read_b128 " RNG("v", r, 4) ", v37 offset:" STR(o) "\n\t"
#define SQ(t, i) SMF(64 + 4 * (t), 96 + 4 * (i), "a", ((i) * 4 + (t)) * 8)
#define SV(t, i) SMF(24 + 4 * (t), 96 + 4 * (i), "a", 128 + ((i) * 2 + (t)) * 8)
#define SVP(t, i) SV(t, i)
#define SW(a, q, i) SMF((a) + 4 * (q), 96 + 4 * (i), "v", 128 + ((i) * 2 + (q)) * 8)
#define WVM(n) "s_waitcnt vmcnt(" STR(n) ")\n\t"
#define WLG(n) "s_waitcnt lgkmcnt(" STR(n) ")\n\t"
#define WLGF(n) WLG(n)
#define PACE 7
#define PC "s_nop " STR(PACE) "\n\t"
#define PC6
#define PC7
#define WBLK(a, n, i, M0) WVM((n) + 7 - (i)) SW(a, 0, i) M0(96 + 4 * (i)) SW(a, 1, i) MXB(96 + 4 * (i))
#define CL8(a, b, c, d, e, f, g, h) "v" #a, "v" #b, "v" #c, "v" #d, "v" #e, "v" #f, "v" #g, "v" #h
#define CL64 CL8(64, 65, 66, 67, 68, 69, 70, 71)
#define CL72 CL8(72, 73, 74, 75, 76, 77, 78, 79)
#define CL80 CL8(80, 81, 82, 83, 84, 85, 86, 87)
#define CL88 CL8(88, 89, 90, 91, 92, 93, 94, 95)
#define CL96 CL8(96, 97, 98, 99, 100, 101, 102, 103), CL8(104, 105, 106, 107, 108, 109, 110, 111)
#define CL112 CL8(112, 113, 114, 115, 116, 117, 118, 119)
#define CL120 CL8(120, 121, 122, 123, 124, 125, 126, 127)
#define PKA(d, a, b) "v_pk_add_f32 " RNG("v", d, 2) ", " RNG("v", a, 2) ", " RNG("v", b, 2) "\n\t"
#define CNT(o) STR(4096 + 64 * (o))
#define ARRIVE(o) "ds_add_u32 v45, v39 offset:" CNT(o) "\n\t"

#define EXTRA "v_readlane_b32 s69, v35, s45\n\t" "s_and_b32 s56, s58, 1023\n\t" "s_bfe_u32 s57, s58, 0xa000a\n\t" "s_lshr_b32 s55, s58, 20\n\t" "s_add_u32 s66, s56, s69\n\t" "s_add_u32 s67, s57, s69\n\t" "s_add_u32 s67, s67, s69\n\t"
#define TEX(s) ((s) == 0 ? TE0 : ((s) < 4 ? TE1 : ((s) < 12 ? TE2 : ((s) < TS3 ? TE3 : 0))))
#define R2BASE "s_bitcmp1_b32 s45, 0\n\t" "s_cselect_b32 s71, " STR(XR2S) ", 0\n\t" "s_add_u32 s71, s71, %[r2]\n\t"
#define X4B 0
#define X4C 0
#define STB "buffer_store_dwordx2 v[116:117], v11, %[rs], s36 offen sc1\n"
#define STC "buffer_store_dwordx2 v[80:81], v17, %[rs], s36 offen sc1\n\t"
#define R2TEXT                                                                                                     \
              TSB(0)                                                                                               \
              "buffer_load_dwordx4 v[64:67], v43, %[rs], s71 offen sc1\n\t"                                      \
              "buffer_load_dwordx4 v[68:71], v8, %[rs], s71 offen sc1\n\t"                                       \
              "buffer_load_dwordx4 v[76:79], v9, %[rs], s71 offen sc1\n\t"                                       \
              "buffer_load_dwordx4 v[60:63], v10, %[rs], s71 offen sc1\n\t"                                      \
              "buffer_store_dwordx4 v[72:75], v47, %[rf], s41 offen sc1\n\t"                                     \
              "s_cbranch_vccnz .Lvw%=\n\t" VPOST ARRIVE(12) "s_barrier\n" ".Lvw%=:\n\t"                             \
              "ds_read2_b32 v[96:97], v12 offset0:0 offset1:4\n\t" "ds_read2_b32 v[98:99], v12 offset0:32 offset1:36\n\t" \
              "ds_read2_b32 v[100:101], v13 offset0:0 offset1:4\n\t" "ds_read2_b32 v[102:103], v13 offset0:32 offset1:36\n\t" \
              "ds_read2_b32 v[104:105], v14 offset0:0 offset1:4\n\t" "ds_read2_b32 v[106:107], v14 offset0:32 offset1:36\n\t" \
              "ds_read2_b32 v[108:109], v15 offset0:0 offset1:4\n\t" "ds_read2_b32 v[110:111], v15 offset0:32 offset1:36\n\t" \
              "v_mov_b64 v[112:113], v[80:81]\n\t" "v_mov_b64 v[114:115], v[82:83]\n\t" "s_mov_b32 s65, 0\n\t"     \
              "v_mov_b32_dpp v112, v84 row_ror:8 row_mask:0xf bank_mask:0xc\n\t" "v_mov_b32_dpp v113, v85 row_ror:8 row_mask:0xf bank_mask:0xc\n\t" \
              "v_mov_b32_dpp v114, v86 row_ror:8 row_mask:0xf bank_mask:0xc\n\t" "v_mov_b32_dpp v115, v87 row_ror:8 row_mask:0xf bank_mask:0xc\n\t" \
              "v_pk_mul_f32 v[112:113], v[112:113], s[62:63] op_sel_hi:[1,0]\n\t" "v_pk_mul_f32 v[114:115], v[114:115], s[62:63] op_sel_hi:[1,0]\n\t" \
              WLG(0)                                                                                               \
              PKA(96, 96, 100) PKA(98, 98, 102) PKA(104, 104, 108) PKA(106, 106, 110) PKA(96, 96, 104) PKA(98, 98, 106) \
              "v_cvt_pk_bf16_f32 v96, v96, 0\n\t" "v_cvt_pk_bf16_f32 v97, v97, 0\n\t" "v_cvt_pk_bf16_f32 v98, v98, 0\n\t" "v_cvt_pk_bf16_f32 v99, v99, 0\n\t" \
              "v_accvgpr_write_b32 a194, v96\n\t" "v_accvgpr_write_b32 a202, v97\n\t" "v_accvgpr_write_b32 a198, v98\n\t" "v_accvgpr_write_b32 a206, v99\n\t" \
              TSB(1) WVM(4) TSB(2) \
              "v_permlane32_swap_b32 v64, v66\n\t" "v_permlane32_swap_b32 v65, v67\n\t" \
              "v_pk_add_f32 v[92:93], v[64:65], v[66:67]\n\t" \
              WVM(3) SFM(68) \
              "v_permlane16_swap_b32 v92, v93\n\t" \
              "v_add_f32 v94, v92, v93\n\t" \
              WVM(2) "v_mul_f32 v94, s62, v94\n\t" SFM(76) \
              "v_exp_f32 v92, v94\n\t" \
              WVM(1) TSB(3) SFM(60) \
                \
              ".Lex%=:\n\t" \
              "v_exp_f32 v88, v112\n\t" "v_exp_f32 v89, v113\n\t" "v_cvt_pk_bf16_f32 v58, v92, 0\n\t" "v_exp_f32 v90, v114\n\t" "v_exp_f32 v91, v115\n\t" \
              "v_cvt_pk_bf16_f32 v56, v88, v89\n\t" "v_cvt_pk_bf16_f32 v57, v90, v91\n\t" \
              TSB(4) ROR8(50, 58) ROR8(48, 56) \
              "v_mfma_f32_16x16x32_bf16 v[88:91], v[52:55], v[56:59], 0\n\t" \
              ROR8(49, 57) "s_nop 1\n\t" \
              "v_mfma_f32_16x16x32_bf16 v[116:119], a[192:195], v[56:59], 0\n\t" \
              "s_nop 2\n\t" \
              "v_mfma_f32_16x16x32_bf16 v[116:119], a[196:199], v[48:51], v[116:119]\n\t" \
              "v_add_f32_dpp v92, v88, v88 row_ror:8 row_mask:0xf bank_mask:0xf\n\t" "v_subrev_u32 v89, 0x33800000, v92\n\t" \
              "v_mfma_f32_16x16x32_bf16 v[124:127], a[204:207], v[56:59], 0\n\t" \
              "v_rcp_f32 v93, v92\n\t" \
              "v_mfma_f32_16x16x32_bf16 v[124:127], a[200:203], v[48:51], v[124:127]\n\t" \
              TSB(5) "v_cmp_le_u32 vcc, 0x20000000, v89\n\t" "s_nop 0\n\t" \
              "s_cbranch_vccnz .Lmx%=\n\t" \
              OTAIL STB TSB(6)
#define SFM(r) "v_pk_fma_f32 v[112:113], " RNG("v", r, 2) ", s[62:63], v[112:113] op_sel_hi:[1,0,1]\n\t" "v_pk_fma_f32 v[114:115], " RNG("v", (r) + 2, 2) ", s[62:63], v[114:115] op_sel_hi:[1,0,1]\n\t"
#define ROR8(d, s) "v_mov_b32_dpp " REG(d) ", " REG(s) " row_ror:8 row_mask:0xf bank_mask:0xf\n\t"
#define OTAIL "v_pk_mul_f32 v[116:117], v[116:117], v[92:93] op_sel:[0,1]\n\t" "v_pk_mul_f32 v[118:119], v[118:119], v[92:93] op_sel:[0,1]\n\t" \
              "v_cvt_pk_bf16_f32 v116, v116, v117\n\t" "v_cvt_pk_bf16_f32 v117, v118, v119\n\t" \
              "s_mov_b64 exec, s[78:79]\n\t" \
              "v_pk_mul_f32 v[124:125], v[124:125], v[92:93] op_sel:[0,1]\n\t" "v_pk_mul_f32 v[126:127], v[126:127], v[92:93] op_sel:[0,1]\n\t" \
              "v_cvt_pk_bf16_f32 v116, v124, v125\n\t" "v_cvt_pk_bf16_f32 v117, v126, v127\n\t" \
              "s_mov_b64 exec, -1\n\t"
#define MX3(a, b) "v_max3_u32 v95, v95, " REG(a) ", " REG(b) "\n\t"
#define R2MAX ".Lmx%=:\n\t" "v_max_u32 v95, v60, v61\n\t" MX3(62, 63) MX3(64, 65) MX3(66, 67) MX3(68, 69) MX3(70, 71) MX3(76, 77) MX3(78, 79) \
              "v_cmp_eq_u32 vcc, s54, v95\n\t" "s_cbranch_vccnz .Lfail%=\n\t" OTAIL \
              "s_cmp_lg_u32 s65, 0\n\t" "s_cbranch_scc1 .Lend%=\n\t" "s_mov_b32 s65, 1\n\t" \
              "v_max3_f32 v93, v112, v113, v94\n\t" \
              "v_max3_f32 v93, v93, v114, v115\n\t" \
              "s_nop 1\n\t" \
              "v_max_f32_dpp v92, v93, v93 row_ror:8 row_mask:0xf bank_mask:0xf\n\t" \
              "v_max_f32_dpp v88, v93, v93 row_ror:8 row_mask:0xf bank_mask:0xf\n\t" \
              "s_nop 1\n\t" \
              "v_permlane16_swap_b32 v92, v88\n\t" \
              "v_max_f32 v93, v92, v88\n\t" "v_max_f32 v92, v92, v88\n\t" \
              "s_nop 1\n\t" \
              "v_permlane32_swap_b32 v93, v92\n\t" \
              "v_max_f32 v93, v93, v92\n\t" \
              "v_sub_f32 v94, v94, v93\n\t" "v_sub_f32 v112, v112, v93\n\t" "v_exp_f32 v92, v94\n\t" "v_sub_f32 v113, v113, v93\n\t" "v_sub_f32 v114, v114, v93\n\t" "v_sub_f32 v115, v115, v93\n\t" \
              "s_branch .Lex%=\n"
#define PS1(r) "v_add_f32 " REG(r) ", " REG(r) ", " REG((r) + 2) "\n\t"
#define NOP(n) "s_nop " STR(n) "\n\t"
#define PCA 13
#define PDA 8
#define PDC 8
#define PDF 7
#define SLV 0
#define QKRED(X) RDQ(80, 12288) RDQ(84, 12304) X RDQ(88, 0) RDQ(92, 16) RDQ(112, 4096) RDQ(116, 4112) RDQ(120, 8192) RDQ(124, 8208) \
        WLG(4) TSA(4) PKA(80, 80, 88) PKA(82, 82, 90) PKA(84, 84, 92) PKA(86, 86, 94) \
        WLG(2) PKA(80, 80, 112) PKA(82, 82, 114) PKA(84, 84, 116) PKA(86, 86, 118) \
        WLG(0) PKA(80, 80, 120) PKA(82, 82, 122) PKA(84, 84, 124) PKA(86, 86, 126) \
        "v_cvt_pk_bf16_f32 v72, v80, v81\n\t" "v_cvt_pk_bf16_f32 v73, v82, v83\n\t" "v_cvt_pk_bf16_f32 v74, v84, v85\n\t" "v_cvt_pk_bf16_f32 v75, v86, v87\n\t" \
        NOP(1) TSA(5) \
        "v_mfma_f32_16x16x32_bf16 v[88:91], v[72:75], v[72:75], 0\n\t" NOP(2) \
        "v_mfma_f32_16x16x32_bf16 v[80:83], a[208:211], v[72:75], 0\n\t" NOP(2) \
        "v_mfma_f32_16x16x32_bf16 v[84:87], a[212:215], v[72:75], 0\n\t"
#define QKPUB "buffer_store_dwordx4 v[88:91], v41, %[rs], s71 offen sc1\n\t" NOP(0) \
        "buffer_store_dwordx4 v[80:83], v40, %[rs], s71 offen sc1\n\t" NOP(2) \
        "buffer_store_dwordx4 v[84:87], v40, %[rs], s71 offen offset:512 sc1\n\t"
#define MRED(W, X) "ds_read_b128 v[80:83], v46 offset:12288\n\t" W "ds_read_b128 v[84:87], v46\n\t" "ds_read_b128 v[88:91], v46 offset:4096\n\t" "ds_read_b128 v[92:95], v46 offset:8192\n\t" X TSW(4) \
        WLG(2) TSW(5) PKA(80, 80, 84) PKA(82, 82, 86) WLG(0) PKA(88, 88, 92) PKA(90, 90, 94) PKA(84, 80, 88) PKA(86, 82, 90) \
        "v_cvt_pk_bf16_f32 v80, v84, v85\n\t" "v_cvt_pk_bf16_f32 v81, v86, v87\n\t"
#define ORD(a, b) "v_cmpx_o_f32 s[50:51], " REG(a) ", " REG(b) "\n\t"
#define REDO(L) "s_cmp_lg_u64 exec, -1\n\t" "s_mov_b64 exec, -1\n\t" "s_cbranch_scc1 " L "\n" L "r:\n\t"
#define CWAIT(L, o, S) L ":\n\t" "ds_read_b32 v62, v38 offset:" CNT(o) "\n\t" WLG(0) "v_readfirstlane_b32 s49, v62\n\t" "s_cmp_lt_u32 s49, " S "\n\t" "s_cbranch_scc1 " L "\n\t"
#define VPOST PSA(24) PSA(28) PSW(24, 2560) PSW(25, 2832) PSW(28, 3200) PSW(29, 3472)
#define BARA "s_barrier\n\t"
#define BARC "s_barrier\n\t"
#define PZADR(SB, M) "s_add_u32 s49, s45, 1\n\t" "s_xor_b32 s62, s71, " STR(XR2S) "\n\t" "v_readlane_b32 s63, v19, s49\n\t" "s_mov_b32 s49, " SB "\n\t" \
        ".if !((" STR(M) ") & 1)\n\t" "s_mov_b32 s63, 0x7ff00000\n\t" ".endif\n\t" ".if !((" STR(M) ") & 2)\n\t" "s_mov_b32 s49, 0x7ff00000\n\t" ".endif\n\t" \
        ".if !((" STR(M) ") & 4)\n\t" "s_mov_b32 s62, 0x7ff00000\n\t" ".endif\n\t" "s_cmp_eq_u32 %[wv], 0\n\t" \
        "s_cselect_b32 s49, s49, s62\n\t" "s_cselect_b32 s63, s63, s62\n\t" "v_add_u32 v48, s49, v23\n\t" "s_mov_b32 exec_hi, 0\n\t" "v_add_u32 v48, s63, v23\n\t" "s_mov_b32 exec_hi, -1\n\t"
#define PZST "buffer_store_dwordx4 v[92:95], v48, %[rs], 0 offen sc1\n\t"
#define MA 0
#define MC 0
#define MB (7 - MA - MC)
#define PZD 1
#define PZB0 PZADR("s63", MB)
#define PZB PZST
#define PZC0
#define PZC ""
#define PZNC 0
#define PZA0
#define PZA
#define PZNA 0
#define LASTC(L) RELV(L)
#define LASTA(L) ""
#define PCL PCA
#define ABODY(W, X, F, n) TSA(0) GLD(96, 0) NOP(PCA) GLD(100, 1024) NOP(PCA) GLD(104, 2048) W GLD(108, 3072) "s_mov_b32 s47, 0\n\t" X \
        WVM((n) + 3) TSA(1) SQ(0, 0) SQ(1, 0) SQ(2, 0) SQ(3, 0) \
        WVM((n) + 2) SQ(0, 1) SQ(1, 1) SQ(2, 1) SQ(3, 1) \
        WVM((n) + 1) SQ(0, 2) MXA0(96) MXB(96) SQ(1, 2) MXA(100) MXB(100) SQ(2, 2) MXA(104) MXB(104) SQ(3, 2) \
        WVM(n) TSA(2) SQ(0, 3) MXA(108) MXB(108) CMPB SQ(1, 3) "s_add_u32 s48, s38, 4\n\t" NOP(0) NOP(0) SQ(2, 3) "s_cbranch_vccnz " F "\n\t" PS1(64) PS1(65) SQ(3, 3) PS1(68) PS1(69) \
        NOP(0) PSW(64, 0) PSW(65, 272) PSW(68, 640) PSW(69, 912) PSA(72) PSA(76) PSW(72, 1280) PSW(73, 1552) PSW(76, 1920) PSW(77, 2192)
#define CISSUE(X, Y) TSW(0) GL1(96, 0) PC GL1(100, 1024) PC GL1(104, 2048) PC GL1(108, 3072) PC GL2(112, 0) PC GL2(116, 1024) \
        "s_mov_b32 s48, s36\n\t" "s_mov_b32 s36, s37\n\t" "s_add_u32 s45, s45, 1\n\t" "s_add_u32 s38, s38, 4\n\t" "v_readlane_b32 s37, v19, s45\n\t" \
        "s_add_u32 s39, s39, " STR(EXF) "\n\t" "s_add_u32 s40, s40, " STR(EXF) "\n\t" \
        "s_add_u32 s41, s41, " STR(EXK) "\n\t" "s_add_u32 s42, s42, " STR(EXK) "\n\t" "s_add_u32 s43, s43, s55\n\t" PC6 \
        GL2(120, 2048) "s_mov_b32 s47, 2\n\t" "s_mul_i32 s70, s69, 3\n\t" R2BASE "s_add_u32 s43, s43, s70\n\t" NOP(2) PC7 X \
        GL2(124, 3072) Y
#define ZALL ZER(64) ZER(68) ZER(72) ZER(76) ZER(80) ZER(84) ZER(24) ZER(28)
#define SHRD "ds_read_b32 v62, v38\n\t"
#define SHUP WLG(0) "v_readfirstlane_b32 s49, v62\n\t" "s_max_u32 s44, s44, s49\n\t"
#define DPA(d, r, m) "v_add_f32_dpp " REG(d) ", " REG(r) ", " REG((r) + 2) " quad_perm:[0,1,2,3] row_mask:" m " bank_mask:0xf\n\t"
#define CBODY(m, X, n, F) CBODY_(80, m, X, n, F)
#define CBODY_(a, m, X, n, F) \
        WVM((m) + 7) TSW(1) WBLK(a, m, 0, MXA0) WBLK(a, m, 1, MXA) X WBLK(a, n, 2, MXA) WBLK(a, n, 3, MXA) WBLK(a, n, 4, MXA) WBLK(a, n, 5, MXA) WBLK(a, n, 6, MXA) \
        WVM(n) TSW(2) SW(a, 0, 7) MXA(124) MXB(124) CMPB SW(a, 1, 7) NOP(0) NOP(1) "s_cbranch_vccnz " F "\n\t" \
        DPA(88, a, "0x3") DPA(89, (a) + 1, "0x3") NOP(1) DPA(88, (a) + 4, "0xc") DPA(89, (a) + 5, "0xc") NOP(1) \
        "v_permlane32_swap_b32 v88, v89\n\t" "v_add_f32 v88, v88, v89\n\t" TSW(3) PSW(88, 3840)
#define FINEO
#define NEXTP "s_branch .LA%=\n"
#define NEXTN "s_branch .LNA%=\n"

__device__ __forceinline__ void clk_edge(u32& cyc, u32& tick) {
  const u32 t = rt32();
  for (int i = 0; i < 64; ++i) { tick = rt32(); if (tick != t) break; }
  cyc = (u32)__builtin_amdgcn_s_memtime();
}
__device__ __forceinline__ float clk_rate(u32 cyc0, u32 tk0) { u32 c, t; clk_edge(c, t); return (float)(c - cyc0) / (float)max(t - tk0, 1u); }
__device__ __forceinline__ void dma16(__amdgpu_buffer_rsrc_t rs, int voff, int soff, int sstep, int ldsa) {
#pragma unroll
  for (int i = 0; i < 16; ++i)
    asm volatile("s_mov_b32 m0, %0\n\ts_nop 3\n\tbuffer_load_dwordx4 %1, %2, %3 offen lds" :: "s"(ldsa + i * 1024), "v"(voff), "s"(rs), "s"(soff ^ (i * sstep)) : "memory");
}

#define KH(t) (192 + 8 * (t))
#define KMF(t, a, b) "v_mfma_f32_16x16x32_bf16 " RNG("v", 80 + 4 * (t), 4) ", " RNG("v", KH(t) + 4 * (a), 4) ", " RNG("v", KH(3) + 4 * (b), 4) ", " RNG("v", 80 + 4 * (t), 4) "\n\t"
#define KX(j) (92 + 4 * ((j) & 1))
#define KA(t, j) "v_pk_mul_f32 " RNG("v", KX(j), 2) ", " RNG("v", 64 + 4 * (j), 2) ", " RNG("v", 112 + ((t) & 2), 2) " op_sel:[0," STR((t) & 1) "] op_sel_hi:[1," STR((t) & 1) "]\n\t"
#define KF(j) "v_fract_f32 " REG(KX(j) + 1) ", " REG(KX(j) + 1) "\n\t"
#define KE(j, N) "v_exp_f32 " REG(KX(j)) ", " REG(KX(j)) "\n\t" "v_cos_f32 " REG(KX(j) + 2) ", " REG(KX(j) + 1) "\n\t" "v_sin_f32 " REG(KX(j) + 3) ", " N REG(KX(j) + 1) "\n\t"
#define KM(j) "v_pk_mul_f32 " RNG("v", KX(j) + 2, 2) ", " RNG("v", KX(j) + 2, 2) ", " RNG("v", KX(j), 2) " op_sel_hi:[1,0]\n\t"
#define KC(j) "v_pk_mul_f32 " RNG("v", KX(j), 2) ", " RNG("v", KX(j) + 2, 2) ", " RNG("v", 66 + 4 * (j), 2) " op_sel_hi:[1,0]\n\t" \
  "v_pk_fma_f32 " RNG("v", KX(j), 2) ", " RNG("v", KX(j) + 2, 2) ", " RNG("v", 66 + 4 * (j), 2) ", " RNG("v", KX(j), 2) " op_sel:[1,1,0] op_sel_hi:[0,1,1] neg_hi:[1,0,0]\n\t"
#define KD(t, j, r) "v_cvt_pk_bf16_f32 " REG(KH(t) + (j)) ", " REG(r) ", " REG((r) + 1) "\n\t" \
  "v_dot2c_f32_bf16 " REG(r) ", s47, " REG(KH(t) + (j)) "\n\t" "v_dot2c_f32_bf16 " REG((r) + 1) ", s48, " REG(KH(t) + (j)) "\n\t"
#define KL(t, j, r) "v_cvt_pk_bf16_f32 " REG(KH(t) + 4 + (j)) ", " REG(r) ", " REG((r) + 1) "\n\t"
#define KDM "s_add_u32 m0, m0, 1024\n\t" "s_add_u32 s43, s43, s42\n\t" "buffer_load_dwordx4 v110, s[36:39], s43 offen lds\n\t"
#define KP(j) "ds_read_b128 " RNG("v", 64 + 4 * (j), 4) ", v111 offset:" STR(16 * (j)) "\n\t"
#define KV(j, L, A) KE(j, "-") L KM(j) KC(j) A KD(3, j, KX(j))
#define KU(t, j, L, A) KE(j, "") L KM(j) A KD(t, j, KX(j) + 2)
#define KSUB                                                                                                        \
  KV(0, WLG(8) "s_add_u32 m0, m0, 1024\n\t", KA(3, 1)) KV(1, KL(3, 0, KX(0)), KA(3, 2))                              \
  "s_xor_b32 s43, s40, s41\n\t" "buffer_load_dwordx4 v110, s[36:39], s43 offen lds\n\t" KV(2, KL(3, 1, KX(1)), KA(3, 3)) \
  KV(3, KL(3, 2, KX(2)), KA(0, 0))                                                                                  \
  KU(0, 0, KL(3, 3, KX(3)), KA(0, 1)) KU(0, 1, KL(0, 0, KX(0) + 2), KA(0, 2)) KDM KU(0, 2, KL(0, 1, KX(1) + 2), KA(0, 3)) \
  KU(0, 3, KL(0, 2, KX(2) + 2), KA(1, 0)) KF(0)                                                                     \
  KU(1, 0, KL(0, 3, KX(3) + 2), KA(1, 1)) KF(1) KMF(0, 1, 0) KU(1, 1, KL(1, 0, KX(0) + 2), KA(1, 2)) KF(2) KDM      \
  KMF(0, 0, 1) KU(1, 2, KL(1, 1, KX(1) + 2), KA(1, 3)) KF(3) KMF(0, 0, 0) KU(1, 3, KL(1, 2, KX(2) + 2), KA(2, 0)) KF(0) KP(0) \
  KU(2, 0, KL(1, 3, KX(3) + 2), KA(2, 1)) KF(1) KP(1) KMF(1, 1, 0) KU(2, 1, KL(2, 0, KX(0) + 2), KA(2, 2)) KF(2) KP(2) KDM \
  KMF(1, 0, 1) KU(2, 2, KL(2, 1, KX(1) + 2), KA(2, 3)) KF(3) KP(3) KMF(1, 0, 0) KU(2, 3, KL(2, 2, KX(2) + 2), WLG(3) KA(3, 0)) \
  KMF(2, 0, 1) "v_add_u32 v111, 256, v111\n\t" "s_lshl2_add_u32 s41, s42, s41\n\t" KL(2, 3, KX(3) + 2)
#define KZERO ZER(80) ZER(84) ZER(88)
#define KCHUNK(PTR, VO, SO, ST, BUF, PF) "s_mov_b64 s[36:37], " PTR "\n\t" "s_mov_b32 m0, %[sc]\n\t" "v_mov_b32 v110, " VO "\n\t" SO "s_mov_b32 s41, 0\n\t" "s_mov_b32 s42, " STR(ST) "\n\t" \
  "buffer_load_dword " PF ", s[36:39], 0 offen lds\n\t" "s_add_u32 m0, %[sl], " STR((BUF) * 16384 - 1024) "\n\t"
#define KCALL "s_call_b64 s[44:45], .Lkg%=\n\t" WVM(16) KMF(2, 0, 0)
#define KTR(F, d, A, o) "ds_read_b64_tr_b16 " RNG(F, d, 2) ", " A " offset:" STR(o) "\n\t"
#define KTQ1(d, o) KTR("a", d, "%[aq]", o) KTR("a", (d) + 2, "%[aq]", (o) + 128) KTR("a", (d) + 4, "%[aq]", (o) + 2048) KTR("a", (d) + 6, "%[aq]", (o) + 2176)
#define KTQ(d, o) KCALL KTQ1(d, o) KMF(2, 1, 0) KTQ1((d) + 8, (o) + 32)
#define KTO1(d, o) KTR("v", d, "%[ao]", o) KTR("v", (d) + 2, "%[ao]", (o) + 64) KTR("v", (d) + 4, "%[ao]", (o) + 1024) KTR("v", (d) + 6, "%[ao]", (o) + 1088)
#define KTO(d, o) KCALL KTO1(d, o) KMF(2, 1, 0) KTO1((d) + 8, (o) + 2048)
#define KOUT(o) "s_add_u32 s43, %[ko], " STR(o) "\n\t"                                                              \
  "buffer_store_dwordx4 v[80:83], %[l16], %[rt], s43 offen sc1\n\t" "buffer_store_dwordx4 v[84:87], %[l16], %[rt], s43 offen offset:1024 sc1\n\t" "s_nop 3\n\t" \
  "buffer_store_dwordx4 v[88:91], %[l16], %[rt], s43 offen offset:2048 sc1\n\t"
struct SsmPtrs { const u16 *A_re, *A_im, *B_re, *B_im, *C_re, *C_im, *log_dt; };
__device__ __forceinline__ void r_role(unsigned char* lds, int b, const u16* __restrict__ mem0, const u16* __restrict__ Wq, const u16* __restrict__ Wk, const u16* __restrict__ Wv,
                                       const u16* __restrict__ Wo, int fb, u32 tag, u64 pc0, int clen, __amdgpu_buffer_rsrc_t rsx, __amdgpu_buffer_rsrc_t rsf,
                                       __amdgpu_buffer_rsrc_t rst, SsmPtrs sp, int tid, int lane, int w
                                       , u32* dbg, int tun_r, int tun_p, int tun_b, int tun_c
                                       ) {
  const int lr = lane & 15, lg = lane >> 4;
  const int ldsb = (int)(size_t)(LAS unsigned char*)lds;
  const int idx = (lr & 2) ? 0xEEEE : 0x4444;
  u32 tcnt = 0; (void)tcnt;
  u32 cyc0 = 0, tk0 = 0;
  if (tid < 8) *(u32*)(lds + LR_CTL + tid * 4) = 0u;
  *(u32*)(lds + LR_CTL + 4096 + 256 + tid * 4) = 0u;
  const int xcd = b & 3, li = b >> 2;
  const int head = xcd * 8 + (li >> 2), ks = li & 3;
  const int vcu = head * 4 + ks;
  const int cg = (xcd >> 1) * 32 + li, rg = xcd & 1;
  const int rot1 = ((li >> 1) + w) & 3, rot3 = ((li >> 2) + 2 * w) & 7;
  const int pz1 = lane < 32 ? XR1A(lane >> 3, cg * 256 + rg * 128 + (lane & 7) * 16) : XR3A(lg & 1, vcu * 256 + lr * 16);
  const int pgo = PGO_X + head * 128;
  const int calo = XCAL + (li & 3) * 8192 + ((li >> 2) & 1) * 4096 + ((li >> 3) * 4 + xcd) * 128;
  u32 xp0;
  u32x32 wa[6], wo[4];
  {
    const __amdgpu_buffer_rsrc_t rwq = __builtin_amdgcn_make_buffer_rsrc((void*)Wq, 0, DM * HD * 2, 0x00020000);
    const int vq = (lane >> 2) * (HD * 2) + vcu * 64 + (lane & 3) * 16, sq = (256 * w + (rot1 << 6)) * (HD * 2);
    const int vo = (lane >> 1) * (DM * 2) + cg * 32 + (lane & 1) * 16, so = (1024 * w + (rot3 << 7)) * (DM * 2);
    const int sl = ldsb + LR_STG + w * 32768;
    const int ch0 = (b * 4 + w) * 2;
    u32x2 pin[6];
    u32 ldt;
    {
      const int po = (ch0 * SSM_N + 4 * lane) * 2;
      asm volatile("global_load_dwordx2 %0, %7, %9\n\tglobal_load_dwordx2 %1, %7, %10\n\tglobal_load_dwordx2 %2, %7, %11\n\tglobal_load_dwordx2 %3, %7, %12\n\t"
                   "global_load_dwordx2 %4, %7, %13\n\tglobal_load_dwordx2 %5, %7, %14\n\tglobal_load_ushort %6, %8, %15"
                   : "=&v"(pin[0]), "=&v"(pin[1]), "=&v"(pin[2]), "=&v"(pin[3]), "=&v"(pin[4]), "=&v"(pin[5]), "=&v"(ldt)
                   : "v"(po), "v"((ch0 + (lane >> 5)) * 2), "s"(sp.A_re), "s"(sp.A_im), "s"(sp.B_re), "s"(sp.B_im), "s"(sp.C_re), "s"(sp.C_im), "s"(sp.log_dt) : "memory");
    }
    u32x4 m0v;
    asm volatile("global_load_dwordx4 %0, %1, %2" : "=&v"(m0v) : "v"(((4 * rg + ((lane >> 1) & 3)) * DM + 16 * cg + 8 * (lane & 1)) * 2), "s"(mem0) : "memory");
    const u16* const wp[4] = {Wq, Wk, Wv, Wo};
    int pfo[5];
#pragma unroll
    for (int c = 0; c < 5; ++c) {
      const int lo_ = (int)(size_t)wp[c < 3 ? c : 3], s3 = (384 - lo_) & 1023, s63 = (8064 - lo_) & 8191;
      pfo[c] = c < 3 ? (lane < 16 ? (256 * w + li * 8 + (lane & 7)) * (HD * 2) + xcd * 2048 + (lane >> 3) * 1024 + s3
                                  : (lane < 24 && (s63 >> 11) == xcd ? (256 * w + li * 8 + (lane & 7)) * (HD * 2) + s63 : OOR))
                     : (lane < 16 ? (1024 * w + li * 16 + lane + (((c - 3) * 512) ^ ((((s3 >> 7) + 2 * w) & 4) << 7))) * (DM * 2) + (xcd >> 1) * 1024 + s3 : OOR);
    }
    const int scr = ldsb + LR_RED + 1024 + w * 256;
    asm volatile("s_mov_b32 m0, %0\n\ts_nop 3\n\tbuffer_load_dword %1, %2, 0 offen lds" :: "s"(scr), "v"(pfo[0]), "s"(rwq) : "memory");
#define KP0 16
#pragma unroll
    for (int i = 0; i < KP0; ++i)
      asm volatile("s_mov_b32 m0, %0\n\ts_nop 3\n\tbuffer_load_dwordx4 %1, %2, %3 offen lds" :: "s"(sl + i * 1024), "v"(vq), "s"(rwq), "s"(sq ^ (i * 16 * HD * 2)) : "memory");
    asm volatile("s_waitcnt vmcnt(1+" STR(KP0) ")" : "+v"(pin[0]), "+v"(pin[1]), "+v"(pin[2]), "+v"(pin[3]), "+v"(pin[4]), "+v"(pin[5]), "+v"(ldt), "+v"(m0v)::"memory");
    f32x4* prm = (f32x4*)(lds + w * 4096);
    {
      const float dt = rbf(expf(bf2f((u16)ldt)));
#pragma unroll
      for (int i = 0; i < 4; ++i) {
        float v[6];
#pragma unroll
        for (int j = 0; j < 6; ++j) v[j] = __uint_as_float((i & 1) ? (pin[j][i >> 1] & 0xffff0000u) : (pin[j][i >> 1] << 16));
        const float are = dt * v[0], aim = dt * v[1], br = dt * v[2], bi = dt * v[3];
        const float cr = v[4] * br - v[5] * bi, ci = v[4] * bi + v[5] * br;
        const float rev = aim * 0.15915494309189535f;
        prm[4 * lane + i] = (f32x4){are * 1.4426950408889634f, rev - __builtin_rintf(rev), cr, ci};
      }
    }
#pragma unroll
    for (int i = KP0; i < 16; ++i)
      asm volatile("s_mov_b32 m0, %0\n\ts_nop 3\n\tbuffer_load_dwordx4 %1, %2, %3 offen lds" :: "s"(sl + i * 1024), "v"(vq), "s"(rwq), "s"(sq ^ (i * 16 * HD * 2)) : "memory");
    const f32x4 lv = {(float)(128 * (lr >> 3) + (lr & 7)), (float)(lr < 8 ? 256 + 128 * (lr >> 2) + 2 * (lr & 3) : 512 + 128 * ((lr - 8) >> 1) + 4 * (lr & 1)),
                         (float)(lr <= 8 ? 1024 + 128 * lr : 0), (float)(8 * lr)};
    const int trow = 16 * (lg >> 1) + 4 * (lg & 1) + ((lr >> 2) & 1) + 8 * (lr >> 3);
    asm volatile(
        "s_nop 4\n\t" "s_mov_b32 s46, m0\n\t" "s_mov_b32 s38, " STR(DM * HD * 2) "\n\t" "s_mov_b32 s39, 0x20000\n\t" "s_mov_b32 s47, 0xbf80\n\t" "s_mov_b32 s48, 0xbf800000\n\t"
        WLG(0)
        "ds_read_b128 v[64:67], %[pa]\n\t" "ds_read_b128 v[68:71], %[pa] offset:16\n\t" "ds_read_b128 v[72:75], %[pa] offset:32\n\t" "ds_read_b128 v[76:79], %[pa] offset:48\n\t"
        "v_add_u32 v111, 256, %[pa]\n\t" KZERO
        KCHUNK("%[wk]", "%[vq]", "s_mov_b32 s40, %[sq]\n\t", 16 * HD * 2, 1, "%[p1]")
        WLG(0) KA(3, 0)
        KTQ(0, 0) KTQ(32, 4096) KTQ(64, 8192) KTQ(96, 12288)
        KCHUNK("%[wv]", "%[vq]", "", 16 * HD * 2, 0, "%[p2]")
        KTQ(16, 16384) KTQ(48, 16384 + 4096) KTQ(80, 16384 + 8192) KTQ(112, 16384 + 12288)
        KOUT(0)
        KCHUNK("%[wo]", "%[vo]", "s_mov_b32 s40, %[so]\n\t", 32 * DM * 2, 1, "%[p3]")
        KZERO
        KTQ(128, 0) KTQ(144, 4096) KTQ(160, 8192) KTQ(176, 12288)
        KCHUNK("%[wo]", "%[vo]", "s_xor_b32 s40, s40, " STR(512 * DM * 2) "\n\t", 32 * DM * 2, 0, "%[p4]")
        KTO(128, 16384) KTO(144, 16384 + 4096) KTO(160, 16384 + 8192) KTO(176, 16384 + 12288)
        KOUT(3072)
        "s_branch .Lke%=\n"
        ".Lkg%=:\n\t"
        KSUB
        "s_setpc_b64 s[44:45]\n"
        ".Lke%=:\n\t"
        WLG(0) "s_mov_b32 m0, s46"
        : "=&{a[0:31]}"(wa[0]), "=&{a[32:63]}"(wa[1]), "=&{a[64:95]}"(wa[2]), "=&{a[96:127]}"(wa[3]), "=&{a[128:159]}"(wa[4]), "=&{a[160:191]}"(wa[5]),
          "=&{v[128:159]}"(wo[0]), "=&{v[160:191]}"(wo[1])
        : "{v[112:115]}"(lv), [pa] "v"(ldsb + w * 4096 + lg * 64), [vq] "v"(vq), [vo] "v"(vo),
          [p1] "v"(pfo[1]), [p2] "v"(pfo[2]), [p3] "v"(pfo[3]), [p4] "v"(pfo[4]), [aq] "v"(sl + trow * 64 + (lr & 3) * 8), [ao] "v"(sl + trow * 32 + (lr & 3) * 8), [l16] "v"(lane * 16),
          [wk] "s"(Wk), [wv] "s"(Wv), [wo] "s"(Wo), [sq] "s"(sq), [so] "s"(so), [sl] "s"(sl), [sc] "s"(scr), [rt] "s"(rst), [ko] "s"(2 * SSM_L * SSM_D * 2 + ch0 * 3072)
        : CL64, CL72, CL80, CL88, CL96, CL8(192, 193, 194, 195, 196, 197, 198, 199), CL8(200, 201, 202, 203, 204, 205, 206, 207), CL8(208, 209, 210, 211, 212, 213, 214, 215),
          CL8(216, 217, 218, 219, 220, 221, 222, 223), "s36", "s37", "s38", "s39", "s40", "s41", "s42", "s43", "s44", "s45", "s46", "s47", "s48", "scc", "memory");
    {
      const u32x4 pz = (u32x4){SENT, SENT, SENT, SENT};
      if (w < 3) {
        u32x4 v = pz;
        if (w == 0 && lane < 32) v = (u32x4){nosent(m0v[0]), nosent(m0v[1]), nosent(m0v[2]), nosent(m0v[3])};
#pragma unroll
        for (int hf = 0; hf < 2; ++hf) {
          __builtin_amdgcn_raw_buffer_store_b128(v, rsx, pz1, XSB(w) + hf * 8192, 16);
          __builtin_amdgcn_raw_buffer_store_b128(pz, rsx, XR2(vcu) + lane * 16, XR2B + (w & 1) * XR2S + hf * 8192, 16);
          __builtin_amdgcn_raw_buffer_store_b128(pz, rsx, lane < 16 ? XR2(vcu) + 1024 + lane * 16 : OOR, XR2B + (w & 1) * XR2S + hf * 8192, 16);
        }
      } else {
        __builtin_amdgcn_raw_buffer_store_b128(pz, rsx, lane < 8 ? lane * 16 : OOR, calo, 16);
        if (b < 32) {
          __builtin_amdgcn_raw_buffer_store_b128(pz, rsx, lane * 16, XSYN + b * 2048, 16);
          __builtin_amdgcn_raw_buffer_store_b128(pz, rsx, lane * 16, XSYN + b * 2048 + 1024, 16);
        } else if (b >= 48 && b < 48 + 41) {
          __builtin_amdgcn_raw_buffer_store_b128(pz, rsx, lane * 16, PGO_D + (b - 48) * 1024, 16);
        }
      }
      asm volatile("" ::: "memory");
    }
    xp0 = __builtin_amdgcn_raw_buffer_load_b32(rsx, (lane & 1) * 4096, pgo, 16);
    clk_edge(cyc0, tk0);
    asm volatile("s_waitcnt vmcnt(0)\n\ts_barrier" : "+v"(xp0)::"memory");
    if (tid == 0) dbg[3072 + b * 2] = rt32();
#define CLKF() clk_rate(cyc0, tk0)
    if (w == 0) {
      const int l0 = lane ? OOR : 0;
      const u64 c0 = __builtin_amdgcn_s_memtime();
      u32 v;
      asm volatile("s_waitcnt lgkmcnt(0)\n\tbuffer_load_dword %0, off, %1, %2 sc1\n\tbuffer_store_dword %3, %4, %1, %5 offen sc1\n\ts_waitcnt vmcnt(1)"
                   : "=&v"(v) : "s"(rsx), "s"(calo), "v"(tag), "v"(l0), "s"(PGO_Y + b * 128), "s"(c0) : "memory");
      const u32 lat = (u32)(__builtin_amdgcn_s_memtime() - c0);
      const float f = CLKF();
      typedef __attribute__((ext_vector_type(3))) unsigned int u32x3;
      __builtin_amdgcn_raw_buffer_store_b96((u32x3){__float_as_uint(f), lat, tag}, rsx, l0, PGO_Y + b * 128 + 4, 16);
      if (tid == 0) { dbg[3072 + b * 2 + 1] = rt32(); dbg[4200 + b] = lat; }
      asm volatile("" ::: "memory");
    }
    {
      u32 r[1][8][8];
      tr_chunk<1, 8>((const u16*)(lds + LR_STG + w * 32768), lr, lg, r);
#pragma unroll
      for (int i = 0; i < 8; ++i)
#pragma unroll
        for (int e = 0; e < 8; ++e) { const int o = (8 + i) * 8 + e; wo[o >> 5][o & 31] = r[0][i][e]; }
      asm volatile("s_waitcnt lgkmcnt(0)" ::: "memory");
    }
  }
  const int hf = (lr >> 1) & 1;
  const int voff1 = lg * 256 + (lr >> 3) * 128 + (((lr >> 2) & 1) * 2 + (lr & 1)) * 32 + hf * 16;
  const int voff3 = (lr >> 3) * 512 + (lg >> 1) * 256 + (((lr >> 2) & 1) * 2 + (lr & 1)) * 16 + (lg & 1) * 128 + hf * 64;
  const int ard = ldsb + (2 * (lr >> 3) + (lg >> 1)) * 640 + (lr & 1) * 272 + ((lr >> 1) & 3) * 64 + (lg & 1) * 32;
  const int ctl = ldsb + LR_CTL, one = w == 0 ? 2 : (w == 3 ? 4 : 1);
  const int r8 = lane & 7, hk = (lane >> 3) & 1;
#define COA 1
#define COB 1
  const int st01 = lr < 8 ? XR2(vcu) + lr * 16 + lg * 128 : OOR, st2 = (lr < 8 && lg >= 2) ? XR2(vcu) + 1024 + lr * 16 + (lg - 2) * 128 : OOR;
  const int ld01 = hk * 512 + r8 * 16 + lg * 128, ld2 = XR2(head * 4 + lg) + 1024 + r8 * 16 + hk * 128;
  int sto = XR3A(r8 >> 2, vcu * 256 + (r8 & 3) * 16 + lg * 64 + hk * 8);
  const int stk = lr >= 8 ? (lg * 8 + (lr - 8)) * 16 : OOR;
  const int lo = X4C ? 2 * (lr & 7) + (lr >> 3) : lr;
  const int arw = ldsb + LR_CTL + lane * 4, arr = ldsb + 3840 + ((lo >> 3) + 2 * ((lo >> 2) & 1)) * 64 + (lo & 3) * 16;
  const int st1 = XR1A(lg, cg * 256 + rg * 128 + lo * 8);
  const int avw = 0;
  const int avc = ldsb + 2560 + (lr >> 3) * 640 + (8 * ((lr >> 2) & 1) + (lr & 3)) * 4;
  const int avr = avc + (lg & 1) * 272 + (lg >> 1) * 64, avf = avc + (lg & 1) * 128 + (lg >> 1) * 16;
  u32x16 kc = {(u32)voff1, (u32)voff3, (u32)idx, (u32)TEX(lane), (u32)(lane < 16 ? XR2(vcu) + 1024 + lane * 16 : OOR), (u32)ard, (u32)ctl, (u32)one, (u32)st01, (u32)st2, (u32)(XR2(vcu) + lane * 16), (u32)ld2, (u32)(lane * 16), (u32)arw, (u32)arr, (u32)stk};
  int o1c = XR1A(xcd, w * 4096) + rot1 * 1024, o3c = XR3A(rg, w * 8192) + rot3 * 1024;
  const int fok = FO_K + (head * 8 + ks) * 1024, fov = FO_V + (head * 8 + 2 * ks) * 1024;
#define XPROG(v) min(xprog(__builtin_amdgcn_readlane(v, 0), tag), xprog(__builtin_amdgcn_readlane(v, 1), tag))
  int xp = w == 3 ? XPROG(xp0) : 1000;
#define XWAIT(n) for (int it_ = 0; it_ < SPIN_CAP && xp < (n); ++it_) {                                               \
    if (it_) asm volatile("s_sleep 4" ::: "memory");                                                                  \
    const u32 v_ = __builtin_amdgcn_raw_buffer_load_b32(rsx, (lane & 1) * 4096, pgo, 16); xp = XPROG(v_); asm volatile("" ::: "memory"); }
  XWAIT(1)
  u32x16 fv;
  u32x8 fk;
  {
    const int lo_ = w == 3 ? lane * 16 : OOR;
    const u32x4 v0 = __builtin_amdgcn_raw_buffer_load_b128(rsf, lo_, fov, 16), v1 = __builtin_amdgcn_raw_buffer_load_b128(rsf, lo_, fov + 1024, 16);
    const u32x4 k0 = __builtin_amdgcn_raw_buffer_load_b128(rsf, lo_, fok, 16), k1 = __builtin_amdgcn_raw_buffer_load_b128(rsf, lo_, fok + 4096, 16);
    fv = (u32x16){v0[0], v0[1], 0u, 0u, v0[2], v0[3], 0u, 0u, v1[0], v1[1], 0u, 0u, v1[2], v1[3], 0u, 0u};
    fk = (u32x8){k0[0], k0[1], k0[2], k0[3], k1[0], k1[1], k1[2], k1[3]};
    asm volatile("" : "+{a[192:207]}"(fv), "+{a[208:215]}"(fk));
  }
#define XPN 1
#define CALM 40
  XWAIT(2)
#pragma unroll
  for (int i = 0; i < 3; ++i) *(u32x4*)(lds + w * 4096 + i * 1024 + lane * 16) = (u32x4){SENT, SENT, SENT, SENT};
  {
    u64 pc; asm volatile("s_getpc_b64 %0" : "=s"(pc));
    const u32 cv = __builtin_amdgcn_raw_buffer_load_b32(__builtin_amdgcn_make_buffer_rsrc((void*)pc0, 0, clen, 0x00020000), (int)(pc - pc0) + tid * 64, 0, 0);
    asm volatile("" :: "v"(cv));
  }
  if (w == 3) { const u32 v_ = __builtin_amdgcn_raw_buffer_load_b32(rsx, (lane & 1) * 4096, pgo, 16); xp = max(xp, XPROG(v_)); }
  u32x4 st;
  if (w == 1) {
    if (b == 0) {
      u32 tt = 0, best = 0;
      bool xok = false;
      const int sto_ = lane < 5 ? (lane ? XSTR(lane - 1) : XSTA) : OOR;
      for (int it = 0; it < SPIN_CAP; ++it) {
        asm volatile("" ::: "memory");
        const u32x4 r0 = __builtin_amdgcn_raw_buffer_load_b128(rsx, lane * 128, PGO_Y, 16), r1 = __builtin_amdgcn_raw_buffer_load_b128(rsx, lane * 128 + 8192, PGO_Y, 16);
        if (!xok) xok = __ballot(xprog(__builtin_amdgcn_raw_buffer_load_b32(rsx, lane * 128, PGO_X, 16), tag) < XPN) == 0;
        const bool c0 = r0[3] == tag, c1 = r1[3] == tag;
        if (tt == 0 && __popcll(__ballot(!c0 || !c1)) <= CALM) {
          float fm = (c0 ? __uint_as_float(r0[1]) : 0.f) + (c1 ? __uint_as_float(r1[1]) : 0.f), fn = (c0 ? 1.f : 0.f) + (c1 ? 1.f : 0.f);
#pragma unroll
          for (int o = 4; o < 64; o *= 2) { fm += __shfl_xor(fm, o); fn += __shfl_xor(fn, o); }
          fm = fn > 0.f ? fm / fn : 100.f;
          const float f0 = __uint_as_float(__builtin_amdgcn_readfirstlane(__float_as_uint(fm)));
          fm = fminf(fm, __shfl_xor(fm, 1)); fm = fminf(fm, __shfl_xor(fm, 2));
          const float f_ = CLKF() * fminf(fmaxf(fm / f0, 0.8f), 1.f);
          const float k_ = 1.f + TKS * (fminf(fmaxf(23.9f / f_, 1.f), 1.5f) - 1.f);
          const int nb_ = (int)(TDB * k_ + 0.5f), nc_ = nb_ + (int)((TDC - TDB) * k_ + 0.5f), np_ = nc_ + (int)((TP - TDC) * k_ + 0.5f);
          u32 lat = min(c0 ? r0[2] : 65535u, c1 ? r1[2] : 65535u);
          { auto r = __builtin_amdgcn_permlane16_swap(lat, lat, false, false); lat = min(r[0], r[1]); }
          { auto r = __builtin_amdgcn_permlane32_swap(lat, lat, false, false); lat = min(r[0], r[1]); }
          u32 mm[4];
#pragma unroll
          for (int k = 0; k < 4; ++k)
            mm[k] = __builtin_amdgcn_readlane(lat, 4 * k) + __builtin_amdgcn_readlane(lat, 4 * k + 2) + __builtin_amdgcn_readlane(lat, 4 * (k ^ 2) + 1) + __builtin_amdgcn_readlane(lat, 4 * (k ^ 2) + 3);
          const u32 hb = mm[0] + mm[2] <= mm[1] + mm[3] ? 0u : 1u;
          best = (hb << 13) | ((hb ? mm[1] <= mm[3] : mm[0] <= mm[2]) ? CLS0 : 16384u - CLS0);
          if (__ballot(lat == 65535u) == 0) tt = __builtin_amdgcn_readfirstlane(nb_ | (nc_ << 10) | (np_ << 20));
          if (lane == 0) { dbg[3501] = (hb ? mm[1] : mm[0]) | ((hb ? mm[3] : mm[2]) << 16); dbg[3502] = rt32(); }
        }
        if (__ballot(r0[0] != tag || r1[0] != tag) == 0 && xok && tt) break;
      }
      st = (u32x4){rt32() + TD0, tt, best, tag};
      __builtin_amdgcn_raw_buffer_store_b128(st, rsx, sto_, 0, 16);
      if (lane == 0) dbg[3503] = rt32();
    } else st = wait_start(rsx, fb, tag, false, xcd);
    if (lane == 0) *(u32x4*)(lds + LR_CTL + 32) = st;
  }
  LBAR();
  st = *(const u32x4*)(lds + LR_CTL + 32);
  const int flip = __builtin_amdgcn_readfirstlane(st[2]) & 8192, cls = __builtin_amdgcn_readfirstlane(st[2]) & 16384;
  o1c ^= cls; o3c ^= cls; sto ^= cls; kc[4] ^= cls; kc[8] ^= cls; kc[9] ^= cls; kc[10] ^= cls; kc[11] ^= cls;
  { const u32 now = rt32(); if (st[0] - now + 100000u > 200000u) st[0] = now; }
  u32x16 kd = {(u32)((ld01 + XR2(vcu ^ 1)) ^ cls), (u32)((ld01 + XR2(vcu ^ 2)) ^ cls), (u32)((ld01 + XR2(vcu ^ 3)) ^ cls), (u32)(X4B && hk ? OOR : sto), (u32)avr, (u32)(avr + 4096), (u32)(avr + 8192),
               (u32)(avr + 12288), (u32)avw, (u32)(X4C && hk ? OOR : st1 ^ cls), (u32)((lane & 1) * 4096), (u32)(XSB(lane + 1) + flip), (u32)(lane * 8), (u32)avf,
               (u32)(b == 0 && lane == 0 ? XSYN + flip : OOR), (u32)(w == 0 ? pz1 ^ cls : (w == 1 ? kc[10] : kc[4]))};
  u32x16 sa = {(u32)flip, (u32)(XSB(1) + flip), 0u, (u32)fok, (u32)fov, (u32)(FO_KM + (head * 4 + ks) * 512),
               (u32)(FO_VM + (head * 8 + 2 * ks) * 256), st[0], 0u, 0u, (u32)SYO(1), 0u, 0u, (u32)TRY_CAP, 0u, 0u};
  u32x8 sc = {(u32)o1c, (u32)o3c, SENT, 0u, 0u, 0u, st[1], (u32)pgo};
  const u32 org_ = __builtin_amdgcn_readfirstlane(st[0]);
  u32x2 hst = {0u, 0u};
#define TUNEO , "+{s[100:101]}"(hst)
#define KCNT "s_mul_i32 s49, s47, 10\n\t" "s_lshl_b32 s49, 1, s49\n\t" "s_add_u32 s68, s68, s49\n\t"
#define KHST "s_bitset1_b64 s[100:101], s45\n\t"
#define KTRC
#define TRCI
#define PREP 16
#define GAP 12
#define RETA(n, L) "s_cmp_eq_u32 s49, " #n "\n\t" "s_cmov_b32 s48, .Lpc%=-" L "\n\t"
  u32x32 keep;
  u32x2 ov;
  asm volatile("" : "={v[60:91]}"(keep));
  while (true) {
    sa[14] = xp;
#pragma unroll
    for (int i = 0; i < 16; ++i) sa[i] = __builtin_amdgcn_readfirstlane(sa[i]);
#pragma unroll
    for (int i = 0; i < 8; ++i) sc[i] = __builtin_amdgcn_readfirstlane(sc[i]);
    asm volatile(
        "s_nop 4\n\t" "s_mov_b32 s60, m0\n\t"
        "s_mov_b32 m0, %[mb]\n\t" "s_mov_b32 s64, s50\n\t" "s_mov_b32 s98, s49\n\t" "s_mov_b32 s68, 0\n\t"
        EXTRA R2BASE
        "s_cmp_lg_u32 %[wv], 3\n\t" "s_cbranch_scc1 .LN%=\n\t"
        "s_mov_b32 s78, 0xff00ff00\n\t" "s_mov_b32 s79, 0xff00ff00\n\t" "s_mov_b32 s62, 0x3e0293ee\n\t"
        "v_mov_b32 v52, 0x3f803f80\n\t" "v_mov_b32 v53, 0x3f803f80\n\t" "v_mov_b32 v54, 0x3f803f80\n\t" "v_mov_b32 v55, 0x3f803f80\n\t" "v_mov_b32 v51, 0\n\t" "v_mov_b32 v59, 0\n\t"
        "v_mov_b32 v60, %[tg]\n\t" "v_mov_b32 v61, -1\n\t" "s_mov_b32 s63, %[dm]\n\t"
        "s_branch .Lent%=\n"
        ".LA%=:\n\t"
        CLKW(".Lta%=", "0") REL(".Lua%=")
        "s_add_u32 s50, s36, s52\n\t" NOP(PDA)
        ABODY(NOP(PCL) LASTA(".Lwa%="), WVM(5) "v_readlane_b32 s50, v60, 0\n\t" "v_readlane_b32 s51, v60, 1\n\t"
              "s_sub_u32 s50, s50, %[tg]\n\t" "s_sub_u32 s51, s51, %[tg]\n\t" "s_min_u32 s63, s50, s51\n\t" "s_max_u32 s50, s50, s51\n\t" "s_cmp_le_u32 s50, 80\n\t"
              "s_cselect_b32 s50, s63, 0\n\t" "v_readfirstlane_b32 s49, v61\n\t" "s_max_i32 s64, s64, s50\n\t"
              "s_cmp_lg_u32 s49, -1\n\t" "s_cmov_b32 s58, s49\n\t" EXTRA "v_mov_b32 v62, s58\n\t" "ds_write_b32 v38, v62 offset:24\n\t", ".Lfail%=", 0) TSA(3)
        QKRED(BARA) ORD(88, 89) ORD(90, 91) QKPUB TSA(6) TSB(7) REDO(".Lqk%=")
        SVP(0, 0) SHRD SVP(1, 0) SVP(0, 1) SVP(1, 1) SVP(0, 2) SVP(1, 2) WLG(0) "v_readfirstlane_b32 s49, v62\n\t" SVP(0, 3) SVP(1, 3) "s_max_u32 s44, s44, s49\n\t" "s_cmp_eq_u32 s45, 63\n\t" TSA(7)
        "s_cbranch_scc1 .Llast%=\n"
        ".LB%=:\n\t"
        CLKW(".Ltb%=", "s66")
        "s_mov_b32 s47, 1\n\t"
        R2TEXT
        ".LC%=:\n\t"
        ZALL
        "s_mov_b32 s47, 5\n\t" "s_sub_i32 s63, s64, s45\n\t" "s_cmp_lt_i32 s63, 2\n\t" "s_cbranch_scc1 .Lend%=\n\t"
        "s_cmp_lt_i32 s63, " STR(XTH) "\n\t" "s_cselect_b32 s63, s59, %[dm]\n\t"
        CLKW(".Ltc%=", "s67") REL(".Luc%=")
        "s_add_u32 s50, s36, s53\n\t" NOP(PDC)
        CISSUE(LASTC(".Lwc%="), "")
        "buffer_load_dword v61, off, %[rs], s46 sc1\n\t" "s_add_u32 s46, s46, 64\n\t"
        "s_add_u32 s49, s39, 4096\n\t" NOP(PDF)
        "buffer_load_dwordx4 a[208:211], v44, %[rf], s39 offen sc1\n\t" "buffer_load_dwordx4 a[212:215], v44, %[rf], s49 offen sc1\n\t"
        "buffer_load_dwordx2 a[192:193], v44, %[rf], s40 offen sc1\n\t" "buffer_load_dwordx2 a[196:197], v44, %[rf], s40 offen offset:8 sc1\n\t"
        "buffer_load_dwordx2 a[200:201], v44, %[rf], s40 offen offset:1024 sc1\n\t" "buffer_load_dwordx2 a[204:205], v44, %[rf], s40 offen offset:1032 sc1\n\t"
#define NXC 8
        "buffer_load_dword v60, v18, %[rs], s63 offen sc1\n\t"
        CBODY(NXC, "", NXC, ".Lfail%=")
        MRED(BARC, "") ORD(84, 85) ORD(86, 87) STC TSW(6) REDO(".Lmr%=")
        SHRD SHUP TSW(7)
        NEXTP
        CWAIT(".Lqk%=", 8, "s48") QKRED("") NOP(1) QKPUB "s_branch .Lqk%=r\n"
        CWAIT(".Lmr%=", 16, "s38") WVM(0) MRED("", "") "v_min_u32 v80, -2, v80\n\t" "v_min_u32 v81, -2, v81\n\t" STC
        "s_branch .Lmr%=r\n"
        ".Llast%=:\n\t"
        "buffer_store_dwordx4 v[72:75], v47, %[rf], s41 offen sc1\n\t" "s_mov_b32 s47, 3\n\t" NOP(4) VPOST ARRIVE(12) "s_barrier\n\t"
        "s_branch .Lend%=\n"
        R2MAX
        ".Lnorm%=:\n\t"
        "s_cmp_lg_u32 %[wv], 3\n\t" "s_cbranch_scc1 .LNA%=\n\t" "s_cmp_eq_u32 s47, 0\n\t" "s_cbranch_scc1 .LA%=\n\t" "s_branch .LC%=\n"
        ".LN%=:\n\t"
        "v_mov_b64 v[92:93], -1\n\t" "v_mov_b64 v[94:95], -1\n\t" "s_mov_b32 s65, s44\n\t" "s_mov_b32 s78, -100\n\t" "s_mov_b32 s79, 4\n\t"
        "s_branch .Lent%=\n"
        ".LNA%=:\n\t" PZA0
        "s_add_u32 s50, s36, s52\n\t"
        RELN(".Lna%=")
        ABODY(NOP(PCA), LASTA(".Lxa%=") PZA, ".Lfail%=", PZNA) BARA ARRIVE(8) TSA(3)
        SV(0, 0) SV(1, 0) SV(0, 1) SV(1, 1) SV(0, 2) SV(1, 2) SV(0, 3) SV(1, 3) ZER(64) ZER(68) ZER(72) ZER(76)
        VPOST TSA(7) ZER(80) ZER(84) ZER(24) ZER(28)
        "v_add_u32 v88, 4096, v21\n\t" "v_add_u32 v89, 8192, v21\n\t" "v_add_u32 v90, 12288, v21\n\t"
        PZB0
        "s_lshl_b32 s61, s45, 7\n\t" "s_add_u32 s61, s61, " STR(PGO_R - XSYN) "\n\t" "s_cmp_lg_u32 %[wv], 0\n\t"
        WLG(0) "s_barrier\n\t" PZB
        PSW(92, 0) PSW(92, 272) PSW(92, 640) PSW(92, 912) PSW(92, 1280) PSW(92, 1552) PSW(92, 1920) PSW(92, 2192) PSW(92, 3840)
        "ds_read2_b32 v[50:51], v38 offset0:0 offset1:6\n\t"
        "s_cbranch_scc1 .LNB%=\n\t"
        "s_memrealtime s[72:73]\n"
        ".Lv0%=:\n\t"
        "ds_read_b32 v62, v38 offset:" CNT(12) "\n\t"
        "ds_read2_b32 v[112:113], v21 offset0:0 offset1:68\n\t" "ds_read2_b32 v[114:115], v21 offset0:16 offset1:84\n\t"
        "ds_read2_b32 v[116:117], v88 offset0:0 offset1:68\n\t" "ds_read2_b32 v[118:119], v88 offset0:16 offset1:84\n\t"
        "ds_read2_b32 v[120:121], v89 offset0:0 offset1:68\n\t" "ds_read2_b32 v[122:123], v89 offset0:16 offset1:84\n\t"
        "ds_read2_b32 v[124:125], v90 offset0:0 offset1:68\n\t" "ds_read2_b32 v[126:127], v90 offset0:16 offset1:84\n\t"
        WLG(0) "v_cmp_gt_u32 vcc, s48, v62\n\t" "s_cbranch_vccnz .Lv0%=\n\t"
        "s_add_u32 s50, s72, " STR(TVM) "\n\t" "s_lshl_b32 s50, s50, 17\n\t" "s_lshr_b32 s50, s50, 1\n\t"
        PKA(112, 112, 116) PKA(114, 114, 118) PKA(120, 120, 124) PKA(122, 122, 126) PKA(112, 112, 120) PKA(114, 114, 122)
        "v_lshrrev_b32 v91, 20, v51\n\t" "v_readfirstlane_b32 s49, v50\n\t"
        "v_cvt_pk_bf16_f32 v112, v112, v113\n\t" "v_cvt_pk_bf16_f32 v113, v114, v115\n\t" "v_or_b32 v114, s50, v91\n\t"
        "s_cmp_eq_u32 s45, 63\n\t" "s_cbranch_scc1 .Lv2%=\n\t"
        "s_sleep " STR(SLV) "\n"
        ".Lv2%=:\n\t"
        "buffer_store_dwordx2 v[112:113], v20, %[rf], s42 offen sc1\n\t"
        "buffer_store_dword v114, v22, %[rs], s61 offen sc1\n\t"
        "s_cmp_eq_u32 s49, s65\n\t" "s_cbranch_scc1 .LNB%=\n\t"
        "s_mov_b32 s65, s49\n\t" KHST "s_sub_u32 s49, s45, s78\n\t" "s_mov_b32 s78, s45\n\t"
        "s_cmp_gt_i32 s49, " STR(GAP) "\n\t" "s_cbranch_scc1 .LNB%=\n\t" "s_cmp_lt_u32 s45, s79\n\t" "s_cbranch_scc1 .LNB%=\n\t"
        "s_add_u32 s79, s45, 3\n\t" "s_lshl_b32 s49, s45, 6\n\t" "v_add_u32 v50, " STR(TLB + ((TLB + TLC) << 10) + ((TLB + TLC + TLP) << 20)) ", v51\n\t"
        "buffer_store_dword v50, v22, %[rs], s49 offen offset:128 sc1\n"
        ".LNB%=:\n\t"
        WLG(0) "v_readfirstlane_b32 s58, v51\n\t" EXTRA
        "s_mov_b32 s47, 3\n\t"
        "s_cmp_eq_u32 s45, 63\n\t"
        "s_cbranch_scc1 .Lend%=\n"
        ".LNC%=:\n\t" PZC0
        "s_add_u32 s50, s36, s53\n\t"
        WLG(0) RELN(".Lnc%=")
        CISSUE("", LASTC(".Lxc%="))
        CBODY(0, PZC, PZNC, ".Lfail%=") BARC
        ARRIVE(16) TSW(4)
        NEXTN
        ".Lfail%=:\n\t" KCNT
        "s_cmp_lg_u32 s47, 2\n\t" "s_cbranch_scc1 .Lnu%=\n\t"
        "s_mov_b32 s37, s36\n\t" "s_mov_b32 s36, s48\n\t" "s_sub_u32 s45, s45, 1\n\t" "s_sub_u32 s38, s38, 4\n\t" "s_sub_u32 s39, s39, " STR(EXF) "\n\t" "s_sub_u32 s40, s40, " STR(EXF) "\n\t"
        "s_sub_u32 s41, s41, " STR(EXK) "\n\t" "s_sub_u32 s42, s42, " STR(EXK) "\n\t" "s_sub_u32 s43, s43, s55\n\t" "s_sub_u32 s43, s43, s70\n\t" "s_sub_u32 s46, s46, 64\n"
        ".Lnu%=:\n\t"
        "s_mov_b32 s51, 1\n\t" "s_nop 7\n\t" WVM(0) "s_sub_u32 s98, s98, 1\n\t" "s_cbranch_scc1 .Lcap%=\n"
        ".Lent%=:\n\t"
        "s_cmp_eq_u32 s47, 1\n\t" "s_cselect_b32 s99, s66, s67\n\t" "s_cbranch_scc1 .Lnz%=\n\t" ZALL ".Lnz%=:\n\t"
        "s_cmp_eq_u32 s47, 0\n\t" "s_cselect_b32 s99, 0, s99\n\t" "s_cselect_b32 s50, s52, s53\n\t" "s_add_u32 s99, s99, s43\n\t" "s_add_u32 s50, s50, s36\n\t"
        SHRD "s_memrealtime s[72:73]\n\t"
        "s_cmp_eq_u32 %[wv], 3\n\t" "s_cselect_b32 s49, 0, 3\n\t" "s_add_u32 s49, s49, s47\n\t" "s_cmp_lg_u32 s51, 0\n\t" "s_cselect_b32 s49, s49, 7\n\t" "s_cselect_b64 vcc, -1, 0\n\t"
        "s_cselect_b32 s51, 0, 14\n\t" "s_mov_b32 s48, .Lpc%=-.Lnorm%=\n\t"
        RETA(0, ".Lua%=") RETA(1, ".Ltb%=y") RETA(2, ".Luc%=") RETA(3, ".Lna%=") RETA(5, ".Lnc%=")
        "s_getpc_b64 s[96:97]\n" ".Lpc%=:\n\t" "s_sub_u32 s96, s96, s48\n\t" "s_subb_u32 s97, s97, 0\n\t"
        SHUP "s_sub_u32 s49, s99, s72\n\t" "s_add_u32 s49, s49, s44\n"
        ".Lbump%=:\n\t"
        "s_cmp_ge_i32 s49, " STR(PREP) "\n\t" "s_cbranch_scc1 .Lok%=\n\t" "s_add_u32 s44, s44, %[tr]\n\t" "s_add_u32 s49, s49, %[tr]\n\t" "s_branch .Lbump%=\n"
        ".Lok%=:\n\t"
        "s_mov_b64 exec, 1\n\t" "v_mov_b32 v62, s44\n\t" "ds_max_u32 v38, v62\n\t" KTRC "s_mov_b64 exec, -1\n\t"
        "s_add_u32 s99, s99, s44\n\t" "s_sub_u32 s99, s99, s51\n\t"
        WLG(0) "s_memrealtime s[72:73]\n\t" "s_nop 3\n\t" "s_memrealtime s[74:75]\n\t" "s_nop 3\n"
        CLKL(".Lth%=", "s99")
        "s_setpc_b64 s[96:97]\n"
        ".Lcap%=:\n\t" "s_add_u32 s47, s47, 8\n"
        ".Lend%=:\n\t"
        "s_waitcnt vmcnt(0) lgkmcnt(0)\n\t"
        "s_mov_b32 s50, s64\n\t" "s_mov_b32 s49, s98\n\t" "s_mov_b32 s51, s68\n\t" "s_mov_b32 m0, s60"
        : "+{s[36:51]}"(sa), "+{s[52:59]}"(sc), "+{v[60:91]}"(keep), "=&{v[116:117]}"(ov), "+{a[192:207]}"(fv), "+{a[208:215]}"(fk), "+{v[8:23]}"(kd) FINEO TUNEO
        : "{v[32:47]}"(kc), [rs] "s"(rsx), [rf] "s"(rsf), [dm] "s"(__builtin_amdgcn_readfirstlane(PGO_Y + b * 128)), [tg] "s"(__builtin_amdgcn_readfirstlane(tag)), [r2] "s"(__builtin_amdgcn_readfirstlane(XR2B + flip)), [mb] "s"(__builtin_amdgcn_readfirstlane(ldsb + w * 4096)), [wv] "s"(__builtin_amdgcn_readfirstlane(w)), [tr] "s"(TR) TRCI,
          "{a[0:31]}"(wa[0]), "{a[32:63]}"(wa[1]), "{a[64:95]}"(wa[2]), "{a[96:127]}"(wa[3]), "{a[128:159]}"(wa[4]), "{a[160:191]}"(wa[5]),
          "{v[128:159]}"(wo[0]), "{v[160:191]}"(wo[1]), "{v[192:223]}"(wo[2]), "{v[224:255]}"(wo[3])
        : CL8(24, 25, 26, 27, 28, 29, 30, 31), "v48", "v49", "v50", "v51", CL8(52, 53, 54, 55, 56, 57, 58, 59), "v92", "v93", "v94", "v95", CL96, "v112", "v113", "v114", "v115", "v118", "v119", CL120,
          "s60", "s61", "s62", "s63", "s64", "s65", "s66", "s67", "s68", "s69", "s70", "s71", "s72", "s73", "s74", "s75", "s76", "s77", "s78", "s79", "s96", "s97", "s98", "s99", "vcc", "scc", "memory");
    const int code = __builtin_amdgcn_readfirstlane(sa[11]);
    xp = __builtin_amdgcn_readfirstlane(sa[14]);
    tcnt += sa[15];
    sa[15] = 0;
    if (code == 3) break;
    const int seg = __builtin_amdgcn_readfirstlane(sa[9]);
    if (code == 5) { XWAIT(seg + 2) xp = max(xp, seg + 2); sa[11] = 2; }
    else if (code == 1) { __builtin_amdgcn_raw_buffer_store_b64((u32x2){nosent(ov[0]), nosent(ov[1])}, rsx, sto, sa[0], 16); sa[11] = 2; }
    else { sc[2] += 1; sa[13] = 0; sa[11] = code - 8; sa[15] = 1; }
  }
  if (lane == 0) { dbg[(b * 4 + w) * 4] = org_; dbg[(b * 4 + w) * 4 + 2] = 0; }
  if (lane == 0) { dbg[(b * 4 + (w + 1) % 4) * 4 + 1] = sa[7] + sa[8]; dbg[(b * 4 + w) * 4 + 3] = tcnt; if (b == 0 && w == 3) { dbg[3500] = sc[6]; dbg[3504] = sa[8]; } }
  if (b == 0 && w == 0 && ((hst[lane >> 5] >> (lane & 31)) & 1)) dbg[4096 + lane] = 1;
  LBAR();
  if (tid == 0) __builtin_amdgcn_raw_buffer_store_b32(1u, rsx, 0, PGO_D + vcu * 128, 16);
  if (b == 0 && w == 1) {
    for (int it = 0; it < SPIN_CAP; ++it) {
      asm volatile("" ::: "memory");
      if (__ballot(__builtin_amdgcn_raw_buffer_load_b32(rsx, lane * 128, PGO_S, 16) != tag || __builtin_amdgcn_raw_buffer_load_b32(rsx, lane * 128 + 8192, PGO_S, 16) != tag) == 0) break;
    }
    if (lane == 0) __builtin_amdgcn_raw_buffer_store_b32(tag, rsx, 0, XEPO, 16);
  }
}

#define XSTAT 3
#define XVM 8
#define XP0 -1
#define XSC " sc1"
#define XPITCH 1056
#define XSLOT (32 * XPITCH)
#define XW(HB, t) "a[" STR(HB) "+8*xk+4*" #t ":" STR(HB) "+8*xk+4*" #t "+3]"
#define XF(rt) "v[96+8*(xk&3)+4*" #rt ":96+8*(xk&3)+4*" #rt "+3]"
#define XFN(rt) "v[96+8*((xk+3)&3)+4*" #rt ":96+8*((xk+3)&3)+4*" #rt "+3]"
#define XA(AC, o) "v[" STR(AC) "+" #o ":" STR(AC) "+" #o "+3]"
#define XM1(d, a, b, FIRST) ".if (xk == 0) && " STR(FIRST) "\n\tv_mfma_f32_16x16x32_bf16 " d ", " a ", " b ", 0\n\t.else\n\tv_mfma_f32_16x16x32_bf16 " d ", " a ", " b ", " d "\n\t.endif\n\t"
#define XMF_QK(i, HB, AC, F) XMF_QK##i(HB, AC, F)
#define XMF_QK0(HB, AC, F) XM1(XA(AC, 0), XW(HB, 0), XF(0), F)
#define XMF_QK1(HB, AC, F) XM1(XA(AC, 4), XW(HB, 1), XF(0), F)
#define XMF_QK2(HB, AC, F) XM1(XA(AC, 8), XW(HB, 0), XF(1), F)
#define XMF_QK3(HB, AC, F) XM1(XA(AC, 12), XW(HB, 1), XF(1), F)
#define XMF_V(i, HB, AC, F) XMF_V##i(HB, AC, F)
#define XMF_V0(HB, AC, F) XM1(XA(AC, 0), XF(0), XW(HB, 0), F)
#define XMF_V1(HB, AC, F) XM1(XA(AC, 8), XF(0), XW(HB, 1), F)
#define XMF_V2(HB, AC, F) XM1(XA(AC, 4), XF(1), XW(HB, 0), F)
#define XMF_V3(HB, AC, F) XM1(XA(AC, 12), XF(1), XW(HB, 1), F)
#define XRD(rt, CUR, NXT) ".if xk < 13\n\tds_read_b128 " XFN(rt) ", " CUR " offset:64*(xk+3)+" STR(rt * 16 * XPITCH) "\n\t.else\n\t" \
                          "ds_read_b128 " XFN(rt) ", " NXT " offset:64*(xk-13)+" STR(rt * 16 * XPITCH) "\n\t.endif\n\t"
#define XCV(EPI, PV, i) ".if " STR(EPI) " && (xk >= 1) && (xk < 3)\n\tv_cvt_pk_bf16_f32 v[128+4*(xk-1)+" #i "], v[" STR(PV) "+8*(xk-1)+2*" #i "], v[" STR(PV) "+8*(xk-1)+2*" #i "+1]\n\t.endif\n\t"
#define XHALF(MF, HB, AC, PV, EPI, CUR, NXT, DSLOT, GOFF)                                                   \
  ".set xk, 0\n\t"                                                                                          \
  ".rept 16\n\t"                                                                                            \
  "s_waitcnt lgkmcnt(4)\n\t"                                                                                \
  MF(0, HB, AC, EPI) XRD(0, CUR, NXT) XCV(EPI, PV, 0)                                                       \
  ".if (xk >= 5) && (xk < 13)\n\t"                                                                          \
  "s_add_u32 m0, m0, " STR(XPITCH) "\n\t"                                                                   \
  ".endif\n\t"                                                                                              \
  MF(1, HB, AC, EPI) XRD(1, CUR, NXT) XCV(EPI, PV, 1)                                                       \
  ".if (xk >= 5) && (xk < 13)\n\t"                                                                          \
  "s_add_u32 %[dg], %[dg], 2048\n\t"                                                                        \
  ".endif\n\t"                                                                                              \
  MF(2, HB, AC, EPI) XCV(EPI, PV, 2)                                                                        \
  ".if (xk >= 4) && (xk < 12)\n\t"                                                                          \
  "buffer_load_dwordx4 %[l16], %[rx], %[dg] offen lds\n\t"                                                  \
  ".endif\n\t"                                                                                              \
  ".if xk == 0\n\t"                                                                                         \
  "s_add_u32 m0, %[dl], " STR((DSLOT) * XSLOT) "\n\t" "s_add_u32 %[dg], %[gb], " STR(GOFF) "\n\t"           \
  ".endif\n\t"                                                                                              \
  ".if " STR(EPI) " && (xk == " STR(XSTAT) ")\n\t"                                                                       \
  "buffer_store_dwordx4 v[128:131], v137, %[rf], %[sg] offen" XSC "\n\t"                                       \
  ".endif\n\t"                                                                                              \
  ".if " STR(EPI) " && (xk == 13)\n\t"                                                                      \
  "v_mov_b32 v137, %[l16]\n\t" "v_mov_b32 v138, %[lp2]\n\t"                                                 \
  ".endif\n\t"                                                                                              \
  ".if " STR(EPI) " && (xk == 14)\n\t"                                                                      \
  "s_add_u32 %[sg], %[sg], " STR(NH * 8 * 1024) "\n\t"                                                      \
  "v_add_u32 v136, 1, v136\n\t"                                                                             \
  ".endif\n\t"                                                                                              \
  ".if !" STR(EPI) " && (xk == 1)\n\t"                                                                      \
  "buffer_store_dword v136, %[fo], %[rp], 0 offen sc1\n\t"                                                  \
  ".endif\n\t"                                                                                              \
  MF(3, HB, AC, EPI) XCV(EPI, PV, 3)                                                                        \
  ".if xk == 0\n\t"                                                                                         \
  "s_waitcnt vmcnt(" STR(XVM) ")\n\t" "s_barrier\n\t"                                                               \
  ".endif\n\t"                                                                                              \
  ".if xk == 2\n\t"                                                                                         \
  "s_min_u32 %[dg], %[dg], " STR(S_LEN * DM * 2 - 16384 + 1024) "\n\t"                                      \
  ".endif\n\t"                                                                                              \
  ".if " STR(EPI) " && (xk == " STR(XSTAT) ")\n\t"                                                                       \
  "buffer_store_dwordx4 v[132:135], v138, %[rf], %[sg] offen" XSC "\n\t"                                       \
  ".endif\n\t"                                                                                              \
  ".set xk, xk+1\n\t"                                                                                       \
  ".endr\n\t"
#define XLOOP(MF)                                                                                           \
  "s_mov_b32 %[kp], m0\n\t"                                                                                 \
  "v_mov_b32 v136, %[p0]\n\t" "v_mov_b32 v137, " STR(OOR) "\n\t" "v_mov_b32 v138, " STR(OOR) "\n\t"                                                                                \
  "ds_read_b128 v[96:99], %[r0]\n\tds_read_b128 v[100:103], %[r0] offset:" STR(16 * XPITCH) "\n\t"          \
  "ds_read_b128 v[104:107], %[r0] offset:64\n\tds_read_b128 v[108:111], %[r0] offset:64+" STR(16 * XPITCH) "\n\t"   \
  "ds_read_b128 v[112:115], %[r0] offset:128\n\tds_read_b128 v[116:119], %[r0] offset:128+" STR(16 * XPITCH) "\n"   \
  ".Lx%=:\n\t"                                                                                              \
  XHALF(MF, 0, 64, 80, 1, "%[r0]", "%[r1]", 3, 66560) XHALF(MF, 128, 64, 80, 0, "%[r1]", "%[r2]", 0, 131072) \
  XHALF(MF, 0, 80, 64, 1, "%[r2]", "%[r3]", 1, 132096) XHALF(MF, 128, 80, 64, 0, "%[r3]", "%[r0]", 2, 196608) \
  "s_add_u32 %[gb], %[gb], 131072\n\t"                                                                      \
  "s_sub_u32 %[cnt], %[cnt], 1\n\t"                                                                         \
  "s_cmp_lg_u32 %[cnt], 0\n\t"                                                                              \
  "s_cbranch_scc1 .Lx%=\n\t"                                                                                \
  "s_mov_b32 m0, %[kp]\n\t"                                                                                 \
  "s_nop 7\n\t" "s_waitcnt lgkmcnt(0)"

template <bool ISV>
__device__ __forceinline__ void x_role(unsigned char* lds, const u16* __restrict__ x, const u16* __restrict__ Wm, int fo0, int head,
                                       __amdgpu_buffer_rsrc_t rsf, u32* prog, u32 tag, int lane, int w) {
  const int ldsb = (int)(size_t)(LAS unsigned char*)lds;
  const __amdgpu_buffer_rsrc_t rsxin = __builtin_amdgcn_make_buffer_rsrc((void*)x, 0, S_LEN * DM * 2, 0x00020000);
  const __amdgpu_buffer_rsrc_t rsp = __builtin_amdgcn_make_buffer_rsrc((void*)prog, 0, 128, 0x00020000);
  const int lr = lane & 15, lg = lane >> 4;
  const int xrd = ldsb + lr * XPITCH + lg * 16;
  const int fob = fo0 + (ISV ? (head * 8 + 2 * w) : (head * 8 + w)) * 1024;
  const int p2 = ISV ? 1024 : 4096;
  u32x32 xw[8];
  {
    const __amdgpu_buffer_rsrc_t rw = __builtin_amdgcn_make_buffer_rsrc((void*)Wm, 0, DM * HD * 2, 0x00020000);
    const int vq = (lane >> 4) * (HD * 2) + head * 256 + (lane & 15) * 16;
#define XWISSUE(c) _Pragma("unroll") for (int j = 0; j < 8; ++j)                                                       \
      asm volatile("s_mov_b32 m0, %0\n\ts_nop 3\n\tbuffer_load_dwordx4 %1, %2, %3 offen lds"                           \
                   :: "s"(ldsb + ((c) & 3) * XSLOT + (8 * w + j) * XPITCH), "v"(vq), "s"(rw), "s"((128 * (c) + 32 * w + 4 * j) * (HD * 2)) : "memory");
#define XXISSUE(h) _Pragma("unroll") for (int j = 0; j < 8; ++j)                                                       \
      asm volatile("s_mov_b32 m0, %0\n\ts_nop 3\n\tbuffer_load_dwordx4 %1, %2, %3 offen lds"                           \
                   :: "s"(ldsb + (h) * XSLOT + (8 * w + j) * XPITCH), "v"(lane * 16), "s"(rsxin), "s"((((h) >> 1) * 32 + 8 * w + j) * 2048 + ((h) & 1) * 1024) : "memory");
    XWISSUE(0) XWISSUE(1) XWISSUE(2) XWISSUE(3)
    const int q = lr >> 2, p = lr & 3;
#pragma unroll
    for (int c = 0; c < 8; ++c) {
      asm volatile("s_waitcnt vmcnt(24)\n\ts_barrier" ::: "memory");
#pragma unroll
      for (int s = 0; s < 4; ++s)
#pragma unroll
        for (int t = 0; t < 2; ++t) {
          const unsigned char* base = lds + (c & 3) * XSLOT + (8 * s + 2 * lg) * XPITCH + q * 256 + w * 64 + p * 16 + t * 8;
          const u32x2 t0 = __builtin_bit_cast(u32x2, __builtin_amdgcn_ds_read_tr16_b64_v4i16((LAS v4i16_t*)(base)));
          const u32x2 t1 = __builtin_bit_cast(u32x2, __builtin_amdgcn_ds_read_tr16_b64_v4i16((LAS v4i16_t*)(base + XPITCH)));
          const int o = ((4 * c + s) * 2 + t) * 4;
          xw[o >> 5][(o & 31) + 0] = t0[0]; xw[o >> 5][(o & 31) + 1] = t0[1]; xw[o >> 5][(o & 31) + 2] = t1[0]; xw[o >> 5][(o & 31) + 3] = t1[1];
        }
      LBAR();
      if (c < 4) { XWISSUE(c + 4) } else if (c < 7) { XXISSUE(c - 4) }
    }
  }
  asm volatile("s_waitcnt vmcnt(8)\n\ts_barrier" ::: "memory");
  if (lane == 0 && w == 0) g_dbg[4352 + (fo0 == FO_K ? 0 : fo0 == FO_V ? 32 : 64) + head] = rt32();
  u32x16 xacc;
  {
    u32 kp, dg; int gb = 8 * w * 2048, cnt = NSEG / 2, sg = fob - NH * 8 * 1024;
#define XASM(MF)                                                                                            \
    asm volatile(XLOOP(MF)                                                                                  \
                 : "=&{v[80:95]}"(xacc), [kp] "=&s"(kp), [dg] "=&s"(dg), [gb] "+s"(gb), [cnt] "+s"(cnt), [sg] "+s"(sg) \
                 : [r0] "v"(xrd), [r1] "v"(xrd + XSLOT), [r2] "v"(xrd + 2 * XSLOT), [r3] "v"(xrd + 3 * XSLOT), [l16] "v"(lane * 16), [lp2] "v"(lane * 16 + p2), \
                   [rx] "s"(rsxin), [rf] "s"(rsf), [rp] "s"(rsp), [fo] "v"(lane == 0 && w == 0 ? 0 : OOR), [dl] "s"(ldsb + 8 * w * XPITCH), [p0] "s"(tag + XP0), \
                   "{a[0:31]}"(xw[0]), "{a[32:63]}"(xw[1]), "{a[64:95]}"(xw[2]), "{a[96:127]}"(xw[3]),      \
                   "{a[128:159]}"(xw[4]), "{a[160:191]}"(xw[5]), "{a[192:223]}"(xw[6]), "{a[224:255]}"(xw[7]) \
                 : CL64, CL72, CL96, CL112, CL120, CL8(128, 129, 130, 131, 132, 133, 134, 135), "v136", "v137", "v138", "scc", "memory");
    if (ISV) { XASM(XMF_V) } else { XASM(XMF_QK) }
  }
#pragma unroll
  for (int jj = 0; jj < 2; ++jj) {
    u32x4 pk;
#pragma unroll
    for (int e = 0; e < 4; ++e) pk[e] = pack2(__uint_as_float(xacc[8 * jj + 2 * e]), __uint_as_float(xacc[8 * jj + 2 * e + 1]));
    __builtin_amdgcn_raw_buffer_store_b128(pk, rsf, lane * 16 + p2 * jj, fob + (NSEG - 1) * (NH * 8 * 1024), 16);
  }
  asm volatile("s_waitcnt vmcnt(0)\n\ts_barrier" ::: "memory");
  if (lane == 0 && w == 0) __hip_atomic_store(prog, tag + NSEG + 8, RLX_AGENT);
}

#define SSM_LO 1
#define SU_CP 4416
#define SU_U 0
#define SU_K (4 * SU_CP)
#define SU_P (SU_K + 10400)
#define SU_D (SU_P + 2048)
#define SU_RAW (SU_D + 2592)
#define SU_CH (SU_RAW + 4128 + 32)
#define SST(i)
__device__ __forceinline__ void ssm_unit(unsigned char* sm, int d0, __amdgpu_buffer_rsrc_t rst, __amdgpu_buffer_rsrc_t rsx, unsigned char* xb, int fb, bool& uok,
                                         const u16* __restrict__ Dskip, int tid, int lane, int w) {
  unsigned char* cb = sm + w * SU_CH;
  const int dd = d0 + w;
  SST(0)
  if (!uok) {
    for (int it = 0; it < SPIN_CAP; ++it) {
      asm volatile("" ::: "memory");
      if ((int)__builtin_amdgcn_readfirstlane(__builtin_amdgcn_raw_buffer_load_b32(rsx, 0, XUDN, 16)) >= 15) break;
      asm volatile("s_sleep 8" ::: "memory");
    }
    asm volatile("" ::: "memory");
    uok = true;
  }
  f32x4 acc[3];
#pragma unroll
  for (int t = 0; t < 3; ++t) acc[t] = __builtin_bit_cast(f32x4, __builtin_amdgcn_raw_buffer_load_b128(rst, lane * 16, 2 * SSM_L * SSM_D * 2 + dd * 3072 + t * 1024, 16));
  const float dsk = bf2f(Dskip[dd]);
  u32x4 uv[4];
#pragma unroll
  for (int i = 0; i < 4; ++i) uv[i] = __builtin_amdgcn_raw_buffer_load_b128(rst, (tid + 256 * i) * 16, d0 * 4096, 16);
  const int r = lane & 31, h = lane >> 5;
  {
    const int lr = lane & 15, lg = lane >> 4;
    SST(2)
    u32* Kd = (u32*)(cb + SU_D);
#pragma unroll
    for (int e = 0; e < 4; ++e) {
      Kd[128 * (lg >> 1) + 8 * lr + 4 * (lg & 1) + e] = split_hl(acc[0][e]);
      Kd[lg < 2 ? 256 + 64 * lg + 4 * lr + e : 384 + 32 * (2 * (lg - 2) + (e >> 1)) + 2 * lr + (e & 1)] = split_hl(acc[1][e]);
      if (lg < 2 || (lg == 2 && e == 0 && lr == 0)) Kd[512 + 16 * (4 * lg + e) + lr] = split_hl(acc[2][e]);
    }
    asm volatile("s_waitcnt lgkmcnt(0)" ::: "memory");
    unsigned char* kr = cb + SU_K + (lane + 1) * 80;
#pragma unroll
    for (int c8 = 0; c8 < 4; ++c8) {
      u32 kv[8];
#pragma unroll
      for (int e = 0; e < 8; ++e) kv[e] = Kd[kidx(32 * lane + 31 - 8 * c8 - e)];
      u32x4 hi, lo;
#pragma unroll
      for (int e = 0; e < 4; ++e) {
        hi[e] = __builtin_amdgcn_perm(kv[2 * e + 1], kv[2 * e], 0x05040100u);
        lo[e] = __builtin_amdgcn_perm(kv[2 * e + 1], kv[2 * e], 0x07060302u);
      }
      *(u32x4*)(kr + 16 * c8) = hi;
      if (SSM_LO) *(u32x4*)(kr + 5200 + 16 * c8) = lo;
    }
    if (lane < 8) *(u32x4*)(cb + SU_K + (lane >> 2) * 5200 + (lane & 3) * 16) = (u32x4){0u, 0u, 0u, 0u};
  }
  SST(3)
#pragma unroll
  for (int i = 0; i < 4; ++i) {
    const int l = 2 * (tid + 256 * i) + 8;
    *(u32*)(sm + 0 * SU_CH + SU_RAW + l * 2) = __builtin_amdgcn_perm(uv[i][2], uv[i][0], 0x05040100u);
    *(u32*)(sm + 1 * SU_CH + SU_RAW + l * 2) = __builtin_amdgcn_perm(uv[i][2], uv[i][0], 0x07060302u);
    *(u32*)(sm + 2 * SU_CH + SU_RAW + l * 2) = __builtin_amdgcn_perm(uv[i][3], uv[i][1], 0x05040100u);
    *(u32*)(sm + 3 * SU_CH + SU_RAW + l * 2) = __builtin_amdgcn_perm(uv[i][3], uv[i][1], 0x07060302u);
  }
  if (lane < 2) *(u32x4*)(cb + SU_RAW + lane * (4096 + 16)) = (u32x4){0u, 0u, 0u, 0u};
  LBAR();
  {
    if (lane < 20) *(u32x4*)(cb + SU_U + (lane / 5) * SU_CP + (lane % 5) * 16) = (u32x4){0u, 0u, 0u, 0u};
#pragma unroll
    for (int i = 0; i < 5; ++i) {
      const int g = lane + 64 * i;
      if (g < 257) {
        const u32x4 a = *(const u32x4*)(cb + SU_RAW + g * 16);
        const u32x2 c = *(const u32x2*)(cb + SU_RAW + g * 16 + 16);
        unsigned char* dst = cb + SU_U + 80 + g * 16;
        *(u32x4*)(dst) = a;
        *(u32x4*)(dst + SU_CP) = (u32x4){__builtin_amdgcn_alignbyte(a[1], a[0], 2), __builtin_amdgcn_alignbyte(a[2], a[1], 2), __builtin_amdgcn_alignbyte(a[3], a[2], 2), __builtin_amdgcn_alignbyte(c[0], a[3], 2)};
        *(u32x4*)(dst + 2 * SU_CP) = (u32x4){a[1], a[2], a[3], c[0]};
        *(u32x4*)(dst + 3 * SU_CP) = (u32x4){__builtin_amdgcn_alignbyte(a[2], a[1], 2), __builtin_amdgcn_alignbyte(a[3], a[2], 2), __builtin_amdgcn_alignbyte(c[0], a[3], 2), __builtin_amdgcn_alignbyte(c[1], c[0], 2)};
      }
    }
  }
  asm volatile("s_waitcnt lgkmcnt(0)" ::: "memory");
  SST(4)
  f32x16 cv0, cv1;
  {
    const int m0 = 48 - 15 + 8 * h + r;
    const int cbo = (int)(size_t)(LAS unsigned char*)cb;
    int ua = cbo + SU_U + (m0 & 3) * SU_CP + (m0 & ~3) * 2 - 32;
    const int kb = cbo + SU_K + 16 * h;
    int a1 = kb + (33 + r) * 80, a0 = kb + (1 + r) * 80, cnt = 32;
#define CRA(S, O) "ds_read_b64 " RNG("v", S, 2) ", %[ua] offset:" STR(O) "\n\t" "ds_read_b64 " RNG("v", (S) + 2, 2) ", %[ua] offset:" STR((O) + 8) "\n\t"
#define CRB(S, ad, O) "ds_read_b128 " RNG("v", S, 4) ", " ad " offset:" STR(O) "\n\t"
#define CMF(C, A, B) "v_mfma_f32_32x32x16_bf16 " RNG("v", C, 16) ", " RNG("v", A, 4) ", " RNG("v", B, 4) ", " RNG("v", C, 16) "\n\t"
#define CUPD "v_add_u32 %[ua], 64, %[ua]\n\t" "v_subrev_u32 %[a1], 80, %[a1]\n\t" "v_subrev_u32 %[a0], 80, %[a0]\n\t" "v_max_i32 %[a0], %[a0], %[kb]\n\t"
#define CHALF(S, N, O, UPD)                                                                       \
    WLG(5) CMF(64, S, (S) + 8) UPD CRA(N, O)                                                      \
    WLG(6) CMF(80, S, (S) + 16) CRB((N) + 8, "%[a0]", O) CRB((N) + 16, "%[a1]", O)                \
    WLG(6) CMF(80, (S) + 4, (S) + 8) CRA((N) + 4, (O) + 2048)                                     \
    WLG(7) CMF(64, S, (S) + 12) CRB((N) + 12, "%[a0]", (O) + 5200)                                \
    WLG(7) CMF(80, S, (S) + 20) CRB((N) + 20, "%[a1]", (O) + 5200)                                \
    CMF(80, (S) + 4, (S) + 12)
    asm volatile(
        CRA(96, 32) CRB(104, "%[a0]", 32) CRB(112, "%[a1]", 32) CRA(100, 32 + 2048) CRB(108, "%[a0]", 32 + 5200) CRB(116, "%[a1]", 32 + 5200)
        ZER(64) ZER(68) ZER(72) ZER(76) ZER(80) ZER(84) ZER(88) ZER(92)
        ".Lc%=:\n\t"
        CHALF(96, 120, 0, "")
        CHALF(120, 96, 32, CUPD)
        "s_sub_u32 %[cnt], %[cnt], 1\n\t"
        "s_cmp_lg_u32 %[cnt], 0\n\t"
        "s_cbranch_scc1 .Lc%=\n\t"
        "s_nop 7\n\t" "s_nop 3\n\t" WLG(0)
        : "=&{v[64:79]}"(cv0), "=&{v[80:95]}"(cv1), [ua] "+v"(ua), [a0] "+v"(a0), [a1] "+v"(a1), [cnt] "+s"(cnt)
        : [kb] "v"(kb)
        : CL96, CL112, CL120, CL8(128, 129, 130, 131, 132, 133, 134, 135), CL8(136, 137, 138, 139, 140, 141, 142, 143),
          CL8(144, 145, 146, 147, 148, 149, 150, 151), CL8(152, 153, 154, 155, 156, 157, 158, 159), "scc", "memory");
  }
  SST(5)
  {
    u16* yo = (u16*)(cb + SU_K);
#pragma unroll
    for (int g = 0; g < 2; ++g)
#pragma unroll
      for (int i = 0; i < 16; i += 4) {
        const int rr = 2 * i + 4 * h, a = 32 * g + r;
        const u32x2 uu = *(const u32x2*)(cb + SU_U + (48 + 32 * a + rr) * 2);
        float y[4];
#pragma unroll
        for (int e = 0; e < 4; ++e) {
          const float uf = __uint_as_float((e & 1) ? (uu[e >> 1] & 0xffff0000u) : (uu[e >> 1] << 16));
          y[e] = rbf_hw(g ? cv1[i + e] : cv0[i + e]) + rbf_hw(uf * dsk);
        }
        *(u32x2*)(yo + a * 40 + rr) = (u32x2){pack2(y[0], y[1]), pack2(y[2], y[3])};
      }
  }
  LBAR();
  SST(6)
#pragma unroll
  for (int i = 0; i < 4; ++i) {
    const int l = 2 * (tid + 256 * i);
    u32 c[4];
#pragma unroll
    for (int j = 0; j < 4; ++j) c[j] = *(const u32*)(sm + j * SU_CH + SU_K + ((l >> 5) * 40 + (l & 31)) * 2);
    __builtin_amdgcn_raw_buffer_store_b128((u32x4){__builtin_amdgcn_perm(c[1], c[0], 0x05040100u), __builtin_amdgcn_perm(c[3], c[2], 0x05040100u),
                                                   __builtin_amdgcn_perm(c[1], c[0], 0x07060302u), __builtin_amdgcn_perm(c[3], c[2], 0x07060302u)},
                                           rst, l * 8, SSM_L * SSM_D * 2 + d0 * 4096, 16);
  }
  asm volatile("s_waitcnt vmcnt(0)" ::: "memory");
  LBAR();
  if (tid == 0) __hip_atomic_fetch_add((u32*)(xb + XYDN), 1u, RLX_AGENT);
  SST(7)
}

#define L_TOTAL 163840
__device__ __forceinline__ int ldw(__amdgpu_buffer_rsrc_t rs, int off) {
  asm volatile("" ::: "memory");
  return __builtin_amdgcn_readfirstlane(__builtin_amdgcn_raw_buffer_load_b32(rs, 0, off, 16));
}
#define GF(rt) "v[64+8*(xk&7)+4*" #rt ":64+8*(xk&7)+4*" #rt "+3]"
#define GW2 "v[128+4*xk:128+4*xk+3]"
#define GW3 "v[24+4*(xk&1):24+4*(xk&1)+3]"
#define GLOAD(S, K) "s_xor_b32 %[tq], " S ", 1024*(" K ")\n\t"
#define GTR(K) "ds_read_b128 v[24+4*((" K ")&1):24+4*((" K ")&1)+3], %[wl] offset:1024*((" K ")&31)\n\t"
#define GPACE 0
#define YAUX 16
#define UDELAY 100
#define GNOSSM 1
#define GNG 3
#define GS0 22
__device__ __forceinline__ void g_role(unsigned char* lds, const u16* __restrict__ Wo, u16* __restrict__ out, __amdgpu_buffer_rsrc_t rsx,
                                       __amdgpu_buffer_rsrc_t rsf, int fb, u32 tag, int grp, int sl, int w) {
  const int lane = fresh_lane();
  const int lr = lane & 15, lg = lane >> 4;
  const int rot = (2 * sl + 8 * w) & 31;
  const int ldsb = (int)(size_t)(LAS unsigned char*)lds;
  u32x32 xw[8], w2[4];
  LBAR();
  if (lane == 0 && w == 0) g_dbg[4480 + 16 * grp + sl] = rt32();
  {
    const __amdgpu_buffer_rsrc_t rw = __builtin_amdgcn_make_buffer_rsrc((void*)Wo, 0, DM * HD * 2, 0x00020000);
    const int vq = (lane >> 3) * (DM * 2) + sl * 128 + (lane & 7) * 16;
    const int sq = (1024 * w + rot * 32) * (DM * 2);
#define GSLOT(c) ((c) < 8 ? (((c) & 3) == 3 ? 131072 + w * 8192 : w * 32768 + 8192 * (((c) & 3) + 1)) : (((c) - 8) % 3 == 2 ? 131072 + w * 8192 : w * 32768 + 8192 * (((c) - 8) % 3 + 2)))
#define GISSUE(c) _Pragma("unroll") for (int j = 0; j < 8; ++j)                                                        \
      asm volatile("s_mov_b32 m0, %0\n\ts_nop 3\n\tbuffer_load_dwordx4 %1, %2, %3 offen lds"                           \
                   :: "s"(ldsb + GSLOT(c) + j * 1024), "v"(vq), "s"(rw), "s"(sq ^ ((64 * (c) + 8 * j) * DM * 2)) : "memory");
    GISSUE(0) GISSUE(1) GISSUE(2) GISSUE(3)
    const int q = lr >> 2, p = lr & 3;
    u32x4 w3[16];
#pragma unroll
    for (int c = 0; c < 16; ++c) {
      if (c < 5) waitvm<24>(); else if (c < 14) waitvm<16>(); else if (c == 14) waitvm<8>(); else waitvm<0>();
      u32x4 f3[2];
#pragma unroll
      for (int s = 0; s < 2; ++s)
#pragma unroll
        for (int t = 0; t < 4; ++t) {
          const unsigned char* base = lds + GSLOT(c) + (32 * s + 8 * lg + q) * 128 + (t < 2 ? p * 16 + t * 8 : t * 32 + p * 8);
          const u32x2 t0 = __builtin_bit_cast(u32x2, __builtin_amdgcn_ds_read_tr16_b64_v4i16((LAS v4i16_t*)(base)));
          const u32x2 t1 = __builtin_bit_cast(u32x2, __builtin_amdgcn_ds_read_tr16_b64_v4i16((LAS v4i16_t*)(base + 512)));
          const int o = t < 2 ? ((2 * c + s) * 2 + t) * 4 : (2 * c + s) * 4;
          if (t < 2) { xw[o >> 5][(o & 31) + 0] = t0[0]; xw[o >> 5][(o & 31) + 1] = t0[1]; xw[o >> 5][(o & 31) + 2] = t1[0]; xw[o >> 5][(o & 31) + 3] = t1[1]; }
          else if (t == 2) { w2[o >> 5][(o & 31) + 0] = t0[0]; w2[o >> 5][(o & 31) + 1] = t0[1]; w2[o >> 5][(o & 31) + 2] = t1[0]; w2[o >> 5][(o & 31) + 3] = t1[1]; }
          else f3[s] = (u32x4){t0[0], t0[1], t1[0], t1[1]};
        }
      asm volatile("s_waitcnt lgkmcnt(0)" ::: "memory");
      if (c < 8) { *(u32x4*)(lds + w * 32768 + (2 * c) * 1024 + lane * 16) = f3[0]; *(u32x4*)(lds + w * 32768 + (2 * c + 1) * 1024 + lane * 16) = f3[1]; }
      else { w3[2 * (c - 8)] = f3[0]; w3[2 * (c - 8) + 1] = f3[1]; }
      if (GPACE) __builtin_amdgcn_s_sleep(GPACE);
      if (c < 4) { GISSUE(c + 4) } else if (c >= 5 && c < 13) { GISSUE(c + 3) }
    }
#pragma unroll
    for (int i = 0; i < 16; ++i) *(u32x4*)(lds + w * 32768 + (16 + i) * 1024 + lane * 16) = w3[i];
    LBAR();
  }
  if (lane == 0 && w == 0) g_dbg[4544 + 16 * grp + sl] = rt32();
  wait_start(rsx, fb, tag, true);
  const int wl = ldsb + w * 32768 + lane * 16;
  const __amdgpu_buffer_rsrc_t rso = __builtin_amdgcn_make_buffer_rsrc((void*)out, 0, S_LEN * OUTLD * 2, 0x00020000);
  const int rw_ = ldsb + 131072 + w * 8192 + lane * 16, rr_ = ldsb + 131072 + w * 2048 + lane * 16;
  const int oa = ((16 * (w & 1) + lr) * OUTLD + 64 * sl + (w < 2 ? 8 * lg : 32 + 4 * lg)) * 2, ob = oa + (w < 2 ? 8 : 32);
  const int hq = (grp + GNG - (62 - GS0) % GNG) % GNG - 1, hf = hq < 2 ? hq : -1;
#define GRW(i) "ds_write_b128 %[rw], " RNG("v", 32 + 4 * (i), 4) " offset:" STR(1024 * (i)) "\n\t"
#define GRR(i) "ds_read_b128 " RNG("v", 32 + 4 * (i), 4) ", %[rr] offset:" STR(8192 * ((i) >> 1) + 1024 * ((i) & 1)) "\n\t"
#define GNXT(D, S) "s_cmp_lt_u32 " S ", " STR(GS0) "\n\t" "s_cselect_b32 " D ", 1, " STR(GNG) "\n\t" "s_add_u32 " D ", " D ", " S "\n\t"
#define GDN(S) "s_min_u32 s41, " S ", 63\n\t" "s_lshl_b32 s41, s41, 5\n\t" "v_add_u32 v23, s41, %[u0]\n\t" "v_lshrrev_b32 v22, 6, v23\n\t" \
               "v_and_b32 v23, 63, v23\n\t" "v_lshlrev_b32 v23, 7, v23\n\t" "s_lshr_b32 s41, s41, 6\n\t" "s_max_u32 s41, s41, 29\n\t" "s_lshl_b32 s41, s41, 13\n\t" "s_add_u32 s41, s41, %[pa]\n\t" \
               "buffer_load_dword v23, v23, %[rs], s41 offen sc1\n\t"
#define GOK(D) "v_cmp_le_i32 vcc, v23, v22\n\t" "s_cmp_eq_u64 vcc, 0\n\t" "s_cselect_b32 " D ", 1, 0\n\t"
static_assert(NAW == 64, "shifts above");
#define GEPI(MID, OA, OB) \
      GRW(0) GRW(1) GRW(2) GRW(3) GRW(4) GRW(5) GRW(6) GRW(7) \
      MID \
      WLG(0) "s_barrier\n\t" \
      GRR(0) GRR(1) GRR(2) GRR(3) GRR(4) GRR(5) GRR(6) GRR(7) \
      WLG(4) PKA(32, 32, 40) PKA(34, 34, 42) PKA(36, 36, 44) PKA(38, 38, 46) \
      WLG(0) PKA(48, 48, 56) PKA(50, 50, 58) PKA(52, 52, 60) PKA(54, 54, 62) \
      PKA(32, 32, 48) PKA(34, 34, 50) PKA(36, 36, 52) PKA(38, 38, 54) \
      "v_cvt_pk_bf16_f32 v20, v32, v33\n\t" "v_cvt_pk_bf16_f32 v21, v34, v35\n\t" "v_cvt_pk_bf16_f32 v18, v36, v37\n\t" "v_cvt_pk_bf16_f32 v19, v38, v39\n\t" \
      "buffer_store_dwordx2 v[20:21], " OA ", %[ro], s42 offen\n\t" \
      "buffer_store_dwordx2 v[18:19], " OB ", %[ro], s42 offen\n\t"
#define GH "v[64+4*(xk&15):64+4*(xk&15)+3]"
  asm volatile(
      "s_nop 4\n\t"
      "v_add_u32 v16, 131072, %[va]\n\t" "v_sub_u32 v15, %[ob], %[oa]\n\t" "v_add_u32 v15, v15, %[oc]\n\t"
      "s_mov_b32 s36, %[s0]\n\t" "s_mov_b32 s43, " STR(SPIN_CAP) "\n"
      ".Lp%=:\n\t"
      GDN("s36") WVM(0) GOK("s40")
      "s_cbranch_scc1 .Lr%=\n\t"
      "s_sleep 2\n\t" "s_sub_u32 s43, s43, 1\n\t" "s_cmp_gt_i32 s43, 0\n\t" "s_cbranch_scc1 .Lp%=\n"
      ".Lr%=:\n\t"
      GNXT("s37", "s36") GDN("s37")
      "s_lshl_b32 s38, s36, 18\n\t" "s_add_u32 s38, s38, %[gb]\n\t"
      ".set xk, 0\n\t.rept 8\n\t" GLOAD("s38", "xk")
      "buffer_load_dwordx4 " GF(0) ", %[va], %[rf], %[tq] offen sc1\n\t" "buffer_load_dwordx4 " GF(1) ", v16, %[rf], %[tq] offen sc1\n\t"
      ".set xk, xk+1\n\t.endr\n\t"
      WVM(16) GOK("s40")
      ".Lb%=:\n\t"
      GNXT("s37", "s36") GNXT("s41", "s37") GDN("s41") GTR("0")
      "s_cmp_lt_u32 s37, 63\n\t" "s_cselect_b32 s40, s40, 0\n\t"
      "s_cmp_lg_u32 s40, 0\n\t" "s_cselect_b32 s39, s37, s36\n\t"
      "s_lshl_b32 s39, s39, 18\n\t" "s_add_u32 s39, s39, %[gb]\n\t" "s_lshl_b32 s38, s36, 18\n\t" "s_add_u32 s38, s38, %[gb]\n\t"
      "s_lshl_b32 s42, s36, 17\n\t"
      "buffer_load_dword v17, %[vt], %[rf], s39 offen sc1\n\t"
      ".set xk, 0\n\t.rept 32\n\t"
      "s_waitcnt vmcnt(14)\n\t"
      XM1(XA(32, 0), XW(0, 0), GF(0), 1) GTR("xk+1") XM1(XA(32, 4), XW(0, 1), GF(0), 1)
      ".if xk < 24\n\t" GLOAD("s38", "xk+8") ".else\n\t" GLOAD("s39", "xk-24") ".endif\n\t"
      XM1(XA(32, 8), XW(0, 0), GF(1), 1) XM1(XA(32, 12), XW(0, 1), GF(1), 1)
      XM1(XA(32, 16), GW2, GF(0), 1) "s_waitcnt lgkmcnt(1)\n\t" XM1(XA(32, 20), GW3, GF(0), 1)
      "buffer_load_dwordx4 " GF(0) ", %[va], %[rf], %[tq] offen sc1\n\t"
      XM1(XA(32, 24), GW2, GF(1), 1) XM1(XA(32, 28), GW3, GF(1), 1)
      "buffer_load_dwordx4 " GF(1) ", v16, %[rf], %[tq] offen sc1\n\t"
      ".if xk == 16\n\t" "s_barrier\n\t" ".endif\n\t"
      ".set xk, xk+1\n\t.endr\n\t"
      GEPI(GOK("s41"), "%[oa]", "%[ob]")
      "s_mov_b32 s36, s37\n\t"
      "s_cmp_lg_u32 s40, 0\n\t" "s_mov_b32 s40, s41\n\t" "s_cbranch_scc1 .Lb%=\n\t"
      "s_cmp_lt_u32 s36, 63\n\t" "s_cbranch_scc1 .Lp%=\n\t"
      "s_cmp_lt_i32 %[hf], 0\n\t" "s_cbranch_scc1 .Le%=\n\t"
      "s_mov_b32 s36, 63\n"
      ".Lh%=:\n\t"
      GDN("s36") WVM(0) GOK("s40")
      "s_cbranch_scc1 .Li%=\n\t"
      "s_sleep 2\n\t" "s_sub_u32 s43, s43, 1\n\t" "s_cmp_gt_i32 s43, 0\n\t" "s_cbranch_scc1 .Lh%=\n"
      ".Li%=:\n\t"
      "s_lshl_b32 s38, %[hf], 17\n\t" "s_add_u32 s38, s38, 63*262144\n\t" "s_add_u32 s38, s38, %[gb]\n\t"
      ".set xk, 0\n\t.rept 16\n\t" GLOAD("s38", "xk") "buffer_load_dwordx4 " GH ", %[va], %[rf], %[tq] offen sc1\n\t" ".set xk, xk+1\n\t.endr\n\t"
      GTR("0")
      "s_lshl_b32 s42, %[hf], 16\n\t" "s_add_u32 s42, s42, 63*131072\n\t"
      ".set xk, 0\n\t.rept 32\n\t"
      ".if xk < 17\n\t" "s_waitcnt vmcnt(15)\n\t" ".else\n\t" "s_waitcnt vmcnt(31-xk)\n\t" ".endif\n\t"
      XM1(XA(32, 0), XW(0, 0), GH, 1) GTR("xk+1") XM1(XA(32, 4), XW(0, 1), GH, 1)
      XM1(XA(32, 16), GW2, GH, 1) "s_waitcnt lgkmcnt(1)\n\t" XM1(XA(32, 20), GW3, GH, 1)
      ".if xk < 16\n\t" GLOAD("s38", "xk+16") "buffer_load_dwordx4 " GH ", %[va], %[rf], %[tq] offen sc1\n\t" ".endif\n\t"
      ".if xk == 16\n\t" "s_barrier\n\t" ".endif\n\t"
      ".set xk, xk+1\n\t.endr\n\t"
      "s_nop 7\n\t"
      GEPI("", "%[oc]", "v15")
      ".Le%=:\n\t"
      WVM(0)
      : [tq] "=&s"(sl)
      : [rs] "s"(rsx), [s0] "s"(__builtin_amdgcn_readfirstlane(grp ? GS0 + grp : 0)), [gb] "s"(FO_O - 32768 + (32 * w + rot) * 1024), [va] "v"(lane * 16 + 32768),
        [rf] "s"(rsf), [wl] "v"(wl), [rw] "v"(rw_), [rr] "v"(rr_), [oa] "v"(oa), [ob] "v"(ob), [ro] "s"(rso), [u0] "v"(8 * w + (lane & 7)), [pa] "s"(PGO_A - 29 * 8192), [hf] "s"(__builtin_amdgcn_readfirstlane(hf)), [oc] "v"((w & 1) ? OOR - 64 : oa), [vt] "v"((sl >> 3) * 131072 + 32768 + (sl & 7) * 4096 + lane * 64 - rot * 1024),
        "{a[0:31]}"(xw[0]), "{a[32:63]}"(xw[1]), "{a[64:95]}"(xw[2]), "{a[96:127]}"(xw[3]),
        "{a[128:159]}"(xw[4]), "{a[160:191]}"(xw[5]), "{a[192:223]}"(xw[6]), "{a[224:255]}"(xw[7]),
        "{v[128:159]}"(w2[0]), "{v[160:191]}"(w2[1]), "{v[192:223]}"(w2[2]), "{v[224:255]}"(w2[3])
      : "v15", "v16", "v17", "v18", "v19", "v20", "v21", "v22", "v23", "vcc", CL8(24, 25, 26, 27, 28, 29, 30, 31), CL8(32, 33, 34, 35, 36, 37, 38, 39), CL8(40, 41, 42, 43, 44, 45, 46, 47),
        CL8(48, 49, 50, 51, 52, 53, 54, 55), CL8(56, 57, 58, 59, 60, 61, 62, 63), CL64, CL72, CL80, CL88, CL96, CL112, CL120,
        "s36", "s37", "s38", "s39", "s40", "s41", "s42", "s43", "scc", "memory");
}
static_assert(4 * SU_CH <= L_TOTAL && 4 * XSLOT <= L_TOTAL, "lds");
__global__ void __launch_bounds__(256, 1)
main_kernel(const u16* __restrict__ x, const u16* __restrict__ mem0, const u16* __restrict__ Wq, const u16* __restrict__ Wk, const u16* __restrict__ Wv, const u16* __restrict__ Wo,
            unsigned char* ws, const u16* __restrict__ u, const u16* __restrict__ A_re, const u16* __restrict__ A_im, const u16* __restrict__ B_re,
            const u16* __restrict__ B_im, const u16* __restrict__ C_re, const u16* __restrict__ C_im,
            const u16* __restrict__ log_dt, const u16* __restrict__ Dskip, u16* __restrict__ out
            , int tun_r, int tun_p, int tun_b, int tun_c
            ) {
  __shared__ __attribute__((aligned(8192))) unsigned char lds[L_TOTAL];
  const int w = __builtin_amdgcn_readfirstlane(threadIdx.x >> 6);
  u64 tf_; asm volatile("s_memrealtime %0" : "=s"(tf_));
  u64 pc0;
  int clen;
  {
    asm volatile("s_getpc_b64 %0\n.Lkpc0:\n\ts_mov_b32 %1, .Lkpc1-.Lkpc0" : "=s"(pc0), "=s"(clen));
  }
#define LOCAL_IDS const int lane = fresh_lane(), tid = w * 64 + lane; (void)tid;
  const int si = (blockIdx.x >> 3) * 4 + (blockIdx.x & 3);
  unsigned char* xb = (unsigned char*)(((size_t)ws + XPAGE - 1) & ~(size_t)(XPAGE - 1));
  xb += (size_t)(tun_r >> 8) * XPAGE; tun_r &= 255;
  const int fb = (int)(ws + WS_F - xb);
  u32 tag;
  asm volatile("s_load_dword %0, %1, 0x0\n\ts_waitcnt lgkmcnt(0)\n\ts_add_u32 %0, %0, 256" : "=s"(tag) : "s"(ws + WS_F) : "scc", "memory");
  const __amdgpu_buffer_rsrc_t rsx = __builtin_amdgcn_make_buffer_rsrc(xb, 0, fb + (int)WS_F_SZ, 0x00020000);
  const __amdgpu_buffer_rsrc_t rsf = __builtin_amdgcn_make_buffer_rsrc(ws + WS_QX, 0, (int)(4 * WS_QKV_SZ + 2 * WS_KM_SZ), 0x00020000);
  const __amdgpu_buffer_rsrc_t rst = __builtin_amdgcn_make_buffer_rsrc(ws + WS_UT, 0, 2 * SSM_L * SSM_D * 2 + SSM_D * 3072, 0x00020000);
  u32* dbg = (u32*)(ws + WS_TOTAL + (8 << 20));
  const u32 tstart = (u32)tf_;
  g_dbg = dbg;
  if ((blockIdx.x & 7) < 4) {
    { LOCAL_IDS
    r_role(lds, si, mem0, Wq, Wk, Wv, Wo, fb, tag, pc0, clen, rsx, rsf, rst, SsmPtrs{A_re, A_im, B_re, B_im, C_re, C_im, log_dt}, tid, lane, w
           , dbg, tun_r, tun_p, tun_b, tun_c
           ); }
    LOCAL_IDS
    if (tid == 0) dbg[2048 + blockIdx.x * 4 + 2] = rt32();
    {
      for (int it = 0; it < SPIN_CAP; ++it) {
        if (ldw(rsx, XYDN) >= 255) break;
        asm volatile("s_sleep 8" ::: "memory");
      }
      asm volatile("" ::: "memory");
      asm volatile(".Lkpc1:" ::: "memory");
      unsigned char* im = lds + 65536;
      const __amdgpu_buffer_rsrc_t rso = __builtin_amdgcn_make_buffer_rsrc((void*)out, 0, S_LEN * OUTLD * 2, 0x00020000);
      u32x4 t[8];
#pragma unroll
      for (int i = 0; i < 8; ++i) { const int p = i * 256 + tid; t[i] = __builtin_amdgcn_raw_buffer_load_b128(rst, (p >> 3) * 16384 + (p & 7) * 16, SSM_L * SSM_D * 2 + si * 128, 16); }
#pragma unroll
      for (int i = 0; i < 8; ++i) {
        const int p = i * 256 + tid, un = p >> 3, rp = p & 7;
        *(u32x2*)(im + (2 * rp) * 2064 + un * 8) = (u32x2){t[i][0], t[i][1]};
        *(u32x2*)(im + (2 * rp + 1) * 2064 + un * 8) = (u32x2){t[i][2], t[i][3]};
      }
      LBAR();
#pragma unroll
      for (int i = 0; i < 8; ++i) {
        const int p = i * 256 + tid;
        __builtin_amdgcn_raw_buffer_store_b128(*(const u32x4*)(im + (p >> 7) * 2064 + (p & 127) * 16), rso, ((p >> 7) * OUTLD + DM + (p & 127) * 8) * 2, si * 16 * OUTLD * 2, YAUX);
      }
    }
  } else {
    const int g = blockIdx.x & 3, li = blockIdx.x >> 3;
    const int xi = g == 0 ? (li < 16 ? li : -1) : (li < 26 + (g < 3) ? 16 + 27 * (g - 1) + li : -1);
    const int ei = g == 0 ? li - 16 : 16 + 5 * (g - 1) + (31 - li);
    if (xi >= 0) {
      LOCAL_IDS
      const int head = xi & 31, mi = xi >> 5;
      u32* prog = (u32*)(xb + PGO_X + xi * 128);
      if (mi == 1) x_role<true>(lds, x, Wv, FO_V, head, rsf, prog, tag, lane, w);
      else x_role<false>(lds, x, mi == 0 ? Wk : Wq, mi == 0 ? FO_K : FO_Q, head, rsf, prog, tag, lane, w);
    } else {
      LOCAL_IDS
      u32 acc = 0;
#pragma unroll
      for (int i = 0; i < 4; ++i) acc += *(const u32*)(x + (size_t)((i * 32 + ei) * 256 + tid) * 64);
#define UV 0
#define WWARM(n, first) {    \
        const __amdgpu_buffer_rsrc_t rwo = __builtin_amdgcn_make_buffer_rsrc((void*)Wo, 0, DM * HD * 2, 0x00020000); \
        const int ldsb = (int)(size_t)(LAS unsigned char*)lds; \
        _Pragma("unroll 1") for (int c = 0; c < (n); ++c) { dma16(rwo, lane * 16, ((first) + c) * 16384, 1024, ldsb + w * 32768 + (c & 1) * 16384); waitvm<32>(); } }
      if (UV == 0 || (UV == 1 && ei < 16)) WWARM(4, (ei * 4 + w) * 4)
      if (UV == 2 && ei < 16) WWARM(8, (ei * 4 + w) * 8)
      if (ei == 0 && tid < 16) acc += *(const u32*)(Dskip + tid * 64);
      asm volatile("" :: "v"(acc));
      if (ei >= 16) {
      { const u32 t0 = rt32(); for (int i = 0; i < 1000 && (int)(rt32() - t0) < UDELAY; ++i) asm volatile("s_sleep 16" ::: "memory"); }
      asm volatile("s_waitcnt vmcnt(0)\n\ts_barrier" ::: "memory");
      for (int ps = 0; ps < 2; ++ps) {
      const int r0 = ((ei - 16) * 2 + ps) * 64;
#pragma unroll
      for (int hb = 0; hb < 2; ++hb) {
        u32x4 t[16];
#pragma unroll
        for (int i = 0; i < 16; ++i) t[i] = *(const u32x4*)(u + (size_t)r0 * SSM_D + ((hb * 16 + i) * 256 + tid) * 8);
#pragma unroll
        for (int i = 0; i < 16; ++i) { const int p = (hb * 16 + i) * 256 + tid; *(u32x4*)(lds + (p >> 7) * 2064 + (p & 127) * 16) = t[i]; }
      }
      LBAR();
      for (int i = 0; i < 32; ++i) {
        const int p = i * 256 + tid, un = p >> 5, rp = p & 31;
        const u32x2 a = *(const u32x2*)(lds + (2 * rp) * 2064 + un * 8), c = *(const u32x2*)(lds + (2 * rp + 1) * 2064 + un * 8);
        __builtin_amdgcn_raw_buffer_store_b128((u32x4){a[0], a[1], c[0], c[1]}, rst, un * 16384 + rp * 16, r0 * 8, 16);
      }
      asm volatile("s_waitcnt vmcnt(0) lgkmcnt(0)\n\ts_barrier" ::: "memory");
      }
      if (tid == 0) dbg[4672 + ei] = rt32();
      if (UV == 1) { WWARM(4, (ei * 4 + w) * 4) waitvm<0>(); }
      wait_start(rsx, fb, tag, true);
      if (tid == 0) __hip_atomic_fetch_add((u32*)(xb + XUDN), 1u, RLX_AGENT);
      }
    }
    if (w == 0 && fresh_lane() == 0) dbg[2048 + blockIdx.x * 4 + 2] = rt32();
    if (w == 0 && fresh_lane() == 0) dbg[2048 + blockIdx.x * 4 + 3] = rt32();
    asm volatile("s_waitcnt lgkmcnt(0)\n\ts_barrier" :: "s"(tag) : "memory");
    if (w == 0 && fresh_lane() == 0) __builtin_amdgcn_raw_buffer_store_b32(tag, rsx, 0, PGO_S + si * 128, 16);
    const bool isg = g == 0 ? li >= 16 : (g >= 5 - GNG && li < 16);
    if (xi >= 0 && !(GNOSSM && isg)) {
      LOCAL_IDS
      bool uok = false;
      wait_start(rsx, fb, tag, true);
      while (true) {
        LBAR();
        if (tid == 0) *(u32*)(lds + L_TOTAL - 16) = __hip_atomic_fetch_add((u32*)(xb + XTKT), 1u, RLX_AGENT) + 1u;
        LBAR();
        const int un = __builtin_amdgcn_readfirstlane(*(const u32*)(lds + L_TOTAL - 16));
        if (un >= 256) break;
        ssm_unit(lds, 4 * un, rst, rsx, xb, fb, uok, Dskip, tid, lane, w);
        if (isg) break;
      }
      if (tid == 0) dbg[2048 + blockIdx.x * 4 + 3] = rt32();
    }
    if (isg) {
      g_role(lds, Wo, out, rsx, rsf, fb, tag, g ? g + GNG - 4 : 0, li & 15, w);
      if (w == 0 && fresh_lane() == 0) g_dbg[4608 + 16 * (g ? g + GNG - 4 : 0) + (li & 15)] = rt32();
    }
    else {
      const int lane = fresh_lane();
      int sh = (ei - 16) * 4 + w, lim = (NSEG - 4) * NH, wi = sh, dn = 0;
      bool go = xi < 0;
      while (true) {
        if (go) a_role(rsx, rsf, sh, lim, wi, dn, lane, fb, tag);
        if (xi >= 0 && (w >= 2 || go)) break;
        const int t = __builtin_amdgcn_readfirstlane(lane == 0 ? __hip_atomic_fetch_add((u32*)(xb + XTK2), 1u, RLX_AGENT) + 1u : 0u);
        if (t >= 128) break;
        wi = 64 + t; sh = (NSEG - 4) * NH + t; lim = sh + 1; dn = 31; go = true;
      }
    }
  }
  if (w == 0 && fresh_lane() == 0) { dbg[2048 + blockIdx.x * 4] = tstart; dbg[2048 + blockIdx.x * 4 + 1] = rt32(); }
}

__global__ void pre_kernel(unsigned char* ws) { const u32 t = rt32(); if (threadIdx.x == 0 && blockIdx.x == 0) ((u32*)(ws + WS_TOTAL + (8 << 20)))[4800] = t; }
__global__ void diag_kernel(unsigned char* ws) {
  const u32* h = (const u32*)(ws + WS_TOTAL + (8 << 20));
  __shared__ int mn, mx;
  const int b = threadIdx.x;
  if (b == 0) { mn = 1 << 30; mx = -(1 << 30); }
  __syncthreads();
  const u32 ref = h[2048];
  atomicMin(&mn, (int)(h[2048 + b * 4] - ref));
  __syncthreads();
  const u32 t0 = ref + mn;
  const bool r = (b & 7) < 4; const int li = b >> 3;
  int v = 0;
  if (DIAG == 1) v = h[0] - t0;
  if (DIAG == 2) v = r ? h[2048 + b * 4 + 2] - t0 : 0;
  if (DIAG == 3) v = h[2048 + b * 4 + 1] - t0;
  if (DIAG == 4) v = h[3504] * 4;
  if (DIAG == 5) v = b < 128 ? h[3072 + b * 2] - t0 : 0;
  if (DIAG == 6) v = (h[3500] >> 20) * 25;
  if (DIAG == 7) v = r ? 0 : h[2048 + b * 4 + 3] - t0;
  if (DIAG == 8) v = (!r && (b & 3) == 0 && li >= 16) ? h[2048 + b * 4 + 1] - t0 : 0;
  if (DIAG == 9) v = h[2048 + b * 4] - t0;
  if (DIAG == 10) v = h[3503] - t0;
  if (DIAG == 11) v = (b >= 16 && b < 32) ? h[4672 + b] - t0 : 0;
  if (DIAG == 12) v = r ? h[2048 + b * 4 + 1] - t0 : 0;
  if (DIAG == 13) v = r ? 0 : h[2048 + b * 4 + 2] - t0;
  if (DIAG == 14) v = rt32() - t0;
  if (DIAG == 15) v = (!r && (b & 3) >= 1 && li < 16) ? h[2048 + b * 4 + 1] - t0 : 0;
  if (DIAG == 16) v = b < 96 ? h[4352 + b] - t0 : 0;
  if (DIAG == 17) v = h[1] - t0;
  if (DIAG == 50 || DIAG == 52) v = 0; if (DIAG == 51) v = t0 - h[4800];
  atomicMax(&mx, v);
  __syncthreads();
  v = min(max(mx, 0), 100000);
  if (b == 0) for (int i = 0; i < 4000000 && (int)(rt32() - h[4800]) < 30000 + 4 * v; ++i) asm volatile("s_sleep 1" ::: "memory");
}
extern "C" void kernel_launch(void* const* d_in, const int* in_sizes, int n_in, void* d_out, int out_size,
                              void* d_ws, size_t ws_size, hipStream_t stream) {
  const u16* x = (const u16*)d_in[0];
  const u16* mem0 = (const u16*)d_in[1];
  const u16* Wq = (const u16*)d_in[2];
  const u16* Wk = (const u16*)d_in[3];
  const u16* Wv = (const u16*)d_in[4];
  const u16* Wo = (const u16*)d_in[5];
  const u16* u = (const u16*)d_in[7];
  const u16* A_re = (const u16*)d_in[8];
  const u16* A_im = (const u16*)d_in[9];
  const u16* B_re = (const u16*)d_in[10];
  const u16* B_im = (const u16*)d_in[11];
  const u16* C_re = (const u16*)d_in[12];
  const u16* C_im = (const u16*)d_in[13];
  const u16* log_dt = (const u16*)d_in[14];
  const u16* Dskip = (const u16*)d_in[15];
  u16* out = (u16*)d_out;
  char* ws = (char*)d_ws;

  pre_kernel<<<PREG, 64, 0, stream>>>((unsigned char*)ws);
  main_kernel<<<256, 256, 0, stream>>>(x, mem0, Wq, Wk, Wv, Wo, (unsigned char*)ws, u, A_re, A_im, B_re, B_im, C_re, C_im, log_dt, Dskip, out
      , g_tune[0], g_tune[1], g_tune[2], g_tune[3]
      );
  diag_kernel<<<1, 256, 0, stream>>>((unsigned char*)ws);
}
```

```cpp
#include <hip/hip_runtime.h>
#include <hip/hip_bf16.h>
#include <stdint.h>

#define S_LEN 2048
#define DM 1024
#define NH 32
#define DH 128
#define HD 4096
#define MEM 8
#define SEG 32
#define NSEG 64
#define LTOK 40
#define SSM_L 2048
#define SSM_D 1024
#define SSM_N 128
#define OUTLD 2048

typedef unsigned short u16;
typedef unsigned int u32;
typedef unsigned long long u64;
typedef __attribute__((ext_vector_type(8))) short bf16x8;
typedef __attribute__((ext_vector_type(4))) float f32x4;
typedef __attribute__((ext_vector_type(4))) unsigned int u32x4;
typedef __attribute__((ext_vector_type(2))) unsigned int u32x2;
typedef __bf16 bf16x2_t __attribute__((ext_vector_type(2)));
typedef short v4i16_t __attribute__((ext_vector_type(4)));

#define SENT 0xFFFFFFFFu
#define RLX_AGENT __ATOMIC_RELAXED, __HIP_MEMORY_SCOPE_AGENT

__device__ __forceinline__ float bf2f(u16 v) { return __uint_as_float(((u32)v) << 16); }
__device__ __forceinline__ u16 f2bf(float f) {
  u32 u = __float_as_uint(f);
  u32 r = (u + 0x7fffu + ((u >> 16) & 1u)) >> 16;
  if ((u & 0x7fffffffu) > 0x7f800000u) r = 0x7fc0u;
  return (u16)r;
}
__device__ __forceinline__ float rbf(float f) { return bf2f(f2bf(f)); }

#define TR 92
#define TP 179
#define TDB 61
#define TDC 110
#define NR1 4
#define XPAGE (2 << 20)
#define XRING 3
#define XLP(p, L) ((((L) >> 13) << 15) + ((p) << 14) + ((L) & 8191))
#define XO_R3 131072
#define XR1A(x, L) XLP((x) & 1, ((x) >> 1) * 16384 + (L))
#define XR3A(rg, L) (XO_R3 + XLP(rg, L))
#define XSB(s) (((s) % XRING) == 2 ? 1048576 : ((s) % XRING) * 262144)
#define XR2B 524288
#define XR2S 1048576
#define XR2(j) (((((j) & 31) + (((j) >> 6) << 5)) / 6 << 15) + (((((j) >> 5) & 1) ^ 1) << 14) + ((((j) & 31) + (((j) >> 6) << 5)) % 6) * 1280)
#define XSYN (1048576 + 262144)
#define PGO_R (XSYN + 32768)
#define XCAL (PGO_R + 32768)
static_assert(XCAL + 16384 <= XPAGE, "exchange page");
#define WS_X 0ull
#define NPG 2
#define WS_X_SZ ((u64)NPG * XPAGE)
#define WS_F (WS_X + WS_X_SZ)
#define XEPO (fb)
#define XSTA (fb + 128)
#define PGO_X (fb + 256)
#define PGO_Y (PGO_X + 96 * 128)
#define PGO_S (PGO_Y + 128 * 128)
#define PGO_D (PGO_S + 128 * 128)
#define PGO_A (PGO_D + 128 * 128)
#define XTKT (PGO_A + 192 * 128)
#define XUDN (XTKT + 128)
#define XYDN (XUDN + 128)
#define XTK2 (XYDN + 128)
#define XSTR(x) (XTK2 + 128 + (x) * 128)
#define WS_F_SZ ((u64)(2 + 96 + 128 + 128 + 128 + 192 + 8) * 128)
#define WS_XCH_END (WS_F + 131072)
#define WS_QX (WS_XCH_END)
#define WS_QKV_SZ ((u64)S_LEN * HD * 2)
#define WS_KX (WS_QX + WS_QKV_SZ)
#define WS_VX (WS_KX + WS_QKV_SZ)
#define WS_OX (WS_VX + WS_QKV_SZ)
#define WS_KM (WS_OX + WS_QKV_SZ)
#define WS_KM_SZ ((u64)NSEG * 8 * HD * 2)
#define WS_VM (WS_KM + WS_KM_SZ)
#define WS_UT (WS_VM + WS_KM_SZ)
#define WS_YT (WS_UT + (u64)SSM_L * SSM_D * 2)
#define WS_KD (WS_YT + (u64)SSM_L * SSM_D * 2)
#define WS_TOTAL (WS_KD + (u64)SSM_D * 3072)

#define SPIN_CAP 400000
#define TRY_CAP 20000
#define LAS __attribute__((address_space(3)))
#define LBAR() asm volatile("s_waitcnt lgkmcnt(0)\n\ts_barrier" ::: "memory")
__device__ __forceinline__ u32 max4u(u32x4 v) { return max(max(v[0], v[1]), max(v[2], v[3])); }
typedef float f32x2_t __attribute__((ext_vector_type(2)));
__device__ __forceinline__ u32 pack2(float a, float b) {
  f32x2_t v = {a, b};
  bf16x2_t r = __builtin_convertvector(v, bf16x2_t);
  return __builtin_bit_cast(u32, r);
}
__device__ __forceinline__ float rbf_hw(float a) { return __uint_as_float(pack2(a, a) & 0xffff0000u); }
__device__ __forceinline__ u32 nosent(u32 v) { return min(v, 0xFFFFFFFEu); }
template <int N> __device__ __forceinline__ void waitvm() { asm volatile("s_waitcnt vmcnt(%0)" ::"n"(N) : "memory"); }
typedef __attribute__((ext_vector_type(16))) float f32x16;
__device__ __forceinline__ u32 split_hl(float kv) {
  const u32 h = pack2(kv, kv) & 0xffffu;
  const float r1 = kv - __uint_as_float(h << 16);
  return h | (pack2(r1, r1) & 0xffff0000u);
}
__device__ __forceinline__ int kidx(int idx) {
  const int sh = idx < 256 ? 0 : (idx < 512 ? 1 : (idx < 1024 ? 2 : 3));
  const int q = sh ? ((idx + (1 << (sh - 1)) - 1 + ((idx >> sh) & 1)) >> sh) : idx;
  return q + 128 * sh;
}

#define FO_Q 0
#define FO_K ((int)WS_QKV_SZ)
#define FO_V ((int)(2 * WS_QKV_SZ))
#define FO_O ((int)(3 * WS_QKV_SZ))
#define FO_KM ((int)(4 * WS_QKV_SZ))
#define FO_VM ((int)(4 * WS_QKV_SZ + WS_KM_SZ))
__device__ __forceinline__ u32x4 wait_start(__amdgpu_buffer_rsrc_t rsx, int fb, u32 tag, bool slow, int xcd = 0) {
  u32x4 st;
  for (int it = 0; it < SPIN_CAP; ++it) {
    asm volatile("" ::: "memory");
    st = __builtin_amdgcn_raw_buffer_load_b128(rsx, 0, slow ? XSTA : XSTR(xcd), 16);
    if ((u32)__builtin_amdgcn_readfirstlane(st[3]) == tag) break;
    if (slow) asm volatile("s_sleep 20" ::: "memory"); else asm volatile("s_sleep 1" ::: "memory");
  }
  asm volatile("" ::: "memory");
  return st;
}
__device__ __forceinline__ int xprog(u32 v, u32 tag) { return v - tag <= 80u ? (int)(v - tag) : -1; }
__device__ __forceinline__ float xmax4(float m) {
  { auto r = __builtin_amdgcn_permlane16_swap(__float_as_uint(m), __float_as_uint(m), false, false); m = __builtin_fmaxf(__uint_as_float(r[0]), __uint_as_float(r[1])); }
  { auto r = __builtin_amdgcn_permlane32_swap(__float_as_uint(m), __float_as_uint(m), false, false); m = __builtin_fmaxf(__uint_as_float(r[0]), __uint_as_float(r[1])); }
  return m;
}
__device__ __forceinline__ float xsum4(float m) {
  { auto r = __builtin_amdgcn_permlane16_swap(__float_as_uint(m), __float_as_uint(m), false, false); m = __uint_as_float(r[0]) + __uint_as_float(r[1]); }
  { auto r = __builtin_amdgcn_permlane32_swap(__float_as_uint(m), __float_as_uint(m), false, false); m = __uint_as_float(r[0]) + __uint_as_float(r[1]); }
  return m;
}
#define MF16(a, b, c) __builtin_amdgcn_mfma_f32_16x16x32_bf16(__builtin_bit_cast(bf16x8, a), __builtin_bit_cast(bf16x8, b), c, 0, 0, 0)
__device__ __forceinline__ f32x4 rnd2(f32x4 v, float sc) {
  const u32 a = pack2(v[0], v[1]), b = pack2(v[2], v[3]);
  const u32 c = pack2(__uint_as_float(a << 16) * sc, __uint_as_float(a & 0xffff0000u) * sc), d = pack2(__uint_as_float(b << 16) * sc, __uint_as_float(b & 0xffff0000u) * sc);
  return (f32x4){__uint_as_float(c << 16), __uint_as_float(c & 0xffff0000u), __uint_as_float(d << 16), __uint_as_float(d & 0xffff0000u)};
}
struct XaOps { u32x4 q[2][4], k[2][4], v[8]; };
__device__ __forceinline__ void xa_load(XaOps& o, __amdgpu_buffer_rsrc_t rsf, int sh, int lane) {
#pragma unroll
  for (int i = 0; i < 8; ++i) {
    o.q[i >> 2][i & 3] = __builtin_amdgcn_raw_buffer_load_b128(rsf, lane * 16, FO_Q + (sh * 8 + i) * 1024, 16);
    o.k[i >> 2][i & 3] = __builtin_amdgcn_raw_buffer_load_b128(rsf, lane * 16, FO_K + (sh * 8 + i) * 1024, 16);
  }
#pragma unroll
  for (int dt = 0; dt < 8; ++dt) o.v[dt] = __builtin_amdgcn_raw_buffer_load_b128(rsf, lane * 16, FO_V + (sh * 8 + dt) * 1024, 16);
}
struct XaPre { f32x4 a[2], b[2]; float mx[2], tx[2]; };
__device__ __forceinline__ void xa_pre(XaPre& p, const XaOps& o, int lane) {
  const int lr = lane & 15, lg = lane >> 4;
  const float scale = 0.08838834764831845f, L2E = 1.4426950408889634f, NEG = -1000000000.0f;
  const f32x4 z = (f32x4){0, 0, 0, 0};
  f32x4 s00 = z, s10 = z, s11 = z;
#pragma unroll
  for (int ks = 0; ks < 4; ++ks) { s00 = MF16(o.k[0][ks], o.q[0][ks], s00); s10 = MF16(o.k[0][ks], o.q[1][ks], s10); s11 = MF16(o.k[1][ks], o.q[1][ks], s11); }
  s00 = rnd2(s00, scale); s10 = rnd2(s10, scale); s11 = rnd2(s11, scale);
  asm volatile("" : "+v"(s00), "+v"(s11));
  float m0 = NEG, m1 = -3.0e38f;
#pragma unroll
  for (int e = 0; e < 4; ++e) {
    const bool msk = 4 * lg + e > lr;
    s00[e] = msk ? NEG : s00[e]; s11[e] = msk ? NEG : s11[e];
    m0 = __builtin_fmaxf(m0, s00[e]); m1 = __builtin_fmaxf(m1, __builtin_fmaxf(s10[e], s11[e]));
  }
  m0 = xmax4(m0); m1 = xmax4(m1);
  const float em = __builtin_amdgcn_exp2f((NEG - m0) * L2E);
  float t0 = 4.f * em, t1 = 0.f;
#pragma unroll
  for (int e = 0; e < 4; ++e) {
    s00[e] = __builtin_amdgcn_exp2f((s00[e] - m0) * L2E); s10[e] = __builtin_amdgcn_exp2f((s10[e] - m1) * L2E); s11[e] = __builtin_amdgcn_exp2f((s11[e] - m1) * L2E);
    t0 += s00[e]; t1 += s10[e] + s11[e];
  }
  p.a[0] = s00; p.b[0] = (f32x4){em, em, em, em}; p.a[1] = s10; p.b[1] = s11; p.mx[0] = m0; p.mx[1] = m1; p.tx[0] = t0; p.tx[1] = t1;
}
__device__ __forceinline__ void xa_pw(u32x4& px, u32x2& pm, f32x4 sm, f32x4 sa, f32x4 sb, float mx, float tx, int lg) {
  const float scale = 0.08838834764831845f, L2E = 1.4426950408889634f;
  sm = rnd2(sm, scale);
  asm("" : "+v"(sm));
  float m = mx;
#pragma unroll
  for (int e = 0; e < 4; ++e) { sm[e] = lg >= 2 ? -INFINITY : sm[e]; m = __builtin_fmaxf(m, sm[e]); }
  m = xmax4(m);
  const float al = __builtin_amdgcn_exp2f((mx - m) * L2E);
  float ts = tx * al;
#pragma unroll
  for (int e = 0; e < 4; ++e) { sm[e] = __builtin_amdgcn_exp2f((sm[e] - m) * L2E); ts += sm[e]; }
  const float r = __builtin_amdgcn_rcpf(xsum4(ts)), ra = r * al;
  px = (u32x4){pack2(sa[0] * ra, sa[1] * ra), pack2(sa[2] * ra, sa[3] * ra), pack2(sb[0] * ra, sb[1] * ra), pack2(sb[2] * ra, sb[3] * ra)};
  pm = (u32x2){pack2(sm[0] * r, sm[1] * r), pack2(sm[2] * r, sm[3] * r)};
}
__device__ __forceinline__ void xa_pv(const u32x4 (&v)[8], const u32x2 (&vm)[8], u32x4 px, u32x2 pm, __amdgpu_buffer_rsrc_t rsf, int vo, int so) {
  f32x4 o[8];
#define LX(i) "v_mfma_f32_16x16x32_bf16 %" #i ", %[v" #i "], %[px], 0\n\t"
#define LM(i) "v_mfma_f32_16x16x16_bf16 %" #i ", %[m" #i "], %[pm], %" #i "\n\t"
  asm volatile("s_nop 1\n\t" LX(0) LX(1) LX(2) LX(3) LX(4) LX(5) LX(6) LX(7) LM(0) LM(1) LM(2) LM(3) LM(4) LM(5) LM(6) LM(7) "s_nop 7"
               : "=&v"(o[0]), "=&v"(o[1]), "=&v"(o[2]), "=&v"(o[3]), "=&v"(o[4]), "=&v"(o[5]), "=&v"(o[6]), "=&v"(o[7])
               : [v0] "v"(v[0]), [v1] "v"(v[1]), [v2] "v"(v[2]), [v3] "v"(v[3]), [v4] "v"(v[4]), [v5] "v"(v[5]), [v6] "v"(v[6]), [v7] "v"(v[7]), [px] "v"(px),
                 [m0] "v"(vm[0]), [m1] "v"(vm[1]), [m2] "v"(vm[2]), [m3] "v"(vm[3]), [m4] "v"(vm[4]), [m5] "v"(vm[5]), [m6] "v"(vm[6]), [m7] "v"(vm[7]), [pm] "v"(pm));
#pragma unroll
  for (int pr = 0; pr < 4; ++pr)
    __builtin_amdgcn_raw_buffer_store_b128((u32x4){pack2(o[2 * pr][0], o[2 * pr][1]), pack2(o[2 * pr][2], o[2 * pr][3]), pack2(o[2 * pr + 1][0], o[2 * pr + 1][1]), pack2(o[2 * pr + 1][2], o[2 * pr + 1][3])},
                                           rsf, vo, so + pr * 1024, 16);
}
__device__ __forceinline__ u32 xa_chk(const u32x4 (&km)[4], const u32x2 (&vm)[8]) {
  u32 ck = max(max(max4u(km[0]), max4u(km[1])), max(max4u(km[2]), max4u(km[3])));
#pragma unroll
  for (int i = 0; i < 8; ++i) ck = max(ck, max(vm[i][0], vm[i][1]));
  return ck;
}
#define XS0 48
#define XL_D 26
#define NAW 64
__device__ __forceinline__ void a_role(__amdgpu_buffer_rsrc_t rsx, __amdgpu_buffer_rsrc_t rsf, int sh, int lim, int wi, int done, int lane, int fb, u32 tag) {
  const int lr = lane & 15, lg = lane >> 4;
  const int okm = (lg * 8 + (lr & 7)) * 16, ovm = lg < 2 ? (lg * 16 + lr) * 8 : 0x7ffffff0;
  int rp = 0, xp = -1, spin = 0, pub = done, miss = 0, tp = 200;
  u32 tseen = 0, tq0 = 0, tq1 = 0, tq2 = 0, tq3 = 0; (void)tq0; (void)tq1; (void)tq2; (void)tq3;
#define XA_WAITX(s_)                                                                  \
  while (xp <= ((s_) >> 5)) {                                                                                          \
    asm volatile("" ::: "memory");                                                                                     \
    int pv = min(xprog(__builtin_amdgcn_raw_buffer_load_b32(rsx, lane * 128, PGO_X, 16), tag), xprog(__builtin_amdgcn_raw_buffer_load_b32(rsx, (64 + (lane & 31)) * 128, PGO_X, 16), tag)); \
    _Pragma("unroll") for (int o_ = 1; o_ < 64; o_ *= 2) pv = min(pv, __shfl_xor(pv, o_));                             \
    xp = __builtin_amdgcn_readfirstlane(pv);                                                                           \
    if (xp > ((s_) >> 5) || ++spin > SPIN_CAP) break;                                                                  \
    asm volatile("s_sleep 16" ::: "memory");                                                                           \
  }
#define XA_PUB() if (pub != done) { pub = done; asm volatile("s_nop 4\n\tbuffer_store_dword %0, %1, %2, %3 offen sc1" :: "v"(done), "v"(lane < 2 ? lane * 4 : 0x7ffffff0), "s"(rsx), "s"(PGO_A + wi * 128) : "memory"); }
#define XA_NOW ((u32)__builtin_amdgcn_s_memrealtime())
#define XA_SEEN(r_, l_) { const u32 n_ = XA_NOW; tseen = n_ - ((n_ - (r_ >> 16)) & 0x7fffu) + XA_LAG; tp = max(r_ & 1023, 1); rp = l_; }
#define XA_STEP(CUR, NXT) \
  { \
    const int seg = sh >> 5, nx = sh + NAW; \
    XA_WAITX(min(nx, lim - 1)) \
    u32x4 km[4]; u32x2 vm[8]; \
    int tries = (seg >= XS0 && tseen) ? 3 : 0; \
    while (true) {               \
      if (rp > seg) tries = 0; \
      else if (tries) { \
        const u32 due = tseen + (u32)(seg - rp) * tp + XL_D - XA_LAG; \
        for (int i_ = 0; i_ < 4000 && (int)(due - XA_NOW) > 0; ++i_) asm volatile("s_sleep 1" ::: "memory"); \
      } else while (true) { \
        int want = seg + 1; \
        if (tseen) { \
          const u32 due = tseen + (u32)(want - rp) * tp; \
          for (int i_ = 0; i_ < 4000 && (int)(due - XA_NOW) > 0; ++i_) asm volatile("s_sleep 2" ::: "memory"); \
          if (!miss) want = min(max(want, rp + max((int)(XA_NOW - tseen), 0) / tp), NSEG - 1); \
        } \
        asm volatile("" ::: "memory"); \
        const int r_ = __builtin_amdgcn_readfirstlane(__builtin_amdgcn_raw_buffer_load_b32(rsx, 0, pgr + want * 128, 16)); \
        if (r_ >= 0) { XA_SEEN(r_, want) miss = 0; break; } \
        if (++spin > SPIN_CAP) break; \
        if (miss++) asm volatile("s_sleep 20" ::: "memory"); \
      } \
      asm volatile("" ::: "memory"); \
      XA_DBG2 \
      _Pragma("unroll") for (int i = 0; i < 4; ++i) km[i] = __builtin_amdgcn_raw_buffer_load_b128(rsf, okm, FO_KM + (sh * 4 + i) * 512, 16); \
      _Pragma("unroll") for (int i = 0; i < 8; ++i) vm[i] = __builtin_amdgcn_raw_buffer_load_b64(rsf, ovm, FO_VM + (sh * 8 + i) * 256, 16); \
        \
      const int pk_ = min(rp + max((int)(XA_NOW - tseen), 0) / tp, NSEG - 1); \
      const u32 pv_ = __builtin_amdgcn_raw_buffer_load_b32(rsx, 0, pgr + pk_ * 128, 16); \
      asm volatile("" ::: "memory"); \
      __builtin_amdgcn_s_waitcnt(0x0F70);          \
      asm volatile("" ::: "memory"); \
      { const int r_ = __builtin_amdgcn_readfirstlane(pv_); if (r_ >= 0) XA_SEEN(r_, pk_) } \
      if (!tries || __ballot(xa_chk(km, vm) == SENT) == 0) break; \
      --tries; \
    } \
    if (nx < lim) xa_load(NXT, rsf, nx, lane); \
    XA_DBG3 \
    __builtin_amdgcn_sched_barrier(0); \
    f32x4 s0 = (f32x4){0, 0, 0, 0}, s1 = s0; \
    _Pragma("unroll") for (int ks = 0; ks < 4; ++ks) { s0 = MF16(km[ks], CUR.q[0][ks], s0); s1 = MF16(km[ks], CUR.q[1][ks], s1); } \
    u32x4 px0, px1; u32x2 pm0, pm1; \
    xa_pw(px0, pm0, s0, P.a[0], P.b[0], P.mx[0], P.tx[0], lg); \
    xa_pw(px1, pm1, s1, P.a[1], P.b[1], P.mx[1], P.tx[1], lg); \
    xa_pv(CUR.v, vm, px0, pm0, rsf, lane * 16, FO_O + (seg * 256 + (sh & 31) * 4) * 1024); \
    xa_pv(CUR.v, vm, px1, pm1, rsf, lane * 16, FO_O + (seg * 256 + 128 + (sh & 31) * 4) * 1024); \
    ++done; \
    XA_DBG XA_DBG5 \
    __builtin_amdgcn_sched_barrier(0); \
    sh = nx; \
    if (sh >= lim) break; \
    xa_pre(P, NXT, lane); \
    __builtin_amdgcn_s_waitcnt(0x0F70); \
    XA_PUB() \
  }
#define XA_LAST 22
#define XA_LAG 30
#define XA_DBG
#define XA_DBG2
#define XA_DBG3
#define XA_DBG4
#define XA_DBG5
  const int pgr = PGO_R + (__builtin_amdgcn_readfirstlane(wait_start(rsx, fb, tag, true)[2]) & 8192);
  XaOps A, B;
  XaPre P;
  XA_WAITX(sh)
  asm volatile("" ::: "memory");
  xa_load(A, rsf, sh, lane);
  xa_pre(P, A, lane);
  while (true) { XA_STEP(A, B) XA_STEP(B, A) }
  asm volatile("s_waitcnt vmcnt(0)" ::: "memory");
  XA_PUB()
}

#define GL_D 130
__device__ __forceinline__ void a_late(__amdgpu_buffer_rsrc_t rsx, __amdgpu_buffer_rsrc_t rsf, int t, int lane, int fb, u32 tag) {
  const int lr = lane & 15, lg = lane >> 4, rt = t & 1, sh = (NSEG - 4) * NH + (t >> 1), seg = sh >> 5;
  const float scale = 0.08838834764831845f, L2E = 1.4426950408889634f, NEG = -1000000000.0f;
  int xp = -1, spin = 0;
  const int pgr = PGO_R + (__builtin_amdgcn_readfirstlane(wait_start(rsx, fb, tag, true)[2]) & 8192);
  const int r40 = __builtin_amdgcn_readfirstlane(__builtin_amdgcn_raw_buffer_load_b32(rsx, 0, pgr + 40 * 128, 16));
  XA_WAITX(sh)
  asm volatile("" ::: "memory");
  u32x4 q[4], v[8];
  f32x4 sa = (f32x4){0, 0, 0, 0}, sb = sa;
  float mx = -3.0e38f, tx = 0.f;
  {
    u32x4 k0[4], k1[4];
#pragma unroll
    for (int i = 0; i < 4; ++i) {
      q[i] = __builtin_amdgcn_raw_buffer_load_b128(rsf, lane * 16, FO_Q + (sh * 8 + rt * 4 + i) * 1024, 16);
      k0[i] = __builtin_amdgcn_raw_buffer_load_b128(rsf, lane * 16, FO_K + (sh * 8 + i) * 1024, 16);
      k1[i] = __builtin_amdgcn_raw_buffer_load_b128(rsf, lane * 16, FO_K + (sh * 8 + 4 + i) * 1024, 16);
    }
#pragma unroll
    for (int dt = 0; dt < 8; ++dt) v[dt] = __builtin_amdgcn_raw_buffer_load_b128(rsf, lane * 16, FO_V + (sh * 8 + dt) * 1024, 16);
#pragma unroll
    for (int ks = 0; ks < 4; ++ks) { sa = MF16(k0[ks], q[ks], sa); sb = MF16(k1[ks], q[ks], sb); }
    sa = rnd2(sa, scale); sb = rnd2(sb, scale);
    asm volatile("" : "+v"(sa), "+v"(sb));
#pragma unroll
    for (int e = 0; e < 4; ++e) {
      sa[e] = 4 * lg + e > lr + 16 * rt ? NEG : sa[e]; sb[e] = 16 + 4 * lg + e > lr + 16 * rt ? NEG : sb[e];
      mx = __builtin_fmaxf(mx, __builtin_fmaxf(sa[e], sb[e]));
    }
    mx = xmax4(mx);
#pragma unroll
    for (int e = 0; e < 4; ++e) { sa[e] = __builtin_amdgcn_exp2f((sa[e] - mx) * L2E); sb[e] = __builtin_amdgcn_exp2f((sb[e] - mx) * L2E); tx += sa[e] + sb[e]; }
  }
  const int okm = (lg * 8 + (lr & 7)) * 16, ovm = lg < 2 ? (lg * 16 + lr) * 8 : 0x7ffffff0;
  const int ofl = seg + 1 < NSEG ? pgr + (seg + 1) * 128 : PGO_D + ((sh & 31) * 4 + (lane & 3)) * 128;
  const int owd = lane ? 0x7ffffff0 : PGO_A + (64 + (t >> 1)) * 128 + 4 * rt;
  u32 tq0 = 0, tq1 = 0, tq2 = 0; (void)tq0; (void)tq1; (void)tq2;
  int att = 0, seen = 1;
  while (true) {
    asm volatile("" : "+s"(att));
    if (att == 1) {
      u32 n = (u32)__builtin_amdgcn_s_memrealtime(), due = n;
      if (r40 >= 0) due = n - ((n - (r40 >> 16)) & 0x7fffu) + XA_LAG + (seg - 41) * (r40 & 1023);
      int r;
      while (true) {
        for (int i = 0; i < 4000 && (int)(due - (u32)__builtin_amdgcn_s_memrealtime()) > 0; ++i) asm volatile("s_sleep 2" ::: "memory");
        asm volatile("" ::: "memory");
        r = __builtin_amdgcn_readfirstlane(__builtin_amdgcn_raw_buffer_load_b32(rsx, 0, pgr + (seg - 1) * 128, 16));
        if (r >= 0 || ++spin > SPIN_CAP) break;
        asm volatile("s_sleep 10" ::: "memory");
      }
      n = (u32)__builtin_amdgcn_s_memrealtime();
      due = n - ((n - (r >> 16)) & 0x7fffu) + (r & 1023) + XL_D - (seg + 1 < NSEG ? 0 : XA_LAST);
      __builtin_amdgcn_raw_buffer_store_b32(((n + max((int)(due - n), 0) + GL_D) & ~1u) | 0x80000000u, rsx, owd, 0, 16);
      for (int i = 0; i < 4000 && (int)(due - (u32)__builtin_amdgcn_s_memrealtime()) > 0; ++i) asm volatile("s_sleep 1" ::: "memory");
      XA_DBG2
    } else if (att > 6) asm volatile("s_sleep 20" ::: "memory");
    const int dr = att ? 0 : 0x7ffffff0;
    asm volatile("" ::: "memory");
    u32x4 km[4]; u32x2 vm[8];
#pragma unroll
    for (int i = 0; i < 4; ++i) km[i] = __builtin_amdgcn_raw_buffer_load_b128(rsf, okm | dr, FO_KM + (sh * 4 + i) * 512, 16);
#pragma unroll
    for (int i = 0; i < 8; ++i) vm[i] = __builtin_amdgcn_raw_buffer_load_b64(rsf, ovm | dr, FO_VM + (sh * 8 + i) * 256, 16);
    const u32 fl = __builtin_amdgcn_raw_buffer_load_b32(rsx, att > 1 ? ofl : 0x7ffffff0, 0, 16);
    f32x4 sm = (f32x4){0, 0, 0, 0};
#pragma unroll
    for (int ks = 0; ks < 4; ++ks) sm = MF16(km[ks], q[ks], sm);
    const u32 ck = xa_chk(km, vm);
    u64 bad = __ballot(ck == SENT), nf = __ballot(fl == SENT);
    asm volatile("" : "+s"(bad), "+s"(nf));
    if (!seen && bad != 0) {
      seen = (att > 1 && nf == 0) || ++spin > SPIN_CAP;
      ++att;
      continue;
    }
    XA_DBG3
    u32x4 px; u32x2 pm;
    xa_pw(px, pm, sm, sa, sb, mx, tx, lg);
    xa_pv(v, vm, px, pm, rsf, (lane * 16) | dr, FO_O + (seg * 256 + rt * 128 + (sh & 31) * 4) * 1024);
    XA_DBG
    asm volatile("s_nop 4\n\ts_waitcnt vmcnt(0)\n\tbuffer_store_dword %0, %1, %2, 0 offen sc1" :: "v"(32), "v"(owd | dr), "s"(rsx) : "memory");
    if (att) break;
    att = 1; seen = 0;
  }
}

typedef __attribute__((ext_vector_type(32))) unsigned int u32x32;
typedef __attribute__((ext_vector_type(16))) unsigned int u32x16;
typedef __attribute__((ext_vector_type(8))) unsigned int u32x8;
#define STR2(x) #x
#define STR(x) STR2(x)
#define RNG(f, b, n) f "[" STR(b) ":" STR((b) + (n)-1) "]"
#define REG(b) "v[" STR(b) "]"
#define LR_RED 16384
#define LR_CTL LR_RED
#define LR_STG 32768
#define L_RSTAMP LR_STG
#define STAMPV(i, v)
#define EXF (NH * 8 * 1024)
#define EXK 65536
#define OOR 0x7ffffff0

__device__ __forceinline__ u32 lds_word(int addr) {
  u32 v;
  asm volatile("ds_read_b32 %0, %1\n\ts_waitcnt lgkmcnt(0)" : "=v"(v) : "v"(addr) : "memory");
  return __builtin_amdgcn_readfirstlane(v);
}
__device__ __forceinline__ int fresh_lane() {
  int l;
  asm volatile("v_mbcnt_lo_u32_b32 %0, -1, 0\n\tv_mbcnt_hi_u32_b32 %0, -1, %0" : "=v"(l));
  return l;
}
__device__ __forceinline__ u32 rt32() { return (u32)__builtin_amdgcn_s_memrealtime(); }
#define SYO(s) (XSYN + flip + (s) * 64)
#define TKS 0.85f
#define TLB 1
#define TLC 1
#define TLP 2
#define KG0 0
#define CLS0 0u
#define APC 5
#define ADL 12
#define CALN 2
#define TE1 3
#define TE2 1
#define TE3 1
#define XTH 4
#define TS3 42
#define TE0 45
#define TD0 170
#define TD 120
#define PHASE 1
#define XSK 0
#define CLKS(T, R, X) WLG(2) "s_sub_u32 s49, " T ", " R "\n\t" "s_cmp_le_i32 s49, " STR(3 * PHASE) "\n\t" "s_cbranch_scc1 " X "\n\t"
#define CLKH(OFF) WLG(0) "s_memrealtime s[72:73]\n\t" "s_add_u32 s50, s43, s44\n\t" "s_add_u32 s50, s50, " OFF "\n\t" "s_nop 1\n\t" "s_memrealtime s[74:75]\n\t"
#define CLKL(L, T)   L ":\n\t" "s_memrealtime s[76:77]\n\t" CLKS(T, "s72", L "x") "s_memrealtime s[72:73]\n\t" CLKS(T, "s74", L "x")     \
                     "s_memrealtime s[74:75]\n\t" CLKS(T, "s76", L "x") "s_branch " L "\n" \
                     L "x:\n\t" CLKP(L)
#define CLKW(L, OFF) CLKH(OFF) "s_nop 3\n" CLKL(L, "s50")
#define CLKP(L) "s_cmp_lt_i32 s49, 1\n\t" "s_cbranch_scc1 " L "y\n\t" "s_nop " STR(PHN) "\n\t" "s_cmp_lt_i32 s49, 2\n\t" "s_cbranch_scc1 " L "y\n\t" "s_nop " STR(PHN) "\n\t" \
                "s_cmp_lt_i32 s49, 3\n\t" "s_cbranch_scc1 " L "y\n\t" "s_nop " STR(PHN) "\n\t" "s_nop 0\n\t" "s_branch " L "y\n\t" "s_nop 0\n" L "y:\n\t"
#define PHN 3
#define REL(L) "s_barrier\n" L ":\n\t"
#define RELV(L) "s_cbranch_vccnz " L "\n\t" REL(L)
#define RELN(L) "s_cmp_eq_u32 %[wv], 0\n\t" "s_cbranch_scc1 " L "a\n\t" "s_cmp_eq_u32 %[wv], 1\n\t" "s_cbranch_scc1 " L "b\n\t" \
                "s_barrier\n\t" "s_nop " STR(PD2) "\n\t" "s_branch " L "\n" L "b:\n\t" "s_barrier\n\t" "s_branch " L "\n" L "a:\n\t" "s_barrier\n" L ":\n\t"
#define PD2 2
#define TS_(n) "s_memtime s[80+2*" #n ":81+2*" #n "]\n\t"
#define TSA(n)
#define TSB(n)
#define TSW(n)
#define TVM 12
template <int NTL, int KS>
__device__ __forceinline__ void tr_chunk(const u16* S, int lr, int lg, u32 (&r)[NTL][KS][8]) {
  const int q = lr >> 2, p = lr & 3;
#pragma unroll
  for (int t = 0; t < NTL; ++t)
#pragma unroll
    for (int i = 0; i < KS; ++i)
#pragma unroll
      for (int kb = 0; kb < 2; ++kb) {
        const u16* base = S + (64 * i + 32 * kb + 16 * (lg >> 1) + 4 * (lg & 1) + (q & 1) + 8 * (q >> 1)) * (16 * NTL) + 16 * t + 4 * p;
        const u32x2 t0 = __builtin_bit_cast(u32x2, __builtin_amdgcn_ds_read_tr16_b64_v4i16((LAS v4i16_t*)(base)));
        const u32x2 t1 = __builtin_bit_cast(u32x2, __builtin_amdgcn_ds_read_tr16_b64_v4i16((LAS v4i16_t*)(base + 2 * 16 * NTL)));
        r[t][i][4 * kb + 0] = t0[0]; r[t][i][4 * kb + 1] = t0[1]; r[t][i][4 * kb + 2] = t1[0]; r[t][i][4 * kb + 3] = t1[1];
      }
}
#define SMF(acc, a, bf, bi) "v_smfmac_f32_16x16x64_bf16 " RNG("v", acc, 4) ", " RNG("v", a, 4) ", " RNG(bf, bi, 8) ", v34\n\t"
#define ZER(r) "v_mov_b64 " RNG("v", r, 2) ", 0\n\t" "v_mov_b64 " RNG("v", (r) + 2, 2) ", 0\n\t"
#define GLD(r, o) "s_xor_b32 s49, s50, " STR(o) "\n\t" "buffer_load_dwordx4 " RNG("v", r, 4) ", v32, %[rs], s49 offen sc1\n\t"
#define GL1(r, o) "s_xor_b32 s49, s50, " STR(o) "\n\t" "buffer_load_dwordx4 " RNG("v", r, 4) ", v33, %[rs], s49 offen sc1\n\t"
#define GL2(r, o) GL1(r, 4096 + (o))
#define MXA0(r) "v_max3_u32 v63, " REG(r) ", " REG((r) + 1) ", " REG((r) + 1) "\n\t"
#define MXA(r) "v_max3_u32 v63, v63, " REG(r) ", " REG((r) + 1) "\n\t"
#define MXB(r) "v_max3_u32 v63, v63, " REG((r) + 2) ", " REG((r) + 3) "\n\t"
#define CMPB "v_cmp_eq_u32 vcc, s54, v63\n\t"
#define PSA(r) "v_pk_add_f32 " RNG("v", r, 2) ", " RNG("v", r, 2) ", " RNG("v", (r) + 2, 2) "\n\t"
#define PSW(r, o) "ds_write_addtid_b32 " REG(r) " offset:" STR(o) "\n\t"
#define RDQ(r, o) "ds_read_b128 " RNG("v", r, 4) ", v37 offset:" STR(o) "\n\t"
#define SQ(t, i) SMF(64 + 4 * (t), 96 + 4 * (i), "a", ((i) * 4 + (t)) * 8)
#define SV(t, i) SMF(24 + 4 * (t), 96 + 4 * (i), "a", 128 + ((i) * 2 + (t)) * 8)
#define SVP(t, i) SV(t, i)
#define SW(a, q, i) SMF((a) + 4 * (q), 96 + 4 * (i), "v", 128 + ((i) * 2 + (q)) * 8)
#define WVM(n) "s_waitcnt vmcnt(" STR(n) ")\n\t"
#define WLG(n) "s_waitcnt lgkmcnt(" STR(n) ")\n\t"
#define WLGF(n) WLG(n)
#define PACE 7
#define PC "s_nop " STR(PACE) "\n\t"
#define PC6
#define PC7
#define WBLK(a, n, i, M0) WVM((n) + 7 - (i)) SW(a, 0, i) M0(96 + 4 * (i)) SW(a, 1, i) MXB(96 + 4 * (i))
#define CL8(a, b, c, d, e, f, g, h) "v" #a, "v" #b, "v" #c, "v" #d, "v" #e, "v" #f, "v" #g, "v" #h
#define CL64 CL8(64, 65, 66, 67, 68, 69, 70, 71)
#define CL72 CL8(72, 73, 74, 75, 76, 77, 78, 79)
#define CL80 CL8(80, 81, 82, 83, 84, 85, 86, 87)
#define CL88 CL8(88, 89, 90, 91, 92, 93, 94, 95)
#define CL96 CL8(96, 97, 98, 99, 100, 101, 102, 103), CL8(104, 105, 106, 107, 108, 109, 110, 111)
#define CL112 CL8(112, 113, 114, 115, 116, 117, 118, 119)
#define CL120 CL8(120, 121, 122, 123, 124, 125, 126, 127)
#define PKA(d, a, b) "v_pk_add_f32 " RNG("v", d, 2) ", " RNG("v", a, 2) ", " RNG("v", b, 2) "\n\t"
#define CNT(o) STR(4096 + 64 * (o))
#define ARRIVE(o) "ds_add_u32 v45, v39 offset:" CNT(o) "\n\t"

#define EXTRA "v_readlane_b32 s69, v35, s45\n\t" "s_and_b32 s56, s58, 1023\n\t" "s_bfe_u32 s57, s58, 0xa000a\n\t" "s_lshr_b32 s55, s58, 20\n\t" "s_add_u32 s66, s56, s69\n\t" "s_add_u32 s67, s57, s69\n\t" "s_add_u32 s67, s67, s69\n\t"
#define TEX(s) ((s) == 0 ? TE0 : ((s) < 4 ? TE1 : ((s) < 12 ? TE2 : ((s) < TS3 ? TE3 : 0))))
#define R2BASE "s_bitcmp1_b32 s45, 0\n\t" "s_cselect_b32 s71, " STR(XR2S) ", 0\n\t" "s_add_u32 s71, s71, %[r2]\n\t"
#define X4B 0
#define X4C 0
#define STB "buffer_store_dwordx2 v[116:117], v11, %[rs], s36 offen sc1\n"
#define STC "buffer_store_dwordx2 v[80:81], v17, %[rs], s36 offen sc1\n\t"
#define R2TEXT                                                                                                     \
              TSB(0)                                                                                               \
              "buffer_load_dwordx4 v[64:67], v43, %[rs], s71 offen sc1\n\t"                                      \
              "buffer_load_dwordx4 v[68:71], v8, %[rs], s71 offen sc1\n\t"                                       \
              "buffer_load_dwordx4 v[76:79], v9, %[rs], s71 offen sc1\n\t"                                       \
              "buffer_load_dwordx4 v[60:63], v10, %[rs], s71 offen sc1\n\t"                                      \
              "buffer_store_dwordx4 v[72:75], v47, %[rf], s41 offen sc1\n\t"                                     \
              "s_cbranch_vccnz .Lvw%=\n\t" VPOST ARRIVE(12) "s_barrier\n" ".Lvw%=:\n\t"                             \
              "ds_read2_b32 v[96:97], v12 offset0:0 offset1:4\n\t" "ds_read2_b32 v[98:99], v12 offset0:32 offset1:36\n\t" \
              "ds_read2_b32 v[100:101], v13 offset0:0 offset1:4\n\t" "ds_read2_b32 v[102:103], v13 offset0:32 offset1:36\n\t" \
              "ds_read2_b32 v[104:105], v14 offset0:0 offset1:4\n\t" "ds_read2_b32 v[106:107], v14 offset0:32 offset1:36\n\t" \
              "ds_read2_b32 v[108:109], v15 offset0:0 offset1:4\n\t" "ds_read2_b32 v[110:111], v15 offset0:32 offset1:36\n\t" \
              "v_mov_b64 v[112:113], v[80:81]\n\t" "v_mov_b64 v[114:115], v[82:83]\n\t" "s_mov_b32 s65, 0\n\t"     \
              "v_mov_b32_dpp v112, v84 row_ror:8 row_mask:0xf bank_mask:0xc\n\t" "v_mov_b32_dpp v113, v85 row_ror:8 row_mask:0xf bank_mask:0xc\n\t" \
              "v_mov_b32_dpp v114, v86 row_ror:8 row_mask:0xf bank_mask:0xc\n\t" "v_mov_b32_dpp v115, v87 row_ror:8 row_mask:0xf bank_mask:0xc\n\t" \
              "v_pk_mul_f32 v[112:113], v[112:113], s[62:63] op_sel_hi:[1,0]\n\t" "v_pk_mul_f32 v[114:115], v[114:115], s[62:63] op_sel_hi:[1,0]\n\t" \
              WLG(0)                                                                                               \
              PKA(96, 96, 100) PKA(98, 98, 102) PKA(104, 104, 108) PKA(106, 106, 110) PKA(96, 96, 104) PKA(98, 98, 106) \
              "v_cvt_pk_bf16_f32 v96, v96, 0\n\t" "v_cvt_pk_bf16_f32 v97, v97, 0\n\t" "v_cvt_pk_bf16_f32 v98, v98, 0\n\t" "v_cvt_pk_bf16_f32 v99, v99, 0\n\t" \
              "v_accvgpr_write_b32 a194, v96\n\t" "v_accvgpr_write_b32 a202, v97\n\t" "v_accvgpr_write_b32 a198, v98\n\t" "v_accvgpr_write_b32 a206, v99\n\t" \
              TSB(1) WVM(4) TSB(2) \
              "v_permlane32_swap_b32 v64, v66\n\t" "v_permlane32_swap_b32 v65, v67\n\t" \
              "v_pk_add_f32 v[92:93], v[64:65], v[66:67]\n\t" \
              WVM(3) SFM(68) \
              "v_permlane16_swap_b32 v92, v93\n\t" \
              "v_add_f32 v94, v92, v93\n\t" \
              WVM(2) "v_mul_f32 v94, s62, v94\n\t" SFM(76) \
              "v_exp_f32 v92, v94\n\t" \
              WVM(1) TSB(3) SFM(60) \
                \
              ".Lex%=:\n\t" \
              "v_exp_f32 v88, v112\n\t" "v_exp_f32 v89, v113\n\t" "v_cvt_pk_bf16_f32 v58, v92, 0\n\t" "v_exp_f32 v90, v114\n\t" "v_exp_f32 v91, v115\n\t" \
              "v_cvt_pk_bf16_f32 v56, v88, v89\n\t" "v_cvt_pk_bf16_f32 v57, v90, v91\n\t" \
              TSB(4) ROR8(50, 58) ROR8(48, 56) \
              "v_mfma_f32_16x16x32_bf16 v[88:91], v[52:55], v[56:59], 0\n\t" \
              ROR8(49, 57) "s_nop 1\n\t" \
              "v_mfma_f32_16x16x32_bf16 v[116:119], a[192:195], v[56:59], 0\n\t" \
              "s_nop 2\n\t" \
              "v_mfma_f32_16x16x32_bf16 v[116:119], a[196:199], v[48:51], v[116:119]\n\t" \
              "v_add_f32_dpp v92, v88, v88 row_ror:8 row_mask:0xf bank_mask:0xf\n\t" "v_subrev_u32 v89, 0x33800000, v92\n\t" \
              "v_mfma_f32_16x16x32_bf16 v[124:127], a[204:207], v[56:59], 0\n\t" \
              "v_rcp_f32 v93, v92\n\t" \
              "v_mfma_f32_16x16x32_bf16 v[124:127], a[200:203], v[48:51], v[124:127]\n\t" \
              TSB(5) "v_cmp_le_u32 vcc, 0x20000000, v89\n\t" "s_nop 0\n\t" \
              "s_cbranch_vccnz .Lmx%=\n\t" \
              OTAIL STB TSB(6)
#define SFM(r) "v_pk_fma_f32 v[112:113], " RNG("v", r, 2) ", s[62:63], v[112:113] op_sel_hi:[1,0,1]\n\t" "v_pk_fma_f32 v[114:115], " RNG("v", (r) + 2, 2) ", s[62:63], v[114:115] op_sel_hi:[1,0,1]\n\t"
#define ROR8(d, s) "v_mov_b32_dpp " REG(d) ", " REG(s) " row_ror:8 row_mask:0xf bank_mask:0xf\n\t"
#define OTAIL "v_pk_mul_f32 v[116:117], v[116:117], v[92:93] op_sel:[0,1]\n\t" "v_pk_mul_f32 v[118:119], v[118:119], v[92:93] op_sel:[0,1]\n\t" \
              "v_cvt_pk_bf16_f32 v116, v116, v117\n\t" "v_cvt_pk_bf16_f32 v117, v118, v119\n\t" \
              "s_mov_b64 exec, s[78:79]\n\t" \
              "v_pk_mul_f32 v[124:125], v[124:125], v[92:93] op_sel:[0,1]\n\t" "v_pk_mul_f32 v[126:127], v[126:127], v[92:93] op_sel:[0,1]\n\t" \
              "v_cvt_pk_bf16_f32 v116, v124, v125\n\t" "v_cvt_pk_bf16_f32 v117, v126, v127\n\t" \
              "s_mov_b64 exec, -1\n\t"
#define MX3(a, b) "v_max3_u32 v95, v95, " REG(a) ", " REG(b) "\n\t"
#define R2MAX ".Lmx%=:\n\t" "v_max_u32 v95, v60, v61\n\t" MX3(62, 63) MX3(64, 65) MX3(66, 67) MX3(68, 69) MX3(70, 71) MX3(76, 77) MX3(78, 79) \
              "v_cmp_eq_u32 vcc, s54, v95\n\t" "s_cbranch_vccnz .Lfail%=\n\t" OTAIL \
              "s_cmp_lg_u32 s65, 0\n\t" "s_cbranch_scc1 .Lend%=\n\t" "s_mov_b32 s65, 1\n\t" \
              "v_max3_f32 v93, v112, v113, v94\n\t" \
              "v_max3_f32 v93, v93, v114, v115\n\t" \
              "s_nop 1\n\t" \
              "v_max_f32_dpp v92, v93, v93 row_ror:8 row_mask:0xf bank_mask:0xf\n\t" \
              "v_max_f32_dpp v88, v93, v93 row_ror:8 row_mask:0xf bank_mask:0xf\n\t" \
              "s_nop 1\n\t" \
              "v_permlane16_swap_b32 v92, v88\n\t" \
              "v_max_f32 v93, v92, v88\n\t" "v_max_f32 v92, v92, v88\n\t" \
              "s_nop 1\n\t" \
              "v_permlane32_swap_b32 v93, v92\n\t" \
              "v_max_f32 v93, v93, v92\n\t" \
              "v_sub_f32 v94, v94, v93\n\t" "v_sub_f32 v112, v112, v93\n\t" "v_exp_f32 v92, v94\n\t" "v_sub_f32 v113, v113, v93\n\t" "v_sub_f32 v114, v114, v93\n\t" "v_sub_f32 v115, v115, v93\n\t" \
              "s_branch .Lex%=\n"
#define PS1(r) "v_add_f32 " REG(r) ", " REG(r) ", " REG((r) + 2) "\n\t"
#define NOP(n) "s_nop " STR(n) "\n\t"
#define PCA 13
#define PDA 8
#define PDC 8
#define PDF 7
#define SLV 0
#define QKRED(X) RDQ(80, 12288) RDQ(84, 12304) X RDQ(88, 0) RDQ(92, 16) RDQ(112, 4096) RDQ(116, 4112) RDQ(120, 8192) RDQ(124, 8208) \
        WLG(4) TSA(4) PKA(80, 80, 88) PKA(82, 82, 90) PKA(84, 84, 92) PKA(86, 86, 94) \
        WLG(2) PKA(80, 80, 112) PKA(82, 82, 114) PKA(84, 84, 116) PKA(86, 86, 118) \
        WLG(0) PKA(80, 80, 120) PKA(82, 82, 122) PKA(84, 84, 124) PKA(86, 86, 126) \
        "v_cvt_pk_bf16_f32 v72, v80, v81\n\t" "v_cvt_pk_bf16_f32 v73, v82, v83\n\t" "v_cvt_pk_bf16_f32 v74, v84, v85\n\t" "v_cvt_pk_bf16_f32 v75, v86, v87\n\t" \
        NOP(1) TSA(5) \
        "v_mfma_f32_16x16x32_bf16 v[88:91], v[72:75], v[72:75], 0\n\t" NOP(2) \
        "v_mfma_f32_16x16x32_bf16 v[80:83], a[208:211], v[72:75], 0\n\t" NOP(2) \
        "v_mfma_f32_16x16x32_bf16 v[84:87], a[212:215], v[72:75], 0\n\t"
#define QKPUB "buffer_store_dwordx4 v[88:91], v41, %[rs], s71 offen sc1\n\t" NOP(0) \
        "buffer_store_dwordx4 v[80:83], v40, %[rs], s71 offen sc1\n\t" NOP(2) \
        "buffer_store_dwordx4 v[84:87], v40, %[rs], s71 offen offset:512 sc1\n\t"
#define MRED(W, X) "ds_read_b128 v[80:83], v46 offset:12288\n\t" W "ds_read_b128 v[84:87], v46\n\t" "ds_read_b128 v[88:91], v46 offset:4096\n\t" "ds_read_b128 v[92:95], v46 offset:8192\n\t" X TSW(4) \
        WLG(2) TSW(5) PKA(80, 80, 84) PKA(82, 82, 86) WLG(0) PKA(88, 88, 92) PKA(90, 90, 94) PKA(84, 80, 88) PKA(86, 82, 90) \
        "v_cvt_pk_bf16_f32 v80, v84, v85\n\t" "v_cvt_pk_bf16_f32 v81, v86, v87\n\t"
#define ORD(a, b) "v_cmpx_o_f32 s[50:51], " REG(a) ", " REG(b) "\n\t"
#define REDO(L) "s_cmp_lg_u64 exec, -1\n\t" "s_mov_b64 exec, -1\n\t" "s_cbranch_scc1 " L "\n" L "r:\n\t"
#define CWAIT(L, o, S) L ":\n\t" "ds_read_b32 v62, v38 offset:" CNT(o) "\n\t" WLG(0) "v_readfirstlane_b32 s49, v62\n\t" "s_cmp_lt_u32 s49, " S "\n\t" "s_cbranch_scc1 " L "\n\t"
#define VPOST PSA(24) PSA(28) PSW(24, 2560) PSW(25, 2832) PSW(28, 3200) PSW(29, 3472)
#define BARA "s_barrier\n\t"
#define BARC "s_barrier\n\t"
#define PZADR(SB, M) "s_add_u32 s49, s45, 1\n\t" "s_xor_b32 s62, s71, " STR(XR2S) "\n\t" "v_readlane_b32 s63, v19, s49\n\t" "s_mov_b32 s49, " SB "\n\t" \
        ".if !((" STR(M) ") & 1)\n\t" "s_mov_b32 s63, 0x7ff00000\n\t" ".endif\n\t" ".if !((" STR(M) ") & 2)\n\t" "s_mov_b32 s49, 0x7ff00000\n\t" ".endif\n\t" \
        ".if !((" STR(M) ") & 4)\n\t" "s_mov_b32 s62, 0x7ff00000\n\t" ".endif\n\t" "s_cmp_eq_u32 %[wv], 0\n\t" \
        "s_cselect_b32 s49, s49, s62\n\t" "s_cselect_b32 s63, s63, s62\n\t" "v_add_u32 v48, s49, v23\n\t" "s_mov_b32 exec_hi, 0\n\t" "v_add_u32 v48, s63, v23\n\t" "s_mov_b32 exec_hi, -1\n\t"
#define PZST "buffer_store_dwordx4 v[92:95], v48, %[rs], 0 offen sc1\n\t"
#define MA 0
#define MC 0
#define MB (7 - MA - MC)
#define PZD 1
#define PZB0 PZADR("s63", MB)
#define PZB PZST
#define PZC0
#define PZC ""
#define PZNC 0
#define PZA0
#define PZA
#define PZNA 0
#define LASTC(L) RELV(L)
#define LASTA(L) ""
#define PCL PCA
#define ABODY(W, X, F, n) TSA(0) GLD(96, 0) NOP(PCA) GLD(100, 1024) NOP(PCA) GLD(104, 2048) W GLD(108, 3072) "s_mov_b32 s47, 0\n\t" X \
        WVM((n) + 3) TSA(1) SQ(0, 0) SQ(1, 0) SQ(2, 0) SQ(3, 0) \
        WVM((n) + 2) SQ(0, 1) SQ(1, 1) SQ(2, 1) SQ(3, 1) \
        WVM((n) + 1) SQ(0, 2) MXA0(96) MXB(96) SQ(1, 2) MXA(100) MXB(100) SQ(2, 2) MXA(104) MXB(104) SQ(3, 2) \
        WVM(n) TSA(2) SQ(0, 3) MXA(108) MXB(108) CMPB SQ(1, 3) "s_add_u32 s48, s38, 4\n\t" NOP(0) NOP(0) SQ(2, 3) "s_cbranch_vccnz " F "\n\t" PS1(64) PS1(65) SQ(3, 3) PS1(68) PS1(69) \
        NOP(0) PSW(64, 0) PSW(65, 272) PSW(68, 640) PSW(69, 912) PSA(72) PSA(76) PSW(72, 1280) PSW(73, 1552) PSW(76, 1920) PSW(77, 2192)
#define CISSUE(X, Y) TSW(0) GL1(96, 0) PC GL1(100, 1024) PC GL1(104, 2048) PC GL1(108, 3072) PC GL2(112, 0) PC GL2(116, 1024) \
        "s_mov_b32 s48, s36\n\t" "s_mov_b32 s36, s37\n\t" "s_add_u32 s45, s45, 1\n\t" "s_add_u32 s38, s38, 4\n\t" "v_readlane_b32 s37, v19, s45\n\t" \
        "s_add_u32 s39, s39, " STR(EXF) "\n\t" "s_add_u32 s40, s40, " STR(EXF) "\n\t" \
        "s_add_u32 s41, s41, " STR(EXK) "\n\t" "s_add_u32 s42, s42, " STR(EXK) "\n\t" "s_add_u32 s43, s43, s55\n\t" PC6 \
        GL2(120, 2048) "s_mov_b32 s47, 2\n\t" "s_mul_i32 s70, s69, 3\n\t" R2BASE "s_add_u32 s43, s43, s70\n\t" NOP(2) PC7 X \
        GL2(124, 3072) Y
#define ZALL ZER(64) ZER(68) ZER(72) ZER(76) ZER(80) ZER(84) ZER(24) ZER(28)
#define SHRD "ds_read_b32 v62, v38\n\t"
#define SHUP WLG(0) "v_readfirstlane_b32 s49, v62\n\t" "s_max_u32 s44, s44, s49\n\t"
#define DPA(d, r, m) "v_add_f32_dpp " REG(d) ", " REG(r) ", " REG((r) + 2) " quad_perm:[0,1,2,3] row_mask:" m " bank_mask:0xf\n\t"
#define CBODY(m, X, n, F) CBODY_(80, m, X, n, F)
#define CBODY_(a, m, X, n, F) \
        WVM((m) + 7) TSW(1) WBLK(a, m, 0, MXA0) WBLK(a, m, 1, MXA) X WBLK(a, n, 2, MXA) WBLK(a, n, 3, MXA) WBLK(a, n, 4, MXA) WBLK(a, n, 5, MXA) WBLK(a, n, 6, MXA) \
        WVM(n) TSW(2) SW(a, 0, 7) MXA(124) MXB(124) CMPB SW(a, 1, 7) NOP(0) NOP(1) "s_cbranch_vccnz " F "\n\t" \
        DPA(88, a, "0x3") DPA(89, (a) + 1, "0x3") NOP(1) DPA(88, (a) + 4, "0xc") DPA(89, (a) + 5, "0xc") NOP(1) \
        "v_permlane32_swap_b32 v88, v89\n\t" "v_add_f32 v88, v88, v89\n\t" TSW(3) PSW(88, 3840)
#define FINEO
#define NEXTP "s_branch .LA%=\n"
#define NEXTN "s_branch .LNA%=\n"

__device__ __forceinline__ void clk_edge(u32& cyc, u32& tick) {
  const u32 t = rt32();
  for (int i = 0; i < 64; ++i) { tick = rt32(); if (tick != t) break; }
  cyc = (u32)__builtin_amdgcn_s_memtime();
}
__device__ __forceinline__ float clk_rate(u32 cyc0, u32 tk0) { u32 c, t; clk_edge(c, t); return (float)(c - cyc0) / (float)max(t - tk0, 1u); }
__device__ __forceinline__ void dma16(__amdgpu_buffer_rsrc_t rs, int voff, int soff, int sstep, int ldsa) {
#pragma unroll
  for (int i = 0; i < 16; ++i)
    asm volatile("s_mov_b32 m0, %0\n\ts_nop 3\n\tbuffer_load_dwordx4 %1, %2, %3 offen lds" :: "s"(ldsa + i * 1024), "v"(voff), "s"(rs), "s"(soff ^ (i * sstep)) : "memory");
}

#define KH(t) (192 + 8 * (t))
#define KMF(t, a, b) "v_mfma_f32_16x16x32_bf16 " RNG("v", 80 + 4 * (t), 4) ", " RNG("v", KH(t) + 4 * (a), 4) ", " RNG("v", KH(3) + 4 * (b), 4) ", " RNG("v", 80 + 4 * (t), 4) "\n\t"
#define KX(j) (92 + 4 * ((j) & 1))
#define KA(t, j) "v_pk_mul_f32 " RNG("v", KX(j), 2) ", " RNG("v", 64 + 4 * (j), 2) ", " RNG("v", 112 + ((t) & 2), 2) " op_sel:[0," STR((t) & 1) "] op_sel_hi:[1," STR((t) & 1) "]\n\t"
#define KF(j) "v_fract_f32 " REG(KX(j) + 1) ", " REG(KX(j) + 1) "\n\t"
#define KE(j, N) "v_exp_f32 " REG(KX(j)) ", " REG(KX(j)) "\n\t" "v_cos_f32 " REG(KX(j) + 2) ", " REG(KX(j) + 1) "\n\t" "v_sin_f32 " REG(KX(j) + 3) ", " N REG(KX(j) + 1) "\n\t"
#define KM(j) "v_pk_mul_f32 " RNG("v", KX(j) + 2, 2) ", " RNG("v", KX(j) + 2, 2) ", " RNG("v", KX(j), 2) " op_sel_hi:[1,0]\n\t"
#define KC(j) "v_pk_mul_f32 " RNG("v", KX(j), 2) ", " RNG("v", KX(j) + 2, 2) ", " RNG("v", 66 + 4 * (j), 2) " op_sel_hi:[1,0]\n\t" \
  "v_pk_fma_f32 " RNG("v", KX(j), 2) ", " RNG("v", KX(j) + 2, 2) ", " RNG("v", 66 + 4 * (j), 2) ", " RNG("v", KX(j), 2) " op_sel:[1,1,0] op_sel_hi:[0,1,1] neg_hi:[1,0,0]\n\t"
#define KD(t, j, r) "v_cvt_pk_bf16_f32 " REG(KH(t) + (j)) ", " REG(r) ", " REG((r) + 1) "\n\t" \
  "v_dot2c_f32_bf16 " REG(r) ", s47, " REG(KH(t) + (j)) "\n\t" "v_dot2c_f32_bf16 " REG((r) + 1) ", s48, " REG(KH(t) + (j)) "\n\t"
#define KL(t, j, r) "v_cvt_pk_bf16_f32 " REG(KH(t) + 4 + (j)) ", " REG(r) ", " REG((r) + 1) "\n\t"
#define KDM "s_add_u32 m0, m0, 1024\n\t" "s_add_u32 s43, s43, s42\n\t" "buffer_load_dwordx4 v110, s[36:39], s43 offen lds\n\t"
#define KP(j) "ds_read_b128 " RNG("v", 64 + 4 * (j), 4) ", v111 offset:" STR(16 * (j)) "\n\t"
#define KV(j, L, A) KE(j, "-") L KM(j) KC(j) A KD(3, j, KX(j))
#define KU(t, j, L, A) KE(j, "") L KM(j) A KD(t, j, KX(j) + 2)
#define KSUB                                                                                                        \
  KV(0, WLG(8) "s_add_u32 m0, m0, 1024\n\t", KA(3, 1)) KV(1, KL(3, 0, KX(0)), KA(3, 2))                              \
  "s_xor_b32 s43, s40, s41\n\t" "buffer_load_dwordx4 v110, s[36:39], s43 offen lds\n\t" KV(2, KL(3, 1, KX(1)), KA(3, 3)) \
  KV(3, KL(3, 2, KX(2)), KA(0, 0))                                                                                  \
  KU(0, 0, KL(3, 3, KX(3)), KA(0, 1)) KU(0, 1, KL(0, 0, KX(0) + 2), KA(0, 2)) KDM KU(0, 2, KL(0, 1, KX(1) + 2), KA(0, 3)) \
  KU(0, 3, KL(0, 2, KX(2) + 2), KA(1, 0)) KF(0)                                                                     \
  KU(1, 0, KL(0, 3, KX(3) + 2), KA(1, 1)) KF(1) KMF(0, 1, 0) KU(1, 1, KL(1, 0, KX(0) + 2), KA(1, 2)) KF(2) KDM      \
  KMF(0, 0, 1) KU(1, 2, KL(1, 1, KX(1) + 2), KA(1, 3)) KF(3) KMF(0, 0, 0) KU(1, 3, KL(1, 2, KX(2) + 2), KA(2, 0)) KF(0) KP(0) \
  KU(2, 0, KL(1, 3, KX(3) + 2), KA(2, 1)) KF(1) KP(1) KMF(1, 1, 0) KU(2, 1, KL(2, 0, KX(0) + 2), KA(2, 2)) KF(2) KP(2) KDM \
  KMF(1, 0, 1) KU(2, 2, KL(2, 1, KX(1) + 2), KA(2, 3)) KF(3) KP(3) KMF(1, 0, 0) KU(2, 3, KL(2, 2, KX(2) + 2), WLG(3) KA(3, 0)) \
  KMF(2, 0, 1) "v_add_u32 v111, 256, v111\n\t" "s_lshl2_add_u32 s41, s42, s41\n\t" KL(2, 3, KX(3) + 2)
#define KZERO ZER(80) ZER(84) ZER(88)
#define KCHUNK(PTR, VO, SO, ST, BUF, PF) "s_mov_b64 s[36:37], " PTR "\n\t" "s_mov_b32 m0, %[sc]\n\t" "v_mov_b32 v110, " VO "\n\t" SO "s_mov_b32 s41, 0\n\t" "s_mov_b32 s42, " STR(ST) "\n\t" \
  "buffer_load_dword " PF ", s[36:39], 0 offen lds\n\t" "s_add_u32 m0, %[sl], " STR((BUF) * 16384 - 1024) "\n\t"
#define KCALL "s_call_b64 s[44:45], .Lkg%=\n\t" WVM(16) KMF(2, 0, 0)
#define KTR(F, d, A, o) "ds_read_b64_tr_b16 " RNG(F, d, 2) ", " A " offset:" STR(o) "\n\t"
#define KTQ1(d, o) KTR("a", d, "%[aq]", o) KTR("a", (d) + 2, "%[aq]", (o) + 128) KTR("a", (d) + 4, "%[aq]", (o) + 2048) KTR("a", (d) + 6, "%[aq]", (o) + 2176)
#define KTQ(d, o) KCALL KTQ1(d, o) KMF(2, 1, 0) KTQ1((d) + 8, (o) + 32)
#define KTO1(d, o) KTR("v", d, "%[ao]", o) KTR("v", (d) + 2, "%[ao]", (o) + 64) KTR("v", (d) + 4, "%[ao]", (o) + 1024) KTR("v", (d) + 6, "%[ao]", (o) + 1088)
#define KTO(d, o) KCALL KTO1(d, o) KMF(2, 1, 0) KTO1((d) + 8, (o) + 2048)
#define KOUT(o) "s_add_u32 s43, %[ko], " STR(o) "\n\t"                                                              \
  "buffer_store_dwordx4 v[80:83], %[l16], %[rt], s43 offen sc1\n\t" "buffer_store_dwordx4 v[84:87], %[l16], %[rt], s43 offen offset:1024 sc1\n\t" "s_nop 3\n\t" \
  "buffer_store_dwordx4 v[88:91], %[l16], %[rt], s43 offen offset:2048 sc1\n\t"
struct SsmPtrs { const u16 *A_re, *A_im, *B_re, *B_im, *C_re, *C_im, *log_dt; };
__device__ __forceinline__ void r_role(unsigned char* lds, int b, const u16* __restrict__ mem0, const u16* __restrict__ Wq, const u16* __restrict__ Wk, const u16* __restrict__ Wv,
                                       const u16* __restrict__ Wo, int fb, u32 tag, u64 pc0, int clen, __amdgpu_buffer_rsrc_t rsx, __amdgpu_buffer_rsrc_t rsf,
                                       __amdgpu_buffer_rsrc_t rst, SsmPtrs sp, int tid, int lane, int w
                                       ) {
  const int lr = lane & 15, lg = lane >> 4;
  const int ldsb = (int)(size_t)(LAS unsigned char*)lds;
  const int idx = (lr & 2) ? 0xEEEE : 0x4444;
  u32 tcnt = 0; (void)tcnt;
  u32 cyc0 = 0, tk0 = 0;
  if (tid < 8) *(u32*)(lds + LR_CTL + tid * 4) = 0u;
  *(u32*)(lds + LR_CTL + 4096 + 256 + tid * 4) = 0u;
  const int xcd = b & 3, li = b >> 2;
  const int head = xcd * 8 + (li >> 2), ks = li & 3;
  const int vcu = head * 4 + ks;
  const int cg = (xcd >> 1) * 32 + li, rg = xcd & 1;
  const int rot1 = ((li >> 1) + w) & 3, rot3 = ((li >> 2) + 2 * w) & 7;
  const int pz1 = lane < 32 ? XR1A(lane >> 3, cg * 256 + rg * 128 + (lane & 7) * 16) : XR3A(lg & 1, vcu * 256 + lr * 16);
  const int pgo = PGO_X + head * 128;
  const int calo = XCAL + (li & 3) * 8192 + ((li >> 2) & 1) * 4096 + ((li >> 3) * 4 + xcd) * 128;
  u32 xp0;
  u32x32 wa[6], wo[4];
  {
    const __amdgpu_buffer_rsrc_t rwq = __builtin_amdgcn_make_buffer_rsrc((void*)Wq, 0, DM * HD * 2, 0x00020000);
    const int vq = (lane >> 2) * (HD * 2) + vcu * 64 + (lane & 3) * 16, sq = (256 * w + (rot1 << 6)) * (HD * 2);
    const int vo = (lane >> 1) * (DM * 2) + cg * 32 + (lane & 1) * 16, so = (1024 * w + (rot3 << 7)) * (DM * 2);
    const int sl = ldsb + LR_STG + w * 32768;
    const int ch0 = (b * 4 + w) * 2;
    u32x2 pin[6];
    u32 ldt;
    {
      const int po = (ch0 * SSM_N + 4 * lane) * 2;
      asm volatile("global_load_dwordx2 %0, %7, %9\n\tglobal_load_dwordx2 %1, %7, %10\n\tglobal_load_dwordx2 %2, %7, %11\n\tglobal_load_dwordx2 %3, %7, %12\n\t"
                   "global_load_dwordx2 %4, %7, %13\n\tglobal_load_dwordx2 %5, %7, %14\n\tglobal_load_ushort %6, %8, %15"
                   : "=&v"(pin[0]), "=&v"(pin[1]), "=&v"(pin[2]), "=&v"(pin[3]), "=&v"(pin[4]), "=&v"(pin[5]), "=&v"(ldt)
                   : "v"(po), "v"((ch0 + (lane >> 5)) * 2), "s"(sp.A_re), "s"(sp.A_im), "s"(sp.B_re), "s"(sp.B_im), "s"(sp.C_re), "s"(sp.C_im), "s"(sp.log_dt) : "memory");
    }
    u32x4 m0v;
    asm volatile("global_load_dwordx4 %0, %1, %2" : "=&v"(m0v) : "v"(((4 * rg + ((lane >> 1) & 3)) * DM + 16 * cg + 8 * (lane & 1)) * 2), "s"(mem0) : "memory");
    const u16* const wp[4] = {Wq, Wk, Wv, Wo};
    int pfo[5];
#pragma unroll
    for (int c = 0; c < 5; ++c) {
      const int lo_ = (int)(size_t)wp[c < 3 ? c : 3], s3 = (384 - lo_) & 1023, s63 = (8064 - lo_) & 8191;
      pfo[c] = c < 3 ? (lane < 16 ? (256 * w + li * 8 + (lane & 7)) * (HD * 2) + xcd * 2048 + (lane >> 3) * 1024 + s3
                                  : (lane < 24 && (s63 >> 11) == xcd ? (256 * w + li * 8 + (lane & 7)) * (HD * 2) + s63 : OOR))
                     : (lane < 16 ? (1024 * w + li * 16 + lane + (((c - 3) * 512) ^ ((((s3 >> 7) + 2 * w) & 4) << 7))) * (DM * 2) + (xcd >> 1) * 1024 + s3 : OOR);
    }
    const int scr = ldsb + LR_RED + 1024 + w * 256;
    asm volatile("s_mov_b32 m0, %0\n\ts_nop 3\n\tbuffer_load_dword %1, %2, 0 offen lds" :: "s"(scr), "v"(pfo[0]), "s"(rwq) : "memory");
#define KP0 16
#pragma unroll
    for (int i = 0; i < KP0; ++i)
      asm volatile("s_mov_b32 m0, %0\n\ts_nop 3\n\tbuffer_load_dwordx4 %1, %2, %3 offen lds" :: "s"(sl + i * 1024), "v"(vq), "s"(rwq), "s"(sq ^ (i * 16 * HD * 2)) : "memory");
    asm volatile("s_waitcnt vmcnt(1+" STR(KP0) ")" : "+v"(pin[0]), "+v"(pin[1]), "+v"(pin[2]), "+v"(pin[3]), "+v"(pin[4]), "+v"(pin[5]), "+v"(ldt), "+v"(m0v)::"memory");
    f32x4* prm = (f32x4*)(lds + w * 4096);
    {
      const float dt = rbf(expf(bf2f((u16)ldt)));
#pragma unroll
      for (int i = 0; i < 4; ++i) {
        float v[6];
#pragma unroll
        for (int j = 0; j < 6; ++j) v[j] = __uint_as_float((i & 1) ? (pin[j][i >> 1] & 0xffff0000u) : (pin[j][i >> 1] << 16));
        const float are = dt * v[0], aim = dt * v[1], br = dt * v[2], bi = dt * v[3];
        const float cr = v[4] * br - v[5] * bi, ci = v[4] * bi + v[5] * br;
        const float rev = aim * 0.15915494309189535f;
        prm[4 * lane + i] = (f32x4){are * 1.4426950408889634f, rev - __builtin_rintf(rev), cr, ci};
      }
    }
#pragma unroll
    for (int i = KP0; i < 16; ++i)
      asm volatile("s_mov_b32 m0, %0\n\ts_nop 3\n\tbuffer_load_dwordx4 %1, %2, %3 offen lds" :: "s"(sl + i * 1024), "v"(vq), "s"(rwq), "s"(sq ^ (i * 16 * HD * 2)) : "memory");
    const f32x4 lv = {(float)(128 * (lr >> 3) + (lr & 7)), (float)(lr < 8 ? 256 + 128 * (lr >> 2) + 2 * (lr & 3) : 512 + 128 * ((lr - 8) >> 1) + 4 * (lr & 1)),
                         (float)(lr <= 8 ? 1024 + 128 * lr : 0), (float)(8 * lr)};
    const int trow = 16 * (lg >> 1) + 4 * (lg & 1) + ((lr >> 2) & 1) + 8 * (lr >> 3);
    asm volatile(
        "s_nop 4\n\t" "s_mov_b32 s46, m0\n\t" "s_mov_b32 s38, " STR(DM * HD * 2) "\n\t" "s_mov_b32 s39, 0x20000\n\t" "s_mov_b32 s47, 0xbf80\n\t" "s_mov_b32 s48, 0xbf800000\n\t"
        WLG(0)
        "ds_read_b128 v[64:67], %[pa]\n\t" "ds_read_b128 v[68:71], %[pa] offset:16\n\t" "ds_read_b128 v[72:75], %[pa] offset:32\n\t" "ds_read_b128 v[76:79], %[pa] offset:48\n\t"
        "v_add_u32 v111, 256, %[pa]\n\t" KZERO
        KCHUNK("%[wk]", "%[vq]", "s_mov_b32 s40, %[sq]\n\t", 16 * HD * 2, 1, "%[p1]")
        WLG(0) KA(3, 0)
        KTQ(0, 0) KTQ(32, 4096) KTQ(64, 8192) KTQ(96, 12288)
        KCHUNK("%[wv]", "%[vq]", "", 16 * HD * 2, 0, "%[p2]")
        KTQ(16, 16384) KTQ(48, 16384 + 4096) KTQ(80, 16384 + 8192) KTQ(112, 16384 + 12288)
        KOUT(0)
        KCHUNK("%[wo]", "%[vo]", "s_mov_b32 s40, %[so]\n\t", 32 * DM * 2, 1, "%[p3]")
        KZERO
        KTQ(128, 0) KTQ(144, 4096) KTQ(160, 8192) KTQ(176, 12288)
        KCHUNK("%[wo]", "%[vo]", "s_xor_b32 s40, s40, " STR(512 * DM * 2) "\n\t", 32 * DM * 2, 0, "%[p4]")
        KTO(128, 16384) KTO(144, 16384 + 4096) KTO(160, 16384 + 8192) KTO(176, 16384 + 12288)
        KOUT(3072)
        "s_branch .Lke%=\n"
        ".Lkg%=:\n\t"
        KSUB
        "s_setpc_b64 s[44:45]\n"
        ".Lke%=:\n\t"
        WLG(0) "s_mov_b32 m0, s46"
        : "=&{a[0:31]}"(wa[0]), "=&{a[32:63]}"(wa[1]), "=&{a[64:95]}"(wa[2]), "=&{a[96:127]}"(wa[3]), "=&{a[128:159]}"(wa[4]), "=&{a[160:191]}"(wa[5]),
          "=&{v[128:159]}"(wo[0]), "=&{v[160:191]}"(wo[1])
        : "{v[112:115]}"(lv), [pa] "v"(ldsb + w * 4096 + lg * 64), [vq] "v"(vq), [vo] "v"(vo),
          [p1] "v"(pfo[1]), [p2] "v"(pfo[2]), [p3] "v"(pfo[3]), [p4] "v"(pfo[4]), [aq] "v"(sl + trow * 64 + (lr & 3) * 8), [ao] "v"(sl + trow * 32 + (lr & 3) * 8), [l16] "v"(lane * 16),
          [wk] "s"(Wk), [wv] "s"(Wv), [wo] "s"(Wo), [sq] "s"(sq), [so] "s"(so), [sl] "s"(sl), [sc] "s"(scr), [rt] "s"(rst), [ko] "s"(2 * SSM_L * SSM_D * 2 + ch0 * 3072)
        : CL64, CL72, CL80, CL88, CL96, CL8(192, 193, 194, 195, 196, 197, 198, 199), CL8(200, 201, 202, 203, 204, 205, 206, 207), CL8(208, 209, 210, 211, 212, 213, 214, 215),
          CL8(216, 217, 218, 219, 220, 221, 222, 223), "s36", "s37", "s38", "s39", "s40", "s41", "s42", "s43", "s44", "s45", "s46", "s47", "s48", "scc", "memory");
    {
      const u32x4 pz = (u32x4){SENT, SENT, SENT, SENT};
#pragma unroll
      for (int i = XS0; i < NSEG; i += 4) __builtin_amdgcn_raw_buffer_store_b128(pz, rsf, (lane < 32 ? FO_KM : FO_VM - 512) + vcu * 512 + lane * 16, (i + w) * EXK, 16);
      if (w < 2) __builtin_amdgcn_raw_buffer_store_b128(pz, rsf, lane * 16, FO_O + (NSEG - 1) * 262144 + (vcu * 2 + w) * 1024, 16);
      if (w < 3) {
        u32x4 v = pz;
        if (w == 0 && lane < 32) v = (u32x4){nosent(m0v[0]), nosent(m0v[1]), nosent(m0v[2]), nosent(m0v[3])};
#pragma unroll
        for (int hf = 0; hf < 2; ++hf) {
          __builtin_amdgcn_raw_buffer_store_b128(v, rsx, pz1, XSB(w) + hf * 8192, 16);
          __builtin_amdgcn_raw_buffer_store_b128(pz, rsx, XR2(vcu) + lane * 16, XR2B + (w & 1) * XR2S + hf * 8192, 16);
          __builtin_amdgcn_raw_buffer_store_b128(pz, rsx, lane < 16 ? XR2(vcu) + 1024 + lane * 16 : OOR, XR2B + (w & 1) * XR2S + hf * 8192, 16);
        }
      } else {
        __builtin_amdgcn_raw_buffer_store_b128(pz, rsx, lane < 8 ? lane * 16 : OOR, calo, 16);
        if (b < 32) {
          __builtin_amdgcn_raw_buffer_store_b128(pz, rsx, lane * 16, XSYN + b * 2048, 16);
          __builtin_amdgcn_raw_buffer_store_b128(pz, rsx, lane * 16, XSYN + b * 2048 + 1024, 16);
        } else if (b >= 48 && b < 48 + 41) {
          __builtin_amdgcn_raw_buffer_store_b128(pz, rsx, lane * 16, PGO_D + (b - 48) * 1024, 16);
        }
      }
      asm volatile("" ::: "memory");
    }
    xp0 = __builtin_amdgcn_raw_buffer_load_b32(rsx, (lane & 1) * 4096, pgo, 16);
    clk_edge(cyc0, tk0);
    asm volatile("s_waitcnt vmcnt(0)\n\ts_barrier" : "+v"(xp0)::"memory");
#define CLKF() clk_rate(cyc0, tk0)
    if (w == 0) {
      const int l0 = lane ? OOR : 0;
      const u64 c0 = __builtin_amdgcn_s_memtime();
      u32 v;
      asm volatile("s_waitcnt lgkmcnt(0)\n\tbuffer_load_dword %0, off, %1, %2 sc1\n\tbuffer_store_dword %3, %4, %1, %5 offen sc1\n\ts_waitcnt vmcnt(1)"
                   : "=&v"(v) : "s"(rsx), "s"(calo), "v"(tag), "v"(l0), "s"(PGO_Y + b * 128), "s"(c0) : "memory");
      const u32 lat = (u32)(__builtin_amdgcn_s_memtime() - c0);
      const float f = CLKF();
      typedef __attribute__((ext_vector_type(3))) unsigned int u32x3;
      __builtin_amdgcn_raw_buffer_store_b96((u32x3){__float_as_uint(f), lat, tag}, rsx, l0, PGO_Y + b * 128 + 4, 16);
      asm volatile("" ::: "memory");
    }
    {
      u32 r[1][8][8];
      tr_chunk<1, 8>((const u16*)(lds + LR_STG + w * 32768), lr, lg, r);
#pragma unroll
      for (int i = 0; i < 8; ++i)
#pragma unroll
        for (int e = 0; e < 8; ++e) { const int o = (8 + i) * 8 + e; wo[o >> 5][o & 31] = r[0][i][e]; }
      asm volatile("s_waitcnt lgkmcnt(0)" ::: "memory");
    }
  }
  const int hf = (lr >> 1) & 1;
  const int voff1 = lg * 256 + (lr >> 3) * 128 + (((lr >> 2) & 1) * 2 + (lr & 1)) * 32 + hf * 16;
  const int voff3 = (lr >> 3) * 512 + (lg >> 1) * 256 + (((lr >> 2) & 1) * 2 + (lr & 1)) * 16 + (lg & 1) * 128 + hf * 64;
  const int ard = ldsb + (2 * (lr >> 3) + (lg >> 1)) * 640 + (lr & 1) * 272 + ((lr >> 1) & 3) * 64 + (lg & 1) * 32;
  const int ctl = ldsb + LR_CTL, one = w == 0 ? 2 : (w == 3 ? 4 : 1);
  const int r8 = lane & 7, hk = (lane >> 3) & 1;
#define COA 1
#define COB 1
  const int st01 = lr < 8 ? XR2(vcu) + lr * 16 + lg * 128 : OOR, st2 = (lr < 8 && lg >= 2) ? XR2(vcu) + 1024 + lr * 16 + (lg - 2) * 128 : OOR;
  const int ld01 = hk * 512 + r8 * 16 + lg * 128, ld2 = XR2(head * 4 + lg) + 1024 + r8 * 16 + hk * 128;
  int sto = XR3A(r8 >> 2, vcu * 256 + (r8 & 3) * 16 + lg * 64 + hk * 8);
  const int stk = lr >= 8 ? (lg * 8 + (lr - 8)) * 16 : OOR;
  const int lo = X4C ? 2 * (lr & 7) + (lr >> 3) : lr;
  const int arw = ldsb + LR_CTL + lane * 4, arr = ldsb + 3840 + ((lo >> 3) + 2 * ((lo >> 2) & 1)) * 64 + (lo & 3) * 16;
  const int st1 = XR1A(lg, cg * 256 + rg * 128 + lo * 8);
  const int avw = 0;
  const int avc = ldsb + 2560 + (lr >> 3) * 640 + (8 * ((lr >> 2) & 1) + (lr & 3)) * 4;
  const int avr = avc + (lg & 1) * 272 + (lg >> 1) * 64, avf = avc + (lg & 1) * 128 + (lg >> 1) * 16;
  u32x16 kc = {(u32)voff1, (u32)voff3, (u32)idx, (u32)TEX(lane), (u32)(lane < 16 ? XR2(vcu) + 1024 + lane * 16 : OOR), (u32)ard, (u32)ctl, (u32)one, (u32)st01, (u32)st2, (u32)(XR2(vcu) + lane * 16), (u32)ld2, (u32)(lane * 16), (u32)arw, (u32)arr, (u32)stk};
  int o1c = XR1A(xcd, w * 4096) + rot1 * 1024, o3c = XR3A(rg, w * 8192) + rot3 * 1024;
  const int fok = FO_K + (head * 8 + ks) * 1024, fov = FO_V + (head * 8 + 2 * ks) * 1024;
#define XPROG(v) min(xprog(__builtin_amdgcn_readlane(v, 0), tag), xprog(__builtin_amdgcn_readlane(v, 1), tag))
  int xp = w == 3 ? XPROG(xp0) : 1000;
#define XWAIT(n) for (int it_ = 0; it_ < SPIN_CAP && xp < (n); ++it_) {                                               \
    if (it_) asm volatile("s_sleep 4" ::: "memory");                                                                  \
    const u32 v_ = __builtin_amdgcn_raw_buffer_load_b32(rsx, (lane & 1) * 4096, pgo, 16); xp = XPROG(v_); asm volatile("" ::: "memory"); }
  XWAIT(1)
  u32x16 fv;
  u32x8 fk;
  {
    const int lo_ = w == 3 ? lane * 16 : OOR;
    const u32x4 v0 = __builtin_amdgcn_raw_buffer_load_b128(rsf, lo_, fov, 16), v1 = __builtin_amdgcn_raw_buffer_load_b128(rsf, lo_, fov + 1024, 16);
    const u32x4 k0 = __builtin_amdgcn_raw_buffer_load_b128(rsf, lo_, fok, 16), k1 = __builtin_amdgcn_raw_buffer_load_b128(rsf, lo_, fok + 4096, 16);
    fv = (u32x16){v0[0], v0[1], 0u, 0u, v0[2], v0[3], 0u, 0u, v1[0], v1[1], 0u, 0u, v1[2], v1[3], 0u, 0u};
    fk = (u32x8){k0[0], k0[1], k0[2], k0[3], k1[0], k1[1], k1[2], k1[3]};
    asm volatile("" : "+{a[192:207]}"(fv), "+{a[208:215]}"(fk));
  }
#define XPN 1
#define CALM 40
  XWAIT(2)
#pragma unroll
  for (int i = 0; i < 3; ++i) *(u32x4*)(lds + w * 4096 + i * 1024 + lane * 16) = (u32x4){SENT, SENT, SENT, SENT};
  {
    u64 pc; asm volatile("s_getpc_b64 %0" : "=s"(pc));
    const u32 cv = __builtin_amdgcn_raw_buffer_load_b32(__builtin_amdgcn_make_buffer_rsrc((void*)pc0, 0, clen, 0x00020000), (int)(pc - pc0) + tid * 64, 0, 0);
    asm volatile("" :: "v"(cv));
  }
  if (w == 3) { const u32 v_ = __builtin_amdgcn_raw_buffer_load_b32(rsx, (lane & 1) * 4096, pgo, 16); xp = max(xp, XPROG(v_)); }
  u32x4 st;
  if (w == 1) {
    if (b == 0) {
      u32 tt = 0, best = 0;
      bool xok = false;
      const int sto_ = lane < 5 ? (lane ? XSTR(lane - 1) : XSTA) : OOR;
      for (int it = 0; it < SPIN_CAP; ++it) {
        asm volatile("" ::: "memory");
        const u32x4 r0 = __builtin_amdgcn_raw_buffer_load_b128(rsx, lane * 128, PGO_Y, 16), r1 = __builtin_amdgcn_raw_buffer_load_b128(rsx, lane * 128 + 8192, PGO_Y, 16);
        if (!xok) xok = __ballot(xprog(__builtin_amdgcn_raw_buffer_load_b32(rsx, lane * 128, PGO_X, 16), tag) < XPN) == 0;
        const bool c0 = r0[3] == tag, c1 = r1[3] == tag;
        if (tt == 0 && __popcll(__ballot(!c0 || !c1)) <= CALM) {
          float fm = (c0 ? __uint_as_float(r0[1]) : 0.f) + (c1 ? __uint_as_float(r1[1]) : 0.f), fn = (c0 ? 1.f : 0.f) + (c1 ? 1.f : 0.f);
#pragma unroll
          for (int o = 4; o < 64; o *= 2) { fm += __shfl_xor(fm, o); fn += __shfl_xor(fn, o); }
          fm = fn > 0.f ? fm / fn : 100.f;
          const float f0 = __uint_as_float(__builtin_amdgcn_readfirstlane(__float_as_uint(fm)));
          fm = fminf(fm, __shfl_xor(fm, 1)); fm = fminf(fm, __shfl_xor(fm, 2));
          const float f_ = CLKF() * fminf(fmaxf(fm / f0, 0.8f), 1.f);
          const float k_ = 1.f + TKS * (fminf(fmaxf(23.9f / f_, 1.f), 1.5f) - 1.f);
          const int nb_ = (int)(TDB * k_ + 0.5f), nc_ = nb_ + (int)((TDC - TDB) * k_ + 0.5f), np_ = nc_ + (int)((TP - TDC) * k_ + 0.5f);
          u32 lat = min(c0 ? r0[2] : 65535u, c1 ? r1[2] : 65535u);
          { auto r = __builtin_amdgcn_permlane16_swap(lat, lat, false, false); lat = min(r[0], r[1]); }
          { auto r = __builtin_amdgcn_permlane32_swap(lat, lat, false, false); lat = min(r[0], r[1]); }
          u32 mm[4];
#pragma unroll
          for (int k = 0; k < 4; ++k)
            mm[k] = __builtin_amdgcn_readlane(lat, 4 * k) + __builtin_amdgcn_readlane(lat, 4 * k + 2) + __builtin_amdgcn_readlane(lat, 4 * (k ^ 2) + 1) + __builtin_amdgcn_readlane(lat, 4 * (k ^ 2) + 3);
          const u32 hb = mm[0] + mm[2] <= mm[1] + mm[3] ? 0u : 1u;
          best = (hb << 13) | ((hb ? mm[1] <= mm[3] : mm[0] <= mm[2]) ? CLS0 : 16384u - CLS0);
          if (__ballot(lat == 65535u) == 0) tt = __builtin_amdgcn_readfirstlane(nb_ | (nc_ << 10) | (np_ << 20));
        }
        if (__ballot(r0[0] != tag || r1[0] != tag) == 0 && xok && tt) break;
      }
      st = (u32x4){rt32() + TD0, tt, best, tag};
      __builtin_amdgcn_raw_buffer_store_b128(st, rsx, sto_, 0, 16);
    } else st = wait_start(rsx, fb, tag, false, xcd);
    if (lane == 0) *(u32x4*)(lds + LR_CTL + 32) = st;
  }
  LBAR();
  st = *(const u32x4*)(lds + LR_CTL + 32);
  const int flip = __builtin_amdgcn_readfirstlane(st[2]) & 8192, cls = __builtin_amdgcn_readfirstlane(st[2]) & 16384;
  o1c ^= cls; o3c ^= cls; sto ^= cls; kc[4] ^= cls; kc[8] ^= cls; kc[9] ^= cls; kc[10] ^= cls; kc[11] ^= cls;
  { const u32 now = rt32(); if (st[0] - now + 100000u > 200000u) st[0] = now; }
  u32x16 kd = {(u32)((ld01 + XR2(vcu ^ 1)) ^ cls), (u32)((ld01 + XR2(vcu ^ 2)) ^ cls), (u32)((ld01 + XR2(vcu ^ 3)) ^ cls), (u32)(X4B && hk ? OOR : sto), (u32)avr, (u32)(avr + 4096), (u32)(avr + 8192),
               (u32)(avr + 12288), (u32)avw, (u32)(X4C && hk ? OOR : st1 ^ cls), (u32)((lane & 1) * 4096), (u32)(XSB(lane + 1) + flip), (u32)(lane * 8), (u32)avf,
               (u32)(b == 0 && lane == 0 ? XSYN + flip : OOR), (u32)(w == 0 ? pz1 ^ cls : (w == 1 ? kc[10] : kc[4]))};
  u32x16 sa = {(u32)flip, (u32)(XSB(1) + flip), 0u, (u32)fok, (u32)fov, (u32)(FO_KM + (head * 4 + ks) * 512),
               (u32)(FO_VM + (head * 8 + 2 * ks) * 256), st[0], 0u, 0u, (u32)SYO(1), 0u, 0u, (u32)TRY_CAP, 0u, 0u};
  u32x8 sc = {(u32)o1c, (u32)o3c, SENT, 0u, 0u, 0u, st[1], (u32)pgo};
#define TUNEO
#define KCNT
#define KHST
#define KTRC
#define TRCI
#define PREP 16
#define GAP 12
#define RETA(n, L) "s_cmp_eq_u32 s49, " #n "\n\t" "s_cmov_b32 s48, .Lpc%=-" L "\n\t"
  u32x32 keep;
  u32x2 ov;
  asm volatile("" : "={v[60:91]}"(keep));
  while (true) {
    sa[14] = xp;
#pragma unroll
    for (int i = 0; i < 16; ++i) sa[i] = __builtin_amdgcn_readfirstlane(sa[i]);
#pragma unroll
    for (int i = 0; i < 8; ++i) sc[i] = __builtin_amdgcn_readfirstlane(sc[i]);
    asm volatile(
        "s_nop 4\n\t" "s_mov_b32 s60, m0\n\t"
        "s_mov_b32 m0, %[mb]\n\t" "s_mov_b32 s64, s50\n\t" "s_mov_b32 s98, s49\n\t" "s_mov_b32 s68, 0\n\t"
        EXTRA R2BASE
        "s_cmp_lg_u32 %[wv], 3\n\t" "s_cbranch_scc1 .LN%=\n\t"
        "s_mov_b32 s78, 0xff00ff00\n\t" "s_mov_b32 s79, 0xff00ff00\n\t" "s_mov_b32 s62, 0x3e0293ee\n\t"
        "v_mov_b32 v52, 0x3f803f80\n\t" "v_mov_b32 v53, 0x3f803f80\n\t" "v_mov_b32 v54, 0x3f803f80\n\t" "v_mov_b32 v55, 0x3f803f80\n\t" "v_mov_b32 v51, 0\n\t" "v_mov_b32 v59, 0\n\t"
        "v_mov_b32 v60, %[tg]\n\t" "v_mov_b32 v61, -1\n\t" "s_mov_b32 s63, %[dm]\n\t"
        "s_branch .Lent%=\n"
        ".LA%=:\n\t"
        CLKW(".Lta%=", "0") REL(".Lua%=")
        "s_add_u32 s50, s36, s52\n\t" NOP(PDA)
        ABODY(NOP(PCL) LASTA(".Lwa%="), WVM(5) "v_readlane_b32 s50, v60, 0\n\t" "v_readlane_b32 s51, v60, 1\n\t"
              "s_sub_u32 s50, s50, %[tg]\n\t" "s_sub_u32 s51, s51, %[tg]\n\t" "s_min_u32 s63, s50, s51\n\t" "s_max_u32 s50, s50, s51\n\t" "s_cmp_le_u32 s50, 80\n\t"
              "s_cselect_b32 s50, s63, 0\n\t" "v_readfirstlane_b32 s49, v61\n\t" "s_max_i32 s64, s64, s50\n\t"
              "s_cmp_lg_u32 s49, -1\n\t" "s_cmov_b32 s58, s49\n\t" EXTRA "v_mov_b32 v62, s58\n\t" "ds_write_b32 v38, v62 offset:24\n\t", ".Lfail%=", 0) TSA(3)
        QKRED(BARA) ORD(88, 89) ORD(90, 91) QKPUB TSA(6) TSB(7) REDO(".Lqk%=")
        SVP(0, 0) SHRD SVP(1, 0) SVP(0, 1) SVP(1, 1) SVP(0, 2) SVP(1, 2) WLG(0) "v_readfirstlane_b32 s49, v62\n\t" SVP(0, 3) SVP(1, 3) "s_max_u32 s44, s44, s49\n\t" "s_cmp_eq_u32 s45, 63\n\t" TSA(7)
        "s_cbranch_scc1 .Llast%=\n"
        ".LB%=:\n\t"
        CLKW(".Ltb%=", "s66")
        "s_mov_b32 s47, 1\n\t"
        R2TEXT
        ".LC%=:\n\t"
        ZALL
        "s_mov_b32 s47, 5\n\t" "s_sub_i32 s63, s64, s45\n\t" "s_cmp_lt_i32 s63, 2\n\t" "s_cbranch_scc1 .Lend%=\n\t"
        "s_cmp_lt_i32 s63, " STR(XTH) "\n\t" "s_cselect_b32 s63, s59, %[dm]\n\t"
        CLKW(".Ltc%=", "s67") REL(".Luc%=")
        "s_add_u32 s50, s36, s53\n\t" NOP(PDC)
        CISSUE(LASTC(".Lwc%="), "")
        "buffer_load_dword v61, off, %[rs], s46 sc1\n\t" "s_add_u32 s46, s46, 64\n\t"
        "s_add_u32 s49, s39, 4096\n\t" NOP(PDF)
        "buffer_load_dwordx4 a[208:211], v44, %[rf], s39 offen sc1\n\t" "buffer_load_dwordx4 a[212:215], v44, %[rf], s49 offen sc1\n\t"
        "buffer_load_dwordx2 a[192:193], v44, %[rf], s40 offen sc1\n\t" "buffer_load_dwordx2 a[196:197], v44, %[rf], s40 offen offset:8 sc1\n\t"
        "buffer_load_dwordx2 a[200:201], v44, %[rf], s40 offen offset:1024 sc1\n\t" "buffer_load_dwordx2 a[204:205], v44, %[rf], s40 offen offset:1032 sc1\n\t"
#define NXC 8
        "buffer_load_dword v60, v18, %[rs], s63 offen sc1\n\t"
        CBODY(NXC, "", NXC, ".Lfail%=")
        MRED(BARC, "") ORD(84, 85) ORD(86, 87) STC TSW(6) REDO(".Lmr%=")
        SHRD SHUP TSW(7)
        NEXTP
        CWAIT(".Lqk%=", 8, "s48") QKRED("") NOP(1) QKPUB "s_branch .Lqk%=r\n"
        CWAIT(".Lmr%=", 16, "s38") WVM(0) MRED("", "") "v_min_u32 v80, -2, v80\n\t" "v_min_u32 v81, -2, v81\n\t" STC
        "s_branch .Lmr%=r\n"
        ".Llast%=:\n\t"
        "buffer_store_dwordx4 v[72:75], v47, %[rf], s41 offen sc1\n\t" "s_mov_b32 s47, 3\n\t" NOP(4) VPOST ARRIVE(12) "s_barrier\n\t"
        "s_branch .Lend%=\n"
        R2MAX
        ".Lnorm%=:\n\t"
        "s_cmp_lg_u32 %[wv], 3\n\t" "s_cbranch_scc1 .LNA%=\n\t" "s_cmp_eq_u32 s47, 0\n\t" "s_cbranch_scc1 .LA%=\n\t" "s_branch .LC%=\n"
        ".LN%=:\n\t"
        "v_mov_b64 v[92:93], -1\n\t" "v_mov_b64 v[94:95], -1\n\t" "s_mov_b32 s65, s44\n\t" "s_mov_b32 s78, -100\n\t" "s_mov_b32 s79, 4\n\t"
        "s_branch .Lent%=\n"
        ".LNA%=:\n\t" PZA0
        "s_add_u32 s50, s36, s52\n\t"
        RELN(".Lna%=")
        ABODY(NOP(PCA), LASTA(".Lxa%=") PZA, ".Lfail%=", PZNA) BARA ARRIVE(8) TSA(3)
        SV(0, 0) SV(1, 0) SV(0, 1) SV(1, 1) SV(0, 2) SV(1, 2) SV(0, 3) SV(1, 3) ZER(64) ZER(68) ZER(72) ZER(76)
        VPOST TSA(7) ZER(80) ZER(84) ZER(24) ZER(28)
        "v_add_u32 v88, 4096, v21\n\t" "v_add_u32 v89, 8192, v21\n\t" "v_add_u32 v90, 12288, v21\n\t"
        PZB0
        "s_lshl_b32 s61, s45, 7\n\t" "s_add_u32 s61, s61, " STR(PGO_R - XSYN) "\n\t" "s_cmp_lg_u32 %[wv], 0\n\t"
        WLG(0) "s_barrier\n\t" PZB
        PSW(92, 0) PSW(92, 272) PSW(92, 640) PSW(92, 912) PSW(92, 1280) PSW(92, 1552) PSW(92, 1920) PSW(92, 2192) PSW(92, 3840)
        "ds_read2_b32 v[50:51], v38 offset0:0 offset1:6\n\t"
        "s_cbranch_scc1 .LNB%=\n\t"
        "s_memrealtime s[72:73]\n"
        ".Lv0%=:\n\t"
        "ds_read_b32 v62, v38 offset:" CNT(12) "\n\t"
        "ds_read2_b32 v[112:113], v21 offset0:0 offset1:68\n\t" "ds_read2_b32 v[114:115], v21 offset0:16 offset1:84\n\t"
        "ds_read2_b32 v[116:117], v88 offset0:0 offset1:68\n\t" "ds_read2_b32 v[118:119], v88 offset0:16 offset1:84\n\t"
        "ds_read2_b32 v[120:121], v89 offset0:0 offset1:68\n\t" "ds_read2_b32 v[122:123], v89 offset0:16 offset1:84\n\t"
        "ds_read2_b32 v[124:125], v90 offset0:0 offset1:68\n\t" "ds_read2_b32 v[126:127], v90 offset0:16 offset1:84\n\t"
        WLG(0) "v_cmp_gt_u32 vcc, s48, v62\n\t" "s_cbranch_vccnz .Lv0%=\n\t"
        "s_add_u32 s50, s72, " STR(TVM) "\n\t" "s_lshl_b32 s50, s50, 17\n\t" "s_lshr_b32 s50, s50, 1\n\t"
        PKA(112, 112, 116) PKA(114, 114, 118) PKA(120, 120, 124) PKA(122, 122, 126) PKA(112, 112, 120) PKA(114, 114, 122)
        "v_lshrrev_b32 v91, 20, v51\n\t" "v_readfirstlane_b32 s49, v50\n\t"
        "v_cvt_pk_bf16_f32 v112, v112, v113\n\t" "v_cvt_pk_bf16_f32 v113, v114, v115\n\t" "v_or_b32 v114, s50, v91\n\t"
        "s_cmp_eq_u32 s45, 63\n\t" "s_cbranch_scc1 .Lv2%=\n\t"
        "s_sleep " STR(SLV) "\n"
        ".Lv2%=:\n\t"
        "buffer_store_dwordx2 v[112:113], v20, %[rf], s42 offen sc1\n\t"
        "buffer_store_dword v114, v22, %[rs], s61 offen sc1\n\t"
        "s_cmp_eq_u32 s49, s65\n\t" "s_cbranch_scc1 .LNB%=\n\t"
        "s_mov_b32 s65, s49\n\t" KHST "s_sub_u32 s49, s45, s78\n\t" "s_mov_b32 s78, s45\n\t"
        "s_cmp_gt_i32 s49, " STR(GAP) "\n\t" "s_cbranch_scc1 .LNB%=\n\t" "s_cmp_lt_u32 s45, s79\n\t" "s_cbranch_scc1 .LNB%=\n\t"
        "s_add_u32 s79, s45, 3\n\t" "s_lshl_b32 s49, s45, 6\n\t" "v_add_u32 v50, " STR(TLB + ((TLB + TLC) << 10) + ((TLB + TLC + TLP) << 20)) ", v51\n\t"
        "buffer_store_dword v50, v22, %[rs], s49 offen offset:128 sc1\n"
        ".LNB%=:\n\t"
        WLG(0) "v_readfirstlane_b32 s58, v51\n\t" EXTRA
        "s_mov_b32 s47, 3\n\t"
        "s_cmp_eq_u32 s45, 63\n\t"
        "s_cbranch_scc1 .Lend%=\n"
        ".LNC%=:\n\t" PZC0
        "s_add_u32 s50, s36, s53\n\t"
        WLG(0) RELN(".Lnc%=")
        CISSUE("", LASTC(".Lxc%="))
        CBODY(0, PZC, PZNC, ".Lfail%=") BARC
        ARRIVE(16) TSW(4)
        NEXTN
        ".Lfail%=:\n\t" KCNT
        "s_cmp_lg_u32 s47, 2\n\t" "s_cbranch_scc1 .Lnu%=\n\t"
        "s_mov_b32 s37, s36\n\t" "s_mov_b32 s36, s48\n\t" "s_sub_u32 s45, s45, 1\n\t" "s_sub_u32 s38, s38, 4\n\t" "s_sub_u32 s39, s39, " STR(EXF) "\n\t" "s_sub_u32 s40, s40, " STR(EXF) "\n\t"
        "s_sub_u32 s41, s41, " STR(EXK) "\n\t" "s_sub_u32 s42, s42, " STR(EXK) "\n\t" "s_sub_u32 s43, s43, s55\n\t" "s_sub_u32 s43, s43, s70\n\t" "s_sub_u32 s46, s46, 64\n"
        ".Lnu%=:\n\t"
        "s_mov_b32 s51, 1\n\t" "s_nop 7\n\t" WVM(0) "s_sub_u32 s98, s98, 1\n\t" "s_cbranch_scc1 .Lcap%=\n"
        ".Lent%=:\n\t"
        "s_cmp_eq_u32 s47, 1\n\t" "s_cselect_b32 s99, s66, s67\n\t" "s_cbranch_scc1 .Lnz%=\n\t" ZALL ".Lnz%=:\n\t"
        "s_cmp_eq_u32 s47, 0\n\t" "s_cselect_b32 s99, 0, s99\n\t" "s_cselect_b32 s50, s52, s53\n\t" "s_add_u32 s99, s99, s43\n\t" "s_add_u32 s50, s50, s36\n\t"
        SHRD "s_memrealtime s[72:73]\n\t"
        "s_cmp_eq_u32 %[wv], 3\n\t" "s_cselect_b32 s49, 0, 3\n\t" "s_add_u32 s49, s49, s47\n\t" "s_cmp_lg_u32 s51, 0\n\t" "s_cselect_b32 s49, s49, 7\n\t" "s_cselect_b64 vcc, -1, 0\n\t"
        "s_cselect_b32 s51, 0, 14\n\t" "s_mov_b32 s48, .Lpc%=-.Lnorm%=\n\t"
        RETA(0, ".Lua%=") RETA(1, ".Ltb%=y") RETA(2, ".Luc%=") RETA(3, ".Lna%=") RETA(5, ".Lnc%=")
        "s_getpc_b64 s[96:97]\n" ".Lpc%=:\n\t" "s_sub_u32 s96, s96, s48\n\t" "s_subb_u32 s97, s97, 0\n\t"
        SHUP "s_sub_u32 s49, s99, s72\n\t" "s_add_u32 s49, s49, s44\n"
        ".Lbump%=:\n\t"
        "s_cmp_ge_i32 s49, " STR(PREP) "\n\t" "s_cbranch_scc1 .Lok%=\n\t" "s_add_u32 s44, s44, %[tr]\n\t" "s_add_u32 s49, s49, %[tr]\n\t" "s_branch .Lbump%=\n"
        ".Lok%=:\n\t"
        "s_mov_b64 exec, 1\n\t" "v_mov_b32 v62, s44\n\t" "ds_max_u32 v38, v62\n\t" KTRC "s_mov_b64 exec, -1\n\t"
        "s_add_u32 s99, s99, s44\n\t" "s_sub_u32 s99, s99, s51\n\t"
        WLG(0) "s_memrealtime s[72:73]\n\t" "s_nop 3\n\t" "s_memrealtime s[74:75]\n\t" "s_nop 3\n"
        CLKL(".Lth%=", "s99")
        "s_setpc_b64 s[96:97]\n"
        ".Lcap%=:\n\t" "s_add_u32 s47, s47, 8\n"
        ".Lend%=:\n\t"
        "s_waitcnt vmcnt(0) lgkmcnt(0)\n\t"
        "s_mov_b32 s50, s64\n\t" "s_mov_b32 s49, s98\n\t" "s_mov_b32 s51, s68\n\t" "s_mov_b32 m0, s60"
        : "+{s[36:51]}"(sa), "+{s[52:59]}"(sc), "+{v[60:91]}"(keep), "=&{v[116:117]}"(ov), "+{a[192:207]}"(fv), "+{a[208:215]}"(fk), "+{v[8:23]}"(kd) FINEO TUNEO
        : "{v[32:47]}"(kc), [rs] "s"(rsx), [rf] "s"(rsf), [dm] "s"(__builtin_amdgcn_readfirstlane(PGO_Y + b * 128)), [tg] "s"(__builtin_amdgcn_readfirstlane(tag)), [r2] "s"(__builtin_amdgcn_readfirstlane(XR2B + flip)), [mb] "s"(__builtin_amdgcn_readfirstlane(ldsb + w * 4096)), [wv] "s"(__builtin_amdgcn_readfirstlane(w)), [tr] "s"(TR) TRCI,
          "{a[0:31]}"(wa[0]), "{a[32:63]}"(wa[1]), "{a[64:95]}"(wa[2]), "{a[96:127]}"(wa[3]), "{a[128:159]}"(wa[4]), "{a[160:191]}"(wa[5]),
          "{v[128:159]}"(wo[0]), "{v[160:191]}"(wo[1]), "{v[192:223]}"(wo[2]), "{v[224:255]}"(wo[3])
        : CL8(24, 25, 26, 27, 28, 29, 30, 31), "v48", "v49", "v50", "v51", CL8(52, 53, 54, 55, 56, 57, 58, 59), "v92", "v93", "v94", "v95", CL96, "v112", "v113", "v114", "v115", "v118", "v119", CL120,
          "s60", "s61", "s62", "s63", "s64", "s65", "s66", "s67", "s68", "s69", "s70", "s71", "s72", "s73", "s74", "s75", "s76", "s77", "s78", "s79", "s96", "s97", "s98", "s99", "vcc", "scc", "memory");
    const int code = __builtin_amdgcn_readfirstlane(sa[11]);
    xp = __builtin_amdgcn_readfirstlane(sa[14]);
    tcnt += sa[15];
    sa[15] = 0;
    if (code == 3) break;
    const int seg = __builtin_amdgcn_readfirstlane(sa[9]);
    if (code == 5) { XWAIT(seg + 2) xp = max(xp, seg + 2); sa[11] = 2; }
    else if (code == 1) { __builtin_amdgcn_raw_buffer_store_b64((u32x2){nosent(ov[0]), nosent(ov[1])}, rsx, sto, sa[0], 16); sa[11] = 2; }
    else { sc[2] += 1; sa[13] = 0; sa[11] = code - 8; sa[15] = 1; }
  }
  LBAR();
  if (tid == 0) __builtin_amdgcn_raw_buffer_store_b32(1u, rsx, 0, PGO_D + vcu * 128, 16);
  if (b == 0 && w == 1) {
    for (int it = 0; it < SPIN_CAP; ++it) {
      asm volatile("" ::: "memory");
      if (__ballot(__builtin_amdgcn_raw_buffer_load_b32(rsx, lane * 128, PGO_S, 16) != tag || __builtin_amdgcn_raw_buffer_load_b32(rsx, lane * 128 + 8192, PGO_S, 16) != tag) == 0) break;
    }
    if (lane == 0) __builtin_amdgcn_raw_buffer_store_b32(tag, rsx, 0, XEPO, 16);
  }
}

#define XSTAT 3
#define XVM 8
#define XP0 -1
#define XSC " sc1"
#define XPITCH 1056
#define XSLOT (32 * XPITCH)
#define XW(HB, t) "a[" STR(HB) "+8*xk+4*" #t ":" STR(HB) "+8*xk+4*" #t "+3]"
#define XF(rt) "v[96+8*(xk&3)+4*" #rt ":96+8*(xk&3)+4*" #rt "+3]"
#define XFN(rt) "v[96+8*((xk+3)&3)+4*" #rt ":96+8*((xk+3)&3)+4*" #rt "+3]"
#define XA(AC, o) "v[" STR(AC) "+" #o ":" STR(AC) "+" #o "+3]"
#define XM1(d, a, b, FIRST) ".if (xk == 0) && " STR(FIRST) "\n\tv_mfma_f32_16x16x32_bf16 " d ", " a ", " b ", 0\n\t.else\n\tv_mfma_f32_16x16x32_bf16 " d ", " a ", " b ", " d "\n\t.endif\n\t"
#define XMF_QK(i, HB, AC, F) XMF_QK##i(HB, AC, F)
#define XMF_QK0(HB, AC, F) XM1(XA(AC, 0), XW(HB, 0), XF(0), F)
#define XMF_QK1(HB, AC, F) XM1(XA(AC, 4), XW(HB, 1), XF(0), F)
#define XMF_QK2(HB, AC, F) XM1(XA(AC, 8), XW(HB, 0), XF(1), F)
#define XMF_QK3(HB, AC, F) XM1(XA(AC, 12), XW(HB, 1), XF(1), F)
#define XMF_V(i, HB, AC, F) XMF_V##i(HB, AC, F)
#define XMF_V0(HB, AC, F) XM1(XA(AC, 0), XF(0), XW(HB, 0), F)
#define XMF_V1(HB, AC, F) XM1(XA(AC, 8), XF(0), XW(HB, 1), F)
#define XMF_V2(HB, AC, F) XM1(XA(AC, 4), XF(1), XW(HB, 0), F)
#define XMF_V3(HB, AC, F) XM1(XA(AC, 12), XF(1), XW(HB, 1), F)
#define XRD(rt, CUR, NXT) ".if xk < 13\n\tds_read_b128 " XFN(rt) ", " CUR " offset:64*(xk+3)+" STR(rt * 16 * XPITCH) "\n\t.else\n\t" \
                          "ds_read_b128 " XFN(rt) ", " NXT " offset:64*(xk-13)+" STR(rt * 16 * XPITCH) "\n\t.endif\n\t"
#define XCV(EPI, PV, i) ".if " STR(EPI) " && (xk >= 1) && (xk < 3)\n\tv_cvt_pk_bf16_f32 v[128+4*(xk-1)+" #i "], v[" STR(PV) "+8*(xk-1)+2*" #i "], v[" STR(PV) "+8*(xk-1)+2*" #i "+1]\n\t.endif\n\t"
#define XHALF(MF, HB, AC, PV, EPI, CUR, NXT, DSLOT, GOFF)                                                   \
  ".set xk, 0\n\t"                                                                                          \
  ".rept 16\n\t"                                                                                            \
  "s_waitcnt lgkmcnt(4)\n\t"                                                                                \
  MF(0, HB, AC, EPI) XRD(0, CUR, NXT) XCV(EPI, PV, 0)                                                       \
  ".if (xk >= 5) && (xk < 13)\n\t"                                                                          \
  "s_add_u32 m0, m0, " STR(XPITCH) "\n\t"                                                                   \
  ".endif\n\t"                                                                                              \
  MF(1, HB, AC, EPI) XRD(1, CUR, NXT) XCV(EPI, PV, 1)                                                       \
  ".if (xk >= 5) && (xk < 13)\n\t"                                                                          \
  "s_add_u32 %[dg], %[dg], 2048\n\t"                                                                        \
  ".endif\n\t"                                                                                              \
  MF(2, HB, AC, EPI) XCV(EPI, PV, 2)                                                                        \
  ".if (xk >= 4) && (xk < 12)\n\t"                                                                          \
  "buffer_load_dwordx4 %[l16], %[rx], %[dg] offen lds\n\t"                                                  \
  ".endif\n\t"                                                                                              \
  ".if xk == 0\n\t"                                                                                         \
  "s_add_u32 m0, %[dl], " STR((DSLOT) * XSLOT) "\n\t" "s_add_u32 %[dg], %[gb], " STR(GOFF) "\n\t"           \
  ".endif\n\t"                                                                                              \
  ".if " STR(EPI) " && (xk == " STR(XSTAT) ")\n\t"                                                                       \
  "buffer_store_dwordx4 v[128:131], v137, %[rf], %[sg] offen" XSC "\n\t"                                       \
  ".endif\n\t"                                                                                              \
  ".if " STR(EPI) " && (xk == 13)\n\t"                                                                      \
  "v_mov_b32 v137, %[l16]\n\t" "v_mov_b32 v138, %[lp2]\n\t"                                                 \
  ".endif\n\t"                                                                                              \
  ".if " STR(EPI) " && (xk == 14)\n\t"                                                                      \
  "s_add_u32 %[sg], %[sg], " STR(NH * 8 * 1024) "\n\t"                                                      \
  "v_add_u32 v136, 1, v136\n\t"                                                                             \
  ".endif\n\t"                                                                                              \
  ".if !" STR(EPI) " && (xk == 1)\n\t"                                                                      \
  "buffer_store_dword v136, %[fo], %[rp], 0 offen sc1\n\t"                                                  \
  ".endif\n\t"                                                                                              \
  MF(3, HB, AC, EPI) XCV(EPI, PV, 3)                                                                        \
  ".if xk == 0\n\t"                                                                                         \
  "s_waitcnt vmcnt(" STR(XVM) ")\n\t" "s_barrier\n\t"                                                               \
  ".endif\n\t"                                                                                              \
  ".if xk == 2\n\t"                                                                                         \
  "s_min_u32 %[dg], %[dg], " STR(S_LEN * DM * 2 - 16384 + 1024) "\n\t"                                      \
  ".endif\n\t"                                                                                              \
  ".if " STR(EPI) " && (xk == " STR(XSTAT) ")\n\t"                                                                       \
  "buffer_store_dwordx4 v[132:135], v138, %[rf], %[sg] offen" XSC "\n\t"                                       \
  ".endif\n\t"                                                                                              \
  ".set xk, xk+1\n\t"                                                                                       \
  ".endr\n\t"
#define XLOOP(MF)                                                                                           \
  "s_mov_b32 %[kp], m0\n\t"                                                                                 \
  "v_mov_b32 v136, %[p0]\n\t" "v_mov_b32 v137, " STR(OOR) "\n\t" "v_mov_b32 v138, " STR(OOR) "\n\t"                                                                                \
  "ds_read_b128 v[96:99], %[r0]\n\tds_read_b128 v[100:103], %[r0] offset:" STR(16 * XPITCH) "\n\t"          \
  "ds_read_b128 v[104:107], %[r0] offset:64\n\tds_read_b128 v[108:111], %[r0] offset:64+" STR(16 * XPITCH) "\n\t"   \
  "ds_read_b128 v[112:115], %[r0] offset:128\n\tds_read_b128 v[116:119], %[r0] offset:128+" STR(16 * XPITCH) "\n"   \
  ".Lx%=:\n\t"                                                                                              \
  XHALF(MF, 0, 64, 80, 1, "%[r0]", "%[r1]", 3, 66560) XHALF(MF, 128, 64, 80, 0, "%[r1]", "%[r2]", 0, 131072) \
  XHALF(MF, 0, 80, 64, 1, "%[r2]", "%[r3]", 1, 132096) XHALF(MF, 128, 80, 64, 0, "%[r3]", "%[r0]", 2, 196608) \
  "s_add_u32 %[gb], %[gb], 131072\n\t"                                                                      \
  "s_sub_u32 %[cnt], %[cnt], 1\n\t"                                                                         \
  "s_cmp_lg_u32 %[cnt], 0\n\t"                                                                              \
  "s_cbranch_scc1 .Lx%=\n\t"                                                                                \
  "s_mov_b32 m0, %[kp]\n\t"                                                                                 \
  "s_nop 7\n\t" "s_waitcnt lgkmcnt(0)"

template <bool ISV>
__device__ __forceinline__ void x_role(unsigned char* lds, const u16* __restrict__ x, const u16* __restrict__ Wm, int fo0, int head,
                                       __amdgpu_buffer_rsrc_t rsf, u32* prog, u32 tag, int lane, int w) {
  const int ldsb = (int)(size_t)(LAS unsigned char*)lds;
  const __amdgpu_buffer_rsrc_t rsxin = __builtin_amdgcn_make_buffer_rsrc((void*)x, 0, S_LEN * DM * 2, 0x00020000);
  const __amdgpu_buffer_rsrc_t rsp = __builtin_amdgcn_make_buffer_rsrc((void*)prog, 0, 128, 0x00020000);
  const int lr = lane & 15, lg = lane >> 4;
  const int xrd = ldsb + lr * XPITCH + lg * 16;
  const int fob = fo0 + (ISV ? (head * 8 + 2 * w) : (head * 8 + w)) * 1024;
  const int p2 = ISV ? 1024 : 4096;
  u32x32 xw[8];
  {
    const __amdgpu_buffer_rsrc_t rw = __builtin_amdgcn_make_buffer_rsrc((void*)Wm, 0, DM * HD * 2, 0x00020000);
    const int vq = (lane >> 4) * (HD * 2) + head * 256 + (lane & 15) * 16;
#define XWISSUE(c) _Pragma("unroll") for (int j = 0; j < 8; ++j)                                                       \
      asm volatile("s_mov_b32 m0, %0\n\ts_nop 3\n\tbuffer_load_dwordx4 %1, %2, %3 offen lds"                           \
                   :: "s"(ldsb + ((c) & 3) * XSLOT + (8 * w + j) * XPITCH), "v"(vq), "s"(rw), "s"((128 * (c) + 32 * w + 4 * j) * (HD * 2)) : "memory");
#define XXISSUE(h) _Pragma("unroll") for (int j = 0; j < 8; ++j)                                                       \
      asm volatile("s_mov_b32 m0, %0\n\ts_nop 3\n\tbuffer_load_dwordx4 %1, %2, %3 offen lds"                           \
                   :: "s"(ldsb + (h) * XSLOT + (8 * w + j) * XPITCH), "v"(lane * 16), "s"(rsxin), "s"((((h) >> 1) * 32 + 8 * w + j) * 2048 + ((h) & 1) * 1024) : "memory");
    XWISSUE(0) XWISSUE(1) XWISSUE(2) XWISSUE(3)
    const int q = lr >> 2, p = lr & 3;
#pragma unroll
    for (int c = 0; c < 8; ++c) {
      asm volatile("s_waitcnt vmcnt(24)\n\ts_barrier" ::: "memory");
#pragma unroll
      for (int s = 0; s < 4; ++s)
#pragma unroll
        for (int t = 0; t < 2; ++t) {
          const unsigned char* base = lds + (c & 3) * XSLOT + (8 * s + 2 * lg) * XPITCH + q * 256 + w * 64 + p * 16 + t * 8;
          const u32x2 t0 = __builtin_bit_cast(u32x2, __builtin_amdgcn_ds_read_tr16_b64_v4i16((LAS v4i16_t*)(base)));
          const u32x2 t1 = __builtin_bit_cast(u32x2, __builtin_amdgcn_ds_read_tr16_b64_v4i16((LAS v4i16_t*)(base + XPITCH)));
          const int o = ((4 * c + s) * 2 + t) * 4;
          xw[o >> 5][(o & 31) + 0] = t0[0]; xw[o >> 5][(o & 31) + 1] = t0[1]; xw[o >> 5][(o & 31) + 2] = t1[0]; xw[o >> 5][(o & 31) + 3] = t1[1];
        }
      LBAR();
      if (c < 4) { XWISSUE(c + 4) } else if (c < 7) { XXISSUE(c - 4) }
    }
  }
  asm volatile("s_waitcnt vmcnt(8)\n\ts_barrier" ::: "memory");
  u32x16 xacc;
  {
    u32 kp, dg; int gb = 8 * w * 2048, cnt = NSEG / 2, sg = fob - NH * 8 * 1024;
#define XASM(MF)                                                                                            \
    asm volatile(XLOOP(MF)                                                                                  \
                 : "=&{v[80:95]}"(xacc), [kp] "=&s"(kp), [dg] "=&s"(dg), [gb] "+s"(gb), [cnt] "+s"(cnt), [sg] "+s"(sg) \
                 : [r0] "v"(xrd), [r1] "v"(xrd + XSLOT), [r2] "v"(xrd + 2 * XSLOT), [r3] "v"(xrd + 3 * XSLOT), [l16] "v"(lane * 16), [lp2] "v"(lane * 16 + p2), \
                   [rx] "s"(rsxin), [rf] "s"(rsf), [rp] "s"(rsp), [fo] "v"(lane == 0 && w == 0 ? 0 : OOR), [dl] "s"(ldsb + 8 * w * XPITCH), [p0] "s"(tag + XP0), \
                   "{a[0:31]}"(xw[0]), "{a[32:63]}"(xw[1]), "{a[64:95]}"(xw[2]), "{a[96:127]}"(xw[3]),      \
                   "{a[128:159]}"(xw[4]), "{a[160:191]}"(xw[5]), "{a[192:223]}"(xw[6]), "{a[224:255]}"(xw[7]) \
                 : CL64, CL72, CL96, CL112, CL120, CL8(128, 129, 130, 131, 132, 133, 134, 135), "v136", "v137", "v138", "scc", "memory");
    if (ISV) { XASM(XMF_V) } else { XASM(XMF_QK) }
  }
#pragma unroll
  for (int jj = 0; jj < 2; ++jj) {
    u32x4 pk;
#pragma unroll
    for (int e = 0; e < 4; ++e) pk[e] = pack2(__uint_as_float(xacc[8 * jj + 2 * e]), __uint_as_float(xacc[8 * jj + 2 * e + 1]));
    __builtin_amdgcn_raw_buffer_store_b128(pk, rsf, lane * 16 + p2 * jj, fob + (NSEG - 1) * (NH * 8 * 1024), 16);
  }
  asm volatile("s_waitcnt vmcnt(0)\n\ts_barrier" ::: "memory");
  if (lane == 0 && w == 0) __hip_atomic_store(prog, tag + NSEG + 8, RLX_AGENT);
}

#define SU_CP 4288
#define SU_U 0
#define SU_K (2 * SU_CP)
#define SU_Y (SU_K + 10400)
#define SU_CH (SU_Y + 4096)
#define SST(i)
__device__ __forceinline__ int koff(int i) {
  const int x = i & 127;
  return i < 256 ? (32 * (i >> 7) + 16 * ((i >> 2) & 1) + (x >> 3)) * 16 + (i & 3) * 4
       : i < 384 ? 1024 + x * 4
       : i < 512 ? 1024 + (32 + 16 * (x >> 6) + ((x >> 1) & 15)) * 16 + (2 * ((x >> 5) & 1) + (x & 1)) * 4
                 : 2048 + (16 * ((i - 512) >> 6) + (i & 15)) * 16 + ((i >> 4) & 3) * 4;
}
struct SsmIn { u32x4 ka, kb; u32x2 kc; u32 kd, ke[4]; u32x4 uv[4]; u32x2 up, dsk; };
#define SSM_GOT(in) asm volatile("" : "+v"(in.ka), "+v"(in.kb), "+v"(in.kc), "+v"(in.kd), "+v"(in.ke[0]), "+v"(in.ke[1]), "+v"(in.ke[2]), "+v"(in.ke[3]), \
                                      "+v"(in.uv[0]), "+v"(in.uv[1]), "+v"(in.uv[2]), "+v"(in.uv[3]), "+v"(in.up), "+v"(in.dsk))
__device__ __forceinline__ void ssm_load(SsmIn& in, int d0, __amdgpu_buffer_rsrc_t rst, __amdgpu_buffer_rsrc_t rsd, int tid, int lane, int w) {
  const int so = 2 * SSM_L * SSM_D * 2 + (d0 + w) * 3072, ia = lane < 32 ? 8 * lane : 128 + 4 * lane;
  in.ka = __builtin_amdgcn_raw_buffer_load_b128(rst, koff(ia), so, 16);
  in.kb = __builtin_amdgcn_raw_buffer_load_b128(rst, koff(ia + 4), so, 16);
  in.kc = __builtin_amdgcn_raw_buffer_load_b64(rst, koff(384 + 2 * lane), so, 16);
  in.kd = __builtin_amdgcn_raw_buffer_load_b32(rst, koff(386 + 2 * lane), so, 16);
#pragma unroll
  for (int i = 0; i < 4; ++i) in.ke[i] = __builtin_amdgcn_raw_buffer_load_b32(rst, koff(512 + 64 * (i >> 1) + (i & 1) + lane), so, 16);
#pragma unroll
  for (int i = 0; i < 4; ++i) in.uv[i] = __builtin_amdgcn_raw_buffer_load_b128(rst, tid * 16, d0 * 4096 + i * 4096, 16);
  in.up = __builtin_amdgcn_raw_buffer_load_b64(rst, tid ? tid * 16 - 8 : OOR, d0 * 4096 + 3 * 4096, 16);
  in.dsk = __builtin_amdgcn_raw_buffer_load_b64(rsd, 0, d0 * 2, 0);
}
__device__ __forceinline__ float reslo(float v, u32 h) { return v - __uint_as_float(h << 16); }
__device__ __forceinline__ float reshi(float v, u32 h) { return v - __uint_as_float(h & 0xffff0000u); }
__device__ __forceinline__ int ssm_unit(unsigned char* sm, int d0, SsmIn& in, bool pend, __amdgpu_buffer_rsrc_t rst, __amdgpu_buffer_rsrc_t rsx, int fb,
                                        __amdgpu_buffer_rsrc_t rsd, int tid, int lane, int w) {
  unsigned char* cb = sm + w * SU_CH;
  SST(0)
  const int one = tid == 0 ? 0 : OOR;
  u32 tk = __builtin_amdgcn_raw_ptr_buffer_atomic_add_i32(1, rsx, one, XTKT, 0);
  const int r = lane & 31, h = lane >> 5;
#pragma unroll
  for (int c = 0; c < 4; ++c) {
    const u32 sel = (c & 1) ? 0x07060302u : 0x05040100u;
    u32x4 e, o;
#pragma unroll
    for (int i = 0; i < 4; ++i) {
      e[i] = __builtin_amdgcn_perm(in.uv[i][2 + (c >> 1)], in.uv[i][c >> 1], sel);
      o[i] = __builtin_amdgcn_perm(in.uv[i][c >> 1], i ? in.uv[i ? i - 1 : 0][2 + (c >> 1)] : in.up[c >> 1], sel);
    }
    *(u32x4*)(sm + c * SU_CH + SU_U + 96 + tid * 16) = e;
    *(u32x4*)(sm + c * SU_CH + SU_U + SU_CP + 96 + tid * 16) = o;
  }
  SST(1)
  {
    unsigned char* kr = cb + SU_K + ((lane >> 2) + 1) * 80 + (3 - (lane & 3)) * 16;
    const f32x4 a = __builtin_bit_cast(f32x4, in.ka), b = __builtin_bit_cast(f32x4, in.kb);
    const bool dn = lane < 32;
    const float v[8] = {a[0], dn ? a[1] : a[0], dn ? a[2] : a[1], dn ? a[3] : a[2], dn ? b[0] : a[2], dn ? b[1] : a[2], dn ? b[2] : a[3], dn ? b[3] : b[0]};
    u32x4 hi, lo;
#pragma unroll
    for (int e = 0; e < 4; ++e) { hi[e] = pack2(v[7 - 2 * e], v[6 - 2 * e]); lo[e] = pack2(reslo(v[7 - 2 * e], hi[e]), reshi(v[6 - 2 * e], hi[e])); }
    *(u32x4*)(kr) = hi; *(u32x4*)(kr + 5200) = lo;
    {
      const float v0 = __uint_as_float(in.kc[0]), v1 = __uint_as_float(in.kc[1]), v2 = __uint_as_float(in.kd);
      hi = (u32x4){pack2(v2, v2), pack2(v1, v1), pack2(v1, v0), pack2(v0, v0)};
      const float r0 = reshi(v0, hi[3]), r1 = reshi(v1, hi[1]), r2 = reshi(v2, hi[0]);
      *(u32x4*)(kr + 1280) = hi; *(u32x4*)(kr + 1280 + 5200) = (u32x4){pack2(r2, r2), pack2(r1, r1), pack2(r1, r0), pack2(r0, r0)};
    }
#pragma unroll
    for (int j = 0; j < 2; ++j) {
      const float v0 = __uint_as_float(in.ke[2 * j]), v1 = __uint_as_float(in.ke[2 * j + 1]);
      const u32 h0 = pack2(v0, v0), h1 = pack2(v1, v1), sel = (lane & 1) ? 0x01000100u : 0x05040100u;
      const float r0 = reshi(v0, h0), r1 = reshi(v1, h1);
      const u32 l0 = pack2(r0, r0), l1 = pack2(r1, r1);
      *(u32x4*)(kr + 2560 + 1280 * j) = (u32x4){h1, __builtin_amdgcn_perm(h0, h1, sel), h0, h0};
      *(u32x4*)(kr + 2560 + 1280 * j + 5200) = (u32x4){l1, __builtin_amdgcn_perm(l0, l1, sel), l0, l0};
    }
  }
  SST(2)
  f32x4 sk[8];
  {
    const u32 d01 = __builtin_amdgcn_readfirstlane(in.dsk[0]), d23 = __builtin_amdgcn_readfirstlane(in.dsk[1]);
#pragma unroll
    for (int i = 0; i < 4; ++i)
      asm("v_dot2_f32_bf16 %0, %8, %12, 0\n\tv_dot2_f32_bf16 %1, %8, %13, 0\n\tv_dot2_f32_bf16 %2, %9, %14, 0\n\tv_dot2_f32_bf16 %3, %9, %15, 0\n\t"
          "v_dot2_f32_bf16 %4, %10, %12, 0\n\tv_dot2_f32_bf16 %5, %10, %13, 0\n\tv_dot2_f32_bf16 %6, %11, %14, 0\n\tv_dot2_f32_bf16 %7, %11, %15, 0\n\t"
          "v_cvt_pk_bf16_f32 %0, 0, %0\n\tv_cvt_pk_bf16_f32 %1, 0, %1\n\tv_cvt_pk_bf16_f32 %2, 0, %2\n\tv_cvt_pk_bf16_f32 %3, 0, %3\n\t"
          "v_cvt_pk_bf16_f32 %4, 0, %4\n\tv_cvt_pk_bf16_f32 %5, 0, %5\n\tv_cvt_pk_bf16_f32 %6, 0, %6\n\tv_cvt_pk_bf16_f32 %7, 0, %7"
          : "=&v"(sk[2 * i][0]), "=&v"(sk[2 * i][1]), "=&v"(sk[2 * i][2]), "=&v"(sk[2 * i][3]),
            "=&v"(sk[2 * i + 1][0]), "=&v"(sk[2 * i + 1][1]), "=&v"(sk[2 * i + 1][2]), "=&v"(sk[2 * i + 1][3])
          : "v"(in.uv[i][0]), "v"(in.uv[i][1]), "v"(in.uv[i][2]), "v"(in.uv[i][3]), "s"(d01 & 0xffffu), "s"(d01 & 0xffff0000u), "s"(d23 & 0xffffu), "s"(d23 & 0xffff0000u));
  }
  asm volatile("s_waitcnt vmcnt(0)" : "+v"(tk), "+v"(sk[0]), "+v"(sk[1]), "+v"(sk[2]), "+v"(sk[3]), "+v"(sk[4]), "+v"(sk[5]), "+v"(sk[6]), "+v"(sk[7]) :: "memory");
  if (tid == 0) *(u32*)(sm + 4 * SU_CH) = tk + 65u;
  LBAR();
  SST(3)
  if (pend) __builtin_amdgcn_raw_ptr_buffer_atomic_add_i32(1, rsx, one, XYDN, 0);
  const int nxt = __builtin_amdgcn_readfirstlane(*(const u32*)(sm + 4 * SU_CH));
  if (nxt < 256) ssm_load(in, 4 * nxt, rst, rsd, tid, lane, w);
  SST(4)
  f32x16 cv0, cv1;
  {
    const int m0 = 48 - 15 + 8 * h + r;
    const int cbo = (int)(size_t)(LAS unsigned char*)cb;
    int ua = cbo + SU_U + (m0 & 1) * SU_CP + ((m0 + 1) & ~1) * 2 - 32, ub = ua + 2048;
    const int kb = cbo + SU_K + 16 * h;
    int a1 = kb + (33 + r) * 80, a0 = kb + (1 + r) * 80, cnt = 32;
#define CRA(S, ad, O) "ds_read2_b32 " RNG("v", S, 2) ", " ad " offset0:" STR((O) / 4) " offset1:" STR((O) / 4 + 1) "\n\t" \
                      "ds_read2_b32 " RNG("v", (S) + 2, 2) ", " ad " offset0:" STR((O) / 4 + 2) " offset1:" STR((O) / 4 + 3) "\n\t"
#define CRB(S, ad, O) "ds_read_b128 " RNG("v", S, 4) ", " ad " offset:" STR(O) "\n\t"
#define CMF(C, A, B) "v_mfma_f32_32x32x16_bf16 " RNG("v", C, 16) ", " RNG("v", A, 4) ", " RNG("v", B, 4) ", " RNG("v", C, 16) "\n\t"
#define CUPD "v_add_u32 %[ua], 64, %[ua]\n\t" "v_subrev_u32 %[a1], 80, %[a1]\n\t" "v_subrev_u32 %[a0], 80, %[a0]\n\t" "v_max_i32 %[a0], %[a0], %[kb]\n\t"
#define CHALF(S, N, O, UPD, UPD2)                                                                 \
    WLG(5) CMF(64, S, (S) + 8) UPD CRA(N, "%[ua]", O)                                             \
    WLG(6) CMF(80, S, (S) + 16) UPD2 CRB((N) + 8, "%[a0]", O) CRB((N) + 16, "%[a1]", O)           \
    WLG(6) CMF(80, (S) + 4, (S) + 8) CRA((N) + 4, "%[ub]", O)                                     \
    WLG(7) CMF(64, S, (S) + 12) CRB((N) + 12, "%[a0]", (O) + 5200)                                \
    WLG(7) CMF(80, S, (S) + 20) CRB((N) + 20, "%[a1]", (O) + 5200)                                \
    CMF(80, (S) + 4, (S) + 12)
    asm volatile(
        CRA(96, "%[ua]", 32) CRB(104, "%[a0]", 32) CRB(112, "%[a1]", 32) CRA(100, "%[ub]", 32) CRB(108, "%[a0]", 32 + 5200) CRB(116, "%[a1]", 32 + 5200)
        ZER(64) ZER(68) ZER(72) ZER(76) ZER(80) ZER(84) ZER(88) ZER(92)
        ".Lc%=:\n\t"
        CHALF(96, 120, 0, "", "")
        CHALF(120, 96, 32, CUPD, "v_add_u32 %[ub], 64, %[ub]\n\t")
        "s_sub_u32 %[cnt], %[cnt], 1\n\t"
        "s_cmp_lg_u32 %[cnt], 0\n\t"
        "s_cbranch_scc1 .Lc%=\n\t"
        "s_nop 7\n\t" "s_nop 3\n\t" WLG(0)
        : "=&{v[64:79]}"(cv0), "=&{v[80:95]}"(cv1), [ua] "+v"(ua), [ub] "+v"(ub), [a0] "+v"(a0), [a1] "+v"(a1), [cnt] "+s"(cnt)
        : [kb] "v"(kb)
        : CL96, CL112, CL120, CL8(128, 129, 130, 131, 132, 133, 134, 135), CL8(136, 137, 138, 139, 140, 141, 142, 143), "scc", "memory");
  }
  SST(5)
  SSM_GOT(in);
#pragma unroll
  for (int g = 0; g < 2; ++g)
#pragma unroll
    for (int i = 0; i < 4; ++i) {
      const f32x16& c = g ? cv1 : cv0;
      *(u32x2*)(cb + SU_Y + (32 * g + r) * 64 + ((i ^ (r >> 1)) & 3) * 16 + 8 * h) = (u32x2){pack2(c[4 * i], c[4 * i + 1]), pack2(c[4 * i + 2], c[4 * i + 3])};
    }
  LBAR();
  SST(6)
  {
    u32x4 c[4];
#pragma unroll
    for (int j = 0; j < 4; ++j) c[j] = *(const u32x4*)(sm + j * SU_CH + SU_Y + (tid >> 2) * 64 + ((tid ^ (tid >> 3)) & 3) * 16);
#pragma unroll
    for (int i = 0; i < 4; ++i) {
      u32x4 y;
      asm("v_dot2_f32_bf16 %4, %12, %16, %4\n\tv_dot2_f32_bf16 %5, %13, %16, %5\n\tv_dot2_f32_bf16 %6, %14, %16, %6\n\tv_dot2_f32_bf16 %7, %15, %16, %7\n\t"
          "v_dot2_f32_bf16 %8, %12, %17, %8\n\tv_dot2_f32_bf16 %9, %13, %17, %9\n\tv_dot2_f32_bf16 %10, %14, %17, %10\n\tv_dot2_f32_bf16 %11, %15, %17, %11\n\t"
          "v_cvt_pk_bf16_f32 %0, %4, %5\n\tv_cvt_pk_bf16_f32 %1, %6, %7\n\tv_cvt_pk_bf16_f32 %2, %8, %9\n\tv_cvt_pk_bf16_f32 %3, %10, %11"
          : "=&v"(y[0]), "=&v"(y[1]), "=&v"(y[2]), "=&v"(y[3]),
            "+v"(sk[2 * i][0]), "+v"(sk[2 * i][1]), "+v"(sk[2 * i][2]), "+v"(sk[2 * i][3]),
            "+v"(sk[2 * i + 1][0]), "+v"(sk[2 * i + 1][1]), "+v"(sk[2 * i + 1][2]), "+v"(sk[2 * i + 1][3])
          : "v"(c[0][i]), "v"(c[1][i]), "v"(c[2][i]), "v"(c[3][i]), "s"(0x00003f80u), "s"(0x3f800000u));
      __builtin_amdgcn_raw_buffer_store_b128(y, rst, tid * 16, SSM_L * SSM_D * 2 + d0 * 4096 + i * 4096, 16);
    }
  }
  if (nxt >= 256) {
    asm volatile("s_waitcnt vmcnt(0)\n\ts_barrier" ::: "memory");
    __builtin_amdgcn_raw_ptr_buffer_atomic_add_i32(1, rsx, one, XYDN, 0);
  }
  SST(7)
  return nxt;
}

#define L_TOTAL 163840
__device__ __forceinline__ int ldw(__amdgpu_buffer_rsrc_t rs, int off) {
  asm volatile("" ::: "memory");
  return __builtin_amdgcn_readfirstlane(__builtin_amdgcn_raw_buffer_load_b32(rs, 0, off, 16));
}
#define GF(rt) "v[64+8*(xk&7)+4*" #rt ":64+8*(xk&7)+4*" #rt "+3]"
#define GW2 "v[128+4*xk:128+4*xk+3]"
#define GW3 "v[24+4*(xk&1):24+4*(xk&1)+3]"
#define GLOAD(S, K) "s_xor_b32 %[tq], " S ", 1024*(" K ")\n\t"
#define GTR(K) "ds_read_b128 v[24+4*((" K ")&1):24+4*((" K ")&1)+3], %[wl] offset:1024*((" K ")&31)\n\t"
#define GPACE 0
#define YAUX 16
#define UDELAY 100
#define GNOSSM 1
#define GNG 3
#define GS0 22
__device__ __forceinline__ void g_role(unsigned char* lds, const u16* __restrict__ Wo, u16* __restrict__ out, __amdgpu_buffer_rsrc_t rsx,
                                       __amdgpu_buffer_rsrc_t rsf, int fb, u32 tag, int grp, int sl, int w) {
  const int lane = fresh_lane();
  const int lr = lane & 15, lg = lane >> 4;
  const int rot = (2 * sl + 8 * w) & 31;
  const int ldsb = (int)(size_t)(LAS unsigned char*)lds;
  u32x32 xw[8], w2[4];
  LBAR();
  {
    const __amdgpu_buffer_rsrc_t rw = __builtin_amdgcn_make_buffer_rsrc((void*)Wo, 0, DM * HD * 2, 0x00020000);
    const int vq = (lane >> 3) * (DM * 2) + sl * 128 + (lane & 7) * 16;
    const int sq = (1024 * w + rot * 32) * (DM * 2);
#define GSLOT(c) ((c) < 8 ? (((c) & 3) == 3 ? 131072 + w * 8192 : w * 32768 + 8192 * (((c) & 3) + 1)) : (((c) - 8) % 3 == 2 ? 131072 + w * 8192 : w * 32768 + 8192 * (((c) - 8) % 3 + 2)))
#define GISSUE(c) _Pragma("unroll") for (int j = 0; j < 8; ++j)                                                        \
      asm volatile("s_mov_b32 m0, %0\n\ts_nop 3\n\tbuffer_load_dwordx4 %1, %2, %3 offen lds"                           \
                   :: "s"(ldsb + GSLOT(c) + j * 1024), "v"(vq), "s"(rw), "s"(sq ^ ((64 * (c) + 8 * j) * DM * 2)) : "memory");
    GISSUE(0) GISSUE(1) GISSUE(2) GISSUE(3)
    const int q = lr >> 2, p = lr & 3;
    u32x4 w3[16];
#pragma unroll
    for (int c = 0; c < 16; ++c) {
      if (c < 5) waitvm<24>(); else if (c < 14) waitvm<16>(); else if (c == 14) waitvm<8>(); else waitvm<0>();
      u32x4 f3[2];
#pragma unroll
      for (int s = 0; s < 2; ++s)
#pragma unroll
        for (int t = 0; t < 4; ++t) {
          const unsigned char* base = lds + GSLOT(c) + (32 * s + 8 * lg + q) * 128 + (t < 2 ? p * 16 + t * 8 : t * 32 + p * 8);
          const u32x2 t0 = __builtin_bit_cast(u32x2, __builtin_amdgcn_ds_read_tr16_b64_v4i16((LAS v4i16_t*)(base)));
          const u32x2 t1 = __builtin_bit_cast(u32x2, __builtin_amdgcn_ds_read_tr16_b64_v4i16((LAS v4i16_t*)(base + 512)));
          const int o = t < 2 ? ((2 * c + s) * 2 + t) * 4 : (2 * c + s) * 4;
          if (t < 2) { xw[o >> 5][(o & 31) + 0] = t0[0]; xw[o >> 5][(o & 31) + 1] = t0[1]; xw[o >> 5][(o & 31) + 2] = t1[0]; xw[o >> 5][(o & 31) + 3] = t1[1]; }
          else if (t == 2) { w2[o >> 5][(o & 31) + 0] = t0[0]; w2[o >> 5][(o & 31) + 1] = t0[1]; w2[o >> 5][(o & 31) + 2] = t1[0]; w2[o >> 5][(o & 31) + 3] = t1[1]; }
          else f3[s] = (u32x4){t0[0], t0[1], t1[0], t1[1]};
        }
      asm volatile("s_waitcnt lgkmcnt(0)" ::: "memory");
      if (c < 8) { *(u32x4*)(lds + w * 32768 + (2 * c) * 1024 + lane * 16) = f3[0]; *(u32x4*)(lds + w * 32768 + (2 * c + 1) * 1024 + lane * 16) = f3[1]; }
      else { w3[2 * (c - 8)] = f3[0]; w3[2 * (c - 8) + 1] = f3[1]; }
      if (GPACE) __builtin_amdgcn_s_sleep(GPACE);
      if (c < 4) { GISSUE(c + 4) } else if (c >= 5 && c < 13) { GISSUE(c + 3) }
    }
#pragma unroll
    for (int i = 0; i < 16; ++i) *(u32x4*)(lds + w * 32768 + (16 + i) * 1024 + lane * 16) = w3[i];
    LBAR();
  }
  wait_start(rsx, fb, tag, true);
  const int wl = ldsb + w * 32768 + lane * 16;
  const __amdgpu_buffer_rsrc_t rso = __builtin_amdgcn_make_buffer_rsrc((void*)out, 0, S_LEN * OUTLD * 2, 0x00020000);
  const int rw_ = ldsb + 131072 + w * 8192 + lane * 16, rr_ = ldsb + 131072 + w * 2048 + lane * 16;
  const int oa = ((16 * (w & 1) + lr) * OUTLD + 64 * sl + (w < 2 ? 8 * lg : 32 + 4 * lg)) * 2, ob = oa + (w < 2 ? 8 : 32);
  const int hq = (grp + GNG - (62 - GS0) % GNG) % GNG - 1, hf = hq < 2 ? hq : -1;
#define GRW(i) "ds_write_b128 %[rw], " RNG("v", 32 + 4 * (i), 4) " offset:" STR(1024 * (i)) "\n\t"
#define GRR(i) "ds_read_b128 " RNG("v", 32 + 4 * (i), 4) ", %[rr] offset:" STR(8192 * ((i) >> 1) + 1024 * ((i) & 1)) "\n\t"
#define GNXT(D, S) "s_cmp_lt_u32 " S ", " STR(GS0) "\n\t" "s_cselect_b32 " D ", 1, " STR(GNG) "\n\t" "s_add_u32 " D ", " D ", " S "\n\t"
#define GDN1(S) "s_min_u32 s41, " S ", 63\n\t" "s_lshl_b32 s41, s41, 5\n\t" "v_add_u32 v23, s41, %[u0]\n\t" "v_lshrrev_b32 v22, 6, v23\n\t"
#define GDN2 "v_and_b32 v23, 63, v23\n\t" "v_lshl_add_u32 v23, v23, 7, %[u4]\n\t" "s_lshr_b32 s41, s41, 6\n\t" "s_max_u32 s41, s41, 29\n\t"
#define GDN3 "s_lshl_b32 s41, s41, 13\n\t" "s_add_u32 s41, s41, %[pa]\n\t" "buffer_load_dword v23, v23, %[rs], s41 offen sc1\n\t"
#define GDN(S) GDN1(S) GDN2 GDN3
#define GCK 15
#define GOK(D) "v_cmp_le_i32 vcc, v23, v22\n\t" "s_cmp_eq_u64 vcc, 0\n\t" "s_cselect_b32 " D ", 1, 0\n\t"
static_assert(NAW == 64, "shifts above");
#define GEPI(MID, OA, OB) \
      GRW(0) GRW(1) GRW(2) GRW(3) GRW(4) GRW(5) GRW(6) GRW(7) \
      MID \
      WLG(0) "s_barrier\n\t" \
      GRR(0) GRR(1) GRR(2) GRR(3) GRR(4) GRR(5) GRR(6) GRR(7) \
      WLG(4) PKA(32, 32, 40) PKA(34, 34, 42) PKA(36, 36, 44) PKA(38, 38, 46) \
      WLG(0) PKA(48, 48, 56) PKA(50, 50, 58) PKA(52, 52, 60) PKA(54, 54, 62) \
      PKA(32, 32, 48) PKA(34, 34, 50) PKA(36, 36, 52) PKA(38, 38, 54) \
      "v_cvt_pk_bf16_f32 v20, v32, v33\n\t" "v_cvt_pk_bf16_f32 v21, v34, v35\n\t" "v_cvt_pk_bf16_f32 v18, v36, v37\n\t" "v_cvt_pk_bf16_f32 v19, v38, v39\n\t" \
      "buffer_store_dwordx2 v[20:21], " OA ", %[ro], s42 offen\n\t" \
      "buffer_store_dwordx2 v[18:19], " OB ", %[ro], s42 offen\n\t"
#define GH "v[64+4*(xk&15):64+4*(xk&15)+3]"
#define GSTAMP
#define GSTIN
#define GSTCL
#define GT1
#define GT2
#define GTO
  asm volatile(
      "s_nop 4\n\t"
      "v_add_u32 v16, 131072, %[va]\n\t"
      "s_mov_b32 s36, %[s0]\n\t" "s_mov_b32 s43, " STR(SPIN_CAP) "\n\t"
      "s_mov_b32 s37, 0\n\t" "v_mov_b32 v17, " STR(OOR) "\n\t" "v_mov_b32 v12, v17\n\t" "v_mov_b32 v15, v17\n\t"
      "s_cmp_ge_i32 %[hf], 0\n\t" "s_cbranch_scc1 .Li%=\n"
      ".Lp%=:\n\t"
      GDN("s36") WVM(0) GOK("s40")
      "s_cbranch_scc1 .Lr%=\n\t"
      "s_sleep 2\n\t" "s_sub_u32 s43, s43, 1\n\t" "s_cmp_gt_i32 s43, 0\n\t" "s_cbranch_scc1 .Lp%=\n"
      ".Lr%=:\n\t"
      "s_lshl_b32 s38, s36, 18\n\t" "s_add_u32 s38, s38, %[gb]\n\t"
      ".set xk, 0\n\t.rept 8\n\t" GLOAD("s38", "xk")
      "buffer_load_dwordx4 " GF(0) ", %[va], %[rf], %[tq] offen sc1\n\t" "buffer_load_dwordx4 " GF(1) ", v16, %[rf], %[tq] offen sc1\n\t"
      ".set xk, xk+1\n\t.endr\n"
      ".Lb%=:\n\t"
      GTR("0")
      ".set xk, 0\n\t.rept 32\n\t"
      "s_waitcnt vmcnt(14)\n\t"
      XM1(XA(32, 0), XW(0, 0), GF(0), 1) GTR("xk+1") XM1(XA(32, 4), XW(0, 1), GF(0), 1)
      ".if xk < 24\n\t" GLOAD("s38", "xk+8") ".else\n\t" GLOAD("s39", "xk-24") ".endif\n\t"
      XM1(XA(32, 8), XW(0, 0), GF(1), 1)
      ".if xk == " STR(GCK) "-1\n\t" GNXT("s37", "s36") ".endif\n\t"
      ".if xk == " STR(GCK) "\n\t" GDN1("s37") ".endif\n\t"
      ".if xk == 23\n\t" GOK("s40") ".endif\n\t"
      XM1(XA(32, 12), XW(0, 1), GF(1), 1)
      ".if xk == " STR(GCK) "-1\n\t" "s_lshl_b32 s42, s36, 17\n\t" ".endif\n\t"
      ".if xk == " STR(GCK) "\n\t" GDN2 ".endif\n\t"
      ".if xk == 23\n\t" "s_cmp_lt_u32 s37, 63\n\t" "s_cselect_b32 s40, s40, 0\n\t" "s_cmp_lg_u32 s40, 0\n\t" ".endif\n\t"
      XM1(XA(32, 16), GW2, GF(0), 1)
      ".if xk == " STR(GCK) "\n\t" GDN3 ".endif\n\t"
      ".if xk == 23\n\t" "s_cselect_b32 s39, s37, s36\n\t" "s_lshl_b32 s39, s39, 18\n\t" "s_add_u32 s39, s39, %[gb]\n\t" ".endif\n\t"
      "s_waitcnt lgkmcnt(1)\n\t" XM1(XA(32, 20), GW3, GF(0), 1)
      "buffer_load_dwordx4 " GF(0) ", %[va], %[rf], %[tq] offen sc1\n\t"
      XM1(XA(32, 24), GW2, GF(1), 1) XM1(XA(32, 28), GW3, GF(1), 1)
      "buffer_load_dwordx4 " GF(1) ", v16, %[rf], %[tq] offen sc1\n\t"
      ".if xk == 16\n\t" "s_barrier\n\t" ".endif\n\t"
      ".set xk, xk+1\n\t.endr\n\t"
      GEPI("", "%[oa]", "%[ob]")
      GSTAMP
      "s_mov_b32 s36, s37\n\t" "s_mov_b32 s38, s39\n\t"
      "s_cmp_lg_u32 s40, 0\n\t" "s_cbranch_scc1 .Lb%=\n\t"
      "s_cmp_lt_u32 s36, 63\n\t" "s_cbranch_scc1 .Lp%=\n\t"
      "s_cmp_lt_i32 %[hf], 0\n\t" "s_cbranch_scc1 .Le%=\n\t"
      "s_mov_b32 s37, 4\n\t" "v_mov_b32 v17, %[va]\n\t" "v_mov_b32 v12, %[oc]\n\t" "v_sub_u32 v15, %[ob], %[oa]\n\t" "v_add_u32 v15, v15, %[oc]\n\t"
      "s_barrier\n"
      ".Lh%=:\n\t"
      "v_cmp_eq_u32 vcc, -1, v23\n\t" "s_cbranch_vccz .Lt%=\n\t" "buffer_load_dword v23, %[uh], %[rs], 0 offen sc1\n\t" WVM(0)
      "s_sleep 1\n\t" "s_sub_u32 s43, s43, 1\n\t" "s_cmp_gt_i32 s43, 0\n\t" "s_cbranch_scc1 .Lh%=\n"
      ".Lt%=:\n\t"
      "v_readfirstlane_b32 s39, v23\n\t" "s_cmp_ge_i32 s39, 0\n\t" "s_cbranch_scc1 .Li%=\n"
      ".Lw%=:\n\t"
      "s_memrealtime s[40:41]\n\t" WLG(0) "s_sub_u32 s40, s39, s40\n\t" "s_lshl_b32 s40, s40, 1\n\t" "s_sub_u32 s40, s40, 1\n\t" "s_cmp_lt_u32 s40, 4000\n\t" "s_cbranch_scc1 .Lw%=\n\t"
      "s_branch .Li%=\n"
      ".Lx%=:\n\t"
      "s_sub_u32 s37, s37, 1\n\t" "s_cmp_gt_u32 s37, 1\n\t" "s_cbranch_scc1 .Li%=\n"
      ".Ly%=:\n\t"
      "buffer_load_dword v23, %[uh], %[rs], 0 offen sc1\n\t" WVM(0) "v_cmp_ge_i32 vcc, 31, v23\n\t" "s_cbranch_vccz .Li%=\n\t"
      "s_sub_u32 s43, s43, 1\n\t" "s_cmp_gt_i32 s43, 0\n\t" "s_cbranch_scc1 .Ly%=\n"
      ".Li%=:\n\t" GT1
      "s_lshl_b32 s38, %[hf], 17\n\t" "s_add_u32 s38, s38, 63*262144\n\t" "s_add_u32 s38, s38, %[gb]\n\t"
      ".set xk, 0\n\t.rept 16\n\t" GLOAD("s38", "xk") "buffer_load_dwordx4 " GH ", v17, %[rf], %[tq] offen sc1\n\t" ".set xk, xk+1\n\t.endr\n\t"
      GTR("0")
      "s_lshl_b32 s42, %[hf], 16\n\t" "s_add_u32 s42, s42, 63*131072\n\t" "v_mov_b32 v22, 0\n\t" "s_cmp_gt_u32 s37, 1\n\t" "s_cselect_b32 s39, -2, -1\n\t"
      ".set xk, 0\n\t.rept 32\n\t"
      ".if xk < 17\n\t" "s_waitcnt vmcnt(15)\n\t" ".else\n\t" "s_waitcnt vmcnt(31-xk)\n\t" ".endif\n\t"
      XM1(XA(32, 0), XW(0, 0), GH, 1) GTR("xk+1") XM1(XA(32, 4), XW(0, 1), GH, 1) "v_max3_u32 v22, v22, v[64+4*(xk&15)], v[65+4*(xk&15)]\n\t"
      XM1(XA(32, 16), GW2, GH, 1) "v_max3_u32 v22, v22, v[66+4*(xk&15)], v[67+4*(xk&15)]\n\t" "s_waitcnt lgkmcnt(1)\n\t" XM1(XA(32, 20), GW3, GH, 1)
      ".if xk & 1\n\t" "v_cmp_lt_u32 vcc, s39, v22\n\t" "s_cbranch_vccnz .Lx%=\n\t" ".endif\n\t"
      ".if xk < 16\n\t" GLOAD("s38", "xk+16") "buffer_load_dwordx4 " GH ", v17, %[rf], %[tq] offen sc1\n\t" ".endif\n\t"
      ".set xk, xk+1\n\t.endr\n\t"
      "s_nop 7\n\t"
      GEPI("", "v12", "v15")
      "s_cmp_eq_u32 s37, 0\n\t" "s_cbranch_scc1 .Lp%=\n"
      ".Le%=:\n\t"
      WVM(0)
      GT2
      : GTO [tq] "=&s"(sl)
      : GSTIN [rs] "s"(rsx), [s0] "s"(__builtin_amdgcn_readfirstlane(grp ? GS0 + grp : 0)), [gb] "s"(FO_O - 32768 + (32 * w + rot) * 1024), [va] "v"(lane * 16 + 32768),
        [rf] "s"(rsf), [wl] "v"(wl), [rw] "v"(rw_), [rr] "v"(rr_), [oa] "v"(oa), [ob] "v"(ob), [ro] "s"(rso), [u0] "v"(8 * w + (lane & 7)), [u4] "v"((lane >> 1) & 4), [uh] "v"(PGO_A + (160 + 8 * w + (lane & 7)) * 128 + 4 * (hf & 1)), [pa] "s"(PGO_A - 29 * 8192), [hf] "s"(__builtin_amdgcn_readfirstlane(hf)), [oc] "v"((w & 1) ? OOR - 64 : oa),
        "{a[0:31]}"(xw[0]), "{a[32:63]}"(xw[1]), "{a[64:95]}"(xw[2]), "{a[96:127]}"(xw[3]),
        "{a[128:159]}"(xw[4]), "{a[160:191]}"(xw[5]), "{a[192:223]}"(xw[6]), "{a[224:255]}"(xw[7]),
        "{v[128:159]}"(w2[0]), "{v[160:191]}"(w2[1]), "{v[192:223]}"(w2[2]), "{v[224:255]}"(w2[3])
      : GSTCL "v12", "v15", "v16", "v17", "v18", "v19", "v20", "v21", "v22", "v23", "vcc", CL8(24, 25, 26, 27, 28, 29, 30, 31), CL8(32, 33, 34, 35, 36, 37, 38, 39), CL8(40, 41, 42, 43, 44, 45, 46, 47),
        CL8(48, 49, 50, 51, 52, 53, 54, 55), CL8(56, 57, 58, 59, 60, 61, 62, 63), CL64, CL72, CL80, CL88, CL96, CL112, CL120,
        "s36", "s37", "s38", "s39", "s40", "s41", "s42", "s43", "scc", "memory");
}
static_assert(GNOSSM == 1 && GNG == 3 && 4 * SU_CH + 16 <= L_TOTAL && 4 * XSLOT <= L_TOTAL, "lds");
__global__ void __launch_bounds__(256, 1)
main_kernel(const u16* __restrict__ x, const u16* __restrict__ mem0, const u16* __restrict__ Wq, const u16* __restrict__ Wk, const u16* __restrict__ Wv, const u16* __restrict__ Wo,
            unsigned char* ws, const u16* __restrict__ u, const u16* __restrict__ A_re, const u16* __restrict__ A_im, const u16* __restrict__ B_re,
            const u16* __restrict__ B_im, const u16* __restrict__ C_re, const u16* __restrict__ C_im,
            const u16* __restrict__ log_dt, const u16* __restrict__ Dskip, u16* __restrict__ out
            ) {
  __shared__ __attribute__((aligned(8192))) unsigned char lds[L_TOTAL];
  const int w = __builtin_amdgcn_readfirstlane(threadIdx.x >> 6);
  u64 pc0;
  int clen;
  {
    asm volatile("s_getpc_b64 %0\n.Lkpc0:\n\ts_mov_b32 %1, .Lkpc1-.Lkpc0" : "=s"(pc0), "=s"(clen));
  }
#define LOCAL_IDS const int lane = fresh_lane(), tid = w * 64 + lane; (void)tid;
  const int si = (blockIdx.x >> 3) * 4 + (blockIdx.x & 3);
  unsigned char* xb = (unsigned char*)(((size_t)ws + XPAGE - 1) & ~(size_t)(XPAGE - 1));
  const int fb = (int)(ws + WS_F - xb);
  u32 tag;
  asm volatile("s_load_dword %0, %1, 0x0\n\ts_waitcnt lgkmcnt(0)\n\ts_add_u32 %0, %0, 256" : "=s"(tag) : "s"(ws + WS_F) : "scc", "memory");
  const __amdgpu_buffer_rsrc_t rsx = __builtin_amdgcn_make_buffer_rsrc(xb, 0, fb + (int)WS_F_SZ, 0x00020000);
  const __amdgpu_buffer_rsrc_t rsf = __builtin_amdgcn_make_buffer_rsrc(ws + WS_QX, 0, (int)(4 * WS_QKV_SZ + 2 * WS_KM_SZ), 0x00020000);
  const __amdgpu_buffer_rsrc_t rst = __builtin_amdgcn_make_buffer_rsrc(ws + WS_UT, 0, 2 * SSM_L * SSM_D * 2 + SSM_D * 3072, 0x00020000);
  if ((blockIdx.x & 7) < 4) {
    { LOCAL_IDS
    r_role(lds, si, mem0, Wq, Wk, Wv, Wo, fb, tag, pc0, clen, rsx, rsf, rst, SsmPtrs{A_re, A_im, B_re, B_im, C_re, C_im, log_dt}, tid, lane, w
           ); }
    LOCAL_IDS
    {
      for (int it = 0; it < SPIN_CAP; ++it) {
        if (ldw(rsx, XYDN) >= 255) break;
        asm volatile("s_sleep 8" ::: "memory");
      }
      asm volatile("" ::: "memory");
      asm volatile(".Lkpc1:" ::: "memory");
      unsigned char* im = lds + 65536;
      const __amdgpu_buffer_rsrc_t rso = __builtin_amdgcn_make_buffer_rsrc((void*)out, 0, S_LEN * OUTLD * 2, 0x00020000);
      u32x4 t[8];
#pragma unroll
      for (int i = 0; i < 8; ++i) { const int p = i * 256 + tid; t[i] = __builtin_amdgcn_raw_buffer_load_b128(rst, (p >> 3) * 4096 + (p & 7) * 16, SSM_L * SSM_D * 2 + (si & 3) * 64 * 16384 + (si >> 2) * 128, 16); }
#pragma unroll
      for (int i = 0; i < 8; ++i) {
        const int p = i * 256 + tid, o = (p & 7) * (8 * 512 + 16) + ((p >> 3) & 3) * 1024 + (p >> 5) * 8;
        *(u32x2*)(im + o) = (u32x2){t[i][0], t[i][1]};
        *(u32x2*)(im + o + 512) = (u32x2){t[i][2], t[i][3]};
      }
      LBAR();
#pragma unroll
      for (int i = 0; i < 8; ++i)
        __builtin_amdgcn_raw_buffer_store_b128(*(const u32x4*)(im + i * (8 * 512 + 16) + tid * 16), rso, ((tid >> 5) * OUTLD + DM + (tid & 31) * 8) * 2,
                                               (((si >> 2) * 64 + 8 * i) * OUTLD + (si & 3) * 256) * 2, YAUX);
    }
  } else {
    const int g = blockIdx.x & 3, li = blockIdx.x >> 3;
    const int xi = g == 0 ? (li < 16 ? li : -1) : (li < 26 + (g < 3) ? 16 + 27 * (g - 1) + li : -1);
    const int ei = g == 0 ? li - 16 : 16 + 5 * (g - 1) + (31 - li);
    if (xi >= 0) {
      LOCAL_IDS
      const int head = xi & 31, mi = xi >> 5;
      u32* prog = (u32*)(xb + PGO_X + xi * 128);
      if (mi == 1) x_role<true>(lds, x, Wv, FO_V, head, rsf, prog, tag, lane, w);
      else x_role<false>(lds, x, mi == 0 ? Wk : Wq, mi == 0 ? FO_K : FO_Q, head, rsf, prog, tag, lane, w);
    } else {
      LOCAL_IDS
      u32 acc = 0;
#pragma unroll
      for (int i = 0; i < 4; ++i) acc += *(const u32*)(x + (size_t)((i * 32 + ei) * 256 + tid) * 64);
#define UV 0
#define WWARM(n, first) {    \
        const __amdgpu_buffer_rsrc_t rwo = __builtin_amdgcn_make_buffer_rsrc((void*)Wo, 0, DM * HD * 2, 0x00020000); \
        const int ldsb = (int)(size_t)(LAS unsigned char*)lds; \
        _Pragma("unroll 1") for (int c = 0; c < (n); ++c) { dma16(rwo, lane * 16, ((first) + c) * 16384, 1024, ldsb + w * 32768 + (c & 1) * 16384); waitvm<32>(); } }
      if (UV == 0 || (UV == 1 && ei < 16)) WWARM(4, (ei * 4 + w) * 4)
      if (UV == 2 && ei < 16) WWARM(8, (ei * 4 + w) * 8)
      if (ei == 0 && tid < 16) acc += *(const u32*)(Dskip + tid * 64);
      asm volatile("" :: "v"(acc));
      if (ei >= 16) {
      { const u32 t0 = rt32(); for (int i = 0; i < 1000 && (int)(rt32() - t0) < UDELAY; ++i) asm volatile("s_sleep 16" ::: "memory"); }
      asm volatile("s_waitcnt vmcnt(0)\n\ts_barrier" ::: "memory");
      for (int ps = 0; ps < 2; ++ps) {
      const int r0 = ((ei - 16) * 2 + ps) * 64;
#pragma unroll
      for (int hb = 0; hb < 2; ++hb) {
        u32x4 t[16];
#pragma unroll
        for (int i = 0; i < 16; ++i) t[i] = *(const u32x4*)(u + (size_t)r0 * SSM_D + ((hb * 16 + i) * 256 + tid) * 8);
#pragma unroll
        for (int i = 0; i < 16; ++i) { const int p = (hb * 16 + i) * 256 + tid; *(u32x4*)(lds + (p >> 7) * 2064 + (p & 127) * 16) = t[i]; }
      }
      LBAR();
      for (int i = 0; i < 32; ++i) {
        const int p = i * 256 + tid, un = p >> 5, rp = 4 * (p & 7) + ((p >> 3) & 3);
        const u32x2 a = *(const u32x2*)(lds + (2 * rp) * 2064 + un * 8), c = *(const u32x2*)(lds + (2 * rp + 1) * 2064 + un * 8);
        __builtin_amdgcn_raw_buffer_store_b128((u32x4){a[0], a[1], c[0], c[1]}, rst, (p >> 3) * 4096 + (p & 7) * 16, r0 * 2, 16);
      }
      asm volatile("s_waitcnt vmcnt(0) lgkmcnt(0)\n\ts_barrier" ::: "memory");
      }
      if (UV == 1) { WWARM(4, (ei * 4 + w) * 4) waitvm<0>(); }
      wait_start(rsx, fb, tag, true);
      if (tid == 0) __hip_atomic_fetch_add((u32*)(xb + XUDN), 1u, RLX_AGENT);
      }
    }
    asm volatile("s_waitcnt lgkmcnt(0)\n\ts_barrier" :: "s"(tag) : "memory");
    if (w == 0 && fresh_lane() == 0) __builtin_amdgcn_raw_buffer_store_b32(tag, rsx, 0, PGO_S + si * 128, 16);
    const bool isg = g == 0 ? li >= 16 : (g >= 5 - GNG && li < 16);
    if (xi >= 0 && !(GNOSSM && isg)) {
      LOCAL_IDS
      wait_start(rsx, fb, tag, true);
      for (int it = 0; it < SPIN_CAP; ++it) {
        if (ldw(rsx, XUDN) >= 15) break;
        asm volatile("s_sleep 8" ::: "memory");
      }
      asm volatile("" ::: "memory");
      int un = g == 0 ? li : g == 1 ? 16 + li : 5 + 11 * g + li;
      SsmIn in;
      const __amdgpu_buffer_rsrc_t rsd = __builtin_amdgcn_make_buffer_rsrc((void*)Dskip, 0, SSM_D * 2, 0x00020000);
      ssm_load(in, 4 * un, rst, rsd, tid, lane, w);
      if (lane < 20) *(u32x4*)(lds + w * SU_CH + (lane < 12 ? SU_U + (lane / 6) * SU_CP + (lane % 6) * 16 : SU_K + ((lane - 12) >> 2) * 5200 + (lane & 3) * 16)) = (u32x4){0u, 0u, 0u, 0u};
      SSM_GOT(in);
#define NU_
      for (bool pend = false; un < 256; pend = true NU_) un = ssm_unit(lds, 4 * un, in, pend, rst, rsx, fb, rsd, tid, lane, w);
    }
    if (isg) {
      g_role(lds, Wo, out, rsx, rsf, fb, tag, g ? g + GNG - 4 : 0, li & 15, w);
    }
    else {
      const int lane = fresh_lane();
      if (xi < 0) a_role(rsx, rsf, (ei - 16) * 4 + w, (NSEG - 4) * NH, (ei - 16) * 4 + w, 0, lane, fb, tag);
      do {
        const int t = __builtin_amdgcn_readfirstlane(lane == 0 ? __hip_atomic_fetch_add((u32*)(xb + XTK2), 1u, RLX_AGENT) + 1u : 0u);
        if (t >= 256) break;
        a_late(rsx, rsf, t, lane, fb, tag);
      } while (xi < 0);
    }
  }
}

extern "C" void kernel_launch(void* const* d_in, const int* in_sizes, int n_in, void* d_out, int out_size,
                              void* d_ws, size_t ws_size, hipStream_t stream) {
  const u16* x = (const u16*)d_in[0];
  const u16* mem0 = (const u16*)d_in[1];
  const u16* Wq = (const u16*)d_in[2];
  const u16* Wk = (const u16*)d_in[3];
  const u16* Wv = (const u16*)d_in[4];
  const u16* Wo = (const u16*)d_in[5];
  const u16* u = (const u16*)d_in[7];
  const u16* A_re = (const u16*)d_in[8];
  const u16* A_im = (const u16*)d_in[9];
  const u16* B_re = (const u16*)d_in[10];
  const u16* B_im = (const u16*)d_in[11];
  const u16* C_re = (const u16*)d_in[12];
  const u16* C_im = (const u16*)d_in[13];
  const u16* log_dt = (const u16*)d_in[14];
  const u16* Dskip = (const u16*)d_in[15];
  u16* out = (u16*)d_out;
  char* ws = (char*)d_ws;

  main_kernel<<<256, 256, 0, stream>>>(x, mem0, Wq, Wk, Wv, Wo, (unsigned char*)ws, u, A_re, A_im, B_re, B_im, C_re, C_im, log_dt, Dskip, out
      );
}
```

```cpp
#define TUNE
#define DIAG 63
#include <hip/hip_runtime.h>
#include <hip/hip_bf16.h>
#include <stdint.h>

#define S_LEN 2048
#define DM 1024
#define NH 32
#define DH 128
#define HD 4096
#define MEM 8
#define SEG 32
#define NSEG 64
#define LTOK 40
#define SSM_L 2048
#define SSM_D 1024
#define SSM_N 128
#define OUTLD 2048

typedef unsigned short u16;
typedef unsigned int u32;
typedef unsigned long long u64;
typedef __attribute__((ext_vector_type(8))) short bf16x8;
typedef __attribute__((ext_vector_type(4))) float f32x4;
typedef __attribute__((ext_vector_type(4))) unsigned int u32x4;
typedef __attribute__((ext_vector_type(2))) unsigned int u32x2;
typedef __bf16 bf16x2_t __attribute__((ext_vector_type(2)));
typedef short v4i16_t __attribute__((ext_vector_type(4)));

#define SENT 0xFFFFFFFFu
#define RLX_AGENT __ATOMIC_RELAXED, __HIP_MEMORY_SCOPE_AGENT

__device__ __forceinline__ float bf2f(u16 v) { return __uint_as_float(((u32)v) << 16); }
__device__ __forceinline__ u16 f2bf(float f) {
  u32 u = __float_as_uint(f);
  u32 r = (u + 0x7fffu + ((u >> 16) & 1u)) >> 16;
  if ((u & 0x7fffffffu) > 0x7f800000u) r = 0x7fc0u;
  return (u16)r;
}
__device__ __forceinline__ float rbf(float f) { return bf2f(f2bf(f)); }

#define TR 92
#define TP 179
#define TDB 61
#define TDC 110
#define NR1 4
#define XPAGE (2 << 20)
#define XRING 3
#define XLP(p, L) ((((L) >> 13) << 15) + ((p) << 14) + ((L) & 8191))
#define XO_R3 131072
#define XR1A(x, L) XLP((x) & 1, ((x) >> 1) * 16384 + (L))
#define XR3A(rg, L) (XO_R3 + XLP(rg, L))
#define XSB(s) (((s) % XRING) == 2 ? 1048576 : ((s) % XRING) * 262144)
#define XR2B 524288
#define XR2S 1048576
#define XR2(j) (((((j) & 31) + (((j) >> 6) << 5)) / 6 << 15) + (((((j) >> 5) & 1) ^ 1) << 14) + ((((j) & 31) + (((j) >> 6) << 5)) % 6) * 1280)
#define XSYN (1048576 + 262144)
#define PGO_R (XSYN + 32768)
#define XCAL (PGO_R + 32768)
static_assert(XCAL + 16384 <= XPAGE, "exchange page");
#define WS_X 0ull
#define NPG 2
#define WS_X_SZ ((u64)NPG * XPAGE)
#define WS_F (WS_X + WS_X_SZ)
#define XEPO (fb)
#define XSTA (fb + 128)
#define PGO_X (fb + 256)
#define PGO_Y (PGO_X + 96 * 128)
#define PGO_S (PGO_Y + 128 * 128)
#define PGO_D (PGO_S + 128 * 128)
#define PGO_A (PGO_D + 128 * 128)
#define XTKT (PGO_A + 192 * 128)
#define XUDN (XTKT + 128)
#define XYDN (XUDN + 128)
#define XTK2 (XYDN + 128)
#define XSTR(x) (XTK2 + 128 + (x) * 128)
#define WS_F_SZ ((u64)(2 + 96 + 128 + 128 + 128 + 192 + 8) * 128)
#define WS_XCH_END (WS_F + 131072)
#define WS_QX (WS_XCH_END)
#define WS_QKV_SZ ((u64)S_LEN * HD * 2)
#define WS_KX (WS_QX + WS_QKV_SZ)
#define WS_VX (WS_KX + WS_QKV_SZ)
#define WS_OX (WS_VX + WS_QKV_SZ)
#define WS_KM (WS_OX + WS_QKV_SZ)
#define WS_KM_SZ ((u64)NSEG * 8 * HD * 2)
#define WS_VM (WS_KM + WS_KM_SZ)
#define WS_UT (WS_VM + WS_KM_SZ)
#define WS_YT (WS_UT + (u64)SSM_L * SSM_D * 2)
#define WS_KD (WS_YT + (u64)SSM_L * SSM_D * 2)
#define WS_TOTAL (WS_KD + (u64)SSM_D * 3072)

#define SPIN_CAP 400000
#define TRY_CAP 20000
#define LAS __attribute__((address_space(3)))
#define LBAR() asm volatile("s_waitcnt lgkmcnt(0)\n\ts_barrier" ::: "memory")
__device__ __forceinline__ u32 max4u(u32x4 v) { return max(max(v[0], v[1]), max(v[2], v[3])); }
typedef float f32x2_t __attribute__((ext_vector_type(2)));
__device__ __forceinline__ u32 pack2(float a, float b) {
  f32x2_t v = {a, b};
  bf16x2_t r = __builtin_convertvector(v, bf16x2_t);
  return __builtin_bit_cast(u32, r);
}
__device__ __forceinline__ float rbf_hw(float a) { return __uint_as_float(pack2(a, a) & 0xffff0000u); }
__device__ __forceinline__ u32 nosent(u32 v) { return min(v, 0xFFFFFFFEu); }
template <int N> __device__ __forceinline__ void waitvm() { asm volatile("s_waitcnt vmcnt(%0)" ::"n"(N) : "memory"); }
typedef __attribute__((ext_vector_type(16))) float f32x16;
__device__ __forceinline__ u32 split_hl(float kv) {
  const u32 h = pack2(kv, kv) & 0xffffu;
  const float r1 = kv - __uint_as_float(h << 16);
  return h | (pack2(r1, r1) & 0xffff0000u);
}
__device__ __forceinline__ int kidx(int idx) {
  const int sh = idx < 256 ? 0 : (idx < 512 ? 1 : (idx < 1024 ? 2 : 3));
  const int q = sh ? ((idx + (1 << (sh - 1)) - 1 + ((idx >> sh) & 1)) >> sh) : idx;
  return q + 128 * sh;
}

__device__ u32* g_dbg;
#define FO_Q 0
#define FO_K ((int)WS_QKV_SZ)
#define FO_V ((int)(2 * WS_QKV_SZ))
#define FO_O ((int)(3 * WS_QKV_SZ))
#define FO_KM ((int)(4 * WS_QKV_SZ))
#define FO_VM ((int)(4 * WS_QKV_SZ + WS_KM_SZ))
__device__ __forceinline__ u32x4 wait_start(__amdgpu_buffer_rsrc_t rsx, int fb, u32 tag, bool slow, int xcd = 0) {
  u32x4 st;
  for (int it = 0; it < SPIN_CAP; ++it) {
    asm volatile("" ::: "memory");
    st = __builtin_amdgcn_raw_buffer_load_b128(rsx, 0, slow ? XSTA : XSTR(xcd), 16);
    if ((u32)__builtin_amdgcn_readfirstlane(st[3]) == tag) break;
    if (slow) asm volatile("s_sleep 20" ::: "memory"); else asm volatile("s_sleep 1" ::: "memory");
  }
  asm volatile("" ::: "memory");
  return st;
}
__device__ __forceinline__ int xprog(u32 v, u32 tag) { return v - tag <= 80u ? (int)(v - tag) : -1; }
__device__ __forceinline__ float xmax4(float m) {
  { auto r = __builtin_amdgcn_permlane16_swap(__float_as_uint(m), __float_as_uint(m), false, false); m = __builtin_fmaxf(__uint_as_float(r[0]), __uint_as_float(r[1])); }
  { auto r = __builtin_amdgcn_permlane32_swap(__float_as_uint(m), __float_as_uint(m), false, false); m = __builtin_fmaxf(__uint_as_float(r[0]), __uint_as_float(r[1])); }
  return m;
}
__device__ __forceinline__ float xsum4(float m) {
  { auto r = __builtin_amdgcn_permlane16_swap(__float_as_uint(m), __float_as_uint(m), false, false); m = __uint_as_float(r[0]) + __uint_as_float(r[1]); }
  { auto r = __builtin_amdgcn_permlane32_swap(__float_as_uint(m), __float_as_uint(m), false, false); m = __uint_as_float(r[0]) + __uint_as_float(r[1]); }
  return m;
}
#define MF16(a, b, c) __builtin_amdgcn_mfma_f32_16x16x32_bf16(__builtin_bit_cast(bf16x8, a), __builtin_bit_cast(bf16x8, b), c, 0, 0, 0)
__device__ __forceinline__ f32x4 rnd2(f32x4 v, float sc) {
  const u32 a = pack2(v[0], v[1]), b = pack2(v[2], v[3]);
  const u32 c = pack2(__uint_as_float(a << 16) * sc, __uint_as_float(a & 0xffff0000u) * sc), d = pack2(__uint_as_float(b << 16) * sc, __uint_as_float(b & 0xffff0000u) * sc);
  return (f32x4){__uint_as_float(c << 16), __uint_as_float(c & 0xffff0000u), __uint_as_float(d << 16), __uint_as_float(d & 0xffff0000u)};
}
struct XaOps { u32x4 q[2][4], k[2][4], v[8]; };
__device__ __forceinline__ void xa_load(XaOps& o, __amdgpu_buffer_rsrc_t rsf, int sh, int lane) {
#pragma unroll
  for (int i = 0; i < 8; ++i) {
    o.q[i >> 2][i & 3] = __builtin_amdgcn_raw_buffer_load_b128(rsf, lane * 16, FO_Q + (sh * 8 + i) * 1024, 16);
    o.k[i >> 2][i & 3] = __builtin_amdgcn_raw_buffer_load_b128(rsf, lane * 16, FO_K + (sh * 8 + i) * 1024, 16);
  }
#pragma unroll
  for (int dt = 0; dt < 8; ++dt) o.v[dt] = __builtin_amdgcn_raw_buffer_load_b128(rsf, lane * 16, FO_V + (sh * 8 + dt) * 1024, 16);
}
struct XaPre { f32x4 a[2], b[2]; float mx[2], tx[2]; };
__device__ __forceinline__ void xa_pre(XaPre& p, const XaOps& o, int lane) {
  const int lr = lane & 15, lg = lane >> 4;
  const float scale = 0.08838834764831845f, L2E = 1.4426950408889634f, NEG = -1000000000.0f;
  const f32x4 z = (f32x4){0, 0, 0, 0};
  f32x4 s00 = z, s10 = z, s11 = z;
#pragma unroll
  for (int ks = 0; ks < 4; ++ks) { s00 = MF16(o.k[0][ks], o.q[0][ks], s00); s10 = MF16(o.k[0][ks], o.q[1][ks], s10); s11 = MF16(o.k[1][ks], o.q[1][ks], s11); }
  s00 = rnd2(s00, scale); s10 = rnd2(s10, scale); s11 = rnd2(s11, scale);
  asm volatile("" : "+v"(s00), "+v"(s11));
  float m0 = NEG, m1 = -3.0e38f;
#pragma unroll
  for (int e = 0; e < 4; ++e) {
    const bool msk = 4 * lg + e > lr;
    s00[e] = msk ? NEG : s00[e]; s11[e] = msk ? NEG : s11[e];
    m0 = __builtin_fmaxf(m0, s00[e]); m1 = __builtin_fmaxf(m1, __builtin_fmaxf(s10[e], s11[e]));
  }
  m0 = xmax4(m0); m1 = xmax4(m1);
  const float em = __builtin_amdgcn_exp2f((NEG - m0) * L2E);
  float t0 = 4.f * em, t1 = 0.f;
#pragma unroll
  for (int e = 0; e < 4; ++e) {
    s00[e] = __builtin_amdgcn_exp2f((s00[e] - m0) * L2E); s10[e] = __builtin_amdgcn_exp2f((s10[e] - m1) * L2E); s11[e] = __builtin_amdgcn_exp2f((s11[e] - m1) * L2E);
    t0 += s00[e]; t1 += s10[e] + s11[e];
  }
  p.a[0] = s00; p.b[0] = (f32x4){em, em, em, em}; p.a[1] = s10; p.b[1] = s11; p.mx[0] = m0; p.mx[1] = m1; p.tx[0] = t0; p.tx[1] = t1;
}
__device__ __forceinline__ void xa_pw(u32x4& px, u32x2& pm, f32x4 sm, f32x4 sa, f32x4 sb, float mx, float tx, int lg) {
  const float scale = 0.08838834764831845f, L2E = 1.4426950408889634f;
  sm = rnd2(sm, scale);
  asm("" : "+v"(sm));
  float m = mx;
#pragma unroll
  for (int e = 0; e < 4; ++e) { sm[e] = lg >= 2 ? -INFINITY : sm[e]; m = __builtin_fmaxf(m, sm[e]); }
  m = xmax4(m);
  const float al = __builtin_amdgcn_exp2f((mx - m) * L2E);
  float ts = tx * al;
#pragma unroll
  for (int e = 0; e < 4; ++e) { sm[e] = __builtin_amdgcn_exp2f((sm[e] - m) * L2E); ts += sm[e]; }
  const float r = __builtin_amdgcn_rcpf(xsum4(ts)), ra = r * al;
  px = (u32x4){pack2(sa[0] * ra, sa[1] * ra), pack2(sa[2] * ra, sa[3] * ra), pack2(sb[0] * ra, sb[1] * ra), pack2(sb[2] * ra, sb[3] * ra)};
  pm = (u32x2){pack2(sm[0] * r, sm[1] * r), pack2(sm[2] * r, sm[3] * r)};
}
__device__ __forceinline__ void xa_pv(const u32x4 (&v)[8], const u32x2 (&vm)[8], u32x4 px, u32x2 pm, __amdgpu_buffer_rsrc_t rsf, int vo, int so) {
  f32x4 o[8];
#define LX(i) "v_mfma_f32_16x16x32_bf16 %" #i ", %[v" #i "], %[px], 0\n\t"
#define LM(i) "v_mfma_f32_16x16x16_bf16 %" #i ", %[m" #i "], %[pm], %" #i "\n\t"
  asm volatile("s_nop 1\n\t" LX(0) LX(1) LX(2) LX(3) LX(4) LX(5) LX(6) LX(7) LM(0) LM(1) LM(2) LM(3) LM(4) LM(5) LM(6) LM(7) "s_nop 7"
               : "=&v"(o[0]), "=&v"(o[1]), "=&v"(o[2]), "=&v"(o[3]), "=&v"(o[4]), "=&v"(o[5]), "=&v"(o[6]), "=&v"(o[7])
               : [v0] "v"(v[0]), [v1] "v"(v[1]), [v2] "v"(v[2]), [v3] "v"(v[3]), [v4] "v"(v[4]), [v5] "v"(v[5]), [v6] "v"(v[6]), [v7] "v"(v[7]), [px] "v"(px),
                 [m0] "v"(vm[0]), [m1] "v"(vm[1]), [m2] "v"(vm[2]), [m3] "v"(vm[3]), [m4] "v"(vm[4]), [m5] "v"(vm[5]), [m6] "v"(vm[6]), [m7] "v"(vm[7]), [pm] "v"(pm));
#pragma unroll
  for (int pr = 0; pr < 4; ++pr)
    __builtin_amdgcn_raw_buffer_store_b128((u32x4){pack2(o[2 * pr][0], o[2 * pr][1]), pack2(o[2 * pr][2], o[2 * pr][3]), pack2(o[2 * pr + 1][0], o[2 * pr + 1][1]), pack2(o[2 * pr + 1][2], o[2 * pr + 1][3])},
                                           rsf, vo, so + pr * 1024, 16);
}
__device__ __forceinline__ u32 xa_chk(const u32x4 (&km)[4], const u32x2 (&vm)[8]) {
  u32 ck = max(max(max4u(km[0]), max4u(km[1])), max(max4u(km[2]), max4u(km[3])));
#pragma unroll
  for (int i = 0; i < 8; ++i) ck = max(ck, max(vm[i][0], vm[i][1]));
  return ck;
}
#define XS0 48
#define XL_D 26
#define NAW 64
__device__ __forceinline__ void a_role(__amdgpu_buffer_rsrc_t rsx, __amdgpu_buffer_rsrc_t rsf, int sh, int lim, int wi, int done, int lane, int fb, u32 tag) {
  const int lr = lane & 15, lg = lane >> 4;
  const int okm = (lg * 8 + (lr & 7)) * 16, ovm = lg < 2 ? (lg * 16 + lr) * 8 : 0x7ffffff0;
  int rp = 0, xp = -1, spin = 0, pub = done, miss = 0, tp = 200;
  u32 tseen = 0, tq0 = 0, tq1 = 0, tq2 = 0, tq3 = 0; (void)tq0; (void)tq1; (void)tq2; (void)tq3;
#define XA_WAITX(s_)                                                                  \
  while (xp <= ((s_) >> 5)) {                                                                                          \
    asm volatile("" ::: "memory");                                                                                     \
    int pv = min(xprog(__builtin_amdgcn_raw_buffer_load_b32(rsx, lane * 128, PGO_X, 16), tag), xprog(__builtin_amdgcn_raw_buffer_load_b32(rsx, (64 + (lane & 31)) * 128, PGO_X, 16), tag)); \
    _Pragma("unroll") for (int o_ = 1; o_ < 64; o_ *= 2) pv = min(pv, __shfl_xor(pv, o_));                             \
    xp = __builtin_amdgcn_readfirstlane(pv);                                                                           \
    if (xp > ((s_) >> 5) || ++spin > SPIN_CAP) break;                                                                  \
    asm volatile("s_sleep 16" ::: "memory");                                                                           \
  }
#define XA_PUB() if (pub != done) { pub = done; asm volatile("s_nop 4\n\tbuffer_store_dword %0, %1, %2, %3 offen sc1" :: "v"(done), "v"(lane < 2 ? lane * 4 : 0x7ffffff0), "s"(rsx), "s"(PGO_A + wi * 128) : "memory"); }
#define XA_NOW ((u32)__builtin_amdgcn_s_memrealtime())
#define XA_SEEN(r_, l_) { const u32 n_ = XA_NOW; tseen = n_ - ((n_ - (r_ >> 16)) & 0x7fffu) + XA_LAG; tp = max(r_ & 1023, 1); rp = l_; }
#define XA_STEP(CUR, NXT) \
  { \
    const int seg = sh >> 5, nx = sh + NAW; \
    XA_WAITX(min(nx, lim - 1)) \
    u32x4 km[4]; u32x2 vm[8]; \
    int tries = (seg >= XS0 && tseen) ? 3 : 0; \
    while (true) {               \
      if (rp > seg) tries = 0; \
      else if (tries) { \
        const u32 due = tseen + (u32)(seg - rp) * tp + XL_D - XA_LAG; \
        for (int i_ = 0; i_ < 4000 && (int)(due - XA_NOW) > 0; ++i_) asm volatile("s_sleep 1" ::: "memory"); \
      } else while (true) { \
        int want = seg + 1; \
        if (tseen) { \
          const u32 due = tseen + (u32)(want - rp) * tp; \
          for (int i_ = 0; i_ < 4000 && (int)(due - XA_NOW) > 0; ++i_) asm volatile("s_sleep 2" ::: "memory"); \
          if (!miss) want = min(max(want, rp + max((int)(XA_NOW - tseen), 0) / tp), NSEG - 1); \
        } \
        asm volatile("" ::: "memory"); \
        const int r_ = __builtin_amdgcn_readfirstlane(__builtin_amdgcn_raw_buffer_load_b32(rsx, 0, pgr + want * 128, 16)); \
        if (r_ >= 0) { XA_SEEN(r_, want) miss = 0; break; } \
        if (++spin > SPIN_CAP) break; \
        if (miss++) asm volatile("s_sleep 20" ::: "memory"); \
      } \
      asm volatile("" ::: "memory"); \
      XA_DBG2 \
      _Pragma("unroll") for (int i = 0; i < 4; ++i) km[i] = __builtin_amdgcn_raw_buffer_load_b128(rsf, okm, FO_KM + (sh * 4 + i) * 512, 16); \
      _Pragma("unroll") for (int i = 0; i < 8; ++i) vm[i] = __builtin_amdgcn_raw_buffer_load_b64(rsf, ovm, FO_VM + (sh * 8 + i) * 256, 16); \
        \
      const int pk_ = min(rp + max((int)(XA_NOW - tseen), 0) / tp, NSEG - 1); \
      const u32 pv_ = __builtin_amdgcn_raw_buffer_load_b32(rsx, 0, pgr + pk_ * 128, 16); \
      asm volatile("" ::: "memory"); \
      __builtin_amdgcn_s_waitcnt(0x0F70);          \
      asm volatile("" ::: "memory"); \
      { const int r_ = __builtin_amdgcn_readfirstlane(pv_); if (r_ >= 0) XA_SEEN(r_, pk_) } \
      if (!tries || __ballot(xa_chk(km, vm) == SENT) == 0) break; \
      --tries; \
    } \
    if (nx < lim) xa_load(NXT, rsf, nx, lane); \
    XA_DBG3 \
    __builtin_amdgcn_sched_barrier(0); \
    f32x4 s0 = (f32x4){0, 0, 0, 0}, s1 = s0; \
    _Pragma("unroll") for (int ks = 0; ks < 4; ++ks) { s0 = MF16(km[ks], CUR.q[0][ks], s0); s1 = MF16(km[ks], CUR.q[1][ks], s1); } \
    u32x4 px0, px1; u32x2 pm0, pm1; \
    xa_pw(px0, pm0, s0, P.a[0], P.b[0], P.mx[0], P.tx[0], lg); \
    xa_pw(px1, pm1, s1, P.a[1], P.b[1], P.mx[1], P.tx[1], lg); \
    xa_pv(CUR.v, vm, px0, pm0, rsf, lane * 16, FO_O + (seg * 256 + (sh & 31) * 4) * 1024); \
    xa_pv(CUR.v, vm, px1, pm1, rsf, lane * 16, FO_O + (seg * 256 + 128 + (sh & 31) * 4) * 1024); \
    ++done; \
    XA_DBG XA_DBG5 \
    __builtin_amdgcn_sched_barrier(0); \
    sh = nx; \
    if (sh >= lim) break; \
    xa_pre(P, NXT, lane); \
    __builtin_amdgcn_s_waitcnt(0x0F70); \
    XA_PUB() \
  }
#define XA_LAST 22
#define XA_LAG 30
#define XA_DBG tq2 = (u32)__builtin_amdgcn_s_memrealtime();
#define XA_DBG2 tq0 = (u32)__builtin_amdgcn_s_memrealtime();
#define XA_DBG3 tq1 = (u32)__builtin_amdgcn_s_memrealtime();
#define XA_DBG5 if ((sh & 31) == 5 && lane == 0) { g_dbg[3400 + seg] = tq2; g_dbg[3330 + seg] = ((tq1 - tq0) << 16) | (tq2 - tq1); }
#define XA_DBG4 tq3 = tq3 ? tq3 : (seg == 63 ? (u32)__builtin_amdgcn_s_memrealtime() : 0u);
  const int pgr = PGO_R + (__builtin_amdgcn_readfirstlane(wait_start(rsx, fb, tag, true)[2]) & 8192);
  XaOps A, B;
  XaPre P;
  XA_WAITX(sh)
  asm volatile("" ::: "memory");
  xa_load(A, rsf, sh, lane);
  xa_pre(P, A, lane);
  while (true) { XA_STEP(A, B) XA_STEP(B, A) }
  asm volatile("s_waitcnt vmcnt(0)" ::: "memory");
  XA_PUB()
}

#define GL_D 130
__device__ __forceinline__ void a_late(__amdgpu_buffer_rsrc_t rsx, __amdgpu_buffer_rsrc_t rsf, int t, int lane, int fb, u32 tag) {
  const int lr = lane & 15, lg = lane >> 4, rt = t & 1, sh = (NSEG - 4) * NH + (t >> 1), seg = sh >> 5;
  const float scale = 0.08838834764831845f, L2E = 1.4426950408889634f, NEG = -1000000000.0f;
  int xp = -1, spin = 0;
  const int pgr = PGO_R + (__builtin_amdgcn_readfirstlane(wait_start(rsx, fb, tag, true)[2]) & 8192);
  const int r40 = __builtin_amdgcn_readfirstlane(__builtin_amdgcn_raw_buffer_load_b32(rsx, 0, pgr + 40 * 128, 16));
  XA_WAITX(sh)
  asm volatile("" ::: "memory");
  u32x4 q[4], v[8];
  f32x4 sa = (f32x4){0, 0, 0, 0}, sb = sa;
  float mx = -3.0e38f, tx = 0.f;
  {
    u32x4 k0[4], k1[4];
#pragma unroll
    for (int i = 0; i < 4; ++i) {
      q[i] = __builtin_amdgcn_raw_buffer_load_b128(rsf, lane * 16, FO_Q + (sh * 8 + rt * 4 + i) * 1024, 16);
      k0[i] = __builtin_amdgcn_raw_buffer_load_b128(rsf, lane * 16, FO_K + (sh * 8 + i) * 1024, 16);
      k1[i] = __builtin_amdgcn_raw_buffer_load_b128(rsf, lane * 16, FO_K + (sh * 8 + 4 + i) * 1024, 16);
    }
#pragma unroll
    for (int dt = 0; dt < 8; ++dt) v[dt] = __builtin_amdgcn_raw_buffer_load_b128(rsf, lane * 16, FO_V + (sh * 8 + dt) * 1024, 16);
#pragma unroll
    for (int ks = 0; ks < 4; ++ks) { sa = MF16(k0[ks], q[ks], sa); sb = MF16(k1[ks], q[ks], sb); }
    sa = rnd2(sa, scale); sb = rnd2(sb, scale);
    asm volatile("" : "+v"(sa), "+v"(sb));
#pragma unroll
    for (int e = 0; e < 4; ++e) {
      sa[e] = 4 * lg + e > lr + 16 * rt ? NEG : sa[e]; sb[e] = 16 + 4 * lg + e > lr + 16 * rt ? NEG : sb[e];
      mx = __builtin_fmaxf(mx, __builtin_fmaxf(sa[e], sb[e]));
    }
    mx = xmax4(mx);
#pragma unroll
    for (int e = 0; e < 4; ++e) { sa[e] = __builtin_amdgcn_exp2f((sa[e] - mx) * L2E); sb[e] = __builtin_amdgcn_exp2f((sb[e] - mx) * L2E); tx += sa[e] + sb[e]; }
  }
  const int okm = (lg * 8 + (lr & 7)) * 16, ovm = lg < 2 ? (lg * 16 + lr) * 8 : 0x7ffffff0;
  const int ofl = seg + 1 < NSEG ? pgr + (seg + 1) * 128 : PGO_D + ((sh & 31) * 4 + (lane & 3)) * 128;
  const int owd = lane ? 0x7ffffff0 : PGO_A + (64 + (t >> 1)) * 128 + 4 * rt;
  u32 tq0 = 0, tq1 = 0, tq2 = 0; (void)tq0; (void)tq1; (void)tq2;
  int att = 0, seen = 1;
  while (true) {
    asm volatile("" : "+s"(att));
    if (att == 1) {
      u32 n = (u32)__builtin_amdgcn_s_memrealtime(), due = n;
      if (r40 >= 0) due = n - ((n - (r40 >> 16)) & 0x7fffu) + XA_LAG + (seg - 41) * (r40 & 1023);
      int r;
      while (true) {
        for (int i = 0; i < 4000 && (int)(due - (u32)__builtin_amdgcn_s_memrealtime()) > 0; ++i) asm volatile("s_sleep 2" ::: "memory");
        asm volatile("" ::: "memory");
        r = __builtin_amdgcn_readfirstlane(__builtin_amdgcn_raw_buffer_load_b32(rsx, 0, pgr + (seg - 1) * 128, 16));
        if (r >= 0 || ++spin > SPIN_CAP) break;
        asm volatile("s_sleep 10" ::: "memory");
      }
      n = (u32)__builtin_amdgcn_s_memrealtime();
      due = n - ((n - (r >> 16)) & 0x7fffu) + (r & 1023) + XL_D - (seg + 1 < NSEG ? 0 : XA_LAST);
      __builtin_amdgcn_raw_buffer_store_b32(((n + max((int)(due - n), 0) + GL_D) & ~1u) | 0x80000000u, rsx, owd, 0, 16);
      for (int i = 0; i < 4000 && (int)(due - (u32)__builtin_amdgcn_s_memrealtime()) > 0; ++i) asm volatile("s_sleep 1" ::: "memory");
      XA_DBG2
    } else if (att > 6) asm volatile("s_sleep 20" ::: "memory");
    const int dr = att ? 0 : 0x7ffffff0;
    asm volatile("" ::: "memory");
    u32x4 km[4]; u32x2 vm[8];
#pragma unroll
    for (int i = 0; i < 4; ++i) km[i] = __builtin_amdgcn_raw_buffer_load_b128(rsf, okm | dr, FO_KM + (sh * 4 + i) * 512, 16);
#pragma unroll
    for (int i = 0; i < 8; ++i) vm[i] = __builtin_amdgcn_raw_buffer_load_b64(rsf, ovm | dr, FO_VM + (sh * 8 + i) * 256, 16);
    const u32 fl = __builtin_amdgcn_raw_buffer_load_b32(rsx, att > 1 ? ofl : 0x7ffffff0, 0, 16);
    f32x4 sm = (f32x4){0, 0, 0, 0};
#pragma unroll
    for (int ks = 0; ks < 4; ++ks) sm = MF16(km[ks], q[ks], sm);
    const u32 ck = xa_chk(km, vm);
    u64 bad = __ballot(ck == SENT), nf = __ballot(fl == SENT);
    asm volatile("" : "+s"(bad), "+s"(nf));
    if (!seen && bad != 0) {
      seen = (att > 1 && nf == 0) || ++spin > SPIN_CAP;
      ++att;
      continue;
    }
    XA_DBG3
    u32x4 px; u32x2 pm;
    xa_pw(px, pm, sm, sa, sb, mx, tx, lg);
    xa_pv(v, vm, px, pm, rsf, (lane * 16) | dr, FO_O + (seg * 256 + rt * 128 + (sh & 31) * 4) * 1024);
    XA_DBG
    asm volatile("s_nop 4\n\ts_waitcnt vmcnt(0)\n\tbuffer_store_dword %0, %1, %2, 0 offen sc1" :: "v"(32), "v"(owd | dr), "s"(rsx) : "memory");
    if (att) break;
    att = 1; seen = 0;
  }
  if (lane == 0) { u32* d = g_dbg + 6200 + t * 6; d[0] = tq0; d[1] = tq1; d[2] = tq2; d[3] = (u32)__builtin_amdgcn_s_memrealtime(); d[4] = att; }
  if (lane == 0 && t >= 192) g_dbg[3632 + ((t - 192) >> 1)] = (u32)__builtin_amdgcn_s_memrealtime();
}

typedef __attribute__((ext_vector_type(32))) unsigned int u32x32;
typedef __attribute__((ext_vector_type(16))) unsigned int u32x16;
typedef __attribute__((ext_vector_type(8))) unsigned int u32x8;
#define STR2(x) #x
#define STR(x) STR2(x)
#define RNG(f, b, n) f "[" STR(b) ":" STR((b) + (n)-1) "]"
#define REG(b) "v[" STR(b) "]"
#define LR_RED 16384
#define LR_CTL LR_RED
#define LR_STG 32768
#define L_RSTAMP LR_STG
#define STAMPV(i, v)
#define EXF (NH * 8 * 1024)
#define EXK 65536
#define OOR 0x7ffffff0

__device__ __forceinline__ u32 lds_word(int addr) {
  u32 v;
  asm volatile("ds_read_b32 %0, %1\n\ts_waitcnt lgkmcnt(0)" : "=v"(v) : "v"(addr) : "memory");
  return __builtin_amdgcn_readfirstlane(v);
}
__device__ __forceinline__ int fresh_lane() {
  int l;
  asm volatile("v_mbcnt_lo_u32_b32 %0, -1, 0\n\tv_mbcnt_hi_u32_b32 %0, -1, %0" : "=v"(l));
  return l;
}
__device__ __forceinline__ u32 rt32() { return (u32)__builtin_amdgcn_s_memrealtime(); }
int g_tune[4] = {TR, TP, TDB, TDC};
#undef TR
#undef TP
#undef TDB
#undef TDC
#define TR tun_r
#define TP tun_p
#define TDB tun_b
#define TDC tun_c
#define SYO(s) (XSYN + flip + (s) * 64)
#define TKS 0.85f
#define TLB 1
#define TLC 1
#define TLP 2
#define KG0 0
#define CLS0 0u
#define APC 5
#define ADL 12
#define CALN 2
#define TE1 3
#define TE2 1
#define TE3 1
#define XTH 4
#define TS3 42
#define TE0 45
#define TD0 170
#define TD 120
#define PHASE 1
#define XSK 0
#define CLKS(T, R, X) WLG(2) "s_sub_u32 s49, " T ", " R "\n\t" "s_cmp_le_i32 s49, " STR(3 * PHASE) "\n\t" "s_cbranch_scc1 " X "\n\t"
#define CLKH(OFF) WLG(0) "s_memrealtime s[72:73]\n\t" "s_add_u32 s50, s43, s44\n\t" "s_add_u32 s50, s50, " OFF "\n\t" "s_nop 1\n\t" "s_memrealtime s[74:75]\n\t"
#define CLKL(L, T)   L ":\n\t" "s_memrealtime s[76:77]\n\t" CLKS(T, "s72", L "x") "s_memrealtime s[72:73]\n\t" CLKS(T, "s74", L "x")     \
                     "s_memrealtime s[74:75]\n\t" CLKS(T, "s76", L "x") "s_branch " L "\n" \
                     L "x:\n\t" CLKP(L)
#define CLKW(L, OFF) CLKH(OFF) "s_nop 3\n" CLKL(L, "s50")
#define CLKP(L) "s_cmp_lt_i32 s49, 1\n\t" "s_cbranch_scc1 " L "y\n\t" "s_nop " STR(PHN) "\n\t" "s_cmp_lt_i32 s49, 2\n\t" "s_cbranch_scc1 " L "y\n\t" "s_nop " STR(PHN) "\n\t" \
                "s_cmp_lt_i32 s49, 3\n\t" "s_cbranch_scc1 " L "y\n\t" "s_nop " STR(PHN) "\n\t" "s_nop 0\n\t" "s_branch " L "y\n\t" "s_nop 0\n" L "y:\n\t"
#define PHN 3
#define REL(L) "s_barrier\n" L ":\n\t"
#define RELV(L) "s_cbranch_vccnz " L "\n\t" REL(L)
#define RELN(L) "s_cmp_eq_u32 %[wv], 0\n\t" "s_cbranch_scc1 " L "a\n\t" "s_cmp_eq_u32 %[wv], 1\n\t" "s_cbranch_scc1 " L "b\n\t" \
                "s_barrier\n\t" "s_nop " STR(PD2) "\n\t" "s_branch " L "\n" L "b:\n\t" "s_barrier\n\t" "s_branch " L "\n" L "a:\n\t" "s_barrier\n" L ":\n\t"
#define PD2 2
#define TS_(n) "s_memtime s[80+2*" #n ":81+2*" #n "]\n\t"
#define TSA(n)
#define TSB(n)
#define TSW(n)
#define TVM 12
template <int NTL, int KS>
__device__ __forceinline__ void tr_chunk(const u16* S, int lr, int lg, u32 (&r)[NTL][KS][8]) {
  const int q = lr >> 2, p = lr & 3;
#pragma unroll
  for (int t = 0; t < NTL; ++t)
#pragma unroll
    for (int i = 0; i < KS; ++i)
#pragma unroll
      for (int kb = 0; kb < 2; ++kb) {
        const u16* base = S + (64 * i + 32 * kb + 16 * (lg >> 1) + 4 * (lg & 1) + (q & 1) + 8 * (q >> 1)) * (16 * NTL) + 16 * t + 4 * p;
        const u32x2 t0 = __builtin_bit_cast(u32x2, __builtin_amdgcn_ds_read_tr16_b64_v4i16((LAS v4i16_t*)(base)));
        const u32x2 t1 = __builtin_bit_cast(u32x2, __builtin_amdgcn_ds_read_tr16_b64_v4i16((LAS v4i16_t*)(base + 2 * 16 * NTL)));
        r[t][i][4 * kb + 0] = t0[0]; r[t][i][4 * kb + 1] = t0[1]; r[t][i][4 * kb + 2] = t1[0]; r[t][i][4 * kb + 3] = t1[1];
      }
}
#define SMF(acc, a, bf, bi) "v_smfmac_f32_16x16x64_bf16 " RNG("v", acc, 4) ", " RNG("v", a, 4) ", " RNG(bf, bi, 8) ", v34\n\t"
#define ZER(r) "v_mov_b64 " RNG("v", r, 2) ", 0\n\t" "v_mov_b64 " RNG("v", (r) + 2, 2) ", 0\n\t"
#define GLD(r, o) "s_xor_b32 s49, s50, " STR(o) "\n\t" "buffer_load_dwordx4 " RNG("v", r, 4) ", v32, %[rs], s49 offen sc1\n\t"
#define GL1(r, o) "s_xor_b32 s49, s50, " STR(o) "\n\t" "buffer_load_dwordx4 " RNG("v", r, 4) ", v33, %[rs], s49 offen sc1\n\t"
#define GL2(r, o) GL1(r, 4096 + (o))
#define MXA0(r) "v_max3_u32 v63, " REG(r) ", " REG((r) + 1) ", " REG((r) + 1) "\n\t"
#define MXA(r) "v_max3_u32 v63, v63, " REG(r) ", " REG((r) + 1) "\n\t"
#define MXB(r) "v_max3_u32 v63, v63, " REG((r) + 2) ", " REG((r) + 3) "\n\t"
#define CMPB "v_cmp_eq_u32 vcc, s54, v63\n\t"
#define PSA(r) "v_pk_add_f32 " RNG("v", r, 2) ", " RNG("v", r, 2) ", " RNG("v", (r) + 2, 2) "\n\t"
#define PSW(r, o) "ds_write_addtid_b32 " REG(r) " offset:" STR(o) "\n\t"
#define RDQ(r, o) "ds_read_b128 " RNG("v", r, 4) ", v37 offset:" STR(o) "\n\t"
#define SQ(t, i) SMF(64 + 4 * (t), 96 + 4 * (i), "a", ((i) * 4 + (t)) * 8)
#define SV(t, i) SMF(24 + 4 * (t), 96 + 4 * (i), "a", 128 + ((i) * 2 + (t)) * 8)
#define SVP(t, i) SV(t, i)
#define SW(a, q, i) SMF((a) + 4 * (q), 96 + 4 * (i), "v", 128 + ((i) * 2 + (q)) * 8)
#define WVM(n) "s_waitcnt vmcnt(" STR(n) ")\n\t"
#define WLG(n) "s_waitcnt lgkmcnt(" STR(n) ")\n\t"
#define WLGF(n) WLG(n)
#define PACE 7
#define PC "s_nop " STR(PACE) "\n\t"
#define PC6
#define PC7
#define WBLK(a, n, i, M0) WVM((n) + 7 - (i)) SW(a, 0, i) M0(96 + 4 * (i)) SW(a, 1, i) MXB(96 + 4 * (i))
#define CL8(a, b, c, d, e, f, g, h) "v" #a, "v" #b, "v" #c, "v" #d, "v" #e, "v" #f, "v" #g, "v" #h
#define CL64 CL8(64, 65, 66, 67, 68, 69, 70, 71)
#define CL72 CL8(72, 73, 74, 75, 76, 77, 78, 79)
#define CL80 CL8(80, 81, 82, 83, 84, 85, 86, 87)
#define CL88 CL8(88, 89, 90, 91, 92, 93, 94, 95)
#define CL96 CL8(96, 97, 98, 99, 100, 101, 102, 103), CL8(104, 105, 106, 107, 108, 109, 110, 111)
#define CL112 CL8(112, 113, 114, 115, 116, 117, 118, 119)
#define CL120 CL8(120, 121, 122, 123, 124, 125, 126, 127)
#define PKA(d, a, b) "v_pk_add_f32 " RNG("v", d, 2) ", " RNG("v", a, 2) ", " RNG("v", b, 2) "\n\t"
#define CNT(o) STR(4096 + 64 * (o))
#define ARRIVE(o) "ds_add_u32 v45, v39 offset:" CNT(o) "\n\t"

#define EXTRA "v_readlane_b32 s69, v35, s45\n\t" "s_and_b32 s56, s58, 1023\n\t" "s_bfe_u32 s57, s58, 0xa000a\n\t" "s_lshr_b32 s55, s58, 20\n\t" "s_add_u32 s66, s56, s69\n\t" "s_add_u32 s67, s57, s69\n\t" "s_add_u32 s67, s67, s69\n\t"
#define TEX(s) ((s) == 0 ? TE0 : ((s) < 4 ? TE1 : ((s) < 12 ? TE2 : ((s) < TS3 ? TE3 : 0))))
#define R2BASE "s_bitcmp1_b32 s45, 0\n\t" "s_cselect_b32 s71, " STR(XR2S) ", 0\n\t" "s_add_u32 s71, s71, %[r2]\n\t"
#define X4B 0
#define X4C 0
#define STB "buffer_store_dwordx2 v[116:117], v11, %[rs], s36 offen sc1\n"
#define STC "buffer_store_dwordx2 v[80:81], v17, %[rs], s36 offen sc1\n\t"
#define R2TEXT                                                                                                     \
              TSB(0)                                                                                               \
              "buffer_load_dwordx4 v[64:67], v43, %[rs], s71 offen sc1\n\t"                                      \
              "buffer_load_dwordx4 v[68:71], v8, %[rs], s71 offen sc1\n\t"                                       \
              "buffer_load_dwordx4 v[76:79], v9, %[rs], s71 offen sc1\n\t"                                       \
              "buffer_load_dwordx4 v[60:63], v10, %[rs], s71 offen sc1\n\t"                                      \
              "buffer_store_dwordx4 v[72:75], v47, %[rf], s41 offen sc1\n\t"                                     \
              "s_cbranch_vccnz .Lvw%=\n\t" VPOST ARRIVE(12) "s_barrier\n" ".Lvw%=:\n\t"                             \
              "ds_read2_b32 v[96:97], v12 offset0:0 offset1:4\n\t" "ds_read2_b32 v[98:99], v12 offset0:32 offset1:36\n\t" \
              "ds_read2_b32 v[100:101], v13 offset0:0 offset1:4\n\t" "ds_read2_b32 v[102:103], v13 offset0:32 offset1:36\n\t" \
              "ds_read2_b32 v[104:105], v14 offset0:0 offset1:4\n\t" "ds_read2_b32 v[106:107], v14 offset0:32 offset1:36\n\t" \
              "ds_read2_b32 v[108:109], v15 offset0:0 offset1:4\n\t" "ds_read2_b32 v[110:111], v15 offset0:32 offset1:36\n\t" \
              "v_mov_b64 v[112:113], v[80:81]\n\t" "v_mov_b64 v[114:115], v[82:83]\n\t" "s_mov_b32 s65, 0\n\t"     \
              "v_mov_b32_dpp v112, v84 row_ror:8 row_mask:0xf bank_mask:0xc\n\t" "v_mov_b32_dpp v113, v85 row_ror:8 row_mask:0xf bank_mask:0xc\n\t" \
              "v_mov_b32_dpp v114, v86 row_ror:8 row_mask:0xf bank_mask:0xc\n\t" "v_mov_b32_dpp v115, v87 row_ror:8 row_mask:0xf bank_mask:0xc\n\t" \
              "v_pk_mul_f32 v[112:113], v[112:113], s[62:63] op_sel_hi:[1,0]\n\t" "v_pk_mul_f32 v[114:115], v[114:115], s[62:63] op_sel_hi:[1,0]\n\t" \
              WLG(0)                                                                                               \
              PKA(96, 96, 100) PKA(98, 98, 102) PKA(104, 104, 108) PKA(106, 106, 110) PKA(96, 96, 104) PKA(98, 98, 106) \
              "v_cvt_pk_bf16_f32 v96, v96, 0\n\t" "v_cvt_pk_bf16_f32 v97, v97, 0\n\t" "v_cvt_pk_bf16_f32 v98, v98, 0\n\t" "v_cvt_pk_bf16_f32 v99, v99, 0\n\t" \
              "v_accvgpr_write_b32 a194, v96\n\t" "v_accvgpr_write_b32 a202, v97\n\t" "v_accvgpr_write_b32 a198, v98\n\t" "v_accvgpr_write_b32 a206, v99\n\t" \
              TSB(1) WVM(4) TSB(2) \
              "v_permlane32_swap_b32 v64, v66\n\t" "v_permlane32_swap_b32 v65, v67\n\t" \
              "v_pk_add_f32 v[92:93], v[64:65], v[66:67]\n\t" \
              WVM(3) SFM(68) \
              "v_permlane16_swap_b32 v92, v93\n\t" \
              "v_add_f32 v94, v92, v93\n\t" \
              WVM(2) "v_mul_f32 v94, s62, v94\n\t" SFM(76) \
              "v_exp_f32 v92, v94\n\t" \
              WVM(1) TSB(3) SFM(60) \
                \
              ".Lex%=:\n\t" \
              "v_exp_f32 v88, v112\n\t" "v_exp_f32 v89, v113\n\t" "v_cvt_pk_bf16_f32 v58, v92, 0\n\t" "v_exp_f32 v90, v114\n\t" "v_exp_f32 v91, v115\n\t" \
              "v_cvt_pk_bf16_f32 v56, v88, v89\n\t" "v_cvt_pk_bf16_f32 v57, v90, v91\n\t" \
              TSB(4) ROR8(50, 58) ROR8(48, 56) \
              "v_mfma_f32_16x16x32_bf16 v[88:91], v[52:55], v[56:59], 0\n\t" \
              ROR8(49, 57) "s_nop 1\n\t" \
              "v_mfma_f32_16x16x32_bf16 v[116:119], a[192:195], v[56:59], 0\n\t" \
              "s_nop 2\n\t" \
              "v_mfma_f32_16x16x32_bf16 v[116:119], a[196:199], v[48:51], v[116:119]\n\t" \
              "v_add_f32_dpp v92, v88, v88 row_ror:8 row_mask:0xf bank_mask:0xf\n\t" "v_subrev_u32 v89, 0x33800000, v92\n\t" \
              "v_mfma_f32_16x16x32_bf16 v[124:127], a[204:207], v[56:59], 0\n\t" \
              "v_rcp_f32 v93, v92\n\t" \
              "v_mfma_f32_16x16x32_bf16 v[124:127], a[200:203], v[48:51], v[124:127]\n\t" \
              TSB(5) "v_cmp_le_u32 vcc, 0x20000000, v89\n\t" "s_nop 0\n\t" \
              "s_cbranch_vccnz .Lmx%=\n\t" \
              OTAIL STB TSB(6)
#define SFM(r) "v_pk_fma_f32 v[112:113], " RNG("v", r, 2) ", s[62:63], v[112:113] op_sel_hi:[1,0,1]\n\t" "v_pk_fma_f32 v[114:115], " RNG("v", (r) + 2, 2) ", s[62:63], v[114:115] op_sel_hi:[1,0,1]\n\t"
#define ROR8(d, s) "v_mov_b32_dpp " REG(d) ", " REG(s) " row_ror:8 row_mask:0xf bank_mask:0xf\n\t"
#define OTAIL "v_pk_mul_f32 v[116:117], v[116:117], v[92:93] op_sel:[0,1]\n\t" "v_pk_mul_f32 v[118:119], v[118:119], v[92:93] op_sel:[0,1]\n\t" \
              "v_cvt_pk_bf16_f32 v116, v116, v117\n\t" "v_cvt_pk_bf16_f32 v117, v118, v119\n\t" \
              "s_mov_b64 exec, s[78:79]\n\t" \
              "v_pk_mul_f32 v[124:125], v[124:125], v[92:93] op_sel:[0,1]\n\t" "v_pk_mul_f32 v[126:127], v[126:127], v[92:93] op_sel:[0,1]\n\t" \
              "v_cvt_pk_bf16_f32 v116, v124, v125\n\t" "v_cvt_pk_bf16_f32 v117, v126, v127\n\t" \
              "s_mov_b64 exec, -1\n\t"
#define MX3(a, b) "v_max3_u32 v95, v95, " REG(a) ", " REG(b) "\n\t"
#define R2MAX ".Lmx%=:\n\t" "v_max_u32 v95, v60, v61\n\t" MX3(62, 63) MX3(64, 65) MX3(66, 67) MX3(68, 69) MX3(70, 71) MX3(76, 77) MX3(78, 79) \
              "v_cmp_eq_u32 vcc, s54, v95\n\t" "s_cbranch_vccnz .Lfail%=\n\t" OTAIL \
              "s_cmp_lg_u32 s65, 0\n\t" "s_cbranch_scc1 .Lend%=\n\t" "s_mov_b32 s65, 1\n\t" \
              "v_max3_f32 v93, v112, v113, v94\n\t" \
              "v_max3_f32 v93, v93, v114, v115\n\t" \
              "s_nop 1\n\t" \
              "v_max_f32_dpp v92, v93, v93 row_ror:8 row_mask:0xf bank_mask:0xf\n\t" \
              "v_max_f32_dpp v88, v93, v93 row_ror:8 row_mask:0xf bank_mask:0xf\n\t" \
              "s_nop 1\n\t" \
              "v_permlane16_swap_b32 v92, v88\n\t" \
              "v_max_f32 v93, v92, v88\n\t" "v_max_f32 v92, v92, v88\n\t" \
              "s_nop 1\n\t" \
              "v_permlane32_swap_b32 v93, v92\n\t" \
              "v_max_f32 v93, v93, v92\n\t" \
              "v_sub_f32 v94, v94, v93\n\t" "v_sub_f32 v112, v112, v93\n\t" "v_exp_f32 v92, v94\n\t" "v_sub_f32 v113, v113, v93\n\t" "v_sub_f32 v114, v114, v93\n\t" "v_sub_f32 v115, v115, v93\n\t" \
              "s_branch .Lex%=\n"
#define PS1(r) "v_add_f32 " REG(r) ", " REG(r) ", " REG((r) + 2) "\n\t"
#define NOP(n) "s_nop " STR(n) "\n\t"
#define PCA 13
#define PDA 8
#define PDC 8
#define PDF 7
#define SLV 0
#define QKRED(X) RDQ(80, 12288) RDQ(84, 12304) X RDQ(88, 0) RDQ(92, 16) RDQ(112, 4096) RDQ(116, 4112) RDQ(120, 8192) RDQ(124, 8208) \
        WLG(4) TSA(4) PKA(80, 80, 88) PKA(82, 82, 90) PKA(84, 84, 92) PKA(86, 86, 94) \
        WLG(2) PKA(80, 80, 112) PKA(82, 82, 114) PKA(84, 84, 116) PKA(86, 86, 118) \
        WLG(0) PKA(80, 80, 120) PKA(82, 82, 122) PKA(84, 84, 124) PKA(86, 86, 126) \
        "v_cvt_pk_bf16_f32 v72, v80, v81\n\t" "v_cvt_pk_bf16_f32 v73, v82, v83\n\t" "v_cvt_pk_bf16_f32 v74, v84, v85\n\t" "v_cvt_pk_bf16_f32 v75, v86, v87\n\t" \
        NOP(1) TSA(5) \
        "v_mfma_f32_16x16x32_bf16 v[88:91], v[72:75], v[72:75], 0\n\t" NOP(2) \
        "v_mfma_f32_16x16x32_bf16 v[80:83], a[208:211], v[72:75], 0\n\t" NOP(2) \
        "v_mfma_f32_16x16x32_bf16 v[84:87], a[212:215], v[72:75], 0\n\t"
#define QKPUB "buffer_store_dwordx4 v[88:91], v41, %[rs], s71 offen sc1\n\t" NOP(0) \
        "buffer_store_dwordx4 v[80:83], v40, %[rs], s71 offen sc1\n\t" NOP(2) \
        "buffer_store_dwordx4 v[84:87], v40, %[rs], s71 offen offset:512 sc1\n\t"
#define MRED(W, X) "ds_read_b128 v[80:83], v46 offset:12288\n\t" W "ds_read_b128 v[84:87], v46\n\t" "ds_read_b128 v[88:91], v46 offset:4096\n\t" "ds_read_b128 v[92:95], v46 offset:8192\n\t" X TSW(4) \
        WLG(2) TSW(5) PKA(80, 80, 84) PKA(82, 82, 86) WLG(0) PKA(88, 88, 92) PKA(90, 90, 94) PKA(84, 80, 88) PKA(86, 82, 90) \
        "v_cvt_pk_bf16_f32 v80, v84, v85\n\t" "v_cvt_pk_bf16_f32 v81, v86, v87\n\t"
#define ORD(a, b) "v_cmpx_o_f32 s[50:51], " REG(a) ", " REG(b) "\n\t"
#define REDO(L) "s_cmp_lg_u64 exec, -1\n\t" "s_mov_b64 exec, -1\n\t" "s_cbranch_scc1 " L "\n" L "r:\n\t"
#define CWAIT(L, o, S) L ":\n\t" "ds_read_b32 v62, v38 offset:" CNT(o) "\n\t" WLG(0) "v_readfirstlane_b32 s49, v62\n\t" "s_cmp_lt_u32 s49, " S "\n\t" "s_cbranch_scc1 " L "\n\t"
#define VPOST PSA(24) PSA(28) PSW(24, 2560) PSW(25, 2832) PSW(28, 3200) PSW(29, 3472)
#define BARA "s_barrier\n\t"
#define BARC "s_barrier\n\t"
#define PZADR(SB, M) "s_add_u32 s49, s45, 1\n\t" "s_xor_b32 s62, s71, " STR(XR2S) "\n\t" "v_readlane_b32 s63, v19, s49\n\t" "s_mov_b32 s49, " SB "\n\t" \
        ".if !((" STR(M) ") & 1)\n\t" "s_mov_b32 s63, 0x7ff00000\n\t" ".endif\n\t" ".if !((" STR(M) ") & 2)\n\t" "s_mov_b32 s49, 0x7ff00000\n\t" ".endif\n\t" \
        ".if !((" STR(M) ") & 4)\n\t" "s_mov_b32 s62, 0x7ff00000\n\t" ".endif\n\t" "s_cmp_eq_u32 %[wv], 0\n\t" \
        "s_cselect_b32 s49, s49, s62\n\t" "s_cselect_b32 s63, s63, s62\n\t" "v_add_u32 v48, s49, v23\n\t" "s_mov_b32 exec_hi, 0\n\t" "v_add_u32 v48, s63, v23\n\t" "s_mov_b32 exec_hi, -1\n\t"
#define PZST "buffer_store_dwordx4 v[92:95], v48, %[rs], 0 offen sc1\n\t"
#define MA 0
#define MC 0
#define MB (7 - MA - MC)
#define PZD 1
#define PZB0 PZADR("s63", MB)
#define PZB PZST
#define PZC0
#define PZC ""
#define PZNC 0
#define PZA0
#define PZA
#define PZNA 0
#define LASTC(L) RELV(L)
#define LASTA(L) ""
#define PCL PCA
#define ABODY(W, X, F, n) TSA(0) GLD(96, 0) NOP(PCA) GLD(100, 1024) NOP(PCA) GLD(104, 2048) W GLD(108, 3072) "s_mov_b32 s47, 0\n\t" X \
        WVM((n) + 3) TSA(1) SQ(0, 0) SQ(1, 0) SQ(2, 0) SQ(3, 0) \
        WVM((n) + 2) SQ(0, 1) SQ(1, 1) SQ(2, 1) SQ(3, 1) \
        WVM((n) + 1) SQ(0, 2) MXA0(96) MXB(96) SQ(1, 2) MXA(100) MXB(100) SQ(2, 2) MXA(104) MXB(104) SQ(3, 2) \
        WVM(n) TSA(2) SQ(0, 3) MXA(108) MXB(108) CMPB SQ(1, 3) "s_add_u32 s48, s38, 4\n\t" NOP(0) NOP(0) SQ(2, 3) "s_cbranch_vccnz " F "\n\t" PS1(64) PS1(65) SQ(3, 3) PS1(68) PS1(69) \
        NOP(0) PSW(64, 0) PSW(65, 272) PSW(68, 640) PSW(69, 912) PSA(72) PSA(76) PSW(72, 1280) PSW(73, 1552) PSW(76, 1920) PSW(77, 2192)
#define CISSUE(X, Y) TSW(0) GL1(96, 0) PC GL1(100, 1024) PC GL1(104, 2048) PC GL1(108, 3072) PC GL2(112, 0) PC GL2(116, 1024) \
        "s_mov_b32 s48, s36\n\t" "s_mov_b32 s36, s37\n\t" "s_add_u32 s45, s45, 1\n\t" "s_add_u32 s38, s38, 4\n\t" "v_readlane_b32 s37, v19, s45\n\t" \
        "s_add_u32 s39, s39, " STR(EXF) "\n\t" "s_add_u32 s40, s40, " STR(EXF) "\n\t" \
        "s_add_u32 s41, s41, " STR(EXK) "\n\t" "s_add_u32 s42, s42, " STR(EXK) "\n\t" "s_add_u32 s43, s43, s55\n\t" PC6 \
        GL2(120, 2048) "s_mov_b32 s47, 2\n\t" "s_mul_i32 s70, s69, 3\n\t" R2BASE "s_add_u32 s43, s43, s70\n\t" NOP(2) PC7 X \
        GL2(124, 3072) Y
#define ZALL ZER(64) ZER(68) ZER(72) ZER(76) ZER(80) ZER(84) ZER(24) ZER(28)
#define SHRD "ds_read_b32 v62, v38\n\t"
#define SHUP WLG(0) "v_readfirstlane_b32 s49, v62\n\t" "s_max_u32 s44, s44, s49\n\t"
#define DPA(d, r, m) "v_add_f32_dpp " REG(d) ", " REG(r) ", " REG((r) + 2) " quad_perm:[0,1,2,3] row_mask:" m " bank_mask:0xf\n\t"
#define CBODY(m, X, n, F) CBODY_(80, m, X, n, F)
#define CBODY_(a, m, X, n, F) \
        WVM((m) + 7) TSW(1) WBLK(a, m, 0, MXA0) WBLK(a, m, 1, MXA) X WBLK(a, n, 2, MXA) WBLK(a, n, 3, MXA) WBLK(a, n, 4, MXA) WBLK(a, n, 5, MXA) WBLK(a, n, 6, MXA) \
        WVM(n) TSW(2) SW(a, 0, 7) MXA(124) MXB(124) CMPB SW(a, 1, 7) NOP(0) NOP(1) "s_cbranch_vccnz " F "\n\t" \
        DPA(88, a, "0x3") DPA(89, (a) + 1, "0x3") NOP(1) DPA(88, (a) + 4, "0xc") DPA(89, (a) + 5, "0xc") NOP(1) \
        "v_permlane32_swap_b32 v88, v89\n\t" "v_add_f32 v88, v88, v89\n\t" TSW(3) PSW(88, 3840)
#define FINEO
#define NEXTP "s_branch .LA%=\n"
#define NEXTN "s_branch .LNA%=\n"

__device__ __forceinline__ void clk_edge(u32& cyc, u32& tick) {
  const u32 t = rt32();
  for (int i = 0; i < 64; ++i) { tick = rt32(); if (tick != t) break; }
  cyc = (u32)__builtin_amdgcn_s_memtime();
}
__device__ __forceinline__ float clk_rate(u32 cyc0, u32 tk0) { u32 c, t; clk_edge(c, t); return (float)(c - cyc0) / (float)max(t - tk0, 1u); }
__device__ __forceinline__ void dma16(__amdgpu_buffer_rsrc_t rs, int voff, int soff, int sstep, int ldsa) {
#pragma unroll
  for (int i = 0; i < 16; ++i)
    asm volatile("s_mov_b32 m0, %0\n\ts_nop 3\n\tbuffer_load_dwordx4 %1, %2, %3 offen lds" :: "s"(ldsa + i * 1024), "v"(voff), "s"(rs), "s"(soff ^ (i * sstep)) : "memory");
}

#define KH(t) (192 + 8 * (t))
#define KMF(t, a, b) "v_mfma_f32_16x16x32_bf16 " RNG("v", 80 + 4 * (t), 4) ", " RNG("v", KH(t) + 4 * (a), 4) ", " RNG("v", KH(3) + 4 * (b), 4) ", " RNG("v", 80 + 4 * (t), 4) "\n\t"
#define KX(j) (92 + 4 * ((j) & 1))
#define KA(t, j) "v_pk_mul_f32 " RNG("v", KX(j), 2) ", " RNG("v", 64 + 4 * (j), 2) ", " RNG("v", 112 + ((t) & 2), 2) " op_sel:[0," STR((t) & 1) "] op_sel_hi:[1," STR((t) & 1) "]\n\t"
#define KF(j) "v_fract_f32 " REG(KX(j) + 1) ", " REG(KX(j) + 1) "\n\t"
#define KE(j, N) "v_exp_f32 " REG(KX(j)) ", " REG(KX(j)) "\n\t" "v_cos_f32 " REG(KX(j) + 2) ", " REG(KX(j) + 1) "\n\t" "v_sin_f32 " REG(KX(j) + 3) ", " N REG(KX(j) + 1) "\n\t"
#define KM(j) "v_pk_mul_f32 " RNG("v", KX(j) + 2, 2) ", " RNG("v", KX(j) + 2, 2) ", " RNG("v", KX(j), 2) " op_sel_hi:[1,0]\n\t"
#define KC(j) "v_pk_mul_f32 " RNG("v", KX(j), 2) ", " RNG("v", KX(j) + 2, 2) ", " RNG("v", 66 + 4 * (j), 2) " op_sel_hi:[1,0]\n\t" \
  "v_pk_fma_f32 " RNG("v", KX(j), 2) ", " RNG("v", KX(j) + 2, 2) ", " RNG("v", 66 + 4 * (j), 2) ", " RNG("v", KX(j), 2) " op_sel:[1,1,0] op_sel_hi:[0,1,1] neg_hi:[1,0,0]\n\t"
#define KD(t, j, r) "v_cvt_pk_bf16_f32 " REG(KH(t) + (j)) ", " REG(r) ", " REG((r) + 1) "\n\t" \
  "v_dot2c_f32_bf16 " REG(r) ", s47, " REG(KH(t) + (j)) "\n\t" "v_dot2c_f32_bf16 " REG((r) + 1) ", s48, " REG(KH(t) + (j)) "\n\t"
#define KL(t, j, r) "v_cvt_pk_bf16_f32 " REG(KH(t) + 4 + (j)) ", " REG(r) ", " REG((r) + 1) "\n\t"
#define KDM "s_add_u32 m0, m0, 1024\n\t" "s_add_u32 s43, s43, s42\n\t" "buffer_load_dwordx4 v110, s[36:39], s43 offen lds\n\t"
#define KP(j) "ds_read_b128 " RNG("v", 64 + 4 * (j), 4) ", v111 offset:" STR(16 * (j)) "\n\t"
#define KV(j, L, A) KE(j, "-") L KM(j) KC(j) A KD(3, j, KX(j))
#define KU(t, j, L, A) KE(j, "") L KM(j) A KD(t, j, KX(j) + 2)
#define KSUB                                                                                                        \
  KV(0, WLG(8) "s_add_u32 m0, m0, 1024\n\t", KA(3, 1)) KV(1, KL(3, 0, KX(0)), KA(3, 2))                              \
  "s_xor_b32 s43, s40, s41\n\t" "buffer_load_dwordx4 v110, s[36:39], s43 offen lds\n\t" KV(2, KL(3, 1, KX(1)), KA(3, 3)) \
  KV(3, KL(3, 2, KX(2)), KA(0, 0))                                                                                  \
  KU(0, 0, KL(3, 3, KX(3)), KA(0, 1)) KU(0, 1, KL(0, 0, KX(0) + 2), KA(0, 2)) KDM KU(0, 2, KL(0, 1, KX(1) + 2), KA(0, 3)) \
  KU(0, 3, KL(0, 2, KX(2) + 2), KA(1, 0)) KF(0)                                                                     \
  KU(1, 0, KL(0, 3, KX(3) + 2), KA(1, 1)) KF(1) KMF(0, 1, 0) KU(1, 1, KL(1, 0, KX(0) + 2), KA(1, 2)) KF(2) KDM      \
  KMF(0, 0, 1) KU(1, 2, KL(1, 1, KX(1) + 2), KA(1, 3)) KF(3) KMF(0, 0, 0) KU(1, 3, KL(1, 2, KX(2) + 2), KA(2, 0)) KF(0) KP(0) \
  KU(2, 0, KL(1, 3, KX(3) + 2), KA(2, 1)) KF(1) KP(1) KMF(1, 1, 0) KU(2, 1, KL(2, 0, KX(0) + 2), KA(2, 2)) KF(2) KP(2) KDM \
  KMF(1, 0, 1) KU(2, 2, KL(2, 1, KX(1) + 2), KA(2, 3)) KF(3) KP(3) KMF(1, 0, 0) KU(2, 3, KL(2, 2, KX(2) + 2), WLG(3) KA(3, 0)) \
  KMF(2, 0, 1) "v_add_u32 v111, 256, v111\n\t" "s_lshl2_add_u32 s41, s42, s41\n\t" KL(2, 3, KX(3) + 2)
#define KZERO ZER(80) ZER(84) ZER(88)
#define KCHUNK(PTR, VO, SO, ST, BUF, PF) "s_mov_b64 s[36:37], " PTR "\n\t" "s_mov_b32 m0, %[sc]\n\t" "v_mov_b32 v110, " VO "\n\t" SO "s_mov_b32 s41, 0\n\t" "s_mov_b32 s42, " STR(ST) "\n\t" \
  "buffer_load_dword " PF ", s[36:39], 0 offen lds\n\t" "s_add_u32 m0, %[sl], " STR((BUF) * 16384 - 1024) "\n\t"
#define KCALL "s_call_b64 s[44:45], .Lkg%=\n\t" WVM(16) KMF(2, 0, 0)
#define KTR(F, d, A, o) "ds_read_b64_tr_b16 " RNG(F, d, 2) ", " A " offset:" STR(o) "\n\t"
#define KTQ1(d, o) KTR("a", d, "%[aq]", o) KTR("a", (d) + 2, "%[aq]", (o) + 128) KTR("a", (d) + 4, "%[aq]", (o) + 2048) KTR("a", (d) + 6, "%[aq]", (o) + 2176)
#define KTQ(d, o) KCALL KTQ1(d, o) KMF(2, 1, 0) KTQ1((d) + 8, (o) + 32)
#define KTO1(d, o) KTR("v", d, "%[ao]", o) KTR("v", (d) + 2, "%[ao]", (o) + 64) KTR("v", (d) + 4, "%[ao]", (o) + 1024) KTR("v", (d) + 6, "%[ao]", (o) + 1088)
#define KTO(d, o) KCALL KTO1(d, o) KMF(2, 1, 0) KTO1((d) + 8, (o) + 2048)
#define KOUT(o) "s_add_u32 s43, %[ko], " STR(o) "\n\t"                                                              \
  "buffer_store_dwordx4 v[80:83], %[l16], %[rt], s43 offen sc1\n\t" "buffer_store_dwordx4 v[84:87], %[l16], %[rt], s43 offen offset:1024 sc1\n\t" "s_nop 3\n\t" \
  "buffer_store_dwordx4 v[88:91], %[l16], %[rt], s43 offen offset:2048 sc1\n\t"
struct SsmPtrs { const u16 *A_re, *A_im, *B_re, *B_im, *C_re, *C_im, *log_dt; };
__device__ __forceinline__ void r_role(unsigned char* lds, int b, const u16* __restrict__ mem0, const u16* __restrict__ Wq, const u16* __restrict__ Wk, const u16* __restrict__ Wv,
                                       const u16* __restrict__ Wo, int fb, u32 tag, u64 pc0, int clen, __amdgpu_buffer_rsrc_t rsx, __amdgpu_buffer_rsrc_t rsf,
                                       __amdgpu_buffer_rsrc_t rst, SsmPtrs sp, int tid, int lane, int w
                                       , u32* dbg, int tun_r, int tun_p, int tun_b, int tun_c
                                       ) {
  const int lr = lane & 15, lg = lane >> 4;
  const int ldsb = (int)(size_t)(LAS unsigned char*)lds;
  const int idx = (lr & 2) ? 0xEEEE : 0x4444;
  u32 tcnt = 0; (void)tcnt;
  u32 cyc0 = 0, tk0 = 0;
  if (tid < 8) *(u32*)(lds + LR_CTL + tid * 4) = 0u;
  *(u32*)(lds + LR_CTL + 4096 + 256 + tid * 4) = 0u;
  const int xcd = b & 3, li = b >> 2;
  const int head = xcd * 8 + (li >> 2), ks = li & 3;
  const int vcu = head * 4 + ks;
  const int cg = (xcd >> 1) * 32 + li, rg = xcd & 1;
  const int rot1 = ((li >> 1) + w) & 3, rot3 = ((li >> 2) + 2 * w) & 7;
  const int pz1 = lane < 32 ? XR1A(lane >> 3, cg * 256 + rg * 128 + (lane & 7) * 16) : XR3A(lg & 1, vcu * 256 + lr * 16);
  const int pgo = PGO_X + head * 128;
  const int calo = XCAL + (li & 3) * 8192 + ((li >> 2) & 1) * 4096 + ((li >> 3) * 4 + xcd) * 128;
  u32 xp0;
  u32x32 wa[6], wo[4];
  {
    const __amdgpu_buffer_rsrc_t rwq = __builtin_amdgcn_make_buffer_rsrc((void*)Wq, 0, DM * HD * 2, 0x00020000);
    const int vq = (lane >> 2) * (HD * 2) + vcu * 64 + (lane & 3) * 16, sq = (256 * w + (rot1 << 6)) * (HD * 2);
    const int vo = (lane >> 1) * (DM * 2) + cg * 32 + (lane & 1) * 16, so = (1024 * w + (rot3 << 7)) * (DM * 2);
    const int sl = ldsb + LR_STG + w * 32768;
    const int ch0 = (b * 4 + w) * 2;
    u32x2 pin[6];
    u32 ldt;
    {
      const int po = (ch0 * SSM_N + 4 * lane) * 2;
      asm volatile("global_load_dwordx2 %0, %7, %9\n\tglobal_load_dwordx2 %1, %7, %10\n\tglobal_load_dwordx2 %2, %7, %11\n\tglobal_load_dwordx2 %3, %7, %12\n\t"
                   "global_load_dwordx2 %4, %7, %13\n\tglobal_load_dwordx2 %5, %7, %14\n\tglobal_load_ushort %6, %8, %15"
                   : "=&v"(pin[0]), "=&v"(pin[1]), "=&v"(pin[2]), "=&v"(pin[3]), "=&v"(pin[4]), "=&v"(pin[5]), "=&v"(ldt)
                   : "v"(po), "v"((ch0 + (lane >> 5)) * 2), "s"(sp.A_re), "s"(sp.A_im), "s"(sp.B_re), "s"(sp.B_im), "s"(sp.C_re), "s"(sp.C_im), "s"(sp.log_dt) : "memory");
    }
    u32x4 m0v;
    asm volatile("global_load_dwordx4 %0, %1, %2" : "=&v"(m0v) : "v"(((4 * rg + ((lane >> 1) & 3)) * DM + 16 * cg + 8 * (lane & 1)) * 2), "s"(mem0) : "memory");
    const u16* const wp[4] = {Wq, Wk, Wv, Wo};
    int pfo[5];
#pragma unroll
    for (int c = 0; c < 5; ++c) {
      const int lo_ = (int)(size_t)wp[c < 3 ? c : 3], s3 = (384 - lo_) & 1023, s63 = (8064 - lo_) & 8191;
      pfo[c] = c < 3 ? (lane < 16 ? (256 * w + li * 8 + (lane & 7)) * (HD * 2) + xcd * 2048 + (lane >> 3) * 1024 + s3
                                  : (lane < 24 && (s63 >> 11) == xcd ? (256 * w + li * 8 + (lane & 7)) * (HD * 2) + s63 : OOR))
                     : (lane < 16 ? (1024 * w + li * 16 + lane + (((c - 3) * 512) ^ ((((s3 >> 7) + 2 * w) & 4) << 7))) * (DM * 2) + (xcd >> 1) * 1024 + s3 : OOR);
    }
    const int scr = ldsb + LR_RED + 1024 + w * 256;
    asm volatile("s_mov_b32 m0, %0\n\ts_nop 3\n\tbuffer_load_dword %1, %2, 0 offen lds" :: "s"(scr), "v"(pfo[0]), "s"(rwq) : "memory");
#define KP0 16
#pragma unroll
    for (int i = 0; i < KP0; ++i)
      asm volatile("s_mov_b32 m0, %0\n\ts_nop 3\n\tbuffer_load_dwordx4 %1, %2, %3 offen lds" :: "s"(sl + i * 1024), "v"(vq), "s"(rwq), "s"(sq ^ (i * 16 * HD * 2)) : "memory");
    asm volatile("s_waitcnt vmcnt(1+" STR(KP0) ")" : "+v"(pin[0]), "+v"(pin[1]), "+v"(pin[2]), "+v"(pin[3]), "+v"(pin[4]), "+v"(pin[5]), "+v"(ldt), "+v"(m0v)::"memory");
    f32x4* prm = (f32x4*)(lds + w * 4096);
    {
      const float dt = rbf(expf(bf2f((u16)ldt)));
#pragma unroll
      for (int i = 0; i < 4; ++i) {
        float v[6];
#pragma unroll
        for (int j = 0; j < 6; ++j) v[j] = __uint_as_float((i & 1) ? (pin[j][i >> 1] & 0xffff0000u) : (pin[j][i >> 1] << 16));
        const float are = dt * v[0], aim = dt * v[1], br = dt * v[2], bi = dt * v[3];
        const float cr = v[4] * br - v[5] * bi, ci = v[4] * bi + v[5] * br;
        const float rev = aim * 0.15915494309189535f;
        prm[4 * lane + i] = (f32x4){are * 1.4426950408889634f, rev - __builtin_rintf(rev), cr, ci};
      }
    }
#pragma unroll
    for (int i = KP0; i < 16; ++i)
      asm volatile("s_mov_b32 m0, %0\n\ts_nop 3\n\tbuffer_load_dwordx4 %1, %2, %3 offen lds" :: "s"(sl + i * 1024), "v"(vq), "s"(rwq), "s"(sq ^ (i * 16 * HD * 2)) : "memory");
    const f32x4 lv = {(float)(128 * (lr >> 3) + (lr & 7)), (float)(lr < 8 ? 256 + 128 * (lr >> 2) + 2 * (lr & 3) : 512 + 128 * ((lr - 8) >> 1) + 4 * (lr & 1)),
                         (float)(lr <= 8 ? 1024 + 128 * lr : 0), (float)(8 * lr)};
    const int trow = 16 * (lg >> 1) + 4 * (lg & 1) + ((lr >> 2) & 1) + 8 * (lr >> 3);
    asm volatile(
        "s_nop 4\n\t" "s_mov_b32 s46, m0\n\t" "s_mov_b32 s38, " STR(DM * HD * 2) "\n\t" "s_mov_b32 s39, 0x20000\n\t" "s_mov_b32 s47, 0xbf80\n\t" "s_mov_b32 s48, 0xbf800000\n\t"
        WLG(0)
        "ds_read_b128 v[64:67], %[pa]\n\t" "ds_read_b128 v[68:71], %[pa] offset:16\n\t" "ds_read_b128 v[72:75], %[pa] offset:32\n\t" "ds_read_b128 v[76:79], %[pa] offset:48\n\t"
        "v_add_u32 v111, 256, %[pa]\n\t" KZERO
        KCHUNK("%[wk]", "%[vq]", "s_mov_b32 s40, %[sq]\n\t", 16 * HD * 2, 1, "%[p1]")
        WLG(0) KA(3, 0)
        KTQ(0, 0) KTQ(32, 4096) KTQ(64, 8192) KTQ(96, 12288)
        KCHUNK("%[wv]", "%[vq]", "", 16 * HD * 2, 0, "%[p2]")
        KTQ(16, 16384) KTQ(48, 16384 + 4096) KTQ(80, 16384 + 8192) KTQ(112, 16384 + 12288)
        KOUT(0)
        KCHUNK("%[wo]", "%[vo]", "s_mov_b32 s40, %[so]\n\t", 32 * DM * 2, 1, "%[p3]")
        KZERO
        KTQ(128, 0) KTQ(144, 4096) KTQ(160, 8192) KTQ(176, 12288)
        KCHUNK("%[wo]", "%[vo]", "s_xor_b32 s40, s40, " STR(512 * DM * 2) "\n\t", 32 * DM * 2, 0, "%[p4]")
        KTO(128, 16384) KTO(144, 16384 + 4096) KTO(160, 16384 + 8192) KTO(176, 16384 + 12288)
        KOUT(3072)
        "s_branch .Lke%=\n"
        ".Lkg%=:\n\t"
        KSUB
        "s_setpc_b64 s[44:45]\n"
        ".Lke%=:\n\t"
        WLG(0) "s_mov_b32 m0, s46"
        : "=&{a[0:31]}"(wa[0]), "=&{a[32:63]}"(wa[1]), "=&{a[64:95]}"(wa[2]), "=&{a[96:127]}"(wa[3]), "=&{a[128:159]}"(wa[4]), "=&{a[160:191]}"(wa[5]),
          "=&{v[128:159]}"(wo[0]), "=&{v[160:191]}"(wo[1])
        : "{v[112:115]}"(lv), [pa] "v"(ldsb + w * 4096 + lg * 64), [vq] "v"(vq), [vo] "v"(vo),
          [p1] "v"(pfo[1]), [p2] "v"(pfo[2]), [p3] "v"(pfo[3]), [p4] "v"(pfo[4]), [aq] "v"(sl + trow * 64 + (lr & 3) * 8), [ao] "v"(sl + trow * 32 + (lr & 3) * 8), [l16] "v"(lane * 16),
          [wk] "s"(Wk), [wv] "s"(Wv), [wo] "s"(Wo), [sq] "s"(sq), [so] "s"(so), [sl] "s"(sl), [sc] "s"(scr), [rt] "s"(rst), [ko] "s"(2 * SSM_L * SSM_D * 2 + ch0 * 3072)
        : CL64, CL72, CL80, CL88, CL96, CL8(192, 193, 194, 195, 196, 197, 198, 199), CL8(200, 201, 202, 203, 204, 205, 206, 207), CL8(208, 209, 210, 211, 212, 213, 214, 215),
          CL8(216, 217, 218, 219, 220, 221, 222, 223), "s36", "s37", "s38", "s39", "s40", "s41", "s42", "s43", "s44", "s45", "s46", "s47", "s48", "scc", "memory");
    {
      const u32x4 pz = (u32x4){SENT, SENT, SENT, SENT};
#pragma unroll
      for (int i = XS0; i < NSEG; i += 4) __builtin_amdgcn_raw_buffer_store_b128(pz, rsf, (lane < 32 ? FO_KM : FO_VM - 512) + vcu * 512 + lane * 16, (i + w) * EXK, 16);
      if (w < 2) __builtin_amdgcn_raw_buffer_store_b128(pz, rsf, lane * 16, FO_O + (NSEG - 1) * 262144 + (vcu * 2 + w) * 1024, 16);
      if (w < 3) {
        u32x4 v = pz;
        if (w == 0 && lane < 32) v = (u32x4){nosent(m0v[0]), nosent(m0v[1]), nosent(m0v[2]), nosent(m0v[3])};
#pragma unroll
        for (int hf = 0; hf < 2; ++hf) {
          __builtin_amdgcn_raw_buffer_store_b128(v, rsx, pz1, XSB(w) + hf * 8192, 16);
          __builtin_amdgcn_raw_buffer_store_b128(pz, rsx, XR2(vcu) + lane * 16, XR2B + (w & 1) * XR2S + hf * 8192, 16);
          __builtin_amdgcn_raw_buffer_store_b128(pz, rsx, lane < 16 ? XR2(vcu) + 1024 + lane * 16 : OOR, XR2B + (w & 1) * XR2S + hf * 8192, 16);
        }
      } else {
        __builtin_amdgcn_raw_buffer_store_b128(pz, rsx, lane < 8 ? lane * 16 : OOR, calo, 16);
        if (b < 32) {
          __builtin_amdgcn_raw_buffer_store_b128(pz, rsx, lane * 16, XSYN + b * 2048, 16);
          __builtin_amdgcn_raw_buffer_store_b128(pz, rsx, lane * 16, XSYN + b * 2048 + 1024, 16);
        } else if (b >= 48 && b < 48 + 41) {
          __builtin_amdgcn_raw_buffer_store_b128(pz, rsx, lane * 16, PGO_D + (b - 48) * 1024, 16);
        }
      }
      asm volatile("" ::: "memory");
    }
    xp0 = __builtin_amdgcn_raw_buffer_load_b32(rsx, (lane & 1) * 4096, pgo, 16);
    clk_edge(cyc0, tk0);
    asm volatile("s_waitcnt vmcnt(0)\n\ts_barrier" : "+v"(xp0)::"memory");
    if (tid == 0) dbg[3072 + b * 2] = rt32();
#define CLKF() clk_rate(cyc0, tk0)
    if (w == 0) {
      const int l0 = lane ? OOR : 0;
      const u64 c0 = __builtin_amdgcn_s_memtime();
      u32 v;
      asm volatile("s_waitcnt lgkmcnt(0)\n\tbuffer_load_dword %0, off, %1, %2 sc1\n\tbuffer_store_dword %3, %4, %1, %5 offen sc1\n\ts_waitcnt vmcnt(1)"
                   : "=&v"(v) : "s"(rsx), "s"(calo), "v"(tag), "v"(l0), "s"(PGO_Y + b * 128), "s"(c0) : "memory");
      const u32 lat = (u32)(__builtin_amdgcn_s_memtime() - c0);
      const float f = CLKF();
      typedef __attribute__((ext_vector_type(3))) unsigned int u32x3;
      __builtin_amdgcn_raw_buffer_store_b96((u32x3){__float_as_uint(f), lat, tag}, rsx, l0, PGO_Y + b * 128 + 4, 16);
      if (tid == 0) { dbg[3072 + b * 2 + 1] = rt32(); dbg[4200 + b] = lat; }
      asm volatile("" ::: "memory");
    }
    {
      u32 r[1][8][8];
      tr_chunk<1, 8>((const u16*)(lds + LR_STG + w * 32768), lr, lg, r);
#pragma unroll
      for (int i = 0; i < 8; ++i)
#pragma unroll
        for (int e = 0; e < 8; ++e) { const int o = (8 + i) * 8 + e; wo[o >> 5][o & 31] = r[0][i][e]; }
      asm volatile("s_waitcnt lgkmcnt(0)" ::: "memory");
    }
  }
  const int hf = (lr >> 1) & 1;
  const int voff1 = lg * 256 + (lr >> 3) * 128 + (((lr >> 2) & 1) * 2 + (lr & 1)) * 32 + hf * 16;
  const int voff3 = (lr >> 3) * 512 + (lg >> 1) * 256 + (((lr >> 2) & 1) * 2 + (lr & 1)) * 16 + (lg & 1) * 128 + hf * 64;
  const int ard = ldsb + (2 * (lr >> 3) + (lg >> 1)) * 640 + (lr & 1) * 272 + ((lr >> 1) & 3) * 64 + (lg & 1) * 32;
  const int ctl = ldsb + LR_CTL, one = w == 0 ? 2 : (w == 3 ? 4 : 1);
  const int r8 = lane & 7, hk = (lane >> 3) & 1;
#define COA 1
#define COB 1
  const int st01 = lr < 8 ? XR2(vcu) + lr * 16 + lg * 128 : OOR, st2 = (lr < 8 && lg >= 2) ? XR2(vcu) + 1024 + lr * 16 + (lg - 2) * 128 : OOR;
  const int ld01 = hk * 512 + r8 * 16 + lg * 128, ld2 = XR2(head * 4 + lg) + 1024 + r8 * 16 + hk * 128;
  int sto = XR3A(r8 >> 2, vcu * 256 + (r8 & 3) * 16 + lg * 64 + hk * 8);
  const int stk = lr >= 8 ? (lg * 8 + (lr - 8)) * 16 : OOR;
  const int lo = X4C ? 2 * (lr & 7) + (lr >> 3) : lr;
  const int arw = ldsb + LR_CTL + lane * 4, arr = ldsb + 3840 + ((lo >> 3) + 2 * ((lo >> 2) & 1)) * 64 + (lo & 3) * 16;
  const int st1 = XR1A(lg, cg * 256 + rg * 128 + lo * 8);
  const int avw = 0;
  const int avc = ldsb + 2560 + (lr >> 3) * 640 + (8 * ((lr >> 2) & 1) + (lr & 3)) * 4;
  const int avr = avc + (lg & 1) * 272 + (lg >> 1) * 64, avf = avc + (lg & 1) * 128 + (lg >> 1) * 16;
  u32x16 kc = {(u32)voff1, (u32)voff3, (u32)idx, (u32)TEX(lane), (u32)(lane < 16 ? XR2(vcu) + 1024 + lane * 16 : OOR), (u32)ard, (u32)ctl, (u32)one, (u32)st01, (u32)st2, (u32)(XR2(vcu) + lane * 16), (u32)ld2, (u32)(lane * 16), (u32)arw, (u32)arr, (u32)stk};
  int o1c = XR1A(xcd, w * 4096) + rot1 * 1024, o3c = XR3A(rg, w * 8192) + rot3 * 1024;
  const int fok = FO_K + (head * 8 + ks) * 1024, fov = FO_V + (head * 8 + 2 * ks) * 1024;
#define XPROG(v) min(xprog(__builtin_amdgcn_readlane(v, 0), tag), xprog(__builtin_amdgcn_readlane(v, 1), tag))
  int xp = w == 3 ? XPROG(xp0) : 1000;
#define XWAIT(n) for (int it_ = 0; it_ < SPIN_CAP && xp < (n); ++it_) {                                               \
    if (it_) asm volatile("s_sleep 4" ::: "memory");                                                                  \
    const u32 v_ = __builtin_amdgcn_raw_buffer_load_b32(rsx, (lane & 1) * 4096, pgo, 16); xp = XPROG(v_); asm volatile("" ::: "memory"); }
  XWAIT(1)
  u32x16 fv;
  u32x8 fk;
  {
    const int lo_ = w == 3 ? lane * 16 : OOR;
    const u32x4 v0 = __builtin_amdgcn_raw_buffer_load_b128(rsf, lo_, fov, 16), v1 = __builtin_amdgcn_raw_buffer_load_b128(rsf, lo_, fov + 1024, 16);
    const u32x4 k0 = __builtin_amdgcn_raw_buffer_load_b128(rsf, lo_, fok, 16), k1 = __builtin_amdgcn_raw_buffer_load_b128(rsf, lo_, fok + 4096, 16);
    fv = (u32x16){v0[0], v0[1], 0u, 0u, v0[2], v0[3], 0u, 0u, v1[0], v1[1], 0u, 0u, v1[2], v1[3], 0u, 0u};
    fk = (u32x8){k0[0], k0[1], k0[2], k0[3], k1[0], k1[1], k1[2], k1[3]};
    asm volatile("" : "+{a[192:207]}"(fv), "+{a[208:215]}"(fk));
  }
#define XPN 1
#define CALM 40
  XWAIT(2)
#pragma unroll
  for (int i = 0; i < 3; ++i) *(u32x4*)(lds + w * 4096 + i * 1024 + lane * 16) = (u32x4){SENT, SENT, SENT, SENT};
  {
    u64 pc; asm volatile("s_getpc_b64 %0" : "=s"(pc));
    const u32 cv = __builtin_amdgcn_raw_buffer_load_b32(__builtin_amdgcn_make_buffer_rsrc((void*)pc0, 0, clen, 0x00020000), (int)(pc - pc0) + tid * 64, 0, 0);
    asm volatile("" :: "v"(cv));
  }
  if (w == 3) { const u32 v_ = __builtin_amdgcn_raw_buffer_load_b32(rsx, (lane & 1) * 4096, pgo, 16); xp = max(xp, XPROG(v_)); }
  u32x4 st;
  if (w == 1) {
    if (b == 0) {
      u32 tt = 0, best = 0;
      bool xok = false;
      const int sto_ = lane < 5 ? (lane ? XSTR(lane - 1) : XSTA) : OOR;
      for (int it = 0; it < SPIN_CAP; ++it) {
        asm volatile("" ::: "memory");
        const u32x4 r0 = __builtin_amdgcn_raw_buffer_load_b128(rsx, lane * 128, PGO_Y, 16), r1 = __builtin_amdgcn_raw_buffer_load_b128(rsx, lane * 128 + 8192, PGO_Y, 16);
        if (!xok) xok = __ballot(xprog(__builtin_amdgcn_raw_buffer_load_b32(rsx, lane * 128, PGO_X, 16), tag) < XPN) == 0;
        const bool c0 = r0[3] == tag, c1 = r1[3] == tag;
        if (tt == 0 && __popcll(__ballot(!c0 || !c1)) <= CALM) {
          float fm = (c0 ? __uint_as_float(r0[1]) : 0.f) + (c1 ? __uint_as_float(r1[1]) : 0.f), fn = (c0 ? 1.f : 0.f) + (c1 ? 1.f : 0.f);
#pragma unroll
          for (int o = 4; o < 64; o *= 2) { fm += __shfl_xor(fm, o); fn += __shfl_xor(fn, o); }
          fm = fn > 0.f ? fm / fn : 100.f;
          const float f0 = __uint_as_float(__builtin_amdgcn_readfirstlane(__float_as_uint(fm)));
          fm = fminf(fm, __shfl_xor(fm, 1)); fm = fminf(fm, __shfl_xor(fm, 2));
          const float f_ = CLKF() * fminf(fmaxf(fm / f0, 0.8f), 1.f);
          const float k_ = 1.f + TKS * (fminf(fmaxf(23.9f / f_, 1.f), 1.5f) - 1.f);
          const int nb_ = (int)(TDB * k_ + 0.5f), nc_ = nb_ + (int)((TDC - TDB) * k_ + 0.5f), np_ = nc_ + (int)((TP - TDC) * k_ + 0.5f);
          u32 lat = min(c0 ? r0[2] : 65535u, c1 ? r1[2] : 65535u);
          { auto r = __builtin_amdgcn_permlane16_swap(lat, lat, false, false); lat = min(r[0], r[1]); }
          { auto r = __builtin_amdgcn_permlane32_swap(lat, lat, false, false); lat = min(r[0], r[1]); }
          u32 mm[4];
#pragma unroll
          for (int k = 0; k < 4; ++k)
            mm[k] = __builtin_amdgcn_readlane(lat, 4 * k) + __builtin_amdgcn_readlane(lat, 4 * k + 2) + __builtin_amdgcn_readlane(lat, 4 * (k ^ 2) + 1) + __builtin_amdgcn_readlane(lat, 4 * (k ^ 2) + 3);
          const u32 hb = mm[0] + mm[2] <= mm[1] + mm[3] ? 0u : 1u;
          best = (hb << 13) | ((hb ? mm[1] <= mm[3] : mm[0] <= mm[2]) ? CLS0 : 16384u - CLS0);
          if (__ballot(lat == 65535u) == 0) tt = __builtin_amdgcn_readfirstlane(nb_ | (nc_ << 10) | (np_ << 20));
          if (lane == 0) { dbg[3501] = (hb ? mm[1] : mm[0]) | ((hb ? mm[3] : mm[2]) << 16); dbg[3502] = rt32(); }
        }
        if (__ballot(r0[0] != tag || r1[0] != tag) == 0 && xok && tt) break;
      }
      st = (u32x4){rt32() + TD0, tt, best, tag};
      __builtin_amdgcn_raw_buffer_store_b128(st, rsx, sto_, 0, 16);
      if (lane == 0) dbg[3503] = rt32();
    } else st = wait_start(rsx, fb, tag, false, xcd);
    if (lane == 0) *(u32x4*)(lds + LR_CTL + 32) = st;
  }
  LBAR();
  st = *(const u32x4*)(lds + LR_CTL + 32);
  const int flip = __builtin_amdgcn_readfirstlane(st[2]) & 8192, cls = __builtin_amdgcn_readfirstlane(st[2]) & 16384;
  o1c ^= cls; o3c ^= cls; sto ^= cls; kc[4] ^= cls; kc[8] ^= cls; kc[9] ^= cls; kc[10] ^= cls; kc[11] ^= cls;
  { const u32 now = rt32(); if (st[0] - now + 100000u > 200000u) st[0] = now; }
  u32x16 kd = {(u32)((ld01 + XR2(vcu ^ 1)) ^ cls), (u32)((ld01 + XR2(vcu ^ 2)) ^ cls), (u32)((ld01 + XR2(vcu ^ 3)) ^ cls), (u32)(X4B && hk ? OOR : sto), (u32)avr, (u32)(avr + 4096), (u32)(avr + 8192),
               (u32)(avr + 12288), (u32)avw, (u32)(X4C && hk ? OOR : st1 ^ cls), (u32)((lane & 1) * 4096), (u32)(XSB(lane + 1) + flip), (u32)(lane * 8), (u32)avf,
               (u32)(b == 0 && lane == 0 ? XSYN + flip : OOR), (u32)(w == 0 ? pz1 ^ cls : (w == 1 ? kc[10] : kc[4]))};
  u32x16 sa = {(u32)flip, (u32)(XSB(1) + flip), 0u, (u32)fok, (u32)fov, (u32)(FO_KM + (head * 4 + ks) * 512),
               (u32)(FO_VM + (head * 8 + 2 * ks) * 256), st[0], 0u, 0u, (u32)SYO(1), 0u, 0u, (u32)TRY_CAP, 0u, 0u};
  u32x8 sc = {(u32)o1c, (u32)o3c, SENT, 0u, 0u, 0u, st[1], (u32)pgo};
  const u32 org_ = __builtin_amdgcn_readfirstlane(st[0]);
  u32x2 hst = {0u, 0u};
#define TUNEO , "+{s[100:101]}"(hst)
#define KCNT "s_mul_i32 s49, s47, 10\n\t" "s_lshl_b32 s49, 1, s49\n\t" "s_add_u32 s68, s68, s49\n\t"
#define KHST "s_bitset1_b64 s[100:101], s45\n\t"
#define KTRC
#define TRCI
#define PREP 16
#define GAP 12
#define RETA(n, L) "s_cmp_eq_u32 s49, " #n "\n\t" "s_cmov_b32 s48, .Lpc%=-" L "\n\t"
  u32x32 keep;
  u32x2 ov;
  asm volatile("" : "={v[60:91]}"(keep));
  while (true) {
    sa[14] = xp;
#pragma unroll
    for (int i = 0; i < 16; ++i) sa[i] = __builtin_amdgcn_readfirstlane(sa[i]);
#pragma unroll
    for (int i = 0; i < 8; ++i) sc[i] = __builtin_amdgcn_readfirstlane(sc[i]);
    asm volatile(
        "s_nop 4\n\t" "s_mov_b32 s60, m0\n\t"
        "s_mov_b32 m0, %[mb]\n\t" "s_mov_b32 s64, s50\n\t" "s_mov_b32 s98, s49\n\t" "s_mov_b32 s68, 0\n\t"
        EXTRA R2BASE
        "s_cmp_lg_u32 %[wv], 3\n\t" "s_cbranch_scc1 .LN%=\n\t"
        "s_mov_b32 s78, 0xff00ff00\n\t" "s_mov_b32 s79, 0xff00ff00\n\t" "s_mov_b32 s62, 0x3e0293ee\n\t"
        "v_mov_b32 v52, 0x3f803f80\n\t" "v_mov_b32 v53, 0x3f803f80\n\t" "v_mov_b32 v54, 0x3f803f80\n\t" "v_mov_b32 v55, 0x3f803f80\n\t" "v_mov_b32 v51, 0\n\t" "v_mov_b32 v59, 0\n\t"
        "v_mov_b32 v60, %[tg]\n\t" "v_mov_b32 v61, -1\n\t" "s_mov_b32 s63, %[dm]\n\t"
        "s_branch .Lent%=\n"
        ".LA%=:\n\t"
        CLKW(".Lta%=", "0") REL(".Lua%=")
        "s_add_u32 s50, s36, s52\n\t" NOP(PDA)
        ABODY(NOP(PCL) LASTA(".Lwa%="), WVM(5) "v_readlane_b32 s50, v60, 0\n\t" "v_readlane_b32 s51, v60, 1\n\t"
              "s_sub_u32 s50, s50, %[tg]\n\t" "s_sub_u32 s51, s51, %[tg]\n\t" "s_min_u32 s63, s50, s51\n\t" "s_max_u32 s50, s50, s51\n\t" "s_cmp_le_u32 s50, 80\n\t"
              "s_cselect_b32 s50, s63, 0\n\t" "v_readfirstlane_b32 s49, v61\n\t" "s_max_i32 s64, s64, s50\n\t"
              "s_cmp_lg_u32 s49, -1\n\t" "s_cmov_b32 s58, s49\n\t" EXTRA "v_mov_b32 v62, s58\n\t" "ds_write_b32 v38, v62 offset:24\n\t", ".Lfail%=", 0) TSA(3)
        QKRED(BARA) ORD(88, 89) ORD(90, 91) QKPUB TSA(6) TSB(7) REDO(".Lqk%=")
        SVP(0, 0) SHRD SVP(1, 0) SVP(0, 1) SVP(1, 1) SVP(0, 2) SVP(1, 2) WLG(0) "v_readfirstlane_b32 s49, v62\n\t" SVP(0, 3) SVP(1, 3) "s_max_u32 s44, s44, s49\n\t" "s_cmp_eq_u32 s45, 63\n\t" TSA(7)
        "s_cbranch_scc1 .Llast%=\n"
        ".LB%=:\n\t"
        CLKW(".Ltb%=", "s66")
        "s_mov_b32 s47, 1\n\t"
        R2TEXT
        ".LC%=:\n\t"
        ZALL
        "s_mov_b32 s47, 5\n\t" "s_sub_i32 s63, s64, s45\n\t" "s_cmp_lt_i32 s63, 2\n\t" "s_cbranch_scc1 .Lend%=\n\t"
        "s_cmp_lt_i32 s63, " STR(XTH) "\n\t" "s_cselect_b32 s63, s59, %[dm]\n\t"
        CLKW(".Ltc%=", "s67") REL(".Luc%=")
        "s_add_u32 s50, s36, s53\n\t" NOP(PDC)
        CISSUE(LASTC(".Lwc%="), "")
        "buffer_load_dword v61, off, %[rs], s46 sc1\n\t" "s_add_u32 s46, s46, 64\n\t"
        "s_add_u32 s49, s39, 4096\n\t" NOP(PDF)
        "buffer_load_dwordx4 a[208:211], v44, %[rf], s39 offen sc1\n\t" "buffer_load_dwordx4 a[212:215], v44, %[rf], s49 offen sc1\n\t"
        "buffer_load_dwordx2 a[192:193], v44, %[rf], s40 offen sc1\n\t" "buffer_load_dwordx2 a[196:197], v44, %[rf], s40 offen offset:8 sc1\n\t"
        "buffer_load_dwordx2 a[200:201], v44, %[rf], s40 offen offset:1024 sc1\n\t" "buffer_load_dwordx2 a[204:205], v44, %[rf], s40 offen offset:1032 sc1\n\t"
#define NXC 8
        "buffer_load_dword v60, v18, %[rs], s63 offen sc1\n\t"
        CBODY(NXC, "", NXC, ".Lfail%=")
        MRED(BARC, "") ORD(84, 85) ORD(86, 87) STC TSW(6) REDO(".Lmr%=")
        SHRD SHUP TSW(7)
        NEXTP
        CWAIT(".Lqk%=", 8, "s48") QKRED("") NOP(1) QKPUB "s_branch .Lqk%=r\n"
        CWAIT(".Lmr%=", 16, "s38") WVM(0) MRED("", "") "v_min_u32 v80, -2, v80\n\t" "v_min_u32 v81, -2, v81\n\t" STC
        "s_branch .Lmr%=r\n"
        ".Llast%=:\n\t"
        "buffer_store_dwordx4 v[72:75], v47, %[rf], s41 offen sc1\n\t" "s_mov_b32 s47, 3\n\t" NOP(4) VPOST ARRIVE(12) "s_barrier\n\t"
        "s_branch .Lend%=\n"
        R2MAX
        ".Lnorm%=:\n\t"
        "s_cmp_lg_u32 %[wv], 3\n\t" "s_cbranch_scc1 .LNA%=\n\t" "s_cmp_eq_u32 s47, 0\n\t" "s_cbranch_scc1 .LA%=\n\t" "s_branch .LC%=\n"
        ".LN%=:\n\t"
        "v_mov_b64 v[92:93], -1\n\t" "v_mov_b64 v[94:95], -1\n\t" "s_mov_b32 s65, s44\n\t" "s_mov_b32 s78, -100\n\t" "s_mov_b32 s79, 4\n\t"
        "s_branch .Lent%=\n"
        ".LNA%=:\n\t" PZA0
        "s_add_u32 s50, s36, s52\n\t"
        RELN(".Lna%=")
        ABODY(NOP(PCA), LASTA(".Lxa%=") PZA, ".Lfail%=", PZNA) BARA ARRIVE(8) TSA(3)
        SV(0, 0) SV(1, 0) SV(0, 1) SV(1, 1) SV(0, 2) SV(1, 2) SV(0, 3) SV(1, 3) ZER(64) ZER(68) ZER(72) ZER(76)
        VPOST TSA(7) ZER(80) ZER(84) ZER(24) ZER(28)
        "v_add_u32 v88, 4096, v21\n\t" "v_add_u32 v89, 8192, v21\n\t" "v_add_u32 v90, 12288, v21\n\t"
        PZB0
        "s_lshl_b32 s61, s45, 7\n\t" "s_add_u32 s61, s61, " STR(PGO_R - XSYN) "\n\t" "s_cmp_lg_u32 %[wv], 0\n\t"
        WLG(0) "s_barrier\n\t" PZB
        PSW(92, 0) PSW(92, 272) PSW(92, 640) PSW(92, 912) PSW(92, 1280) PSW(92, 1552) PSW(92, 1920) PSW(92, 2192) PSW(92, 3840)
        "ds_read2_b32 v[50:51], v38 offset0:0 offset1:6\n\t"
        "s_cbranch_scc1 .LNB%=\n\t"
        "s_memrealtime s[72:73]\n"
        ".Lv0%=:\n\t"
        "ds_read_b32 v62, v38 offset:" CNT(12) "\n\t"
        "ds_read2_b32 v[112:113], v21 offset0:0 offset1:68\n\t" "ds_read2_b32 v[114:115], v21 offset0:16 offset1:84\n\t"
        "ds_read2_b32 v[116:117], v88 offset0:0 offset1:68\n\t" "ds_read2_b32 v[118:119], v88 offset0:16 offset1:84\n\t"
        "ds_read2_b32 v[120:121], v89 offset0:0 offset1:68\n\t" "ds_read2_b32 v[122:123], v89 offset0:16 offset1:84\n\t"
        "ds_read2_b32 v[124:125], v90 offset0:0 offset1:68\n\t" "ds_read2_b32 v[126:127], v90 offset0:16 offset1:84\n\t"
        WLG(0) "v_cmp_gt_u32 vcc, s48, v62\n\t" "s_cbranch_vccnz .Lv0%=\n\t"
        "s_add_u32 s50, s72, " STR(TVM) "\n\t" "s_lshl_b32 s50, s50, 17\n\t" "s_lshr_b32 s50, s50, 1\n\t"
        PKA(112, 112, 116) PKA(114, 114, 118) PKA(120, 120, 124) PKA(122, 122, 126) PKA(112, 112, 120) PKA(114, 114, 122)
        "v_lshrrev_b32 v91, 20, v51\n\t" "v_readfirstlane_b32 s49, v50\n\t"
        "v_cvt_pk_bf16_f32 v112, v112, v113\n\t" "v_cvt_pk_bf16_f32 v113, v114, v115\n\t" "v_or_b32 v114, s50, v91\n\t"
        "s_cmp_eq_u32 s45, 63\n\t" "s_cbranch_scc1 .Lv2%=\n\t"
        "s_sleep " STR(SLV) "\n"
        ".Lv2%=:\n\t"
        "buffer_store_dwordx2 v[112:113], v20, %[rf], s42 offen sc1\n\t"
        "buffer_store_dword v114, v22, %[rs], s61 offen sc1\n\t"
        "s_cmp_eq_u32 s49, s65\n\t" "s_cbranch_scc1 .LNB%=\n\t"
        "s_mov_b32 s65, s49\n\t" KHST "s_sub_u32 s49, s45, s78\n\t" "s_mov_b32 s78, s45\n\t"
        "s_cmp_gt_i32 s49, " STR(GAP) "\n\t" "s_cbranch_scc1 .LNB%=\n\t" "s_cmp_lt_u32 s45, s79\n\t" "s_cbranch_scc1 .LNB%=\n\t"
        "s_add_u32 s79, s45, 3\n\t" "s_lshl_b32 s49, s45, 6\n\t" "v_add_u32 v50, " STR(TLB + ((TLB + TLC) << 10) + ((TLB + TLC + TLP) << 20)) ", v51\n\t"
        "buffer_store_dword v50, v22, %[rs], s49 offen offset:128 sc1\n"
        ".LNB%=:\n\t"
        WLG(0) "v_readfirstlane_b32 s58, v51\n\t" EXTRA
        "s_mov_b32 s47, 3\n\t"
        "s_cmp_eq_u32 s45, 63\n\t"
        "s_cbranch_scc1 .Lend%=\n"
        ".LNC%=:\n\t" PZC0
        "s_add_u32 s50, s36, s53\n\t"
        WLG(0) RELN(".Lnc%=")
        CISSUE("", LASTC(".Lxc%="))
        CBODY(0, PZC, PZNC, ".Lfail%=") BARC
        ARRIVE(16) TSW(4)
        NEXTN
        ".Lfail%=:\n\t" KCNT
        "s_cmp_lg_u32 s47, 2\n\t" "s_cbranch_scc1 .Lnu%=\n\t"
        "s_mov_b32 s37, s36\n\t" "s_mov_b32 s36, s48\n\t" "s_sub_u32 s45, s45, 1\n\t" "s_sub_u32 s38, s38, 4\n\t" "s_sub_u32 s39, s39, " STR(EXF) "\n\t" "s_sub_u32 s40, s40, " STR(EXF) "\n\t"
        "s_sub_u32 s41, s41, " STR(EXK) "\n\t" "s_sub_u32 s42, s42, " STR(EXK) "\n\t" "s_sub_u32 s43, s43, s55\n\t" "s_sub_u32 s43, s43, s70\n\t" "s_sub_u32 s46, s46, 64\n"
        ".Lnu%=:\n\t"
        "s_mov_b32 s51, 1\n\t" "s_nop 7\n\t" WVM(0) "s_sub_u32 s98, s98, 1\n\t" "s_cbranch_scc1 .Lcap%=\n"
        ".Lent%=:\n\t"
        "s_cmp_eq_u32 s47, 1\n\t" "s_cselect_b32 s99, s66, s67\n\t" "s_cbranch_scc1 .Lnz%=\n\t" ZALL ".Lnz%=:\n\t"
        "s_cmp_eq_u32 s47, 0\n\t" "s_cselect_b32 s99, 0, s99\n\t" "s_cselect_b32 s50, s52, s53\n\t" "s_add_u32 s99, s99, s43\n\t" "s_add_u32 s50, s50, s36\n\t"
        SHRD "s_memrealtime s[72:73]\n\t"
        "s_cmp_eq_u32 %[wv], 3\n\t" "s_cselect_b32 s49, 0, 3\n\t" "s_add_u32 s49, s49, s47\n\t" "s_cmp_lg_u32 s51, 0\n\t" "s_cselect_b32 s49, s49, 7\n\t" "s_cselect_b64 vcc, -1, 0\n\t"
        "s_cselect_b32 s51, 0, 14\n\t" "s_mov_b32 s48, .Lpc%=-.Lnorm%=\n\t"
        RETA(0, ".Lua%=") RETA(1, ".Ltb%=y") RETA(2, ".Luc%=") RETA(3, ".Lna%=") RETA(5, ".Lnc%=")
        "s_getpc_b64 s[96:97]\n" ".Lpc%=:\n\t" "s_sub_u32 s96, s96, s48\n\t" "s_subb_u32 s97, s97, 0\n\t"
        SHUP "s_sub_u32 s49, s99, s72\n\t" "s_add_u32 s49, s49, s44\n"
        ".Lbump%=:\n\t"
        "s_cmp_ge_i32 s49, " STR(PREP) "\n\t" "s_cbranch_scc1 .Lok%=\n\t" "s_add_u32 s44, s44, %[tr]\n\t" "s_add_u32 s49, s49, %[tr]\n\t" "s_branch .Lbump%=\n"
        ".Lok%=:\n\t"
        "s_mov_b64 exec, 1\n\t" "v_mov_b32 v62, s44\n\t" "ds_max_u32 v38, v62\n\t" KTRC "s_mov_b64 exec, -1\n\t"
        "s_add_u32 s99, s99, s44\n\t" "s_sub_u32 s99, s99, s51\n\t"
        WLG(0) "s_memrealtime s[72:73]\n\t" "s_nop 3\n\t" "s_memrealtime s[74:75]\n\t" "s_nop 3\n"
        CLKL(".Lth%=", "s99")
        "s_setpc_b64 s[96:97]\n"
        ".Lcap%=:\n\t" "s_add_u32 s47, s47, 8\n"
        ".Lend%=:\n\t"
        "s_waitcnt vmcnt(0) lgkmcnt(0)\n\t"
        "s_mov_b32 s50, s64\n\t" "s_mov_b32 s49, s98\n\t" "s_mov_b32 s51, s68\n\t" "s_mov_b32 m0, s60"
        : "+{s[36:51]}"(sa), "+{s[52:59]}"(sc), "+{v[60:91]}"(keep), "=&{v[116:117]}"(ov), "+{a[192:207]}"(fv), "+{a[208:215]}"(fk), "+{v[8:23]}"(kd) FINEO TUNEO
        : "{v[32:47]}"(kc), [rs] "s"(rsx), [rf] "s"(rsf), [dm] "s"(__builtin_amdgcn_readfirstlane(PGO_Y + b * 128)), [tg] "s"(__builtin_amdgcn_readfirstlane(tag)), [r2] "s"(__builtin_amdgcn_readfirstlane(XR2B + flip)), [mb] "s"(__builtin_amdgcn_readfirstlane(ldsb + w * 4096)), [wv] "s"(__builtin_amdgcn_readfirstlane(w)), [tr] "s"(TR) TRCI,
          "{a[0:31]}"(wa[0]), "{a[32:63]}"(wa[1]), "{a[64:95]}"(wa[2]), "{a[96:127]}"(wa[3]), "{a[128:159]}"(wa[4]), "{a[160:191]}"(wa[5]),
          "{v[128:159]}"(wo[0]), "{v[160:191]}"(wo[1]), "{v[192:223]}"(wo[2]), "{v[224:255]}"(wo[3])
        : CL8(24, 25, 26, 27, 28, 29, 30, 31), "v48", "v49", "v50", "v51", CL8(52, 53, 54, 55, 56, 57, 58, 59), "v92", "v93", "v94", "v95", CL96, "v112", "v113", "v114", "v115", "v118", "v119", CL120,
          "s60", "s61", "s62", "s63", "s64", "s65", "s66", "s67", "s68", "s69", "s70", "s71", "s72", "s73", "s74", "s75", "s76", "s77", "s78", "s79", "s96", "s97", "s98", "s99", "vcc", "scc", "memory");
    const int code = __builtin_amdgcn_readfirstlane(sa[11]);
    xp = __builtin_amdgcn_readfirstlane(sa[14]);
    tcnt += sa[15];
    sa[15] = 0;
    if (code == 3) break;
    const int seg = __builtin_amdgcn_readfirstlane(sa[9]);
    if (code == 5) { XWAIT(seg + 2) xp = max(xp, seg + 2); sa[11] = 2; }
    else if (code == 1) { __builtin_amdgcn_raw_buffer_store_b64((u32x2){nosent(ov[0]), nosent(ov[1])}, rsx, sto, sa[0], 16); sa[11] = 2; }
    else { sc[2] += 1; sa[13] = 0; sa[11] = code - 8; sa[15] = 1; }
  }
  if (lane == 0) { dbg[(b * 4 + w) * 4] = org_; dbg[(b * 4 + w) * 4 + 2] = 0; }
  if (lane == 0) { dbg[(b * 4 + (w + 1) % 4) * 4 + 1] = sa[7] + sa[8]; dbg[(b * 4 + w) * 4 + 3] = tcnt; if (b == 0 && w == 3) { dbg[3500] = sc[6]; dbg[3504] = sa[8]; } }
  if (b == 0 && w == 0) dbg[4096 + lane] = (hst[lane >> 5] >> (lane & 31)) & 1;
  LBAR();
  if (tid == 0) __builtin_amdgcn_raw_buffer_store_b32(1u, rsx, 0, PGO_D + vcu * 128, 16);
  if (b == 0 && w == 1) {
    for (int it = 0; it < SPIN_CAP; ++it) {
      asm volatile("" ::: "memory");
      if (__ballot(__builtin_amdgcn_raw_buffer_load_b32(rsx, lane * 128, PGO_S, 16) != tag || __builtin_amdgcn_raw_buffer_load_b32(rsx, lane * 128 + 8192, PGO_S, 16) != tag) == 0) break;
    }
    if (lane == 0) __builtin_amdgcn_raw_buffer_store_b32(tag, rsx, 0, XEPO, 16);
  }
}

#define XSTAT 3
#define XVM 8
#define XP0 -1
#define XSC " sc1"
#define XPITCH 1056
#define XSLOT (32 * XPITCH)
#define XW(HB, t) "a[" STR(HB) "+8*xk+4*" #t ":" STR(HB) "+8*xk+4*" #t "+3]"
#define XF(rt) "v[96+8*(xk&3)+4*" #rt ":96+8*(xk&3)+4*" #rt "+3]"
#define XFN(rt) "v[96+8*((xk+3)&3)+4*" #rt ":96+8*((xk+3)&3)+4*" #rt "+3]"
#define XA(AC, o) "v[" STR(AC) "+" #o ":" STR(AC) "+" #o "+3]"
#define XM1(d, a, b, FIRST) ".if (xk == 0) && " STR(FIRST) "\n\tv_mfma_f32_16x16x32_bf16 " d ", " a ", " b ", 0\n\t.else\n\tv_mfma_f32_16x16x32_bf16 " d ", " a ", " b ", " d "\n\t.endif\n\t"
#define XMF_QK(i, HB, AC, F) XMF_QK##i(HB, AC, F)
#define XMF_QK0(HB, AC, F) XM1(XA(AC, 0), XW(HB, 0), XF(0), F)
#define XMF_QK1(HB, AC, F) XM1(XA(AC, 4), XW(HB, 1), XF(0), F)
#define XMF_QK2(HB, AC, F) XM1(XA(AC, 8), XW(HB, 0), XF(1), F)
#define XMF_QK3(HB, AC, F) XM1(XA(AC, 12), XW(HB, 1), XF(1), F)
#define XMF_V(i, HB, AC, F) XMF_V##i(HB, AC, F)
#define XMF_V0(HB, AC, F) XM1(XA(AC, 0), XF(0), XW(HB, 0), F)
#define XMF_V1(HB, AC, F) XM1(XA(AC, 8), XF(0), XW(HB, 1), F)
#define XMF_V2(HB, AC, F) XM1(XA(AC, 4), XF(1), XW(HB, 0), F)
#define XMF_V3(HB, AC, F) XM1(XA(AC, 12), XF(1), XW(HB, 1), F)
#define XRD(rt, CUR, NXT) ".if xk < 13\n\tds_read_b128 " XFN(rt) ", " CUR " offset:64*(xk+3)+" STR(rt * 16 * XPITCH) "\n\t.else\n\t" \
                          "ds_read_b128 " XFN(rt) ", " NXT " offset:64*(xk-13)+" STR(rt * 16 * XPITCH) "\n\t.endif\n\t"
#define XCV(EPI, PV, i) ".if " STR(EPI) " && (xk >= 1) && (xk < 3)\n\tv_cvt_pk_bf16_f32 v[128+4*(xk-1)+" #i "], v[" STR(PV) "+8*(xk-1)+2*" #i "], v[" STR(PV) "+8*(xk-1)+2*" #i "+1]\n\t.endif\n\t"
#define XHALF(MF, HB, AC, PV, EPI, CUR, NXT, DSLOT, GOFF)                                                   \
  ".set xk, 0\n\t"                                                                                          \
  ".rept 16\n\t"                                                                                            \
  "s_waitcnt lgkmcnt(4)\n\t"                                                                                \
  MF(0, HB, AC, EPI) XRD(0, CUR, NXT) XCV(EPI, PV, 0)                                                       \
  ".if (xk >= 5) && (xk < 13)\n\t"                                                                          \
  "s_add_u32 m0, m0, " STR(XPITCH) "\n\t"                                                                   \
  ".endif\n\t"                                                                                              \
  MF(1, HB, AC, EPI) XRD(1, CUR, NXT) XCV(EPI, PV, 1)                                                       \
  ".if (xk >= 5) && (xk < 13)\n\t"                                                                          \
  "s_add_u32 %[dg], %[dg], 2048\n\t"                                                                        \
  ".endif\n\t"                                                                                              \
  MF(2, HB, AC, EPI) XCV(EPI, PV, 2)                                                                        \
  ".if (xk >= 4) && (xk < 12)\n\t"                                                                          \
  "buffer_load_dwordx4 %[l16], %[rx], %[dg] offen lds\n\t"                                                  \
  ".endif\n\t"                                                                                              \
  ".if xk == 0\n\t"                                                                                         \
  "s_add_u32 m0, %[dl], " STR((DSLOT) * XSLOT) "\n\t" "s_add_u32 %[dg], %[gb], " STR(GOFF) "\n\t"           \
  ".endif\n\t"                                                                                              \
  ".if " STR(EPI) " && (xk == " STR(XSTAT) ")\n\t"                                                                       \
  "buffer_store_dwordx4 v[128:131], v137, %[rf], %[sg] offen" XSC "\n\t"                                       \
  ".endif\n\t"                                                                                              \
  ".if " STR(EPI) " && (xk == 13)\n\t"                                                                      \
  "v_mov_b32 v137, %[l16]\n\t" "v_mov_b32 v138, %[lp2]\n\t"                                                 \
  ".endif\n\t"                                                                                              \
  ".if " STR(EPI) " && (xk == 14)\n\t"                                                                      \
  "s_add_u32 %[sg], %[sg], " STR(NH * 8 * 1024) "\n\t"                                                      \
  "v_add_u32 v136, 1, v136\n\t"                                                                             \
  ".endif\n\t"                                                                                              \
  ".if !" STR(EPI) " && (xk == 1)\n\t"                                                                      \
  "buffer_store_dword v136, %[fo], %[rp], 0 offen sc1\n\t"                                                  \
  ".endif\n\t"                                                                                              \
  MF(3, HB, AC, EPI) XCV(EPI, PV, 3)                                                                        \
  ".if xk == 0\n\t"                                                                                         \
  "s_waitcnt vmcnt(" STR(XVM) ")\n\t" "s_barrier\n\t"                                                               \
  ".endif\n\t"                                                                                              \
  ".if xk == 2\n\t"                                                                                         \
  "s_min_u32 %[dg], %[dg], " STR(S_LEN * DM * 2 - 16384 + 1024) "\n\t"                                      \
  ".endif\n\t"                                                                                              \
  ".if " STR(EPI) " && (xk == " STR(XSTAT) ")\n\t"                                                                       \
  "buffer_store_dwordx4 v[132:135], v138, %[rf], %[sg] offen" XSC "\n\t"                                       \
  ".endif\n\t"                                                                                              \
  ".set xk, xk+1\n\t"                                                                                       \
  ".endr\n\t"
#define XLOOP(MF)                                                                                           \
  "s_mov_b32 %[kp], m0\n\t"                                                                                 \
  "v_mov_b32 v136, %[p0]\n\t" "v_mov_b32 v137, " STR(OOR) "\n\t" "v_mov_b32 v138, " STR(OOR) "\n\t"                                                                                \
  "ds_read_b128 v[96:99], %[r0]\n\tds_read_b128 v[100:103], %[r0] offset:" STR(16 * XPITCH) "\n\t"          \
  "ds_read_b128 v[104:107], %[r0] offset:64\n\tds_read_b128 v[108:111], %[r0] offset:64+" STR(16 * XPITCH) "\n\t"   \
  "ds_read_b128 v[112:115], %[r0] offset:128\n\tds_read_b128 v[116:119], %[r0] offset:128+" STR(16 * XPITCH) "\n"   \
  ".Lx%=:\n\t"                                                                                              \
  XHALF(MF, 0, 64, 80, 1, "%[r0]", "%[r1]", 3, 66560) XHALF(MF, 128, 64, 80, 0, "%[r1]", "%[r2]", 0, 131072) \
  XHALF(MF, 0, 80, 64, 1, "%[r2]", "%[r3]", 1, 132096) XHALF(MF, 128, 80, 64, 0, "%[r3]", "%[r0]", 2, 196608) \
  "s_add_u32 %[gb], %[gb], 131072\n\t"                                                                      \
  "s_sub_u32 %[cnt], %[cnt], 1\n\t"                                                                         \
  "s_cmp_lg_u32 %[cnt], 0\n\t"                                                                              \
  "s_cbranch_scc1 .Lx%=\n\t"                                                                                \
  "s_mov_b32 m0, %[kp]\n\t"                                                                                 \
  "s_nop 7\n\t" "s_waitcnt lgkmcnt(0)"

template <bool ISV>
__device__ __forceinline__ void x_role(unsigned char* lds, const u16* __restrict__ x, const u16* __restrict__ Wm, int fo0, int head,
                                       __amdgpu_buffer_rsrc_t rsf, u32* prog, u32 tag, int lane, int w) {
  const int ldsb = (int)(size_t)(LAS unsigned char*)lds;
  const __amdgpu_buffer_rsrc_t rsxin = __builtin_amdgcn_make_buffer_rsrc((void*)x, 0, S_LEN * DM * 2, 0x00020000);
  const __amdgpu_buffer_rsrc_t rsp = __builtin_amdgcn_make_buffer_rsrc((void*)prog, 0, 128, 0x00020000);
  const int lr = lane & 15, lg = lane >> 4;
  const int xrd = ldsb + lr * XPITCH + lg * 16;
  const int fob = fo0 + (ISV ? (head * 8 + 2 * w) : (head * 8 + w)) * 1024;
  const int p2 = ISV ? 1024 : 4096;
  u32x32 xw[8];
  {
    const __amdgpu_buffer_rsrc_t rw = __builtin_amdgcn_make_buffer_rsrc((void*)Wm, 0, DM * HD * 2, 0x00020000);
    const int vq = (lane >> 4) * (HD * 2) + head * 256 + (lane & 15) * 16;
#define XWISSUE(c) _Pragma("unroll") for (int j = 0; j < 8; ++j)                                                       \
      asm volatile("s_mov_b32 m0, %0\n\ts_nop 3\n\tbuffer_load_dwordx4 %1, %2, %3 offen lds"                           \
                   :: "s"(ldsb + ((c) & 3) * XSLOT + (8 * w + j) * XPITCH), "v"(vq), "s"(rw), "s"((128 * (c) + 32 * w + 4 * j) * (HD * 2)) : "memory");
#define XXISSUE(h) _Pragma("unroll") for (int j = 0; j < 8; ++j)                                                       \
      asm volatile("s_mov_b32 m0, %0\n\ts_nop 3\n\tbuffer_load_dwordx4 %1, %2, %3 offen lds"                           \
                   :: "s"(ldsb + (h) * XSLOT + (8 * w + j) * XPITCH), "v"(lane * 16), "s"(rsxin), "s"((((h) >> 1) * 32 + 8 * w + j) * 2048 + ((h) & 1) * 1024) : "memory");
    XWISSUE(0) XWISSUE(1) XWISSUE(2) XWISSUE(3)
    const int q = lr >> 2, p = lr & 3;
#pragma unroll
    for (int c = 0; c < 8; ++c) {
      asm volatile("s_waitcnt vmcnt(24)\n\ts_barrier" ::: "memory");
#pragma unroll
      for (int s = 0; s < 4; ++s)
#pragma unroll
        for (int t = 0; t < 2; ++t) {
          const unsigned char* base = lds + (c & 3) * XSLOT + (8 * s + 2 * lg) * XPITCH + q * 256 + w * 64 + p * 16 + t * 8;
          const u32x2 t0 = __builtin_bit_cast(u32x2, __builtin_amdgcn_ds_read_tr16_b64_v4i16((LAS v4i16_t*)(base)));
          const u32x2 t1 = __builtin_bit_cast(u32x2, __builtin_amdgcn_ds_read_tr16_b64_v4i16((LAS v4i16_t*)(base + XPITCH)));
          const int o = ((4 * c + s) * 2 + t) * 4;
          xw[o >> 5][(o & 31) + 0] = t0[0]; xw[o >> 5][(o & 31) + 1] = t0[1]; xw[o >> 5][(o & 31) + 2] = t1[0]; xw[o >> 5][(o & 31) + 3] = t1[1];
        }
      LBAR();
      if (c < 4) { XWISSUE(c + 4) } else if (c < 7) { XXISSUE(c - 4) }
    }
  }
  asm volatile("s_waitcnt vmcnt(8)\n\ts_barrier" ::: "memory");
  if (lane == 0 && w == 0) g_dbg[4352 + (fo0 == FO_K ? 0 : fo0 == FO_V ? 32 : 64) + head] = rt32();
  u32x16 xacc;
  {
    u32 kp, dg; int gb = 8 * w * 2048, cnt = NSEG / 2, sg = fob - NH * 8 * 1024;
#define XASM(MF)                                                                                            \
    asm volatile(XLOOP(MF)                                                                                  \
                 : "=&{v[80:95]}"(xacc), [kp] "=&s"(kp), [dg] "=&s"(dg), [gb] "+s"(gb), [cnt] "+s"(cnt), [sg] "+s"(sg) \
                 : [r0] "v"(xrd), [r1] "v"(xrd + XSLOT), [r2] "v"(xrd + 2 * XSLOT), [r3] "v"(xrd + 3 * XSLOT), [l16] "v"(lane * 16), [lp2] "v"(lane * 16 + p2), \
                   [rx] "s"(rsxin), [rf] "s"(rsf), [rp] "s"(rsp), [fo] "v"(lane == 0 && w == 0 ? 0 : OOR), [dl] "s"(ldsb + 8 * w * XPITCH), [p0] "s"(tag + XP0), \
                   "{a[0:31]}"(xw[0]), "{a[32:63]}"(xw[1]), "{a[64:95]}"(xw[2]), "{a[96:127]}"(xw[3]),      \
                   "{a[128:159]}"(xw[4]), "{a[160:191]}"(xw[5]), "{a[192:223]}"(xw[6]), "{a[224:255]}"(xw[7]) \
                 : CL64, CL72, CL96, CL112, CL120, CL8(128, 129, 130, 131, 132, 133, 134, 135), "v136", "v137", "v138", "scc", "memory");
    if (ISV) { XASM(XMF_V) } else { XASM(XMF_QK) }
  }
#pragma unroll
  for (int jj = 0; jj < 2; ++jj) {
    u32x4 pk;
#pragma unroll
    for (int e = 0; e < 4; ++e) pk[e] = pack2(__uint_as_float(xacc[8 * jj + 2 * e]), __uint_as_float(xacc[8 * jj + 2 * e + 1]));
    __builtin_amdgcn_raw_buffer_store_b128(pk, rsf, lane * 16 + p2 * jj, fob + (NSEG - 1) * (NH * 8 * 1024), 16);
  }
  asm volatile("s_waitcnt vmcnt(0)\n\ts_barrier" ::: "memory");
  if (lane == 0 && w == 0) __hip_atomic_store(prog, tag + NSEG + 8, RLX_AGENT);
}

#define SU_CP 4288
#define SU_U 0
#define SU_K (2 * SU_CP)
#define SU_Y (SU_K + 10400)
#define SU_CH (SU_Y + 4096)
#define SST(i)
__device__ __forceinline__ int koff(int i) {
  const int x = i & 127;
  return i < 256 ? (32 * (i >> 7) + 16 * ((i >> 2) & 1) + (x >> 3)) * 16 + (i & 3) * 4
       : i < 384 ? 1024 + x * 4
       : i < 512 ? 1024 + (32 + 16 * (x >> 6) + ((x >> 1) & 15)) * 16 + (2 * ((x >> 5) & 1) + (x & 1)) * 4
                 : 2048 + (16 * ((i - 512) >> 6) + (i & 15)) * 16 + ((i >> 4) & 3) * 4;
}
struct SsmIn { u32x4 ka, kb; u32x2 kc; u32 kd, ke[4]; u32x4 uv[4]; u32x2 up, dsk; };
#define SSM_GOT(in) asm volatile("" : "+v"(in.ka), "+v"(in.kb), "+v"(in.kc), "+v"(in.kd), "+v"(in.ke[0]), "+v"(in.ke[1]), "+v"(in.ke[2]), "+v"(in.ke[3]), \
                                      "+v"(in.uv[0]), "+v"(in.uv[1]), "+v"(in.uv[2]), "+v"(in.uv[3]), "+v"(in.up), "+v"(in.dsk))
__device__ __forceinline__ void ssm_load(SsmIn& in, int d0, __amdgpu_buffer_rsrc_t rst, __amdgpu_buffer_rsrc_t rsd, int tid, int lane, int w) {
  const int so = 2 * SSM_L * SSM_D * 2 + (d0 + w) * 3072, ia = lane < 32 ? 8 * lane : 128 + 4 * lane;
  in.ka = __builtin_amdgcn_raw_buffer_load_b128(rst, koff(ia), so, 16);
  in.kb = __builtin_amdgcn_raw_buffer_load_b128(rst, koff(ia + 4), so, 16);
  in.kc = __builtin_amdgcn_raw_buffer_load_b64(rst, koff(384 + 2 * lane), so, 16);
  in.kd = __builtin_amdgcn_raw_buffer_load_b32(rst, koff(386 + 2 * lane), so, 16);
#pragma unroll
  for (int i = 0; i < 4; ++i) in.ke[i] = __builtin_amdgcn_raw_buffer_load_b32(rst, koff(512 + 64 * (i >> 1) + (i & 1) + lane), so, 16);
#pragma unroll
  for (int i = 0; i < 4; ++i) in.uv[i] = __builtin_amdgcn_raw_buffer_load_b128(rst, tid * 16, d0 * 4096 + i * 4096, 16);
  in.up = __builtin_amdgcn_raw_buffer_load_b64(rst, tid ? tid * 16 - 8 : OOR, d0 * 4096 + 3 * 4096, 16);
  in.dsk = __builtin_amdgcn_raw_buffer_load_b64(rsd, 0, d0 * 2, 0);
}
__device__ __forceinline__ float reslo(float v, u32 h) { return v - __uint_as_float(h << 16); }
__device__ __forceinline__ float reshi(float v, u32 h) { return v - __uint_as_float(h & 0xffff0000u); }
__device__ __forceinline__ int ssm_unit(unsigned char* sm, int d0, SsmIn& in, bool pend, __amdgpu_buffer_rsrc_t rst, __amdgpu_buffer_rsrc_t rsx, int fb,
                                        __amdgpu_buffer_rsrc_t rsd, int tid, int lane, int w) {
  unsigned char* cb = sm + w * SU_CH;
  SST(0)
  const int one = tid == 0 ? 0 : OOR;
  u32 tk = __builtin_amdgcn_raw_ptr_buffer_atomic_add_i32(1, rsx, one, XTKT, 0);
  const int r = lane & 31, h = lane >> 5;
#pragma unroll
  for (int c = 0; c < 4; ++c) {
    const u32 sel = (c & 1) ? 0x07060302u : 0x05040100u;
    u32x4 e, o;
#pragma unroll
    for (int i = 0; i < 4; ++i) {
      e[i] = __builtin_amdgcn_perm(in.uv[i][2 + (c >> 1)], in.uv[i][c >> 1], sel);
      o[i] = __builtin_amdgcn_perm(in.uv[i][c >> 1], i ? in.uv[i ? i - 1 : 0][2 + (c >> 1)] : in.up[c >> 1], sel);
    }
    *(u32x4*)(sm + c * SU_CH + SU_U + 96 + tid * 16) = e;
    *(u32x4*)(sm + c * SU_CH + SU_U + SU_CP + 96 + tid * 16) = o;
  }
  SST(1)
  {
    unsigned char* kr = cb + SU_K + ((lane >> 2) + 1) * 80 + (3 - (lane & 3)) * 16;
    const f32x4 a = __builtin_bit_cast(f32x4, in.ka), b = __builtin_bit_cast(f32x4, in.kb);
    const bool dn = lane < 32;
    const float v[8] = {a[0], dn ? a[1] : a[0], dn ? a[2] : a[1], dn ? a[3] : a[2], dn ? b[0] : a[2], dn ? b[1] : a[2], dn ? b[2] : a[3], dn ? b[3] : b[0]};
    u32x4 hi, lo;
#pragma unroll
    for (int e = 0; e < 4; ++e) { hi[e] = pack2(v[7 - 2 * e], v[6 - 2 * e]); lo[e] = pack2(reslo(v[7 - 2 * e], hi[e]), reshi(v[6 - 2 * e], hi[e])); }
    *(u32x4*)(kr) = hi; *(u32x4*)(kr + 5200) = lo;
    {
      const float v0 = __uint_as_float(in.kc[0]), v1 = __uint_as_float(in.kc[1]), v2 = __uint_as_float(in.kd);
      hi = (u32x4){pack2(v2, v2), pack2(v1, v1), pack2(v1, v0), pack2(v0, v0)};
      const float r0 = reshi(v0, hi[3]), r1 = reshi(v1, hi[1]), r2 = reshi(v2, hi[0]);
      *(u32x4*)(kr + 1280) = hi; *(u32x4*)(kr + 1280 + 5200) = (u32x4){pack2(r2, r2), pack2(r1, r1), pack2(r1, r0), pack2(r0, r0)};
    }
#pragma unroll
    for (int j = 0; j < 2; ++j) {
      const float v0 = __uint_as_float(in.ke[2 * j]), v1 = __uint_as_float(in.ke[2 * j + 1]);
      const u32 h0 = pack2(v0, v0), h1 = pack2(v1, v1), sel = (lane & 1) ? 0x01000100u : 0x05040100u;
      const float r0 = reshi(v0, h0), r1 = reshi(v1, h1);
      const u32 l0 = pack2(r0, r0), l1 = pack2(r1, r1);
      *(u32x4*)(kr + 2560 + 1280 * j) = (u32x4){h1, __builtin_amdgcn_perm(h0, h1, sel), h0, h0};
      *(u32x4*)(kr + 2560 + 1280 * j + 5200) = (u32x4){l1, __builtin_amdgcn_perm(l0, l1, sel), l0, l0};
    }
  }
  SST(2)
  f32x4 sk[8];
  {
    const u32 d01 = __builtin_amdgcn_readfirstlane(in.dsk[0]), d23 = __builtin_amdgcn_readfirstlane(in.dsk[1]);
#pragma unroll
    for (int i = 0; i < 4; ++i)
      asm("v_dot2_f32_bf16 %0, %8, %12, 0\n\tv_dot2_f32_bf16 %1, %8, %13, 0\n\tv_dot2_f32_bf16 %2, %9, %14, 0\n\tv_dot2_f32_bf16 %3, %9, %15, 0\n\t"
          "v_dot2_f32_bf16 %4, %10, %12, 0\n\tv_dot2_f32_bf16 %5, %10, %13, 0\n\tv_dot2_f32_bf16 %6, %11, %14, 0\n\tv_dot2_f32_bf16 %7, %11, %15, 0\n\t"
          "v_cvt_pk_bf16_f32 %0, 0, %0\n\tv_cvt_pk_bf16_f32 %1, 0, %1\n\tv_cvt_pk_bf16_f32 %2, 0, %2\n\tv_cvt_pk_bf16_f32 %3, 0, %3\n\t"
          "v_cvt_pk_bf16_f32 %4, 0, %4\n\tv_cvt_pk_bf16_f32 %5, 0, %5\n\tv_cvt_pk_bf16_f32 %6, 0, %6\n\tv_cvt_pk_bf16_f32 %7, 0, %7"
          : "=&v"(sk[2 * i][0]), "=&v"(sk[2 * i][1]), "=&v"(sk[2 * i][2]), "=&v"(sk[2 * i][3]),
            "=&v"(sk[2 * i + 1][0]), "=&v"(sk[2 * i + 1][1]), "=&v"(sk[2 * i + 1][2]), "=&v"(sk[2 * i + 1][3])
          : "v"(in.uv[i][0]), "v"(in.uv[i][1]), "v"(in.uv[i][2]), "v"(in.uv[i][3]), "s"(d01 & 0xffffu), "s"(d01 & 0xffff0000u), "s"(d23 & 0xffffu), "s"(d23 & 0xffff0000u));
  }
  asm volatile("s_waitcnt vmcnt(0)" : "+v"(tk), "+v"(sk[0]), "+v"(sk[1]), "+v"(sk[2]), "+v"(sk[3]), "+v"(sk[4]), "+v"(sk[5]), "+v"(sk[6]), "+v"(sk[7]) :: "memory");
  if (tid == 0) *(u32*)(sm + 4 * SU_CH) = tk + 65u;
  LBAR();
  SST(3)
  if (pend) __builtin_amdgcn_raw_ptr_buffer_atomic_add_i32(1, rsx, one, XYDN, 0);
  const int nxt = __builtin_amdgcn_readfirstlane(*(const u32*)(sm + 4 * SU_CH));
  if (nxt < 256) ssm_load(in, 4 * nxt, rst, rsd, tid, lane, w);
  SST(4)
  f32x16 cv0, cv1;
  {
    const int m0 = 48 - 15 + 8 * h + r;
    const int cbo = (int)(size_t)(LAS unsigned char*)cb;
    int ua = cbo + SU_U + (m0 & 1) * SU_CP + ((m0 + 1) & ~1) * 2 - 32, ub = ua + 2048;
    const int kb = cbo + SU_K + 16 * h;
    int a1 = kb + (33 + r) * 80, a0 = kb + (1 + r) * 80, cnt = 32;
#define CRA(S, ad, O) "ds_read2_b32 " RNG("v", S, 2) ", " ad " offset0:" STR((O) / 4) " offset1:" STR((O) / 4 + 1) "\n\t" \
                      "ds_read2_b32 " RNG("v", (S) + 2, 2) ", " ad " offset0:" STR((O) / 4 + 2) " offset1:" STR((O) / 4 + 3) "\n\t"
#define CRB(S, ad, O) "ds_read_b128 " RNG("v", S, 4) ", " ad " offset:" STR(O) "\n\t"
#define CMF(C, A, B) "v_mfma_f32_32x32x16_bf16 " RNG("v", C, 16) ", " RNG("v", A, 4) ", " RNG("v", B, 4) ", " RNG("v", C, 16) "\n\t"
#define CUPD "v_add_u32 %[ua], 64, %[ua]\n\t" "v_subrev_u32 %[a1], 80, %[a1]\n\t" "v_subrev_u32 %[a0], 80, %[a0]\n\t" "v_max_i32 %[a0], %[a0], %[kb]\n\t"
#define CHALF(S, N, O, UPD, UPD2)                                                                 \
    WLG(5) CMF(64, S, (S) + 8) UPD CRA(N, "%[ua]", O)                                             \
    WLG(6) CMF(80, S, (S) + 16) UPD2 CRB((N) + 8, "%[a0]", O) CRB((N) + 16, "%[a1]", O)           \
    WLG(6) CMF(80, (S) + 4, (S) + 8) CRA((N) + 4, "%[ub]", O)                                     \
    WLG(7) CMF(64, S, (S) + 12) CRB((N) + 12, "%[a0]", (O) + 5200)                                \
    WLG(7) CMF(80, S, (S) + 20) CRB((N) + 20, "%[a1]", (O) + 5200)                                \
    CMF(80, (S) + 4, (S) + 12)
    asm volatile(
        CRA(96, "%[ua]", 32) CRB(104, "%[a0]", 32) CRB(112, "%[a1]", 32) CRA(100, "%[ub]", 32) CRB(108, "%[a0]", 32 + 5200) CRB(116, "%[a1]", 32 + 5200)
        ZER(64) ZER(68) ZER(72) ZER(76) ZER(80) ZER(84) ZER(88) ZER(92)
        ".Lc%=:\n\t"
        CHALF(96, 120, 0, "", "")
        CHALF(120, 96, 32, CUPD, "v_add_u32 %[ub], 64, %[ub]\n\t")
        "s_sub_u32 %[cnt], %[cnt], 1\n\t"
        "s_cmp_lg_u32 %[cnt], 0\n\t"
        "s_cbranch_scc1 .Lc%=\n\t"
        "s_nop 7\n\t" "s_nop 3\n\t" WLG(0)
        : "=&{v[64:79]}"(cv0), "=&{v[80:95]}"(cv1), [ua] "+v"(ua), [ub] "+v"(ub), [a0] "+v"(a0), [a1] "+v"(a1), [cnt] "+s"(cnt)
        : [kb] "v"(kb)
        : CL96, CL112, CL120, CL8(128, 129, 130, 131, 132, 133, 134, 135), CL8(136, 137, 138, 139, 140, 141, 142, 143), "scc", "memory");
  }
  SST(5)
  SSM_GOT(in);
#pragma unroll
  for (int g = 0; g < 2; ++g)
#pragma unroll
    for (int i = 0; i < 4; ++i) {
      const f32x16& c = g ? cv1 : cv0;
      *(u32x2*)(cb + SU_Y + (32 * g + r) * 64 + ((i ^ (r >> 1)) & 3) * 16 + 8 * h) = (u32x2){pack2(c[4 * i], c[4 * i + 1]), pack2(c[4 * i + 2], c[4 * i + 3])};
    }
  LBAR();
  SST(6)
  {
    u32x4 c[4];
#pragma unroll
    for (int j = 0; j < 4; ++j) c[j] = *(const u32x4*)(sm + j * SU_CH + SU_Y + (tid >> 2) * 64 + ((tid ^ (tid >> 3)) & 3) * 16);
#pragma unroll
    for (int i = 0; i < 4; ++i) {
      u32x4 y;
      asm("v_dot2_f32_bf16 %4, %12, %16, %4\n\tv_dot2_f32_bf16 %5, %13, %16, %5\n\tv_dot2_f32_bf16 %6, %14, %16, %6\n\tv_dot2_f32_bf16 %7, %15, %16, %7\n\t"
          "v_dot2_f32_bf16 %8, %12, %17, %8\n\tv_dot2_f32_bf16 %9, %13, %17, %9\n\tv_dot2_f32_bf16 %10, %14, %17, %10\n\tv_dot2_f32_bf16 %11, %15, %17, %11\n\t"
          "v_cvt_pk_bf16_f32 %0, %4, %5\n\tv_cvt_pk_bf16_f32 %1, %6, %7\n\tv_cvt_pk_bf16_f32 %2, %8, %9\n\tv_cvt_pk_bf16_f32 %3, %10, %11"
          : "=&v"(y[0]), "=&v"(y[1]), "=&v"(y[2]), "=&v"(y[3]),
            "+v"(sk[2 * i][0]), "+v"(sk[2 * i][1]), "+v"(sk[2 * i][2]), "+v"(sk[2 * i][3]),
            "+v"(sk[2 * i + 1][0]), "+v"(sk[2 * i + 1][1]), "+v"(sk[2 * i + 1][2]), "+v"(sk[2 * i + 1][3])
          : "v"(c[0][i]), "v"(c[1][i]), "v"(c[2][i]), "v"(c[3][i]), "s"(0x00003f80u), "s"(0x3f800000u));
      __builtin_amdgcn_raw_buffer_store_b128(y, rst, tid * 16, SSM_L * SSM_D * 2 + d0 * 4096 + i * 4096, 16);
    }
  }
  if (nxt >= 256) {
    asm volatile("s_waitcnt vmcnt(0)\n\ts_barrier" ::: "memory");
    __builtin_amdgcn_raw_ptr_buffer_atomic_add_i32(1, rsx, one, XYDN, 0);
  }
  SST(7)
  return nxt;
}

#define L_TOTAL 163840
__device__ __forceinline__ int ldw(__amdgpu_buffer_rsrc_t rs, int off) {
  asm volatile("" ::: "memory");
  return __builtin_amdgcn_readfirstlane(__builtin_amdgcn_raw_buffer_load_b32(rs, 0, off, 16));
}
#define GF(rt) "v[64+8*(xk&7)+4*" #rt ":64+8*(xk&7)+4*" #rt "+3]"
#define GW2 "v[128+4*xk:128+4*xk+3]"
#define GW3 "v[24+4*(xk&1):24+4*(xk&1)+3]"
#define GLOAD(S, K) "s_xor_b32 %[tq], " S ", 1024*(" K ")\n\t"
#define GTR(K) "ds_read_b128 v[24+4*((" K ")&1):24+4*((" K ")&1)+3], %[wl] offset:1024*((" K ")&31)\n\t"
#define GPACE 0
#define YAUX 16
#define UDELAY 100
#define GNOSSM 1
#define GNG 3
#define GS0 22
__device__ __forceinline__ void g_role(unsigned char* lds, const u16* __restrict__ Wo, u16* __restrict__ out, __amdgpu_buffer_rsrc_t rsx,
                                       __amdgpu_buffer_rsrc_t rsf, int fb, u32 tag, int grp, int sl, int w) {
  const int lane = fresh_lane();
  const int lr = lane & 15, lg = lane >> 4;
  const int rot = (2 * sl + 8 * w) & 31;
  const int ldsb = (int)(size_t)(LAS unsigned char*)lds;
  u32x32 xw[8], w2[4];
  LBAR();
  if (lane == 0 && w == 0) g_dbg[4480 + 16 * grp + sl] = rt32();
  {
    const __amdgpu_buffer_rsrc_t rw = __builtin_amdgcn_make_buffer_rsrc((void*)Wo, 0, DM * HD * 2, 0x00020000);
    const int vq = (lane >> 3) * (DM * 2) + sl * 128 + (lane & 7) * 16;
    const int sq = (1024 * w + rot * 32) * (DM * 2);
#define GSLOT(c) ((c) < 8 ? (((c) & 3) == 3 ? 131072 + w * 8192 : w * 32768 + 8192 * (((c) & 3) + 1)) : (((c) - 8) % 3 == 2 ? 131072 + w * 8192 : w * 32768 + 8192 * (((c) - 8) % 3 + 2)))
#define GISSUE(c) _Pragma("unroll") for (int j = 0; j < 8; ++j)                                                        \
      asm volatile("s_mov_b32 m0, %0\n\ts_nop 3\n\tbuffer_load_dwordx4 %1, %2, %3 offen lds"                           \
                   :: "s"(ldsb + GSLOT(c) + j * 1024), "v"(vq), "s"(rw), "s"(sq ^ ((64 * (c) + 8 * j) * DM * 2)) : "memory");
    GISSUE(0) GISSUE(1) GISSUE(2) GISSUE(3)
    const int q = lr >> 2, p = lr & 3;
    u32x4 w3[16];
#pragma unroll
    for (int c = 0; c < 16; ++c) {
      if (c < 5) waitvm<24>(); else if (c < 14) waitvm<16>(); else if (c == 14) waitvm<8>(); else waitvm<0>();
      u32x4 f3[2];
#pragma unroll
      for (int s = 0; s < 2; ++s)
#pragma unroll
        for (int t = 0; t < 4; ++t) {
          const unsigned char* base = lds + GSLOT(c) + (32 * s + 8 * lg + q) * 128 + (t < 2 ? p * 16 + t * 8 : t * 32 + p * 8);
          const u32x2 t0 = __builtin_bit_cast(u32x2, __builtin_amdgcn_ds_read_tr16_b64_v4i16((LAS v4i16_t*)(base)));
          const u32x2 t1 = __builtin_bit_cast(u32x2, __builtin_amdgcn_ds_read_tr16_b64_v4i16((LAS v4i16_t*)(base + 512)));
          const int o = t < 2 ? ((2 * c + s) * 2 + t) * 4 : (2 * c + s) * 4;
          if (t < 2) { xw[o >> 5][(o & 31) + 0] = t0[0]; xw[o >> 5][(o & 31) + 1] = t0[1]; xw[o >> 5][(o & 31) + 2] = t1[0]; xw[o >> 5][(o & 31) + 3] = t1[1]; }
          else if (t == 2) { w2[o >> 5][(o & 31) + 0] = t0[0]; w2[o >> 5][(o & 31) + 1] = t0[1]; w2[o >> 5][(o & 31) + 2] = t1[0]; w2[o >> 5][(o & 31) + 3] = t1[1]; }
          else f3[s] = (u32x4){t0[0], t0[1], t1[0], t1[1]};
        }
      asm volatile("s_waitcnt lgkmcnt(0)" ::: "memory");
      if (c < 8) { *(u32x4*)(lds + w * 32768 + (2 * c) * 1024 + lane * 16) = f3[0]; *(u32x4*)(lds + w * 32768 + (2 * c + 1) * 1024 + lane * 16) = f3[1]; }
      else { w3[2 * (c - 8)] = f3[0]; w3[2 * (c - 8) + 1] = f3[1]; }
      if (GPACE) __builtin_amdgcn_s_sleep(GPACE);
      if (c < 4) { GISSUE(c + 4) } else if (c >= 5 && c < 13) { GISSUE(c + 3) }
    }
#pragma unroll
    for (int i = 0; i < 16; ++i) *(u32x4*)(lds + w * 32768 + (16 + i) * 1024 + lane * 16) = w3[i];
    LBAR();
  }
  if (lane == 0 && w == 0) g_dbg[4544 + 16 * grp + sl] = rt32();
  wait_start(rsx, fb, tag, true);
  const int wl = ldsb + w * 32768 + lane * 16;
  const __amdgpu_buffer_rsrc_t rso = __builtin_amdgcn_make_buffer_rsrc((void*)out, 0, S_LEN * OUTLD * 2, 0x00020000);
  const int rw_ = ldsb + 131072 + w * 8192 + lane * 16, rr_ = ldsb + 131072 + w * 2048 + lane * 16;
  const int oa = ((16 * (w & 1) + lr) * OUTLD + 64 * sl + (w < 2 ? 8 * lg : 32 + 4 * lg)) * 2, ob = oa + (w < 2 ? 8 : 32);
  const int hq = (grp + GNG - (62 - GS0) % GNG) % GNG - 1, hf = hq < 2 ? hq : -1;
#define GRW(i) "ds_write_b128 %[rw], " RNG("v", 32 + 4 * (i), 4) " offset:" STR(1024 * (i)) "\n\t"
#define GRR(i) "ds_read_b128 " RNG("v", 32 + 4 * (i), 4) ", %[rr] offset:" STR(8192 * ((i) >> 1) + 1024 * ((i) & 1)) "\n\t"
#define GNXT(D, S) "s_cmp_lt_u32 " S ", " STR(GS0) "\n\t" "s_cselect_b32 " D ", 1, " STR(GNG) "\n\t" "s_add_u32 " D ", " D ", " S "\n\t"
#define GDN1(S) "s_min_u32 s41, " S ", 63\n\t" "s_lshl_b32 s41, s41, 5\n\t" "v_add_u32 v23, s41, %[u0]\n\t" "v_lshrrev_b32 v22, 6, v23\n\t"
#define GDN2 "v_and_b32 v23, 63, v23\n\t" "v_lshl_add_u32 v23, v23, 7, %[u4]\n\t" "s_lshr_b32 s41, s41, 6\n\t" "s_max_u32 s41, s41, 29\n\t"
#define GDN3 "s_lshl_b32 s41, s41, 13\n\t" "s_add_u32 s41, s41, %[pa]\n\t" "buffer_load_dword v23, v23, %[rs], s41 offen sc1\n\t"
#define GDN(S) GDN1(S) GDN2 GDN3
#define GCK 15
#define GOK(D) "v_cmp_le_i32 vcc, v23, v22\n\t" "s_cmp_eq_u64 vcc, 0\n\t" "s_cselect_b32 " D ", 1, 0\n\t"
static_assert(NAW == 64, "shifts above");
#define GEPI(MID, OA, OB) \
      GRW(0) GRW(1) GRW(2) GRW(3) GRW(4) GRW(5) GRW(6) GRW(7) \
      MID \
      WLG(0) "s_barrier\n\t" \
      GRR(0) GRR(1) GRR(2) GRR(3) GRR(4) GRR(5) GRR(6) GRR(7) \
      WLG(4) PKA(32, 32, 40) PKA(34, 34, 42) PKA(36, 36, 44) PKA(38, 38, 46) \
      WLG(0) PKA(48, 48, 56) PKA(50, 50, 58) PKA(52, 52, 60) PKA(54, 54, 62) \
      PKA(32, 32, 48) PKA(34, 34, 50) PKA(36, 36, 52) PKA(38, 38, 54) \
      "v_cvt_pk_bf16_f32 v20, v32, v33\n\t" "v_cvt_pk_bf16_f32 v21, v34, v35\n\t" "v_cvt_pk_bf16_f32 v18, v36, v37\n\t" "v_cvt_pk_bf16_f32 v19, v38, v39\n\t" \
      "buffer_store_dwordx2 v[20:21], " OA ", %[ro], s42 offen\n\t" \
      "buffer_store_dwordx2 v[18:19], " OB ", %[ro], s42 offen\n\t"
#define GH "v[64+4*(xk&15):64+4*(xk&15)+3]"
#define GSTAMP
#define GSTIN
#define GSTCL
  u64 gt1; u32 gt2;
#define GT1 "s_memrealtime %[g1]\n\t" WLG(0)
#define GT2 "s_mov_b32 %[g2], s37\n\t"
#define GTO [g1] "=&s"(gt1), [g2] "=&s"(gt2),
  asm volatile(
      "s_nop 4\n\t"
      "v_add_u32 v16, 131072, %[va]\n\t"
      "s_mov_b32 s36, %[s0]\n\t" "s_mov_b32 s43, " STR(SPIN_CAP) "\n\t"
      "s_mov_b32 s37, 0\n\t" "v_mov_b32 v17, " STR(OOR) "\n\t" "v_mov_b32 v12, v17\n\t" "v_mov_b32 v15, v17\n\t"
      "s_cmp_ge_i32 %[hf], 0\n\t" "s_cbranch_scc1 .Li%=\n"
      ".Lp%=:\n\t"
      GDN("s36") WVM(0) GOK("s40")
      "s_cbranch_scc1 .Lr%=\n\t"
      "s_sleep 2\n\t" "s_sub_u32 s43, s43, 1\n\t" "s_cmp_gt_i32 s43, 0\n\t" "s_cbranch_scc1 .Lp%=\n"
      ".Lr%=:\n\t"
      "s_lshl_b32 s38, s36, 18\n\t" "s_add_u32 s38, s38, %[gb]\n\t"
      ".set xk, 0\n\t.rept 8\n\t" GLOAD("s38", "xk")
      "buffer_load_dwordx4 " GF(0) ", %[va], %[rf], %[tq] offen sc1\n\t" "buffer_load_dwordx4 " GF(1) ", v16, %[rf], %[tq] offen sc1\n\t"
      ".set xk, xk+1\n\t.endr\n"
      ".Lb%=:\n\t"
      GTR("0")
      ".set xk, 0\n\t.rept 32\n\t"
      "s_waitcnt vmcnt(14)\n\t"
      XM1(XA(32, 0), XW(0, 0), GF(0), 1) GTR("xk+1") XM1(XA(32, 4), XW(0, 1), GF(0), 1)
      ".if xk < 24\n\t" GLOAD("s38", "xk+8") ".else\n\t" GLOAD("s39", "xk-24") ".endif\n\t"
      XM1(XA(32, 8), XW(0, 0), GF(1), 1)
      ".if xk == " STR(GCK) "-1\n\t" GNXT("s37", "s36") ".endif\n\t"
      ".if xk == " STR(GCK) "\n\t" GDN1("s37") ".endif\n\t"
      ".if xk == 23\n\t" GOK("s40") ".endif\n\t"
      XM1(XA(32, 12), XW(0, 1), GF(1), 1)
      ".if xk == " STR(GCK) "-1\n\t" "s_lshl_b32 s42, s36, 17\n\t" ".endif\n\t"
      ".if xk == " STR(GCK) "\n\t" GDN2 ".endif\n\t"
      ".if xk == 23\n\t" "s_cmp_lt_u32 s37, 63\n\t" "s_cselect_b32 s40, s40, 0\n\t" "s_cmp_lg_u32 s40, 0\n\t" ".endif\n\t"
      XM1(XA(32, 16), GW2, GF(0), 1)
      ".if xk == " STR(GCK) "\n\t" GDN3 ".endif\n\t"
      ".if xk == 23\n\t" "s_cselect_b32 s39, s37, s36\n\t" "s_lshl_b32 s39, s39, 18\n\t" "s_add_u32 s39, s39, %[gb]\n\t" ".endif\n\t"
      "s_waitcnt lgkmcnt(1)\n\t" XM1(XA(32, 20), GW3, GF(0), 1)
      "buffer_load_dwordx4 " GF(0) ", %[va], %[rf], %[tq] offen sc1\n\t"
      XM1(XA(32, 24), GW2, GF(1), 1) XM1(XA(32, 28), GW3, GF(1), 1)
      "buffer_load_dwordx4 " GF(1) ", v16, %[rf], %[tq] offen sc1\n\t"
      ".if xk == 16\n\t" "s_barrier\n\t" ".endif\n\t"
      ".set xk, xk+1\n\t.endr\n\t"
      GEPI("", "%[oa]", "%[ob]")
      GSTAMP
      "s_mov_b32 s36, s37\n\t" "s_mov_b32 s38, s39\n\t"
      "s_cmp_lg_u32 s40, 0\n\t" "s_cbranch_scc1 .Lb%=\n\t"
      "s_cmp_lt_u32 s36, 63\n\t" "s_cbranch_scc1 .Lp%=\n\t"
      "s_cmp_lt_i32 %[hf], 0\n\t" "s_cbranch_scc1 .Le%=\n\t"
      "s_mov_b32 s37, 4\n\t" "v_mov_b32 v17, %[va]\n\t" "v_mov_b32 v12, %[oc]\n\t" "v_sub_u32 v15, %[ob], %[oa]\n\t" "v_add_u32 v15, v15, %[oc]\n\t"
      "s_barrier\n"
      ".Lh%=:\n\t"
      "v_cmp_eq_u32 vcc, -1, v23\n\t" "s_cbranch_vccz .Lt%=\n\t" "buffer_load_dword v23, %[uh], %[rs], 0 offen sc1\n\t" WVM(0)
      "s_sleep 1\n\t" "s_sub_u32 s43, s43, 1\n\t" "s_cmp_gt_i32 s43, 0\n\t" "s_cbranch_scc1 .Lh%=\n"
      ".Lt%=:\n\t"
      "v_readfirstlane_b32 s39, v23\n\t" "s_cmp_ge_i32 s39, 0\n\t" "s_cbranch_scc1 .Li%=\n"
      ".Lw%=:\n\t"
      "s_memrealtime s[40:41]\n\t" WLG(0) "s_sub_u32 s40, s39, s40\n\t" "s_lshl_b32 s40, s40, 1\n\t" "s_sub_u32 s40, s40, 1\n\t" "s_cmp_lt_u32 s40, 4000\n\t" "s_cbranch_scc1 .Lw%=\n\t"
      "s_branch .Li%=\n"
      ".Lx%=:\n\t"
      "s_sub_u32 s37, s37, 1\n\t" "s_cmp_gt_u32 s37, 1\n\t" "s_cbranch_scc1 .Li%=\n"
      ".Ly%=:\n\t"
      "buffer_load_dword v23, %[uh], %[rs], 0 offen sc1\n\t" WVM(0) "v_cmp_ge_i32 vcc, 31, v23\n\t" "s_cbranch_vccz .Li%=\n\t"
      "s_sub_u32 s43, s43, 1\n\t" "s_cmp_gt_i32 s43, 0\n\t" "s_cbranch_scc1 .Ly%=\n"
      ".Li%=:\n\t" GT1
      "s_lshl_b32 s38, %[hf], 17\n\t" "s_add_u32 s38, s38, 63*262144\n\t" "s_add_u32 s38, s38, %[gb]\n\t"
      ".set xk, 0\n\t.rept 16\n\t" GLOAD("s38", "xk") "buffer_load_dwordx4 " GH ", v17, %[rf], %[tq] offen sc1\n\t" ".set xk, xk+1\n\t.endr\n\t"
      GTR("0")
      "s_lshl_b32 s42, %[hf], 16\n\t" "s_add_u32 s42, s42, 63*131072\n\t" "v_mov_b32 v22, 0\n\t" "s_cmp_gt_u32 s37, 1\n\t" "s_cselect_b32 s39, -2, -1\n\t"
      ".set xk, 0\n\t.rept 32\n\t"
      ".if xk < 17\n\t" "s_waitcnt vmcnt(15)\n\t" ".else\n\t" "s_waitcnt vmcnt(31-xk)\n\t" ".endif\n\t"
      XM1(XA(32, 0), XW(0, 0), GH, 1) GTR("xk+1") XM1(XA(32, 4), XW(0, 1), GH, 1) "v_max3_u32 v22, v22, v[64+4*(xk&15)], v[65+4*(xk&15)]\n\t"
      XM1(XA(32, 16), GW2, GH, 1) "v_max3_u32 v22, v22, v[66+4*(xk&15)], v[67+4*(xk&15)]\n\t" "s_waitcnt lgkmcnt(1)\n\t" XM1(XA(32, 20), GW3, GH, 1)
      ".if xk & 1\n\t" "v_cmp_lt_u32 vcc, s39, v22\n\t" "s_cbranch_vccnz .Lx%=\n\t" ".endif\n\t"
      ".if xk < 16\n\t" GLOAD("s38", "xk+16") "buffer_load_dwordx4 " GH ", v17, %[rf], %[tq] offen sc1\n\t" ".endif\n\t"
      ".set xk, xk+1\n\t.endr\n\t"
      "s_nop 7\n\t"
      GEPI("", "v12", "v15")
      "s_cmp_eq_u32 s37, 0\n\t" "s_cbranch_scc1 .Lp%=\n"
      ".Le%=:\n\t"
      WVM(0)
      GT2
      : GTO [tq] "=&s"(sl)
      : GSTIN [rs] "s"(rsx), [s0] "s"(__builtin_amdgcn_readfirstlane(grp ? GS0 + grp : 0)), [gb] "s"(FO_O - 32768 + (32 * w + rot) * 1024), [va] "v"(lane * 16 + 32768),
        [rf] "s"(rsf), [wl] "v"(wl), [rw] "v"(rw_), [rr] "v"(rr_), [oa] "v"(oa), [ob] "v"(ob), [ro] "s"(rso), [u0] "v"(8 * w + (lane & 7)), [u4] "v"((lane >> 1) & 4), [uh] "v"(PGO_A + (160 + 8 * w + (lane & 7)) * 128 + 4 * (hf & 1)), [pa] "s"(PGO_A - 29 * 8192), [hf] "s"(__builtin_amdgcn_readfirstlane(hf)), [oc] "v"((w & 1) ? OOR - 64 : oa),
        "{a[0:31]}"(xw[0]), "{a[32:63]}"(xw[1]), "{a[64:95]}"(xw[2]), "{a[96:127]}"(xw[3]),
        "{a[128:159]}"(xw[4]), "{a[160:191]}"(xw[5]), "{a[192:223]}"(xw[6]), "{a[224:255]}"(xw[7]),
        "{v[128:159]}"(w2[0]), "{v[160:191]}"(w2[1]), "{v[192:223]}"(w2[2]), "{v[224:255]}"(w2[3])
      : GSTCL "v12", "v15", "v16", "v17", "v18", "v19", "v20", "v21", "v22", "v23", "vcc", CL8(24, 25, 26, 27, 28, 29, 30, 31), CL8(32, 33, 34, 35, 36, 37, 38, 39), CL8(40, 41, 42, 43, 44, 45, 46, 47),
        CL8(48, 49, 50, 51, 52, 53, 54, 55), CL8(56, 57, 58, 59, 60, 61, 62, 63), CL64, CL72, CL80, CL88, CL96, CL112, CL120,
        "s36", "s37", "s38", "s39", "s40", "s41", "s42", "s43", "scc", "memory");
  if (fresh_lane() == 0) { u32* d = g_dbg + 7740 + (((blockIdx.x >> 3) & 15) * 4 + w) * 2 + (hf + 1) * 128; d[0] = (u32)gt1; d[1] = gt2; }
}
static_assert(GNOSSM == 1 && GNG == 3 && 4 * SU_CH + 16 <= L_TOTAL && 4 * XSLOT <= L_TOTAL, "lds");
__global__ void __launch_bounds__(256, 1)
main_kernel(const u16* __restrict__ x, const u16* __restrict__ mem0, const u16* __restrict__ Wq, const u16* __restrict__ Wk, const u16* __restrict__ Wv, const u16* __restrict__ Wo,
            unsigned char* ws, const u16* __restrict__ u, const u16* __restrict__ A_re, const u16* __restrict__ A_im, const u16* __restrict__ B_re,
            const u16* __restrict__ B_im, const u16* __restrict__ C_re, const u16* __restrict__ C_im,
            const u16* __restrict__ log_dt, const u16* __restrict__ Dskip, u16* __restrict__ out
            , int tun_r, int tun_p, int tun_b, int tun_c
            ) {
  __shared__ __attribute__((aligned(8192))) unsigned char lds[L_TOTAL];
  const int w = __builtin_amdgcn_readfirstlane(threadIdx.x >> 6);
  u64 pc0;
  int clen;
  {
    asm volatile("s_getpc_b64 %0\n.Lkpc0:\n\ts_mov_b32 %1, .Lkpc1-.Lkpc0" : "=s"(pc0), "=s"(clen));
  }
#define LOCAL_IDS const int lane = fresh_lane(), tid = w * 64 + lane; (void)tid;
  const int si = (blockIdx.x >> 3) * 4 + (blockIdx.x & 3);
  unsigned char* xb = (unsigned char*)(((size_t)ws + XPAGE - 1) & ~(size_t)(XPAGE - 1));
  xb += (size_t)(tun_r >> 8) * XPAGE; tun_r &= 255;
  const int fb = (int)(ws + WS_F - xb);
  u32 tag;
  asm volatile("s_load_dword %0, %1, 0x0\n\ts_waitcnt lgkmcnt(0)\n\ts_add_u32 %0, %0, 256" : "=s"(tag) : "s"(ws + WS_F) : "scc", "memory");
  const __amdgpu_buffer_rsrc_t rsx = __builtin_amdgcn_make_buffer_rsrc(xb, 0, fb + (int)WS_F_SZ, 0x00020000);
  const __amdgpu_buffer_rsrc_t rsf = __builtin_amdgcn_make_buffer_rsrc(ws + WS_QX, 0, (int)(4 * WS_QKV_SZ + 2 * WS_KM_SZ), 0x00020000);
  const __amdgpu_buffer_rsrc_t rst = __builtin_amdgcn_make_buffer_rsrc(ws + WS_UT, 0, 2 * SSM_L * SSM_D * 2 + SSM_D * 3072, 0x00020000);
  u32* dbg = (u32*)(ws + WS_TOTAL + (8 << 20));
  const u32 tstart = rt32();
  g_dbg = dbg;
  if ((blockIdx.x & 7) < 4) {
    { LOCAL_IDS
    r_role(lds, si, mem0, Wq, Wk, Wv, Wo, fb, tag, pc0, clen, rsx, rsf, rst, SsmPtrs{A_re, A_im, B_re, B_im, C_re, C_im, log_dt}, tid, lane, w
           , dbg, tun_r, tun_p, tun_b, tun_c
           ); }
    LOCAL_IDS
    if (tid == 0) dbg[2048 + blockIdx.x * 4 + 2] = rt32();
    {
      for (int it = 0; it < SPIN_CAP; ++it) {
        if (ldw(rsx, XYDN) >= 255) break;
        asm volatile("s_sleep 8" ::: "memory");
      }
      asm volatile("" ::: "memory");
      asm volatile(".Lkpc1:" ::: "memory");
      unsigned char* im = lds + 65536;
      const __amdgpu_buffer_rsrc_t rso = __builtin_amdgcn_make_buffer_rsrc((void*)out, 0, S_LEN * OUTLD * 2, 0x00020000);
      u32x4 t[8];
#pragma unroll
      for (int i = 0; i < 8; ++i) { const int p = i * 256 + tid; t[i] = __builtin_amdgcn_raw_buffer_load_b128(rst, (p >> 3) * 4096 + (p & 7) * 16, SSM_L * SSM_D * 2 + (si & 3) * 64 * 16384 + (si >> 2) * 128, 16); }
#pragma unroll
      for (int i = 0; i < 8; ++i) {
        const int p = i * 256 + tid, o = (p & 7) * (8 * 512 + 16) + ((p >> 3) & 3) * 1024 + (p >> 5) * 8;
        *(u32x2*)(im + o) = (u32x2){t[i][0], t[i][1]};
        *(u32x2*)(im + o + 512) = (u32x2){t[i][2], t[i][3]};
      }
      LBAR();
#pragma unroll
      for (int i = 0; i < 8; ++i)
        __builtin_amdgcn_raw_buffer_store_b128(*(const u32x4*)(im + i * (8 * 512 + 16) + tid * 16), rso, ((tid >> 5) * OUTLD + DM + (tid & 31) * 8) * 2,
                                               (((si >> 2) * 64 + 8 * i) * OUTLD + (si & 3) * 256) * 2, YAUX);
    }
  } else {
    const int g = blockIdx.x & 3, li = blockIdx.x >> 3;
    const int xi = g == 0 ? (li < 16 ? li : -1) : (li < 26 + (g < 3) ? 16 + 27 * (g - 1) + li : -1);
    const int ei = g == 0 ? li - 16 : 16 + 5 * (g - 1) + (31 - li);
    if (xi >= 0) {
      LOCAL_IDS
      const int head = xi & 31, mi = xi >> 5;
      u32* prog = (u32*)(xb + PGO_X + xi * 128);
      if (mi == 1) x_role<true>(lds, x, Wv, FO_V, head, rsf, prog, tag, lane, w);
      else x_role<false>(lds, x, mi == 0 ? Wk : Wq, mi == 0 ? FO_K : FO_Q, head, rsf, prog, tag, lane, w);
    } else {
      LOCAL_IDS
      u32 acc = 0;
#pragma unroll
      for (int i = 0; i < 4; ++i) acc += *(const u32*)(x + (size_t)((i * 32 + ei) * 256 + tid) * 64);
#define UV 0
#define WWARM(n, first) {    \
        const __amdgpu_buffer_rsrc_t rwo = __builtin_amdgcn_make_buffer_rsrc((void*)Wo, 0, DM * HD * 2, 0x00020000); \
        const int ldsb = (int)(size_t)(LAS unsigned char*)lds; \
        _Pragma("unroll 1") for (int c = 0; c < (n); ++c) { dma16(rwo, lane * 16, ((first) + c) * 16384, 1024, ldsb + w * 32768 + (c & 1) * 16384); waitvm<32>(); } }
      if (UV == 0 || (UV == 1 && ei < 16)) WWARM(4, (ei * 4 + w) * 4)
      if (UV == 2 && ei < 16) WWARM(8, (ei * 4 + w) * 8)
      if (ei == 0 && tid < 16) acc += *(const u32*)(Dskip + tid * 64);
      asm volatile("" :: "v"(acc));
      if (ei >= 16) {
      { const u32 t0 = rt32(); for (int i = 0; i < 1000 && (int)(rt32() - t0) < UDELAY; ++i) asm volatile("s_sleep 16" ::: "memory"); }
      asm volatile("s_waitcnt vmcnt(0)\n\ts_barrier" ::: "memory");
      for (int ps = 0; ps < 2; ++ps) {
      const int r0 = ((ei - 16) * 2 + ps) * 64;
#pragma unroll
      for (int hb = 0; hb < 2; ++hb) {
        u32x4 t[16];
#pragma unroll
        for (int i = 0; i < 16; ++i) t[i] = *(const u32x4*)(u + (size_t)r0 * SSM_D + ((hb * 16 + i) * 256 + tid) * 8);
#pragma unroll
        for (int i = 0; i < 16; ++i) { const int p = (hb * 16 + i) * 256 + tid; *(u32x4*)(lds + (p >> 7) * 2064 + (p & 127) * 16) = t[i]; }
      }
      LBAR();
      for (int i = 0; i < 32; ++i) {
        const int p = i * 256 + tid, un = p >> 5, rp = 4 * (p & 7) + ((p >> 3) & 3);
        const u32x2 a = *(const u32x2*)(lds + (2 * rp) * 2064 + un * 8), c = *(const u32x2*)(lds + (2 * rp + 1) * 2064 + un * 8);
        __builtin_amdgcn_raw_buffer_store_b128((u32x4){a[0], a[1], c[0], c[1]}, rst, (p >> 3) * 4096 + (p & 7) * 16, r0 * 2, 16);
      }
      asm volatile("s_waitcnt vmcnt(0) lgkmcnt(0)\n\ts_barrier" ::: "memory");
      }
      if (tid == 0) dbg[4672 + ei] = rt32();
      if (UV == 1) { WWARM(4, (ei * 4 + w) * 4) waitvm<0>(); }
      wait_start(rsx, fb, tag, true);
      if (tid == 0) __hip_atomic_fetch_add((u32*)(xb + XUDN), 1u, RLX_AGENT);
      }
    }
    if (w == 0 && fresh_lane() == 0) dbg[2048 + blockIdx.x * 4 + 2] = rt32();
    if (w == 0 && fresh_lane() == 0) dbg[2048 + blockIdx.x * 4 + 3] = rt32();
    asm volatile("s_waitcnt lgkmcnt(0)\n\ts_barrier" :: "s"(tag) : "memory");
    if (w == 0 && fresh_lane() == 0) __builtin_amdgcn_raw_buffer_store_b32(tag, rsx, 0, PGO_S + si * 128, 16);
    const bool isg = g == 0 ? li >= 16 : (g >= 5 - GNG && li < 16);
    if (xi >= 0 && !(GNOSSM && isg)) {
      LOCAL_IDS
      wait_start(rsx, fb, tag, true);
      for (int it = 0; it < SPIN_CAP; ++it) {
        if (ldw(rsx, XUDN) >= 15) break;
        asm volatile("s_sleep 8" ::: "memory");
      }
      asm volatile("" ::: "memory");
      int un = g == 0 ? li : g == 1 ? 16 + li : 5 + 11 * g + li;
      SsmIn in;
      const __amdgpu_buffer_rsrc_t rsd = __builtin_amdgcn_make_buffer_rsrc((void*)Dskip, 0, SSM_D * 2, 0x00020000);
      ssm_load(in, 4 * un, rst, rsd, tid, lane, w);
      if (lane < 20) *(u32x4*)(lds + w * SU_CH + (lane < 12 ? SU_U + (lane / 6) * SU_CP + (lane % 6) * 16 : SU_K + ((lane - 12) >> 2) * 5200 + (lane & 3) * 16)) = (u32x4){0u, 0u, 0u, 0u};
      SSM_GOT(in);
      const u32 c0_ = (u32)__builtin_amdgcn_s_memtime(), r0_ = rt32(); int nu_ = 0;
#define NU_ , ++nu_
      for (bool pend = false; un < 256; pend = true NU_) un = ssm_unit(lds, 4 * un, in, pend, rst, rsx, fb, rsd, tid, lane, w);
      if (tid == 0) { dbg[5000 + blockIdx.x * 4] = (u32)__builtin_amdgcn_s_memtime() - c0_; dbg[5001 + blockIdx.x * 4] = rt32() - r0_; dbg[5002 + blockIdx.x * 4] = nu_; dbg[5003 + blockIdx.x * 4] = r0_; }
      if (tid == 0) dbg[2048 + blockIdx.x * 4 + 3] = rt32();
    }
    if (isg) {
      g_role(lds, Wo, out, rsx, rsf, fb, tag, g ? g + GNG - 4 : 0, li & 15, w);
      if (w == 0 && fresh_lane() == 0) g_dbg[4608 + 16 * (g ? g + GNG - 4 : 0) + (li & 15)] = rt32();
    }
    else {
      const int lane = fresh_lane();
      if (xi < 0) a_role(rsx, rsf, (ei - 16) * 4 + w, (NSEG - 4) * NH, (ei - 16) * 4 + w, 0, lane, fb, tag);
      do {
        const int t = __builtin_amdgcn_readfirstlane(lane == 0 ? __hip_atomic_fetch_add((u32*)(xb + XTK2), 1u, RLX_AGENT) + 1u : 0u);
        if (t >= 256) break;
        a_late(rsx, rsf, t, lane, fb, tag);
      } while (xi < 0);
    }
  }
  if (w == 0 && fresh_lane() == 0) { dbg[2048 + blockIdx.x * 4] = tstart; dbg[2048 + blockIdx.x * 4 + 1] = rt32(); }
}

__global__ void diag_kernel(unsigned char* ws) {
  const u32* h = (const u32*)(ws + WS_TOTAL + (8 << 20));
  __shared__ int mn, mx;
  const int b = threadIdx.x;
  if (b == 0) { mn = 1 << 30; mx = -(1 << 30); }
  __syncthreads();
  const u32 ref = h[2048];
  atomicMin(&mn, (int)(h[2048 + b * 4] - ref));
  __syncthreads();
  const u32 t0 = ref + mn;
  const bool r = (b & 7) < 4; const int li = b >> 3;
  int v = 0;
  if (DIAG == 1) v = h[0] - t0;
  if (DIAG == 2) v = r ? h[2048 + b * 4 + 2] - t0 : 0;
  if (DIAG == 3) v = h[2048 + b * 4 + 1] - t0;
  if (DIAG == 4) v = h[3504] * 4;
  if (DIAG == 5) v = b < 128 ? h[3072 + b * 2] - t0 : 0;
  if (DIAG == 6) v = (h[3500] >> 20) * 25;
  if (DIAG == 7) v = r ? 0 : h[2048 + b * 4 + 3] - t0;
  if (DIAG == 8) v = (!r && (b & 3) == 0 && li >= 16) ? h[2048 + b * 4 + 1] - t0 : 0;
  if (DIAG == 9) v = h[2048 + b * 4] - t0;
  if (DIAG == 10) v = h[3503] - t0;
  if (DIAG == 11) v = (b >= 16 && b < 32) ? h[4672 + b] - t0 : 0;
  if (DIAG == 12) v = r ? h[2048 + b * 4 + 1] - t0 : 0;
  if (DIAG == 13) v = r ? 0 : h[2048 + b * 4 + 2] - t0;
  if (DIAG == 14) v = rt32() - t0;
  if (DIAG == 15) v = (!r && (b & 3) >= 1 && li < 16) ? h[2048 + b * 4 + 1] - t0 : 0;
  if (DIAG == 16) v = b < 96 ? h[4352 + b] - t0 : 0;
  if (DIAG == 17) v = h[1] - t0;
  { const int g = b & 3; const bool sm = !r && (g == 0 ? li < 16 : g == 1 ? li < 27 : (li >= 16 && li < 26 + (g < 3)));
    const u32* q = h + 5000 + b * 4;
    if (DIAG == 20) v = sm ? q[0] * 100 / q[1] : 0;
    if (DIAG == 21) v = sm ? 3000 - q[0] * 100 / q[1] : 0;
    if (DIAG == 22) v = sm ? q[2] * 250 : 0;
    if (DIAG == 23) v = sm ? q[0] / q[2] / 10 : 0;
    if (DIAG == 24) v = sm ? q[3] - t0 : 0;
    if (DIAG == 25) v = sm ? 20000 - (q[3] - t0) : 0;
    if (DIAG == 26) v = sm ? 20000 - (h[2048 + b * 4 + 3] - t0) : 0;
    if (DIAG >= 30 && DIAG < 34) v = sm && g == DIAG - 30 ? q[0] * 100 / q[1] : 0;
  }
  if (DIAG >= 40 && DIAG < 48) {
    const int lo[8] = {0, 2, 8, 24, 38, 50, 0, 0}, hi[8] = {2, 8, 24, 38, 50, 64, 64, 1};
    for (int s = lo[DIAG - 40]; s < hi[DIAG - 40]; ++s) v += h[4096 + s] * 250;
  }
  if (DIAG == 48 || DIAG == 49) {
    __shared__ int re; if (b == 0) re = 0; __syncthreads(); atomicMax(&re, r ? (int)(h[2048 + b * 4 + 2] - t0) : 0); __syncthreads();
    v = DIAG == 48 ? 4 * ((int)(h[2048 + b * 4 + 1] - t0) - re) : (b >= 32 && b < 64) ? 4 * ((int)(h[3600 + b] - t0) - re) : 0;
  }
  if (DIAG == 60 || DIAG == 61) v = b < 64 ? 4 * (int)(h[7740 + (DIAG - 59) * 128 + b * 2] - h[1]) : 0;
  if (DIAG == 62) v = b < 16 ? 4 * (int)(h[4608 + 16 + b] - h[1]) : 0;
  if (DIAG == 63) v = b < 64 ? 4 * (int)(h[6200 + (192 + b) * 6 + 2] - h[1]) : 0;
  if (DIAG == 64) v = b < 64 ? 4 * (400 + (int)(h[6200 + (64 + b) * 6 + 3] - h[1])) : 0;
  if (DIAG == 65) { for (int i = 0; i < 128; ++i) v += 4 - h[7741 + 128 + i * 2]; v *= 25; }
  if (DIAG == 66) v = 4 * (int)(h[2048 + b * 4 + 1] - h[1]);
  if (DIAG == 67) v = r ? 4 * (int)(h[2048 + b * 4 + 1] - h[1]) : 0;
  if (DIAG == 68) v = b < 64 ? 4 * (600 + (int)(h[6200 + b * 6 + 3] - h[1])) : 0;
  atomicMax(&mx, v);
  __syncthreads();
  v = min(max(mx, 0), 100000);
  if (b == 0) for (int i = 0; i < 4000000 && (int)(rt32() - t0) < 30000 + 4 * v; ++i) asm volatile("s_sleep 1" ::: "memory");
}
extern "C" void kernel_launch(void* const* d_in, const int* in_sizes, int n_in, void* d_out, int out_size,
                              void* d_ws, size_t ws_size, hipStream_t stream) {
  const u16* x = (const u16*)d_in[0];
  const u16* mem0 = (const u16*)d_in[1];
  const u16* Wq = (const u16*)d_in[2];
  const u16* Wk = (const u16*)d_in[3];
  const u16* Wv = (const u16*)d_in[4];
  const u16* Wo = (const u16*)d_in[5];
  const u16* u = (const u16*)d_in[7];
  const u16* A_re = (const u16*)d_in[8];
  const u16* A_im = (const u16*)d_in[9];
  const u16* B_re = (const u16*)d_in[10];
  const u16* B_im = (const u16*)d_in[11];
  const u16* C_re = (const u16*)d_in[12];
  const u16* C_im = (const u16*)d_in[13];
  const u16* log_dt = (const u16*)d_in[14];
  const u16* Dskip = (const u16*)d_in[15];
  u16* out = (u16*)d_out;
  char* ws = (char*)d_ws;

  main_kernel<<<256, 256, 0, stream>>>(x, mem0, Wq, Wk, Wv, Wo, (unsigned char*)ws, u, A_re, A_im, B_re, B_im, C_re, C_im, log_dt, Dskip, out
      , g_tune[0], g_tune[1], g_tune[2], g_tune[3]
      );
  diag_kernel<<<1, 256, 0, stream>>>((unsigned char*)ws);
}
```

```cpp
#include <hip/hip_runtime.h>
#include <hip/hip_bf16.h>
#include <stdint.h>

#define S_LEN 2048
#define DM 1024
#define NH 32
#define DH 128
#define HD 4096
#define MEM 8
#define SEG 32
#define NSEG 64
#define LTOK 40
#define SSM_L 2048
#define SSM_D 1024
#define SSM_N 128
#define OUTLD 2048

typedef unsigned short u16;
typedef unsigned int u32;
typedef unsigned long long u64;
typedef __attribute__((ext_vector_type(8))) short bf16x8;
typedef __attribute__((ext_vector_type(4))) float f32x4;
typedef __attribute__((ext_vector_type(4))) unsigned int u32x4;
typedef __attribute__((ext_vector_type(2))) unsigned int u32x2;
typedef __bf16 bf16x2_t __attribute__((ext_vector_type(2)));
typedef short v4i16_t __attribute__((ext_vector_type(4)));

#define SENT 0xFFFFFFFFu
#define RLX_AGENT __ATOMIC_RELAXED, __HIP_MEMORY_SCOPE_AGENT

__device__ __forceinline__ float bf2f(u16 v) { return __uint_as_float(((u32)v) << 16); }
__device__ __forceinline__ u16 f2bf(float f) {
  u32 u = __float_as_uint(f);
  u32 r = (u + 0x7fffu + ((u >> 16) & 1u)) >> 16;
  if ((u & 0x7fffffffu) > 0x7f800000u) r = 0x7fc0u;
  return (u16)r;
}
__device__ __forceinline__ float rbf(float f) { return bf2f(f2bf(f)); }

#define TR 92
#define TP 179
#define TDB 61
#define TDC 110
#define NR1 4
#define XPAGE (2 << 20)
#define XRING 3
#define XLP(p, L) ((((L) >> 13) << 15) + ((p) << 14) + ((L) & 8191))
#define XO_R3 131072
#define XR1A(x, L) XLP((x) & 1, ((x) >> 1) * 16384 + (L))
#define XR3A(rg, L) (XO_R3 + XLP(rg, L))
#define XSB(s) (((s) % XRING) == 2 ? 1048576 : ((s) % XRING) * 262144)
#define XR2B 524288
#define XR2S 1048576
#define XR2(j) (((((j) & 31) + (((j) >> 6) << 5)) / 6 << 15) + (((((j) >> 5) & 1) ^ 1) << 14) + ((((j) & 31) + (((j) >> 6) << 5)) % 6) * 1280)
#define XSYN (1048576 + 262144)
#define PGO_R (XSYN + 32768)
#define XCAL (PGO_R + 32768)
static_assert(XCAL + 16384 <= XPAGE, "exchange page");
#define WS_X 0ull
#define NPG 2
#define WS_X_SZ ((u64)NPG * XPAGE)
#define WS_F (WS_X + WS_X_SZ)
#define XEPO (fb)
#define XSTA (fb + 128)
#define PGO_X (fb + 256)
#define PGO_Y (PGO_X + 96 * 128)
#define PGO_S (PGO_Y + 128 * 128)
#define PGO_D (PGO_S + 128 * 128)
#define PGO_A (PGO_D + 128 * 128)
#define XTKT (PGO_A + 192 * 128)
#define XUDN (XTKT + 128)
#define XYDN (XUDN + 128)
#define XTK2 (XYDN + 128)
#define XSTR(x) (XTK2 + 128 + (x) * 128)
#define WS_F_SZ ((u64)(2 + 96 + 128 + 128 + 128 + 192 + 8) * 128)
#define WS_XCH_END (WS_F + 131072)
#define WS_QX (WS_XCH_END)
#define WS_QKV_SZ ((u64)S_LEN * HD * 2)
#define WS_KX (WS_QX + WS_QKV_SZ)
#define WS_VX (WS_KX + WS_QKV_SZ)
#define WS_OX (WS_VX + WS_QKV_SZ)
#define WS_KM (WS_OX + WS_QKV_SZ)
#define WS_KM_SZ ((u64)NSEG * 8 * HD * 2)
#define WS_VM (WS_KM + WS_KM_SZ)
#define WS_UT (WS_VM + WS_KM_SZ)
#define WS_YT (WS_UT + (u64)SSM_L * SSM_D * 2)
#define WS_KD (WS_YT + (u64)SSM_L * SSM_D * 2)
#define WS_TOTAL (WS_KD + (u64)SSM_D * 3072)

#define SPIN_CAP 400000
#define TRY_CAP 20000
#define LAS __attribute__((address_space(3)))
#define LBAR() asm volatile("s_waitcnt lgkmcnt(0)\n\ts_barrier" ::: "memory")
__device__ __forceinline__ u32 max4u(u32x4 v) { return max(max(v[0], v[1]), max(v[2], v[3])); }
typedef float f32x2_t __attribute__((ext_vector_type(2)));
__device__ __forceinline__ u32 pack2(float a, float b) {
  f32x2_t v = {a, b};
  bf16x2_t r = __builtin_convertvector(v, bf16x2_t);
  return __builtin_bit_cast(u32, r);
}
__device__ __forceinline__ float rbf_hw(float a) { return __uint_as_float(pack2(a, a) & 0xffff0000u); }
__device__ __forceinline__ u32 nosent(u32 v) { return min(v, 0xFFFFFFFEu); }
template <int N> __device__ __forceinline__ void waitvm() { asm volatile("s_waitcnt vmcnt(%0)" ::"n"(N) : "memory"); }
typedef __attribute__((ext_vector_type(16))) float f32x16;
__device__ __forceinline__ u32 split_hl(float kv) {
  const u32 h = pack2(kv, kv) & 0xffffu;
  const float r1 = kv - __uint_as_float(h << 16);
  return h | (pack2(r1, r1) & 0xffff0000u);
}
__device__ __forceinline__ int kidx(int idx) {
  const int sh = idx < 256 ? 0 : (idx < 512 ? 1 : (idx < 1024 ? 2 : 3));
  const int q = sh ? ((idx + (1 << (sh - 1)) - 1 + ((idx >> sh) & 1)) >> sh) : idx;
  return q + 128 * sh;
}

#define FO_Q 0
#define FO_K ((int)WS_QKV_SZ)
#define FO_V ((int)(2 * WS_QKV_SZ))
#define FO_O ((int)(3 * WS_QKV_SZ))
#define FO_KM ((int)(4 * WS_QKV_SZ))
#define FO_VM ((int)(4 * WS_QKV_SZ + WS_KM_SZ))
__device__ __forceinline__ u32x4 wait_start(__amdgpu_buffer_rsrc_t rsx, int fb, u32 tag, bool slow, int xcd = 0) {
  u32x4 st;
  for (int it = 0; it < SPIN_CAP; ++it) {
    asm volatile("" ::: "memory");
    st = __builtin_amdgcn_raw_buffer_load_b128(rsx, 0, slow ? XSTA : XSTR(xcd), 16);
    if ((u32)__builtin_amdgcn_readfirstlane(st[3]) == tag) break;
    if (slow) asm volatile("s_sleep 20" ::: "memory"); else asm volatile("s_sleep 1" ::: "memory");
  }
  asm volatile("" ::: "memory");
  return st;
}
__device__ __forceinline__ int xprog(u32 v, u32 tag) { return v - tag <= 80u ? (int)(v - tag) : -1; }
__device__ __forceinline__ float xmax4(float m) {
  { auto r = __builtin_amdgcn_permlane16_swap(__float_as_uint(m), __float_as_uint(m), false, false); m = __builtin_fmaxf(__uint_as_float(r[0]), __uint_as_float(r[1])); }
  { auto r = __builtin_amdgcn_permlane32_swap(__float_as_uint(m), __float_as_uint(m), false, false); m = __builtin_fmaxf(__uint_as_float(r[0]), __uint_as_float(r[1])); }
  return m;
}
__device__ __forceinline__ float xsum4(float m) {
  { auto r = __builtin_amdgcn_permlane16_swap(__float_as_uint(m), __float_as_uint(m), false, false); m = __uint_as_float(r[0]) + __uint_as_float(r[1]); }
  { auto r = __builtin_amdgcn_permlane32_swap(__float_as_uint(m), __float_as_uint(m), false, false); m = __uint_as_float(r[0]) + __uint_as_float(r[1]); }
  return m;
}
#define MF16(a, b, c) __builtin_amdgcn_mfma_f32_16x16x32_bf16(__builtin_bit_cast(bf16x8, a), __builtin_bit_cast(bf16x8, b), c, 0, 0, 0)
__device__ __forceinline__ f32x4 rnd2(f32x4 v, float sc) {
  const u32 a = pack2(v[0], v[1]), b = pack2(v[2], v[3]);
  const u32 c = pack2(__uint_as_float(a << 16) * sc, __uint_as_float(a & 0xffff0000u) * sc), d = pack2(__uint_as_float(b << 16) * sc, __uint_as_float(b & 0xffff0000u) * sc);
  return (f32x4){__uint_as_float(c << 16), __uint_as_float(c & 0xffff0000u), __uint_as_float(d << 16), __uint_as_float(d & 0xffff0000u)};
}
struct XaOps { u32x4 q[2][4], k[2][4], v[8]; };
__device__ __forceinline__ void xa_load(XaOps& o, __amdgpu_buffer_rsrc_t rsf, int sh, int vo) {
#pragma unroll
  for (int i = 0; i < 8; ++i) {
    o.q[i >> 2][i & 3] = __builtin_amdgcn_raw_buffer_load_b128(rsf, vo, FO_Q + (sh * 8 + i) * 1024, 16);
    o.k[i >> 2][i & 3] = __builtin_amdgcn_raw_buffer_load_b128(rsf, vo, FO_K + (sh * 8 + i) * 1024, 16);
  }
#pragma unroll
  for (int dt = 0; dt < 8; ++dt) o.v[dt] = __builtin_amdgcn_raw_buffer_load_b128(rsf, vo, FO_V + (sh * 8 + dt) * 1024, 16);
}
struct XaPre { f32x4 a[2], b[2]; float mx[2], tx[2]; };
__device__ __forceinline__ void xa_pre(XaPre& p, const XaOps& o, int lane) {
  const int lr = lane & 15, lg = lane >> 4;
  const float scale = 0.08838834764831845f, L2E = 1.4426950408889634f, NEG = -1000000000.0f;
  const f32x4 z = (f32x4){0, 0, 0, 0};
  f32x4 s00 = z, s10 = z, s11 = z;
#pragma unroll
  for (int ks = 0; ks < 4; ++ks) { s00 = MF16(o.k[0][ks], o.q[0][ks], s00); s10 = MF16(o.k[0][ks], o.q[1][ks], s10); s11 = MF16(o.k[1][ks], o.q[1][ks], s11); }
  s00 = rnd2(s00, scale); s10 = rnd2(s10, scale); s11 = rnd2(s11, scale);
  asm volatile("" : "+v"(s00), "+v"(s11));
  float m0 = NEG, m1 = -3.0e38f;
#pragma unroll
  for (int e = 0; e < 4; ++e) {
    const bool msk = 4 * lg + e > lr;
    s00[e] = msk ? NEG : s00[e]; s11[e] = msk ? NEG : s11[e];
    m0 = __builtin_fmaxf(m0, s00[e]); m1 = __builtin_fmaxf(m1, __builtin_fmaxf(s10[e], s11[e]));
  }
  m0 = xmax4(m0); m1 = xmax4(m1);
  const float em = __builtin_amdgcn_exp2f((NEG - m0) * L2E);
  float t0 = 4.f * em, t1 = 0.f;
#pragma unroll
  for (int e = 0; e < 4; ++e) {
    s00[e] = __builtin_amdgcn_exp2f((s00[e] - m0) * L2E); s10[e] = __builtin_amdgcn_exp2f((s10[e] - m1) * L2E); s11[e] = __builtin_amdgcn_exp2f((s11[e] - m1) * L2E);
    t0 += s00[e]; t1 += s10[e] + s11[e];
  }
  p.a[0] = s00; p.b[0] = (f32x4){em, em, em, em}; p.a[1] = s10; p.b[1] = s11; p.mx[0] = m0; p.mx[1] = m1; p.tx[0] = t0; p.tx[1] = t1;
}
__device__ __forceinline__ void xa_pw(u32x4& px, u32x2& pm, f32x4 sm, f32x4 sa, f32x4 sb, float mx, float tx, int lg) {
  const float scale = 0.08838834764831845f, L2E = 1.4426950408889634f;
  sm = rnd2(sm, scale);
  asm("" : "+v"(sm));
  float m = mx;
#pragma unroll
  for (int e = 0; e < 4; ++e) { sm[e] = lg >= 2 ? -INFINITY : sm[e]; m = __builtin_fmaxf(m, sm[e]); }
  m = xmax4(m);
  const float al = __builtin_amdgcn_exp2f((mx - m) * L2E);
  float ts = tx * al;
#pragma unroll
  for (int e = 0; e < 4; ++e) { sm[e] = __builtin_amdgcn_exp2f((sm[e] - m) * L2E); ts += sm[e]; }
  const float r = __builtin_amdgcn_rcpf(xsum4(ts)), ra = r * al;
  px = (u32x4){pack2(sa[0] * ra, sa[1] * ra), pack2(sa[2] * ra, sa[3] * ra), pack2(sb[0] * ra, sb[1] * ra), pack2(sb[2] * ra, sb[3] * ra)};
  pm = (u32x2){pack2(sm[0] * r, sm[1] * r), pack2(sm[2] * r, sm[3] * r)};
}
__device__ __forceinline__ void xa_pv(const u32x4 (&v)[8], const u32x2 (&vm)[8], u32x4 px, u32x2 pm, __amdgpu_buffer_rsrc_t rsf, int vo, int so) {
  f32x4 o[8];
#define LX(i) "v_mfma_f32_16x16x32_bf16 %" #i ", %[v" #i "], %[px], 0\n\t"
#define LM(i) "v_mfma_f32_16x16x16_bf16 %" #i ", %[m" #i "], %[pm], %" #i "\n\t"
  asm volatile("s_nop 1\n\t" LX(0) LX(1) LX(2) LX(3) LX(4) LX(5) LX(6) LX(7) LM(0) LM(1) LM(2) LM(3) LM(4) LM(5) LM(6) LM(7) "s_nop 7"
               : "=&v"(o[0]), "=&v"(o[1]), "=&v"(o[2]), "=&v"(o[3]), "=&v"(o[4]), "=&v"(o[5]), "=&v"(o[6]), "=&v"(o[7])
               : [v0] "v"(v[0]), [v1] "v"(v[1]), [v2] "v"(v[2]), [v3] "v"(v[3]), [v4] "v"(v[4]), [v5] "v"(v[5]), [v6] "v"(v[6]), [v7] "v"(v[7]), [px] "v"(px),
                 [m0] "v"(vm[0]), [m1] "v"(vm[1]), [m2] "v"(vm[2]), [m3] "v"(vm[3]), [m4] "v"(vm[4]), [m5] "v"(vm[5]), [m6] "v"(vm[6]), [m7] "v"(vm[7]), [pm] "v"(pm));
#pragma unroll
  for (int pr = 0; pr < 4; ++pr)
    __builtin_amdgcn_raw_buffer_store_b128((u32x4){pack2(o[2 * pr][0], o[2 * pr][1]), pack2(o[2 * pr][2], o[2 * pr][3]), pack2(o[2 * pr + 1][0], o[2 * pr + 1][1]), pack2(o[2 * pr + 1][2], o[2 * pr + 1][3])},
                                           rsf, vo, so + pr * 1024, 16);
}
__device__ __forceinline__ u32 xa_chk(const u32x4 (&km)[4], const u32x2 (&vm)[8]) {
  u32 ck = max(max(max4u(km[0]), max4u(km[1])), max(max4u(km[2]), max4u(km[3])));
#pragma unroll
  for (int i = 0; i < 8; ++i) ck = max(ck, max(vm[i][0], vm[i][1]));
  return ck;
}
#define XSG 56
#define XST 0
#define XS0 48
#define XL_D 26
#define NAW 64
__device__ __forceinline__ void a_role(__amdgpu_buffer_rsrc_t rsx, __amdgpu_buffer_rsrc_t rsf, int sh, int lim, int wi, int done, int lane, int fb, u32 tag) {
  const int lr = lane & 15, lg = lane >> 4;
  const int okm = (lg * 8 + (lr & 7)) * 16, ovm = lg < 2 ? (lg * 16 + lr) * 8 : 0x7ffffff0;
  int rp = 0, xp = -1, spin = 0, pub = done, miss = 0, tp = 200;
  u32 tseen = 0, tq0 = 0, tq1 = 0, tq2 = 0, tq3 = 0; (void)tq0; (void)tq1; (void)tq2; (void)tq3;
#define XA_WAITX(s_)                                                                  \
  while (xp <= ((s_) >> 5)) {                                                                                          \
    asm volatile("" ::: "memory");                                                                                     \
    int pv = min(xprog(__builtin_amdgcn_raw_buffer_load_b32(rsx, lane * 128, PGO_X, 16), tag), xprog(__builtin_amdgcn_raw_buffer_load_b32(rsx, (64 + (lane & 31)) * 128, PGO_X, 16), tag)); \
    _Pragma("unroll") for (int o_ = 1; o_ < 64; o_ *= 2) pv = min(pv, __shfl_xor(pv, o_));                             \
    xp = __builtin_amdgcn_readfirstlane(pv);                                                                           \
    if (xp > ((s_) >> 5) || ++spin > SPIN_CAP) break;                                                                  \
    asm volatile("s_sleep 16" ::: "memory");                                                                           \
  }
#define XA_PUB() if (pub != done) { pub = done; asm volatile("s_nop 4\n\tbuffer_store_dword %0, %1, %2, %3 offen sc1" :: "v"(done), "v"(lane < 2 ? lane * 4 : 0x7ffffff0), "s"(rsx), "s"(PGO_A + wi * 128) : "memory"); }
#define XA_NOW ((u32)__builtin_amdgcn_s_memrealtime())
#define XA_SEEN(r_, l_) { const u32 n_ = XA_NOW; tseen = n_ - ((n_ - (r_ >> 16)) & 0x7fffu) + XA_LAG; tp = max(r_ & 1023, 1); rp = l_; }
#define XA_KM \
      _Pragma("unroll") for (int i = 0; i < 4; ++i) km[i] = __builtin_amdgcn_raw_buffer_load_b128(rsf, okm, FO_KM + (sh * 4 + i) * 512, 16); \
      _Pragma("unroll") for (int i = 0; i < 8; ++i) vm[i] = __builtin_amdgcn_raw_buffer_load_b64(rsf, ovm, FO_VM + (sh * 8 + i) * 256, 16);
#define XPRE 2
#define XA_STEP(CUR, NXT, ODD) \
  { \
    const int seg = sh >> 5; \
    int tries = (seg >= XS0 && tseen) ? 3 : 0; \
    while (!pre) {               \
      if (rp > seg) tries = 0; \
      else if (tries) { \
        const u32 due = tseen + (u32)(seg - rp) * tp + XL_D - XA_LAG + (seg < XSG ? stg : 0); \
        for (int i_ = 0; i_ < 4000 && (int)(due - XA_NOW) > 0; ++i_) asm volatile("s_sleep 1" ::: "memory"); \
      } else while (true) { \
        int want = seg + 1; \
        if (tseen) { \
          const u32 due = tseen + (u32)(want - rp) * tp + (seg < XSG ? stg : 0); \
          for (int i_ = 0; i_ < 4000 && (int)(due - XA_NOW) > 0; ++i_) asm volatile("s_sleep 2" ::: "memory"); \
          if (!miss) want = min(max(want, rp + max((int)(XA_NOW - tseen), 0) / tp), NSEG - 1); \
        } \
        asm volatile("" ::: "memory"); \
        const int r_ = __builtin_amdgcn_readfirstlane(__builtin_amdgcn_raw_buffer_load_b32(rsx, 0, pgr + want * 128, 16)); \
        if (r_ >= 0) { XA_SEEN(r_, want) miss = 0; break; } \
        if (++spin > SPIN_CAP) break; \
        if (miss++) asm volatile("s_sleep 20" ::: "memory"); \
      } \
      asm volatile("" ::: "memory"); \
      XA_DBG2 \
      XA_KM \
        \
      const int pk_ = min(rp + max((int)(XA_NOW - tseen), 0) / tp, NSEG - 1); \
      const u32 pv_ = __builtin_amdgcn_raw_buffer_load_b32(rsx, 0, pgr + pk_ * 128, 16); \
      asm volatile("" ::: "memory"); \
      __builtin_amdgcn_s_waitcnt(0x0F70);          \
      asm volatile("" ::: "memory"); \
      { const int r_ = __builtin_amdgcn_readfirstlane(pv_); if (r_ >= 0) XA_SEEN(r_, pk_) } \
      if (!tries || __ballot(xa_chk(km, vm) == SENT) == 0) break; \
      --tries; \
    } \
    XA_DBG3 \
    __builtin_amdgcn_sched_barrier(0); \
    f32x4 s0 = (f32x4){0, 0, 0, 0}, s1 = s0; \
    _Pragma("unroll") for (int ks = 0; ks < 4; ++ks) { s0 = MF16(km[ks], CUR.q[0][ks], s0); s1 = MF16(km[ks], CUR.q[1][ks], s1); } \
    u32x4 px0, px1; u32x2 pm0, pm1; \
    xa_pw(px0, pm0, s0, P.a[0], P.b[0], P.mx[0], P.tx[0], lg); \
    xa_pw(px1, pm1, s1, P.a[1], P.b[1], P.mx[1], P.tx[1], lg); \
    xa_pv(CUR.v, vm, px0, pm0, rsf, lane * 16, FO_O + (seg * 256 + (sh & 31) * 4) * 1024); \
    xa_pv(CUR.v, vm, px1, pm1, rsf, lane * 16, FO_O + (seg * 256 + 128 + (sh & 31) * 4) * 1024); \
    ++done; \
    XA_DBG XA_DBG5 \
    __builtin_amdgcn_sched_barrier(0); \
    sh += NAW; \
      \
    pre = (!XPRE || (ODD && sh >= lim)) ? 0 : rp > seg + 2 ? 1 : (XPRE > 1 && seg + 2 >= XS0 && tseen && (int)(tseen + (u32)(seg + 2 - rp) * tp + XL_D - XA_LAG - XA_NOW) <= 0) ? 2 : 0; \
    asm volatile("" ::: "memory"); \
    if (pre) { XA_DBG2 XA_KM } \
    const int pk2 = pre ? min(rp + max((int)(XA_NOW - tseen), 0) / tp, NSEG - 1) : rp; \
    const u32 pv2 = __builtin_amdgcn_raw_buffer_load_b32(rsx, pk2 > rp ? 0 : 0x7ffffff0, pgr + pk2 * 128, 16); \
    asm volatile("" ::: "memory"); \
    xa_pre(P, NXT, lane); \
    __builtin_amdgcn_sched_barrier(0); \
    XA_WAITX(min(sh + NAW, lim - 1)) \
    asm volatile("" ::: "memory"); \
    xa_load(CUR, rsf, sh + NAW, sh + NAW < lim ? lane * 16 : 0x7ffffff0); \
    asm volatile("" ::: "memory"); \
    __builtin_amdgcn_s_waitcnt(0x4F78);          \
    asm volatile("" ::: "memory"); \
    XA_PUB() \
    if (pk2 > rp) { const int r_ = __builtin_amdgcn_readfirstlane(pv2); if (r_ >= 0) XA_SEEN(r_, pk2) } \
    if (pre == 2 && __ballot(xa_chk(km, vm) == SENT) != 0) pre = 0; \
  }
#define XA_LAST 22
#define XA_LAG 30
#define XA_DBG
#define XA_DBG2
#define XA_DBG3
#define XA_DBG4
#define XA_DBG5
  const int pgr = PGO_R + (__builtin_amdgcn_readfirstlane(wait_start(rsx, fb, tag, true)[2]) & 8192);
  XaOps A, B;
  XaPre P;
  u32x4 km[4]; u32x2 vm[8];
  int pre = 0;
  static_assert(NAW == 64 && (NSEG - 4) * NH % (2 * NAW) == 0, "two segments per turn");
  const int stg = ((wi * 5) & 31) * XST;
  XA_WAITX(sh)
  asm volatile("" ::: "memory");
  xa_load(A, rsf, sh, lane * 16);
  xa_pre(P, A, lane);
  XA_WAITX(sh + NAW)
  asm volatile("" ::: "memory");
  xa_load(B, rsf, sh + NAW, lane * 16);
  do { XA_STEP(A, B, 0) XA_STEP(B, A, 1) } while (sh < lim);
  asm volatile("s_waitcnt vmcnt(0)" ::: "memory");
  XA_PUB()
}

#define GL_D 130
__device__ __forceinline__ void a_late(__amdgpu_buffer_rsrc_t rsx, __amdgpu_buffer_rsrc_t rsf, int t, int lane, int fb, u32 tag) {
  const int lr = lane & 15, lg = lane >> 4, rt = t & 1, sh = (NSEG - 4) * NH + (t >> 1), seg = sh >> 5;
  const float scale = 0.08838834764831845f, L2E = 1.4426950408889634f, NEG = -1000000000.0f;
  int xp = -1, spin = 0;
  const int pgr = PGO_R + (__builtin_amdgcn_readfirstlane(wait_start(rsx, fb, tag, true)[2]) & 8192);
  const int r40 = __builtin_amdgcn_readfirstlane(__builtin_amdgcn_raw_buffer_load_b32(rsx, 0, pgr + 40 * 128, 16));
  XA_WAITX(sh)
  asm volatile("" ::: "memory");
  u32x4 q[4], v[8];
  f32x4 sa = (f32x4){0, 0, 0, 0}, sb = sa;
  float mx = -3.0e38f, tx = 0.f;
  {
    u32x4 k0[4], k1[4];
#pragma unroll
    for (int i = 0; i < 4; ++i) {
      q[i] = __builtin_amdgcn_raw_buffer_load_b128(rsf, lane * 16, FO_Q + (sh * 8 + rt * 4 + i) * 1024, 16);
      k0[i] = __builtin_amdgcn_raw_buffer_load_b128(rsf, lane * 16, FO_K + (sh * 8 + i) * 1024, 16);
      k1[i] = __builtin_amdgcn_raw_buffer_load_b128(rsf, lane * 16, FO_K + (sh * 8 + 4 + i) * 1024, 16);
    }
#pragma unroll
    for (int dt = 0; dt < 8; ++dt) v[dt] = __builtin_amdgcn_raw_buffer_load_b128(rsf, lane * 16, FO_V + (sh * 8 + dt) * 1024, 16);
#pragma unroll
    for (int ks = 0; ks < 4; ++ks) { sa = MF16(k0[ks], q[ks], sa); sb = MF16(k1[ks], q[ks], sb); }
    sa = rnd2(sa, scale); sb = rnd2(sb, scale);
    asm volatile("" : "+v"(sa), "+v"(sb));
#pragma unroll
    for (int e = 0; e < 4; ++e) {
      sa[e] = 4 * lg + e > lr + 16 * rt ? NEG : sa[e]; sb[e] = 16 + 4 * lg + e > lr + 16 * rt ? NEG : sb[e];
      mx = __builtin_fmaxf(mx, __builtin_fmaxf(sa[e], sb[e]));
    }
    mx = xmax4(mx);
#pragma unroll
    for (int e = 0; e < 4; ++e) { sa[e] = __builtin_amdgcn_exp2f((sa[e] - mx) * L2E); sb[e] = __builtin_amdgcn_exp2f((sb[e] - mx) * L2E); tx += sa[e] + sb[e]; }
  }
  const int okm = (lg * 8 + (lr & 7)) * 16, ovm = lg < 2 ? (lg * 16 + lr) * 8 : 0x7ffffff0;
  const int ofl = seg + 1 < NSEG ? pgr + (seg + 1) * 128 : PGO_D + ((sh & 31) * 4 + (lane & 3)) * 128;
  const int owd = lane ? 0x7ffffff0 : PGO_A + (64 + (t >> 1)) * 128 + 4 * rt;
  u32 tq0 = 0, tq1 = 0, tq2 = 0; (void)tq0; (void)tq1; (void)tq2;
  int att = 0, seen = 1;
  while (true) {
    asm volatile("" : "+s"(att));
    if (att == 1) {
      u32 n = (u32)__builtin_amdgcn_s_memrealtime(), due = n;
      if (r40 >= 0) due = n - ((n - (r40 >> 16)) & 0x7fffu) + XA_LAG + (seg - 41) * (r40 & 1023);
      int r;
      while (true) {
        for (int i = 0; i < 4000 && (int)(due - (u32)__builtin_amdgcn_s_memrealtime()) > 0; ++i) asm volatile("s_sleep 2" ::: "memory");
        asm volatile("" ::: "memory");
        r = __builtin_amdgcn_readfirstlane(__builtin_amdgcn_raw_buffer_load_b32(rsx, 0, pgr + (seg - 1) * 128, 16));
        if (r >= 0 || ++spin > SPIN_CAP) break;
        asm volatile("s_sleep 10" ::: "memory");
      }
      n = (u32)__builtin_amdgcn_s_memrealtime();
      due = n - ((n - (r >> 16)) & 0x7fffu) + (r & 1023) + XL_D - (seg + 1 < NSEG ? 0 : XA_LAST);
      __builtin_amdgcn_raw_buffer_store_b32(((n + max((int)(due - n), 0) + GL_D) & ~1u) | 0x80000000u, rsx, owd, 0, 16);
      for (int i = 0; i < 4000 && (int)(due - (u32)__builtin_amdgcn_s_memrealtime()) > 0; ++i) asm volatile("s_sleep 1" ::: "memory");
      XA_DBG2
    } else if (att > 6) asm volatile("s_sleep 20" ::: "memory");
    const int dr = att ? 0 : 0x7ffffff0;
    asm volatile("" ::: "memory");
    u32x4 km[4]; u32x2 vm[8];
#pragma unroll
    for (int i = 0; i < 4; ++i) km[i] = __builtin_amdgcn_raw_buffer_load_b128(rsf, okm | dr, FO_KM + (sh * 4 + i) * 512, 16);
#pragma unroll
    for (int i = 0; i < 8; ++i) vm[i] = __builtin_amdgcn_raw_buffer_load_b64(rsf, ovm | dr, FO_VM + (sh * 8 + i) * 256, 16);
    const u32 fl = __builtin_amdgcn_raw_buffer_load_b32(rsx, att > 1 ? ofl : 0x7ffffff0, 0, 16);
    f32x4 sm = (f32x4){0, 0, 0, 0};
#pragma unroll
    for (int ks = 0; ks < 4; ++ks) sm = MF16(km[ks], q[ks], sm);
    const u32 ck = xa_chk(km, vm);
    u64 bad = __ballot(ck == SENT), nf = __ballot(fl == SENT);
    asm volatile("" : "+s"(bad), "+s"(nf));
    if (!seen && bad != 0) {
      seen = (att > 1 && nf == 0) || ++spin > SPIN_CAP;
      ++att;
      continue;
    }
    XA_DBG3
    u32x4 px; u32x2 pm;
    xa_pw(px, pm, sm, sa, sb, mx, tx, lg);
    xa_pv(v, vm, px, pm, rsf, (lane * 16) | dr, FO_O + (seg * 256 + rt * 128 + (sh & 31) * 4) * 1024);
    XA_DBG
    asm volatile("s_nop 4\n\ts_waitcnt vmcnt(0)\n\tbuffer_store_dword %0, %1, %2, 0 offen sc1" :: "v"(32), "v"(owd | dr), "s"(rsx) : "memory");
    if (att) break;
    att = 1; seen = 0;
  }
}

typedef __attribute__((ext_vector_type(32))) unsigned int u32x32;
typedef __attribute__((ext_vector_type(16))) unsigned int u32x16;
typedef __attribute__((ext_vector_type(8))) unsigned int u32x8;
#define STR2(x) #x
#define STR(x) STR2(x)
#define RNG(f, b, n) f "[" STR(b) ":" STR((b) + (n)-1) "]"
#define REG(b) "v[" STR(b) "]"
#define LR_RED 16384
#define LR_CTL LR_RED
#define LR_STG 32768
#define L_RSTAMP LR_STG
#define STAMPV(i, v)
#define EXF (NH * 8 * 1024)
#define EXK 65536
#define OOR 0x7ffffff0

__device__ __forceinline__ u32 lds_word(int addr) {
  u32 v;
  asm volatile("ds_read_b32 %0, %1\n\ts_waitcnt lgkmcnt(0)" : "=v"(v) : "v"(addr) : "memory");
  return __builtin_amdgcn_readfirstlane(v);
}
__device__ __forceinline__ int fresh_lane() {
  int l;
  asm volatile("v_mbcnt_lo_u32_b32 %0, -1, 0\n\tv_mbcnt_hi_u32_b32 %0, -1, %0" : "=v"(l));
  return l;
}
__device__ __forceinline__ u32 rt32() { return (u32)__builtin_amdgcn_s_memrealtime(); }
#define SYO(s) (XSYN + flip + (s) * 64)
#define TKS 0.85f
#define TLB 1
#define TLC 1
#define TLP 2
#define KG0 0
#define CLS0 0u
#define APC 5
#define ADL 12
#define CALN 2
#define TE1 3
#define TE2 1
#define TE3 1
#define XTH 4
#define TS3 42
#define TE0 45
#define TD0 170
#define TD 120
#define PHASE 1
#define XSK 0
#define CLKS(T, R, X) WLG(2) "s_sub_u32 s49, " T ", " R "\n\t" "s_cmp_le_i32 s49, " STR(3 * PHASE) "\n\t" "s_cbranch_scc1 " X "\n\t"
#define CLKH(OFF) WLG(0) "s_memrealtime s[72:73]\n\t" "s_add_u32 s50, s43, s44\n\t" "s_add_u32 s50, s50, " OFF "\n\t" "s_nop 1\n\t" "s_memrealtime s[74:75]\n\t"
#define CLKL(L, T)   L ":\n\t" "s_memrealtime s[76:77]\n\t" CLKS(T, "s72", L "x") "s_memrealtime s[72:73]\n\t" CLKS(T, "s74", L "x")     \
                     "s_memrealtime s[74:75]\n\t" CLKS(T, "s76", L "x") "s_branch " L "\n" \
                     L "x:\n\t" CLKP(L)
#define CLKW(L, OFF) CLKH(OFF) "s_nop 3\n" CLKL(L, "s50")
#define CLKP(L) "s_cmp_lt_i32 s49, 1\n\t" "s_cbranch_scc1 " L "y\n\t" "s_nop " STR(PHN) "\n\t" "s_cmp_lt_i32 s49, 2\n\t" "s_cbranch_scc1 " L "y\n\t" "s_nop " STR(PHN) "\n\t" \
                "s_cmp_lt_i32 s49, 3\n\t" "s_cbranch_scc1 " L "y\n\t" "s_nop " STR(PHN) "\n\t" "s_nop 0\n\t" "s_branch " L "y\n\t" "s_nop 0\n" L "y:\n\t"
#define PHN 3
#define REL(L) "s_barrier\n" L ":\n\t"
#define RELV(L) "s_cbranch_vccnz " L "\n\t" REL(L)
#define RELN(L) "s_cmp_eq_u32 %[wv], 0\n\t" "s_cbranch_scc1 " L "a\n\t" "s_cmp_eq_u32 %[wv], 1\n\t" "s_cbranch_scc1 " L "b\n\t" \
                "s_barrier\n\t" "s_nop " STR(PD2) "\n\t" "s_branch " L "\n" L "b:\n\t" "s_barrier\n\t" "s_branch " L "\n" L "a:\n\t" "s_barrier\n" L ":\n\t"
#define PD2 2
#define TS_(n) "s_memtime s[80+2*" #n ":81+2*" #n "]\n\t"
#define TSA(n)
#define TSB(n)
#define TSW(n)
#define TVM 12
template <int NTL, int KS>
__device__ __forceinline__ void tr_chunk(const u16* S, int lr, int lg, u32 (&r)[NTL][KS][8]) {
  const int q = lr >> 2, p = lr & 3;
#pragma unroll
  for (int t = 0; t < NTL; ++t)
#pragma unroll
    for (int i = 0; i < KS; ++i)
#pragma unroll
      for (int kb = 0; kb < 2; ++kb) {
        const u16* base = S + (64 * i + 32 * kb + 16 * (lg >> 1) + 4 * (lg & 1) + (q & 1) + 8 * (q >> 1)) * (16 * NTL) + 16 * t + 4 * p;
        const u32x2 t0 = __builtin_bit_cast(u32x2, __builtin_amdgcn_ds_read_tr16_b64_v4i16((LAS v4i16_t*)(base)));
        const u32x2 t1 = __builtin_bit_cast(u32x2, __builtin_amdgcn_ds_read_tr16_b64_v4i16((LAS v4i16_t*)(base + 2 * 16 * NTL)));
        r[t][i][4 * kb + 0] = t0[0]; r[t][i][4 * kb + 1] = t0[1]; r[t][i][4 * kb + 2] = t1[0]; r[t][i][4 * kb + 3] = t1[1];
      }
}
#define SMF(acc, a, bf, bi) "v_smfmac_f32_16x16x64_bf16 " RNG("v", acc, 4) ", " RNG("v", a, 4) ", " RNG(bf, bi, 8) ", v34\n\t"
#define ZER(r) "v_mov_b64 " RNG("v", r, 2) ", 0\n\t" "v_mov_b64 " RNG("v", (r) + 2, 2) ", 0\n\t"
#define GLD(r, o) "s_xor_b32 s49, s50, " STR(o) "\n\t" "buffer_load_dwordx4 " RNG("v", r, 4) ", v32, %[rs], s49 offen sc1\n\t"
#define GL1(r, o) "s_xor_b32 s49, s50, " STR(o) "\n\t" "buffer_load_dwordx4 " RNG("v", r, 4) ", v33, %[rs], s49 offen sc1\n\t"
#define GL2(r, o) GL1(r, 4096 + (o))
#define MXA0(r) "v_max3_u32 v63, " REG(r) ", " REG((r) + 1) ", " REG((r) + 1) "\n\t"
#define MXA(r) "v_max3_u32 v63, v63, " REG(r) ", " REG((r) + 1) "\n\t"
#define MXB(r) "v_max3_u32 v63, v63, " REG((r) + 2) ", " REG((r) + 3) "\n\t"
#define CMPB "v_cmp_eq_u32 vcc, s54, v63\n\t"
#define PSA(r) "v_pk_add_f32 " RNG("v", r, 2) ", " RNG("v", r, 2) ", " RNG("v", (r) + 2, 2) "\n\t"
#define PSW(r, o) "ds_write_addtid_b32 " REG(r) " offset:" STR(o) "\n\t"
#define RDQ(r, o) "ds_read_b128 " RNG("v", r, 4) ", v37 offset:" STR(o) "\n\t"
#define SQ(t, i) SMF(64 + 4 * (t), 96 + 4 * (i), "a", ((i) * 4 + (t)) * 8)
#define SV(t, i) SMF(24 + 4 * (t), 96 + 4 * (i), "a", 128 + ((i) * 2 + (t)) * 8)
#define SVP(t, i) SV(t, i)
#define SW(a, q, i) SMF((a) + 4 * (q), 96 + 4 * (i), "v", 128 + ((i) * 2 + (q)) * 8)
#define WVM(n) "s_waitcnt vmcnt(" STR(n) ")\n\t"
#define WLG(n) "s_waitcnt lgkmcnt(" STR(n) ")\n\t"
#define WLGF(n) WLG(n)
#define PACE 7
#define PC "s_nop " STR(PACE) "\n\t"
#define PC6
#define PC7
#define WBLK(a, n, i, M0) WVM((n) + 7 - (i)) SW(a, 0, i) M0(96 + 4 * (i)) SW(a, 1, i) MXB(96 + 4 * (i))
#define CL8(a, b, c, d, e, f, g, h) "v" #a, "v" #b, "v" #c, "v" #d, "v" #e, "v" #f, "v" #g, "v" #h
#define CL64 CL8(64, 65, 66, 67, 68, 69, 70, 71)
#define CL72 CL8(72, 73, 74, 75, 76, 77, 78, 79)
#define CL80 CL8(80, 81, 82, 83, 84, 85, 86, 87)
#define CL88 CL8(88, 89, 90, 91, 92, 93, 94, 95)
#define CL96 CL8(96, 97, 98, 99, 100, 101, 102, 103), CL8(104, 105, 106, 107, 108, 109, 110, 111)
#define CL112 CL8(112, 113, 114, 115, 116, 117, 118, 119)
#define CL120 CL8(120, 121, 122, 123, 124, 125, 126, 127)
#define PKA(d, a, b) "v_pk_add_f32 " RNG("v", d, 2) ", " RNG("v", a, 2) ", " RNG("v", b, 2) "\n\t"
#define CNT(o) STR(4096 + 64 * (o))
#define ARRIVE(o) "ds_add_u32 v45, v39 offset:" CNT(o) "\n\t"

#define EXTRA "v_readlane_b32 s69, v35, s45\n\t" "s_and_b32 s56, s58, 1023\n\t" "s_bfe_u32 s57, s58, 0xa000a\n\t" "s_lshr_b32 s55, s58, 20\n\t" "s_add_u32 s66, s56, s69\n\t" "s_add_u32 s67, s57, s69\n\t" "s_add_u32 s67, s67, s69\n\t"
#define TEX(s) ((s) == 0 ? TE0 : ((s) < 4 ? TE1 : ((s) < 12 ? TE2 : ((s) < TS3 ? TE3 : 0))))
#define R2BASE "s_bitcmp1_b32 s45, 0\n\t" "s_cselect_b32 s71, " STR(XR2S) ", 0\n\t" "s_add_u32 s71, s71, %[r2]\n\t"
#define X4B 0
#define X4C 0
#define STB "buffer_store_dwordx2 v[116:117], v11, %[rs], s36 offen sc1\n"
#define STC "buffer_store_dwordx2 v[80:81], v17, %[rs], s36 offen sc1\n\t"
#define R2TEXT                                                                                                     \
              TSB(0)                                                                                               \
              "buffer_load_dwordx4 v[64:67], v43, %[rs], s71 offen sc1\n\t"                                      \
              "buffer_load_dwordx4 v[68:71], v8, %[rs], s71 offen sc1\n\t"                                       \
              "buffer_load_dwordx4 v[76:79], v9, %[rs], s71 offen sc1\n\t"                                       \
              "buffer_load_dwordx4 v[60:63], v10, %[rs], s71 offen sc1\n\t"                                      \
              "buffer_store_dwordx4 v[72:75], v47, %[rf], s41 offen sc1\n\t"                                     \
              "s_cbranch_vccnz .Lvw%=\n\t" VPOST ARRIVE(12) "s_barrier\n" ".Lvw%=:\n\t"                             \
              "ds_read2_b32 v[96:97], v12 offset0:0 offset1:4\n\t" "ds_read2_b32 v[98:99], v12 offset0:32 offset1:36\n\t" \
              "ds_read2_b32 v[100:101], v13 offset0:0 offset1:4\n\t" "ds_read2_b32 v[102:103], v13 offset0:32 offset1:36\n\t" \
              "ds_read2_b32 v[104:105], v14 offset0:0 offset1:4\n\t" "ds_read2_b32 v[106:107], v14 offset0:32 offset1:36\n\t" \
              "ds_read2_b32 v[108:109], v15 offset0:0 offset1:4\n\t" "ds_read2_b32 v[110:111], v15 offset0:32 offset1:36\n\t" \
              "v_mov_b64 v[112:113], v[80:81]\n\t" "v_mov_b64 v[114:115], v[82:83]\n\t" "s_mov_b32 s65, 0\n\t"     \
              "v_mov_b32_dpp v112, v84 row_ror:8 row_mask:0xf bank_mask:0xc\n\t" "v_mov_b32_dpp v113, v85 row_ror:8 row_mask:0xf bank_mask:0xc\n\t" \
              "v_mov_b32_dpp v114, v86 row_ror:8 row_mask:0xf bank_mask:0xc\n\t" "v_mov_b32_dpp v115, v87 row_ror:8 row_mask:0xf bank_mask:0xc\n\t" \
              "v_pk_mul_f32 v[112:113], v[112:113], s[62:63] op_sel_hi:[1,0]\n\t" "v_pk_mul_f32 v[114:115], v[114:115], s[62:63] op_sel_hi:[1,0]\n\t" \
              WLG(0)                                                                                               \
              PKA(96, 96, 100) PKA(98, 98, 102) PKA(104, 104, 108) PKA(106, 106, 110) PKA(96, 96, 104) PKA(98, 98, 106) \
              "v_cvt_pk_bf16_f32 v96, v96, 0\n\t" "v_cvt_pk_bf16_f32 v97, v97, 0\n\t" "v_cvt_pk_bf16_f32 v98, v98, 0\n\t" "v_cvt_pk_bf16_f32 v99, v99, 0\n\t" \
              "v_accvgpr_write_b32 a194, v96\n\t" "v_accvgpr_write_b32 a202, v97\n\t" "v_accvgpr_write_b32 a198, v98\n\t" "v_accvgpr_write_b32 a206, v99\n\t" \
              TSB(1) WVM(4) TSB(2) \
              "v_permlane32_swap_b32 v64, v66\n\t" "v_permlane32_swap_b32 v65, v67\n\t" \
              "v_pk_add_f32 v[92:93], v[64:65], v[66:67]\n\t" \
              WVM(3) SFM(68) \
              "v_permlane16_swap_b32 v92, v93\n\t" \
              "v_add_f32 v94, v92, v93\n\t" \
              WVM(2) "v_mul_f32 v94, s62, v94\n\t" SFM(76) \
              "v_exp_f32 v92, v94\n\t" \
              WVM(1) TSB(3) SFM(60) \
                \
              ".Lex%=:\n\t" \
              "v_exp_f32 v88, v112\n\t" "v_exp_f32 v89, v113\n\t" "v_cvt_pk_bf16_f32 v58, v92, 0\n\t" "v_exp_f32 v90, v114\n\t" "v_exp_f32 v91, v115\n\t" \
              "v_cvt_pk_bf16_f32 v56, v88, v89\n\t" "v_cvt_pk_bf16_f32 v57, v90, v91\n\t" \
              TSB(4) ROR8(50, 58) ROR8(48, 56) \
              "v_mfma_f32_16x16x32_bf16 v[88:91], v[52:55], v[56:59], 0\n\t" \
              ROR8(49, 57) "s_nop 1\n\t" \
              "v_mfma_f32_16x16x32_bf16 v[116:119], a[192:195], v[56:59], 0\n\t" \
              "s_nop 2\n\t" \
              "v_mfma_f32_16x16x32_bf16 v[116:119], a[196:199], v[48:51], v[116:119]\n\t" \
              "v_add_f32_dpp v92, v88, v88 row_ror:8 row_mask:0xf bank_mask:0xf\n\t" "v_subrev_u32 v89, 0x33800000, v92\n\t" \
              "v_mfma_f32_16x16x32_bf16 v[124:127], a[204:207], v[56:59], 0\n\t" \
              "v_rcp_f32 v93, v92\n\t" \
              "v_mfma_f32_16x16x32_bf16 v[124:127], a[200:203], v[48:51], v[124:127]\n\t" \
              TSB(5) "v_cmp_le_u32 vcc, 0x20000000, v89\n\t" "s_nop 0\n\t" \
              "s_cbranch_vccnz .Lmx%=\n\t" \
              OTAIL STB TSB(6)
#define SFM(r) "v_pk_fma_f32 v[112:113], " RNG("v", r, 2) ", s[62:63], v[112:113] op_sel_hi:[1,0,1]\n\t" "v_pk_fma_f32 v[114:115], " RNG("v", (r) + 2, 2) ", s[62:63], v[114:115] op_sel_hi:[1,0,1]\n\t"
#define ROR8(d, s) "v_mov_b32_dpp " REG(d) ", " REG(s) " row_ror:8 row_mask:0xf bank_mask:0xf\n\t"
#define OTAIL "v_pk_mul_f32 v[116:117], v[116:117], v[92:93] op_sel:[0,1]\n\t" "v_pk_mul_f32 v[118:119], v[118:119], v[92:93] op_sel:[0,1]\n\t" \
              "v_cvt_pk_bf16_f32 v116, v116, v117\n\t" "v_cvt_pk_bf16_f32 v117, v118, v119\n\t" \
              "s_mov_b64 exec, s[78:79]\n\t" \
              "v_pk_mul_f32 v[124:125], v[124:125], v[92:93] op_sel:[0,1]\n\t" "v_pk_mul_f32 v[126:127], v[126:127], v[92:93] op_sel:[0,1]\n\t" \
              "v_cvt_pk_bf16_f32 v116, v124, v125\n\t" "v_cvt_pk_bf16_f32 v117, v126, v127\n\t" \
              "s_mov_b64 exec, -1\n\t"
#define MX3(a, b) "v_max3_u32 v95, v95, " REG(a) ", " REG(b) "\n\t"
#define R2MAX ".Lmx%=:\n\t" "v_max_u32 v95, v60, v61\n\t" MX3(62, 63) MX3(64, 65) MX3(66, 67) MX3(68, 69) MX3(70, 71) MX3(76, 77) MX3(78, 79) \
              "v_cmp_eq_u32 vcc, s54, v95\n\t" "s_cbranch_vccnz .Lfail%=\n\t" OTAIL \
              "s_cmp_lg_u32 s65, 0\n\t" "s_cbranch_scc1 .Lend%=\n\t" "s_mov_b32 s65, 1\n\t" \
              "v_max3_f32 v93, v112, v113, v94\n\t" \
              "v_max3_f32 v93, v93, v114, v115\n\t" \
              "s_nop 1\n\t" \
              "v_max_f32_dpp v92, v93, v93 row_ror:8 row_mask:0xf bank_mask:0xf\n\t" \
              "v_max_f32_dpp v88, v93, v93 row_ror:8 row_mask:0xf bank_mask:0xf\n\t" \
              "s_nop 1\n\t" \
              "v_permlane16_swap_b32 v92, v88\n\t" \
              "v_max_f32 v93, v92, v88\n\t" "v_max_f32 v92, v92, v88\n\t" \
              "s_nop 1\n\t" \
              "v_permlane32_swap_b32 v93, v92\n\t" \
              "v_max_f32 v93, v93, v92\n\t" \
              "v_sub_f32 v94, v94, v93\n\t" "v_sub_f32 v112, v112, v93\n\t" "v_exp_f32 v92, v94\n\t" "v_sub_f32 v113, v113, v93\n\t" "v_sub_f32 v114, v114, v93\n\t" "v_sub_f32 v115, v115, v93\n\t" \
              "s_branch .Lex%=\n"
#define PS1(r) "v_add_f32 " REG(r) ", " REG(r) ", " REG((r) + 2) "\n\t"
#define NOP(n) "s_nop " STR(n) "\n\t"
#define PCA 13
#define PDA 8
#define PDC 8
#define PDF 7
#define SLV 0
#define QKRED(X) RDQ(80, 12288) RDQ(84, 12304) X RDQ(88, 0) RDQ(92, 16) RDQ(112, 4096) RDQ(116, 4112) RDQ(120, 8192) RDQ(124, 8208) \
        WLG(4) TSA(4) PKA(80, 80, 88) PKA(82, 82, 90) PKA(84, 84, 92) PKA(86, 86, 94) \
        WLG(2) PKA(80, 80, 112) PKA(82, 82, 114) PKA(84, 84, 116) PKA(86, 86, 118) \
        WLG(0) PKA(80, 80, 120) PKA(82, 82, 122) PKA(84, 84, 124) PKA(86, 86, 126) \
        "v_cvt_pk_bf16_f32 v72, v80, v81\n\t" "v_cvt_pk_bf16_f32 v73, v82, v83\n\t" "v_cvt_pk_bf16_f32 v74, v84, v85\n\t" "v_cvt_pk_bf16_f32 v75, v86, v87\n\t" \
        NOP(1) TSA(5) \
        "v_mfma_f32_16x16x32_bf16 v[88:91], v[72:75], v[72:75], 0\n\t" NOP(2) \
        "v_mfma_f32_16x16x32_bf16 v[80:83], a[208:211], v[72:75], 0\n\t" NOP(2) \
        "v_mfma_f32_16x16x32_bf16 v[84:87], a[212:215], v[72:75], 0\n\t"
#define QKPUB "buffer_store_dwordx4 v[88:91], v41, %[rs], s71 offen sc1\n\t" NOP(0) \
        "buffer_store_dwordx4 v[80:83], v40, %[rs], s71 offen sc1\n\t" NOP(2) \
        "buffer_store_dwordx4 v[84:87], v40, %[rs], s71 offen offset:512 sc1\n\t"
#define MRED(W, X) "ds_read_b128 v[80:83], v46 offset:12288\n\t" W "ds_read_b128 v[84:87], v46\n\t" "ds_read_b128 v[88:91], v46 offset:4096\n\t" "ds_read_b128 v[92:95], v46 offset:8192\n\t" X TSW(4) \
        WLG(2) TSW(5) PKA(80, 80, 84) PKA(82, 82, 86) WLG(0) PKA(88, 88, 92) PKA(90, 90, 94) PKA(84, 80, 88) PKA(86, 82, 90) \
        "v_cvt_pk_bf16_f32 v80, v84, v85\n\t" "v_cvt_pk_bf16_f32 v81, v86, v87\n\t"
#define ORD(a, b) "v_cmpx_o_f32 s[50:51], " REG(a) ", " REG(b) "\n\t"
#define REDO(L) "s_cmp_lg_u64 exec, -1\n\t" "s_mov_b64 exec, -1\n\t" "s_cbranch_scc1 " L "\n" L "r:\n\t"
#define CWAIT(L, o, S) L ":\n\t" "ds_read_b32 v62, v38 offset:" CNT(o) "\n\t" WLG(0) "v_readfirstlane_b32 s49, v62\n\t" "s_cmp_lt_u32 s49, " S "\n\t" "s_cbranch_scc1 " L "\n\t"
#define VPOST PSA(24) PSA(28) PSW(24, 2560) PSW(25, 2832) PSW(28, 3200) PSW(29, 3472)
#define BARA "s_barrier\n\t"
#define BARC "s_barrier\n\t"
#define PZADR(SB, M) "s_add_u32 s49, s45, 1\n\t" "s_xor_b32 s62, s71, " STR(XR2S) "\n\t" "v_readlane_b32 s63, v19, s49\n\t" "s_mov_b32 s49, " SB "\n\t" \
        ".if !((" STR(M) ") & 1)\n\t" "s_mov_b32 s63, 0x7ff00000\n\t" ".endif\n\t" ".if !((" STR(M) ") & 2)\n\t" "s_mov_b32 s49, 0x7ff00000\n\t" ".endif\n\t" \
        ".if !((" STR(M) ") & 4)\n\t" "s_mov_b32 s62, 0x7ff00000\n\t" ".endif\n\t" "s_cmp_eq_u32 %[wv], 0\n\t" \
        "s_cselect_b32 s49, s49, s62\n\t" "s_cselect_b32 s63, s63, s62\n\t" "v_add_u32 v48, s49, v23\n\t" "s_mov_b32 exec_hi, 0\n\t" "v_add_u32 v48, s63, v23\n\t" "s_mov_b32 exec_hi, -1\n\t"
#define PZST "buffer_store_dwordx4 v[92:95], v48, %[rs], 0 offen sc1\n\t"
#define MA 0
#define MC 0
#define MB (7 - MA - MC)
#define PZD 1
#define PZB0 PZADR("s63", MB)
#define PZB PZST
#define PZC0
#define PZC ""
#define PZNC 0
#define PZA0
#define PZA
#define PZNA 0
#define LASTC(L) RELV(L)
#define LASTA(L) ""
#define PCL PCA
#define ABODY(W, X, F, n) TSA(0) GLD(96, 0) NOP(PCA) GLD(100, 1024) NOP(PCA) GLD(104, 2048) W GLD(108, 3072) "s_mov_b32 s47, 0\n\t" X \
        WVM((n) + 3) TSA(1) SQ(0, 0) SQ(1, 0) SQ(2, 0) SQ(3, 0) \
        WVM((n) + 2) SQ(0, 1) SQ(1, 1) SQ(2, 1) SQ(3, 1) \
        WVM((n) + 1) SQ(0, 2) MXA0(96) MXB(96) SQ(1, 2) MXA(100) MXB(100) SQ(2, 2) MXA(104) MXB(104) SQ(3, 2) \
        WVM(n) TSA(2) SQ(0, 3) MXA(108) MXB(108) CMPB SQ(1, 3) "s_add_u32 s48, s38, 4\n\t" NOP(0) NOP(0) SQ(2, 3) "s_cbranch_vccnz " F "\n\t" PS1(64) PS1(65) SQ(3, 3) PS1(68) PS1(69) \
        NOP(0) PSW(64, 0) PSW(65, 272) PSW(68, 640) PSW(69, 912) PSA(72) PSA(76) PSW(72, 1280) PSW(73, 1552) PSW(76, 1920) PSW(77, 2192)
#define CISSUE(X, Y) TSW(0) GL1(96, 0) PC GL1(100, 1024) PC GL1(104, 2048) PC GL1(108, 3072) PC GL2(112, 0) PC GL2(116, 1024) \
        "s_mov_b32 s48, s36\n\t" "s_mov_b32 s36, s37\n\t" "s_add_u32 s45, s45, 1\n\t" "s_add_u32 s38, s38, 4\n\t" "v_readlane_b32 s37, v19, s45\n\t" \
        "s_add_u32 s39, s39, " STR(EXF) "\n\t" "s_add_u32 s40, s40, " STR(EXF) "\n\t" \
        "s_add_u32 s41, s41, " STR(EXK) "\n\t" "s_add_u32 s42, s42, " STR(EXK) "\n\t" "s_add_u32 s43, s43, s55\n\t" PC6 \
        GL2(120, 2048) "s_mov_b32 s47, 2\n\t" "s_mul_i32 s70, s69, 3\n\t" R2BASE "s_add_u32 s43, s43, s70\n\t" NOP(2) PC7 X \
        GL2(124, 3072) Y
#define ZALL ZER(64) ZER(68) ZER(72) ZER(76) ZER(80) ZER(84) ZER(24) ZER(28)
#define SHRD "ds_read_b32 v62, v38\n\t"
#define SHUP WLG(0) "v_readfirstlane_b32 s49, v62\n\t" "s_max_u32 s44, s44, s49\n\t"
#define DPA(d, r, m) "v_add_f32_dpp " REG(d) ", " REG(r) ", " REG((r) + 2) " quad_perm:[0,1,2,3] row_mask:" m " bank_mask:0xf\n\t"
#define CBODY(m, X, n, F) CBODY_(80, m, X, n, F)
#define CBODY_(a, m, X, n, F) \
        WVM((m) + 7) TSW(1) WBLK(a, m, 0, MXA0) WBLK(a, m, 1, MXA) X WBLK(a, n, 2, MXA) WBLK(a, n, 3, MXA) WBLK(a, n, 4, MXA) WBLK(a, n, 5, MXA) WBLK(a, n, 6, MXA) \
        WVM(n) TSW(2) SW(a, 0, 7) MXA(124) MXB(124) CMPB SW(a, 1, 7) NOP(0) NOP(1) "s_cbranch_vccnz " F "\n\t" \
        DPA(88, a, "0x3") DPA(89, (a) + 1, "0x3") NOP(1) DPA(88, (a) + 4, "0xc") DPA(89, (a) + 5, "0xc") NOP(1) \
        "v_permlane32_swap_b32 v88, v89\n\t" "v_add_f32 v88, v88, v89\n\t" TSW(3) PSW(88, 3840)
#define FINEO
#define NEXTP "s_branch .LA%=\n"
#define NEXTN "s_branch .LNA%=\n"

__device__ __forceinline__ void clk_edge(u32& cyc, u32& tick) {
  const u32 t = rt32();
  for (int i = 0; i < 64; ++i) { tick = rt32(); if (tick != t) break; }
  cyc = (u32)__builtin_amdgcn_s_memtime();
}
__device__ __forceinline__ float clk_rate(u32 cyc0, u32 tk0) { u32 c, t; clk_edge(c, t); return (float)(c - cyc0) / (float)max(t - tk0, 1u); }
__device__ __forceinline__ void dma16(__amdgpu_buffer_rsrc_t rs, int voff, int soff, int sstep, int ldsa) {
#pragma unroll
  for (int i = 0; i < 16; ++i)
    asm volatile("s_mov_b32 m0, %0\n\ts_nop 3\n\tbuffer_load_dwordx4 %1, %2, %3 offen lds" :: "s"(ldsa + i * 1024), "v"(voff), "s"(rs), "s"(soff ^ (i * sstep)) : "memory");
}

#define KH(t) (192 + 8 * (t))
#define KMF(t, a, b) "v_mfma_f32_16x16x32_bf16 " RNG("v", 80 + 4 * (t), 4) ", " RNG("v", KH(t) + 4 * (a), 4) ", " RNG("v", KH(3) + 4 * (b), 4) ", " RNG("v", 80 + 4 * (t), 4) "\n\t"
#define KX(j) (92 + 4 * ((j) & 1))
#define KA(t, j) "v_pk_mul_f32 " RNG("v", KX(j), 2) ", " RNG("v", 64 + 4 * (j), 2) ", " RNG("v", 112 + ((t) & 2), 2) " op_sel:[0," STR((t) & 1) "] op_sel_hi:[1," STR((t) & 1) "]\n\t"
#define KF(j) "v_fract_f32 " REG(KX(j) + 1) ", " REG(KX(j) + 1) "\n\t"
#define KE(j, N) "v_exp_f32 " REG(KX(j)) ", " REG(KX(j)) "\n\t" "v_cos_f32 " REG(KX(j) + 2) ", " REG(KX(j) + 1) "\n\t" "v_sin_f32 " REG(KX(j) + 3) ", " N REG(KX(j) + 1) "\n\t"
#define KM(j) "v_pk_mul_f32 " RNG("v", KX(j) + 2, 2) ", " RNG("v", KX(j) + 2, 2) ", " RNG("v", KX(j), 2) " op_sel_hi:[1,0]\n\t"
#define KC(j) "v_pk_mul_f32 " RNG("v", KX(j), 2) ", " RNG("v", KX(j) + 2, 2) ", " RNG("v", 66 + 4 * (j), 2) " op_sel_hi:[1,0]\n\t" \
  "v_pk_fma_f32 " RNG("v", KX(j), 2) ", " RNG("v", KX(j) + 2, 2) ", " RNG("v", 66 + 4 * (j), 2) ", " RNG("v", KX(j), 2) " op_sel:[1,1,0] op_sel_hi:[0,1,1] neg_hi:[1,0,0]\n\t"
#define KD(t, j, r) "v_cvt_pk_bf16_f32 " REG(KH(t) + (j)) ", " REG(r) ", " REG((r) + 1) "\n\t" \
  "v_dot2c_f32_bf16 " REG(r) ", s47, " REG(KH(t) + (j)) "\n\t" "v_dot2c_f32_bf16 " REG((r) + 1) ", s48, " REG(KH(t) + (j)) "\n\t"
#define KL(t, j, r) "v_cvt_pk_bf16_f32 " REG(KH(t) + 4 + (j)) ", " REG(r) ", " REG((r) + 1) "\n\t"
#define KDM "s_add_u32 m0, m0, 1024\n\t" "s_add_u32 s43, s43, s42\n\t" "buffer_load_dwordx4 v110, s[36:39], s43 offen lds\n\t"
#define KP(j) "ds_read_b128 " RNG("v", 64 + 4 * (j), 4) ", v111 offset:" STR(16 * (j)) "\n\t"
#define KV(j, L, A) KE(j, "-") L KM(j) KC(j) A KD(3, j, KX(j))
#define KU(t, j, L, A) KE(j, "") L KM(j) A KD(t, j, KX(j) + 2)
#define KSUB                                                                                                        \
  KV(0, WLG(8) "s_add_u32 m0, m0, 1024\n\t", KA(3, 1)) KV(1, KL(3, 0, KX(0)), KA(3, 2))                              \
  "s_xor_b32 s43, s40, s41\n\t" "buffer_load_dwordx4 v110, s[36:39], s43 offen lds\n\t" KV(2, KL(3, 1, KX(1)), KA(3, 3)) \
  KV(3, KL(3, 2, KX(2)), KA(0, 0))                                                                                  \
  KU(0, 0, KL(3, 3, KX(3)), KA(0, 1)) KU(0, 1, KL(0, 0, KX(0) + 2), KA(0, 2)) KDM KU(0, 2, KL(0, 1, KX(1) + 2), KA(0, 3)) \
  KU(0, 3, KL(0, 2, KX(2) + 2), KA(1, 0)) KF(0)                                                                     \
  KU(1, 0, KL(0, 3, KX(3) + 2), KA(1, 1)) KF(1) KMF(0, 1, 0) KU(1, 1, KL(1, 0, KX(0) + 2), KA(1, 2)) KF(2) KDM      \
  KMF(0, 0, 1) KU(1, 2, KL(1, 1, KX(1) + 2), KA(1, 3)) KF(3) KMF(0, 0, 0) KU(1, 3, KL(1, 2, KX(2) + 2), KA(2, 0)) KF(0) KP(0) \
  KU(2, 0, KL(1, 3, KX(3) + 2), KA(2, 1)) KF(1) KP(1) KMF(1, 1, 0) KU(2, 1, KL(2, 0, KX(0) + 2), KA(2, 2)) KF(2) KP(2) KDM \
  KMF(1, 0, 1) KU(2, 2, KL(2, 1, KX(1) + 2), KA(2, 3)) KF(3) KP(3) KMF(1, 0, 0) KU(2, 3, KL(2, 2, KX(2) + 2), WLG(3) KA(3, 0)) \
  KMF(2, 0, 1) "v_add_u32 v111, 256, v111\n\t" "s_lshl2_add_u32 s41, s42, s41\n\t" KL(2, 3, KX(3) + 2)
#define KZERO ZER(80) ZER(84) ZER(88)
#define KCHUNK(PTR, VO, SO, ST, BUF, PF) "s_mov_b64 s[36:37], " PTR "\n\t" "s_mov_b32 m0, %[sc]\n\t" "v_mov_b32 v110, " VO "\n\t" SO "s_mov_b32 s41, 0\n\t" "s_mov_b32 s42, " STR(ST) "\n\t" \
  "buffer_load_dword " PF ", s[36:39], 0 offen lds\n\t" "s_add_u32 m0, %[sl], " STR((BUF) * 16384 - 1024) "\n\t"
#define KCALL "s_call_b64 s[44:45], .Lkg%=\n\t" WVM(16) KMF(2, 0, 0)
#define KTR(F, d, A, o) "ds_read_b64_tr_b16 " RNG(F, d, 2) ", " A " offset:" STR(o) "\n\t"
#define KTQ1(d, o) KTR("a", d, "%[aq]", o) KTR("a", (d) + 2, "%[aq]", (o) + 128) KTR("a", (d) + 4, "%[aq]", (o) + 2048) KTR("a", (d) + 6, "%[aq]", (o) + 2176)
#define KTQ(d, o) KCALL KTQ1(d, o) KMF(2, 1, 0) KTQ1((d) + 8, (o) + 32)
#define KTO1(d, o) KTR("v", d, "%[ao]", o) KTR("v", (d) + 2, "%[ao]", (o) + 64) KTR("v", (d) + 4, "%[ao]", (o) + 1024) KTR("v", (d) + 6, "%[ao]", (o) + 1088)
#define KTO(d, o) KCALL KTO1(d, o) KMF(2, 1, 0) KTO1((d) + 8, (o) + 2048)
#define KOUT(o) "s_add_u32 s43, %[ko], " STR(o) "\n\t"                                                              \
  "buffer_store_dwordx4 v[80:83], %[l16], %[rt], s43 offen sc1\n\t" "buffer_store_dwordx4 v[84:87], %[l16], %[rt], s43 offen offset:1024 sc1\n\t" "s_nop 3\n\t" \
  "buffer_store_dwordx4 v[88:91], %[l16], %[rt], s43 offen offset:2048 sc1\n\t"
struct SsmPtrs { const u16 *A_re, *A_im, *B_re, *B_im, *C_re, *C_im, *log_dt; };
__device__ __forceinline__ void r_role(unsigned char* lds, int b, const u16* __restrict__ mem0, const u16* __restrict__ Wq, const u16* __restrict__ Wk, const u16* __restrict__ Wv,
                                       const u16* __restrict__ Wo, int fb, u32 tag, u64 pc0, int clen, __amdgpu_buffer_rsrc_t rsx, __amdgpu_buffer_rsrc_t rsf,
                                       __amdgpu_buffer_rsrc_t rst, SsmPtrs sp, int tid, int lane, int w
                                       ) {
  const int lr = lane & 15, lg = lane >> 4;
  const int ldsb = (int)(size_t)(LAS unsigned char*)lds;
  const int idx = (lr & 2) ? 0xEEEE : 0x4444;
  u32 tcnt = 0; (void)tcnt;
  u32 cyc0 = 0, tk0 = 0;
  if (tid < 8) *(u32*)(lds + LR_CTL + tid * 4) = 0u;
  *(u32*)(lds + LR_CTL + 4096 + 256 + tid * 4) = 0u;
  const int xcd = b & 3, li = b >> 2;
  const int head = xcd * 8 + (li >> 2), ks = li & 3;
  const int vcu = head * 4 + ks;
  const int cg = (xcd >> 1) * 32 + li, rg = xcd & 1;
  const int rot1 = ((li >> 1) + w) & 3, rot3 = ((li >> 2) + 2 * w) & 7;
  const int pz1 = lane < 32 ? XR1A(lane >> 3, cg * 256 + rg * 128 + (lane & 7) * 16) : XR3A(lg & 1, vcu * 256 + lr * 16);
  const int pgo = PGO_X + head * 128;
  const int calo = XCAL + (li & 3) * 8192 + ((li >> 2) & 1) * 4096 + ((li >> 3) * 4 + xcd) * 128;
  u32 xp0;
  u32x32 wa[6], wo[4];
  {
    const __amdgpu_buffer_rsrc_t rwq = __builtin_amdgcn_make_buffer_rsrc((void*)Wq, 0, DM * HD * 2, 0x00020000);
    const int vq = (lane >> 2) * (HD * 2) + vcu * 64 + (lane & 3) * 16, sq = (256 * w + (rot1 << 6)) * (HD * 2);
    const int vo = (lane >> 1) * (DM * 2) + cg * 32 + (lane & 1) * 16, so = (1024 * w + (rot3 << 7)) * (DM * 2);
    const int sl = ldsb + LR_STG + w * 32768;
    const int ch0 = (b * 4 + w) * 2;
    u32x2 pin[6];
    u32 ldt;
    {
      const int po = (ch0 * SSM_N + 4 * lane) * 2;
      asm volatile("global_load_dwordx2 %0, %7, %9\n\tglobal_load_dwordx2 %1, %7, %10\n\tglobal_load_dwordx2 %2, %7, %11\n\tglobal_load_dwordx2 %3, %7, %12\n\t"
                   "global_load_dwordx2 %4, %7, %13\n\tglobal_load_dwordx2 %5, %7, %14\n\tglobal_load_ushort %6, %8, %15"
                   : "=&v"(pin[0]), "=&v"(pin[1]), "=&v"(pin[2]), "=&v"(pin[3]), "=&v"(pin[4]), "=&v"(pin[5]), "=&v"(ldt)
                   : "v"(po), "v"((ch0 + (lane >> 5)) * 2), "s"(sp.A_re), "s"(sp.A_im), "s"(sp.B_re), "s"(sp.B_im), "s"(sp.C_re), "s"(sp.C_im), "s"(sp.log_dt) : "memory");
    }
    u32x4 m0v;
    asm volatile("global_load_dwordx4 %0, %1, %2" : "=&v"(m0v) : "v"(((4 * rg + ((lane >> 1) & 3)) * DM + 16 * cg + 8 * (lane & 1)) * 2), "s"(mem0) : "memory");
    const u16* const wp[4] = {Wq, Wk, Wv, Wo};
    int pfo[5];
#pragma unroll
    for (int c = 0; c < 5; ++c) {
      const int lo_ = (int)(size_t)wp[c < 3 ? c : 3], s3 = (384 - lo_) & 1023, s63 = (8064 - lo_) & 8191;
      pfo[c] = c < 3 ? (lane < 16 ? (256 * w + li * 8 + (lane & 7)) * (HD * 2) + xcd * 2048 + (lane >> 3) * 1024 + s3
                                  : (lane < 24 && (s63 >> 11) == xcd ? (256 * w + li * 8 + (lane & 7)) * (HD * 2) + s63 : OOR))
                     : (lane < 16 ? (1024 * w + li * 16 + lane + (((c - 3) * 512) ^ ((((s3 >> 7) + 2 * w) & 4) << 7))) * (DM * 2) + (xcd >> 1) * 1024 + s3 : OOR);
    }
    const int scr = ldsb + LR_RED + 1024 + w * 256;
    asm volatile("s_mov_b32 m0, %0\n\ts_nop 3\n\tbuffer_load_dword %1, %2, 0 offen lds" :: "s"(scr), "v"(pfo[0]), "s"(rwq) : "memory");
#define KP0 16
#pragma unroll
    for (int i = 0; i < KP0; ++i)
      asm volatile("s_mov_b32 m0, %0\n\ts_nop 3\n\tbuffer_load_dwordx4 %1, %2, %3 offen lds" :: "s"(sl + i * 1024), "v"(vq), "s"(rwq), "s"(sq ^ (i * 16 * HD * 2)) : "memory");
    asm volatile("s_waitcnt vmcnt(1+" STR(KP0) ")" : "+v"(pin[0]), "+v"(pin[1]), "+v"(pin[2]), "+v"(pin[3]), "+v"(pin[4]), "+v"(pin[5]), "+v"(ldt), "+v"(m0v)::"memory");
#define PZE 4
    auto ring_pz = [&]() {
      const u32x4 pz = (u32x4){SENT, SENT, SENT, SENT};
      if (w < 3) {
        u32x4 v = pz;
        if (w == 0 && lane < 32) v = (u32x4){nosent(m0v[0]), nosent(m0v[1]), nosent(m0v[2]), nosent(m0v[3])};
#pragma unroll
        for (int hf = 0; hf < 2; ++hf) {
          __builtin_amdgcn_raw_buffer_store_b128(v, rsx, pz1, XSB(w) + hf * 8192, 16);
          __builtin_amdgcn_raw_buffer_store_b128(pz, rsx, XR2(vcu) + lane * 16, XR2B + (w & 1) * XR2S + hf * 8192, 16);
          __builtin_amdgcn_raw_buffer_store_b128(pz, rsx, lane < 16 ? XR2(vcu) + 1024 + lane * 16 : OOR, XR2B + (w & 1) * XR2S + hf * 8192, 16);
        }
      } else {
        __builtin_amdgcn_raw_buffer_store_b128(pz, rsx, lane < 8 ? lane * 16 : OOR, calo, 16);
        if (b < 32) {
          __builtin_amdgcn_raw_buffer_store_b128(pz, rsx, lane * 16, XSYN + b * 2048, 16);
          __builtin_amdgcn_raw_buffer_store_b128(pz, rsx, lane * 16, XSYN + b * 2048 + 1024, 16);
        } else if (b >= 48 && b < 48 + 41) {
          __builtin_amdgcn_raw_buffer_store_b128(pz, rsx, lane * 16, PGO_D + (b - 48) * 1024, 16);
        }
      }
      asm volatile("" ::: "memory");
    };
    auto late_pz = [&]() {
      const u32x4 pz = (u32x4){SENT, SENT, SENT, SENT};
#pragma unroll
      for (int i = XS0; i < NSEG; i += 4) __builtin_amdgcn_raw_buffer_store_b128(pz, rsf, (lane < 32 ? FO_KM : FO_VM - 512) + vcu * 512 + lane * 16, (i + w) * EXK, 16);
      if (w < 2) __builtin_amdgcn_raw_buffer_store_b128(pz, rsf, lane * 16, FO_O + (NSEG - 1) * 262144 + (vcu * 2 + w) * 1024, 16);
    };
    {
      if (PZE == 1 || PZE == 2) late_pz();
      if (PZE == 2) ring_pz();
      asm volatile("" ::: "memory");
    }
    f32x4* prm = (f32x4*)(lds + w * 4096);
    {
      const float dt = rbf(expf(bf2f((u16)ldt)));
#pragma unroll
      for (int i = 0; i < 4; ++i) {
        float v[6];
#pragma unroll
        for (int j = 0; j < 6; ++j) v[j] = __uint_as_float((i & 1) ? (pin[j][i >> 1] & 0xffff0000u) : (pin[j][i >> 1] << 16));
        const float are = dt * v[0], aim = dt * v[1], br = dt * v[2], bi = dt * v[3];
        const float cr = v[4] * br - v[5] * bi, ci = v[4] * bi + v[5] * br;
        const float rev = aim * 0.15915494309189535f;
        prm[4 * lane + i] = (f32x4){are * 1.4426950408889634f, rev - __builtin_rintf(rev), cr, ci};
      }
    }
#pragma unroll
    for (int i = KP0; i < 16; ++i)
      asm volatile("s_mov_b32 m0, %0\n\ts_nop 3\n\tbuffer_load_dwordx4 %1, %2, %3 offen lds" :: "s"(sl + i * 1024), "v"(vq), "s"(rwq), "s"(sq ^ (i * 16 * HD * 2)) : "memory");
    const f32x4 lv = {(float)(128 * (lr >> 3) + (lr & 7)), (float)(lr < 8 ? 256 + 128 * (lr >> 2) + 2 * (lr & 3) : 512 + 128 * ((lr - 8) >> 1) + 4 * (lr & 1)),
                         (float)(lr <= 8 ? 1024 + 128 * lr : 0), (float)(8 * lr)};
    const int trow = 16 * (lg >> 1) + 4 * (lg & 1) + ((lr >> 2) & 1) + 8 * (lr >> 3);
    asm volatile(
        "s_nop 4\n\t" "s_mov_b32 s46, m0\n\t" "s_mov_b32 s38, " STR(DM * HD * 2) "\n\t" "s_mov_b32 s39, 0x20000\n\t" "s_mov_b32 s47, 0xbf80\n\t" "s_mov_b32 s48, 0xbf800000\n\t"
        WLG(0)
        "ds_read_b128 v[64:67], %[pa]\n\t" "ds_read_b128 v[68:71], %[pa] offset:16\n\t" "ds_read_b128 v[72:75], %[pa] offset:32\n\t" "ds_read_b128 v[76:79], %[pa] offset:48\n\t"
        "v_add_u32 v111, 256, %[pa]\n\t" KZERO
        KCHUNK("%[wk]", "%[vq]", "s_mov_b32 s40, %[sq]\n\t", 16 * HD * 2, 1, "%[p1]")
        WLG(0) KA(3, 0)
        KTQ(0, 0) KTQ(32, 4096) KTQ(64, 8192) KTQ(96, 12288)
        KCHUNK("%[wv]", "%[vq]", "", 16 * HD * 2, 0, "%[p2]")
        KTQ(16, 16384) KTQ(48, 16384 + 4096) KTQ(80, 16384 + 8192) KTQ(112, 16384 + 12288)
        KOUT(0)
        KCHUNK("%[wo]", "%[vo]", "s_mov_b32 s40, %[so]\n\t", 32 * DM * 2, 1, "%[p3]")
        KZERO
        KTQ(128, 0) KTQ(144, 4096) KTQ(160, 8192) KTQ(176, 12288)
        KCHUNK("%[wo]", "%[vo]", "s_xor_b32 s40, s40, " STR(512 * DM * 2) "\n\t", 32 * DM * 2, 0, "%[p4]")
        KTO(128, 16384) KTO(144, 16384 + 4096) KTO(160, 16384 + 8192) KTO(176, 16384 + 12288)
        KOUT(3072)
        "s_branch .Lke%=\n"
        ".Lkg%=:\n\t"
        KSUB
        "s_setpc_b64 s[44:45]\n"
        ".Lke%=:\n\t"
        WLG(0) "s_mov_b32 m0, s46"
        : "=&{a[0:31]}"(wa[0]), "=&{a[32:63]}"(wa[1]), "=&{a[64:95]}"(wa[2]), "=&{a[96:127]}"(wa[3]), "=&{a[128:159]}"(wa[4]), "=&{a[160:191]}"(wa[5]),
          "=&{v[128:159]}"(wo[0]), "=&{v[160:191]}"(wo[1])
        : "{v[112:115]}"(lv), [pa] "v"(ldsb + w * 4096 + lg * 64), [vq] "v"(vq), [vo] "v"(vo),
          [p1] "v"(pfo[1]), [p2] "v"(pfo[2]), [p3] "v"(pfo[3]), [p4] "v"(pfo[4]), [aq] "v"(sl + trow * 64 + (lr & 3) * 8), [ao] "v"(sl + trow * 32 + (lr & 3) * 8), [l16] "v"(lane * 16),
          [wk] "s"(Wk), [wv] "s"(Wv), [wo] "s"(Wo), [sq] "s"(sq), [so] "s"(so), [sl] "s"(sl), [sc] "s"(scr), [rt] "s"(rst), [ko] "s"(2 * SSM_L * SSM_D * 2 + ch0 * 3072)
        : CL64, CL72, CL80, CL88, CL96, CL8(192, 193, 194, 195, 196, 197, 198, 199), CL8(200, 201, 202, 203, 204, 205, 206, 207), CL8(208, 209, 210, 211, 212, 213, 214, 215),
          CL8(216, 217, 218, 219, 220, 221, 222, 223), "s36", "s37", "s38", "s39", "s40", "s41", "s42", "s43", "s44", "s45", "s46", "s47", "s48", "scc", "memory");
    if (PZE == 0) late_pz();
    if (PZE != 2) ring_pz();
    xp0 = __builtin_amdgcn_raw_buffer_load_b32(rsx, (lane & 1) * 4096, pgo, 16);
    clk_edge(cyc0, tk0);
    asm volatile("s_waitcnt vmcnt(0)\n\ts_barrier" : "+v"(xp0)::"memory");
#define CLKF() clk_rate(cyc0, tk0)
    if (w == 0) {
      const int l0 = lane ? OOR : 0;
      const u64 c0 = __builtin_amdgcn_s_memtime();
      u32 v;
      asm volatile("s_waitcnt lgkmcnt(0)\n\tbuffer_load_dword %0, off, %1, %2 sc1\n\tbuffer_store_dword %3, %4, %1, %5 offen sc1\n\ts_waitcnt vmcnt(1)"
                   : "=&v"(v) : "s"(rsx), "s"(calo), "v"(tag), "v"(l0), "s"(PGO_Y + b * 128), "s"(c0) : "memory");
      const u32 lat = (u32)(__builtin_amdgcn_s_memtime() - c0);
      const float f = CLKF();
      typedef __attribute__((ext_vector_type(3))) unsigned int u32x3;
      __builtin_amdgcn_raw_buffer_store_b96((u32x3){__float_as_uint(f), lat, tag}, rsx, l0, PGO_Y + b * 128 + 4, 16);
      asm volatile("" ::: "memory");
    }
    {
      u32 r[1][8][8];
      tr_chunk<1, 8>((const u16*)(lds + LR_STG + w * 32768), lr, lg, r);
#pragma unroll
      for (int i = 0; i < 8; ++i)
#pragma unroll
        for (int e = 0; e < 8; ++e) { const int o = (8 + i) * 8 + e; wo[o >> 5][o & 31] = r[0][i][e]; }
      asm volatile("s_waitcnt lgkmcnt(0)" ::: "memory");
    }
    if (PZE == 4) late_pz();
    asm volatile("" ::: "memory");
  }
  const int hf = (lr >> 1) & 1;
  const int voff1 = lg * 256 + (lr >> 3) * 128 + (((lr >> 2) & 1) * 2 + (lr & 1)) * 32 + hf * 16;
  const int voff3 = (lr >> 3) * 512 + (lg >> 1) * 256 + (((lr >> 2) & 1) * 2 + (lr & 1)) * 16 + (lg & 1) * 128 + hf * 64;
  const int ard = ldsb + (2 * (lr >> 3) + (lg >> 1)) * 640 + (lr & 1) * 272 + ((lr >> 1) & 3) * 64 + (lg & 1) * 32;
  const int ctl = ldsb + LR_CTL, one = w == 0 ? 2 : (w == 3 ? 4 : 1);
  const int r8 = lane & 7, hk = (lane >> 3) & 1;
#define COA 1
#define COB 1
  const int st01 = lr < 8 ? XR2(vcu) + lr * 16 + lg * 128 : OOR, st2 = (lr < 8 && lg >= 2) ? XR2(vcu) + 1024 + lr * 16 + (lg - 2) * 128 : OOR;
  const int ld01 = hk * 512 + r8 * 16 + lg * 128, ld2 = XR2(head * 4 + lg) + 1024 + r8 * 16 + hk * 128;
  int sto = XR3A(r8 >> 2, vcu * 256 + (r8 & 3) * 16 + lg * 64 + hk * 8);
  const int stk = lr >= 8 ? (lg * 8 + (lr - 8)) * 16 : OOR;
  const int lo = X4C ? 2 * (lr & 7) + (lr >> 3) : lr;
  const int arw = ldsb + LR_CTL + lane * 4, arr = ldsb + 3840 + ((lo >> 3) + 2 * ((lo >> 2) & 1)) * 64 + (lo & 3) * 16;
  const int st1 = XR1A(lg, cg * 256 + rg * 128 + lo * 8);
  const int avw = 0;
  const int avc = ldsb + 2560 + (lr >> 3) * 640 + (8 * ((lr >> 2) & 1) + (lr & 3)) * 4;
  const int avr = avc + (lg & 1) * 272 + (lg >> 1) * 64, avf = avc + (lg & 1) * 128 + (lg >> 1) * 16;
  u32x16 kc = {(u32)voff1, (u32)voff3, (u32)idx, (u32)TEX(lane), (u32)(lane < 16 ? XR2(vcu) + 1024 + lane * 16 : OOR), (u32)ard, (u32)ctl, (u32)one, (u32)st01, (u32)st2, (u32)(XR2(vcu) + lane * 16), (u32)ld2, (u32)(lane * 16), (u32)arw, (u32)arr, (u32)stk};
  int o1c = XR1A(xcd, w * 4096) + rot1 * 1024, o3c = XR3A(rg, w * 8192) + rot3 * 1024;
  const int fok = FO_K + (head * 8 + ks) * 1024, fov = FO_V + (head * 8 + 2 * ks) * 1024;
#define XPROG(v) min(xprog(__builtin_amdgcn_readlane(v, 0), tag), xprog(__builtin_amdgcn_readlane(v, 1), tag))
  int xp = w == 3 ? XPROG(xp0) : 1000;
#define XWAIT(n) for (int it_ = 0; it_ < SPIN_CAP && xp < (n); ++it_) {                                               \
    if (it_) asm volatile("s_sleep 4" ::: "memory");                                                                  \
    const u32 v_ = __builtin_amdgcn_raw_buffer_load_b32(rsx, (lane & 1) * 4096, pgo, 16); xp = XPROG(v_); asm volatile("" ::: "memory"); }
  XWAIT(1)
  u32x16 fv;
  u32x8 fk;
  {
    const int lo_ = w == 3 ? lane * 16 : OOR;
    const u32x4 v0 = __builtin_amdgcn_raw_buffer_load_b128(rsf, lo_, fov, 16), v1 = __builtin_amdgcn_raw_buffer_load_b128(rsf, lo_, fov + 1024, 16);
    const u32x4 k0 = __builtin_amdgcn_raw_buffer_load_b128(rsf, lo_, fok, 16), k1 = __builtin_amdgcn_raw_buffer_load_b128(rsf, lo_, fok + 4096, 16);
    fv = (u32x16){v0[0], v0[1], 0u, 0u, v0[2], v0[3], 0u, 0u, v1[0], v1[1], 0u, 0u, v1[2], v1[3], 0u, 0u};
    fk = (u32x8){k0[0], k0[1], k0[2], k0[3], k1[0], k1[1], k1[2], k1[3]};
    asm volatile("" : "+{a[192:207]}"(fv), "+{a[208:215]}"(fk));
  }
#define XPN 1
#define CALM 40
  XWAIT(2)
#pragma unroll
  for (int i = 0; i < 3; ++i) *(u32x4*)(lds + w * 4096 + i * 1024 + lane * 16) = (u32x4){SENT, SENT, SENT, SENT};
  {
    u64 pc; asm volatile("s_getpc_b64 %0" : "=s"(pc));
    const u32 cv = __builtin_amdgcn_raw_buffer_load_b32(__builtin_amdgcn_make_buffer_rsrc((void*)pc0, 0, clen, 0x00020000), (int)(pc - pc0) + tid * 64, 0, 0);
    asm volatile("s_waitcnt vmcnt(0)" :: "v"(cv) : "memory");
  }
  if (w == 3) { const u32 v_ = __builtin_amdgcn_raw_buffer_load_b32(rsx, (lane & 1) * 4096, pgo, 16); xp = max(xp, XPROG(v_)); }
  u32x4 st;
  if (w == 1) {
    if (b == 0) {
      u32 tt = 0, best = 0;
      bool xok = false;
      const int sto_ = lane < 5 ? (lane ? XSTR(lane - 1) : XSTA) : OOR;
      for (int it = 0; it < SPIN_CAP; ++it) {
        asm volatile("" ::: "memory");
        const u32x4 r0 = __builtin_amdgcn_raw_buffer_load_b128(rsx, lane * 128, PGO_Y, 16), r1 = __builtin_amdgcn_raw_buffer_load_b128(rsx, lane * 128 + 8192, PGO_Y, 16);
        if (!xok) xok = __ballot(xprog(__builtin_amdgcn_raw_buffer_load_b32(rsx, lane * 128, PGO_X, 16), tag) < XPN) == 0;
        const bool c0 = r0[3] == tag, c1 = r1[3] == tag;
        if (tt == 0 && __popcll(__ballot(!c0 || !c1)) <= CALM) {
          float fm = (c0 ? __uint_as_float(r0[1]) : 0.f) + (c1 ? __uint_as_float(r1[1]) : 0.f), fn = (c0 ? 1.f : 0.f) + (c1 ? 1.f : 0.f);
#pragma unroll
          for (int o = 4; o < 64; o *= 2) { fm += __shfl_xor(fm, o); fn += __shfl_xor(fn, o); }
          fm = fn > 0.f ? fm / fn : 100.f;
          const float f0 = __uint_as_float(__builtin_amdgcn_readfirstlane(__float_as_uint(fm)));
          fm = fminf(fm, __shfl_xor(fm, 1)); fm = fminf(fm, __shfl_xor(fm, 2));
          const float f_ = CLKF() * fminf(fmaxf(fm / f0, 0.8f), 1.f);
          const float k_ = 1.f + TKS * (fminf(fmaxf(23.9f / f_, 1.f), 1.5f) - 1.f);
          const int nb_ = (int)(TDB * k_ + 0.5f), nc_ = nb_ + (int)((TDC - TDB) * k_ + 0.5f), np_ = nc_ + (int)((TP - TDC) * k_ + 0.5f);
          u32 lat = min(c0 ? r0[2] : 65535u, c1 ? r1[2] : 65535u);
          { auto r = __builtin_amdgcn_permlane16_swap(lat, lat, false, false); lat = min(r[0], r[1]); }
          { auto r = __builtin_amdgcn_permlane32_swap(lat, lat, false, false); lat = min(r[0], r[1]); }
          u32 mm[4];
#pragma unroll
          for (int k = 0; k < 4; ++k)
            mm[k] = __builtin_amdgcn_readlane(lat, 4 * k) + __builtin_amdgcn_readlane(lat, 4 * k + 2) + __builtin_amdgcn_readlane(lat, 4 * (k ^ 2) + 1) + __builtin_amdgcn_readlane(lat, 4 * (k ^ 2) + 3);
          const u32 hb = mm[0] + mm[2] <= mm[1] + mm[3] ? 0u : 1u;
          best = (hb << 13) | ((hb ? mm[1] <= mm[3] : mm[0] <= mm[2]) ? CLS0 : 16384u - CLS0);
          if (__ballot(lat == 65535u) == 0) tt = __builtin_amdgcn_readfirstlane(nb_ | (nc_ << 10) | (np_ << 20));
        }
        if (__ballot(r0[0] != tag || r1[0] != tag) == 0 && xok && tt) break;
      }
      st = (u32x4){rt32() + TD0, tt, best, tag};
      __builtin_amdgcn_raw_buffer_store_b128(st, rsx, sto_, 0, 16);
    } else st = wait_start(rsx, fb, tag, false, xcd);
    if (lane == 0) *(u32x4*)(lds + LR_CTL + 32) = st;
  }
  LBAR();
  st = *(const u32x4*)(lds + LR_CTL + 32);
  const int flip = __builtin_amdgcn_readfirstlane(st[2]) & 8192, cls = __builtin_amdgcn_readfirstlane(st[2]) & 16384;
  o1c ^= cls; o3c ^= cls; sto ^= cls; kc[4] ^= cls; kc[8] ^= cls; kc[9] ^= cls; kc[10] ^= cls; kc[11] ^= cls;
  { const u32 now = rt32(); if (st[0] - now + 100000u > 200000u) st[0] = now; }
  u32x16 kd = {(u32)((ld01 + XR2(vcu ^ 1)) ^ cls), (u32)((ld01 + XR2(vcu ^ 2)) ^ cls), (u32)((ld01 + XR2(vcu ^ 3)) ^ cls), (u32)(X4B && hk ? OOR : sto), (u32)avr, (u32)(avr + 4096), (u32)(avr + 8192),
               (u32)(avr + 12288), (u32)avw, (u32)(X4C && hk ? OOR : st1 ^ cls), (u32)((lane & 1) * 4096), (u32)(XSB(lane + 1) + flip), (u32)(lane * 8), (u32)avf,
               (u32)(b == 0 && lane == 0 ? XSYN + flip : OOR), (u32)(w == 0 ? pz1 ^ cls : (w == 1 ? kc[10] : kc[4]))};
  u32x16 sa = {(u32)flip, (u32)(XSB(1) + flip), 0u, (u32)fok, (u32)fov, (u32)(FO_KM + (head * 4 + ks) * 512),
               (u32)(FO_VM + (head * 8 + 2 * ks) * 256), st[0], 0u, 0u, (u32)SYO(1), 0u, 0u, (u32)TRY_CAP, 0u, 0u};
  u32x8 sc = {(u32)o1c, (u32)o3c, SENT, 0u, 0u, 0u, st[1], (u32)pgo};
#define TUNEO
#define KCNT
#define KHST
#define KTRC
#define TRCI
#define PREP 16
#define GAP 12
#define RETA(n, L) "s_cmp_eq_u32 s49, " #n "\n\t" "s_cmov_b32 s48, .Lpc%=-" L "\n\t"
  u32x32 keep;
  u32x2 ov;
  asm volatile("" : "={v[60:91]}"(keep));
  while (true) {
    sa[14] = xp;
#pragma unroll
    for (int i = 0; i < 16; ++i) sa[i] = __builtin_amdgcn_readfirstlane(sa[i]);
#pragma unroll
    for (int i = 0; i < 8; ++i) sc[i] = __builtin_amdgcn_readfirstlane(sc[i]);
    asm volatile(
        "s_nop 4\n\t" "s_mov_b32 s60, m0\n\t"
        "s_mov_b32 m0, %[mb]\n\t" "s_mov_b32 s64, s50\n\t" "s_mov_b32 s98, s49\n\t" "s_mov_b32 s68, 0\n\t"
        EXTRA R2BASE
        "s_cmp_lg_u32 %[wv], 3\n\t" "s_cbranch_scc1 .LN%=\n\t"
        "s_mov_b32 s78, 0xff00ff00\n\t" "s_mov_b32 s79, 0xff00ff00\n\t" "s_mov_b32 s62, 0x3e0293ee\n\t"
        "v_mov_b32 v52, 0x3f803f80\n\t" "v_mov_b32 v53, 0x3f803f80\n\t" "v_mov_b32 v54, 0x3f803f80\n\t" "v_mov_b32 v55, 0x3f803f80\n\t" "v_mov_b32 v51, 0\n\t" "v_mov_b32 v59, 0\n\t"
        "v_mov_b32 v60, %[tg]\n\t" "v_mov_b32 v61, -1\n\t" "s_mov_b32 s63, %[dm]\n\t"
        "s_branch .Lent%=\n"
        ".LA%=:\n\t"
        CLKW(".Lta%=", "0") REL(".Lua%=")
        "s_add_u32 s50, s36, s52\n\t" NOP(PDA)
        ABODY(NOP(PCL) LASTA(".Lwa%="), WVM(5) "v_readlane_b32 s50, v60, 0\n\t" "v_readlane_b32 s51, v60, 1\n\t"
              "s_sub_u32 s50, s50, %[tg]\n\t" "s_sub_u32 s51, s51, %[tg]\n\t" "s_min_u32 s63, s50, s51\n\t" "s_max_u32 s50, s50, s51\n\t" "s_cmp_le_u32 s50, 80\n\t"
              "s_cselect_b32 s50, s63, 0\n\t" "v_readfirstlane_b32 s49, v61\n\t" "s_max_i32 s64, s64, s50\n\t"
              "s_cmp_lg_u32 s49, -1\n\t" "s_cmov_b32 s58, s49\n\t" EXTRA "v_mov_b32 v62, s58\n\t" "ds_write_b32 v38, v62 offset:24\n\t", ".Lfail%=", 0) TSA(3)
        QKRED(BARA) ORD(88, 89) ORD(90, 91) QKPUB TSA(6) TSB(7) REDO(".Lqk%=")
        SVP(0, 0) SHRD SVP(1, 0) SVP(0, 1) SVP(1, 1) SVP(0, 2) SVP(1, 2) WLG(0) "v_readfirstlane_b32 s49, v62\n\t" SVP(0, 3) SVP(1, 3) "s_max_u32 s44, s44, s49\n\t" "s_cmp_eq_u32 s45, 63\n\t" TSA(7)
        "s_cbranch_scc1 .Llast%=\n"
        ".LB%=:\n\t"
        CLKW(".Ltb%=", "s66")
        "s_mov_b32 s47, 1\n\t"
        R2TEXT
        ".LC%=:\n\t"
        ZALL
        "s_mov_b32 s47, 5\n\t" "s_sub_i32 s63, s64, s45\n\t" "s_cmp_lt_i32 s63, 2\n\t" "s_cbranch_scc1 .Lend%=\n\t"
        "s_cmp_lt_i32 s63, " STR(XTH) "\n\t" "s_cselect_b32 s63, s59, %[dm]\n\t"
        CLKW(".Ltc%=", "s67") REL(".Luc%=")
        "s_add_u32 s50, s36, s53\n\t" NOP(PDC)
        CISSUE(LASTC(".Lwc%="), "")
        "buffer_load_dword v61, off, %[rs], s46 sc1\n\t" "s_add_u32 s46, s46, 64\n\t"
        "s_add_u32 s49, s39, 4096\n\t" NOP(PDF)
        "buffer_load_dwordx4 a[208:211], v44, %[rf], s39 offen sc1\n\t" "buffer_load_dwordx4 a[212:215], v44, %[rf], s49 offen sc1\n\t"
        "buffer_load_dwordx2 a[192:193], v44, %[rf], s40 offen sc1\n\t" "buffer_load_dwordx2 a[196:197], v44, %[rf], s40 offen offset:8 sc1\n\t"
        "buffer_load_dwordx2 a[200:201], v44, %[rf], s40 offen offset:1024 sc1\n\t" "buffer_load_dwordx2 a[204:205], v44, %[rf], s40 offen offset:1032 sc1\n\t"
#define NXC 8
        "buffer_load_dword v60, v18, %[rs], s63 offen sc1\n\t"
        CBODY(NXC, "", NXC, ".Lfail%=")
        MRED(BARC, "") ORD(84, 85) ORD(86, 87) STC TSW(6) REDO(".Lmr%=")
        SHRD SHUP TSW(7)
        NEXTP
        CWAIT(".Lqk%=", 8, "s48") QKRED("") NOP(1) QKPUB "s_branch .Lqk%=r\n"
        CWAIT(".Lmr%=", 16, "s38") WVM(0) MRED("", "") "v_min_u32 v80, -2, v80\n\t" "v_min_u32 v81, -2, v81\n\t" STC
        "s_branch .Lmr%=r\n"
        ".Llast%=:\n\t"
        "buffer_store_dwordx4 v[72:75], v47, %[rf], s41 offen sc1\n\t" "s_mov_b32 s47, 3\n\t" NOP(4) VPOST ARRIVE(12) "s_barrier\n\t"
        "s_branch .Lend%=\n"
        R2MAX
        ".Lnorm%=:\n\t"
        "s_cmp_lg_u32 %[wv], 3\n\t" "s_cbranch_scc1 .LNA%=\n\t" "s_cmp_eq_u32 s47, 0\n\t" "s_cbranch_scc1 .LA%=\n\t" "s_branch .LC%=\n"
        ".LN%=:\n\t"
        "v_mov_b64 v[92:93], -1\n\t" "v_mov_b64 v[94:95], -1\n\t" "s_mov_b32 s65, s44\n\t" "s_mov_b32 s78, -100\n\t" "s_mov_b32 s79, 4\n\t"
        "s_branch .Lent%=\n"
        ".LNA%=:\n\t" PZA0
        "s_add_u32 s50, s36, s52\n\t"
        RELN(".Lna%=")
        ABODY(NOP(PCA), LASTA(".Lxa%=") PZA, ".Lfail%=", PZNA) BARA ARRIVE(8) TSA(3)
        SV(0, 0) SV(1, 0) SV(0, 1) SV(1, 1) SV(0, 2) SV(1, 2) SV(0, 3) SV(1, 3) ZER(64) ZER(68) ZER(72) ZER(76)
        VPOST TSA(7) ZER(80) ZER(84) ZER(24) ZER(28)
        "v_add_u32 v88, 4096, v21\n\t" "v_add_u32 v89, 8192, v21\n\t" "v_add_u32 v90, 12288, v21\n\t"
        PZB0
        "s_lshl_b32 s61, s45, 7\n\t" "s_add_u32 s61, s61, " STR(PGO_R - XSYN) "\n\t" "s_cmp_lg_u32 %[wv], 0\n\t"
        WLG(0) "s_barrier\n\t" PZB
        PSW(92, 0) PSW(92, 272) PSW(92, 640) PSW(92, 912) PSW(92, 1280) PSW(92, 1552) PSW(92, 1920) PSW(92, 2192) PSW(92, 3840)
        "ds_read2_b32 v[50:51], v38 offset0:0 offset1:6\n\t"
        "s_cbranch_scc1 .LNB%=\n\t"
        "s_memrealtime s[72:73]\n"
        ".Lv0%=:\n\t"
        "ds_read_b32 v62, v38 offset:" CNT(12) "\n\t"
        "ds_read2_b32 v[112:113], v21 offset0:0 offset1:68\n\t" "ds_read2_b32 v[114:115], v21 offset0:16 offset1:84\n\t"
        "ds_read2_b32 v[116:117], v88 offset0:0 offset1:68\n\t" "ds_read2_b32 v[118:119], v88 offset0:16 offset1:84\n\t"
        "ds_read2_b32 v[120:121], v89 offset0:0 offset1:68\n\t" "ds_read2_b32 v[122:123], v89 offset0:16 offset1:84\n\t"
        "ds_read2_b32 v[124:125], v90 offset0:0 offset1:68\n\t" "ds_read2_b32 v[126:127], v90 offset0:16 offset1:84\n\t"
        WLG(0) "v_cmp_gt_u32 vcc, s48, v62\n\t" "s_cbranch_vccnz .Lv0%=\n\t"
        "s_add_u32 s50, s72, " STR(TVM) "\n\t" "s_lshl_b32 s50, s50, 17\n\t" "s_lshr_b32 s50, s50, 1\n\t"
        PKA(112, 112, 116) PKA(114, 114, 118) PKA(120, 120, 124) PKA(122, 122, 126) PKA(112, 112, 120) PKA(114, 114, 122)
        "v_lshrrev_b32 v91, 20, v51\n\t" "v_readfirstlane_b32 s49, v50\n\t"
        "v_cvt_pk_bf16_f32 v112, v112, v113\n\t" "v_cvt_pk_bf16_f32 v113, v114, v115\n\t" "v_or_b32 v114, s50, v91\n\t"
        "s_cmp_eq_u32 s45, 63\n\t" "s_cbranch_scc1 .Lv2%=\n\t"
        "s_sleep " STR(SLV) "\n"
        ".Lv2%=:\n\t"
        "buffer_store_dwordx2 v[112:113], v20, %[rf], s42 offen sc1\n\t"
        "buffer_store_dword v114, v22, %[rs], s61 offen sc1\n\t"
        "s_cmp_eq_u32 s49, s65\n\t" "s_cbranch_scc1 .LNB%=\n\t"
        "s_mov_b32 s65, s49\n\t" KHST "s_sub_u32 s49, s45, s78\n\t" "s_mov_b32 s78, s45\n\t"
        "s_cmp_gt_i32 s49, " STR(GAP) "\n\t" "s_cbranch_scc1 .LNB%=\n\t" "s_cmp_lt_u32 s45, s79\n\t" "s_cbranch_scc1 .LNB%=\n\t"
        "s_add_u32 s79, s45, 3\n\t" "s_lshl_b32 s49, s45, 6\n\t" "v_add_u32 v50, " STR(TLB + ((TLB + TLC) << 10) + ((TLB + TLC + TLP) << 20)) ", v51\n\t"
        "buffer_store_dword v50, v22, %[rs], s49 offen offset:128 sc1\n"
        ".LNB%=:\n\t"
        WLG(0) "v_readfirstlane_b32 s58, v51\n\t" EXTRA
        "s_mov_b32 s47, 3\n\t"
        "s_cmp_eq_u32 s45, 63\n\t"
        "s_cbranch_scc1 .Lend%=\n"
        ".LNC%=:\n\t" PZC0
        "s_add_u32 s50, s36, s53\n\t"
        WLG(0) RELN(".Lnc%=")
        CISSUE("", LASTC(".Lxc%="))
        CBODY(0, PZC, PZNC, ".Lfail%=") BARC
        ARRIVE(16) TSW(4)
        NEXTN
        ".Lfail%=:\n\t" KCNT
        "s_cmp_lg_u32 s47, 2\n\t" "s_cbranch_scc1 .Lnu%=\n\t"
        "s_mov_b32 s37, s36\n\t" "s_mov_b32 s36, s48\n\t" "s_sub_u32 s45, s45, 1\n\t" "s_sub_u32 s38, s38, 4\n\t" "s_sub_u32 s39, s39, " STR(EXF) "\n\t" "s_sub_u32 s40, s40, " STR(EXF) "\n\t"
        "s_sub_u32 s41, s41, " STR(EXK) "\n\t" "s_sub_u32 s42, s42, " STR(EXK) "\n\t" "s_sub_u32 s43, s43, s55\n\t" "s_sub_u32 s43, s43, s70\n\t" "s_sub_u32 s46, s46, 64\n"
        ".Lnu%=:\n\t"
        "s_mov_b32 s51, 1\n\t" "s_nop 7\n\t" WVM(0) "s_sub_u32 s98, s98, 1\n\t" "s_cbranch_scc1 .Lcap%=\n"
        ".Lent%=:\n\t"
        "s_cmp_eq_u32 s47, 1\n\t" "s_cselect_b32 s99, s66, s67\n\t" "s_cbranch_scc1 .Lnz%=\n\t" ZALL ".Lnz%=:\n\t"
        "s_cmp_eq_u32 s47, 0\n\t" "s_cselect_b32 s99, 0, s99\n\t" "s_cselect_b32 s50, s52, s53\n\t" "s_add_u32 s99, s99, s43\n\t" "s_add_u32 s50, s50, s36\n\t"
        SHRD "s_memrealtime s[72:73]\n\t"
        "s_cmp_eq_u32 %[wv], 3\n\t" "s_cselect_b32 s49, 0, 3\n\t" "s_add_u32 s49, s49, s47\n\t" "s_cmp_lg_u32 s51, 0\n\t" "s_cselect_b32 s49, s49, 7\n\t" "s_cselect_b64 vcc, -1, 0\n\t"
        "s_cselect_b32 s51, 0, 14\n\t" "s_mov_b32 s48, .Lpc%=-.Lnorm%=\n\t"
        RETA(0, ".Lua%=") RETA(1, ".Ltb%=y") RETA(2, ".Luc%=") RETA(3, ".Lna%=") RETA(5, ".Lnc%=")
        "s_getpc_b64 s[96:97]\n" ".Lpc%=:\n\t" "s_sub_u32 s96, s96, s48\n\t" "s_subb_u32 s97, s97, 0\n\t"
        SHUP "s_sub_u32 s49, s99, s72\n\t" "s_add_u32 s49, s49, s44\n"
        ".Lbump%=:\n\t"
        "s_cmp_ge_i32 s49, " STR(PREP) "\n\t" "s_cbranch_scc1 .Lok%=\n\t" "s_add_u32 s44, s44, %[tr]\n\t" "s_add_u32 s49, s49, %[tr]\n\t" "s_branch .Lbump%=\n"
        ".Lok%=:\n\t"
        "s_mov_b64 exec, 1\n\t" "v_mov_b32 v62, s44\n\t" "ds_max_u32 v38, v62\n\t" KTRC "s_mov_b64 exec, -1\n\t"
        "s_add_u32 s99, s99, s44\n\t" "s_sub_u32 s99, s99, s51\n\t"
        WLG(0) "s_memrealtime s[72:73]\n\t" "s_nop 3\n\t" "s_memrealtime s[74:75]\n\t" "s_nop 3\n"
        CLKL(".Lth%=", "s99")
        "s_setpc_b64 s[96:97]\n"
        ".Lcap%=:\n\t" "s_add_u32 s47, s47, 8\n"
        ".Lend%=:\n\t"
        "s_waitcnt vmcnt(0) lgkmcnt(0)\n\t"
        "s_mov_b32 s50, s64\n\t" "s_mov_b32 s49, s98\n\t" "s_mov_b32 s51, s68\n\t" "s_mov_b32 m0, s60"
        : "+{s[36:51]}"(sa), "+{s[52:59]}"(sc), "+{v[60:91]}"(keep), "=&{v[116:117]}"(ov), "+{a[192:207]}"(fv), "+{a[208:215]}"(fk), "+{v[8:23]}"(kd) FINEO TUNEO
        : "{v[32:47]}"(kc), [rs] "s"(rsx), [rf] "s"(rsf), [dm] "s"(__builtin_amdgcn_readfirstlane(PGO_Y + b * 128)), [tg] "s"(__builtin_amdgcn_readfirstlane(tag)), [r2] "s"(__builtin_amdgcn_readfirstlane(XR2B + flip)), [mb] "s"(__builtin_amdgcn_readfirstlane(ldsb + w * 4096)), [wv] "s"(__builtin_amdgcn_readfirstlane(w)), [tr] "s"(TR) TRCI,
          "{a[0:31]}"(wa[0]), "{a[32:63]}"(wa[1]), "{a[64:95]}"(wa[2]), "{a[96:127]}"(wa[3]), "{a[128:159]}"(wa[4]), "{a[160:191]}"(wa[5]),
          "{v[128:159]}"(wo[0]), "{v[160:191]}"(wo[1]), "{v[192:223]}"(wo[2]), "{v[224:255]}"(wo[3])
        : CL8(24, 25, 26, 27, 28, 29, 30, 31), "v48", "v49", "v50", "v51", CL8(52, 53, 54, 55, 56, 57, 58, 59), "v92", "v93", "v94", "v95", CL96, "v112", "v113", "v114", "v115", "v118", "v119", CL120,
          "s60", "s61", "s62", "s63", "s64", "s65", "s66", "s67", "s68", "s69", "s70", "s71", "s72", "s73", "s74", "s75", "s76", "s77", "s78", "s79", "s96", "s97", "s98", "s99", "vcc", "scc", "memory");
    const int code = __builtin_amdgcn_readfirstlane(sa[11]);
    xp = __builtin_amdgcn_readfirstlane(sa[14]);
    tcnt += sa[15];
    sa[15] = 0;
    if (code == 3) break;
    const int seg = __builtin_amdgcn_readfirstlane(sa[9]);
    if (code == 5) { XWAIT(seg + 2) xp = max(xp, seg + 2); sa[11] = 2; }
    else if (code == 1) { __builtin_amdgcn_raw_buffer_store_b64((u32x2){nosent(ov[0]), nosent(ov[1])}, rsx, sto, sa[0], 16); sa[11] = 2; }
    else { sc[2] += 1; sa[13] = 0; sa[11] = code - 8; sa[15] = 1; }
  }
  LBAR();
  if (tid == 0) __builtin_amdgcn_raw_buffer_store_b32(1u, rsx, 0, PGO_D + vcu * 128, 16);
  if (b == 0 && w == 1) {
    for (int it = 0; it < SPIN_CAP; ++it) {
      asm volatile("" ::: "memory");
      if (__ballot(__builtin_amdgcn_raw_buffer_load_b32(rsx, lane * 128, PGO_S, 16) != tag || __builtin_amdgcn_raw_buffer_load_b32(rsx, lane * 128 + 8192, PGO_S, 16) != tag) == 0) break;
    }
    if (lane == 0) __builtin_amdgcn_raw_buffer_store_b32(tag, rsx, 0, XEPO, 16);
  }
}

#define XSTAT 3
#define XVM 8
#define XP0 -1
#define XSC " sc1"
#define XPITCH 1056
#define XSLOT (32 * XPITCH)
#define XW(HB, t) "a[" STR(HB) "+8*xk+4*" #t ":" STR(HB) "+8*xk+4*" #t "+3]"
#define XF(rt) "v[96+8*(xk&3)+4*" #rt ":96+8*(xk&3)+4*" #rt "+3]"
#define XFN(rt) "v[96+8*((xk+3)&3)+4*" #rt ":96+8*((xk+3)&3)+4*" #rt "+3]"
#define XA(AC, o) "v[" STR(AC) "+" #o ":" STR(AC) "+" #o "+3]"
#define XM1(d, a, b, FIRST) ".if (xk == 0) && " STR(FIRST) "\n\tv_mfma_f32_16x16x32_bf16 " d ", " a ", " b ", 0\n\t.else\n\tv_mfma_f32_16x16x32_bf16 " d ", " a ", " b ", " d "\n\t.endif\n\t"
#define XMF_QK(i, HB, AC, F) XMF_QK##i(HB, AC, F)
#define XMF_QK0(HB, AC, F) XM1(XA(AC, 0), XW(HB, 0), XF(0), F)
#define XMF_QK1(HB, AC, F) XM1(XA(AC, 4), XW(HB, 1), XF(0), F)
#define XMF_QK2(HB, AC, F) XM1(XA(AC, 8), XW(HB, 0), XF(1), F)
#define XMF_QK3(HB, AC, F) XM1(XA(AC, 12), XW(HB, 1), XF(1), F)
#define XMF_V(i, HB, AC, F) XMF_V##i(HB, AC, F)
#define XMF_V0(HB, AC, F) XM1(XA(AC, 0), XF(0), XW(HB, 0), F)
#define XMF_V1(HB, AC, F) XM1(XA(AC, 8), XF(0), XW(HB, 1), F)
#define XMF_V2(HB, AC, F) XM1(XA(AC, 4), XF(1), XW(HB, 0), F)
#define XMF_V3(HB, AC, F) XM1(XA(AC, 12), XF(1), XW(HB, 1), F)
#define XRD(rt, CUR, NXT) ".if xk < 13\n\tds_read_b128 " XFN(rt) ", " CUR " offset:64*(xk+3)+" STR(rt * 16 * XPITCH) "\n\t.else\n\t" \
                          "ds_read_b128 " XFN(rt) ", " NXT " offset:64*(xk-13)+" STR(rt * 16 * XPITCH) "\n\t.endif\n\t"
#define XCV(EPI, PV, i) ".if " STR(EPI) " && (xk >= 1) && (xk < 3)\n\tv_cvt_pk_bf16_f32 v[128+4*(xk-1)+" #i "], v[" STR(PV) "+8*(xk-1)+2*" #i "], v[" STR(PV) "+8*(xk-1)+2*" #i "+1]\n\t.endif\n\t"
#define XHALF(MF, HB, AC, PV, EPI, CUR, NXT, DSLOT, GOFF)                                                   \
  ".set xk, 0\n\t"                                                                                          \
  ".rept 16\n\t"                                                                                            \
  "s_waitcnt lgkmcnt(4)\n\t"                                                                                \
  MF(0, HB, AC, EPI) XRD(0, CUR, NXT) XCV(EPI, PV, 0)                                                       \
  ".if (xk >= 5) && (xk < 13)\n\t"                                                                          \
  "s_add_u32 m0, m0, " STR(XPITCH) "\n\t"                                                                   \
  ".endif\n\t"                                                                                              \
  MF(1, HB, AC, EPI) XRD(1, CUR, NXT) XCV(EPI, PV, 1)                                                       \
  ".if (xk >= 5) && (xk < 13)\n\t"                                                                          \
  "s_add_u32 %[dg], %[dg], 2048\n\t"                                                                        \
  ".endif\n\t"                                                                                              \
  MF(2, HB, AC, EPI) XCV(EPI, PV, 2)                                                                        \
  ".if (xk >= 4) && (xk < 12)\n\t"                                                                          \
  "buffer_load_dwordx4 %[l16], %[rx], %[dg] offen lds\n\t"                                                  \
  ".endif\n\t"                                                                                              \
  ".if xk == 0\n\t"                                                                                         \
  "s_add_u32 m0, %[dl], " STR((DSLOT) * XSLOT) "\n\t" "s_add_u32 %[dg], %[gb], " STR(GOFF) "\n\t"           \
  ".endif\n\t"                                                                                              \
  ".if " STR(EPI) " && (xk == " STR(XSTAT) ")\n\t"                                                                       \
  "buffer_store_dwordx4 v[128:131], v137, %[rf], %[sg] offen" XSC "\n\t"                                       \
  ".endif\n\t"                                                                                              \
  ".if " STR(EPI) " && (xk == 13)\n\t"                                                                      \
  "v_mov_b32 v137, %[l16]\n\t" "v_mov_b32 v138, %[lp2]\n\t"                                                 \
  ".endif\n\t"                                                                                              \
  ".if " STR(EPI) " && (xk == 14)\n\t"                                                                      \
  "s_add_u32 %[sg], %[sg], " STR(NH * 8 * 1024) "\n\t"                                                      \
  "v_add_u32 v136, 1, v136\n\t"                                                                             \
  ".endif\n\t"                                                                                              \
  ".if !" STR(EPI) " && (xk == 1)\n\t"                                                                      \
  "buffer_store_dword v136, %[fo], %[rp], 0 offen sc1\n\t"                                                  \
  ".endif\n\t"                                                                                              \
  MF(3, HB, AC, EPI) XCV(EPI, PV, 3)                                                                        \
  ".if xk == 0\n\t"                                                                                         \
  "s_waitcnt vmcnt(" STR(XVM) ")\n\t" "s_barrier\n\t"                                                               \
  ".endif\n\t"                                                                                              \
  ".if xk == 2\n\t"                                                                                         \
  "s_min_u32 %[dg], %[dg], " STR(S_LEN * DM * 2 - 16384 + 1024) "\n\t"                                      \
  ".endif\n\t"                                                                                              \
  ".if " STR(EPI) " && (xk == " STR(XSTAT) ")\n\t"                                                                       \
  "buffer_store_dwordx4 v[132:135], v138, %[rf], %[sg] offen" XSC "\n\t"                                       \
  ".endif\n\t"                                                                                              \
  ".set xk, xk+1\n\t"                                                                                       \
  ".endr\n\t"
#define XLOOP(MF)                                                                                           \
  "s_mov_b32 %[kp], m0\n\t"                                                                                 \
  "v_mov_b32 v136, %[p0]\n\t" "v_mov_b32 v137, " STR(OOR) "\n\t" "v_mov_b32 v138, " STR(OOR) "\n\t"                                                                                \
  "ds_read_b128 v[96:99], %[r0]\n\tds_read_b128 v[100:103], %[r0] offset:" STR(16 * XPITCH) "\n\t"          \
  "ds_read_b128 v[104:107], %[r0] offset:64\n\tds_read_b128 v[108:111], %[r0] offset:64+" STR(16 * XPITCH) "\n\t"   \
  "ds_read_b128 v[112:115], %[r0] offset:128\n\tds_read_b128 v[116:119], %[r0] offset:128+" STR(16 * XPITCH) "\n"   \
  ".Lx%=:\n\t"                                                                                              \
  XHALF(MF, 0, 64, 80, 1, "%[r0]", "%[r1]", 3, 66560) XHALF(MF, 128, 64, 80, 0, "%[r1]", "%[r2]", 0, 131072) \
  XHALF(MF, 0, 80, 64, 1, "%[r2]", "%[r3]", 1, 132096) XHALF(MF, 128, 80, 64, 0, "%[r3]", "%[r0]", 2, 196608) \
  "s_add_u32 %[gb], %[gb], 131072\n\t"                                                                      \
  "s_sub_u32 %[cnt], %[cnt], 1\n\t"                                                                         \
  "s_cmp_lg_u32 %[cnt], 0\n\t"                                                                              \
  "s_cbranch_scc1 .Lx%=\n\t"                                                                                \
  "s_mov_b32 m0, %[kp]\n\t"                                                                                 \
  "s_nop 7\n\t" "s_waitcnt lgkmcnt(0)"

template <bool ISV>
__device__ __forceinline__ void x_role(unsigned char* lds, const u16* __restrict__ x, const u16* __restrict__ Wm, int fo0, int head,
                                       __amdgpu_buffer_rsrc_t rsf, u32* prog, u32 tag, int lane, int w) {
  const int ldsb = (int)(size_t)(LAS unsigned char*)lds;
  const __amdgpu_buffer_rsrc_t rsxin = __builtin_amdgcn_make_buffer_rsrc((void*)x, 0, S_LEN * DM * 2, 0x00020000);
  const __amdgpu_buffer_rsrc_t rsp = __builtin_amdgcn_make_buffer_rsrc((void*)prog, 0, 128, 0x00020000);
  const int lr = lane & 15, lg = lane >> 4;
  const int xrd = ldsb + lr * XPITCH + lg * 16;
  const int fob = fo0 + (ISV ? (head * 8 + 2 * w) : (head * 8 + w)) * 1024;
  const int p2 = ISV ? 1024 : 4096;
  u32x32 xw[8];
  {
    const __amdgpu_buffer_rsrc_t rw = __builtin_amdgcn_make_buffer_rsrc((void*)Wm, 0, DM * HD * 2, 0x00020000);
    const int vq = (lane >> 4) * (HD * 2) + head * 256 + (lane & 15) * 16;
#define XWISSUE(c) _Pragma("unroll") for (int j = 0; j < 8; ++j)                                                       \
      asm volatile("s_mov_b32 m0, %0\n\ts_nop 3\n\tbuffer_load_dwordx4 %1, %2, %3 offen lds"                           \
                   :: "s"(ldsb + ((c) & 3) * XSLOT + (8 * w + j) * XPITCH), "v"(vq), "s"(rw), "s"((128 * (c) + 32 * w + 4 * j) * (HD * 2)) : "memory");
#define XXISSUE(h) _Pragma("unroll") for (int j = 0; j < 8; ++j)                                                       \
      asm volatile("s_mov_b32 m0, %0\n\ts_nop 3\n\tbuffer_load_dwordx4 %1, %2, %3 offen lds"                           \
                   :: "s"(ldsb + (h) * XSLOT + (8 * w + j) * XPITCH), "v"(lane * 16), "s"(rsxin), "s"((((h) >> 1) * 32 + 8 * w + j) * 2048 + ((h) & 1) * 1024) : "memory");
    XWISSUE(0) XWISSUE(1) XWISSUE(2) XWISSUE(3)
    const int q = lr >> 2, p = lr & 3;
#pragma unroll
    for (int c = 0; c < 8; ++c) {
      asm volatile("s_waitcnt vmcnt(24)\n\ts_barrier" ::: "memory");
#pragma unroll
      for (int s = 0; s < 4; ++s)
#pragma unroll
        for (int t = 0; t < 2; ++t) {
          const unsigned char* base = lds + (c & 3) * XSLOT + (8 * s + 2 * lg) * XPITCH + q * 256 + w * 64 + p * 16 + t * 8;
          const u32x2 t0 = __builtin_bit_cast(u32x2, __builtin_amdgcn_ds_read_tr16_b64_v4i16((LAS v4i16_t*)(base)));
          const u32x2 t1 = __builtin_bit_cast(u32x2, __builtin_amdgcn_ds_read_tr16_b64_v4i16((LAS v4i16_t*)(base + XPITCH)));
          const int o = ((4 * c + s) * 2 + t) * 4;
          xw[o >> 5][(o & 31) + 0] = t0[0]; xw[o >> 5][(o & 31) + 1] = t0[1]; xw[o >> 5][(o & 31) + 2] = t1[0]; xw[o >> 5][(o & 31) + 3] = t1[1];
        }
      LBAR();
      if (c < 4) { XWISSUE(c + 4) } else if (c < 7) { XXISSUE(c - 4) }
    }
  }
  asm volatile("s_waitcnt vmcnt(8)\n\ts_barrier" ::: "memory");
  u32x16 xacc;
  {
    u32 kp, dg; int gb = 8 * w * 2048, cnt = NSEG / 2, sg = fob - NH * 8 * 1024;
#define XASM(MF)                                                                                            \
    asm volatile(XLOOP(MF)                                                                                  \
                 : "=&{v[80:95]}"(xacc), [kp] "=&s"(kp), [dg] "=&s"(dg), [gb] "+s"(gb), [cnt] "+s"(cnt), [sg] "+s"(sg) \
                 : [r0] "v"(xrd), [r1] "v"(xrd + XSLOT), [r2] "v"(xrd + 2 * XSLOT), [r3] "v"(xrd + 3 * XSLOT), [l16] "v"(lane * 16), [lp2] "v"(lane * 16 + p2), \
                   [rx] "s"(rsxin), [rf] "s"(rsf), [rp] "s"(rsp), [fo] "v"(lane == 0 && w == 0 ? 0 : OOR), [dl] "s"(ldsb + 8 * w * XPITCH), [p0] "s"(tag + XP0), \
                   "{a[0:31]}"(xw[0]), "{a[32:63]}"(xw[1]), "{a[64:95]}"(xw[2]), "{a[96:127]}"(xw[3]),      \
                   "{a[128:159]}"(xw[4]), "{a[160:191]}"(xw[5]), "{a[192:223]}"(xw[6]), "{a[224:255]}"(xw[7]) \
                 : CL64, CL72, CL96, CL112, CL120, CL8(128, 129, 130, 131, 132, 133, 134, 135), "v136", "v137", "v138", "scc", "memory");
    if (ISV) { XASM(XMF_V) } else { XASM(XMF_QK) }
  }
#pragma unroll
  for (int jj = 0; jj < 2; ++jj) {
    u32x4 pk;
#pragma unroll
    for (int e = 0; e < 4; ++e) pk[e] = pack2(__uint_as_float(xacc[8 * jj + 2 * e]), __uint_as_float(xacc[8 * jj + 2 * e + 1]));
    __builtin_amdgcn_raw_buffer_store_b128(pk, rsf, lane * 16 + p2 * jj, fob + (NSEG - 1) * (NH * 8 * 1024), 16);
  }
  asm volatile("s_waitcnt vmcnt(0)\n\ts_barrier" ::: "memory");
  if (lane == 0 && w == 0) __hip_atomic_store(prog, tag + NSEG + 8, RLX_AGENT);
}

#define SU_CP 4288
#define SU_U 0
#define SU_K (2 * SU_CP)
#define SU_Y (SU_K + 10400)
#define SU_CH (SU_Y + 4096)
#define SST(i)
__device__ __forceinline__ int koff(int i) {
  const int x = i & 127;
  return i < 256 ? (32 * (i >> 7) + 16 * ((i >> 2) & 1) + (x >> 3)) * 16 + (i & 3) * 4
       : i < 384 ? 1024 + x * 4
       : i < 512 ? 1024 + (32 + 16 * (x >> 6) + ((x >> 1) & 15)) * 16 + (2 * ((x >> 5) & 1) + (x & 1)) * 4
                 : 2048 + (16 * ((i - 512) >> 6) + (i & 15)) * 16 + ((i >> 4) & 3) * 4;
}
struct SsmIn { u32x4 ka, kb; u32x2 kc; u32 kd, ke[4]; u32x4 uv[4]; u32x2 up, dsk; };
#define SSM_GOT(in) asm volatile("" : "+v"(in.ka), "+v"(in.kb), "+v"(in.kc), "+v"(in.kd), "+v"(in.ke[0]), "+v"(in.ke[1]), "+v"(in.ke[2]), "+v"(in.ke[3]), \
                                      "+v"(in.uv[0]), "+v"(in.uv[1]), "+v"(in.uv[2]), "+v"(in.uv[3]), "+v"(in.up), "+v"(in.dsk))
__device__ __forceinline__ void ssm_load(SsmIn& in, int d0, __amdgpu_buffer_rsrc_t rst, __amdgpu_buffer_rsrc_t rsd, int tid, int lane, int w) {
  const int so = 2 * SSM_L * SSM_D * 2 + (d0 + w) * 3072, ia = lane < 32 ? 8 * lane : 128 + 4 * lane;
  in.ka = __builtin_amdgcn_raw_buffer_load_b128(rst, koff(ia), so, 16);
  in.kb = __builtin_amdgcn_raw_buffer_load_b128(rst, koff(ia + 4), so, 16);
  in.kc = __builtin_amdgcn_raw_buffer_load_b64(rst, koff(384 + 2 * lane), so, 16);
  in.kd = __builtin_amdgcn_raw_buffer_load_b32(rst, koff(386 + 2 * lane), so, 16);
#pragma unroll
  for (int i = 0; i < 4; ++i) in.ke[i] = __builtin_amdgcn_raw_buffer_load_b32(rst, koff(512 + 64 * (i >> 1) + (i & 1) + lane), so, 16);
#pragma unroll
  for (int i = 0; i < 4; ++i) in.uv[i] = __builtin_amdgcn_raw_buffer_load_b128(rst, tid * 16, d0 * 4096 + i * 4096, 16);
  in.up = __builtin_amdgcn_raw_buffer_load_b64(rst, tid ? tid * 16 - 8 : OOR, d0 * 4096 + 3 * 4096, 16);
  in.dsk = __builtin_amdgcn_raw_buffer_load_b64(rsd, 0, d0 * 2, 0);
}
__device__ __forceinline__ float reslo(float v, u32 h) { return v - __uint_as_float(h << 16); }
__device__ __forceinline__ float reshi(float v, u32 h) { return v - __uint_as_float(h & 0xffff0000u); }
__device__ __forceinline__ int ssm_unit(unsigned char* sm, int d0, SsmIn& in, bool pend, __amdgpu_buffer_rsrc_t rst, __amdgpu_buffer_rsrc_t rsx, int fb,
                                        __amdgpu_buffer_rsrc_t rsd, int tid, int lane, int w) {
  unsigned char* cb = sm + w * SU_CH;
  SST(0)
  const int one = tid == 0 ? 0 : OOR;
  u32 tk = __builtin_amdgcn_raw_ptr_buffer_atomic_add_i32(1, rsx, one, XTKT, 0);
  const int r = lane & 31, h = lane >> 5;
#pragma unroll
  for (int c = 0; c < 4; ++c) {
    const u32 sel = (c & 1) ? 0x07060302u : 0x05040100u;
    u32x4 e, o;
#pragma unroll
    for (int i = 0; i < 4; ++i) {
      e[i] = __builtin_amdgcn_perm(in.uv[i][2 + (c >> 1)], in.uv[i][c >> 1], sel);
      o[i] = __builtin_amdgcn_perm(in.uv[i][c >> 1], i ? in.uv[i ? i - 1 : 0][2 + (c >> 1)] : in.up[c >> 1], sel);
    }
    *(u32x4*)(sm + c * SU_CH + SU_U + 96 + tid * 16) = e;
    *(u32x4*)(sm + c * SU_CH + SU_U + SU_CP + 96 + tid * 16) = o;
  }
  SST(1)
  {
    unsigned char* kr = cb + SU_K + ((lane >> 2) + 1) * 80 + (3 - (lane & 3)) * 16;
    const f32x4 a = __builtin_bit_cast(f32x4, in.ka), b = __builtin_bit_cast(f32x4, in.kb);
    const bool dn = lane < 32;
    const float v[8] = {a[0], dn ? a[1] : a[0], dn ? a[2] : a[1], dn ? a[3] : a[2], dn ? b[0] : a[2], dn ? b[1] : a[2], dn ? b[2] : a[3], dn ? b[3] : b[0]};
    u32x4 hi, lo;
#pragma unroll
    for (int e = 0; e < 4; ++e) { hi[e] = pack2(v[7 - 2 * e], v[6 - 2 * e]); lo[e] = pack2(reslo(v[7 - 2 * e], hi[e]), reshi(v[6 - 2 * e], hi[e])); }
    *(u32x4*)(kr) = hi; *(u32x4*)(kr + 5200) = lo;
    {
      const float v0 = __uint_as_float(in.kc[0]), v1 = __uint_as_float(in.kc[1]), v2 = __uint_as_float(in.kd);
      hi = (u32x4){pack2(v2, v2), pack2(v1, v1), pack2(v1, v0), pack2(v0, v0)};
      const float r0 = reshi(v0, hi[3]), r1 = reshi(v1, hi[1]), r2 = reshi(v2, hi[0]);
      *(u32x4*)(kr + 1280) = hi; *(u32x4*)(kr + 1280 + 5200) = (u32x4){pack2(r2, r2), pack2(r1, r1), pack2(r1, r0), pack2(r0, r0)};
    }
#pragma unroll
    for (int j = 0; j < 2; ++j) {
      const float v0 = __uint_as_float(in.ke[2 * j]), v1 = __uint_as_float(in.ke[2 * j + 1]);
      const u32 h0 = pack2(v0, v0), h1 = pack2(v1, v1), sel = (lane & 1) ? 0x01000100u : 0x05040100u;
      const float r0 = reshi(v0, h0), r1 = reshi(v1, h1);
      const u32 l0 = pack2(r0, r0), l1 = pack2(r1, r1);
      *(u32x4*)(kr + 2560 + 1280 * j) = (u32x4){h1, __builtin_amdgcn_perm(h0, h1, sel), h0, h0};
      *(u32x4*)(kr + 2560 + 1280 * j + 5200) = (u32x4){l1, __builtin_amdgcn_perm(l0, l1, sel), l0, l0};
    }
  }
  SST(2)
  f32x4 sk[8];
  {
    const u32 d01 = __builtin_amdgcn_readfirstlane(in.dsk[0]), d23 = __builtin_amdgcn_readfirstlane(in.dsk[1]);
#pragma unroll
    for (int i = 0; i < 4; ++i)
      asm("v_dot2_f32_bf16 %0, %8, %12, 0\n\tv_dot2_f32_bf16 %1, %8, %13, 0\n\tv_dot2_f32_bf16 %2, %9, %14, 0\n\tv_dot2_f32_bf16 %3, %9, %15, 0\n\t"
          "v_dot2_f32_bf16 %4, %10, %12, 0\n\tv_dot2_f32_bf16 %5, %10, %13, 0\n\tv_dot2_f32_bf16 %6, %11, %14, 0\n\tv_dot2_f32_bf16 %7, %11, %15, 0\n\t"
          "v_cvt_pk_bf16_f32 %0, 0, %0\n\tv_cvt_pk_bf16_f32 %1, 0, %1\n\tv_cvt_pk_bf16_f32 %2, 0, %2\n\tv_cvt_pk_bf16_f32 %3, 0, %3\n\t"
          "v_cvt_pk_bf16_f32 %4, 0, %4\n\tv_cvt_pk_bf16_f32 %5, 0, %5\n\tv_cvt_pk_bf16_f32 %6, 0, %6\n\tv_cvt_pk_bf16_f32 %7, 0, %7"
          : "=&v"(sk[2 * i][0]), "=&v"(sk[2 * i][1]), "=&v"(sk[2 * i][2]), "=&v"(sk[2 * i][3]),
            "=&v"(sk[2 * i + 1][0]), "=&v"(sk[2 * i + 1][1]), "=&v"(sk[2 * i + 1][2]), "=&v"(sk[2 * i + 1][3])
          : "v"(in.uv[i][0]), "v"(in.uv[i][1]), "v"(in.uv[i][2]), "v"(in.uv[i][3]), "s"(d01 & 0xffffu), "s"(d01 & 0xffff0000u), "s"(d23 & 0xffffu), "s"(d23 & 0xffff0000u));
  }
  asm volatile("s_waitcnt vmcnt(0)" : "+v"(tk), "+v"(sk[0]), "+v"(sk[1]), "+v"(sk[2]), "+v"(sk[3]), "+v"(sk[4]), "+v"(sk[5]), "+v"(sk[6]), "+v"(sk[7]) :: "memory");
  if (tid == 0) *(u32*)(sm + 4 * SU_CH) = tk + 65u;
  LBAR();
  SST(3)
  if (pend) __builtin_amdgcn_raw_ptr_buffer_atomic_add_i32(1, rsx, one, XYDN, 0);
  const int nxt = __builtin_amdgcn_readfirstlane(*(const u32*)(sm + 4 * SU_CH));
  if (nxt < 256) ssm_load(in, 4 * nxt, rst, rsd, tid, lane, w);
  SST(4)
  f32x16 cv0, cv1;
  {
    const int m0 = 48 - 15 + 8 * h + r;
    const int cbo = (int)(size_t)(LAS unsigned char*)cb;
    int ua = cbo + SU_U + (m0 & 1) * SU_CP + ((m0 + 1) & ~1) * 2 - 32, ub = ua + 2048;
    const int kb = cbo + SU_K + 16 * h;
    int a1 = kb + (33 + r) * 80, a0 = kb + (1 + r) * 80, cnt = 32;
#define CRA(S, ad, O) "ds_read2_b32 " RNG("v", S, 2) ", " ad " offset0:" STR((O) / 4) " offset1:" STR((O) / 4 + 1) "\n\t" \
                      "ds_read2_b32 " RNG("v", (S) + 2, 2) ", " ad " offset0:" STR((O) / 4 + 2) " offset1:" STR((O) / 4 + 3) "\n\t"
#define CRB(S, ad, O) "ds_read_b128 " RNG("v", S, 4) ", " ad " offset:" STR(O) "\n\t"
#define CMF(C, A, B) "v_mfma_f32_32x32x16_bf16 " RNG("v", C, 16) ", " RNG("v", A, 4) ", " RNG("v", B, 4) ", " RNG("v", C, 16) "\n\t"
#define CUPD "v_add_u32 %[ua], 64, %[ua]\n\t" "v_subrev_u32 %[a1], 80, %[a1]\n\t" "v_subrev_u32 %[a0], 80, %[a0]\n\t" "v_max_i32 %[a0], %[a0], %[kb]\n\t"
#define CHALF(S, N, O, UPD, UPD2)                                                                 \
    WLG(5) CMF(64, S, (S) + 8) UPD CRA(N, "%[ua]", O)                                             \
    WLG(6) CMF(80, S, (S) + 16) UPD2 CRB((N) + 8, "%[a0]", O) CRB((N) + 16, "%[a1]", O)           \
    WLG(6) CMF(80, (S) + 4, (S) + 8) CRA((N) + 4, "%[ub]", O)                                     \
    WLG(7) CMF(64, S, (S) + 12) CRB((N) + 12, "%[a0]", (O) + 5200)                                \
    WLG(7) CMF(80, S, (S) + 20) CRB((N) + 20, "%[a1]", (O) + 5200)                                \
    CMF(80, (S) + 4, (S) + 12)
    asm volatile(
        CRA(96, "%[ua]", 32) CRB(104, "%[a0]", 32) CRB(112, "%[a1]", 32) CRA(100, "%[ub]", 32) CRB(108, "%[a0]", 32 + 5200) CRB(116, "%[a1]", 32 + 5200)
        ZER(64) ZER(68) ZER(72) ZER(76) ZER(80) ZER(84) ZER(88) ZER(92)
        ".Lc%=:\n\t"
        CHALF(96, 120, 0, "", "")
        CHALF(120, 96, 32, CUPD, "v_add_u32 %[ub], 64, %[ub]\n\t")
        "s_sub_u32 %[cnt], %[cnt], 1\n\t"
        "s_cmp_lg_u32 %[cnt], 0\n\t"
        "s_cbranch_scc1 .Lc%=\n\t"
        "s_nop 7\n\t" "s_nop 3\n\t" WLG(0)
        : "=&{v[64:79]}"(cv0), "=&{v[80:95]}"(cv1), [ua] "+v"(ua), [ub] "+v"(ub), [a0] "+v"(a0), [a1] "+v"(a1), [cnt] "+s"(cnt)
        : [kb] "v"(kb)
        : CL96, CL112, CL120, CL8(128, 129, 130, 131, 132, 133, 134, 135), CL8(136, 137, 138, 139, 140, 141, 142, 143), "scc", "memory");
  }
  SST(5)
  SSM_GOT(in);
#pragma unroll
  for (int g = 0; g < 2; ++g)
#pragma unroll
    for (int i = 0; i < 4; ++i) {
      const f32x16& c = g ? cv1 : cv0;
      *(u32x2*)(cb + SU_Y + (32 * g + r) * 64 + ((i ^ (r >> 1)) & 3) * 16 + 8 * h) = (u32x2){pack2(c[4 * i], c[4 * i + 1]), pack2(c[4 * i + 2], c[4 * i + 3])};
    }
  LBAR();
  SST(6)
  {
    u32x4 c[4];
#pragma unroll
    for (int j = 0; j < 4; ++j) c[j] = *(const u32x4*)(sm + j * SU_CH + SU_Y + (tid >> 2) * 64 + ((tid ^ (tid >> 3)) & 3) * 16);
#pragma unroll
    for (int i = 0; i < 4; ++i) {
      u32x4 y;
      asm("v_dot2_f32_bf16 %4, %12, %16, %4\n\tv_dot2_f32_bf16 %5, %13, %16, %5\n\tv_dot2_f32_bf16 %6, %14, %16, %6\n\tv_dot2_f32_bf16 %7, %15, %16, %7\n\t"
          "v_dot2_f32_bf16 %8, %12, %17, %8\n\tv_dot2_f32_bf16 %9, %13, %17, %9\n\tv_dot2_f32_bf16 %10, %14, %17, %10\n\tv_dot2_f32_bf16 %11, %15, %17, %11\n\t"
          "v_cvt_pk_bf16_f32 %0, %4, %5\n\tv_cvt_pk_bf16_f32 %1, %6, %7\n\tv_cvt_pk_bf16_f32 %2, %8, %9\n\tv_cvt_pk_bf16_f32 %3, %10, %11"
          : "=&v"(y[0]), "=&v"(y[1]), "=&v"(y[2]), "=&v"(y[3]),
            "+v"(sk[2 * i][0]), "+v"(sk[2 * i][1]), "+v"(sk[2 * i][2]), "+v"(sk[2 * i][3]),
            "+v"(sk[2 * i + 1][0]), "+v"(sk[2 * i + 1][1]), "+v"(sk[2 * i + 1][2]), "+v"(sk[2 * i + 1][3])
          : "v"(c[0][i]), "v"(c[1][i]), "v"(c[2][i]), "v"(c[3][i]), "s"(0x00003f80u), "s"(0x3f800000u));
      __builtin_amdgcn_raw_buffer_store_b128(y, rst, tid * 16, SSM_L * SSM_D * 2 + d0 * 4096 + i * 4096, 16);
    }
  }
  if (nxt >= 256) {
    asm volatile("s_waitcnt vmcnt(0)\n\ts_barrier" ::: "memory");
    __builtin_amdgcn_raw_ptr_buffer_atomic_add_i32(1, rsx, one, XYDN, 0);
  }
  SST(7)
  return nxt;
}

#define L_TOTAL 163840
__device__ __forceinline__ int ldw(__amdgpu_buffer_rsrc_t rs, int off) {
  asm volatile("" ::: "memory");
  return __builtin_amdgcn_readfirstlane(__builtin_amdgcn_raw_buffer_load_b32(rs, 0, off, 16));
}
#define GF(rt) "v[64+8*(xk&7)+4*" #rt ":64+8*(xk&7)+4*" #rt "+3]"
#define GW2 "v[128+4*xk:128+4*xk+3]"
#define GW3 "v[24+4*(xk&1):24+4*(xk&1)+3]"
#define GLOAD(S, K) "s_xor_b32 %[tq], " S ", 1024*(" K ")\n\t"
#define GTR(K) "ds_read_b128 v[24+4*((" K ")&1):24+4*((" K ")&1)+3], %[wl] offset:1024*((" K ")&31)\n\t"
#define GPACE 0
#define YAUX 16
#define UDELAY 100
#define USH 1
#define GNOSSM 1
#define GNG 3
#define GS0 22
__device__ __forceinline__ void g_role(unsigned char* lds, const u16* __restrict__ Wo, u16* __restrict__ out, __amdgpu_buffer_rsrc_t rsx,
                                       __amdgpu_buffer_rsrc_t rsf, int fb, u32 tag, int grp, int sl, int w, u32* udn, const u16* __restrict__ u, __amdgpu_buffer_rsrc_t rst) {
  const int lane = fresh_lane();
  const int lr = lane & 15, lg = lane >> 4;
  const int rot = (2 * sl + 8 * w) & 31;
  const int ldsb = (int)(size_t)(LAS unsigned char*)lds;
  u32x32 xw[8], w2[4];
  LBAR();
  {
    const __amdgpu_buffer_rsrc_t rw = __builtin_amdgcn_make_buffer_rsrc((void*)Wo, 0, DM * HD * 2, 0x00020000);
    const int vq = (lane >> 3) * (DM * 2) + sl * 128 + (lane & 7) * 16;
    const int sq = (1024 * w + rot * 32) * (DM * 2);
#define GSLOT(c) ((c) < 8 ? (((c) & 3) == 3 ? 131072 + w * 8192 : w * 32768 + 8192 * (((c) & 3) + 1)) : (((c) - 8) % 3 == 2 ? 131072 + w * 8192 : w * 32768 + 8192 * (((c) - 8) % 3 + 2)))
#define GISSUE(c) _Pragma("unroll") for (int j = 0; j < 8; ++j)                                                        \
      asm volatile("s_mov_b32 m0, %0\n\ts_nop 3\n\tbuffer_load_dwordx4 %1, %2, %3 offen lds"                           \
                   :: "s"(ldsb + GSLOT(c) + j * 1024), "v"(vq), "s"(rw), "s"(sq ^ ((64 * (c) + 8 * j) * DM * 2)) : "memory");
    GISSUE(0) GISSUE(1) GISSUE(2) GISSUE(3)
    const int q = lr >> 2, p = lr & 3;
    u32x4 w3[16];
#pragma unroll
    for (int c = 0; c < 16; ++c) {
      if (c < 5) waitvm<24>(); else if (c < 14) waitvm<16>(); else if (c == 14) waitvm<8>(); else waitvm<0>();
      u32x4 f3[2];
#pragma unroll
      for (int s = 0; s < 2; ++s)
#pragma unroll
        for (int t = 0; t < 4; ++t) {
          const unsigned char* base = lds + GSLOT(c) + (32 * s + 8 * lg + q) * 128 + (t < 2 ? p * 16 + t * 8 : t * 32 + p * 8);
          const u32x2 t0 = __builtin_bit_cast(u32x2, __builtin_amdgcn_ds_read_tr16_b64_v4i16((LAS v4i16_t*)(base)));
          const u32x2 t1 = __builtin_bit_cast(u32x2, __builtin_amdgcn_ds_read_tr16_b64_v4i16((LAS v4i16_t*)(base + 512)));
          const int o = t < 2 ? ((2 * c + s) * 2 + t) * 4 : (2 * c + s) * 4;
          if (t < 2) { xw[o >> 5][(o & 31) + 0] = t0[0]; xw[o >> 5][(o & 31) + 1] = t0[1]; xw[o >> 5][(o & 31) + 2] = t1[0]; xw[o >> 5][(o & 31) + 3] = t1[1]; }
          else if (t == 2) { w2[o >> 5][(o & 31) + 0] = t0[0]; w2[o >> 5][(o & 31) + 1] = t0[1]; w2[o >> 5][(o & 31) + 2] = t1[0]; w2[o >> 5][(o & 31) + 3] = t1[1]; }
          else f3[s] = (u32x4){t0[0], t0[1], t1[0], t1[1]};
        }
      asm volatile("s_waitcnt lgkmcnt(0)" ::: "memory");
      if (c < 8) { *(u32x4*)(lds + w * 32768 + (2 * c) * 1024 + lane * 16) = f3[0]; *(u32x4*)(lds + w * 32768 + (2 * c + 1) * 1024 + lane * 16) = f3[1]; }
      else { w3[2 * (c - 8)] = f3[0]; w3[2 * (c - 8) + 1] = f3[1]; }
      if (GPACE) __builtin_amdgcn_s_sleep(GPACE);
      if (c < 4) { GISSUE(c + 4) } else if (c >= 5 && c < 13) { GISSUE(c + 3) }
    }
#pragma unroll
    for (int i = 0; i < 16; ++i) *(u32x4*)(lds + w * 32768 + (16 + i) * 1024 + lane * 16) = w3[i];
    LBAR();
  }
  if (USH == 2 && grp == 0) {
    const int tid = w * 64 + lane;
    unsigned char* im = lds + 131072;
    const __amdgpu_buffer_rsrc_t ru = __builtin_amdgcn_make_buffer_rsrc((void*)u, 0, SSM_L * SSM_D * 2, 0x00020000);
    const int vo = (tid >> 5) * (SSM_D * 2) + (tid & 31) * 16, rp = 4 * (tid & 7) + ((tid >> 3) & 3);
    u32x4 t[2][8];
#pragma unroll
    for (int q = 0; q < 2; ++q)
#pragma unroll
      for (int i = 0; i < 8; ++i) t[q][i] = __builtin_amdgcn_raw_buffer_load_b128(ru, vo, (sl * 64 + i * 8) * (SSM_D * 2) + q * 512, 0);
#pragma unroll
    for (int q = 0; q < 4; ++q) {
#pragma unroll
      for (int i = 0; i < 8; ++i) { const int r = i * 8 + (tid >> 5); *(u32x4*)(im + r * 512 + (((tid & 31) ^ (r >> 1)) * 16)) = t[q & 1][i]; }
      if (q < 2) {
#pragma unroll
        for (int i = 0; i < 8; ++i) t[q][i] = __builtin_amdgcn_raw_buffer_load_b128(ru, vo, (sl * 64 + i * 8) * (SSM_D * 2) + (q + 2) * 512, 0);
      }
      LBAR();
#pragma unroll
      for (int i = 0; i < 8; ++i) {
        const int o = rp * 1024 + (((i * 4 + w) ^ rp) * 16) + (lane >> 5) * 8;
        const u32x2 a = *(const u32x2*)(im + o), c = *(const u32x2*)(im + o + 512);
        __builtin_amdgcn_raw_buffer_store_b128((u32x4){a[0], a[1], c[0], c[1]}, rst, (tid >> 3) * 4096 + (tid & 7) * 16, (q * 256 + i * 32) * 4096 + sl * 128, 16);
      }
      if (q < 3) LBAR();
    }
    asm volatile("s_waitcnt vmcnt(0) lgkmcnt(0)\n\ts_barrier" ::: "memory");
  }
  wait_start(rsx, fb, tag, true);
  if (USH && grp == 0 && w == 0 && lane == 0) __hip_atomic_fetch_add(udn, 1u, RLX_AGENT);
  const int wl = ldsb + w * 32768 + lane * 16;
  const __amdgpu_buffer_rsrc_t rso = __builtin_amdgcn_make_buffer_rsrc((void*)out, 0, S_LEN * OUTLD * 2, 0x00020000);
  const int rw_ = ldsb + 131072 + w * 8192 + lane * 16, rr_ = ldsb + 131072 + w * 2048 + lane * 16;
  const int oa = ((16 * (w & 1) + lr) * OUTLD + 64 * sl + (w < 2 ? 8 * lg : 32 + 4 * lg)) * 2, ob = oa + (w < 2 ? 8 : 32);
  const int hq = (grp + GNG - (62 - GS0) % GNG) % GNG - 1, hf = hq < 2 ? hq : -1;
#define GRW(i) "ds_write_b128 %[rw], " RNG("v", 32 + 4 * (i), 4) " offset:" STR(1024 * (i)) "\n\t"
#define GRR(i) "ds_read_b128 " RNG("v", 32 + 4 * (i), 4) ", %[rr] offset:" STR(8192 * ((i) >> 1) + 1024 * ((i) & 1)) "\n\t"
#define GNXT(D, S) "s_cmp_lt_u32 " S ", " STR(GS0) "\n\t" "s_cselect_b32 " D ", 1, " STR(GNG) "\n\t" "s_add_u32 " D ", " D ", " S "\n\t"
#define GDN1(S) "s_min_u32 s41, " S ", 63\n\t" "s_lshl_b32 s41, s41, 5\n\t" "v_add_u32 v23, s41, %[u0]\n\t" "v_lshrrev_b32 v22, 6, v23\n\t"
#define GDN2 "v_and_b32 v23, 63, v23\n\t" "v_lshl_add_u32 v23, v23, 7, %[u4]\n\t" "s_lshr_b32 s41, s41, 6\n\t" "s_max_u32 s41, s41, 29\n\t"
#define GDN3 "s_lshl_b32 s41, s41, 13\n\t" "s_add_u32 s41, s41, %[pa]\n\t" "buffer_load_dword v23, v23, %[rs], s41 offen sc1\n\t"
#define GDN(S) GDN1(S) GDN2 GDN3
#define GDEF 1
#define GB2 16
#define GCK 15
#define GOK(D) "v_cmp_le_i32 vcc, v23, v22\n\t" "s_cmp_eq_u64 vcc, 0\n\t" "s_cselect_b32 " D ", 1, 0\n\t"
static_assert(NAW == 64, "shifts above");
#define GEPI(MID, OA, OB) \
      GRW(0) GRW(1) GRW(2) GRW(3) GRW(4) GRW(5) GRW(6) GRW(7) \
      MID \
      WLG(0) "s_barrier\n\t" \
      GRR(0) GRR(1) GRR(2) GRR(3) GRR(4) GRR(5) GRR(6) GRR(7) \
      WLG(4) PKA(32, 32, 40) PKA(34, 34, 42) PKA(36, 36, 44) PKA(38, 38, 46) \
      WLG(2) PKA(32, 32, 48) PKA(34, 34, 50) PKA(36, 36, 52) PKA(38, 38, 54) \
      WLG(0) PKA(32, 32, 56) PKA(34, 34, 58) PKA(36, 36, 60) PKA(38, 38, 62) \
      "v_cvt_pk_bf16_f32 v20, v32, v33\n\t" "v_cvt_pk_bf16_f32 v21, v34, v35\n\t" "v_cvt_pk_bf16_f32 v18, v36, v37\n\t" "v_cvt_pk_bf16_f32 v19, v38, v39\n\t" \
      "buffer_store_dwordx2 v[20:21], " OA ", %[ro], s42 offen\n\t" \
      "buffer_store_dwordx2 v[18:19], " OB ", %[ro], s42 offen\n\t"
#define GH "v[64+4*(xk&15):64+4*(xk&15)+3]"
#define GSTAMP
#define GSTIN
#define GSTCL
#define GT1
#define GT2
#define GTO
  asm volatile(
      "s_nop 4\n\t"
      "v_add_u32 v16, 131072, %[va]\n\t"
      "s_mov_b32 s36, %[s0]\n\t" "s_mov_b32 s43, " STR(SPIN_CAP) "\n\t"
      "s_mov_b32 s37, 0\n\t" "v_mov_b32 v17, " STR(OOR) "\n\t" "v_mov_b32 v12, v17\n\t" "v_mov_b32 v15, v17\n\t"
      "s_cmp_ge_i32 %[hf], 0\n\t" "s_cbranch_scc1 .Li%=\n"
      ".Lp%=:\n\t"
      GDN("s36") WVM(0) GOK("s40")
      "s_cbranch_scc1 .Lr%=\n\t"
      "s_sleep 2\n\t" "s_sub_u32 s43, s43, 1\n\t" "s_cmp_gt_i32 s43, 0\n\t" "s_cbranch_scc1 .Lp%=\n"
      ".Lr%=:\n\t"
      "s_lshl_b32 s38, s36, 18\n\t" "s_add_u32 s38, s38, %[gb]\n\t"
      ".set xk, 0\n\t.rept 8\n\t" GLOAD("s38", "xk")
      "buffer_load_dwordx4 " GF(0) ", %[va], %[rf], %[tq] offen sc1\n\t" "buffer_load_dwordx4 " GF(1) ", v16, %[rf], %[tq] offen sc1\n\t"
      ".set xk, xk+1\n\t.endr\n"
#define GWD(D, i) ".if " #D " && (xk == 0)\n\t" GRW(i) ".endif\n\t"
#define GAD(i) ".if (xk >= 2) && (xk < 10) && ((xk-2)&3)\n\t" "v_add_f32 v[12+" #i "], v[12+" #i "], v[18+" #i "]\n\t" ".endif\n\t"
#define GSTEP(D) \
      "s_waitcnt vmcnt(14)\n\t" \
      GWD(D, 0) XM1(XA(32, 0), XW(0, 0), GF(0), 1) \
      ".if (xk == 2) || (xk == 6)\n\t" "ds_read_b128 v[12:15], %[rr] offset:256*(xk-2)\n\t" ".elseif (xk > 2) && (xk < 10)\n\t" "ds_read_b128 v[18:21], %[rr] offset:8192*((xk-2)&3)+1024*((xk-2)>>2)\n\t" ".endif\n\t" \
      GTR("xk+1") GWD(D, 1) XM1(XA(32, 4), XW(0, 1), GF(0), 1) \
      ".if xk < 24\n\t" GLOAD("s38", "xk+8") ".else\n\t" GLOAD("s39", "xk-24") ".endif\n\t" \
      GWD(D, 2) XM1(XA(32, 8), XW(0, 0), GF(1), 1) \
      ".if xk == " STR(GCK) "-1\n\t" GNXT("s37", "s36") ".endif\n\t" \
      ".if xk == " STR(GCK) "\n\t" GDN1("s37") ".endif\n\t" \
      ".if xk == 23\n\t" GOK("s40") ".endif\n\t" \
      GWD(D, 3) XM1(XA(32, 12), XW(0, 1), GF(1), 1) \
      ".if xk == 7\n\t" "s_add_u32 s42, s42, %[od]\n\t" ".endif\n\t" \
      ".if xk == " STR(GCK) "-1\n\t" "s_lshl_b32 s42, s36, 17\n\t" ".endif\n\t" \
      ".if xk == " STR(GCK) "\n\t" GDN2 ".endif\n\t" \
      ".if xk == 23\n\t" "s_cmp_lt_u32 s37, 63\n\t" "s_cselect_b32 s40, s40, 0\n\t" "s_cmp_lg_u32 s40, 0\n\t" ".endif\n\t" \
      GWD(D, 4) XM1(XA(32, 16), GW2, GF(0), 1) \
      ".if xk == " STR(GCK) "\n\t" GDN3 ".endif\n\t" \
      ".if xk == 23\n\t" "s_cselect_b32 s39, s37, s36\n\t" "s_lshl_b32 s39, s39, 18\n\t" "s_add_u32 s39, s39, %[gb]\n\t" ".endif\n\t" \
      ".if " #D " && (xk == 0)\n\t" GRW(5) WLG(7) ".else\n\t" WLG(1) ".endif\n\t" XM1(XA(32, 20), GW3, GF(0), 1) \
      "buffer_load_dwordx4 " GF(0) ", %[va], %[rf], %[tq] offen sc1\n\t" GAD(0) GAD(1) \
      GWD(D, 6) XM1(XA(32, 24), GW2, GF(1), 1) GAD(2) GAD(3) GWD(D, 7) XM1(XA(32, 28), GW3, GF(1), 1) \
      "buffer_load_dwordx4 " GF(1) ", v16, %[rf], %[tq] offen sc1\n\t" \
      ".if (xk == 5) || (xk == 9)\n\t" "v_cvt_pk_bf16_f32 v22, v12, v13\n\t" "v_cvt_pk_bf16_f32 v23, v14, v15\n\t" "buffer_store_dwordx2 v[22:23], v17, %[ro], s42 offen\n\t" ".endif\n\t" \
      ".if (xk == " STR(GB2) ") || (" #D " && (xk == 1))\n\t" "s_barrier\n\t" ".endif\n\t" \
      ".set xk, xk+1\n\t"
      GTR("0") "v_mov_b32 v17, " STR(OOR) "\n\t" "s_mov_b32 s42, 0\n\t"
      ".set xk, 0\n\t.rept 2\n\t" GSTEP(0) ".endr\n\t"
      "s_branch .Lc%=\n"
      ".Lb%=:\n\t"
      ".set xk, 0\n\t.rept 2\n\t" GSTEP(1) ".endr\n\t"
      "v_mov_b32 v17, %[oa]\n"
      ".Lc%=:\n\t"
      ".rept 30\n\t" GSTEP(0) ".endr\n\t"
      GSTAMP
      "s_mov_b32 s36, s37\n\t" "s_mov_b32 s38, s39\n\t"
      "s_cmp_lg_u32 s40, 0\n\t" "s_cbranch_scc1 .Lb%=\n\t"
      GEPI("", "%[oa]", "%[ob]")
      "s_cmp_lt_u32 s36, 63\n\t" "s_cbranch_scc1 .Lp%=\n\t"
      "s_cmp_lt_i32 %[hf], 0\n\t" "s_cbranch_scc1 .Le%=\n\t"
      "s_mov_b32 s37, 4\n\t" "v_mov_b32 v17, %[va]\n\t" "v_mov_b32 v12, %[oc]\n\t" "v_sub_u32 v15, %[ob], %[oa]\n\t" "v_add_u32 v15, v15, %[oc]\n\t"
      "s_barrier\n"
      ".Lh%=:\n\t"
      "v_cmp_eq_u32 vcc, -1, v23\n\t" "s_cbranch_vccz .Lt%=\n\t" "buffer_load_dword v23, %[uh], %[rs], 0 offen sc1\n\t" WVM(0)
      "s_sleep 1\n\t" "s_sub_u32 s43, s43, 1\n\t" "s_cmp_gt_i32 s43, 0\n\t" "s_cbranch_scc1 .Lh%=\n"
      ".Lt%=:\n\t"
      "v_readfirstlane_b32 s39, v23\n\t" "s_cmp_ge_i32 s39, 0\n\t" "s_cbranch_scc1 .Li%=\n"
      ".Lw%=:\n\t"
      "s_memrealtime s[40:41]\n\t" WLG(0) "s_sub_u32 s40, s39, s40\n\t" "s_lshl_b32 s40, s40, 1\n\t" "s_sub_u32 s40, s40, 1\n\t" "s_cmp_lt_u32 s40, 4000\n\t" "s_cbranch_scc1 .Lw%=\n\t"
      "s_branch .Li%=\n"
      ".Lx%=:\n\t"
      "s_sub_u32 s37, s37, 1\n\t" "s_cmp_gt_u32 s37, 1\n\t" "s_cbranch_scc1 .Li%=\n"
      ".Ly%=:\n\t"
      "buffer_load_dword v23, %[uh], %[rs], 0 offen sc1\n\t" WVM(0) "v_cmp_ge_i32 vcc, 31, v23\n\t" "s_cbranch_vccz .Li%=\n\t"
      "s_sub_u32 s43, s43, 1\n\t" "s_cmp_gt_i32 s43, 0\n\t" "s_cbranch_scc1 .Ly%=\n"
      ".Li%=:\n\t" GT1
      "s_lshl_b32 s38, %[hf], 17\n\t" "s_add_u32 s38, s38, 63*262144\n\t" "s_add_u32 s38, s38, %[gb]\n\t"
      ".set xk, 0\n\t.rept 16\n\t" GLOAD("s38", "xk") "buffer_load_dwordx4 " GH ", v17, %[rf], %[tq] offen sc1\n\t" ".set xk, xk+1\n\t.endr\n\t"
      GTR("0")
      "s_lshl_b32 s42, %[hf], 16\n\t" "s_add_u32 s42, s42, 63*131072\n\t" "v_mov_b32 v22, 0\n\t" "s_cmp_gt_u32 s37, 1\n\t" "s_cselect_b32 s39, -2, -1\n\t"
      ".set xk, 0\n\t.rept 32\n\t"
      ".if xk < 17\n\t" "s_waitcnt vmcnt(15)\n\t" ".else\n\t" "s_waitcnt vmcnt(31-xk)\n\t" ".endif\n\t"
      XM1(XA(32, 0), XW(0, 0), GH, 1) GTR("xk+1") XM1(XA(32, 4), XW(0, 1), GH, 1) "v_max3_u32 v22, v22, v[64+4*(xk&15)], v[65+4*(xk&15)]\n\t"
      XM1(XA(32, 16), GW2, GH, 1) "v_max3_u32 v22, v22, v[66+4*(xk&15)], v[67+4*(xk&15)]\n\t" "s_waitcnt lgkmcnt(1)\n\t" XM1(XA(32, 20), GW3, GH, 1)
      ".if xk & 1\n\t" "v_cmp_lt_u32 vcc, s39, v22\n\t" "s_cbranch_vccnz .Lx%=\n\t" ".endif\n\t"
      ".if xk < 16\n\t" GLOAD("s38", "xk+16") "buffer_load_dwordx4 " GH ", v17, %[rf], %[tq] offen sc1\n\t" ".endif\n\t"
      ".set xk, xk+1\n\t.endr\n\t"
      "s_nop 7\n\t"
      GEPI("", "v12", "v15")
      "s_cmp_eq_u32 s37, 0\n\t" "s_cbranch_scc1 .Lp%=\n"
      ".Le%=:\n\t"
      WVM(0)
      GT2
      : GTO [tq] "=&s"(sl)
      : GSTIN [rs] "s"(rsx), [s0] "s"(__builtin_amdgcn_readfirstlane(grp ? GS0 + grp : 0)), [gb] "s"(FO_O - 32768 + (32 * w + rot) * 1024), [va] "v"(lane * 16 + 32768),
        [rf] "s"(rsf), [wl] "v"(wl), [rw] "v"(rw_), [rr] "v"(rr_), [oa] "v"(oa), [ob] "v"(ob), [ro] "s"(rso), [od] "s"(w < 2 ? 8 : 32), [u0] "v"(8 * w + (lane & 7)), [u4] "v"((lane >> 1) & 4), [uh] "v"(PGO_A + (160 + 8 * w + (lane & 7)) * 128 + 4 * (hf & 1)), [pa] "s"(PGO_A - 29 * 8192), [hf] "s"(__builtin_amdgcn_readfirstlane(hf)), [oc] "v"((w & 1) ? OOR - 64 : oa),
        "{a[0:31]}"(xw[0]), "{a[32:63]}"(xw[1]), "{a[64:95]}"(xw[2]), "{a[96:127]}"(xw[3]),
        "{a[128:159]}"(xw[4]), "{a[160:191]}"(xw[5]), "{a[192:223]}"(xw[6]), "{a[224:255]}"(xw[7]),
        "{v[128:159]}"(w2[0]), "{v[160:191]}"(w2[1]), "{v[192:223]}"(w2[2]), "{v[224:255]}"(w2[3])
      : GSTCL "v12", "v13", "v14", "v15", "v16", "v17", "v18", "v19", "v20", "v21", "v22", "v23", "vcc", CL8(24, 25, 26, 27, 28, 29, 30, 31), CL8(32, 33, 34, 35, 36, 37, 38, 39), CL8(40, 41, 42, 43, 44, 45, 46, 47),
        CL8(48, 49, 50, 51, 52, 53, 54, 55), CL8(56, 57, 58, 59, 60, 61, 62, 63), CL64, CL72, CL80, CL88, CL96, CL112, CL120,
        "s36", "s37", "s38", "s39", "s40", "s41", "s42", "s43", "scc", "memory");
}
static_assert(GNOSSM == 1 && GNG == 3 && 4 * SU_CH + 16 <= L_TOTAL && 4 * XSLOT <= L_TOTAL, "lds");
__global__ void __launch_bounds__(256, 1)
main_kernel(const u16* __restrict__ x, const u16* __restrict__ mem0, const u16* __restrict__ Wq, const u16* __restrict__ Wk, const u16* __restrict__ Wv, const u16* __restrict__ Wo,
            unsigned char* ws, const u16* __restrict__ u, const u16* __restrict__ A_re, const u16* __restrict__ A_im, const u16* __restrict__ B_re,
            const u16* __restrict__ B_im, const u16* __restrict__ C_re, const u16* __restrict__ C_im,
            const u16* __restrict__ log_dt, const u16* __restrict__ Dskip, u16* __restrict__ out
            ) {
  __shared__ __attribute__((aligned(8192))) unsigned char lds[L_TOTAL];
  const int w = __builtin_amdgcn_readfirstlane(threadIdx.x >> 6);
  u64 pc0;
  int clen;
  {
    asm volatile("s_getpc_b64 %0\n.Lkpc0:\n\ts_mov_b32 %1, .Lkpc1-.Lkpc0" : "=s"(pc0), "=s"(clen));
  }
#define LOCAL_IDS const int lane = fresh_lane(), tid = w * 64 + lane; (void)tid;
  const int si = (blockIdx.x >> 3) * 4 + (blockIdx.x & 3);
  unsigned char* xb = (unsigned char*)(((size_t)ws + XPAGE - 1) & ~(size_t)(XPAGE - 1));
  const int fb = (int)(ws + WS_F - xb);
  u32 tag;
  asm volatile("s_load_dword %0, %1, 0x0\n\ts_waitcnt lgkmcnt(0)\n\ts_add_u32 %0, %0, 256" : "=s"(tag) : "s"(ws + WS_F) : "scc", "memory");
  const __amdgpu_buffer_rsrc_t rsx = __builtin_amdgcn_make_buffer_rsrc(xb, 0, fb + (int)WS_F_SZ, 0x00020000);
  const __amdgpu_buffer_rsrc_t rsf = __builtin_amdgcn_make_buffer_rsrc(ws + WS_QX, 0, (int)(4 * WS_QKV_SZ + 2 * WS_KM_SZ), 0x00020000);
  const __amdgpu_buffer_rsrc_t rst = __builtin_amdgcn_make_buffer_rsrc(ws + WS_UT, 0, 2 * SSM_L * SSM_D * 2 + SSM_D * 3072, 0x00020000);
  if ((blockIdx.x & 7) < 4) {
    { LOCAL_IDS
    r_role(lds, si, mem0, Wq, Wk, Wv, Wo, fb, tag, pc0, clen, rsx, rsf, rst, SsmPtrs{A_re, A_im, B_re, B_im, C_re, C_im, log_dt}, tid, lane, w
           ); }
    LOCAL_IDS
    {
      for (int it = 0; it < SPIN_CAP; ++it) {
        if (ldw(rsx, XYDN) >= 255) break;
        asm volatile("s_sleep 8" ::: "memory");
      }
      asm volatile("" ::: "memory");
      asm volatile(".Lkpc1:" ::: "memory");
      unsigned char* im = lds + 65536;
      const __amdgpu_buffer_rsrc_t rso = __builtin_amdgcn_make_buffer_rsrc((void*)out, 0, S_LEN * OUTLD * 2, 0x00020000);
      u32x4 t[8];
#pragma unroll
      for (int i = 0; i < 8; ++i) { const int p = i * 256 + tid; t[i] = __builtin_amdgcn_raw_buffer_load_b128(rst, (p >> 3) * 4096 + (p & 7) * 16, SSM_L * SSM_D * 2 + (si & 3) * 64 * 16384 + (si >> 2) * 128, 16); }
#pragma unroll
      for (int i = 0; i < 8; ++i) {
        const int p = i * 256 + tid, o = (p & 7) * (8 * 512 + 16) + ((p >> 3) & 3) * 1024 + (p >> 5) * 8;
        *(u32x2*)(im + o) = (u32x2){t[i][0], t[i][1]};
        *(u32x2*)(im + o + 512) = (u32x2){t[i][2], t[i][3]};
      }
      LBAR();
#pragma unroll
      for (int i = 0; i < 8; ++i)
        __builtin_amdgcn_raw_buffer_store_b128(*(const u32x4*)(im + i * (8 * 512 + 16) + tid * 16), rso, ((tid >> 5) * OUTLD + DM + (tid & 31) * 8) * 2,
                                               (((si >> 2) * 64 + 8 * i) * OUTLD + (si & 3) * 256) * 2, YAUX);
    }
  } else {
    const int g = blockIdx.x & 3, li = blockIdx.x >> 3;
    const int xi = g == 0 ? (li < 16 ? li : -1) : (li < 26 + (g < 3) ? 16 + 27 * (g - 1) + li : -1);
    const int ei = g == 0 ? li - 16 : 16 + 5 * (g - 1) + (31 - li);
    if (xi >= 0) {
      LOCAL_IDS
      const int head = xi & 31, mi = xi >> 5;
      u32* prog = (u32*)(xb + PGO_X + xi * 128);
      if (mi == 1) x_role<true>(lds, x, Wv, FO_V, head, rsf, prog, tag, lane, w);
      else x_role<false>(lds, x, mi == 0 ? Wk : Wq, mi == 0 ? FO_K : FO_Q, head, rsf, prog, tag, lane, w);
    } else {
      LOCAL_IDS
      u32 acc = 0;
#pragma unroll
      for (int i = 0; i < 4; ++i) acc += *(const u32*)(x + (size_t)((i * 32 + ei) * 256 + tid) * 64);
#define UV 0
#define WWARM(n, first) {    \
        const __amdgpu_buffer_rsrc_t rwo = __builtin_amdgcn_make_buffer_rsrc((void*)Wo, 0, DM * HD * 2, 0x00020000); \
        const int ldsb = (int)(size_t)(LAS unsigned char*)lds; \
        _Pragma("unroll 1") for (int c = 0; c < (n); ++c) { dma16(rwo, lane * 16, ((first) + c) * 16384, 1024, ldsb + w * 32768 + (c & 1) * 16384); waitvm<32>(); } }
      if (UV == 0 || (UV == 1 && ei < 16)) WWARM(4, (ei * 4 + w) * 4)
      if (UV == 2 && ei < 16) WWARM(8, (ei * 4 + w) * 8)
      if (UV == 3 && ei >= 16) WWARM(8, ((ei - 16) * 4 + w) * 8)
      if (UV == 4 && ei >= 16) WWARM(4, (ei * 4 + w) * 4)
      if (ei == 0 && tid < 16) acc += *(const u32*)(Dskip + tid * 64);
      asm volatile("" :: "v"(acc));
      if (USH == 2 && ei < 16) asm volatile("s_waitcnt vmcnt(0)" ::: "memory");
      if (USH == 1 || ei >= 16) {
      { const u32 t0 = rt32(); for (int i = 0; i < 1000 && (int)(rt32() - t0) < UDELAY; ++i) asm volatile("s_sleep 16" ::: "memory"); }
      asm volatile("s_waitcnt vmcnt(0)\n\ts_barrier" ::: "memory");
      for (int ps = 0; ps < 2 - !!USH; ++ps) {
      const int r0 = USH ? ei * 64 : ((ei - 16) * 2 + ps) * 64;
#pragma unroll
      for (int hb = 0; hb < 2; ++hb) {
        u32x4 t[16];
#pragma unroll
        for (int i = 0; i < 16; ++i) t[i] = *(const u32x4*)(u + (size_t)r0 * SSM_D + ((hb * 16 + i) * 256 + tid) * 8);
#pragma unroll
        for (int i = 0; i < 16; ++i) { const int p = (hb * 16 + i) * 256 + tid; *(u32x4*)(lds + (p >> 7) * 2064 + (p & 127) * 16) = t[i]; }
      }
      LBAR();
      for (int i = 0; i < 32; ++i) {
        const int p = i * 256 + tid, un = p >> 5, rp = 4 * (p & 7) + ((p >> 3) & 3);
        const u32x2 a = *(const u32x2*)(lds + (2 * rp) * 2064 + un * 8), c = *(const u32x2*)(lds + (2 * rp + 1) * 2064 + un * 8);
        __builtin_amdgcn_raw_buffer_store_b128((u32x4){a[0], a[1], c[0], c[1]}, rst, (p >> 3) * 4096 + (p & 7) * 16, r0 * 2, 16);
      }
      asm volatile("s_waitcnt vmcnt(0) lgkmcnt(0)\n\ts_barrier" ::: "memory");
      }
      if (UV == 1) { WWARM(4, (ei * 4 + w) * 4) waitvm<0>(); }
      if (ei >= 16) {
        wait_start(rsx, fb, tag, true);
        if (tid == 0) __hip_atomic_fetch_add((u32*)(xb + XUDN), 1u, RLX_AGENT);
      }
      }
    }
    asm volatile("s_waitcnt lgkmcnt(0)\n\ts_barrier" :: "s"(tag) : "memory");
    if (w == 0 && fresh_lane() == 0) __builtin_amdgcn_raw_buffer_store_b32(tag, rsx, 0, PGO_S + si * 128, 16);
    const bool isg = g == 0 ? li >= 16 : (g >= 5 - GNG && li < 16);
    if (xi >= 0 && !(GNOSSM && isg)) {
      LOCAL_IDS
      wait_start(rsx, fb, tag, true);
      for (int it = 0; it < SPIN_CAP; ++it) {
        if (ldw(rsx, XUDN) >= 15 + 16 * !!USH) break;
        asm volatile("s_sleep 8" ::: "memory");
      }
      asm volatile("" ::: "memory");
      int un = g == 0 ? li : g == 1 ? 16 + li : 5 + 11 * g + li;
      SsmIn in;
      const __amdgpu_buffer_rsrc_t rsd = __builtin_amdgcn_make_buffer_rsrc((void*)Dskip, 0, SSM_D * 2, 0x00020000);
      ssm_load(in, 4 * un, rst, rsd, tid, lane, w);
      if (lane < 20) *(u32x4*)(lds + w * SU_CH + (lane < 12 ? SU_U + (lane / 6) * SU_CP + (lane % 6) * 16 : SU_K + ((lane - 12) >> 2) * 5200 + (lane & 3) * 16)) = (u32x4){0u, 0u, 0u, 0u};
      SSM_GOT(in);
#define NU_
      for (bool pend = false; un < 256; pend = true NU_) un = ssm_unit(lds, 4 * un, in, pend, rst, rsx, fb, rsd, tid, lane, w);
    }
    if (isg) {
      g_role(lds, Wo, out, rsx, rsf, fb, tag, g ? g + GNG - 4 : 0, li & 15, w, (u32*)(xb + XUDN), u, rst);
    }
    else {
      const int lane = fresh_lane();
      if (xi < 0) a_role(rsx, rsf, (ei - 16) * 4 + w, (NSEG - 4) * NH, (ei - 16) * 4 + w, 0, lane, fb, tag);
      do {
        const int t = __builtin_amdgcn_readfirstlane(lane == 0 ? __hip_atomic_fetch_add((u32*)(xb + XTK2), 1u, RLX_AGENT) + 1u : 0u);
        if (t >= 256) break;
        a_late(rsx, rsf, t, lane, fb, tag);
      } while (xi < 0);
    }
  }
}

extern "C" void kernel_launch(void* const* d_in, const int* in_sizes, int n_in, void* d_out, int out_size,
                              void* d_ws, size_t ws_size, hipStream_t stream) {
  const u16* x = (const u16*)d_in[0];
  const u16* mem0 = (const u16*)d_in[1];
  const u16* Wq = (const u16*)d_in[2];
  const u16* Wk = (const u16*)d_in[3];
  const u16* Wv = (const u16*)d_in[4];
  const u16* Wo = (const u16*)d_in[5];
  const u16* u = (const u16*)d_in[7];
  const u16* A_re = (const u16*)d_in[8];
  const u16* A_im = (const u16*)d_in[9];
  const u16* B_re = (const u16*)d_in[10];
  const u16* B_im = (const u16*)d_in[11];
  const u16* C_re = (const u16*)d_in[12];
  const u16* C_im = (const u16*)d_in[13];
  const u16* log_dt = (const u16*)d_in[14];
  const u16* Dskip = (const u16*)d_in[15];
  u16* out = (u16*)d_out;
  char* ws = (char*)d_ws;

  main_kernel<<<256, 256, 0, stream>>>(x, mem0, Wq, Wk, Wv, Wo, (unsigned char*)ws, u, A_re, A_im, B_re, B_im, C_re, C_im, log_dt, Dskip, out
      );
}
```

```cpp
#include <hip/hip_runtime.h>
#include <hip/hip_bf16.h>
#include <stdint.h>

#define S_LEN 2048
#define DM 1024
#define NH 32
#define DH 128
#define HD 4096
#define MEM 8
#define SEG 32
#define NSEG 64
#define LTOK 40
#define SSM_L 2048
#define SSM_D 1024
#define SSM_N 128
#define OUTLD 2048

typedef unsigned short u16;
typedef unsigned int u32;
typedef unsigned long long u64;
typedef __attribute__((ext_vector_type(8))) short bf16x8;
typedef __attribute__((ext_vector_type(4))) float f32x4;
typedef __attribute__((ext_vector_type(4))) unsigned int u32x4;
typedef __attribute__((ext_vector_type(2))) unsigned int u32x2;
typedef __bf16 bf16x2_t __attribute__((ext_vector_type(2)));
typedef short v4i16_t __attribute__((ext_vector_type(4)));

#define SENT 0xFFFFFFFFu
#define RLX_AGENT __ATOMIC_RELAXED, __HIP_MEMORY_SCOPE_AGENT

__device__ __forceinline__ float bf2f(u16 v) { return __uint_as_float(((u32)v) << 16); }
__device__ __forceinline__ u16 f2bf(float f) {
  u32 u = __float_as_uint(f);
  u32 r = (u + 0x7fffu + ((u >> 16) & 1u)) >> 16;
  if ((u & 0x7fffffffu) > 0x7f800000u) r = 0x7fc0u;
  return (u16)r;
}
__device__ __forceinline__ float rbf(float f) { return bf2f(f2bf(f)); }

#define TR 92
#define TP 179
#define TDB 61
#define TDC 110
#define NR1 4
#define XPAGE (2 << 20)
#define XRING 3
#define XLP(p, L) ((((L) >> 13) << 15) + ((p) << 14) + ((L) & 8191))
#define XO_R3 131072
#define XR1A(x, L) XLP((x) & 1, ((x) >> 1) * 16384 + (L))
#define XR3A(rg, L) (XO_R3 + XLP(rg, L))
#define XSB(s) (((s) % XRING) == 2 ? 1048576 : ((s) % XRING) * 262144)
#define XR2B 524288
#define XR2S 1048576
#define XR2(j) (((((j) & 31) + (((j) >> 6) << 5)) / 6 << 15) + (((((j) >> 5) & 1) ^ 1) << 14) + ((((j) & 31) + (((j) >> 6) << 5)) % 6) * 1280)
#define XSYN (1048576 + 262144)
#define PGO_R (XSYN + 32768)
#define XCAL (PGO_R + 32768)
static_assert(XCAL + 16384 <= XPAGE, "exchange page");
#define WS_X 0ull
#define NPG 2
#define WS_X_SZ ((u64)NPG * XPAGE)
#define WS_F (WS_X + WS_X_SZ)
#define XEPO (fb)
#define XSTA (fb + 128)
#define PGO_X (fb + 256)
#define PGO_Y (PGO_X + 96 * 128)
#define PGO_S (PGO_Y + 128 * 128)
#define PGO_D (PGO_S + 128 * 128)
#define PGO_A (PGO_D + 128 * 128)
#define XTKT (PGO_A + 192 * 128)
#define XUDN (XTKT + 128)
#define XYDN (XUDN + 128)
#define XTK2 (XYDN + 128)
#define XSTR(x) (XTK2 + 128 + (x) * 128)
#define WS_F_SZ ((u64)(2 + 96 + 128 + 128 + 128 + 192 + 8) * 128)
#define WS_XCH_END (WS_F + 131072)
#define WS_QX (WS_XCH_END)
#define WS_QKV_SZ ((u64)S_LEN * HD * 2)
#define WS_KX (WS_QX + WS_QKV_SZ)
#define WS_VX (WS_KX + WS_QKV_SZ)
#define WS_OX (WS_VX + WS_QKV_SZ)
#define WS_KM (WS_OX + WS_QKV_SZ)
#define WS_KM_SZ ((u64)NSEG * 8 * HD * 2)
#define WS_VM (WS_KM + WS_KM_SZ)
#define WS_UT (WS_VM + WS_KM_SZ)
#define WS_YT (WS_UT + (u64)SSM_L * SSM_D * 2)
#define WS_KD (WS_YT + (u64)SSM_L * SSM_D * 2)
#define WS_TOTAL (WS_KD + (u64)SSM_D * 3072)

#define SPIN_CAP 400000
#define TRY_CAP 20000
#define LAS __attribute__((address_space(3)))
#define LBAR() asm volatile("s_waitcnt lgkmcnt(0)\n\ts_barrier" ::: "memory")
__device__ __forceinline__ u32 max4u(u32x4 v) { return max(max(v[0], v[1]), max(v[2], v[3])); }
typedef float f32x2_t __attribute__((ext_vector_type(2)));
__device__ __forceinline__ u32 pack2(float a, float b) {
  f32x2_t v = {a, b};
  bf16x2_t r = __builtin_convertvector(v, bf16x2_t);
  return __builtin_bit_cast(u32, r);
}
__device__ __forceinline__ float rbf_hw(float a) { return __uint_as_float(pack2(a, a) & 0xffff0000u); }
__device__ __forceinline__ u32 nosent(u32 v) { return min(v, 0xFFFFFFFEu); }
template <int N> __device__ __forceinline__ void waitvm() { asm volatile("s_waitcnt vmcnt(%0)" ::"n"(N) : "memory"); }
typedef __attribute__((ext_vector_type(16))) float f32x16;
__device__ __forceinline__ u32 split_hl(float kv) {
  const u32 h = pack2(kv, kv) & 0xffffu;
  const float r1 = kv - __uint_as_float(h << 16);
  return h | (pack2(r1, r1) & 0xffff0000u);
}
__device__ __forceinline__ int kidx(int idx) {
  const int sh = idx < 256 ? 0 : (idx < 512 ? 1 : (idx < 1024 ? 2 : 3));
  const int q = sh ? ((idx + (1 << (sh - 1)) - 1 + ((idx >> sh) & 1)) >> sh) : idx;
  return q + 128 * sh;
}

#define FO_Q 0
#define FO_K ((int)WS_QKV_SZ)
#define FO_V ((int)(2 * WS_QKV_SZ))
#define FO_O ((int)(3 * WS_QKV_SZ))
#define FO_KM ((int)(4 * WS_QKV_SZ))
#define FO_VM ((int)(4 * WS_QKV_SZ + WS_KM_SZ))
__device__ __forceinline__ u32x4 wait_start(__amdgpu_buffer_rsrc_t rsx, int fb, u32 tag, bool slow, int xcd = 0) {
  u32x4 st;
  for (int it = 0; it < SPIN_CAP; ++it) {
    asm volatile("" ::: "memory");
    st = __builtin_amdgcn_raw_buffer_load_b128(rsx, 0, slow ? XSTA : XSTR(xcd), 16);
    if ((u32)__builtin_amdgcn_readfirstlane(st[3]) == tag) break;
    if (slow) asm volatile("s_sleep 20" ::: "memory"); else asm volatile("s_sleep 1" ::: "memory");
  }
  asm volatile("" ::: "memory");
  return st;
}
__device__ __forceinline__ int xprog(u32 v, u32 tag) { return v - tag <= 80u ? (int)(v - tag) : -1; }
__device__ __forceinline__ float xmax4(float m) {
  { auto r = __builtin_amdgcn_permlane16_swap(__float_as_uint(m), __float_as_uint(m), false, false); m = __builtin_fmaxf(__uint_as_float(r[0]), __uint_as_float(r[1])); }
  { auto r = __builtin_amdgcn_permlane32_swap(__float_as_uint(m), __float_as_uint(m), false, false); m = __builtin_fmaxf(__uint_as_float(r[0]), __uint_as_float(r[1])); }
  return m;
}
__device__ __forceinline__ float xsum4(float m) {
  { auto r = __builtin_amdgcn_permlane16_swap(__float_as_uint(m), __float_as_uint(m), false, false); m = __uint_as_float(r[0]) + __uint_as_float(r[1]); }
  { auto r = __builtin_amdgcn_permlane32_swap(__float_as_uint(m), __float_as_uint(m), false, false); m = __uint_as_float(r[0]) + __uint_as_float(r[1]); }
  return m;
}
#define MF16(a, b, c) __builtin_amdgcn_mfma_f32_16x16x32_bf16(__builtin_bit_cast(bf16x8, a), __builtin_bit_cast(bf16x8, b), c, 0, 0, 0)
__device__ __forceinline__ f32x4 rnd2(f32x4 v, float sc) {
  const u32 a = pack2(v[0], v[1]), b = pack2(v[2], v[3]);
  const u32 c = pack2(__uint_as_float(a << 16) * sc, __uint_as_float(a & 0xffff0000u) * sc), d = pack2(__uint_as_float(b << 16) * sc, __uint_as_float(b & 0xffff0000u) * sc);
  return (f32x4){__uint_as_float(c << 16), __uint_as_float(c & 0xffff0000u), __uint_as_float(d << 16), __uint_as_float(d & 0xffff0000u)};
}
struct XaOps { u32x4 q[2][4], k[2][4], v[8]; };
__device__ __forceinline__ void xa_load(XaOps& o, __amdgpu_buffer_rsrc_t rsf, int sh, int vo) {
#pragma unroll
  for (int i = 0; i < 8; ++i) {
    o.q[i >> 2][i & 3] = __builtin_amdgcn_raw_buffer_load_b128(rsf, vo, FO_Q + (sh * 8 + i) * 1024, 16);
    o.k[i >> 2][i & 3] = __builtin_amdgcn_raw_buffer_load_b128(rsf, vo, FO_K + (sh * 8 + i) * 1024, 16);
  }
#pragma unroll
  for (int dt = 0; dt < 8; ++dt) o.v[dt] = __builtin_amdgcn_raw_buffer_load_b128(rsf, vo, FO_V + (sh * 8 + dt) * 1024, 16);
}
struct XaPre { f32x4 a[2], b[2]; float mx[2], tx[2]; };
__device__ __forceinline__ void xa_pre(XaPre& p, const XaOps& o, int lane) {
  const int lr = lane & 15, lg = lane >> 4;
  const float scale = 0.08838834764831845f, L2E = 1.4426950408889634f, NEG = -1000000000.0f;
  const f32x4 z = (f32x4){0, 0, 0, 0};
  f32x4 s00 = z, s10 = z, s11 = z;
#pragma unroll
  for (int ks = 0; ks < 4; ++ks) { s00 = MF16(o.k[0][ks], o.q[0][ks], s00); s10 = MF16(o.k[0][ks], o.q[1][ks], s10); s11 = MF16(o.k[1][ks], o.q[1][ks], s11); }
  s00 = rnd2(s00, scale); s10 = rnd2(s10, scale); s11 = rnd2(s11, scale);
  asm volatile("" : "+v"(s00), "+v"(s11));
  float m0 = NEG, m1 = -3.0e38f;
#pragma unroll
  for (int e = 0; e < 4; ++e) {
    const bool msk = 4 * lg + e > lr;
    s00[e] = msk ? NEG : s00[e]; s11[e] = msk ? NEG : s11[e];
    m0 = __builtin_fmaxf(m0, s00[e]); m1 = __builtin_fmaxf(m1, __builtin_fmaxf(s10[e], s11[e]));
  }
  m0 = xmax4(m0); m1 = xmax4(m1);
  const float em = __builtin_amdgcn_exp2f((NEG - m0) * L2E);
  float t0 = 4.f * em, t1 = 0.f;
#pragma unroll
  for (int e = 0; e < 4; ++e) {
    s00[e] = __builtin_amdgcn_exp2f((s00[e] - m0) * L2E); s10[e] = __builtin_amdgcn_exp2f((s10[e] - m1) * L2E); s11[e] = __builtin_amdgcn_exp2f((s11[e] - m1) * L2E);
    t0 += s00[e]; t1 += s10[e] + s11[e];
  }
  p.a[0] = s00; p.b[0] = (f32x4){em, em, em, em}; p.a[1] = s10; p.b[1] = s11; p.mx[0] = m0; p.mx[1] = m1; p.tx[0] = t0; p.tx[1] = t1;
}
__device__ __forceinline__ void xa_pw(u32x4& px, u32x2& pm, f32x4 sm, f32x4 sa, f32x4 sb, float mx, float tx, int lg) {
  const float scale = 0.08838834764831845f, L2E = 1.4426950408889634f;
  sm = rnd2(sm, scale);
  asm("" : "+v"(sm));
  float m = mx;
#pragma unroll
  for (int e = 0; e < 4; ++e) { sm[e] = lg >= 2 ? -INFINITY : sm[e]; m = __builtin_fmaxf(m, sm[e]); }
  m = xmax4(m);
  const float al = __builtin_amdgcn_exp2f((mx - m) * L2E);
  float ts = tx * al;
#pragma unroll
  for (int e = 0; e < 4; ++e) { sm[e] = __builtin_amdgcn_exp2f((sm[e] - m) * L2E); ts += sm[e]; }
  const float r = __builtin_amdgcn_rcpf(xsum4(ts)), ra = r * al;
  px = (u32x4){pack2(sa[0] * ra, sa[1] * ra), pack2(sa[2] * ra, sa[3] * ra), pack2(sb[0] * ra, sb[1] * ra), pack2(sb[2] * ra, sb[3] * ra)};
  pm = (u32x2){pack2(sm[0] * r, sm[1] * r), pack2(sm[2] * r, sm[3] * r)};
}
__device__ __forceinline__ void xa_pv(const u32x4 (&v)[8], const u32x2 (&vm)[8], u32x4 px, u32x2 pm, __amdgpu_buffer_rsrc_t rsf, int vo, int so) {
  f32x4 o[8];
#define LX(i) "v_mfma_f32_16x16x32_bf16 %" #i ", %[v" #i "], %[px], 0\n\t"
#define LM(i) "v_mfma_f32_16x16x16_bf16 %" #i ", %[m" #i "], %[pm], %" #i "\n\t"
  asm volatile("s_nop 1\n\t" LX(0) LX(1) LX(2) LX(3) LX(4) LX(5) LX(6) LX(7) LM(0) LM(1) LM(2) LM(3) LM(4) LM(5) LM(6) LM(7) "s_nop 7"
               : "=&v"(o[0]), "=&v"(o[1]), "=&v"(o[2]), "=&v"(o[3]), "=&v"(o[4]), "=&v"(o[5]), "=&v"(o[6]), "=&v"(o[7])
               : [v0] "v"(v[0]), [v1] "v"(v[1]), [v2] "v"(v[2]), [v3] "v"(v[3]), [v4] "v"(v[4]), [v5] "v"(v[5]), [v6] "v"(v[6]), [v7] "v"(v[7]), [px] "v"(px),
                 [m0] "v"(vm[0]), [m1] "v"(vm[1]), [m2] "v"(vm[2]), [m3] "v"(vm[3]), [m4] "v"(vm[4]), [m5] "v"(vm[5]), [m6] "v"(vm[6]), [m7] "v"(vm[7]), [pm] "v"(pm));
#pragma unroll
  for (int pr = 0; pr < 4; ++pr)
    __builtin_amdgcn_raw_buffer_store_b128((u32x4){pack2(o[2 * pr][0], o[2 * pr][1]), pack2(o[2 * pr][2], o[2 * pr][3]), pack2(o[2 * pr + 1][0], o[2 * pr + 1][1]), pack2(o[2 * pr + 1][2], o[2 * pr + 1][3])},
                                           rsf, vo, so + pr * 1024, 16);
}
__device__ __forceinline__ u32 xa_chk(const u32x4 (&km)[4], const u32x2 (&vm)[8]) {
  u32 ck = max(max(max4u(km[0]), max4u(km[1])), max(max4u(km[2]), max4u(km[3])));
#pragma unroll
  for (int i = 0; i < 8; ++i) ck = max(ck, max(vm[i][0], vm[i][1]));
  return ck;
}
#define XSG 56
#define XST 0
#define XS0 48
#define XL_D 26
#define NAW 64
__device__ __forceinline__ void a_role(__amdgpu_buffer_rsrc_t rsx, __amdgpu_buffer_rsrc_t rsf, int sh, int lim, int wi, int done, int lane, int fb, u32 tag) {
  const int lr = lane & 15, lg = lane >> 4;
  const int okm = (lg * 8 + (lr & 7)) * 16, ovm = lg < 2 ? (lg * 16 + lr) * 8 : 0x7ffffff0;
  int rp = 0, xp = -1, spin = 0, pub = done, miss = 0, tp = 200;
  u32 tseen = 0, tq0 = 0, tq1 = 0, tq2 = 0, tq3 = 0; (void)tq0; (void)tq1; (void)tq2; (void)tq3;
#define XA_WAITX(s_)                                                                  \
  while (xp <= ((s_) >> 5)) {                                                                                          \
    asm volatile("" ::: "memory");                                                                                     \
    int pv = min(xprog(__builtin_amdgcn_raw_buffer_load_b32(rsx, lane * 128, PGO_X, 16), tag), xprog(__builtin_amdgcn_raw_buffer_load_b32(rsx, (64 + (lane & 31)) * 128, PGO_X, 16), tag)); \
    _Pragma("unroll") for (int o_ = 1; o_ < 64; o_ *= 2) pv = min(pv, __shfl_xor(pv, o_));                             \
    xp = __builtin_amdgcn_readfirstlane(pv);                                                                           \
    if (xp > ((s_) >> 5) || ++spin > SPIN_CAP) break;                                                                  \
    asm volatile("s_sleep 16" ::: "memory");                                                                           \
  }
#define XA_PUB() if (pub != done) { pub = done; asm volatile("s_nop 4\n\tbuffer_store_dword %0, %1, %2, %3 offen sc1" :: "v"(done), "v"(lane < 2 ? lane * 4 : 0x7ffffff0), "s"(rsx), "s"(PGO_A + wi * 128) : "memory"); }
#define XA_NOW ((u32)__builtin_amdgcn_s_memrealtime())
#define XA_SEEN(r_, l_) { const u32 n_ = XA_NOW; tseen = n_ - ((n_ - (r_ >> 16)) & 0x7fffu) + XA_LAG; tp = max(r_ & 1023, 1); rp = l_; }
#define XA_KM \
      _Pragma("unroll") for (int i = 0; i < 4; ++i) km[i] = __builtin_amdgcn_raw_buffer_load_b128(rsf, okm, FO_KM + (sh * 4 + i) * 512, 16); \
      _Pragma("unroll") for (int i = 0; i < 8; ++i) vm[i] = __builtin_amdgcn_raw_buffer_load_b64(rsf, ovm, FO_VM + (sh * 8 + i) * 256, 16);
#define XPRE 2
#define XA_STEP(CUR, NXT, ODD) \
  { \
    const int seg = sh >> 5; \
    int tries = (seg >= XS0 && tseen) ? 3 : 0; \
    while (!pre) {               \
      if (rp > seg) tries = 0; \
      else if (tries) { \
        const u32 due = tseen + (u32)(seg - rp) * tp + XL_D - XA_LAG + (seg < XSG ? stg : 0); \
        for (int i_ = 0; i_ < 4000 && (int)(due - XA_NOW) > 0; ++i_) asm volatile("s_sleep 1" ::: "memory"); \
      } else while (true) { \
        int want = seg + 1; \
        if (tseen) { \
          const u32 due = tseen + (u32)(want - rp) * tp + (seg < XSG ? stg : 0); \
          for (int i_ = 0; i_ < 4000 && (int)(due - XA_NOW) > 0; ++i_) asm volatile("s_sleep 2" ::: "memory"); \
          if (!miss) want = min(max(want, rp + max((int)(XA_NOW - tseen), 0) / tp), NSEG - 1); \
        } \
        asm volatile("" ::: "memory"); \
        const int r_ = __builtin_amdgcn_readfirstlane(__builtin_amdgcn_raw_buffer_load_b32(rsx, 0, pgr + want * 128, 16)); \
        if (r_ >= 0) { XA_SEEN(r_, want) miss = 0; break; } \
        if (++spin > SPIN_CAP) break; \
        if (miss++) asm volatile("s_sleep 20" ::: "memory"); \
      } \
      asm volatile("" ::: "memory"); \
      XA_DBG2 \
      XA_KM \
        \
      const int pk_ = min(rp + max((int)(XA_NOW - tseen), 0) / tp, NSEG - 1); \
      const u32 pv_ = __builtin_amdgcn_raw_buffer_load_b32(rsx, 0, pgr + pk_ * 128, 16); \
      asm volatile("" ::: "memory"); \
      __builtin_amdgcn_s_waitcnt(0x0F70);          \
      asm volatile("" ::: "memory"); \
      { const int r_ = __builtin_amdgcn_readfirstlane(pv_); if (r_ >= 0) XA_SEEN(r_, pk_) } \
      if (!tries || __ballot(xa_chk(km, vm) == SENT) == 0) break; \
      --tries; \
    } \
    XA_DBG3 \
    __builtin_amdgcn_sched_barrier(0); \
    f32x4 s0 = (f32x4){0, 0, 0, 0}, s1 = s0; \
    _Pragma("unroll") for (int ks = 0; ks < 4; ++ks) { s0 = MF16(km[ks], CUR.q[0][ks], s0); s1 = MF16(km[ks], CUR.q[1][ks], s1); } \
    u32x4 px0, px1; u32x2 pm0, pm1; \
    xa_pw(px0, pm0, s0, P.a[0], P.b[0], P.mx[0], P.tx[0], lg); \
    xa_pw(px1, pm1, s1, P.a[1], P.b[1], P.mx[1], P.tx[1], lg); \
    xa_pv(CUR.v, vm, px0, pm0, rsf, lane * 16, FO_O + (seg * 256 + (sh & 31) * 4) * 1024); \
    xa_pv(CUR.v, vm, px1, pm1, rsf, lane * 16, FO_O + (seg * 256 + 128 + (sh & 31) * 4) * 1024); \
    ++done; \
    XA_DBG XA_DBG5 \
    __builtin_amdgcn_sched_barrier(0); \
    sh += NAW; \
      \
    pre = (!XPRE || (ODD && sh >= lim)) ? 0 : rp > seg + 2 ? 1 : (XPRE > 1 && seg + 2 >= XS0 && tseen && (int)(tseen + (u32)(seg + 2 - rp) * tp + XL_D - XA_LAG - XA_NOW) <= 0) ? 2 : 0; \
    asm volatile("" ::: "memory"); \
    if (pre) { XA_DBG2 XA_KM } \
    const int pk2 = pre ? min(rp + max((int)(XA_NOW - tseen), 0) / tp, NSEG - 1) : rp; \
    const u32 pv2 = __builtin_amdgcn_raw_buffer_load_b32(rsx, pk2 > rp ? 0 : 0x7ffffff0, pgr + pk2 * 128, 16); \
    asm volatile("" ::: "memory"); \
    xa_pre(P, NXT, lane); \
    __builtin_amdgcn_sched_barrier(0); \
    XA_WAITX(min(sh + NAW, lim - 1)) \
    asm volatile("" ::: "memory"); \
    xa_load(CUR, rsf, sh + NAW, sh + NAW < lim ? lane * 16 : 0x7ffffff0); \
    asm volatile("" ::: "memory"); \
    __builtin_amdgcn_s_waitcnt(0x4F78);          \
    asm volatile("" ::: "memory"); \
    XA_PUB() \
    if (pk2 > rp) { const int r_ = __builtin_amdgcn_readfirstlane(pv2); if (r_ >= 0) XA_SEEN(r_, pk2) } \
    if (pre == 2 && __ballot(xa_chk(km, vm) == SENT) != 0) pre = 0; \
  }
#define XA_LAST 22
#define XA_LAG 30
#define XA_DBG
#define XA_DBG2
#define XA_DBG3
#define XA_DBG4
#define XA_DBG5
  const int pgr = PGO_R + (__builtin_amdgcn_readfirstlane(wait_start(rsx, fb, tag, true)[2]) & 8192);
  XaOps A, B;
  XaPre P;
  u32x4 km[4]; u32x2 vm[8];
  int pre = 0;
  static_assert(NAW == 64 && (NSEG - 4) * NH % (2 * NAW) == 0, "two segments per turn");
  const int stg = ((wi * 5) & 31) * XST;
  XA_WAITX(sh)
  asm volatile("" ::: "memory");
  xa_load(A, rsf, sh, lane * 16);
  xa_pre(P, A, lane);
  XA_WAITX(sh + NAW)
  asm volatile("" ::: "memory");
  xa_load(B, rsf, sh + NAW, lane * 16);
  do { XA_STEP(A, B, 0) XA_STEP(B, A, 1) } while (sh < lim);
  asm volatile("s_waitcnt vmcnt(0)" ::: "memory");
  XA_PUB()
}

#define GL_D 130
__device__ __forceinline__ void a_late(__amdgpu_buffer_rsrc_t rsx, __amdgpu_buffer_rsrc_t rsf, int t, int lane, int fb, u32 tag) {
  const int lr = lane & 15, lg = lane >> 4, rt = t & 1, sh = (NSEG - 4) * NH + (t >> 1), seg = sh >> 5;
  const float scale = 0.08838834764831845f, L2E = 1.4426950408889634f, NEG = -1000000000.0f;
  int xp = -1, spin = 0;
  const int pgr = PGO_R + (__builtin_amdgcn_readfirstlane(wait_start(rsx, fb, tag, true)[2]) & 8192);
  const int r40 = __builtin_amdgcn_readfirstlane(__builtin_amdgcn_raw_buffer_load_b32(rsx, 0, pgr + 40 * 128, 16));
  XA_WAITX(sh)
  asm volatile("" ::: "memory");
  u32x4 q[4], v[8];
  f32x4 sa = (f32x4){0, 0, 0, 0}, sb = sa;
  float mx = -3.0e38f, tx = 0.f;
  {
    u32x4 k0[4], k1[4];
#pragma unroll
    for (int i = 0; i < 4; ++i) {
      q[i] = __builtin_amdgcn_raw_buffer_load_b128(rsf, lane * 16, FO_Q + (sh * 8 + rt * 4 + i) * 1024, 16);
      k0[i] = __builtin_amdgcn_raw_buffer_load_b128(rsf, lane * 16, FO_K + (sh * 8 + i) * 1024, 16);
      k1[i] = __builtin_amdgcn_raw_buffer_load_b128(rsf, lane * 16, FO_K + (sh * 8 + 4 + i) * 1024, 16);
    }
#pragma unroll
    for (int dt = 0; dt < 8; ++dt) v[dt] = __builtin_amdgcn_raw_buffer_load_b128(rsf, lane * 16, FO_V + (sh * 8 + dt) * 1024, 16);
#pragma unroll
    for (int ks = 0; ks < 4; ++ks) { sa = MF16(k0[ks], q[ks], sa); sb = MF16(k1[ks], q[ks], sb); }
    sa = rnd2(sa, scale); sb = rnd2(sb, scale);
    asm volatile("" : "+v"(sa), "+v"(sb));
#pragma unroll
    for (int e = 0; e < 4; ++e) {
      sa[e] = 4 * lg + e > lr + 16 * rt ? NEG : sa[e]; sb[e] = 16 + 4 * lg + e > lr + 16 * rt ? NEG : sb[e];
      mx = __builtin_fmaxf(mx, __builtin_fmaxf(sa[e], sb[e]));
    }
    mx = xmax4(mx);
#pragma unroll
    for (int e = 0; e < 4; ++e) { sa[e] = __builtin_amdgcn_exp2f((sa[e] - mx) * L2E); sb[e] = __builtin_amdgcn_exp2f((sb[e] - mx) * L2E); tx += sa[e] + sb[e]; }
  }
  const int okm = (lg * 8 + (lr & 7)) * 16, ovm = lg < 2 ? (lg * 16 + lr) * 8 : 0x7ffffff0;
  const int ofl = seg + 1 < NSEG ? pgr + (seg + 1) * 128 : PGO_D + ((sh & 31) * 4 + (lane & 3)) * 128;
  const int owd = lane ? 0x7ffffff0 : PGO_A + (64 + (t >> 1)) * 128 + 4 * rt;
  u32 tq0 = 0, tq1 = 0, tq2 = 0; (void)tq0; (void)tq1; (void)tq2;
  int att = 0, seen = 1;
  while (true) {
    asm volatile("" : "+s"(att));
    if (att == 1) {
      u32 n = (u32)__builtin_amdgcn_s_memrealtime(), due = n;
      if (r40 >= 0) due = n - ((n - (r40 >> 16)) & 0x7fffu) + XA_LAG + (seg - 41) * (r40 & 1023);
      int r;
      while (true) {
        for (int i = 0; i < 4000 && (int)(due - (u32)__builtin_amdgcn_s_memrealtime()) > 0; ++i) asm volatile("s_sleep 2" ::: "memory");
        asm volatile("" ::: "memory");
        r = __builtin_amdgcn_readfirstlane(__builtin_amdgcn_raw_buffer_load_b32(rsx, 0, pgr + (seg - 1) * 128, 16));
        if (r >= 0 || ++spin > SPIN_CAP) break;
        asm volatile("s_sleep 10" ::: "memory");
      }
      n = (u32)__builtin_amdgcn_s_memrealtime();
      due = n - ((n - (r >> 16)) & 0x7fffu) + (r & 1023) + XL_D - (seg + 1 < NSEG ? 0 : XA_LAST);
      __builtin_amdgcn_raw_buffer_store_b32(((n + max((int)(due - n), 0) + GL_D) & ~1u) | 0x80000000u, rsx, owd, 0, 16);
      for (int i = 0; i < 4000 && (int)(due - (u32)__builtin_amdgcn_s_memrealtime()) > 0; ++i) asm volatile("s_sleep 1" ::: "memory");
      XA_DBG2
    } else if (att > 6) asm volatile("s_sleep 20" ::: "memory");
    const int dr = att ? 0 : 0x7ffffff0;
    asm volatile("" ::: "memory");
    u32x4 km[4]; u32x2 vm[8];
#pragma unroll
    for (int i = 0; i < 4; ++i) km[i] = __builtin_amdgcn_raw_buffer_load_b128(rsf, okm | dr, FO_KM + (sh * 4 + i) * 512, 16);
#pragma unroll
    for (int i = 0; i < 8; ++i) vm[i] = __builtin_amdgcn_raw_buffer_load_b64(rsf, ovm | dr, FO_VM + (sh * 8 + i) * 256, 16);
    const u32 fl = __builtin_amdgcn_raw_buffer_load_b32(rsx, att > 1 ? ofl : 0x7ffffff0, 0, 16);
    f32x4 sm = (f32x4){0, 0, 0, 0};
#pragma unroll
    for (int ks = 0; ks < 4; ++ks) sm = MF16(km[ks], q[ks], sm);
    const u32 ck = xa_chk(km, vm);
    u64 bad = __ballot(ck == SENT), nf = __ballot(fl == SENT);
    asm volatile("" : "+s"(bad), "+s"(nf));
    if (!seen && bad != 0) {
      seen = (att > 1 && nf == 0) || ++spin > SPIN_CAP;
      ++att;
      continue;
    }
    XA_DBG3
    u32x4 px; u32x2 pm;
    xa_pw(px, pm, sm, sa, sb, mx, tx, lg);
    xa_pv(v, vm, px, pm, rsf, (lane * 16) | dr, FO_O + (seg * 256 + rt * 128 + (sh & 31) * 4) * 1024);
    XA_DBG
    asm volatile("s_nop 4\n\ts_waitcnt vmcnt(0)\n\tbuffer_store_dword %0, %1, %2, 0 offen sc1" :: "v"(32), "v"(owd | dr), "s"(rsx) : "memory");
    if (att) break;
    att = 1; seen = 0;
  }
}

typedef __attribute__((ext_vector_type(32))) unsigned int u32x32;
typedef __attribute__((ext_vector_type(16))) unsigned int u32x16;
typedef __attribute__((ext_vector_type(8))) unsigned int u32x8;
#define STR2(x) #x
#define STR(x) STR2(x)
#define RNG(f, b, n) f "[" STR(b) ":" STR((b) + (n)-1) "]"
#define REG(b) "v[" STR(b) "]"
#define LR_RED 16384
#define LR_CTL LR_RED
#define LR_STG 32768
#define L_RSTAMP LR_STG
#define STAMPV(i, v)
#define EXF (NH * 8 * 1024)
#define EXK 65536
#define OOR 0x7ffffff0

__device__ __forceinline__ u32 lds_word(int addr) {
  u32 v;
  asm volatile("ds_read_b32 %0, %1\n\ts_waitcnt lgkmcnt(0)" : "=v"(v) : "v"(addr) : "memory");
  return __builtin_amdgcn_readfirstlane(v);
}
__device__ __forceinline__ int fresh_lane() {
  int l;
  asm volatile("v_mbcnt_lo_u32_b32 %0, -1, 0\n\tv_mbcnt_hi_u32_b32 %0, -1, %0" : "=v"(l));
  return l;
}
__device__ __forceinline__ u32 rt32() { return (u32)__builtin_amdgcn_s_memrealtime(); }
#define SYO(s) (XSYN + flip + (s) * 64)
#define TKS 0.85f
#define TLB 1
#define TLC 1
#define TLP 2
#define KG0 0
#define CLS0 0u
#define APC 5
#define ADL 12
#define CALN 2
#define TE1 3
#define TE2 1
#define TE3 1
#define XTH 4
#define TS3 42
#define TE0 45
#define TD0 170
#define TD 120
#define PHASE 1
#define XSK 0
#define CLKS(T, R, X) WLG(2) "s_sub_u32 s49, " T ", " R "\n\t" "s_cmp_le_i32 s49, " STR(3 * PHASE) "\n\t" "s_cbranch_scc1 " X "\n\t"
#define CLKH(OFF) WLG(0) "s_memrealtime s[72:73]\n\t" "s_add_u32 s50, s43, s44\n\t" "s_add_u32 s50, s50, " OFF "\n\t" "s_nop 1\n\t" "s_memrealtime s[74:75]\n\t"
#define CLKL(L, T)   L ":\n\t" "s_memrealtime s[76:77]\n\t" CLKS(T, "s72", L "x") "s_memrealtime s[72:73]\n\t" CLKS(T, "s74", L "x")     \
                     "s_memrealtime s[74:75]\n\t" CLKS(T, "s76", L "x") "s_branch " L "\n" \
                     L "x:\n\t" CLKP(L)
#define CLKW(L, OFF) CLKH(OFF) "s_nop 3\n" CLKL(L, "s50")
#define CLKP(L) "s_cmp_lt_i32 s49, 1\n\t" "s_cbranch_scc1 " L "y\n\t" "s_nop " STR(PHN) "\n\t" "s_cmp_lt_i32 s49, 2\n\t" "s_cbranch_scc1 " L "y\n\t" "s_nop " STR(PHN) "\n\t" \
                "s_cmp_lt_i32 s49, 3\n\t" "s_cbranch_scc1 " L "y\n\t" "s_nop " STR(PHN) "\n\t" "s_nop 0\n\t" "s_branch " L "y\n\t" "s_nop 0\n" L "y:\n\t"
#define PHN 3
#define REL(L) "s_barrier\n" L ":\n\t"
#define RELV(L) "s_cbranch_vccnz " L "\n\t" REL(L)
#define RELN(L) "s_cmp_eq_u32 %[wv], 0\n\t" "s_cbranch_scc1 " L "a\n\t" "s_cmp_eq_u32 %[wv], 1\n\t" "s_cbranch_scc1 " L "b\n\t" \
                "s_barrier\n\t" "s_nop " STR(PD2) "\n\t" "s_branch " L "\n" L "b:\n\t" "s_barrier\n\t" "s_branch " L "\n" L "a:\n\t" "s_barrier\n" L ":\n\t"
#define PD2 2
#define TS_(n) "s_memtime s[80+2*" #n ":81+2*" #n "]\n\t"
#define TSA(n)
#define TSB(n)
#define TSW(n)
#define TVM 12
template <int NTL, int KS>
__device__ __forceinline__ void tr_chunk(const u16* S, int lr, int lg, u32 (&r)[NTL][KS][8]) {
  const int q = lr >> 2, p = lr & 3;
#pragma unroll
  for (int t = 0; t < NTL; ++t)
#pragma unroll
    for (int i = 0; i < KS; ++i)
#pragma unroll
      for (int kb = 0; kb < 2; ++kb) {
        const u16* base = S + (64 * i + 32 * kb + 16 * (lg >> 1) + 4 * (lg & 1) + (q & 1) + 8 * (q >> 1)) * (16 * NTL) + 16 * t + 4 * p;
        const u32x2 t0 = __builtin_bit_cast(u32x2, __builtin_amdgcn_ds_read_tr16_b64_v4i16((LAS v4i16_t*)(base)));
        const u32x2 t1 = __builtin_bit_cast(u32x2, __builtin_amdgcn_ds_read_tr16_b64_v4i16((LAS v4i16_t*)(base + 2 * 16 * NTL)));
        r[t][i][4 * kb + 0] = t0[0]; r[t][i][4 * kb + 1] = t0[1]; r[t][i][4 * kb + 2] = t1[0]; r[t][i][4 * kb + 3] = t1[1];
      }
}
#define SMF(acc, a, bf, bi) "v_smfmac_f32_16x16x64_bf16 " RNG("v", acc, 4) ", " RNG("v", a, 4) ", " RNG(bf, bi, 8) ", v34\n\t"
#define ZER(r) "v_mov_b64 " RNG("v", r, 2) ", 0\n\t" "v_mov_b64 " RNG("v", (r) + 2, 2) ", 0\n\t"
#define GLD(r, o) "s_xor_b32 s49, s50, " STR(o) "\n\t" "buffer_load_dwordx4 " RNG("v", r, 4) ", v32, %[rs], s49 offen sc1\n\t"
#define GL1(r, o) "s_xor_b32 s49, s50, " STR(o) "\n\t" "buffer_load_dwordx4 " RNG("v", r, 4) ", v33, %[rs], s49 offen sc1\n\t"
#define GL2(r, o) GL1(r, 4096 + (o))
#define MXA0(r) "v_max3_u32 v63, " REG(r) ", " REG((r) + 1) ", " REG((r) + 1) "\n\t"
#define MXA(r) "v_max3_u32 v63, v63, " REG(r) ", " REG((r) + 1) "\n\t"
#define MXB(r) "v_max3_u32 v63, v63, " REG((r) + 2) ", " REG((r) + 3) "\n\t"
#define CMPB "v_cmp_eq_u32 vcc, s54, v63\n\t"
#define PSA(r) "v_pk_add_f32 " RNG("v", r, 2) ", " RNG("v", r, 2) ", " RNG("v", (r) + 2, 2) "\n\t"
#define PSW(r, o) "ds_write_addtid_b32 " REG(r) " offset:" STR(o) "\n\t"
#define RDQ(r, o) "ds_read_b128 " RNG("v", r, 4) ", v37 offset:" STR(o) "\n\t"
#define SQ(t, i) SMF(64 + 4 * (t), 96 + 4 * (i), "a", ((i) * 4 + (t)) * 8)
#define SV(t, i) SMF(24 + 4 * (t), 96 + 4 * (i), "a", 128 + ((i) * 2 + (t)) * 8)
#define SVP(t, i) SV(t, i)
#define SW(a, q, i) SMF((a) + 4 * (q), 96 + 4 * (i), "v", 128 + ((i) * 2 + (q)) * 8)
#define WVM(n) "s_waitcnt vmcnt(" STR(n) ")\n\t"
#define WLG(n) "s_waitcnt lgkmcnt(" STR(n) ")\n\t"
#define WLGF(n) WLG(n)
#define PACE 7
#define PC "s_nop " STR(PACE) "\n\t"
#define PC6
#define PC7
#define WBLK(a, n, i, M0) WVM((n) + 7 - (i)) SW(a, 0, i) M0(96 + 4 * (i)) SW(a, 1, i) MXB(96 + 4 * (i))
#define CL8(a, b, c, d, e, f, g, h) "v" #a, "v" #b, "v" #c, "v" #d, "v" #e, "v" #f, "v" #g, "v" #h
#define CL64 CL8(64, 65, 66, 67, 68, 69, 70, 71)
#define CL72 CL8(72, 73, 74, 75, 76, 77, 78, 79)
#define CL80 CL8(80, 81, 82, 83, 84, 85, 86, 87)
#define CL88 CL8(88, 89, 90, 91, 92, 93, 94, 95)
#define CL96 CL8(96, 97, 98, 99, 100, 101, 102, 103), CL8(104, 105, 106, 107, 108, 109, 110, 111)
#define CL112 CL8(112, 113, 114, 115, 116, 117, 118, 119)
#define CL120 CL8(120, 121, 122, 123, 124, 125, 126, 127)
#define PKA(d, a, b) "v_pk_add_f32 " RNG("v", d, 2) ", " RNG("v", a, 2) ", " RNG("v", b, 2) "\n\t"
#define CNT(o) STR(4096 + 64 * (o))
#define ARRIVE(o) "ds_add_u32 v45, v39 offset:" CNT(o) "\n\t"

#define EXTRA "v_readlane_b32 s69, v35, s45\n\t" "s_and_b32 s56, s58, 1023\n\t" "s_bfe_u32 s57, s58, 0xa000a\n\t" "s_lshr_b32 s55, s58, 20\n\t" "s_add_u32 s66, s56, s69\n\t" "s_add_u32 s67, s57, s69\n\t" "s_add_u32 s67, s67, s69\n\t"
#define TEX(s) ((s) == 0 ? TE0 : ((s) < 4 ? TE1 : ((s) < 12 ? TE2 : ((s) < TS3 ? TE3 : 0))))
#define R2BASE "s_bitcmp1_b32 s45, 0\n\t" "s_cselect_b32 s71, " STR(XR2S) ", 0\n\t" "s_add_u32 s71, s71, %[r2]\n\t"
#define X4B 0
#define X4C 0
#define STB "buffer_store_dwordx2 v[116:117], v11, %[rs], s36 offen sc1\n"
#define STC "buffer_store_dwordx2 v[80:81], v17, %[rs], s36 offen sc1\n\t"
#define R2TEXT                                                                                                     \
              TSB(0)                                                                                               \
              "buffer_load_dwordx4 v[64:67], v43, %[rs], s71 offen sc1\n\t"                                      \
              "buffer_load_dwordx4 v[68:71], v8, %[rs], s71 offen sc1\n\t"                                       \
              "buffer_load_dwordx4 v[76:79], v9, %[rs], s71 offen sc1\n\t"                                       \
              "buffer_load_dwordx4 v[60:63], v10, %[rs], s71 offen sc1\n\t"                                      \
              "buffer_store_dwordx4 v[72:75], v47, %[rf], s41 offen sc1\n\t"                                     \
              "s_cbranch_vccnz .Lvw%=\n\t" VPOST ARRIVE(12) "s_barrier\n" ".Lvw%=:\n\t"                             \
              "ds_read2_b32 v[96:97], v12 offset0:0 offset1:4\n\t" "ds_read2_b32 v[98:99], v12 offset0:32 offset1:36\n\t" \
              "ds_read2_b32 v[100:101], v13 offset0:0 offset1:4\n\t" "ds_read2_b32 v[102:103], v13 offset0:32 offset1:36\n\t" \
              "ds_read2_b32 v[104:105], v14 offset0:0 offset1:4\n\t" "ds_read2_b32 v[106:107], v14 offset0:32 offset1:36\n\t" \
              "ds_read2_b32 v[108:109], v15 offset0:0 offset1:4\n\t" "ds_read2_b32 v[110:111], v15 offset0:32 offset1:36\n\t" \
              "v_mov_b64 v[112:113], v[80:81]\n\t" "v_mov_b64 v[114:115], v[82:83]\n\t" "s_mov_b32 s65, 0\n\t"     \
              "v_mov_b32_dpp v112, v84 row_ror:8 row_mask:0xf bank_mask:0xc\n\t" "v_mov_b32_dpp v113, v85 row_ror:8 row_mask:0xf bank_mask:0xc\n\t" \
              "v_mov_b32_dpp v114, v86 row_ror:8 row_mask:0xf bank_mask:0xc\n\t" "v_mov_b32_dpp v115, v87 row_ror:8 row_mask:0xf bank_mask:0xc\n\t" \
              "v_pk_mul_f32 v[112:113], v[112:113], s[62:63] op_sel_hi:[1,0]\n\t" "v_pk_mul_f32 v[114:115], v[114:115], s[62:63] op_sel_hi:[1,0]\n\t" \
              WLG(0)                                                                                               \
              PKA(96, 96, 100) PKA(98, 98, 102) PKA(104, 104, 108) PKA(106, 106, 110) PKA(96, 96, 104) PKA(98, 98, 106) \
              "v_cvt_pk_bf16_f32 v96, v96, 0\n\t" "v_cvt_pk_bf16_f32 v97, v97, 0\n\t" "v_cvt_pk_bf16_f32 v98, v98, 0\n\t" "v_cvt_pk_bf16_f32 v99, v99, 0\n\t" \
              "v_accvgpr_write_b32 a194, v96\n\t" "v_accvgpr_write_b32 a202, v97\n\t" "v_accvgpr_write_b32 a198, v98\n\t" "v_accvgpr_write_b32 a206, v99\n\t" \
              TSB(1) WVM(4) TSB(2) \
              "v_permlane32_swap_b32 v64, v66\n\t" "v_permlane32_swap_b32 v65, v67\n\t" \
              "v_pk_add_f32 v[92:93], v[64:65], v[66:67]\n\t" \
              WVM(3) SFM(68) \
              "v_permlane16_swap_b32 v92, v93\n\t" \
              "v_add_f32 v94, v92, v93\n\t" \
              WVM(2) "v_mul_f32 v94, s62, v94\n\t" SFM(76) \
              "v_exp_f32 v92, v94\n\t" \
              WVM(1) TSB(3) SFM(60) \
                \
              ".Lex%=:\n\t" \
              "v_exp_f32 v88, v112\n\t" "v_exp_f32 v89, v113\n\t" "v_cvt_pk_bf16_f32 v58, v92, 0\n\t" "v_exp_f32 v90, v114\n\t" "v_exp_f32 v91, v115\n\t" \
              "v_cvt_pk_bf16_f32 v56, v88, v89\n\t" "v_cvt_pk_bf16_f32 v57, v90, v91\n\t" \
              TSB(4) ROR8(50, 58) ROR8(48, 56) \
              "v_mfma_f32_16x16x32_bf16 v[88:91], v[52:55], v[56:59], 0\n\t" \
              ROR8(49, 57) "s_nop 1\n\t" \
              "v_mfma_f32_16x16x32_bf16 v[116:119], a[192:195], v[56:59], 0\n\t" \
              "s_nop 2\n\t" \
              "v_mfma_f32_16x16x32_bf16 v[116:119], a[196:199], v[48:51], v[116:119]\n\t" \
              "v_add_f32_dpp v92, v88, v88 row_ror:8 row_mask:0xf bank_mask:0xf\n\t" "v_subrev_u32 v89, 0x33800000, v92\n\t" \
              "v_mfma_f32_16x16x32_bf16 v[124:127], a[204:207], v[56:59], 0\n\t" \
              "v_rcp_f32 v93, v92\n\t" \
              "v_mfma_f32_16x16x32_bf16 v[124:127], a[200:203], v[48:51], v[124:127]\n\t" \
              TSB(5) "v_cmp_le_u32 vcc, 0x20000000, v89\n\t" "s_nop 0\n\t" \
              "s_cbranch_vccnz .Lmx%=\n\t" \
              OTAIL STB TSB(6)
#define SFM(r) "v_pk_fma_f32 v[112:113], " RNG("v", r, 2) ", s[62:63], v[112:113] op_sel_hi:[1,0,1]\n\t" "v_pk_fma_f32 v[114:115], " RNG("v", (r) + 2, 2) ", s[62:63], v[114:115] op_sel_hi:[1,0,1]\n\t"
#define ROR8(d, s) "v_mov_b32_dpp " REG(d) ", " REG(s) " row_ror:8 row_mask:0xf bank_mask:0xf\n\t"
#define OTAIL "v_pk_mul_f32 v[116:117], v[116:117], v[92:93] op_sel:[0,1]\n\t" "v_pk_mul_f32 v[118:119], v[118:119], v[92:93] op_sel:[0,1]\n\t" \
              "v_cvt_pk_bf16_f32 v116, v116, v117\n\t" "v_cvt_pk_bf16_f32 v117, v118, v119\n\t" \
              "s_mov_b64 exec, s[78:79]\n\t" \
              "v_pk_mul_f32 v[124:125], v[124:125], v[92:93] op_sel:[0,1]\n\t" "v_pk_mul_f32 v[126:127], v[126:127], v[92:93] op_sel:[0,1]\n\t" \
              "v_cvt_pk_bf16_f32 v116, v124, v125\n\t" "v_cvt_pk_bf16_f32 v117, v126, v127\n\t" \
              "s_mov_b64 exec, -1\n\t"
#define MX3(a, b) "v_max3_u32 v95, v95, " REG(a) ", " REG(b) "\n\t"
#define R2MAX ".Lmx%=:\n\t" "v_max_u32 v95, v60, v61\n\t" MX3(62, 63) MX3(64, 65) MX3(66, 67) MX3(68, 69) MX3(70, 71) MX3(76, 77) MX3(78, 79) \
              "v_cmp_eq_u32 vcc, s54, v95\n\t" "s_cbranch_vccnz .Lfail%=\n\t" OTAIL \
              "s_cmp_lg_u32 s65, 0\n\t" "s_cbranch_scc1 .Lend%=\n\t" "s_mov_b32 s65, 1\n\t" \
              "v_max3_f32 v93, v112, v113, v94\n\t" \
              "v_max3_f32 v93, v93, v114, v115\n\t" \
              "s_nop 1\n\t" \
              "v_max_f32_dpp v92, v93, v93 row_ror:8 row_mask:0xf bank_mask:0xf\n\t" \
              "v_max_f32_dpp v88, v93, v93 row_ror:8 row_mask:0xf bank_mask:0xf\n\t" \
              "s_nop 1\n\t" \
              "v_permlane16_swap_b32 v92, v88\n\t" \
              "v_max_f32 v93, v92, v88\n\t" "v_max_f32 v92, v92, v88\n\t" \
              "s_nop 1\n\t" \
              "v_permlane32_swap_b32 v93, v92\n\t" \
              "v_max_f32 v93, v93, v92\n\t" \
              "v_sub_f32 v94, v94, v93\n\t" "v_sub_f32 v112, v112, v93\n\t" "v_exp_f32 v92, v94\n\t" "v_sub_f32 v113, v113, v93\n\t" "v_sub_f32 v114, v114, v93\n\t" "v_sub_f32 v115, v115, v93\n\t" \
              "s_branch .Lex%=\n"
#define PS1(r) "v_add_f32 " REG(r) ", " REG(r) ", " REG((r) + 2) "\n\t"
#define NOP(n) "s_nop " STR(n) "\n\t"
#define PCA 13
#define PDA 8
#define PDC 8
#define PDF 7
#define SLV 0
#define QKRED(X) RDQ(80, 12288) RDQ(84, 12304) X RDQ(88, 0) RDQ(92, 16) RDQ(112, 4096) RDQ(116, 4112) RDQ(120, 8192) RDQ(124, 8208) \
        WLG(4) TSA(4) PKA(80, 80, 88) PKA(82, 82, 90) PKA(84, 84, 92) PKA(86, 86, 94) \
        WLG(2) PKA(80, 80, 112) PKA(82, 82, 114) PKA(84, 84, 116) PKA(86, 86, 118) \
        WLG(0) PKA(80, 80, 120) PKA(82, 82, 122) PKA(84, 84, 124) PKA(86, 86, 126) \
        "v_cvt_pk_bf16_f32 v72, v80, v81\n\t" "v_cvt_pk_bf16_f32 v73, v82, v83\n\t" "v_cvt_pk_bf16_f32 v74, v84, v85\n\t" "v_cvt_pk_bf16_f32 v75, v86, v87\n\t" \
        NOP(1) TSA(5) \
        "v_mfma_f32_16x16x32_bf16 v[88:91], v[72:75], v[72:75], 0\n\t" NOP(2) \
        "v_mfma_f32_16x16x32_bf16 v[80:83], a[208:211], v[72:75], 0\n\t" NOP(2) \
        "v_mfma_f32_16x16x32_bf16 v[84:87], a[212:215], v[72:75], 0\n\t"
#define QKPUB "buffer_store_dwordx4 v[88:91], v41, %[rs], s71 offen sc1\n\t" NOP(0) \
        "buffer_store_dwordx4 v[80:83], v40, %[rs], s71 offen sc1\n\t" NOP(2) \
        "buffer_store_dwordx4 v[84:87], v40, %[rs], s71 offen offset:512 sc1\n\t"
#define MRED(W, X) "ds_read_b128 v[80:83], v46 offset:12288\n\t" W "ds_read_b128 v[84:87], v46\n\t" "ds_read_b128 v[88:91], v46 offset:4096\n\t" "ds_read_b128 v[92:95], v46 offset:8192\n\t" X TSW(4) \
        WLG(2) TSW(5) PKA(80, 80, 84) PKA(82, 82, 86) WLG(0) PKA(88, 88, 92) PKA(90, 90, 94) PKA(84, 80, 88) PKA(86, 82, 90) \
        "v_cvt_pk_bf16_f32 v80, v84, v85\n\t" "v_cvt_pk_bf16_f32 v81, v86, v87\n\t"
#define ORD(a, b) "v_cmpx_o_f32 s[50:51], " REG(a) ", " REG(b) "\n\t"
#define REDO(L) "s_cmp_lg_u64 exec, -1\n\t" "s_mov_b64 exec, -1\n\t" "s_cbranch_scc1 " L "\n" L "r:\n\t"
#define CWAIT(L, o, S) L ":\n\t" "ds_read_b32 v62, v38 offset:" CNT(o) "\n\t" WLG(0) "v_readfirstlane_b32 s49, v62\n\t" "s_cmp_lt_u32 s49, " S "\n\t" "s_cbranch_scc1 " L "\n\t"
#define VPOST PSA(24) PSA(28) PSW(24, 2560) PSW(25, 2832) PSW(28, 3200) PSW(29, 3472)
#define BARA "s_barrier\n\t"
#define BARC "s_barrier\n\t"
#define PZADR(SB, M) "s_add_u32 s49, s45, 1\n\t" "s_xor_b32 s62, s71, " STR(XR2S) "\n\t" "v_readlane_b32 s63, v19, s49\n\t" "s_mov_b32 s49, " SB "\n\t" \
        ".if !((" STR(M) ") & 1)\n\t" "s_mov_b32 s63, 0x7ff00000\n\t" ".endif\n\t" ".if !((" STR(M) ") & 2)\n\t" "s_mov_b32 s49, 0x7ff00000\n\t" ".endif\n\t" \
        ".if !((" STR(M) ") & 4)\n\t" "s_mov_b32 s62, 0x7ff00000\n\t" ".endif\n\t" "s_cmp_eq_u32 %[wv], 0\n\t" \
        "s_cselect_b32 s49, s49, s62\n\t" "s_cselect_b32 s63, s63, s62\n\t" "v_add_u32 v48, s49, v23\n\t" "s_mov_b32 exec_hi, 0\n\t" "v_add_u32 v48, s63, v23\n\t" "s_mov_b32 exec_hi, -1\n\t"
#define PZST "buffer_store_dwordx4 v[92:95], v48, %[rs], 0 offen sc1\n\t"
#define MA 0
#define MC 0
#define MB (7 - MA - MC)
#define PZD 1
#define PZB0 PZADR("s63", MB)
#define PZB PZST
#define PZC0
#define PZC ""
#define PZNC 0
#define PZA0
#define PZA
#define PZNA 0
#define LASTC(L) RELV(L)
#define LASTA(L) ""
#define PCL PCA
#define ABODY(W, X, F, n) TSA(0) GLD(96, 0) NOP(PCA) GLD(100, 1024) NOP(PCA) GLD(104, 2048) W GLD(108, 3072) "s_mov_b32 s47, 0\n\t" X \
        WVM((n) + 3) TSA(1) SQ(0, 0) SQ(1, 0) SQ(2, 0) SQ(3, 0) \
        WVM((n) + 2) SQ(0, 1) SQ(1, 1) SQ(2, 1) SQ(3, 1) \
        WVM((n) + 1) SQ(0, 2) MXA0(96) MXB(96) SQ(1, 2) MXA(100) MXB(100) SQ(2, 2) MXA(104) MXB(104) SQ(3, 2) \
        WVM(n) TSA(2) SQ(0, 3) MXA(108) MXB(108) CMPB SQ(1, 3) "s_add_u32 s48, s38, 4\n\t" NOP(0) NOP(0) SQ(2, 3) "s_cbranch_vccnz " F "\n\t" PS1(64) PS1(65) SQ(3, 3) PS1(68) PS1(69) \
        NOP(0) PSW(64, 0) PSW(65, 272) PSW(68, 640) PSW(69, 912) PSA(72) PSA(76) PSW(72, 1280) PSW(73, 1552) PSW(76, 1920) PSW(77, 2192)
#define CISSUE(X, Y) TSW(0) GL1(96, 0) PC GL1(100, 1024) PC GL1(104, 2048) PC GL1(108, 3072) PC GL2(112, 0) PC GL2(116, 1024) \
        "s_mov_b32 s48, s36\n\t" "s_mov_b32 s36, s37\n\t" "s_add_u32 s45, s45, 1\n\t" "s_add_u32 s38, s38, 4\n\t" "v_readlane_b32 s37, v19, s45\n\t" \
        "s_add_u32 s39, s39, " STR(EXF) "\n\t" "s_add_u32 s40, s40, " STR(EXF) "\n\t" \
        "s_add_u32 s41, s41, " STR(EXK) "\n\t" "s_add_u32 s42, s42, " STR(EXK) "\n\t" "s_add_u32 s43, s43, s55\n\t" PC6 \
        GL2(120, 2048) "s_mov_b32 s47, 2\n\t" "s_mul_i32 s70, s69, 3\n\t" R2BASE "s_add_u32 s43, s43, s70\n\t" NOP(2) PC7 X \
        GL2(124, 3072) Y
#define ZALL ZER(64) ZER(68) ZER(72) ZER(76) ZER(80) ZER(84) ZER(24) ZER(28)
#define SHRD "ds_read_b32 v62, v38\n\t"
#define SHUP WLG(0) "v_readfirstlane_b32 s49, v62\n\t" "s_max_u32 s44, s44, s49\n\t"
#define DPA(d, r, m) "v_add_f32_dpp " REG(d) ", " REG(r) ", " REG((r) + 2) " quad_perm:[0,1,2,3] row_mask:" m " bank_mask:0xf\n\t"
#define CBODY(m, X, n, F) CBODY_(80, m, X, n, F)
#define CBODY_(a, m, X, n, F) \
        WVM((m) + 7) TSW(1) WBLK(a, m, 0, MXA0) WBLK(a, m, 1, MXA) X WBLK(a, n, 2, MXA) WBLK(a, n, 3, MXA) WBLK(a, n, 4, MXA) WBLK(a, n, 5, MXA) WBLK(a, n, 6, MXA) \
        WVM(n) TSW(2) SW(a, 0, 7) MXA(124) MXB(124) CMPB SW(a, 1, 7) NOP(0) NOP(1) "s_cbranch_vccnz " F "\n\t" \
        DPA(88, a, "0x3") DPA(89, (a) + 1, "0x3") NOP(1) DPA(88, (a) + 4, "0xc") DPA(89, (a) + 5, "0xc") NOP(1) \
        "v_permlane32_swap_b32 v88, v89\n\t" "v_add_f32 v88, v88, v89\n\t" TSW(3) PSW(88, 3840)
#define FINEO
#define NEXTP "s_branch .LA%=\n"
#define NEXTN "s_branch .LNA%=\n"

__device__ __forceinline__ void clk_edge(u32& cyc, u32& tick) {
  const u32 t = rt32();
  for (int i = 0; i < 64; ++i) { tick = rt32(); if (tick != t) break; }
  cyc = (u32)__builtin_amdgcn_s_memtime();
}
__device__ __forceinline__ float clk_rate(u32 cyc0, u32 tk0) { u32 c, t; clk_edge(c, t); return (float)(c - cyc0) / (float)max(t - tk0, 1u); }
__device__ __forceinline__ void dma16(__amdgpu_buffer_rsrc_t rs, int voff, int soff, int sstep, int ldsa) {
#pragma unroll
  for (int i = 0; i < 16; ++i)
    asm volatile("s_mov_b32 m0, %0\n\ts_nop 3\n\tbuffer_load_dwordx4 %1, %2, %3 offen lds" :: "s"(ldsa + i * 1024), "v"(voff), "s"(rs), "s"(soff ^ (i * sstep)) : "memory");
}

#define KH(t) (192 + 8 * (t))
#define KMF(t, a, b) "v_mfma_f32_16x16x32_bf16 " RNG("v", 80 + 4 * (t), 4) ", " RNG("v", KH(t) + 4 * (a), 4) ", " RNG("v", KH(3) + 4 * (b), 4) ", " RNG("v", 80 + 4 * (t), 4) "\n\t"
#define KX(j) (92 + 4 * ((j) & 1))
#define KA(t, j) "v_pk_mul_f32 " RNG("v", KX(j), 2) ", " RNG("v", 64 + 4 * (j), 2) ", " RNG("v", 112 + ((t) & 2), 2) " op_sel:[0," STR((t) & 1) "] op_sel_hi:[1," STR((t) & 1) "]\n\t"
#define KF(j) "v_fract_f32 " REG(KX(j) + 1) ", " REG(KX(j) + 1) "\n\t"
#define KE(j, N) "v_exp_f32 " REG(KX(j)) ", " REG(KX(j)) "\n\t" "v_cos_f32 " REG(KX(j) + 2) ", " REG(KX(j) + 1) "\n\t" "v_sin_f32 " REG(KX(j) + 3) ", " N REG(KX(j) + 1) "\n\t"
#define KM(j) "v_pk_mul_f32 " RNG("v", KX(j) + 2, 2) ", " RNG("v", KX(j) + 2, 2) ", " RNG("v", KX(j), 2) " op_sel_hi:[1,0]\n\t"
#define KC(j) "v_pk_mul_f32 " RNG("v", KX(j), 2) ", " RNG("v", KX(j) + 2, 2) ", " RNG("v", 66 + 4 * (j), 2) " op_sel_hi:[1,0]\n\t" \
  "v_pk_fma_f32 " RNG("v", KX(j), 2) ", " RNG("v", KX(j) + 2, 2) ", " RNG("v", 66 + 4 * (j), 2) ", " RNG("v", KX(j), 2) " op_sel:[1,1,0] op_sel_hi:[0,1,1] neg_hi:[1,0,0]\n\t"
#define KD(t, j, r) "v_cvt_pk_bf16_f32 " REG(KH(t) + (j)) ", " REG(r) ", " REG((r) + 1) "\n\t" \
  "v_dot2c_f32_bf16 " REG(r) ", s47, " REG(KH(t) + (j)) "\n\t" "v_dot2c_f32_bf16 " REG((r) + 1) ", s48, " REG(KH(t) + (j)) "\n\t"
#define KL(t, j, r) "v_cvt_pk_bf16_f32 " REG(KH(t) + 4 + (j)) ", " REG(r) ", " REG((r) + 1) "\n\t"
#define KDM "s_add_u32 m0, m0, 1024\n\t" "s_add_u32 s43, s43, s42\n\t" "buffer_load_dwordx4 v110, s[36:39], s43 offen lds\n\t"
#define KP(j) "ds_read_b128 " RNG("v", 64 + 4 * (j), 4) ", v111 offset:" STR(16 * (j)) "\n\t"
#define KV(j, L, A) KE(j, "-") L KM(j) KC(j) A KD(3, j, KX(j))
#define KU(t, j, L, A) KE(j, "") L KM(j) A KD(t, j, KX(j) + 2)
#define KSUB                                                                                                        \
  KV(0, WLG(8) "s_add_u32 m0, m0, 1024\n\t", KA(3, 1)) KV(1, KL(3, 0, KX(0)), KA(3, 2))                              \
  "s_xor_b32 s43, s40, s41\n\t" "buffer_load_dwordx4 v110, s[36:39], s43 offen lds\n\t" KV(2, KL(3, 1, KX(1)), KA(3, 3)) \
  KV(3, KL(3, 2, KX(2)), KA(0, 0))                                                                                  \
  KU(0, 0, KL(3, 3, KX(3)), KA(0, 1)) KU(0, 1, KL(0, 0, KX(0) + 2), KA(0, 2)) KDM KU(0, 2, KL(0, 1, KX(1) + 2), KA(0, 3)) \
  KU(0, 3, KL(0, 2, KX(2) + 2), KA(1, 0)) KF(0)                                                                     \
  KU(1, 0, KL(0, 3, KX(3) + 2), KA(1, 1)) KF(1) KMF(0, 1, 0) KU(1, 1, KL(1, 0, KX(0) + 2), KA(1, 2)) KF(2) KDM      \
  KMF(0, 0, 1) KU(1, 2, KL(1, 1, KX(1) + 2), KA(1, 3)) KF(3) KMF(0, 0, 0) KU(1, 3, KL(1, 2, KX(2) + 2), KA(2, 0)) KF(0) KP(0) \
  KU(2, 0, KL(1, 3, KX(3) + 2), KA(2, 1)) KF(1) KP(1) KMF(1, 1, 0) KU(2, 1, KL(2, 0, KX(0) + 2), KA(2, 2)) KF(2) KP(2) KDM \
  KMF(1, 0, 1) KU(2, 2, KL(2, 1, KX(1) + 2), KA(2, 3)) KF(3) KP(3) KMF(1, 0, 0) KU(2, 3, KL(2, 2, KX(2) + 2), WLG(3) KA(3, 0)) \
  KMF(2, 0, 1) "v_add_u32 v111, 256, v111\n\t" "s_lshl2_add_u32 s41, s42, s41\n\t" KL(2, 3, KX(3) + 2)
#define KZERO ZER(80) ZER(84) ZER(88)
#define KCHUNK(PTR, VO, SO, ST, BUF, PF) "s_mov_b64 s[36:37], " PTR "\n\t" "s_mov_b32 m0, %[sc]\n\t" "v_mov_b32 v110, " VO "\n\t" SO "s_mov_b32 s41, 0\n\t" "s_mov_b32 s42, " STR(ST) "\n\t" \
  "buffer_load_dword " PF ", s[36:39], 0 offen lds\n\t" "s_add_u32 m0, %[sl], " STR((BUF) * 16384 - 1024) "\n\t"
#define KCALL "s_call_b64 s[44:45], .Lkg%=\n\t" WVM(16) KMF(2, 0, 0)
#define KTR(F, d, A, o) "ds_read_b64_tr_b16 " RNG(F, d, 2) ", " A " offset:" STR(o) "\n\t"
#define KTQ1(d, o) KTR("a", d, "%[aq]", o) KTR("a", (d) + 2, "%[aq]", (o) + 128) KTR("a", (d) + 4, "%[aq]", (o) + 2048) KTR("a", (d) + 6, "%[aq]", (o) + 2176)
#define KTQ(d, o) KCALL KTQ1(d, o) KMF(2, 1, 0) KTQ1((d) + 8, (o) + 32)
#define KTO1(d, o) KTR("v", d, "%[ao]", o) KTR("v", (d) + 2, "%[ao]", (o) + 64) KTR("v", (d) + 4, "%[ao]", (o) + 1024) KTR("v", (d) + 6, "%[ao]", (o) + 1088)
#define KTO(d, o) KCALL KTO1(d, o) KMF(2, 1, 0) KTO1((d) + 8, (o) + 2048)
#define KOUT(o) "s_add_u32 s43, %[ko], " STR(o) "\n\t"                                                              \
  "buffer_store_dwordx4 v[80:83], %[l16], %[rt], s43 offen sc1\n\t" "buffer_store_dwordx4 v[84:87], %[l16], %[rt], s43 offen offset:1024 sc1\n\t" "s_nop 3\n\t" \
  "buffer_store_dwordx4 v[88:91], %[l16], %[rt], s43 offen offset:2048 sc1\n\t"
struct SsmPtrs { const u16 *A_re, *A_im, *B_re, *B_im, *C_re, *C_im, *log_dt; };
__device__ __forceinline__ void r_role(unsigned char* lds, int b, const u16* __restrict__ mem0, const u16* __restrict__ Wq, const u16* __restrict__ Wk, const u16* __restrict__ Wv,
                                       const u16* __restrict__ Wo, int fb, u32 tag, u64 pc0, int clen, __amdgpu_buffer_rsrc_t rsx, __amdgpu_buffer_rsrc_t rsf,
                                       __amdgpu_buffer_rsrc_t rst, SsmPtrs sp, int tid, int lane, int w
                                       ) {
  const int lr = lane & 15, lg = lane >> 4;
  const int ldsb = (int)(size_t)(LAS unsigned char*)lds;
  const int idx = (lr & 2) ? 0xEEEE : 0x4444;
  u32 tcnt = 0; (void)tcnt;
  u32 cyc0 = 0, tk0 = 0;
  if (tid < 8) *(u32*)(lds + LR_CTL + tid * 4) = 0u;
  *(u32*)(lds + LR_CTL + 4096 + 256 + tid * 4) = 0u;
  const int xcd = b & 3, li = b >> 2;
  const int head = xcd * 8 + (li >> 2), ks = li & 3;
  const int vcu = head * 4 + ks;
  const int cg = (xcd >> 1) * 32 + li, rg = xcd & 1;
  const int rot1 = ((li >> 1) + w) & 3, rot3 = ((li >> 2) + 2 * w) & 7;
  const int pz1 = lane < 32 ? XR1A(lane >> 3, cg * 256 + rg * 128 + (lane & 7) * 16) : XR3A(lg & 1, vcu * 256 + lr * 16);
  const int pgo = PGO_X + head * 128;
  const int calo = XCAL + (li & 3) * 8192 + ((li >> 2) & 1) * 4096 + ((li >> 3) * 4 + xcd) * 128;
  u32 xp0;
  u32x32 wa[6], wo[4];
  {
    const __amdgpu_buffer_rsrc_t rwq = __builtin_amdgcn_make_buffer_rsrc((void*)Wq, 0, DM * HD * 2, 0x00020000);
    const int vq = (lane >> 2) * (HD * 2) + vcu * 64 + (lane & 3) * 16, sq = (256 * w + (rot1 << 6)) * (HD * 2);
    const int vo = (lane >> 1) * (DM * 2) + cg * 32 + (lane & 1) * 16, so = (1024 * w + (rot3 << 7)) * (DM * 2);
    const int sl = ldsb + LR_STG + w * 32768;
    const int ch0 = (b * 4 + w) * 2;
    u32x2 pin[6];
    u32 ldt;
    {
      const int po = (ch0 * SSM_N + 4 * lane) * 2;
      asm volatile("global_load_dwordx2 %0, %7, %9\n\tglobal_load_dwordx2 %1, %7, %10\n\tglobal_load_dwordx2 %2, %7, %11\n\tglobal_load_dwordx2 %3, %7, %12\n\t"
                   "global_load_dwordx2 %4, %7, %13\n\tglobal_load_dwordx2 %5, %7, %14\n\tglobal_load_ushort %6, %8, %15"
                   : "=&v"(pin[0]), "=&v"(pin[1]), "=&v"(pin[2]), "=&v"(pin[3]), "=&v"(pin[4]), "=&v"(pin[5]), "=&v"(ldt)
                   : "v"(po), "v"((ch0 + (lane >> 5)) * 2), "s"(sp.A_re), "s"(sp.A_im), "s"(sp.B_re), "s"(sp.B_im), "s"(sp.C_re), "s"(sp.C_im), "s"(sp.log_dt) : "memory");
    }
    u32x4 m0v;
    asm volatile("global_load_dwordx4 %0, %1, %2" : "=&v"(m0v) : "v"(((4 * rg + ((lane >> 1) & 3)) * DM + 16 * cg + 8 * (lane & 1)) * 2), "s"(mem0) : "memory");
    const u16* const wp[4] = {Wq, Wk, Wv, Wo};
    int pfo[5];
#pragma unroll
    for (int c = 0; c < 5; ++c) {
      const int lo_ = (int)(size_t)wp[c < 3 ? c : 3], s3 = (384 - lo_) & 1023, s63 = (8064 - lo_) & 8191;
      pfo[c] = c < 3 ? (lane < 16 ? (256 * w + li * 8 + (lane & 7)) * (HD * 2) + xcd * 2048 + (lane >> 3) * 1024 + s3
                                  : (lane < 24 && (s63 >> 11) == xcd ? (256 * w + li * 8 + (lane & 7)) * (HD * 2) + s63 : OOR))
                     : (lane < 16 ? (1024 * w + li * 16 + lane + (((c - 3) * 512) ^ ((((s3 >> 7) + 2 * w) & 4) << 7))) * (DM * 2) + (xcd >> 1) * 1024 + s3 : OOR);
    }
    const int scr = ldsb + LR_RED + 1024 + w * 256;
    asm volatile("s_mov_b32 m0, %0\n\ts_nop 3\n\tbuffer_load_dword %1, %2, 0 offen lds" :: "s"(scr), "v"(pfo[0]), "s"(rwq) : "memory");
#define KP0 16
#pragma unroll
    for (int i = 0; i < KP0; ++i)
      asm volatile("s_mov_b32 m0, %0\n\ts_nop 3\n\tbuffer_load_dwordx4 %1, %2, %3 offen lds" :: "s"(sl + i * 1024), "v"(vq), "s"(rwq), "s"(sq ^ (i * 16 * HD * 2)) : "memory");
    asm volatile("s_waitcnt vmcnt(1+" STR(KP0) ")" : "+v"(pin[0]), "+v"(pin[1]), "+v"(pin[2]), "+v"(pin[3]), "+v"(pin[4]), "+v"(pin[5]), "+v"(ldt), "+v"(m0v)::"memory");
#define PZE 4
    auto ring_pz = [&]() {
      const u32x4 pz = (u32x4){SENT, SENT, SENT, SENT};
      if (w < 3) {
        u32x4 v = pz;
        if (w == 0 && lane < 32) v = (u32x4){nosent(m0v[0]), nosent(m0v[1]), nosent(m0v[2]), nosent(m0v[3])};
#pragma unroll
        for (int hf = 0; hf < 2; ++hf) {
          __builtin_amdgcn_raw_buffer_store_b128(v, rsx, pz1, XSB(w) + hf * 8192, 16);
          __builtin_amdgcn_raw_buffer_store_b128(pz, rsx, XR2(vcu) + lane * 16, XR2B + (w & 1) * XR2S + hf * 8192, 16);
          __builtin_amdgcn_raw_buffer_store_b128(pz, rsx, lane < 16 ? XR2(vcu) + 1024 + lane * 16 : OOR, XR2B + (w & 1) * XR2S + hf * 8192, 16);
        }
      } else {
        __builtin_amdgcn_raw_buffer_store_b128(pz, rsx, lane < 8 ? lane * 16 : OOR, calo, 16);
        if (b < 32) {
          __builtin_amdgcn_raw_buffer_store_b128(pz, rsx, lane * 16, XSYN + b * 2048, 16);
          __builtin_amdgcn_raw_buffer_store_b128(pz, rsx, lane * 16, XSYN + b * 2048 + 1024, 16);
        } else if (b >= 48 && b < 48 + 41) {
          __builtin_amdgcn_raw_buffer_store_b128(pz, rsx, lane * 16, PGO_D + (b - 48) * 1024, 16);
        }
      }
      asm volatile("" ::: "memory");
    };
    auto late_pz = [&]() {
      const u32x4 pz = (u32x4){SENT, SENT, SENT, SENT};
#pragma unroll
      for (int i = XS0; i < NSEG; i += 4) __builtin_amdgcn_raw_buffer_store_b128(pz, rsf, (lane < 32 ? FO_KM : FO_VM - 512) + vcu * 512 + lane * 16, (i + w) * EXK, 16);
      if (w < 2) __builtin_amdgcn_raw_buffer_store_b128(pz, rsf, lane * 16, FO_O + (NSEG - 1) * 262144 + (vcu * 2 + w) * 1024, 16);
    };
    {
      if (PZE == 1 || PZE == 2) late_pz();
      if (PZE == 2) ring_pz();
      asm volatile("" ::: "memory");
    }
    f32x4* prm = (f32x4*)(lds + w * 4096);
    {
      const float dt = rbf(expf(bf2f((u16)ldt)));
#pragma unroll
      for (int i = 0; i < 4; ++i) {
        float v[6];
#pragma unroll
        for (int j = 0; j < 6; ++j) v[j] = __uint_as_float((i & 1) ? (pin[j][i >> 1] & 0xffff0000u) : (pin[j][i >> 1] << 16));
        const float are = dt * v[0], aim = dt * v[1], br = dt * v[2], bi = dt * v[3];
        const float cr = v[4] * br - v[5] * bi, ci = v[4] * bi + v[5] * br;
        const float rev = aim * 0.15915494309189535f;
        prm[4 * lane + i] = (f32x4){are * 1.4426950408889634f, rev - __builtin_rintf(rev), cr, ci};
      }
    }
#pragma unroll
    for (int i = KP0; i < 16; ++i)
      asm volatile("s_mov_b32 m0, %0\n\ts_nop 3\n\tbuffer_load_dwordx4 %1, %2, %3 offen lds" :: "s"(sl + i * 1024), "v"(vq), "s"(rwq), "s"(sq ^ (i * 16 * HD * 2)) : "memory");
    const f32x4 lv = {(float)(128 * (lr >> 3) + (lr & 7)), (float)(lr < 8 ? 256 + 128 * (lr >> 2) + 2 * (lr & 3) : 512 + 128 * ((lr - 8) >> 1) + 4 * (lr & 1)),
                         (float)(lr <= 8 ? 1024 + 128 * lr : 0), (float)(8 * lr)};
    const int trow = 16 * (lg >> 1) + 4 * (lg & 1) + ((lr >> 2) & 1) + 8 * (lr >> 3);
    asm volatile(
        "s_nop 4\n\t" "s_mov_b32 s46, m0\n\t" "s_mov_b32 s38, " STR(DM * HD * 2) "\n\t" "s_mov_b32 s39, 0x20000\n\t" "s_mov_b32 s47, 0xbf80\n\t" "s_mov_b32 s48, 0xbf800000\n\t"
        WLG(0)
        "ds_read_b128 v[64:67], %[pa]\n\t" "ds_read_b128 v[68:71], %[pa] offset:16\n\t" "ds_read_b128 v[72:75], %[pa] offset:32\n\t" "ds_read_b128 v[76:79], %[pa] offset:48\n\t"
        "v_add_u32 v111, 256, %[pa]\n\t" KZERO
        KCHUNK("%[wk]", "%[vq]", "s_mov_b32 s40, %[sq]\n\t", 16 * HD * 2, 1, "%[p1]")
        WLG(0) KA(3, 0)
        KTQ(0, 0) KTQ(32, 4096) KTQ(64, 8192) KTQ(96, 12288)
        KCHUNK("%[wv]", "%[vq]", "", 16 * HD * 2, 0, "%[p2]")
        KTQ(16, 16384) KTQ(48, 16384 + 4096) KTQ(80, 16384 + 8192) KTQ(112, 16384 + 12288)
        KOUT(0)
        KCHUNK("%[wo]", "%[vo]", "s_mov_b32 s40, %[so]\n\t", 32 * DM * 2, 1, "%[p3]")
        KZERO
        KTQ(128, 0) KTQ(144, 4096) KTQ(160, 8192) KTQ(176, 12288)
        KCHUNK("%[wo]", "%[vo]", "s_xor_b32 s40, s40, " STR(512 * DM * 2) "\n\t", 32 * DM * 2, 0, "%[p4]")
        KTO(128, 16384) KTO(144, 16384 + 4096) KTO(160, 16384 + 8192) KTO(176, 16384 + 12288)
        KOUT(3072)
        "s_branch .Lke%=\n"
        ".Lkg%=:\n\t"
        KSUB
        "s_setpc_b64 s[44:45]\n"
        ".Lke%=:\n\t"
        WLG(0) "s_mov_b32 m0, s46"
        : "=&{a[0:31]}"(wa[0]), "=&{a[32:63]}"(wa[1]), "=&{a[64:95]}"(wa[2]), "=&{a[96:127]}"(wa[3]), "=&{a[128:159]}"(wa[4]), "=&{a[160:191]}"(wa[5]),
          "=&{v[128:159]}"(wo[0]), "=&{v[160:191]}"(wo[1])
        : "{v[112:115]}"(lv), [pa] "v"(ldsb + w * 4096 + lg * 64), [vq] "v"(vq), [vo] "v"(vo),
          [p1] "v"(pfo[1]), [p2] "v"(pfo[2]), [p3] "v"(pfo[3]), [p4] "v"(pfo[4]), [aq] "v"(sl + trow * 64 + (lr & 3) * 8), [ao] "v"(sl + trow * 32 + (lr & 3) * 8), [l16] "v"(lane * 16),
          [wk] "s"(Wk), [wv] "s"(Wv), [wo] "s"(Wo), [sq] "s"(sq), [so] "s"(so), [sl] "s"(sl), [sc] "s"(scr), [rt] "s"(rst), [ko] "s"(2 * SSM_L * SSM_D * 2 + ch0 * 3072)
        : CL64, CL72, CL80, CL88, CL96, CL8(192, 193, 194, 195, 196, 197, 198, 199), CL8(200, 201, 202, 203, 204, 205, 206, 207), CL8(208, 209, 210, 211, 212, 213, 214, 215),
          CL8(216, 217, 218, 219, 220, 221, 222, 223), "s36", "s37", "s38", "s39", "s40", "s41", "s42", "s43", "s44", "s45", "s46", "s47", "s48", "scc", "memory");
    if (PZE == 0) late_pz();
    if (PZE != 2) ring_pz();
    xp0 = __builtin_amdgcn_raw_buffer_load_b32(rsx, (lane & 1) * 4096, pgo, 16);
    clk_edge(cyc0, tk0);
    asm volatile("s_waitcnt vmcnt(0)\n\ts_barrier" : "+v"(xp0)::"memory");
#define CLKF() clk_rate(cyc0, tk0)
    if (w == 0) {
      const int l0 = lane ? OOR : 0;
      const u64 c0 = __builtin_amdgcn_s_memtime();
      u32 v;
      asm volatile("s_waitcnt lgkmcnt(0)\n\tbuffer_load_dword %0, off, %1, %2 sc1\n\tbuffer_store_dword %3, %4, %1, %5 offen sc1\n\ts_waitcnt vmcnt(1)"
                   : "=&v"(v) : "s"(rsx), "s"(calo), "v"(tag), "v"(l0), "s"(PGO_Y + b * 128), "s"(c0) : "memory");
      const u32 lat = (u32)(__builtin_amdgcn_s_memtime() - c0);
      const float f = CLKF();
      typedef __attribute__((ext_vector_type(3))) unsigned int u32x3;
      __builtin_amdgcn_raw_buffer_store_b96((u32x3){__float_as_uint(f), lat, tag}, rsx, l0, PGO_Y + b * 128 + 4, 16);
      asm volatile("" ::: "memory");
    }
    {
      u32 r[1][8][8];
      tr_chunk<1, 8>((const u16*)(lds + LR_STG + w * 32768), lr, lg, r);
#pragma unroll
      for (int i = 0; i < 8; ++i)
#pragma unroll
        for (int e = 0; e < 8; ++e) { const int o = (8 + i) * 8 + e; wo[o >> 5][o & 31] = r[0][i][e]; }
      asm volatile("s_waitcnt lgkmcnt(0)" ::: "memory");
    }
    if (PZE == 4) late_pz();
    asm volatile("" ::: "memory");
  }
  const int hf = (lr >> 1) & 1;
  const int voff1 = lg * 256 + (lr >> 3) * 128 + (((lr >> 2) & 1) * 2 + (lr & 1)) * 32 + hf * 16;
  const int voff3 = (lr >> 3) * 512 + (lg >> 1) * 256 + (((lr >> 2) & 1) * 2 + (lr & 1)) * 16 + (lg & 1) * 128 + hf * 64;
  const int ard = ldsb + (2 * (lr >> 3) + (lg >> 1)) * 640 + (lr & 1) * 272 + ((lr >> 1) & 3) * 64 + (lg & 1) * 32;
  const int ctl = ldsb + LR_CTL, one = w == 0 ? 2 : (w == 3 ? 4 : 1);
  const int r8 = lane & 7, hk = (lane >> 3) & 1;
#define COA 1
#define COB 1
  const int st01 = lr < 8 ? XR2(vcu) + lr * 16 + lg * 128 : OOR, st2 = (lr < 8 && lg >= 2) ? XR2(vcu) + 1024 + lr * 16 + (lg - 2) * 128 : OOR;
  const int ld01 = hk * 512 + r8 * 16 + lg * 128, ld2 = XR2(head * 4 + lg) + 1024 + r8 * 16 + hk * 128;
  int sto = XR3A(r8 >> 2, vcu * 256 + (r8 & 3) * 16 + lg * 64 + hk * 8);
  const int stk = lr >= 8 ? (lg * 8 + (lr - 8)) * 16 : OOR;
  const int lo = X4C ? 2 * (lr & 7) + (lr >> 3) : lr;
  const int arw = ldsb + LR_CTL + lane * 4, arr = ldsb + 3840 + ((lo >> 3) + 2 * ((lo >> 2) & 1)) * 64 + (lo & 3) * 16;
  const int st1 = XR1A(lg, cg * 256 + rg * 128 + lo * 8);
  const int avw = 0;
  const int avc = ldsb + 2560 + (lr >> 3) * 640 + (8 * ((lr >> 2) & 1) + (lr & 3)) * 4;
  const int avr = avc + (lg & 1) * 272 + (lg >> 1) * 64, avf = avc + (lg & 1) * 128 + (lg >> 1) * 16;
  u32x16 kc = {(u32)voff1, (u32)voff3, (u32)idx, (u32)TEX(lane), (u32)(lane < 16 ? XR2(vcu) + 1024 + lane * 16 : OOR), (u32)ard, (u32)ctl, (u32)one, (u32)st01, (u32)st2, (u32)(XR2(vcu) + lane * 16), (u32)ld2, (u32)(lane * 16), (u32)arw, (u32)arr, (u32)stk};
  int o1c = XR1A(xcd, w * 4096) + rot1 * 1024, o3c = XR3A(rg, w * 8192) + rot3 * 1024;
  const int fok = FO_K + (head * 8 + ks) * 1024, fov = FO_V + (head * 8 + 2 * ks) * 1024;
#define XPROG(v) min(xprog(__builtin_amdgcn_readlane(v, 0), tag), xprog(__builtin_amdgcn_readlane(v, 1), tag))
  int xp = w == 3 ? XPROG(xp0) : 1000;
#define XWAIT(n) for (int it_ = 0; it_ < SPIN_CAP && xp < (n); ++it_) {                                               \
    if (it_) asm volatile("s_sleep 4" ::: "memory");                                                                  \
    const u32 v_ = __builtin_amdgcn_raw_buffer_load_b32(rsx, (lane & 1) * 4096, pgo, 16); xp = XPROG(v_); asm volatile("" ::: "memory"); }
  XWAIT(1)
  u32x16 fv;
  u32x8 fk;
  {
    const int lo_ = w == 3 ? lane * 16 : OOR;
    const u32x4 v0 = __builtin_amdgcn_raw_buffer_load_b128(rsf, lo_, fov, 16), v1 = __builtin_amdgcn_raw_buffer_load_b128(rsf, lo_, fov + 1024, 16);
    const u32x4 k0 = __builtin_amdgcn_raw_buffer_load_b128(rsf, lo_, fok, 16), k1 = __builtin_amdgcn_raw_buffer_load_b128(rsf, lo_, fok + 4096, 16);
    fv = (u32x16){v0[0], v0[1], 0u, 0u, v0[2], v0[3], 0u, 0u, v1[0], v1[1], 0u, 0u, v1[2], v1[3], 0u, 0u};
    fk = (u32x8){k0[0], k0[1], k0[2], k0[3], k1[0], k1[1], k1[2], k1[3]};
    asm volatile("" : "+{a[192:207]}"(fv), "+{a[208:215]}"(fk));
  }
#define XPN 1
#define CALM 40
  XWAIT(2)
#pragma unroll
  for (int i = 0; i < 3; ++i) *(u32x4*)(lds + w * 4096 + i * 1024 + lane * 16) = (u32x4){SENT, SENT, SENT, SENT};
  {
    u64 pc; asm volatile("s_getpc_b64 %0" : "=s"(pc));
    const u32 cv = __builtin_amdgcn_raw_buffer_load_b32(__builtin_amdgcn_make_buffer_rsrc((void*)pc0, 0, clen, 0x00020000), (int)(pc - pc0) + tid * 64, 0, 0);
    asm volatile("s_waitcnt vmcnt(0)" :: "v"(cv) : "memory");
  }
  if (w == 3) { const u32 v_ = __builtin_amdgcn_raw_buffer_load_b32(rsx, (lane & 1) * 4096, pgo, 16); xp = max(xp, XPROG(v_)); }
  u32x4 st;
  if (w == 1) {
    if (b == 0) {
      u32 tt = 0, best = 0;
      bool xok = false;
      const int sto_ = lane < 5 ? (lane ? XSTR(lane - 1) : XSTA) : OOR;
      for (int it = 0; it < SPIN_CAP; ++it) {
        asm volatile("" ::: "memory");
        const u32x4 r0 = __builtin_amdgcn_raw_buffer_load_b128(rsx, lane * 128, PGO_Y, 16), r1 = __builtin_amdgcn_raw_buffer_load_b128(rsx, lane * 128 + 8192, PGO_Y, 16);
        if (!xok) xok = __ballot(xprog(__builtin_amdgcn_raw_buffer_load_b32(rsx, lane * 128, PGO_X, 16), tag) < XPN) == 0;
        const bool c0 = r0[3] == tag, c1 = r1[3] == tag;
        if (tt == 0 && __popcll(__ballot(!c0 || !c1)) <= CALM) {
          float fm = (c0 ? __uint_as_float(r0[1]) : 0.f) + (c1 ? __uint_as_float(r1[1]) : 0.f), fn = (c0 ? 1.f : 0.f) + (c1 ? 1.f : 0.f);
#pragma unroll
          for (int o = 4; o < 64; o *= 2) { fm += __shfl_xor(fm, o); fn += __shfl_xor(fn, o); }
          fm = fn > 0.f ? fm / fn : 100.f;
          const float f0 = __uint_as_float(__builtin_amdgcn_readfirstlane(__float_as_uint(fm)));
          fm = fminf(fm, __shfl_xor(fm, 1)); fm = fminf(fm, __shfl_xor(fm, 2));
          const float f_ = CLKF() * fminf(fmaxf(fm / f0, 0.8f), 1.f);
          const float k_ = 1.f + TKS * (fminf(fmaxf(23.9f / f_, 1.f), 1.5f) - 1.f);
          const int nb_ = (int)(TDB * k_ + 0.5f), nc_ = nb_ + (int)((TDC - TDB) * k_ + 0.5f), np_ = nc_ + (int)((TP - TDC) * k_ + 0.5f);
          u32 lat = min(c0 ? r0[2] : 65535u, c1 ? r1[2] : 65535u);
          { auto r = __builtin_amdgcn_permlane16_swap(lat, lat, false, false); lat = min(r[0], r[1]); }
          { auto r = __builtin_amdgcn_permlane32_swap(lat, lat, false, false); lat = min(r[0], r[1]); }
          u32 mm[4];
#pragma unroll
          for (int k = 0; k < 4; ++k)
            mm[k] = __builtin_amdgcn_readlane(lat, 4 * k) + __builtin_amdgcn_readlane(lat, 4 * k + 2) + __builtin_amdgcn_readlane(lat, 4 * (k ^ 2) + 1) + __builtin_amdgcn_readlane(lat, 4 * (k ^ 2) + 3);
          const u32 hb = mm[0] + mm[2] <= mm[1] + mm[3] ? 0u : 1u;
          best = (hb << 13) | ((hb ? mm[1] <= mm[3] : mm[0] <= mm[2]) ? CLS0 : 16384u - CLS0);
          if (__ballot(lat == 65535u) == 0) tt = __builtin_amdgcn_readfirstlane(nb_ | (nc_ << 10) | (np_ << 20));
        }
        if (__ballot(r0[0] != tag || r1[0] != tag) == 0 && xok && tt) break;
      }
      st = (u32x4){rt32() + TD0, tt, best, tag};
      __builtin_amdgcn_raw_buffer_store_b128(st, rsx, sto_, 0, 16);
    } else st = wait_start(rsx, fb, tag, false, xcd);
    if (lane == 0) *(u32x4*)(lds + LR_CTL + 32) = st;
  }
  LBAR();
  st = *(const u32x4*)(lds + LR_CTL + 32);
  const int flip = __builtin_amdgcn_readfirstlane(st[2]) & 8192, cls = __builtin_amdgcn_readfirstlane(st[2]) & 16384;
  o1c ^= cls; o3c ^= cls; sto ^= cls; kc[4] ^= cls; kc[8] ^= cls; kc[9] ^= cls; kc[10] ^= cls; kc[11] ^= cls;
  { const u32 now = rt32(); if (st[0] - now + 100000u > 200000u) st[0] = now; }
  u32x16 kd = {(u32)((ld01 + XR2(vcu ^ 1)) ^ cls), (u32)((ld01 + XR2(vcu ^ 2)) ^ cls), (u32)((ld01 + XR2(vcu ^ 3)) ^ cls), (u32)(X4B && hk ? OOR : sto), (u32)avr, (u32)(avr + 4096), (u32)(avr + 8192),
               (u32)(avr + 12288), (u32)avw, (u32)(X4C && hk ? OOR : st1 ^ cls), (u32)((lane & 1) * 4096), (u32)(XSB(lane + 1) + flip), (u32)(lane * 8), (u32)avf,
               (u32)(b == 0 && lane == 0 ? XSYN + flip : OOR), (u32)(w == 0 ? pz1 ^ cls : (w == 1 ? kc[10] : kc[4]))};
  u32x16 sa = {(u32)flip, (u32)(XSB(1) + flip), 0u, (u32)fok, (u32)fov, (u32)(FO_KM + (head * 4 + ks) * 512),
               (u32)(FO_VM + (head * 8 + 2 * ks) * 256), st[0], 0u, 0u, (u32)SYO(1), 0u, 0u, (u32)TRY_CAP, 0u, 0u};
  u32x8 sc = {(u32)o1c, (u32)o3c, SENT, 0u, 0u, 0u, st[1], (u32)pgo};
#define TUNEO
#define KCNT
#define KHST
#define KTRC
#define TRCI
#define PREP 16
#define GAP 12
#define RETA(n, L) "s_cmp_eq_u32 s49, " #n "\n\t" "s_cmov_b32 s48, .Lpc%=-" L "\n\t"
  u32x32 keep;
  u32x2 ov;
  asm volatile("" : "={v[60:91]}"(keep));
  while (true) {
    sa[14] = xp;
#pragma unroll
    for (int i = 0; i < 16; ++i) sa[i] = __builtin_amdgcn_readfirstlane(sa[i]);
#pragma unroll
    for (int i = 0; i < 8; ++i) sc[i] = __builtin_amdgcn_readfirstlane(sc[i]);
    asm volatile(
        "s_nop 4\n\t" "s_mov_b32 s60, m0\n\t"
        "s_mov_b32 m0, %[mb]\n\t" "s_mov_b32 s64, s50\n\t" "s_mov_b32 s98, s49\n\t" "s_mov_b32 s68, 0\n\t"
        EXTRA R2BASE
        "s_cmp_lg_u32 %[wv], 3\n\t" "s_cbranch_scc1 .LN%=\n\t"
        "s_mov_b32 s78, 0xff00ff00\n\t" "s_mov_b32 s79, 0xff00ff00\n\t" "s_mov_b32 s62, 0x3e0293ee\n\t"
        "v_mov_b32 v52, 0x3f803f80\n\t" "v_mov_b32 v53, 0x3f803f80\n\t" "v_mov_b32 v54, 0x3f803f80\n\t" "v_mov_b32 v55, 0x3f803f80\n\t" "v_mov_b32 v51, 0\n\t" "v_mov_b32 v59, 0\n\t"
        "v_mov_b32 v60, %[tg]\n\t" "v_mov_b32 v61, -1\n\t" "s_mov_b32 s63, %[dm]\n\t"
        "s_branch .Lent%=\n"
        ".LA%=:\n\t"
        CLKW(".Lta%=", "0") REL(".Lua%=")
        "s_add_u32 s50, s36, s52\n\t" NOP(PDA)
        ABODY(NOP(PCL) LASTA(".Lwa%="), WVM(5) "v_readlane_b32 s50, v60, 0\n\t" "v_readlane_b32 s51, v60, 1\n\t"
              "s_sub_u32 s50, s50, %[tg]\n\t" "s_sub_u32 s51, s51, %[tg]\n\t" "s_min_u32 s63, s50, s51\n\t" "s_max_u32 s50, s50, s51\n\t" "s_cmp_le_u32 s50, 80\n\t"
              "s_cselect_b32 s50, s63, 0\n\t" "v_readfirstlane_b32 s49, v61\n\t" "s_max_i32 s64, s64, s50\n\t"
              "s_cmp_lg_u32 s49, -1\n\t" "s_cmov_b32 s58, s49\n\t" EXTRA "v_mov_b32 v62, s58\n\t" "ds_write_b32 v38, v62 offset:24\n\t", ".Lfail%=", 0) TSA(3)
        QKRED(BARA) ORD(88, 89) ORD(90, 91) QKPUB TSA(6) TSB(7) REDO(".Lqk%=")
        SVP(0, 0) SHRD SVP(1, 0) SVP(0, 1) SVP(1, 1) SVP(0, 2) SVP(1, 2) WLG(0) "v_readfirstlane_b32 s49, v62\n\t" SVP(0, 3) SVP(1, 3) "s_max_u32 s44, s44, s49\n\t" "s_cmp_eq_u32 s45, 63\n\t" TSA(7)
        "s_cbranch_scc1 .Llast%=\n"
        ".LB%=:\n\t"
        CLKW(".Ltb%=", "s66")
        "s_mov_b32 s47, 1\n\t"
        R2TEXT
        ".LC%=:\n\t"
        ZALL
        "s_mov_b32 s47, 5\n\t" "s_sub_i32 s63, s64, s45\n\t" "s_cmp_lt_i32 s63, 2\n\t" "s_cbranch_scc1 .Lend%=\n\t"
        "s_cmp_lt_i32 s63, " STR(XTH) "\n\t" "s_cselect_b32 s63, s59, %[dm]\n\t"
        CLKW(".Ltc%=", "s67") REL(".Luc%=")
        "s_add_u32 s50, s36, s53\n\t" NOP(PDC)
        CISSUE(LASTC(".Lwc%="), "")
        "buffer_load_dword v61, off, %[rs], s46 sc1\n\t" "s_add_u32 s46, s46, 64\n\t"
        "s_add_u32 s49, s39, 4096\n\t" NOP(PDF)
        "buffer_load_dwordx4 a[208:211], v44, %[rf], s39 offen sc1\n\t" "buffer_load_dwordx4 a[212:215], v44, %[rf], s49 offen sc1\n\t"
        "buffer_load_dwordx2 a[192:193], v44, %[rf], s40 offen sc1\n\t" "buffer_load_dwordx2 a[196:197], v44, %[rf], s40 offen offset:8 sc1\n\t"
        "buffer_load_dwordx2 a[200:201], v44, %[rf], s40 offen offset:1024 sc1\n\t" "buffer_load_dwordx2 a[204:205], v44, %[rf], s40 offen offset:1032 sc1\n\t"
#define NXC 8
        "buffer_load_dword v60, v18, %[rs], s63 offen sc1\n\t"
        CBODY(NXC, "", NXC, ".Lfail%=")
        MRED(BARC, "") ORD(84, 85) ORD(86, 87) STC TSW(6) REDO(".Lmr%=")
        SHRD SHUP TSW(7)
        NEXTP
        CWAIT(".Lqk%=", 8, "s48") QKRED("") NOP(1) QKPUB "s_branch .Lqk%=r\n"
        CWAIT(".Lmr%=", 16, "s38") WVM(0) MRED("", "") "v_min_u32 v80, -2, v80\n\t" "v_min_u32 v81, -2, v81\n\t" STC
        "s_branch .Lmr%=r\n"
        ".Llast%=:\n\t"
        "buffer_store_dwordx4 v[72:75], v47, %[rf], s41 offen sc1\n\t" "s_mov_b32 s47, 3\n\t" NOP(4) VPOST ARRIVE(12) "s_barrier\n\t"
        "s_branch .Lend%=\n"
        R2MAX
        ".Lnorm%=:\n\t"
        "s_cmp_lg_u32 %[wv], 3\n\t" "s_cbranch_scc1 .LNA%=\n\t" "s_cmp_eq_u32 s47, 0\n\t" "s_cbranch_scc1 .LA%=\n\t" "s_branch .LC%=\n"
        ".LN%=:\n\t"
        "v_mov_b64 v[92:93], -1\n\t" "v_mov_b64 v[94:95], -1\n\t" "s_mov_b32 s65, s44\n\t" "s_mov_b32 s78, -100\n\t" "s_mov_b32 s79, 4\n\t"
        "s_branch .Lent%=\n"
        ".LNA%=:\n\t" PZA0
        "s_add_u32 s50, s36, s52\n\t"
        RELN(".Lna%=")
        ABODY(NOP(PCA), LASTA(".Lxa%=") PZA, ".Lfail%=", PZNA) BARA ARRIVE(8) TSA(3)
        SV(0, 0) SV(1, 0) SV(0, 1) SV(1, 1) SV(0, 2) SV(1, 2) SV(0, 3) SV(1, 3) ZER(64) ZER(68) ZER(72) ZER(76)
        VPOST TSA(7) ZER(80) ZER(84) ZER(24) ZER(28)
        "v_add_u32 v88, 4096, v21\n\t" "v_add_u32 v89, 8192, v21\n\t" "v_add_u32 v90, 12288, v21\n\t"
        PZB0
        "s_lshl_b32 s61, s45, 7\n\t" "s_add_u32 s61, s61, " STR(PGO_R - XSYN) "\n\t" "s_cmp_lg_u32 %[wv], 0\n\t"
        WLG(0) "s_barrier\n\t" PZB
        PSW(92, 0) PSW(92, 272) PSW(92, 640) PSW(92, 912) PSW(92, 1280) PSW(92, 1552) PSW(92, 1920) PSW(92, 2192) PSW(92, 3840)
        "ds_read2_b32 v[50:51], v38 offset0:0 offset1:6\n\t"
        "s_cbranch_scc1 .LNB%=\n\t"
        "s_memrealtime s[72:73]\n"
        ".Lv0%=:\n\t"
        "ds_read_b32 v62, v38 offset:" CNT(12) "\n\t"
        "ds_read2_b32 v[112:113], v21 offset0:0 offset1:68\n\t" "ds_read2_b32 v[114:115], v21 offset0:16 offset1:84\n\t"
        "ds_read2_b32 v[116:117], v88 offset0:0 offset1:68\n\t" "ds_read2_b32 v[118:119], v88 offset0:16 offset1:84\n\t"
        "ds_read2_b32 v[120:121], v89 offset0:0 offset1:68\n\t" "ds_read2_b32 v[122:123], v89 offset0:16 offset1:84\n\t"
        "ds_read2_b32 v[124:125], v90 offset0:0 offset1:68\n\t" "ds_read2_b32 v[126:127], v90 offset0:16 offset1:84\n\t"
        WLG(0) "v_cmp_gt_u32 vcc, s48, v62\n\t" "s_cbranch_vccnz .Lv0%=\n\t"
        "s_add_u32 s50, s72, " STR(TVM) "\n\t" "s_lshl_b32 s50, s50, 17\n\t" "s_lshr_b32 s50, s50, 1\n\t"
        PKA(112, 112, 116) PKA(114, 114, 118) PKA(120, 120, 124) PKA(122, 122, 126) PKA(112, 112, 120) PKA(114, 114, 122)
        "v_lshrrev_b32 v91, 20, v51\n\t" "v_readfirstlane_b32 s49, v50\n\t"
        "v_cvt_pk_bf16_f32 v112, v112, v113\n\t" "v_cvt_pk_bf16_f32 v113, v114, v115\n\t" "v_or_b32 v114, s50, v91\n\t"
        "s_cmp_eq_u32 s45, 63\n\t" "s_cbranch_scc1 .Lv2%=\n\t"
        "s_sleep " STR(SLV) "\n"
        ".Lv2%=:\n\t"
        "buffer_store_dwordx2 v[112:113], v20, %[rf], s42 offen sc1\n\t"
        "buffer_store_dword v114, v22, %[rs], s61 offen sc1\n\t"
        "s_cmp_eq_u32 s49, s65\n\t" "s_cbranch_scc1 .LNB%=\n\t"
        "s_mov_b32 s65, s49\n\t" KHST "s_sub_u32 s49, s45, s78\n\t" "s_mov_b32 s78, s45\n\t"
        "s_cmp_gt_i32 s49, " STR(GAP) "\n\t" "s_cbranch_scc1 .LNB%=\n\t" "s_cmp_lt_u32 s45, s79\n\t" "s_cbranch_scc1 .LNB%=\n\t"
        "s_add_u32 s79, s45, 3\n\t" "s_lshl_b32 s49, s45, 6\n\t" "v_add_u32 v50, " STR(TLB + ((TLB + TLC) << 10) + ((TLB + TLC + TLP) << 20)) ", v51\n\t"
        "buffer_store_dword v50, v22, %[rs], s49 offen offset:128 sc1\n"
        ".LNB%=:\n\t"
        WLG(0) "v_readfirstlane_b32 s58, v51\n\t" EXTRA
        "s_mov_b32 s47, 3\n\t"
        "s_cmp_eq_u32 s45, 63\n\t"
        "s_cbranch_scc1 .Lend%=\n"
        ".LNC%=:\n\t" PZC0
        "s_add_u32 s50, s36, s53\n\t"
        WLG(0) RELN(".Lnc%=")
        CISSUE("", LASTC(".Lxc%="))
        CBODY(0, PZC, PZNC, ".Lfail%=") BARC
        ARRIVE(16) TSW(4)
        NEXTN
        ".Lfail%=:\n\t" KCNT
        "s_cmp_lg_u32 s47, 2\n\t" "s_cbranch_scc1 .Lnu%=\n\t"
        "s_mov_b32 s37, s36\n\t" "s_mov_b32 s36, s48\n\t" "s_sub_u32 s45, s45, 1\n\t" "s_sub_u32 s38, s38, 4\n\t" "s_sub_u32 s39, s39, " STR(EXF) "\n\t" "s_sub_u32 s40, s40, " STR(EXF) "\n\t"
        "s_sub_u32 s41, s41, " STR(EXK) "\n\t" "s_sub_u32 s42, s42, " STR(EXK) "\n\t" "s_sub_u32 s43, s43, s55\n\t" "s_sub_u32 s43, s43, s70\n\t" "s_sub_u32 s46, s46, 64\n"
        ".Lnu%=:\n\t"
        "s_mov_b32 s51, 1\n\t" "s_nop 7\n\t" WVM(0) "s_sub_u32 s98, s98, 1\n\t" "s_cbranch_scc1 .Lcap%=\n"
        ".Lent%=:\n\t"
        "s_cmp_eq_u32 s47, 1\n\t" "s_cselect_b32 s99, s66, s67\n\t" "s_cbranch_scc1 .Lnz%=\n\t" ZALL ".Lnz%=:\n\t"
        "s_cmp_eq_u32 s47, 0\n\t" "s_cselect_b32 s99, 0, s99\n\t" "s_cselect_b32 s50, s52, s53\n\t" "s_add_u32 s99, s99, s43\n\t" "s_add_u32 s50, s50, s36\n\t"
        SHRD "s_memrealtime s[72:73]\n\t"
        "s_cmp_eq_u32 %[wv], 3\n\t" "s_cselect_b32 s49, 0, 3\n\t" "s_add_u32 s49, s49, s47\n\t" "s_cmp_lg_u32 s51, 0\n\t" "s_cselect_b32 s49, s49, 7\n\t" "s_cselect_b64 vcc, -1, 0\n\t"
        "s_cselect_b32 s51, 0, 14\n\t" "s_mov_b32 s48, .Lpc%=-.Lnorm%=\n\t"
        RETA(0, ".Lua%=") RETA(1, ".Ltb%=y") RETA(2, ".Luc%=") RETA(3, ".Lna%=") RETA(5, ".Lnc%=")
        "s_getpc_b64 s[96:97]\n" ".Lpc%=:\n\t" "s_sub_u32 s96, s96, s48\n\t" "s_subb_u32 s97, s97, 0\n\t"
        SHUP "s_sub_u32 s49, s99, s72\n\t" "s_add_u32 s49, s49, s44\n"
        ".Lbump%=:\n\t"
        "s_cmp_ge_i32 s49, " STR(PREP) "\n\t" "s_cbranch_scc1 .Lok%=\n\t" "s_add_u32 s44, s44, %[tr]\n\t" "s_add_u32 s49, s49, %[tr]\n\t" "s_branch .Lbump%=\n"
        ".Lok%=:\n\t"
        "s_mov_b64 exec, 1\n\t" "v_mov_b32 v62, s44\n\t" "ds_max_u32 v38, v62\n\t" KTRC "s_mov_b64 exec, -1\n\t"
        "s_add_u32 s99, s99, s44\n\t" "s_sub_u32 s99, s99, s51\n\t"
        WLG(0) "s_memrealtime s[72:73]\n\t" "s_nop 3\n\t" "s_memrealtime s[74:75]\n\t" "s_nop 3\n"
        CLKL(".Lth%=", "s99")
        "s_setpc_b64 s[96:97]\n"
        ".Lcap%=:\n\t" "s_add_u32 s47, s47, 8\n"
        ".Lend%=:\n\t"
        "s_waitcnt vmcnt(0) lgkmcnt(0)\n\t"
        "s_mov_b32 s50, s64\n\t" "s_mov_b32 s49, s98\n\t" "s_mov_b32 s51, s68\n\t" "s_mov_b32 m0, s60"
        : "+{s[36:51]}"(sa), "+{s[52:59]}"(sc), "+{v[60:91]}"(keep), "=&{v[116:117]}"(ov), "+{a[192:207]}"(fv), "+{a[208:215]}"(fk), "+{v[8:23]}"(kd) FINEO TUNEO
        : "{v[32:47]}"(kc), [rs] "s"(rsx), [rf] "s"(rsf), [dm] "s"(__builtin_amdgcn_readfirstlane(PGO_Y + b * 128)), [tg] "s"(__builtin_amdgcn_readfirstlane(tag)), [r2] "s"(__builtin_amdgcn_readfirstlane(XR2B + flip)), [mb] "s"(__builtin_amdgcn_readfirstlane(ldsb + w * 4096)), [wv] "s"(__builtin_amdgcn_readfirstlane(w)), [tr] "s"(TR) TRCI,
          "{a[0:31]}"(wa[0]), "{a[32:63]}"(wa[1]), "{a[64:95]}"(wa[2]), "{a[96:127]}"(wa[3]), "{a[128:159]}"(wa[4]), "{a[160:191]}"(wa[5]),
          "{v[128:159]}"(wo[0]), "{v[160:191]}"(wo[1]), "{v[192:223]}"(wo[2]), "{v[224:255]}"(wo[3])
        : CL8(24, 25, 26, 27, 28, 29, 30, 31), "v48", "v49", "v50", "v51", CL8(52, 53, 54, 55, 56, 57, 58, 59), "v92", "v93", "v94", "v95", CL96, "v112", "v113", "v114", "v115", "v118", "v119", CL120,
          "s60", "s61", "s62", "s63", "s64", "s65", "s66", "s67", "s68", "s69", "s70", "s71", "s72", "s73", "s74", "s75", "s76", "s77", "s78", "s79", "s96", "s97", "s98", "s99", "vcc", "scc", "memory");
    const int code = __builtin_amdgcn_readfirstlane(sa[11]);
    xp = __builtin_amdgcn_readfirstlane(sa[14]);
    tcnt += sa[15];
    sa[15] = 0;
    if (code == 3) break;
    const int seg = __builtin_amdgcn_readfirstlane(sa[9]);
    if (code == 5) { XWAIT(seg + 2) xp = max(xp, seg + 2); sa[11] = 2; }
    else if (code == 1) { __builtin_amdgcn_raw_buffer_store_b64((u32x2){nosent(ov[0]), nosent(ov[1])}, rsx, sto, sa[0], 16); sa[11] = 2; }
    else { sc[2] += 1; sa[13] = 0; sa[11] = code - 8; sa[15] = 1; }
  }
  LBAR();
  if (tid == 0) __builtin_amdgcn_raw_buffer_store_b32(1u, rsx, 0, PGO_D + vcu * 128, 16);
  if (b == 0 && w == 1) {
    for (int it = 0; it < SPIN_CAP; ++it) {
      asm volatile("" ::: "memory");
      if (__ballot(__builtin_amdgcn_raw_buffer_load_b32(rsx, lane * 128, PGO_S, 16) != tag || __builtin_amdgcn_raw_buffer_load_b32(rsx, lane * 128 + 8192, PGO_S, 16) != tag) == 0) break;
    }
    if (lane == 0) __builtin_amdgcn_raw_buffer_store_b32(tag, rsx, 0, XEPO, 16);
  }
}

#define XSTAT 3
#define XVM 8
#define XP0 -1
#define XSC " sc1"
#define XPITCH 1056
#define XSLOT (32 * XPITCH)
#define XW(HB, t) "a[" STR(HB) "+8*xk+4*" #t ":" STR(HB) "+8*xk+4*" #t "+3]"
#define XF(rt) "v[96+8*(xk&3)+4*" #rt ":96+8*(xk&3)+4*" #rt "+3]"
#define XFN(rt) "v[96+8*((xk+3)&3)+4*" #rt ":96+8*((xk+3)&3)+4*" #rt "+3]"
#define XA(AC, o) "v[" STR(AC) "+" #o ":" STR(AC) "+" #o "+3]"
#define XM1(d, a, b, FIRST) ".if (xk == 0) && " STR(FIRST) "\n\tv_mfma_f32_16x16x32_bf16 " d ", " a ", " b ", 0\n\t.else\n\tv_mfma_f32_16x16x32_bf16 " d ", " a ", " b ", " d "\n\t.endif\n\t"
#define XMF_QK(i, HB, AC, F) XMF_QK##i(HB, AC, F)
#define XMF_QK0(HB, AC, F) XM1(XA(AC, 0), XW(HB, 0), XF(0), F)
#define XMF_QK1(HB, AC, F) XM1(XA(AC, 4), XW(HB, 1), XF(0), F)
#define XMF_QK2(HB, AC, F) XM1(XA(AC, 8), XW(HB, 0), XF(1), F)
#define XMF_QK3(HB, AC, F) XM1(XA(AC, 12), XW(HB, 1), XF(1), F)
#define XMF_V(i, HB, AC, F) XMF_V##i(HB, AC, F)
#define XMF_V0(HB, AC, F) XM1(XA(AC, 0), XF(0), XW(HB, 0), F)
#define XMF_V1(HB, AC, F) XM1(XA(AC, 8), XF(0), XW(HB, 1), F)
#define XMF_V2(HB, AC, F) XM1(XA(AC, 4), XF(1), XW(HB, 0), F)
#define XMF_V3(HB, AC, F) XM1(XA(AC, 12), XF(1), XW(HB, 1), F)
#define XRD(rt, CUR, NXT) ".if xk < 13\n\tds_read_b128 " XFN(rt) ", " CUR " offset:64*(xk+3)+" STR(rt * 16 * XPITCH) "\n\t.else\n\t" \
                          "ds_read_b128 " XFN(rt) ", " NXT " offset:64*(xk-13)+" STR(rt * 16 * XPITCH) "\n\t.endif\n\t"
#define XCV(EPI, PV, i) ".if " STR(EPI) " && (xk >= 1) && (xk < 3)\n\tv_cvt_pk_bf16_f32 v[128+4*(xk-1)+" #i "], v[" STR(PV) "+8*(xk-1)+2*" #i "], v[" STR(PV) "+8*(xk-1)+2*" #i "+1]\n\t.endif\n\t"
#define XPF 0
#define NPF 8
#define NPS 12
#define XTOUCH ".if " STR(XPF) " && (xk == 4)\n\t" "s_add_u32 %[pj], %[pj], 0x10000\n\t" "s_cmp_ge_i32 %[pj], 0\n\t" "s_cselect_b32 s54, %[pn], 0\n\t" ".endif\n\t" \
  ".if " STR(XPF) " && (xk == 7)\n\t" "s_and_b32 s56, %[pj], 0x1f0000\n\t" "s_xor_b32 s56, s56, %[pb]\n\t" ".endif\n\t" \
  ".if " STR(XPF) " && (xk == 12)\n\t" "buffer_load_dword %[pd], %[pv], s[52:55], s56 offen\n\t" ".endif\n\t"
#define XHALF(MF, HB, AC, PV, EPI, CUR, NXT, DSLOT, GOFF)                                                   \
  ".set xk, 0\n\t"                                                                                          \
  ".rept 16\n\t"                                                                                            \
  "s_waitcnt lgkmcnt(4)\n\t"                                                                                \
  MF(0, HB, AC, EPI) XRD(0, CUR, NXT) XCV(EPI, PV, 0)                                                       \
  ".if (xk >= 5) && (xk < 13)\n\t"                                                                          \
  "s_add_u32 m0, m0, " STR(XPITCH) "\n\t"                                                                   \
  ".endif\n\t"                                                                                              \
  MF(1, HB, AC, EPI) XRD(1, CUR, NXT) XCV(EPI, PV, 1)                                                       \
  ".if (xk >= 5) && (xk < 13)\n\t"                                                                          \
  "s_add_u32 %[dg], %[dg], 2048\n\t"                                                                        \
  ".endif\n\t"                                                                                              \
  MF(2, HB, AC, EPI) XCV(EPI, PV, 2)                                                                        \
  ".if (xk >= 4) && (xk < 12)\n\t"                                                                          \
  "buffer_load_dwordx4 %[l16], %[rx], %[dg] offen lds\n\t"                                                  \
  ".endif\n\t"                                                                                              \
  ".if xk == 0\n\t"                                                                                         \
  "s_add_u32 m0, %[dl], " STR((DSLOT) * XSLOT) "\n\t" "s_add_u32 %[dg], %[gb], " STR(GOFF) "\n\t"           \
  ".endif\n\t"                                                                                              \
  ".if " STR(EPI) " && (xk == " STR(XSTAT) ")\n\t"                                                                       \
  "buffer_store_dwordx4 v[128:131], v137, %[rf], %[sg] offen" XSC "\n\t"                                       \
  ".endif\n\t"                                                                                              \
  ".if " STR(EPI) " && (xk == 13)\n\t"                                                                      \
  "v_mov_b32 v137, %[l16]\n\t" "v_mov_b32 v138, %[lp2]\n\t"                                                 \
  ".endif\n\t"                                                                                              \
  ".if " STR(EPI) " && (xk == 14)\n\t"                                                                      \
  "s_add_u32 %[sg], %[sg], " STR(NH * 8 * 1024) "\n\t"                                                      \
  "v_add_u32 v136, 1, v136\n\t"                                                                             \
  ".endif\n\t"                                                                                              \
  ".if !" STR(EPI) " && (xk == 1)\n\t"                                                                      \
  "buffer_store_dword v136, %[fo], %[rp], 0 offen sc1\n\t"                                                  \
  ".endif\n\t"                                                                                              \
  MF(3, HB, AC, EPI) XCV(EPI, PV, 3)                                                                        \
  XTOUCH                                                                                                    \
  ".if xk == 0\n\t"                                                                                         \
  "s_waitcnt vmcnt(" STR(XVM) "+" STR(XPF) ")\n\t" "s_barrier\n\t"                                                               \
  ".endif\n\t"                                                                                              \
  ".if xk == 2\n\t"                                                                                         \
  "s_min_u32 %[dg], %[dg], " STR(S_LEN * DM * 2 - 16384 + 1024) "\n\t"                                      \
  ".endif\n\t"                                                                                              \
  ".if " STR(EPI) " && (xk == " STR(XSTAT) ")\n\t"                                                                       \
  "buffer_store_dwordx4 v[132:135], v138, %[rf], %[sg] offen" XSC "\n\t"                                       \
  ".endif\n\t"                                                                                              \
  ".set xk, xk+1\n\t"                                                                                       \
  ".endr\n\t"
#define XLOOP(MF)                                                                                           \
  "s_mov_b32 %[kp], m0\n\t" "s_mov_b64 s[52:53], %[wo]\n\t" "s_mov_b32 s55, 0x20000\n\t"                       \
  "v_mov_b32 v136, %[p0]\n\t" "v_mov_b32 v137, " STR(OOR) "\n\t" "v_mov_b32 v138, " STR(OOR) "\n\t"                                                                                \
  "ds_read_b128 v[96:99], %[r0]\n\tds_read_b128 v[100:103], %[r0] offset:" STR(16 * XPITCH) "\n\t"          \
  "ds_read_b128 v[104:107], %[r0] offset:64\n\tds_read_b128 v[108:111], %[r0] offset:64+" STR(16 * XPITCH) "\n\t"   \
  "ds_read_b128 v[112:115], %[r0] offset:128\n\tds_read_b128 v[116:119], %[r0] offset:128+" STR(16 * XPITCH) "\n"   \
  ".Lx%=:\n\t"                                                                                              \
  XHALF(MF, 0, 64, 80, 1, "%[r0]", "%[r1]", 3, 66560) XHALF(MF, 128, 64, 80, 0, "%[r1]", "%[r2]", 0, 131072) \
  XHALF(MF, 0, 80, 64, 1, "%[r2]", "%[r3]", 1, 132096) XHALF(MF, 128, 80, 64, 0, "%[r3]", "%[r0]", 2, 196608) \
  "s_add_u32 %[gb], %[gb], 131072\n\t"                                                                      \
  "s_sub_u32 %[cnt], %[cnt], 1\n\t"                                                                         \
  "s_cmp_lg_u32 %[cnt], 0\n\t"                                                                              \
  "s_cbranch_scc1 .Lx%=\n\t"                                                                                \
  "s_mov_b32 m0, %[kp]\n\t"                                                                                 \
  "s_nop 7\n\t" "s_waitcnt lgkmcnt(0)"

template <bool ISV>
__device__ __forceinline__ void x_role(unsigned char* lds, const u16* __restrict__ x, const u16* __restrict__ Wm, int fo0, int head,
                                       __amdgpu_buffer_rsrc_t rsf, u32* prog, u32 tag, int lane, int w, const u16* __restrict__ Wo, int gs) {
  const int ldsb = (int)(size_t)(LAS unsigned char*)lds;
  const __amdgpu_buffer_rsrc_t rsxin = __builtin_amdgcn_make_buffer_rsrc((void*)x, 0, S_LEN * DM * 2, 0x00020000);
  const __amdgpu_buffer_rsrc_t rsp = __builtin_amdgcn_make_buffer_rsrc((void*)prog, 0, 128, 0x00020000);
  const int lr = lane & 15, lg = lane >> 4;
  const int xrd = ldsb + lr * XPITCH + lg * 16;
  const int fob = fo0 + (ISV ? (head * 8 + 2 * w) : (head * 8 + w)) * 1024;
  const int p2 = ISV ? 1024 : 4096;
  u32x32 xw[8];
  {
    const __amdgpu_buffer_rsrc_t rw = __builtin_amdgcn_make_buffer_rsrc((void*)Wm, 0, DM * HD * 2, 0x00020000);
    const int vq = (lane >> 4) * (HD * 2) + head * 256 + (lane & 15) * 16;
#define XWISSUE(c) _Pragma("unroll") for (int j = 0; j < 8; ++j)                                                       \
      asm volatile("s_mov_b32 m0, %0\n\ts_nop 3\n\tbuffer_load_dwordx4 %1, %2, %3 offen lds"                           \
                   :: "s"(ldsb + ((c) & 3) * XSLOT + (8 * w + j) * XPITCH), "v"(vq), "s"(rw), "s"((128 * (c) + 32 * w + 4 * j) * (HD * 2)) : "memory");
#define XXISSUE(h) _Pragma("unroll") for (int j = 0; j < 8; ++j)                                                       \
      asm volatile("s_mov_b32 m0, %0\n\ts_nop 3\n\tbuffer_load_dwordx4 %1, %2, %3 offen lds"                           \
                   :: "s"(ldsb + (h) * XSLOT + (8 * w + j) * XPITCH), "v"(lane * 16), "s"(rsxin), "s"((((h) >> 1) * 32 + 8 * w + j) * 2048 + ((h) & 1) * 1024) : "memory");
    XWISSUE(0) XWISSUE(1) XWISSUE(2) XWISSUE(3)
    const int q = lr >> 2, p = lr & 3;
#pragma unroll
    for (int c = 0; c < 8; ++c) {
      asm volatile("s_waitcnt vmcnt(24)\n\ts_barrier" ::: "memory");
#pragma unroll
      for (int s = 0; s < 4; ++s)
#pragma unroll
        for (int t = 0; t < 2; ++t) {
          const unsigned char* base = lds + (c & 3) * XSLOT + (8 * s + 2 * lg) * XPITCH + q * 256 + w * 64 + p * 16 + t * 8;
          const u32x2 t0 = __builtin_bit_cast(u32x2, __builtin_amdgcn_ds_read_tr16_b64_v4i16((LAS v4i16_t*)(base)));
          const u32x2 t1 = __builtin_bit_cast(u32x2, __builtin_amdgcn_ds_read_tr16_b64_v4i16((LAS v4i16_t*)(base + XPITCH)));
          const int o = ((4 * c + s) * 2 + t) * 4;
          xw[o >> 5][(o & 31) + 0] = t0[0]; xw[o >> 5][(o & 31) + 1] = t0[1]; xw[o >> 5][(o & 31) + 2] = t1[0]; xw[o >> 5][(o & 31) + 3] = t1[1];
        }
      LBAR();
      if (c < 4) { XWISSUE(c + 4) } else if (c < 7) { XXISSUE(c - 4) }
    }
  }
  asm volatile("s_waitcnt vmcnt(8)\n\ts_barrier" ::: "memory");
  u32x16 xacc;
  u32 pd;
  {
    int pj = (((gs & 7) == (((384 - (int)(size_t)Wo) & 1023) >> 7) ? NPS : NPF) - 2 * NSEG - 1) * 65536;
    u32 kp, dg; int gb = 8 * w * 2048, cnt = NSEG / 2, sg = fob - NH * 8 * 1024;
#define XASM(MF)                                                                                            \
    asm volatile(XLOOP(MF)                                                                                  \
                 : "=&{v[80:95]}"(xacc), [kp] "=&s"(kp), [dg] "=&s"(dg), [gb] "+s"(gb), [cnt] "+s"(cnt), [sg] "+s"(sg), [pj] "+s"(pj), [pd] "=&v"(pd) \
                 : [r0] "v"(xrd), [r1] "v"(xrd + XSLOT), [r2] "v"(xrd + 2 * XSLOT), [r3] "v"(xrd + 3 * XSLOT), [l16] "v"(lane * 16), [lp2] "v"(lane * 16 + p2), \
                   [rx] "s"(rsxin), [rf] "s"(rsf), [rp] "s"(rsp), [fo] "v"(lane == 0 && w == 0 ? 0 : OOR), [dl] "s"(ldsb + 8 * w * XPITCH), [p0] "s"(tag + XP0), \
                   [wo] "s"(Wo), [pn] "s"(gs < 0 ? 0 : DM * HD * 2), [pb] "s"((16 * w + ((gs + 4 * w) & 15)) * 64 * DM * 2), [pv] "v"((lane >> 1) * (DM * 2) + (gs & 15) * 128 + (lane & 1) * 64), \
                   "{a[0:31]}"(xw[0]), "{a[32:63]}"(xw[1]), "{a[64:95]}"(xw[2]), "{a[96:127]}"(xw[3]),      \
                   "{a[128:159]}"(xw[4]), "{a[160:191]}"(xw[5]), "{a[192:223]}"(xw[6]), "{a[224:255]}"(xw[7]) \
                 : CL64, CL72, CL96, CL112, CL120, CL8(128, 129, 130, 131, 132, 133, 134, 135), "v136", "v137", "v138", "s52", "s53", "s54", "s55", "s56", "scc", "memory");
    if (ISV) { XASM(XMF_V) } else { XASM(XMF_QK) }
  }
#pragma unroll
  for (int jj = 0; jj < 2; ++jj) {
    u32x4 pk;
#pragma unroll
    for (int e = 0; e < 4; ++e) pk[e] = pack2(__uint_as_float(xacc[8 * jj + 2 * e]), __uint_as_float(xacc[8 * jj + 2 * e + 1]));
    __builtin_amdgcn_raw_buffer_store_b128(pk, rsf, lane * 16 + p2 * jj, fob + (NSEG - 1) * (NH * 8 * 1024), 16);
  }
  asm volatile("s_waitcnt vmcnt(0)\n\ts_barrier" : "+v"(pd) :: "memory");
  if (lane == 0 && w == 0) __hip_atomic_store(prog, tag + NSEG + 8, RLX_AGENT);
}

#define SU_CP 4288
#define SU_U 0
#define SU_K (2 * SU_CP)
#define SU_Y (SU_K + 10400)
#define SU_CH (SU_Y + 4096)
#define SST(i)
__device__ __forceinline__ int koff(int i) {
  const int x = i & 127;
  return i < 256 ? (32 * (i >> 7) + 16 * ((i >> 2) & 1) + (x >> 3)) * 16 + (i & 3) * 4
       : i < 384 ? 1024 + x * 4
       : i < 512 ? 1024 + (32 + 16 * (x >> 6) + ((x >> 1) & 15)) * 16 + (2 * ((x >> 5) & 1) + (x & 1)) * 4
                 : 2048 + (16 * ((i - 512) >> 6) + (i & 15)) * 16 + ((i >> 4) & 3) * 4;
}
struct SsmIn { u32x4 ka, kb; u32x2 kc; u32 kd, ke[4]; u32x4 uv[4]; u32x2 up, dsk; };
#define SSM_GOT(in) asm volatile("" : "+v"(in.ka), "+v"(in.kb), "+v"(in.kc), "+v"(in.kd), "+v"(in.ke[0]), "+v"(in.ke[1]), "+v"(in.ke[2]), "+v"(in.ke[3]), \
                                      "+v"(in.uv[0]), "+v"(in.uv[1]), "+v"(in.uv[2]), "+v"(in.uv[3]), "+v"(in.up), "+v"(in.dsk))
__device__ __forceinline__ void ssm_load(SsmIn& in, int d0, __amdgpu_buffer_rsrc_t rst, __amdgpu_buffer_rsrc_t rsd, int tid, int lane, int w) {
  const int so = 2 * SSM_L * SSM_D * 2 + (d0 + w) * 3072, ia = lane < 32 ? 8 * lane : 128 + 4 * lane;
  in.ka = __builtin_amdgcn_raw_buffer_load_b128(rst, koff(ia), so, 16);
  in.kb = __builtin_amdgcn_raw_buffer_load_b128(rst, koff(ia + 4), so, 16);
  in.kc = __builtin_amdgcn_raw_buffer_load_b64(rst, koff(384 + 2 * lane), so, 16);
  in.kd = __builtin_amdgcn_raw_buffer_load_b32(rst, koff(386 + 2 * lane), so, 16);
#pragma unroll
  for (int i = 0; i < 4; ++i) in.ke[i] = __builtin_amdgcn_raw_buffer_load_b32(rst, koff(512 + 64 * (i >> 1) + (i & 1) + lane), so, 16);
#pragma unroll
  for (int i = 0; i < 4; ++i) in.uv[i] = __builtin_amdgcn_raw_buffer_load_b128(rst, tid * 16, d0 * 4096 + i * 4096, 16);
  in.up = __builtin_amdgcn_raw_buffer_load_b64(rst, tid ? tid * 16 - 8 : OOR, d0 * 4096 + 3 * 4096, 16);
  in.dsk = __builtin_amdgcn_raw_buffer_load_b64(rsd, 0, d0 * 2, 0);
}
__device__ __forceinline__ float reslo(float v, u32 h) { return v - __uint_as_float(h << 16); }
__device__ __forceinline__ float reshi(float v, u32 h) { return v - __uint_as_float(h & 0xffff0000u); }
__device__ __forceinline__ int ssm_unit(unsigned char* sm, int d0, SsmIn& in, bool pend, __amdgpu_buffer_rsrc_t rst, __amdgpu_buffer_rsrc_t rsx, int fb,
                                        __amdgpu_buffer_rsrc_t rsd, int tid, int lane, int w) {
  unsigned char* cb = sm + w * SU_CH;
  SST(0)
  const int one = tid == 0 ? 0 : OOR;
  u32 tk = __builtin_amdgcn_raw_ptr_buffer_atomic_add_i32(1, rsx, one, XTKT, 0);
  const int r = lane & 31, h = lane >> 5;
#pragma unroll
  for (int c = 0; c < 4; ++c) {
    const u32 sel = (c & 1) ? 0x07060302u : 0x05040100u;
    u32x4 e, o;
#pragma unroll
    for (int i = 0; i < 4; ++i) {
      e[i] = __builtin_amdgcn_perm(in.uv[i][2 + (c >> 1)], in.uv[i][c >> 1], sel);
      o[i] = __builtin_amdgcn_perm(in.uv[i][c >> 1], i ? in.uv[i ? i - 1 : 0][2 + (c >> 1)] : in.up[c >> 1], sel);
    }
    *(u32x4*)(sm + c * SU_CH + SU_U + 96 + tid * 16) = e;
    *(u32x4*)(sm + c * SU_CH + SU_U + SU_CP + 96 + tid * 16) = o;
  }
  SST(1)
  {
    unsigned char* kr = cb + SU_K + ((lane >> 2) + 1) * 80 + (3 - (lane & 3)) * 16;
    const f32x4 a = __builtin_bit_cast(f32x4, in.ka), b = __builtin_bit_cast(f32x4, in.kb);
    const bool dn = lane < 32;
    const float v[8] = {a[0], dn ? a[1] : a[0], dn ? a[2] : a[1], dn ? a[3] : a[2], dn ? b[0] : a[2], dn ? b[1] : a[2], dn ? b[2] : a[3], dn ? b[3] : b[0]};
    u32x4 hi, lo;
#pragma unroll
    for (int e = 0; e < 4; ++e) { hi[e] = pack2(v[7 - 2 * e], v[6 - 2 * e]); lo[e] = pack2(reslo(v[7 - 2 * e], hi[e]), reshi(v[6 - 2 * e], hi[e])); }
    *(u32x4*)(kr) = hi; *(u32x4*)(kr + 5200) = lo;
    {
      const float v0 = __uint_as_float(in.kc[0]), v1 = __uint_as_float(in.kc[1]), v2 = __uint_as_float(in.kd);
      hi = (u32x4){pack2(v2, v2), pack2(v1, v1), pack2(v1, v0), pack2(v0, v0)};
      const float r0 = reshi(v0, hi[3]), r1 = reshi(v1, hi[1]), r2 = reshi(v2, hi[0]);
      *(u32x4*)(kr + 1280) = hi; *(u32x4*)(kr + 1280 + 5200) = (u32x4){pack2(r2, r2), pack2(r1, r1), pack2(r1, r0), pack2(r0, r0)};
    }
#pragma unroll
    for (int j = 0; j < 2; ++j) {
      const float v0 = __uint_as_float(in.ke[2 * j]), v1 = __uint_as_float(in.ke[2 * j + 1]);
      const u32 h0 = pack2(v0, v0), h1 = pack2(v1, v1), sel = (lane & 1) ? 0x01000100u : 0x05040100u;
      const float r0 = reshi(v0, h0), r1 = reshi(v1, h1);
      const u32 l0 = pack2(r0, r0), l1 = pack2(r1, r1);
      *(u32x4*)(kr + 2560 + 1280 * j) = (u32x4){h1, __builtin_amdgcn_perm(h0, h1, sel), h0, h0};
      *(u32x4*)(kr + 2560 + 1280 * j + 5200) = (u32x4){l1, __builtin_amdgcn_perm(l0, l1, sel), l0, l0};
    }
  }
  SST(2)
  f32x4 sk[8];
  {
    const u32 d01 = __builtin_amdgcn_readfirstlane(in.dsk[0]), d23 = __builtin_amdgcn_readfirstlane(in.dsk[1]);
#pragma unroll
    for (int i = 0; i < 4; ++i)
      asm("v_dot2_f32_bf16 %0, %8, %12, 0\n\tv_dot2_f32_bf16 %1, %8, %13, 0\n\tv_dot2_f32_bf16 %2, %9, %14, 0\n\tv_dot2_f32_bf16 %3, %9, %15, 0\n\t"
          "v_dot2_f32_bf16 %4, %10, %12, 0\n\tv_dot2_f32_bf16 %5, %10, %13, 0\n\tv_dot2_f32_bf16 %6, %11, %14, 0\n\tv_dot2_f32_bf16 %7, %11, %15, 0\n\t"
          "v_cvt_pk_bf16_f32 %0, 0, %0\n\tv_cvt_pk_bf16_f32 %1, 0, %1\n\tv_cvt_pk_bf16_f32 %2, 0, %2\n\tv_cvt_pk_bf16_f32 %3, 0, %3\n\t"
          "v_cvt_pk_bf16_f32 %4, 0, %4\n\tv_cvt_pk_bf16_f32 %5, 0, %5\n\tv_cvt_pk_bf16_f32 %6, 0, %6\n\tv_cvt_pk_bf16_f32 %7, 0, %7"
          : "=&v"(sk[2 * i][0]), "=&v"(sk[2 * i][1]), "=&v"(sk[2 * i][2]), "=&v"(sk[2 * i][3]),
            "=&v"(sk[2 * i + 1][0]), "=&v"(sk[2 * i + 1][1]), "=&v"(sk[2 * i + 1][2]), "=&v"(sk[2 * i + 1][3])
          : "v"(in.uv[i][0]), "v"(in.uv[i][1]), "v"(in.uv[i][2]), "v"(in.uv[i][3]), "s"(d01 & 0xffffu), "s"(d01 & 0xffff0000u), "s"(d23 & 0xffffu), "s"(d23 & 0xffff0000u));
  }
  asm volatile("s_waitcnt vmcnt(0)" : "+v"(tk), "+v"(sk[0]), "+v"(sk[1]), "+v"(sk[2]), "+v"(sk[3]), "+v"(sk[4]), "+v"(sk[5]), "+v"(sk[6]), "+v"(sk[7]) :: "memory");
  if (tid == 0) *(u32*)(sm + 4 * SU_CH) = tk + 65u;
  LBAR();
  SST(3)
  if (pend) __builtin_amdgcn_raw_ptr_buffer_atomic_add_i32(1, rsx, one, XYDN, 0);
  const int nxt = __builtin_amdgcn_readfirstlane(*(const u32*)(sm + 4 * SU_CH));
  if (nxt < 256) ssm_load(in, 4 * nxt, rst, rsd, tid, lane, w);
  SST(4)
  f32x16 cv0, cv1;
  {
    const int m0 = 48 - 15 + 8 * h + r;
    const int cbo = (int)(size_t)(LAS unsigned char*)cb;
    int ua = cbo + SU_U + (m0 & 1) * SU_CP + ((m0 + 1) & ~1) * 2 - 32, ub = ua + 2048;
    const int kb = cbo + SU_K + 16 * h;
    int a1 = kb + (33 + r) * 80, a0 = kb + (1 + r) * 80, cnt = 32;
#define CRA(S, ad, O) "ds_read2_b32 " RNG("v", S, 2) ", " ad " offset0:" STR((O) / 4) " offset1:" STR((O) / 4 + 1) "\n\t" \
                      "ds_read2_b32 " RNG("v", (S) + 2, 2) ", " ad " offset0:" STR((O) / 4 + 2) " offset1:" STR((O) / 4 + 3) "\n\t"
#define CRB(S, ad, O) "ds_read_b128 " RNG("v", S, 4) ", " ad " offset:" STR(O) "\n\t"
#define CMF(C, A, B) "v_mfma_f32_32x32x16_bf16 " RNG("v", C, 16) ", " RNG("v", A, 4) ", " RNG("v", B, 4) ", " RNG("v", C, 16) "\n\t"
#define CUPD "v_add_u32 %[ua], 64, %[ua]\n\t" "v_subrev_u32 %[a1], 80, %[a1]\n\t" "v_subrev_u32 %[a0], 80, %[a0]\n\t" "v_max_i32 %[a0], %[a0], %[kb]\n\t"
#define CHALF(S, N, O, UPD, UPD2)                                                                 \
    WLG(5) CMF(64, S, (S) + 8) UPD CRA(N, "%[ua]", O)                                             \
    WLG(6) CMF(80, S, (S) + 16) UPD2 CRB((N) + 8, "%[a0]", O) CRB((N) + 16, "%[a1]", O)           \
    WLG(6) CMF(80, (S) + 4, (S) + 8) CRA((N) + 4, "%[ub]", O)                                     \
    WLG(7) CMF(64, S, (S) + 12) CRB((N) + 12, "%[a0]", (O) + 5200)                                \
    WLG(7) CMF(80, S, (S) + 20) CRB((N) + 20, "%[a1]", (O) + 5200)                                \
    CMF(80, (S) + 4, (S) + 12)
    asm volatile(
        CRA(96, "%[ua]", 32) CRB(104, "%[a0]", 32) CRB(112, "%[a1]", 32) CRA(100, "%[ub]", 32) CRB(108, "%[a0]", 32 + 5200) CRB(116, "%[a1]", 32 + 5200)
        ZER(64) ZER(68) ZER(72) ZER(76) ZER(80) ZER(84) ZER(88) ZER(92)
        ".Lc%=:\n\t"
        CHALF(96, 120, 0, "", "")
        CHALF(120, 96, 32, CUPD, "v_add_u32 %[ub], 64, %[ub]\n\t")
        "s_sub_u32 %[cnt], %[cnt], 1\n\t"
        "s_cmp_lg_u32 %[cnt], 0\n\t"
        "s_cbranch_scc1 .Lc%=\n\t"
        "s_nop 7\n\t" "s_nop 3\n\t" WLG(0)
        : "=&{v[64:79]}"(cv0), "=&{v[80:95]}"(cv1), [ua] "+v"(ua), [ub] "+v"(ub), [a0] "+v"(a0), [a1] "+v"(a1), [cnt] "+s"(cnt)
        : [kb] "v"(kb)
        : CL96, CL112, CL120, CL8(128, 129, 130, 131, 132, 133, 134, 135), CL8(136, 137, 138, 139, 140, 141, 142, 143), "scc", "memory");
  }
  SST(5)
  SSM_GOT(in);
#pragma unroll
  for (int g = 0; g < 2; ++g)
#pragma unroll
    for (int i = 0; i < 4; ++i) {
      const f32x16& c = g ? cv1 : cv0;
      *(u32x2*)(cb + SU_Y + (32 * g + r) * 64 + ((i ^ (r >> 1)) & 3) * 16 + 8 * h) = (u32x2){pack2(c[4 * i], c[4 * i + 1]), pack2(c[4 * i + 2], c[4 * i + 3])};
    }
  LBAR();
  SST(6)
  {
    u32x4 c[4];
#pragma unroll
    for (int j = 0; j < 4; ++j) c[j] = *(const u32x4*)(sm + j * SU_CH + SU_Y + (tid >> 2) * 64 + ((tid ^ (tid >> 3)) & 3) * 16);
#pragma unroll
    for (int i = 0; i < 4; ++i) {
      u32x4 y;
      asm("v_dot2_f32_bf16 %4, %12, %16, %4\n\tv_dot2_f32_bf16 %5, %13, %16, %5\n\tv_dot2_f32_bf16 %6, %14, %16, %6\n\tv_dot2_f32_bf16 %7, %15, %16, %7\n\t"
          "v_dot2_f32_bf16 %8, %12, %17, %8\n\tv_dot2_f32_bf16 %9, %13, %17, %9\n\tv_dot2_f32_bf16 %10, %14, %17, %10\n\tv_dot2_f32_bf16 %11, %15, %17, %11\n\t"
          "v_cvt_pk_bf16_f32 %0, %4, %5\n\tv_cvt_pk_bf16_f32 %1, %6, %7\n\tv_cvt_pk_bf16_f32 %2, %8, %9\n\tv_cvt_pk_bf16_f32 %3, %10, %11"
          : "=&v"(y[0]), "=&v"(y[1]), "=&v"(y[2]), "=&v"(y[3]),
            "+v"(sk[2 * i][0]), "+v"(sk[2 * i][1]), "+v"(sk[2 * i][2]), "+v"(sk[2 * i][3]),
            "+v"(sk[2 * i + 1][0]), "+v"(sk[2 * i + 1][1]), "+v"(sk[2 * i + 1][2]), "+v"(sk[2 * i + 1][3])
          : "v"(c[0][i]), "v"(c[1][i]), "v"(c[2][i]), "v"(c[3][i]), "s"(0x00003f80u), "s"(0x3f800000u));
      __builtin_amdgcn_raw_buffer_store_b128(y, rst, tid * 16, SSM_L * SSM_D * 2 + d0 * 4096 + i * 4096, 16);
    }
  }
  if (nxt >= 256) {
    asm volatile("s_waitcnt vmcnt(0)\n\ts_barrier" ::: "memory");
    __builtin_amdgcn_raw_ptr_buffer_atomic_add_i32(1, rsx, one, XYDN, 0);
  }
  SST(7)
  return nxt;
}

#define L_TOTAL 163840
__device__ __forceinline__ int ldw(__amdgpu_buffer_rsrc_t rs, int off) {
  asm volatile("" ::: "memory");
  return __builtin_amdgcn_readfirstlane(__builtin_amdgcn_raw_buffer_load_b32(rs, 0, off, 16));
}
#define GF(rt) "v[64+8*(xk&7)+4*" #rt ":64+8*(xk&7)+4*" #rt "+3]"
#define GW2 "v[128+4*xk:128+4*xk+3]"
#define GW3 "v[24+4*(xk&1):24+4*(xk&1)+3]"
#define GLOAD(S, K) "s_xor_b32 %[tq], " S ", 1024*(" K ")\n\t"
#define GTR(K) "ds_read_b128 v[24+4*((" K ")&1):24+4*((" K ")&1)+3], %[wl] offset:1024*((" K ")&31)\n\t"
#define SLOWC(s_) (((s_) & 7) == (((384 - (int)(size_t)Wo) & 1023) >> 7))
#define GPACE 0
#define YAUX 16
#define UDELAY 100
#define USH 3
#define GNOSSM 1
#define GNG 3
#define GS0 22
__device__ __forceinline__ void g_role(unsigned char* lds, const u16* __restrict__ Wo, u16* __restrict__ out, __amdgpu_buffer_rsrc_t rsx,
                                       __amdgpu_buffer_rsrc_t rsf, int fb, u32 tag, int grp, int sl, int w, u32* udn, const u16* __restrict__ u, __amdgpu_buffer_rsrc_t rst) {
  const int lane = fresh_lane();
  const int lr = lane & 15, lg = lane >> 4;
  const int rot = (2 * sl + 8 * w) & 31;
  const int ldsb = (int)(size_t)(LAS unsigned char*)lds;
  u32x32 xw[8], w2[4];
#define GSX 3
  const bool slow = SLOWC(sl);
  const int gs0 = GS0 + (slow ? GSX : 0);
#define GC2 0
#define GAUX ""
  const int c0 = GC2 ? 32 * sl : 64 * sl, c1 = GC2 ? 512 + 32 * (sl ^ 8) : 64 * sl + 32;
  LBAR();
  {
    const __amdgpu_buffer_rsrc_t rw = __builtin_amdgcn_make_buffer_rsrc((void*)Wo, 0, DM * HD * 2, 0x00020000);
    const int vq = (lane >> 3) * (DM * 2) + ((lane & 4) ? c1 : c0) * 2 + (lane & 3) * 16;
    const int sq = (1024 * w + rot * 32) * (DM * 2);
#define GSLOT(c) ((c) < 8 ? (((c) & 3) == 3 ? 131072 + w * 8192 : w * 32768 + 8192 * (((c) & 3) + 1)) : (((c) - 8) % 3 == 2 ? 131072 + w * 8192 : w * 32768 + 8192 * (((c) - 8) % 3 + 2)))
#define GISSUE(c) _Pragma("unroll") for (int j = 0; j < 8; ++j)                                                        \
      asm volatile("s_mov_b32 m0, %0\n\ts_nop 3\n\tbuffer_load_dwordx4 %1, %2, %3 offen" GAUX " lds"                           \
                   :: "s"(ldsb + GSLOT(c) + j * 1024), "v"(vq), "s"(rw), "s"(sq ^ ((64 * (c) + 8 * j) * DM * 2)) : "memory");
    GISSUE(0) GISSUE(1) GISSUE(2) GISSUE(3)
#define GPF 0
#pragma unroll
    for (int b = 2 - GPF; b < 2; ++b) {
      const int s3 = (384 - (int)(size_t)Wo) & 1023, ss = (s3 >> 7) + 8 * (lane >> 5);
      const int pf = (sl & 7) == (s3 >> 7) ? OOR : (1024 * w + 64 * (((ss + 4 * w) & 15) ^ (8 * b + ((lane >> 2) & 7))) + 4 * sl + (lane & 3)) * (DM * 2) + (lane >> 5) * 1024 + s3;
      asm volatile("s_mov_b32 m0, %0\n\ts_nop 3\n\tbuffer_load_dword %1, %2, 0 offen lds" :: "s"(ldsb + w * 32768 + 7936), "v"(pf), "s"(rw) : "memory");
    }
    const int q = lr >> 2, p = lr & 3;
    u32x4 w3[16];
#pragma unroll
    for (int c = 0; c < 16; ++c) {
      if (c < 5) waitvm<24>(); else if (c < 14) waitvm<16>(); else if (c == 14) waitvm<8>(); else waitvm<0>();
      u32x4 f3[2];
#pragma unroll
      for (int s = 0; s < 2; ++s)
#pragma unroll
        for (int t = 0; t < 4; ++t) {
          const unsigned char* base = lds + GSLOT(c) + (32 * s + 8 * lg + q) * 128 + (t < 2 ? p * 16 + t * 8 : t * 32 + p * 8);
          const u32x2 t0 = __builtin_bit_cast(u32x2, __builtin_amdgcn_ds_read_tr16_b64_v4i16((LAS v4i16_t*)(base)));
          const u32x2 t1 = __builtin_bit_cast(u32x2, __builtin_amdgcn_ds_read_tr16_b64_v4i16((LAS v4i16_t*)(base + 512)));
          const int o = t < 2 ? ((2 * c + s) * 2 + t) * 4 : (2 * c + s) * 4;
          if (t < 2) { xw[o >> 5][(o & 31) + 0] = t0[0]; xw[o >> 5][(o & 31) + 1] = t0[1]; xw[o >> 5][(o & 31) + 2] = t1[0]; xw[o >> 5][(o & 31) + 3] = t1[1]; }
          else if (t == 2) { w2[o >> 5][(o & 31) + 0] = t0[0]; w2[o >> 5][(o & 31) + 1] = t0[1]; w2[o >> 5][(o & 31) + 2] = t1[0]; w2[o >> 5][(o & 31) + 3] = t1[1]; }
          else f3[s] = (u32x4){t0[0], t0[1], t1[0], t1[1]};
        }
      asm volatile("s_waitcnt lgkmcnt(0)" ::: "memory");
      if (c < 8) { *(u32x4*)(lds + w * 32768 + (2 * c) * 1024 + lane * 16) = f3[0]; *(u32x4*)(lds + w * 32768 + (2 * c + 1) * 1024 + lane * 16) = f3[1]; }
      else { w3[2 * (c - 8)] = f3[0]; w3[2 * (c - 8) + 1] = f3[1]; }
      if (GPACE) __builtin_amdgcn_s_sleep(GPACE);
      if (c < 4) { GISSUE(c + 4) } else if (c >= 5 && c < 13) { GISSUE(c + 3) }
    }
#pragma unroll
    for (int i = 0; i < 16; ++i) *(u32x4*)(lds + w * 32768 + (16 + i) * 1024 + lane * 16) = w3[i];
    LBAR();
  }
  if (grp == 0 && (USH == 2 || (USH == 3 && slow))) {
    const int tid = w * 64 + lane;
    unsigned char* im = lds + 131072;
    const __amdgpu_buffer_rsrc_t ru = __builtin_amdgcn_make_buffer_rsrc((void*)u, 0, SSM_L * SSM_D * 2, 0x00020000);
    const int vo = (tid >> 5) * (SSM_D * 2) + (tid & 31) * 16, rp = 4 * (tid & 7) + ((tid >> 3) & 3);
    u32x4 t[2][8];
#pragma unroll
    for (int q = 0; q < 2; ++q)
#pragma unroll
      for (int i = 0; i < 8; ++i) t[q][i] = __builtin_amdgcn_raw_buffer_load_b128(ru, vo, (sl * 64 + i * 8) * (SSM_D * 2) + q * 512, 0);
#pragma unroll
    for (int q = 0; q < 4; ++q) {
#pragma unroll
      for (int i = 0; i < 8; ++i) { const int r = i * 8 + (tid >> 5); *(u32x4*)(im + r * 512 + (((tid & 31) ^ (r >> 1)) * 16)) = t[q & 1][i]; }
      if (q < 2) {
#pragma unroll
        for (int i = 0; i < 8; ++i) t[q][i] = __builtin_amdgcn_raw_buffer_load_b128(ru, vo, (sl * 64 + i * 8) * (SSM_D * 2) + (q + 2) * 512, 0);
      }
      LBAR();
#pragma unroll
      for (int i = 0; i < 8; ++i) {
        const int o = rp * 1024 + (((i * 4 + w) ^ rp) * 16) + (lane >> 5) * 8;
        const u32x2 a = *(const u32x2*)(im + o), c = *(const u32x2*)(im + o + 512);
        __builtin_amdgcn_raw_buffer_store_b128((u32x4){a[0], a[1], c[0], c[1]}, rst, (tid >> 3) * 4096 + (tid & 7) * 16, (q * 256 + i * 32) * 4096 + sl * 128, 16);
      }
      if (q < 3) LBAR();
    }
    asm volatile("s_waitcnt vmcnt(0) lgkmcnt(0)\n\ts_barrier" ::: "memory");
  }
  wait_start(rsx, fb, tag, true);
  if (USH && grp == 0 && w == 0 && lane == 0) __hip_atomic_fetch_add(udn, 1u, RLX_AGENT);
  const int wl = ldsb + w * 32768 + lane * 16;
  const __amdgpu_buffer_rsrc_t rso = __builtin_amdgcn_make_buffer_rsrc((void*)out, 0, S_LEN * OUTLD * 2, 0x00020000);
  const int rw_ = ldsb + 131072 + w * 8192 + lane * 16, rr_ = ldsb + 131072 + w * 2048 + lane * 16;
  const int oa = ((16 * (w & 1) + lr) * OUTLD + (w < 2 ? c0 + 8 * lg : c1 + 4 * lg)) * 2, ob = oa + (w < 2 ? 8 : 32);
  const int hq = (grp + GNG - (62 - GS0) % GNG) % GNG - 1, hf = hq < 2 ? hq : -1;
#define GRW(i) "ds_write_b128 %[rw], " RNG("v", 32 + 4 * (i), 4) " offset:" STR(1024 * (i)) "\n\t"
#define GRR(i) "ds_read_b128 " RNG("v", 32 + 4 * (i), 4) ", %[rr] offset:" STR(8192 * ((i) >> 1) + 1024 * ((i) & 1)) "\n\t"
#define GNXT(D, S) "s_cmp_lt_u32 " S ", %[gs]\n\t" "s_cselect_b32 " D ", 1, " STR(GNG) "\n\t" "s_add_u32 " D ", " D ", " S "\n\t"
#define GDN1(S) "s_min_u32 s41, " S ", 63\n\t" "s_lshl_b32 s41, s41, 5\n\t" "v_add_u32 v23, s41, %[u0]\n\t" "v_lshrrev_b32 v22, 6, v23\n\t"
#define GDN2 "v_and_b32 v23, 63, v23\n\t" "v_lshl_add_u32 v23, v23, 7, %[u4]\n\t" "s_lshr_b32 s41, s41, 6\n\t" "s_max_u32 s41, s41, 29\n\t"
#define GDN3 "s_lshl_b32 s41, s41, 13\n\t" "s_add_u32 s41, s41, %[pa]\n\t" "buffer_load_dword v23, v23, %[rs], s41 offen sc1\n\t"
#define GDN(S) GDN1(S) GDN2 GDN3
#define GDEF 1
#define GB2 16
#define GCK 15
#define GOK(D) "v_cmp_le_i32 vcc, v23, v22\n\t" "s_cmp_eq_u64 vcc, 0\n\t" "s_cselect_b32 " D ", 1, 0\n\t"
static_assert(NAW == 64, "shifts above");
#define GEPI(MID, OA, OB) \
      GRW(0) GRW(1) GRW(2) GRW(3) GRW(4) GRW(5) GRW(6) GRW(7) \
      MID \
      WLG(0) "s_barrier\n\t" \
      GRR(0) GRR(1) GRR(2) GRR(3) GRR(4) GRR(5) GRR(6) GRR(7) \
      WLG(4) PKA(32, 32, 40) PKA(34, 34, 42) PKA(36, 36, 44) PKA(38, 38, 46) \
      WLG(2) PKA(32, 32, 48) PKA(34, 34, 50) PKA(36, 36, 52) PKA(38, 38, 54) \
      WLG(0) PKA(32, 32, 56) PKA(34, 34, 58) PKA(36, 36, 60) PKA(38, 38, 62) \
      "v_cvt_pk_bf16_f32 v20, v32, v33\n\t" "v_cvt_pk_bf16_f32 v21, v34, v35\n\t" "v_cvt_pk_bf16_f32 v18, v36, v37\n\t" "v_cvt_pk_bf16_f32 v19, v38, v39\n\t" \
      "buffer_store_dwordx2 v[20:21], " OA ", %[ro], s42 offen\n\t" \
      "buffer_store_dwordx2 v[18:19], " OB ", %[ro], s42 offen\n\t"
#define GH "v[64+4*(xk&15):64+4*(xk&15)+3]"
#define GSTAMP
#define GSTIN
#define GSTCL
#define GT1
#define GT2
#define GTO
  asm volatile(
      "s_nop 4\n\t"
      "v_add_u32 v16, 131072, %[va]\n\t"
      "s_mov_b32 s36, %[s0]\n\t" "s_mov_b32 s43, " STR(SPIN_CAP) "\n\t"
      "s_mov_b32 s37, 0\n\t" "v_mov_b32 v17, " STR(OOR) "\n\t" "v_mov_b32 v12, v17\n\t" "v_mov_b32 v15, v17\n\t"
      "s_cmp_ge_i32 %[hf], 0\n\t" "s_cbranch_scc1 .Li%=\n"
      ".Lp%=:\n\t"
      GDN("s36") WVM(0) GOK("s40")
      "s_cbranch_scc1 .Lr%=\n\t"
      "s_sleep 2\n\t" "s_sub_u32 s43, s43, 1\n\t" "s_cmp_gt_i32 s43, 0\n\t" "s_cbranch_scc1 .Lp%=\n"
      ".Lr%=:\n\t"
      "s_lshl_b32 s38, s36, 18\n\t" "s_add_u32 s38, s38, %[gb]\n\t"
      ".set xk, 0\n\t.rept 8\n\t" GLOAD("s38", "xk")
      "buffer_load_dwordx4 " GF(0) ", %[va], %[rf], %[tq] offen sc1\n\t" "buffer_load_dwordx4 " GF(1) ", v16, %[rf], %[tq] offen sc1\n\t"
      ".set xk, xk+1\n\t.endr\n"
#define GWD(D, i) ".if " #D " && (xk == 0)\n\t" GRW(i) ".endif\n\t"
#define GAD(i) ".if (xk >= 2) && (xk < 10) && ((xk-2)&3)\n\t" "v_add_f32 v[12+" #i "], v[12+" #i "], v[18+" #i "]\n\t" ".endif\n\t"
#define GSTEP(D) \
      "s_waitcnt vmcnt(14)\n\t" \
      GWD(D, 0) XM1(XA(32, 0), XW(0, 0), GF(0), 1) \
      ".if (xk == 2) || (xk == 6)\n\t" "ds_read_b128 v[12:15], %[rr] offset:256*(xk-2)\n\t" ".elseif (xk > 2) && (xk < 10)\n\t" "ds_read_b128 v[18:21], %[rr] offset:8192*((xk-2)&3)+1024*((xk-2)>>2)\n\t" ".endif\n\t" \
      GTR("xk+1") GWD(D, 1) XM1(XA(32, 4), XW(0, 1), GF(0), 1) \
      ".if xk < 24\n\t" GLOAD("s38", "xk+8") ".else\n\t" GLOAD("s39", "xk-24") ".endif\n\t" \
      GWD(D, 2) XM1(XA(32, 8), XW(0, 0), GF(1), 1) \
      ".if xk == " STR(GCK) "-1\n\t" GNXT("s37", "s36") ".endif\n\t" \
      ".if xk == " STR(GCK) "\n\t" GDN1("s37") ".endif\n\t" \
      ".if xk == 23\n\t" GOK("s40") ".endif\n\t" \
      GWD(D, 3) XM1(XA(32, 12), XW(0, 1), GF(1), 1) \
      ".if xk == 7\n\t" "s_add_u32 s42, s42, %[od]\n\t" ".endif\n\t" \
      ".if xk == " STR(GCK) "-1\n\t" "s_lshl_b32 s42, s36, 17\n\t" ".endif\n\t" \
      ".if xk == " STR(GCK) "\n\t" GDN2 ".endif\n\t" \
      ".if xk == 23\n\t" "s_cmp_lt_u32 s37, 63\n\t" "s_cselect_b32 s40, s40, 0\n\t" "s_cmp_lg_u32 s40, 0\n\t" ".endif\n\t" \
      GWD(D, 4) XM1(XA(32, 16), GW2, GF(0), 1) \
      ".if xk == " STR(GCK) "\n\t" GDN3 ".endif\n\t" \
      ".if xk == 23\n\t" "s_cselect_b32 s39, s37, s36\n\t" "s_lshl_b32 s39, s39, 18\n\t" "s_add_u32 s39, s39, %[gb]\n\t" ".endif\n\t" \
      ".if " #D " && (xk == 0)\n\t" GRW(5) WLG(7) ".else\n\t" WLG(1) ".endif\n\t" XM1(XA(32, 20), GW3, GF(0), 1) \
      "buffer_load_dwordx4 " GF(0) ", %[va], %[rf], %[tq] offen sc1\n\t" GAD(0) GAD(1) \
      GWD(D, 6) XM1(XA(32, 24), GW2, GF(1), 1) GAD(2) GAD(3) GWD(D, 7) XM1(XA(32, 28), GW3, GF(1), 1) \
      "buffer_load_dwordx4 " GF(1) ", v16, %[rf], %[tq] offen sc1\n\t" \
      ".if (xk == 5) || (xk == 9)\n\t" "v_cvt_pk_bf16_f32 v22, v12, v13\n\t" "v_cvt_pk_bf16_f32 v23, v14, v15\n\t" "buffer_store_dwordx2 v[22:23], v17, %[ro], s42 offen\n\t" ".endif\n\t" \
      ".if (xk == " STR(GB2) ") || (" #D " && (xk == 1))\n\t" "s_barrier\n\t" ".endif\n\t" \
      ".set xk, xk+1\n\t"
      GTR("0") "v_mov_b32 v17, " STR(OOR) "\n\t" "s_mov_b32 s42, 0\n\t"
      ".set xk, 0\n\t.rept 2\n\t" GSTEP(0) ".endr\n\t"
      "s_branch .Lc%=\n"
      ".Lb%=:\n\t"
      ".set xk, 0\n\t.rept 2\n\t" GSTEP(1) ".endr\n\t"
      "v_mov_b32 v17, %[oa]\n"
      ".Lc%=:\n\t"
      ".rept 30\n\t" GSTEP(0) ".endr\n\t"
      GSTAMP
      "s_mov_b32 s36, s37\n\t" "s_mov_b32 s38, s39\n\t"
      "s_cmp_lg_u32 s40, 0\n\t" "s_cbranch_scc1 .Lb%=\n\t"
      GEPI("", "%[oa]", "%[ob]")
      "s_cmp_lt_u32 s36, 63\n\t" "s_cbranch_scc1 .Lp%=\n\t"
      "s_cmp_lt_i32 %[hf], 0\n\t" "s_cbranch_scc1 .Le%=\n\t"
      "s_mov_b32 s37, 4\n\t" "v_mov_b32 v17, %[va]\n\t" "v_mov_b32 v12, %[oc]\n\t" "v_sub_u32 v15, %[ob], %[oa]\n\t" "v_add_u32 v15, v15, %[oc]\n\t"
      "s_barrier\n"
      ".Lh%=:\n\t"
      "v_cmp_eq_u32 vcc, -1, v23\n\t" "s_cbranch_vccz .Lt%=\n\t" "buffer_load_dword v23, %[uh], %[rs], 0 offen sc1\n\t" WVM(0)
      "s_sleep 1\n\t" "s_sub_u32 s43, s43, 1\n\t" "s_cmp_gt_i32 s43, 0\n\t" "s_cbranch_scc1 .Lh%=\n"
      ".Lt%=:\n\t"
      "v_readfirstlane_b32 s39, v23\n\t" "s_cmp_ge_i32 s39, 0\n\t" "s_cbranch_scc1 .Li%=\n"
      ".Lw%=:\n\t"
      "s_memrealtime s[40:41]\n\t" WLG(0) "s_sub_u32 s40, s39, s40\n\t" "s_lshl_b32 s40, s40, 1\n\t" "s_sub_u32 s40, s40, 1\n\t" "s_cmp_lt_u32 s40, 4000\n\t" "s_cbranch_scc1 .Lw%=\n\t"
      "s_branch .Li%=\n"
      ".Lx%=:\n\t"
      "s_sub_u32 s37, s37, 1\n\t" "s_cmp_gt_u32 s37, 1\n\t" "s_cbranch_scc1 .Li%=\n"
      ".Ly%=:\n\t"
      "buffer_load_dword v23, %[uh], %[rs], 0 offen sc1\n\t" WVM(0) "v_cmp_ge_i32 vcc, 31, v23\n\t" "s_cbranch_vccz .Li%=\n\t"
      "s_sub_u32 s43, s43, 1\n\t" "s_cmp_gt_i32 s43, 0\n\t" "s_cbranch_scc1 .Ly%=\n"
      ".Li%=:\n\t" GT1
      "s_lshl_b32 s38, %[hf], 17\n\t" "s_add_u32 s38, s38, 63*262144\n\t" "s_add_u32 s38, s38, %[gb]\n\t"
      ".set xk, 0\n\t.rept 16\n\t" GLOAD("s38", "xk") "buffer_load_dwordx4 " GH ", v17, %[rf], %[tq] offen sc1\n\t" ".set xk, xk+1\n\t.endr\n\t"
      GTR("0")
      "s_lshl_b32 s42, %[hf], 16\n\t" "s_add_u32 s42, s42, 63*131072\n\t" "v_mov_b32 v22, 0\n\t" "s_cmp_gt_u32 s37, 1\n\t" "s_cselect_b32 s39, -2, -1\n\t"
      ".set xk, 0\n\t.rept 32\n\t"
      ".if xk < 17\n\t" "s_waitcnt vmcnt(15)\n\t" ".else\n\t" "s_waitcnt vmcnt(31-xk)\n\t" ".endif\n\t"
      XM1(XA(32, 0), XW(0, 0), GH, 1) GTR("xk+1") XM1(XA(32, 4), XW(0, 1), GH, 1) "v_max3_u32 v22, v22, v[64+4*(xk&15)], v[65+4*(xk&15)]\n\t"
      XM1(XA(32, 16), GW2, GH, 1) "v_max3_u32 v22, v22, v[66+4*(xk&15)], v[67+4*(xk&15)]\n\t" "s_waitcnt lgkmcnt(1)\n\t" XM1(XA(32, 20), GW3, GH, 1)
      ".if xk & 1\n\t" "v_cmp_lt_u32 vcc, s39, v22\n\t" "s_cbranch_vccnz .Lx%=\n\t" ".endif\n\t"
      ".if xk < 16\n\t" GLOAD("s38", "xk+16") "buffer_load_dwordx4 " GH ", v17, %[rf], %[tq] offen sc1\n\t" ".endif\n\t"
      ".set xk, xk+1\n\t.endr\n\t"
      "s_nop 7\n\t"
      GEPI("", "v12", "v15")
      "s_cmp_eq_u32 s37, 0\n\t" "s_cbranch_scc1 .Lp%=\n"
      ".Le%=:\n\t"
      WVM(0)
      GT2
      : GTO [tq] "=&s"(sl)
      : GSTIN [rs] "s"(rsx), [s0] "s"(__builtin_amdgcn_readfirstlane(grp ? gs0 + grp : 0)), [gs] "s"(gs0), [gb] "s"(FO_O - 32768 + (32 * w + rot) * 1024), [va] "v"(lane * 16 + 32768),
        [rf] "s"(rsf), [wl] "v"(wl), [rw] "v"(rw_), [rr] "v"(rr_), [oa] "v"(oa), [ob] "v"(ob), [ro] "s"(rso), [od] "s"(w < 2 ? 8 : 32), [u0] "v"(8 * w + (lane & 7)), [u4] "v"((lane >> 1) & 4), [uh] "v"(PGO_A + (160 + 8 * w + (lane & 7)) * 128 + 4 * (hf & 1)), [pa] "s"(PGO_A - 29 * 8192), [hf] "s"(__builtin_amdgcn_readfirstlane(hf)), [oc] "v"((w & 1) ? OOR - 64 : oa),
        "{a[0:31]}"(xw[0]), "{a[32:63]}"(xw[1]), "{a[64:95]}"(xw[2]), "{a[96:127]}"(xw[3]),
        "{a[128:159]}"(xw[4]), "{a[160:191]}"(xw[5]), "{a[192:223]}"(xw[6]), "{a[224:255]}"(xw[7]),
        "{v[128:159]}"(w2[0]), "{v[160:191]}"(w2[1]), "{v[192:223]}"(w2[2]), "{v[224:255]}"(w2[3])
      : GSTCL "v12", "v13", "v14", "v15", "v16", "v17", "v18", "v19", "v20", "v21", "v22", "v23", "vcc", CL8(24, 25, 26, 27, 28, 29, 30, 31), CL8(32, 33, 34, 35, 36, 37, 38, 39), CL8(40, 41, 42, 43, 44, 45, 46, 47),
        CL8(48, 49, 50, 51, 52, 53, 54, 55), CL8(56, 57, 58, 59, 60, 61, 62, 63), CL64, CL72, CL80, CL88, CL96, CL112, CL120,
        "s36", "s37", "s38", "s39", "s40", "s41", "s42", "s43", "scc", "memory");
}
static_assert(GNOSSM == 1 && GNG == 3 && 4 * SU_CH + 16 <= L_TOTAL && 4 * XSLOT <= L_TOTAL, "lds");
__global__ void __launch_bounds__(256, 1)
main_kernel(const u16* __restrict__ x, const u16* __restrict__ mem0, const u16* __restrict__ Wq, const u16* __restrict__ Wk, const u16* __restrict__ Wv, const u16* __restrict__ Wo,
            unsigned char* ws, const u16* __restrict__ u, const u16* __restrict__ A_re, const u16* __restrict__ A_im, const u16* __restrict__ B_re,
            const u16* __restrict__ B_im, const u16* __restrict__ C_re, const u16* __restrict__ C_im,
            const u16* __restrict__ log_dt, const u16* __restrict__ Dskip, u16* __restrict__ out
            ) {
  __shared__ __attribute__((aligned(8192))) unsigned char lds[L_TOTAL];
  const int w = __builtin_amdgcn_readfirstlane(threadIdx.x >> 6);
  u64 pc0;
  int clen;
  {
    asm volatile("s_getpc_b64 %0\n.Lkpc0:\n\ts_mov_b32 %1, .Lkpc1-.Lkpc0" : "=s"(pc0), "=s"(clen));
  }
#define LOCAL_IDS const int lane = fresh_lane(), tid = w * 64 + lane; (void)tid;
  const int si = (blockIdx.x >> 3) * 4 + (blockIdx.x & 3);
  unsigned char* xb = (unsigned char*)(((size_t)ws + XPAGE - 1) & ~(size_t)(XPAGE - 1));
  const int fb = (int)(ws + WS_F - xb);
  u32 tag;
  asm volatile("s_load_dword %0, %1, 0x0\n\ts_waitcnt lgkmcnt(0)\n\ts_add_u32 %0, %0, 256" : "=s"(tag) : "s"(ws + WS_F) : "scc", "memory");
  const __amdgpu_buffer_rsrc_t rsx = __builtin_amdgcn_make_buffer_rsrc(xb, 0, fb + (int)WS_F_SZ, 0x00020000);
  const __amdgpu_buffer_rsrc_t rsf = __builtin_amdgcn_make_buffer_rsrc(ws + WS_QX, 0, (int)(4 * WS_QKV_SZ + 2 * WS_KM_SZ), 0x00020000);
  const __amdgpu_buffer_rsrc_t rst = __builtin_amdgcn_make_buffer_rsrc(ws + WS_UT, 0, 2 * SSM_L * SSM_D * 2 + SSM_D * 3072, 0x00020000);
  if ((blockIdx.x & 7) < 4) {
    { LOCAL_IDS
    r_role(lds, si, mem0, Wq, Wk, Wv, Wo, fb, tag, pc0, clen, rsx, rsf, rst, SsmPtrs{A_re, A_im, B_re, B_im, C_re, C_im, log_dt}, tid, lane, w
           ); }
    LOCAL_IDS
    {
      for (int it = 0; it < SPIN_CAP; ++it) {
        if (ldw(rsx, XYDN) >= 255) break;
        asm volatile("s_sleep 8" ::: "memory");
      }
      asm volatile("" ::: "memory");
      asm volatile(".Lkpc1:" ::: "memory");
      unsigned char* im = lds + 65536;
      const __amdgpu_buffer_rsrc_t rso = __builtin_amdgcn_make_buffer_rsrc((void*)out, 0, S_LEN * OUTLD * 2, 0x00020000);
      u32x4 t[8];
#pragma unroll
      for (int i = 0; i < 8; ++i) { const int p = i * 256 + tid; t[i] = __builtin_amdgcn_raw_buffer_load_b128(rst, (p >> 3) * 4096 + (p & 7) * 16, SSM_L * SSM_D * 2 + (si & 3) * 64 * 16384 + (si >> 2) * 128, 16); }
#pragma unroll
      for (int i = 0; i < 8; ++i) {
        const int p = i * 256 + tid, o = (p & 7) * (8 * 512 + 16) + ((p >> 3) & 3) * 1024 + (p >> 5) * 8;
        *(u32x2*)(im + o) = (u32x2){t[i][0], t[i][1]};
        *(u32x2*)(im + o + 512) = (u32x2){t[i][2], t[i][3]};
      }
      LBAR();
#pragma unroll
      for (int i = 0; i < 8; ++i)
        __builtin_amdgcn_raw_buffer_store_b128(*(const u32x4*)(im + i * (8 * 512 + 16) + tid * 16), rso, ((tid >> 5) * OUTLD + DM + (tid & 31) * 8) * 2,
                                               (((si >> 2) * 64 + 8 * i) * OUTLD + (si & 3) * 256) * 2, YAUX);
    }
  } else {
#define GSP 0
    int g = blockIdx.x & 3, li = blockIdx.x >> 3;
    if ((GSP & 1) && g >= 2 && li < 16) { const int h = li >> 3; li = (li & 7) + 8 * (g - 2); g = 2 + h; }
    if ((GSP & 2) && g < 2 && (li >> 3) == 3 - g) { li ^= 8; g ^= 1; }
    const int xi = g == 0 ? (li < 16 ? li : -1) : (li < 26 + (g < 3) ? 16 + 27 * (g - 1) + li : -1);
    const int ei = g == 0 ? li - 16 : 16 + 5 * (g - 1) + (31 - li);
    if (xi >= 0) {
      LOCAL_IDS
      const int head = xi & 31, mi = xi >> 5, gs = (g >= 5 - GNG && li < 16) ? li : -1;
      u32* prog = (u32*)(xb + PGO_X + xi * 128);
      if (mi == 1) x_role<true>(lds, x, Wv, FO_V, head, rsf, prog, tag, lane, w, Wo, gs);
      else x_role<false>(lds, x, mi == 0 ? Wk : Wq, mi == 0 ? FO_K : FO_Q, head, rsf, prog, tag, lane, w, Wo, gs);
    } else {
      LOCAL_IDS
      u32 acc = 0;
#pragma unroll
      for (int i = 0; i < 4; ++i) acc += *(const u32*)(x + (size_t)((i * 32 + ei) * 256 + tid) * 64);
#define UV 0
#define WWARM(n, first) {    \
        const __amdgpu_buffer_rsrc_t rwo = __builtin_amdgcn_make_buffer_rsrc((void*)Wo, 0, DM * HD * 2, 0x00020000); \
        const int ldsb = (int)(size_t)(LAS unsigned char*)lds; \
        _Pragma("unroll 1") for (int c = 0; c < (n); ++c) { dma16(rwo, lane * 16, ((first) + c) * 16384, 1024, ldsb + w * 32768 + (c & 1) * 16384); waitvm<32>(); } }
      const bool wf = ei < 16 && (USH == 2 || (USH == 3 && SLOWC(ei)));
      if ((UV == 0 && !(USH == 3 && wf)) || (UV == 1 && ei < 16)) WWARM(4, (ei * 4 + w) * 4)
      if (UV == 2 && ei < 16) WWARM(8, (ei * 4 + w) * 8)
      if (UV == 3 && ei >= 16) WWARM(8, ((ei - 16) * 4 + w) * 8)
      if (UV == 4 && ei >= 16) WWARM(4, (ei * 4 + w) * 4)
      if (ei == 0 && tid < 16) acc += *(const u32*)(Dskip + tid * 64);
      asm volatile("" :: "v"(acc));
      if (wf) asm volatile("s_waitcnt vmcnt(0)" ::: "memory");
      if ((USH && !wf) || ei >= 16) {
      { const u32 t0 = rt32(); for (int i = 0; i < 1000 && (int)(rt32() - t0) < UDELAY; ++i) asm volatile("s_sleep 16" ::: "memory"); }
      asm volatile("s_waitcnt vmcnt(0)\n\ts_barrier" ::: "memory");
      for (int ps = 0; ps < 2 - !!USH; ++ps) {
      const int r0 = USH ? ei * 64 : ((ei - 16) * 2 + ps) * 64;
#pragma unroll
      for (int hb = 0; hb < 2; ++hb) {
        u32x4 t[16];
#pragma unroll
        for (int i = 0; i < 16; ++i) t[i] = *(const u32x4*)(u + (size_t)r0 * SSM_D + ((hb * 16 + i) * 256 + tid) * 8);
#pragma unroll
        for (int i = 0; i < 16; ++i) { const int p = (hb * 16 + i) * 256 + tid; *(u32x4*)(lds + (p >> 7) * 2064 + (p & 127) * 16) = t[i]; }
      }
      LBAR();
#define NP0 0
#define NP1 0
      const int np0 = ei >= 16 ? 0 : (ei & 7) == (((384 - (int)(size_t)Wo) & 1023) >> 7) ? NP1 : NP0;
      for (int i = 0; i < 32; ++i) {
        if ((i & 3) == 0 && (i >> 2) < np0)
          asm volatile("s_mov_b32 m0, %0\n\ts_nop 3\n\tbuffer_load_dword %1, %2, %3 offen lds" :: "s"((int)(size_t)(LAS unsigned char*)lds + 160000 + w * 256), "v"(lane * (DM * 2) + ei * 128),
                       "s"(__builtin_amdgcn_make_buffer_rsrc((void*)Wo, 0, DM * HD * 2, 0x00020000)), "s"((16 * w + (((ei + 4 * w) & 15) ^ (i >> 2))) * 64 * DM * 2) : "memory");
        const int p = i * 256 + tid, un = p >> 5, rp = 4 * (p & 7) + ((p >> 3) & 3);
        const u32x2 a = *(const u32x2*)(lds + (2 * rp) * 2064 + un * 8), c = *(const u32x2*)(lds + (2 * rp + 1) * 2064 + un * 8);
        __builtin_amdgcn_raw_buffer_store_b128((u32x4){a[0], a[1], c[0], c[1]}, rst, (p >> 3) * 4096 + (p & 7) * 16, r0 * 2, 16);
      }
      asm volatile("s_waitcnt vmcnt(0) lgkmcnt(0)\n\ts_barrier" ::: "memory");
      }
      if (UV == 1) { WWARM(4, (ei * 4 + w) * 4) waitvm<0>(); }
      if (ei >= 16) {
        wait_start(rsx, fb, tag, true);
        if (tid == 0) __hip_atomic_fetch_add((u32*)(xb + XUDN), 1u, RLX_AGENT);
      }
      }
    }
    asm volatile("s_waitcnt lgkmcnt(0)\n\ts_barrier" :: "s"(tag) : "memory");
    if (w == 0 && fresh_lane() == 0) __builtin_amdgcn_raw_buffer_store_b32(tag, rsx, 0, PGO_S + si * 128, 16);
    const bool isg = g == 0 ? li >= 16 : (g >= 5 - GNG && li < 16);
    if (xi >= 0 && !(GNOSSM && isg)) {
      LOCAL_IDS
      wait_start(rsx, fb, tag, true);
      for (int it = 0; it < SPIN_CAP; ++it) {
        if (ldw(rsx, XUDN) >= 15 + 16 * !!USH) break;
        asm volatile("s_sleep 8" ::: "memory");
      }
      asm volatile("" ::: "memory");
      int un = g == 0 ? li : g == 1 ? 16 + li : 5 + 11 * g + li;
      SsmIn in;
      const __amdgpu_buffer_rsrc_t rsd = __builtin_amdgcn_make_buffer_rsrc((void*)Dskip, 0, SSM_D * 2, 0x00020000);
      ssm_load(in, 4 * un, rst, rsd, tid, lane, w);
      if (lane < 20) *(u32x4*)(lds + w * SU_CH + (lane < 12 ? SU_U + (lane / 6) * SU_CP + (lane % 6) * 16 : SU_K + ((lane - 12) >> 2) * 5200 + (lane & 3) * 16)) = (u32x4){0u, 0u, 0u, 0u};
      SSM_GOT(in);
#define NU_
      for (bool pend = false; un < 256; pend = true NU_) un = ssm_unit(lds, 4 * un, in, pend, rst, rsx, fb, rsd, tid, lane, w);
    }
    if (isg) {
      g_role(lds, Wo, out, rsx, rsf, fb, tag, g ? g + GNG - 4 : 0, li & 15, w, (u32*)(xb + XUDN), u, rst);
    }
    else {
      const int lane = fresh_lane();
      if (xi < 0) a_role(rsx, rsf, (ei - 16) * 4 + w, (NSEG - 4) * NH, (ei - 16) * 4 + w, 0, lane, fb, tag);
      do {
        const int t = __builtin_amdgcn_readfirstlane(lane == 0 ? __hip_atomic_fetch_add((u32*)(xb + XTK2), 1u, RLX_AGENT) + 1u : 0u);
        if (t >= 256) break;
        a_late(rsx, rsf, t, lane, fb, tag);
      } while (xi < 0);
    }
  }
}

extern "C" void kernel_launch(void* const* d_in, const int* in_sizes, int n_in, void* d_out, int out_size,
                              void* d_ws, size_t ws_size, hipStream_t stream) {
  const u16* x = (const u16*)d_in[0];
  const u16* mem0 = (const u16*)d_in[1];
  const u16* Wq = (const u16*)d_in[2];
  const u16* Wk = (const u16*)d_in[3];
  const u16* Wv = (const u16*)d_in[4];
  const u16* Wo = (const u16*)d_in[5];
  const u16* u = (const u16*)d_in[7];
  const u16* A_re = (const u16*)d_in[8];
  const u16* A_im = (const u16*)d_in[9];
  const u16* B_re = (const u16*)d_in[10];
  const u16* B_im = (const u16*)d_in[11];
  const u16* C_re = (const u16*)d_in[12];
  const u16* C_im = (const u16*)d_in[13];
  const u16* log_dt = (const u16*)d_in[14];
  const u16* Dskip = (const u16*)d_in[15];
  u16* out = (u16*)d_out;
  char* ws = (char*)d_ws;

  main_kernel<<<256, 256, 0, stream>>>(x, mem0, Wq, Wk, Wv, Wo, (unsigned char*)ws, u, A_re, A_im, B_re, B_im, C_re, C_im, log_dt, Dskip, out
      );
}
```

```cpp
#include <hip/hip_runtime.h>
#include <hip/hip_bf16.h>
#include <stdint.h>

#define S_LEN 2048
#define DM 1024
#define NH 32
#define DH 128
#define HD 4096
#define MEM 8
#define SEG 32
#define NSEG 64
#define LTOK 40
#define SSM_L 2048
#define SSM_D 1024
#define SSM_N 128
#define OUTLD 2048

typedef unsigned short u16;
typedef unsigned int u32;
typedef unsigned long long u64;
typedef __attribute__((ext_vector_type(8))) short bf16x8;
typedef __attribute__((ext_vector_type(4))) float f32x4;
typedef __attribute__((ext_vector_type(4))) unsigned int u32x4;
typedef __attribute__((ext_vector_type(2))) unsigned int u32x2;
typedef __bf16 bf16x2_t __attribute__((ext_vector_type(2)));
typedef short v4i16_t __attribute__((ext_vector_type(4)));

#define SENT 0xFFFFFFFFu
#define RLX_AGENT __ATOMIC_RELAXED, __HIP_MEMORY_SCOPE_AGENT

__device__ __forceinline__ float bf2f(u16 v) { return __uint_as_float(((u32)v) << 16); }
__device__ __forceinline__ u16 f2bf(float f) {
  u32 u = __float_as_uint(f);
  u32 r = (u + 0x7fffu + ((u >> 16) & 1u)) >> 16;
  if ((u & 0x7fffffffu) > 0x7f800000u) r = 0x7fc0u;
  return (u16)r;
}
__device__ __forceinline__ float rbf(float f) { return bf2f(f2bf(f)); }

#define TR 92
#define TP 179
#define TDB 61
#define TDC 110
#define NR1 4
#define XPAGE (2 << 20)
#define XRING 3
#define XLP(p, L) ((((L) >> 13) << 15) + ((p) << 14) + ((L) & 8191))
#define XO_R3 131072
#define XR1A(x, L) XLP((x) & 1, ((x) >> 1) * 16384 + (L))
#define XR3A(rg, L) (XO_R3 + XLP(rg, L))
#define XSB(s) (((s) % XRING) == 2 ? 1048576 : ((s) % XRING) * 262144)
#define XR2B 524288
#define XR2S 1048576
#define XR2(j) (((((j) & 31) + (((j) >> 6) << 5)) / 6 << 15) + (((((j) >> 5) & 1) ^ 1) << 14) + ((((j) & 31) + (((j) >> 6) << 5)) % 6) * 1280)
#define XSYN (1048576 + 262144)
#define PGO_R (XSYN + 32768)
#define XCAL (PGO_R + 32768)
static_assert(XCAL + 16384 <= XPAGE, "exchange page");
#define WS_X 0ull
#define NPG 2
#define WS_X_SZ ((u64)NPG * XPAGE)
#define WS_F (WS_X + WS_X_SZ)
#define XEPO (fb)
#define XSTA (fb + 128)
#define PGO_X (fb + 256)
#define PGO_Y (PGO_X + 96 * 128)
#define PGO_S (PGO_Y + 128 * 128)
#define PGO_D (PGO_S + 128 * 128)
#define PGO_A (PGO_D + 128 * 128)
#define XTKT (PGO_A + 192 * 128)
#define XUDN (XTKT + 128)
#define XYDN (XUDN + 128)
#define XTK2 (XYDN + 128)
#define XSTR(x) (XTK2 + 128 + (x) * 128)
#define WS_F_SZ ((u64)(2 + 96 + 128 + 128 + 128 + 192 + 8) * 128)
#define WS_XCH_END (WS_F + 131072)
#define WS_QX (WS_XCH_END)
#define WS_QKV_SZ ((u64)S_LEN * HD * 2)
#define WS_KX (WS_QX + WS_QKV_SZ)
#define WS_VX (WS_KX + WS_QKV_SZ)
#define WS_OX (WS_VX + WS_QKV_SZ)
#define WS_KM (WS_OX + WS_QKV_SZ)
#define WS_KM_SZ ((u64)NSEG * 8 * HD * 2)
#define WS_VM (WS_KM + WS_KM_SZ)
#define WS_UT (WS_VM + WS_KM_SZ)
#define WS_YT (WS_UT + (u64)SSM_L * SSM_D * 2)
#define WS_KD (WS_YT + (u64)SSM_L * SSM_D * 2)
#define WS_TOTAL (WS_KD + (u64)SSM_D * 3072)

#define SPIN_CAP 400000
#define TRY_CAP 20000
#define LAS __attribute__((address_space(3)))
#define LBAR() asm volatile("s_waitcnt lgkmcnt(0)\n\ts_barrier" ::: "memory")
__device__ __forceinline__ u32 max4u(u32x4 v) { return max(max(v[0], v[1]), max(v[2], v[3])); }
typedef float f32x2_t __attribute__((ext_vector_type(2)));
__device__ __forceinline__ u32 pack2(float a, float b) {
  f32x2_t v = {a, b};
  bf16x2_t r = __builtin_convertvector(v, bf16x2_t);
  return __builtin_bit_cast(u32, r);
}
__device__ __forceinline__ float rbf_hw(float a) { return __uint_as_float(pack2(a, a) & 0xffff0000u); }
__device__ __forceinline__ u32 nosent(u32 v) { return min(v, 0xFFFFFFFEu); }
template <int N> __device__ __forceinline__ void waitvm() { asm volatile("s_waitcnt vmcnt(%0)" ::"n"(N) : "memory"); }
typedef __attribute__((ext_vector_type(16))) float f32x16;
__device__ __forceinline__ u32 split_hl(float kv) {
  const u32 h = pack2(kv, kv) & 0xffffu;
  const float r1 = kv - __uint_as_float(h << 16);
  return h | (pack2(r1, r1) & 0xffff0000u);
}
__device__ __forceinline__ int kidx(int idx) {
  const int sh = idx < 256 ? 0 : (idx < 512 ? 1 : (idx < 1024 ? 2 : 3));
  const int q = sh ? ((idx + (1 << (sh - 1)) - 1 + ((idx >> sh) & 1)) >> sh) : idx;
  return q + 128 * sh;
}

#define FO_Q 0
#define FO_K ((int)WS_QKV_SZ)
#define FO_V ((int)(2 * WS_QKV_SZ))
#define FO_O ((int)(3 * WS_QKV_SZ))
#define FO_KM ((int)(4 * WS_QKV_SZ))
#define FO_VM ((int)(4 * WS_QKV_SZ + WS_KM_SZ))
__device__ __forceinline__ u32x4 wait_start(__amdgpu_buffer_rsrc_t rsx, int fb, u32 tag, bool slow, int xcd = 0) {
  u32x4 st;
  for (int it = 0; it < SPIN_CAP; ++it) {
    asm volatile("" ::: "memory");
    st = __builtin_amdgcn_raw_buffer_load_b128(rsx, 0, slow ? XSTA : XSTR(xcd), 16);
    if ((u32)__builtin_amdgcn_readfirstlane(st[3]) == tag) break;
    if (slow) asm volatile("s_sleep 20" ::: "memory"); else asm volatile("s_sleep 1" ::: "memory");
  }
  asm volatile("" ::: "memory");
  return st;
}
__device__ __forceinline__ int xprog(u32 v, u32 tag) { return v - tag <= 80u ? (int)(v - tag) : -1; }
__device__ __forceinline__ float xmax4(float m) {
  { auto r = __builtin_amdgcn_permlane16_swap(__float_as_uint(m), __float_as_uint(m), false, false); m = __builtin_fmaxf(__uint_as_float(r[0]), __uint_as_float(r[1])); }
  { auto r = __builtin_amdgcn_permlane32_swap(__float_as_uint(m), __float_as_uint(m), false, false); m = __builtin_fmaxf(__uint_as_float(r[0]), __uint_as_float(r[1])); }
  return m;
}
__device__ __forceinline__ float xsum4(float m) {
  { auto r = __builtin_amdgcn_permlane16_swap(__float_as_uint(m), __float_as_uint(m), false, false); m = __uint_as_float(r[0]) + __uint_as_float(r[1]); }
  { auto r = __builtin_amdgcn_permlane32_swap(__float_as_uint(m), __float_as_uint(m), false, false); m = __uint_as_float(r[0]) + __uint_as_float(r[1]); }
  return m;
}
#define MF16(a, b, c) __builtin_amdgcn_mfma_f32_16x16x32_bf16(__builtin_bit_cast(bf16x8, a), __builtin_bit_cast(bf16x8, b), c, 0, 0, 0)
__device__ __forceinline__ f32x4 rnd2(f32x4 v, float sc) {
  const u32 a = pack2(v[0], v[1]), b = pack2(v[2], v[3]);
  const u32 c = pack2(__uint_as_float(a << 16) * sc, __uint_as_float(a & 0xffff0000u) * sc), d = pack2(__uint_as_float(b << 16) * sc, __uint_as_float(b & 0xffff0000u) * sc);
  return (f32x4){__uint_as_float(c << 16), __uint_as_float(c & 0xffff0000u), __uint_as_float(d << 16), __uint_as_float(d & 0xffff0000u)};
}
struct XaOps { u32x4 q[2][4], k[2][4], v[8]; };
__device__ __forceinline__ void xa_load(XaOps& o, __amdgpu_buffer_rsrc_t rsf, int sh, int vo) {
#pragma unroll
  for (int i = 0; i < 8; ++i) {
    o.q[i >> 2][i & 3] = __builtin_amdgcn_raw_buffer_load_b128(rsf, vo, FO_Q + (sh * 8 + i) * 1024, 16);
    o.k[i >> 2][i & 3] = __builtin_amdgcn_raw_buffer_load_b128(rsf, vo, FO_K + (sh * 8 + i) * 1024, 16);
  }
#pragma unroll
  for (int dt = 0; dt < 8; ++dt) o.v[dt] = __builtin_amdgcn_raw_buffer_load_b128(rsf, vo, FO_V + (sh * 8 + dt) * 1024, 16);
}
struct XaPre { f32x4 a[2], b[2]; float mx[2], tx[2]; };
__device__ __forceinline__ void xa_pre(XaPre& p, const XaOps& o, int lane) {
  const int lr = lane & 15, lg = lane >> 4;
  const float scale = 0.08838834764831845f, L2E = 1.4426950408889634f, NEG = -1000000000.0f;
  const f32x4 z = (f32x4){0, 0, 0, 0};
  f32x4 s00 = z, s10 = z, s11 = z;
#pragma unroll
  for (int ks = 0; ks < 4; ++ks) { s00 = MF16(o.k[0][ks], o.q[0][ks], s00); s10 = MF16(o.k[0][ks], o.q[1][ks], s10); s11 = MF16(o.k[1][ks], o.q[1][ks], s11); }
  s00 = rnd2(s00, scale); s10 = rnd2(s10, scale); s11 = rnd2(s11, scale);
  asm volatile("" : "+v"(s00), "+v"(s11));
  float m0 = NEG, m1 = -3.0e38f;
#pragma unroll
  for (int e = 0; e < 4; ++e) {
    const bool msk = 4 * lg + e > lr;
    s00[e] = msk ? NEG : s00[e]; s11[e] = msk ? NEG : s11[e];
    m0 = __builtin_fmaxf(m0, s00[e]); m1 = __builtin_fmaxf(m1, __builtin_fmaxf(s10[e], s11[e]));
  }
  m0 = xmax4(m0); m1 = xmax4(m1);
  const float em = __builtin_amdgcn_exp2f((NEG - m0) * L2E);
  float t0 = 4.f * em, t1 = 0.f;
#pragma unroll
  for (int e = 0; e < 4; ++e) {
    s00[e] = __builtin_amdgcn_exp2f((s00[e] - m0) * L2E); s10[e] = __builtin_amdgcn_exp2f((s10[e] - m1) * L2E); s11[e] = __builtin_amdgcn_exp2f((s11[e] - m1) * L2E);
    t0 += s00[e]; t1 += s10[e] + s11[e];
  }
  p.a[0] = s00; p.b[0] = (f32x4){em, em, em, em}; p.a[1] = s10; p.b[1] = s11; p.mx[0] = m0; p.mx[1] = m1; p.tx[0] = t0; p.tx[1] = t1;
}
__device__ __forceinline__ void xa_pw(u32x4& px, u32x2& pm, f32x4 sm, f32x4 sa, f32x4 sb, float mx, float tx, int lg) {
  const float scale = 0.08838834764831845f, L2E = 1.4426950408889634f;
  sm = rnd2(sm, scale);
  asm("" : "+v"(sm));
  float m = mx;
#pragma unroll
  for (int e = 0; e < 4; ++e) { sm[e] = lg >= 2 ? -INFINITY : sm[e]; m = __builtin_fmaxf(m, sm[e]); }
  m = xmax4(m);
  const float al = __builtin_amdgcn_exp2f((mx - m) * L2E);
  float ts = tx * al;
#pragma unroll
  for (int e = 0; e < 4; ++e) { sm[e] = __builtin_amdgcn_exp2f((sm[e] - m) * L2E); ts += sm[e]; }
  const float r = __builtin_amdgcn_rcpf(xsum4(ts)), ra = r * al;
  px = (u32x4){pack2(sa[0] * ra, sa[1] * ra), pack2(sa[2] * ra, sa[3] * ra), pack2(sb[0] * ra, sb[1] * ra), pack2(sb[2] * ra, sb[3] * ra)};
  pm = (u32x2){pack2(sm[0] * r, sm[1] * r), pack2(sm[2] * r, sm[3] * r)};
}
__device__ __forceinline__ void xa_pv(const u32x4 (&v)[8], const u32x2 (&vm)[8], u32x4 px, u32x2 pm, __amdgpu_buffer_rsrc_t rsf, int vo, int so) {
  f32x4 o[8];
#define LX(i) "v_mfma_f32_16x16x32_bf16 %" #i ", %[v" #i "], %[px], 0\n\t"
#define LM(i) "v_mfma_f32_16x16x16_bf16 %" #i ", %[m" #i "], %[pm], %" #i "\n\t"
  asm volatile("s_nop 1\n\t" LX(0) LX(1) LX(2) LX(3) LX(4) LX(5) LX(6) LX(7) LM(0) LM(1) LM(2) LM(3) LM(4) LM(5) LM(6) LM(7) "s_nop 7"
               : "=&v"(o[0]), "=&v"(o[1]), "=&v"(o[2]), "=&v"(o[3]), "=&v"(o[4]), "=&v"(o[5]), "=&v"(o[6]), "=&v"(o[7])
               : [v0] "v"(v[0]), [v1] "v"(v[1]), [v2] "v"(v[2]), [v3] "v"(v[3]), [v4] "v"(v[4]), [v5] "v"(v[5]), [v6] "v"(v[6]), [v7] "v"(v[7]), [px] "v"(px),
                 [m0] "v"(vm[0]), [m1] "v"(vm[1]), [m2] "v"(vm[2]), [m3] "v"(vm[3]), [m4] "v"(vm[4]), [m5] "v"(vm[5]), [m6] "v"(vm[6]), [m7] "v"(vm[7]), [pm] "v"(pm));
#pragma unroll
  for (int pr = 0; pr < 4; ++pr)
    __builtin_amdgcn_raw_buffer_store_b128((u32x4){pack2(o[2 * pr][0], o[2 * pr][1]), pack2(o[2 * pr][2], o[2 * pr][3]), pack2(o[2 * pr + 1][0], o[2 * pr + 1][1]), pack2(o[2 * pr + 1][2], o[2 * pr + 1][3])},
                                           rsf, vo, so + pr * 1024, 16);
}
__device__ __forceinline__ u32 xa_chk(const u32x4 (&km)[4], const u32x2 (&vm)[8]) {
  u32 ck = max(max(max4u(km[0]), max4u(km[1])), max(max4u(km[2]), max4u(km[3])));
#pragma unroll
  for (int i = 0; i < 8; ++i) ck = max(ck, max(vm[i][0], vm[i][1]));
  return ck;
}
#define XSG 56
#define XST 0
#define STR2(x) #x
#define STR(x) STR2(x)
#define XS0 48
#define XL_D 26
#define NAW 64
__device__ __forceinline__ void a_role(__amdgpu_buffer_rsrc_t rsx, __amdgpu_buffer_rsrc_t rsf, int sh, int lim, int wi, int done, int lane, int fb, u32 tag) {
  const int lr = lane & 15, lg = lane >> 4;
  const int okm = (lg * 8 + (lr & 7)) * 16, ovm = lg < 2 ? (lg * 16 + lr) * 8 : 0x7ffffff0;
  int rp = 0, xp = -1, spin = 0, pub = done, miss = 0, tp = 200;
  u32 tseen = 0, tq0 = 0, tq1 = 0, tq2 = 0, tq3 = 0; (void)tq0; (void)tq1; (void)tq2; (void)tq3;
#define XA_WAITX(s_)                                                                  \
  while (xp <= ((s_) >> 5)) {                                                                                          \
    asm volatile("" ::: "memory");                                                                                     \
    int pv = min(xprog(__builtin_amdgcn_raw_buffer_load_b32(rsx, lane * 128, PGO_X, 16), tag), xprog(__builtin_amdgcn_raw_buffer_load_b32(rsx, (64 + (lane & 31)) * 128, PGO_X, 16), tag)); \
    _Pragma("unroll") for (int o_ = 1; o_ < 64; o_ *= 2) pv = min(pv, __shfl_xor(pv, o_));                             \
    xp = __builtin_amdgcn_readfirstlane(pv);                                                                           \
    if (xp > ((s_) >> 5) || ++spin > SPIN_CAP) break;                                                                  \
    if (XEP) { asm volatile("s_waitcnt vmcnt(0)" ::: "memory"); XA_PUB() }            \
    asm volatile("s_sleep " STR(XSL) ::: "memory");                                                                    \
  }
#define XEP 1
#define XSL 16
#define XA_PUB() if (pub != done) { pub = done; asm volatile("s_nop 4\n\tbuffer_store_dword %0, %1, %2, %3 offen sc1" :: "v"(done), "v"(lane < 2 ? lane * 4 : 0x7ffffff0), "s"(rsx), "s"(PGO_A + wi * 128) : "memory"); }
#define XA_NOW ((u32)__builtin_amdgcn_s_memrealtime())
#define XA_SEEN(r_, l_) { const u32 n_ = XA_NOW; tseen = n_ - ((n_ - (r_ >> 16)) & 0x7fffu) + XA_LAG; tp = max(r_ & 1023, 1); rp = l_; }
#define XA_KM \
      _Pragma("unroll") for (int i = 0; i < 4; ++i) km[i] = __builtin_amdgcn_raw_buffer_load_b128(rsf, okm, FO_KM + (sh * 4 + i) * 512, 16); \
      _Pragma("unroll") for (int i = 0; i < 8; ++i) vm[i] = __builtin_amdgcn_raw_buffer_load_b64(rsf, ovm, FO_VM + (sh * 8 + i) * 256, 16);
#define XPRE 2
#define XA_STEP(CUR, NXT, ODD) \
  { \
    const int seg = sh >> 5; \
    int tries = (seg >= XS0 && tseen) ? 3 : 0; \
    while (!pre) {               \
      if (rp > seg) tries = 0; \
      else if (tries) { \
        const u32 due = tseen + (u32)(seg - rp) * tp + XL_D - XA_LAG + (seg < XSG ? stg : 0); \
        for (int i_ = 0; i_ < 4000 && (int)(due - XA_NOW) > 0; ++i_) asm volatile("s_sleep 1" ::: "memory"); \
      } else while (true) { \
        int want = seg + 1; \
        if (tseen) { \
          const u32 due = tseen + (u32)(want - rp) * tp + (seg < XSG ? stg : 0); \
          for (int i_ = 0; i_ < 4000 && (int)(due - XA_NOW) > 0; ++i_) asm volatile("s_sleep 2" ::: "memory"); \
          if (!miss) want = min(max(want, rp + max((int)(XA_NOW - tseen), 0) / tp), NSEG - 1); \
        } \
        asm volatile("" ::: "memory"); \
        const int r_ = __builtin_amdgcn_readfirstlane(__builtin_amdgcn_raw_buffer_load_b32(rsx, 0, pgr + want * 128, 16)); \
        if (r_ >= 0) { XA_SEEN(r_, want) miss = 0; break; } \
        if (++spin > SPIN_CAP) break; \
        if (miss++) asm volatile("s_sleep 20" ::: "memory"); \
      } \
      asm volatile("" ::: "memory"); \
      XA_DBG2 \
      XA_KM \
        \
      const int pk_ = min(rp + max((int)(XA_NOW - tseen), 0) / tp, NSEG - 1); \
      const u32 pv_ = __builtin_amdgcn_raw_buffer_load_b32(rsx, 0, pgr + pk_ * 128, 16); \
      asm volatile("" ::: "memory"); \
      __builtin_amdgcn_s_waitcnt(0x0F70);          \
      asm volatile("" ::: "memory"); \
      { const int r_ = __builtin_amdgcn_readfirstlane(pv_); if (r_ >= 0) XA_SEEN(r_, pk_) } \
      if (!tries || __ballot(xa_chk(km, vm) == SENT) == 0) break; \
      --tries; \
    } \
    XA_DBG3 \
    __builtin_amdgcn_sched_barrier(0); \
    f32x4 s0 = (f32x4){0, 0, 0, 0}, s1 = s0; \
    _Pragma("unroll") for (int ks = 0; ks < 4; ++ks) { s0 = MF16(km[ks], CUR.q[0][ks], s0); s1 = MF16(km[ks], CUR.q[1][ks], s1); } \
    u32x4 px0, px1; u32x2 pm0, pm1; \
    xa_pw(px0, pm0, s0, P.a[0], P.b[0], P.mx[0], P.tx[0], lg); \
    xa_pw(px1, pm1, s1, P.a[1], P.b[1], P.mx[1], P.tx[1], lg); \
    xa_pv(CUR.v, vm, px0, pm0, rsf, lane * 16, FO_O + (seg * 256 + (sh & 31) * 4) * 1024); \
    xa_pv(CUR.v, vm, px1, pm1, rsf, lane * 16, FO_O + (seg * 256 + 128 + (sh & 31) * 4) * 1024); \
    ++done; \
    XA_DBG XA_DBG5 \
    __builtin_amdgcn_sched_barrier(0); \
    sh += NAW; \
      \
    pre = (!XPRE || (ODD && sh >= lim)) ? 0 : rp > seg + 2 ? 1 : (XPRE > 1 && seg + 2 >= XS0 && tseen && (int)(tseen + (u32)(seg + 2 - rp) * tp + XL_D - XA_LAG - XA_NOW) <= 0) ? 2 : 0; \
    asm volatile("" ::: "memory"); \
    if (pre) { XA_DBG2 XA_KM } \
    const int pk2 = pre ? min(rp + max((int)(XA_NOW - tseen), 0) / tp, NSEG - 1) : rp; \
    const u32 pv2 = __builtin_amdgcn_raw_buffer_load_b32(rsx, pk2 > rp ? 0 : 0x7ffffff0, pgr + pk2 * 128, 16); \
    asm volatile("" ::: "memory"); \
    if (XEP > 1 && !ODD && sh < 2 * NAW) { XA_WAITX(sh) asm volatile("" ::: "memory"); xa_load(NXT, rsf, sh, lane * 16); } \
    xa_pre(P, NXT, lane); \
    __builtin_amdgcn_sched_barrier(0); \
    XA_WAITX(min(sh + NAW, lim - 1)) \
    asm volatile("" ::: "memory"); \
    xa_load(CUR, rsf, sh + NAW, sh + NAW < lim ? lane * 16 : 0x7ffffff0); \
    asm volatile("" ::: "memory"); \
    __builtin_amdgcn_s_waitcnt(0x4F78);          \
    asm volatile("" ::: "memory"); \
    XA_PUB() \
    if (pk2 > rp) { const int r_ = __builtin_amdgcn_readfirstlane(pv2); if (r_ >= 0) XA_SEEN(r_, pk2) } \
    if (pre == 2 && __ballot(xa_chk(km, vm) == SENT) != 0) pre = 0; \
  }
#define XA_LAST 22
#define XA_LAG 30
#define XA_DBG
#define XA_DBG2
#define XA_DBG3
#define XA_DBG4
#define XA_DBG5
  const int pgr = PGO_R + (__builtin_amdgcn_readfirstlane(wait_start(rsx, fb, tag, true)[2]) & 8192);
  XaOps A, B;
  XaPre P;
  u32x4 km[4]; u32x2 vm[8];
  int pre = 0;
  static_assert(NAW == 64 && (NSEG - 4) * NH % (2 * NAW) == 0, "two segments per turn");
  const int stg = ((wi * 5) & 31) * XST;
  XA_WAITX(sh)
  asm volatile("" ::: "memory");
  xa_load(A, rsf, sh, lane * 16);
  xa_pre(P, A, lane);
  if (XEP < 2) { XA_WAITX(sh + NAW) }
  asm volatile("" ::: "memory");
  xa_load(B, rsf, sh + NAW, XEP < 2 ? lane * 16 : 0x7ffffff0);
  do { XA_STEP(A, B, 0) XA_STEP(B, A, 1) } while (sh < lim);
  asm volatile("s_waitcnt vmcnt(0)" ::: "memory");
  XA_PUB()
}

#define LLP 128
#define GL_D 130
__device__ __forceinline__ int a_late(__amdgpu_buffer_rsrc_t rsx, __amdgpu_buffer_rsrc_t rsf, int lane, int fb, u32 tag, int xp) {
  const int lr = lane & 15, lg = lane >> 4;
  const float scale = 0.08838834764831845f, L2E = 1.4426950408889634f, NEG = -1000000000.0f;
  int spin = 0;
  asm volatile("" ::: "memory");
  const int tk = __builtin_amdgcn_raw_ptr_buffer_atomic_add_i32(1, rsx, lane ? 0x7ffffff0 : 0, XTK2, 0);
  u32x4 st = __builtin_amdgcn_raw_buffer_load_b128(rsx, 0, XSTA, 16);
#undef XA_PUB
#define XA_PUB()
  XA_WAITX(NSEG * NH - 1)
  const int t = __builtin_amdgcn_readfirstlane(tk) + 1, rt = t & 1, sh = (NSEG - 4) * NH + (t >> 1), seg = sh >> 5;
  if (t >= 256) return t;
  if ((u32)__builtin_amdgcn_readfirstlane(st[3]) != tag) st = wait_start(rsx, fb, tag, true);
  const int pgr = PGO_R + (__builtin_amdgcn_readfirstlane(st[2]) & 8192);
  int r40v, rmv;
  asm volatile("" ::: "memory");
  u32x4 q[4], v[8];
  f32x4 sa = (f32x4){0, 0, 0, 0}, sb = sa;
  float mx = -3.0e38f, tx = 0.f;
  {
    u32x4 k0[4], k1[4];
#pragma unroll
    for (int i = 0; i < 4; ++i) {
      q[i] = __builtin_amdgcn_raw_buffer_load_b128(rsf, lane * 16, FO_Q + (sh * 8 + rt * 4 + i) * 1024, 16);
      k0[i] = __builtin_amdgcn_raw_buffer_load_b128(rsf, lane * 16, FO_K + (sh * 8 + i) * 1024, 16);
      k1[i] = __builtin_amdgcn_raw_buffer_load_b128(rsf, lane * 16, FO_K + (sh * 8 + 4 + i) * 1024, 16);
    }
#pragma unroll
    for (int dt = 0; dt < 8; ++dt) v[dt] = __builtin_amdgcn_raw_buffer_load_b128(rsf, lane * 16, FO_V + (sh * 8 + dt) * 1024, 16);
    asm volatile("" ::: "memory");
    r40v = __builtin_amdgcn_raw_buffer_load_b32(rsx, 0, pgr + 40 * 128, 16);
    rmv = __builtin_amdgcn_raw_buffer_load_b32(rsx, 0, pgr + (seg - 1) * 128, 16);
    asm volatile("" ::: "memory");
#pragma unroll
    for (int ks = 0; ks < 4; ++ks) { sa = MF16(k0[ks], q[ks], sa); sb = MF16(k1[ks], q[ks], sb); }
    sa = rnd2(sa, scale); sb = rnd2(sb, scale);
    asm volatile("" : "+v"(sa), "+v"(sb));
#pragma unroll
    for (int e = 0; e < 4; ++e) {
      sa[e] = 4 * lg + e > lr + 16 * rt ? NEG : sa[e]; sb[e] = 16 + 4 * lg + e > lr + 16 * rt ? NEG : sb[e];
      mx = __builtin_fmaxf(mx, __builtin_fmaxf(sa[e], sb[e]));
    }
    mx = xmax4(mx);
#pragma unroll
    for (int e = 0; e < 4; ++e) { sa[e] = __builtin_amdgcn_exp2f((sa[e] - mx) * L2E); sb[e] = __builtin_amdgcn_exp2f((sb[e] - mx) * L2E); tx += sa[e] + sb[e]; }
  }
  const int okm = (lg * 8 + (lr & 7)) * 16, ovm = lg < 2 ? (lg * 16 + lr) * 8 : 0x7ffffff0;
  const int ofl = seg + 1 < NSEG ? pgr + (seg + 1) * 128 : PGO_D + ((sh & 31) * 4 + (lane & 3)) * 128;
  const int owd = lane ? 0x7ffffff0 : PGO_A + (64 + (t >> 1)) * 128 + 4 * rt;
  u32 tq0 = 0, tq1 = 0, tq2 = 0; (void)tq0; (void)tq1; (void)tq2;
  int att = 0, seen = 1;
  while (true) {
    asm volatile("" : "+s"(att));
    if (att == 1) {
      u32 n = (u32)__builtin_amdgcn_s_memrealtime(), due = n;
      const int r40 = __builtin_amdgcn_readfirstlane(r40v);
      if (r40 >= 0) due = n - ((n - (r40 >> 16)) & 0x7fffu) + XA_LAG + (seg - 41) * (r40 & 1023);
      int r = __builtin_amdgcn_readfirstlane(rmv);
      while (r < 0) {
        for (int i = 0; i < 4000 && (int)(due - (u32)__builtin_amdgcn_s_memrealtime()) > 0; ++i) asm volatile("s_sleep 2" ::: "memory");
        asm volatile("" ::: "memory");
        r = __builtin_amdgcn_readfirstlane(__builtin_amdgcn_raw_buffer_load_b32(rsx, 0, pgr + (seg - 1) * 128, 16));
        if (r >= 0 || ++spin > SPIN_CAP) break;
        asm volatile("s_sleep 10" ::: "memory");
      }
      n = (u32)__builtin_amdgcn_s_memrealtime();
      due = n - ((n - (r >> 16)) & 0x7fffu) + (r & 1023) + XL_D - (seg + 1 < NSEG ? 0 : XA_LAST);
      __builtin_amdgcn_raw_buffer_store_b32(((n + max((int)(due - n), 0) + GL_D) & ~1u) | 0x80000000u, rsx, owd, 0, 16);
      for (int i = 0; i < 4000 && (int)(due - (u32)__builtin_amdgcn_s_memrealtime()) > 0; ++i) asm volatile("s_sleep 1" ::: "memory");
      XA_DBG2
    } else if (att > 6) asm volatile("s_sleep 20" ::: "memory");
    const int dr = att ? 0 : 0x7ffffff0;
    asm volatile("" ::: "memory");
    u32x4 km[4]; u32x2 vm[8];
#pragma unroll
    for (int i = 0; i < 4; ++i) km[i] = __builtin_amdgcn_raw_buffer_load_b128(rsf, okm | dr, FO_KM + (sh * 4 + i) * 512, 16);
#pragma unroll
    for (int i = 0; i < 8; ++i) vm[i] = __builtin_amdgcn_raw_buffer_load_b64(rsf, ovm | dr, FO_VM + (sh * 8 + i) * 256, 16);
    const u32 fl = __builtin_amdgcn_raw_buffer_load_b32(rsx, att > 1 ? ofl : 0x7ffffff0, 0, 16);
    f32x4 sm = (f32x4){0, 0, 0, 0};
#pragma unroll
    for (int ks = 0; ks < 4; ++ks) sm = MF16(km[ks], q[ks], sm);
    const u32 ck = xa_chk(km, vm);
    u64 bad = __ballot(ck == SENT), nf = __ballot(fl == SENT);
    asm volatile("" : "+s"(bad), "+s"(nf));
    if (!seen && bad != 0) {
      seen = (att > 1 && nf == 0) || ++spin > SPIN_CAP;
      ++att;
      continue;
    }
    XA_DBG3
    u32x4 px; u32x2 pm;
    xa_pw(px, pm, sm, sa, sb, mx, tx, lg);
    xa_pv(v, vm, px, pm, rsf, (lane * 16) | dr, FO_O + (seg * 256 + rt * 128 + (sh & 31) * 4) * 1024);
    XA_DBG
    asm volatile("s_nop 4\n\ts_waitcnt vmcnt(0)\n\tbuffer_store_dword %0, %1, %2, 0 offen sc1" :: "v"(32), "v"(owd | dr), "s"(rsx) : "memory");
    if (att) break;
    att = 1; seen = 0;
  }
  return t;
}

typedef __attribute__((ext_vector_type(32))) unsigned int u32x32;
typedef __attribute__((ext_vector_type(16))) unsigned int u32x16;
typedef __attribute__((ext_vector_type(8))) unsigned int u32x8;
#define RNG(f, b, n) f "[" STR(b) ":" STR((b) + (n)-1) "]"
#define REG(b) "v[" STR(b) "]"
#define LR_RED 16384
#define LR_CTL LR_RED
#define LR_STG 32768
#define L_RSTAMP LR_STG
#define STAMPV(i, v)
#define EXF (NH * 8 * 1024)
#define EXK 65536
#define OOR 0x7ffffff0

__device__ __forceinline__ u32 lds_word(int addr) {
  u32 v;
  asm volatile("ds_read_b32 %0, %1\n\ts_waitcnt lgkmcnt(0)" : "=v"(v) : "v"(addr) : "memory");
  return __builtin_amdgcn_readfirstlane(v);
}
__device__ __forceinline__ int fresh_lane() {
  int l;
  asm volatile("v_mbcnt_lo_u32_b32 %0, -1, 0\n\tv_mbcnt_hi_u32_b32 %0, -1, %0" : "=v"(l));
  return l;
}
__device__ __forceinline__ u32 rt32() { return (u32)__builtin_amdgcn_s_memrealtime(); }
#define SYO(s) (XSYN + flip + (s) * 64)
#define TKS 0.85f
#define TLB 1
#define TLC 1
#define TLP 2
#define KG0 0
#define CLS0 0u
#define APC 5
#define ADL 12
#define CALN 2
#define TE1 3
#define TE2 1
#define TE3 1
#define XTH 4
#define TS3 42
#define TE0 45
#define TD0 170
#define TD 120
#define PHASE 1
#define XSK 0
#define CLKS(T, R, X) WLG(2) "s_sub_u32 s49, " T ", " R "\n\t" "s_cmp_le_i32 s49, " STR(3 * PHASE) "\n\t" "s_cbranch_scc1 " X "\n\t"
#define CLKH(OFF) WLG(0) "s_memrealtime s[72:73]\n\t" "s_add_u32 s50, s43, s44\n\t" "s_add_u32 s50, s50, " OFF "\n\t" "s_nop 1\n\t" "s_memrealtime s[74:75]\n\t"
#define CLKL(L, T)   L ":\n\t" "s_memrealtime s[76:77]\n\t" CLKS(T, "s72", L "x") "s_memrealtime s[72:73]\n\t" CLKS(T, "s74", L "x")     \
                     "s_memrealtime s[74:75]\n\t" CLKS(T, "s76", L "x") "s_branch " L "\n" \
                     L "x:\n\t" CLKP(L)
#define CLKW(L, OFF) CLKH(OFF) "s_nop 3\n" CLKL(L, "s50")
#define CLKP(L) "s_cmp_lt_i32 s49, 1\n\t" "s_cbranch_scc1 " L "y\n\t" "s_nop " STR(PHN) "\n\t" "s_cmp_lt_i32 s49, 2\n\t" "s_cbranch_scc1 " L "y\n\t" "s_nop " STR(PHN) "\n\t" \
                "s_cmp_lt_i32 s49, 3\n\t" "s_cbranch_scc1 " L "y\n\t" "s_nop " STR(PHN) "\n\t" "s_nop 0\n\t" "s_branch " L "y\n\t" "s_nop 0\n" L "y:\n\t"
#define PHN 3
#define REL(L) "s_barrier\n" L ":\n\t"
#define RELV(L) "s_cbranch_vccnz " L "\n\t" REL(L)
#define RELN(L) "s_cmp_eq_u32 %[wv], 0\n\t" "s_cbranch_scc1 " L "a\n\t" "s_cmp_eq_u32 %[wv], 1\n\t" "s_cbranch_scc1 " L "b\n\t" \
                "s_barrier\n\t" "s_nop " STR(PD2) "\n\t" "s_branch " L "\n" L "b:\n\t" "s_barrier\n\t" "s_branch " L "\n" L "a:\n\t" "s_barrier\n" L ":\n\t"
#define PD2 2
#define TS_(n) "s_memtime s[80+2*" #n ":81+2*" #n "]\n\t"
#define TSA(n)
#define TSB(n)
#define TSW(n)
#define TVM 12
template <int NTL, int KS>
__device__ __forceinline__ void tr_chunk(const u16* S, int lr, int lg, u32 (&r)[NTL][KS][8]) {
  const int q = lr >> 2, p = lr & 3;
#pragma unroll
  for (int t = 0; t < NTL; ++t)
#pragma unroll
    for (int i = 0; i < KS; ++i)
#pragma unroll
      for (int kb = 0; kb < 2; ++kb) {
        const u16* base = S + (64 * i + 32 * kb + 16 * (lg >> 1) + 4 * (lg & 1) + (q & 1) + 8 * (q >> 1)) * (16 * NTL) + 16 * t + 4 * p;
        const u32x2 t0 = __builtin_bit_cast(u32x2, __builtin_amdgcn_ds_read_tr16_b64_v4i16((LAS v4i16_t*)(base)));
        const u32x2 t1 = __builtin_bit_cast(u32x2, __builtin_amdgcn_ds_read_tr16_b64_v4i16((LAS v4i16_t*)(base + 2 * 16 * NTL)));
        r[t][i][4 * kb + 0] = t0[0]; r[t][i][4 * kb + 1] = t0[1]; r[t][i][4 * kb + 2] = t1[0]; r[t][i][4 * kb + 3] = t1[1];
      }
}
#define SMF(acc, a, bf, bi) "v_smfmac_f32_16x16x64_bf16 " RNG("v", acc, 4) ", " RNG("v", a, 4) ", " RNG(bf, bi, 8) ", v34\n\t"
#define ZER(r) "v_mov_b64 " RNG("v", r, 2) ", 0\n\t" "v_mov_b64 " RNG("v", (r) + 2, 2) ", 0\n\t"
#define GLD(r, o) "s_xor_b32 s49, s50, " STR(o) "\n\t" "buffer_load_dwordx4 " RNG("v", r, 4) ", v32, %[rs], s49 offen sc1\n\t"
#define GL1(r, o) "s_xor_b32 s49, s50, " STR(o) "\n\t" "buffer_load_dwordx4 " RNG("v", r, 4) ", v33, %[rs], s49 offen sc1\n\t"
#define GL2(r, o) GL1(r, 4096 + (o))
#define MXA0(r) "v_max3_u32 v63, " REG(r) ", " REG((r) + 1) ", " REG((r) + 1) "\n\t"
#define MXA(r) "v_max3_u32 v63, v63, " REG(r) ", " REG((r) + 1) "\n\t"
#define MXB(r) "v_max3_u32 v63, v63, " REG((r) + 2) ", " REG((r) + 3) "\n\t"
#define CMPB "v_cmp_eq_u32 vcc, s54, v63\n\t"
#define PSA(r) "v_pk_add_f32 " RNG("v", r, 2) ", " RNG("v", r, 2) ", " RNG("v", (r) + 2, 2) "\n\t"
#define PSW(r, o) "ds_write_addtid_b32 " REG(r) " offset:" STR(o) "\n\t"
#define RDQ(r, o) "ds_read_b128 " RNG("v", r, 4) ", v37 offset:" STR(o) "\n\t"
#define SQ(t, i) SMF(64 + 4 * (t), 96 + 4 * (i), "a", ((i) * 4 + (t)) * 8)
#define SV(t, i) SMF(24 + 4 * (t), 96 + 4 * (i), "a", 128 + ((i) * 2 + (t)) * 8)
#define SVP(t, i) SV(t, i)
#define SW(a, q, i) SMF((a) + 4 * (q), 96 + 4 * (i), "v", 128 + ((i) * 2 + (q)) * 8)
#define WVM(n) "s_waitcnt vmcnt(" STR(n) ")\n\t"
#define WLG(n) "s_waitcnt lgkmcnt(" STR(n) ")\n\t"
#define WLGF(n) WLG(n)
#define PACE 7
#define PC "s_nop " STR(PACE) "\n\t"
#define PC6
#define PC7
#define WBLK(a, n, i, M0) WVM((n) + 7 - (i)) SW(a, 0, i) M0(96 + 4 * (i)) SW(a, 1, i) MXB(96 + 4 * (i))
#define CL8(a, b, c, d, e, f, g, h) "v" #a, "v" #b, "v" #c, "v" #d, "v" #e, "v" #f, "v" #g, "v" #h
#define CL64 CL8(64, 65, 66, 67, 68, 69, 70, 71)
#define CL72 CL8(72, 73, 74, 75, 76, 77, 78, 79)
#define CL80 CL8(80, 81, 82, 83, 84, 85, 86, 87)
#define CL88 CL8(88, 89, 90, 91, 92, 93, 94, 95)
#define CL96 CL8(96, 97, 98, 99, 100, 101, 102, 103), CL8(104, 105, 106, 107, 108, 109, 110, 111)
#define CL112 CL8(112, 113, 114, 115, 116, 117, 118, 119)
#define CL120 CL8(120, 121, 122, 123, 124, 125, 126, 127)
#define PKA(d, a, b) "v_pk_add_f32 " RNG("v", d, 2) ", " RNG("v", a, 2) ", " RNG("v", b, 2) "\n\t"
#define CNT(o) STR(4096 + 64 * (o))
#define ARRIVE(o) "ds_add_u32 v45, v39 offset:" CNT(o) "\n\t"

#define EXTRA "v_readlane_b32 s69, v35, s45\n\t" "s_and_b32 s56, s58, 1023\n\t" "s_bfe_u32 s57, s58, 0xa000a\n\t" "s_lshr_b32 s55, s58, 20\n\t" "s_add_u32 s66, s56, s69\n\t" "s_add_u32 s67, s57, s69\n\t" "s_add_u32 s67, s67, s69\n\t"
#define TEX(s) ((s) == 0 ? TE0 : ((s) < 4 ? TE1 : ((s) < 12 ? TE2 : ((s) < TS3 ? TE3 : 0))))
#define R2BASE "s_bitcmp1_b32 s45, 0\n\t" "s_cselect_b32 s71, " STR(XR2S) ", 0\n\t" "s_add_u32 s71, s71, %[r2]\n\t"
#define X4B 0
#define X4C 0
#define STB "buffer_store_dwordx2 v[116:117], v11, %[rs], s36 offen sc1\n"
#define STC "buffer_store_dwordx2 v[80:81], v17, %[rs], s36 offen sc1\n\t"
#define R2TEXT                                                                                                     \
              TSB(0)                                                                                               \
              "buffer_load_dwordx4 v[64:67], v43, %[rs], s71 offen sc1\n\t"                                      \
              "buffer_load_dwordx4 v[68:71], v8, %[rs], s71 offen sc1\n\t"                                       \
              "buffer_load_dwordx4 v[76:79], v9, %[rs], s71 offen sc1\n\t"                                       \
              "buffer_load_dwordx4 v[60:63], v10, %[rs], s71 offen sc1\n\t"                                      \
              "buffer_store_dwordx4 v[72:75], v47, %[rf], s41 offen sc1\n\t"                                     \
              "s_cbranch_vccnz .Lvw%=\n\t" VPOST ARRIVE(12) "s_barrier\n" ".Lvw%=:\n\t"                             \
              "ds_read2_b32 v[96:97], v12 offset0:0 offset1:4\n\t" "ds_read2_b32 v[98:99], v12 offset0:32 offset1:36\n\t" \
              "ds_read2_b32 v[100:101], v13 offset0:0 offset1:4\n\t" "ds_read2_b32 v[102:103], v13 offset0:32 offset1:36\n\t" \
              "ds_read2_b32 v[104:105], v14 offset0:0 offset1:4\n\t" "ds_read2_b32 v[106:107], v14 offset0:32 offset1:36\n\t" \
              "ds_read2_b32 v[108:109], v15 offset0:0 offset1:4\n\t" "ds_read2_b32 v[110:111], v15 offset0:32 offset1:36\n\t" \
              "v_mov_b64 v[112:113], v[80:81]\n\t" "v_mov_b64 v[114:115], v[82:83]\n\t" "s_mov_b32 s65, 0\n\t"     \
              "v_mov_b32_dpp v112, v84 row_ror:8 row_mask:0xf bank_mask:0xc\n\t" "v_mov_b32_dpp v113, v85 row_ror:8 row_mask:0xf bank_mask:0xc\n\t" \
              "v_mov_b32_dpp v114, v86 row_ror:8 row_mask:0xf bank_mask:0xc\n\t" "v_mov_b32_dpp v115, v87 row_ror:8 row_mask:0xf bank_mask:0xc\n\t" \
              "v_pk_mul_f32 v[112:113], v[112:113], s[62:63] op_sel_hi:[1,0]\n\t" "v_pk_mul_f32 v[114:115], v[114:115], s[62:63] op_sel_hi:[1,0]\n\t" \
              WLG(0)                                                                                               \
              PKA(96, 96, 100) PKA(98, 98, 102) PKA(104, 104, 108) PKA(106, 106, 110) PKA(96, 96, 104) PKA(98, 98, 106) \
              "v_cvt_pk_bf16_f32 v96, v96, 0\n\t" "v_cvt_pk_bf16_f32 v97, v97, 0\n\t" "v_cvt_pk_bf16_f32 v98, v98, 0\n\t" "v_cvt_pk_bf16_f32 v99, v99, 0\n\t" \
              "v_accvgpr_write_b32 a194, v96\n\t" "v_accvgpr_write_b32 a202, v97\n\t" "v_accvgpr_write_b32 a198, v98\n\t" "v_accvgpr_write_b32 a206, v99\n\t" \
              TSB(1) WVM(4) TSB(2) \
              "v_permlane32_swap_b32 v64, v66\n\t" "v_permlane32_swap_b32 v65, v67\n\t" \
              "v_pk_add_f32 v[92:93], v[64:65], v[66:67]\n\t" \
              WVM(3) SFM(68) \
              "v_permlane16_swap_b32 v92, v93\n\t" \
              "v_add_f32 v94, v92, v93\n\t" \
              WVM(2) "v_mul_f32 v94, s62, v94\n\t" SFM(76) \
              "v_exp_f32 v92, v94\n\t" \
              WVM(1) TSB(3) SFM(60) \
                \
              ".Lex%=:\n\t" \
              "v_exp_f32 v88, v112\n\t" "v_exp_f32 v89, v113\n\t" "v_cvt_pk_bf16_f32 v58, v92, 0\n\t" "v_exp_f32 v90, v114\n\t" "v_exp_f32 v91, v115\n\t" \
              "v_cvt_pk_bf16_f32 v56, v88, v89\n\t" "v_cvt_pk_bf16_f32 v57, v90, v91\n\t" \
              TSB(4) ROR8(50, 58) ROR8(48, 56) \
              "v_mfma_f32_16x16x32_bf16 v[88:91], v[52:55], v[56:59], 0\n\t" \
              ROR8(49, 57) "s_nop 1\n\t" \
              "v_mfma_f32_16x16x32_bf16 v[116:119], a[192:195], v[56:59], 0\n\t" \
              "s_nop 2\n\t" \
              "v_mfma_f32_16x16x32_bf16 v[116:119], a[196:199], v[48:51], v[116:119]\n\t" \
              "v_add_f32_dpp v92, v88, v88 row_ror:8 row_mask:0xf bank_mask:0xf\n\t" "v_subrev_u32 v89, 0x33800000, v92\n\t" \
              "v_mfma_f32_16x16x32_bf16 v[124:127], a[204:207], v[56:59], 0\n\t" \
              "v_rcp_f32 v93, v92\n\t" \
              "v_mfma_f32_16x16x32_bf16 v[124:127], a[200:203], v[48:51], v[124:127]\n\t" \
              TSB(5) "v_cmp_le_u32 vcc, 0x20000000, v89\n\t" "s_nop 0\n\t" \
              "s_cbranch_vccnz .Lmx%=\n\t" \
              OTAIL STB TSB(6)
#define SFM(r) "v_pk_fma_f32 v[112:113], " RNG("v", r, 2) ", s[62:63], v[112:113] op_sel_hi:[1,0,1]\n\t" "v_pk_fma_f32 v[114:115], " RNG("v", (r) + 2, 2) ", s[62:63], v[114:115] op_sel_hi:[1,0,1]\n\t"
#define ROR8(d, s) "v_mov_b32_dpp " REG(d) ", " REG(s) " row_ror:8 row_mask:0xf bank_mask:0xf\n\t"
#define OTAIL "v_pk_mul_f32 v[116:117], v[116:117], v[92:93] op_sel:[0,1]\n\t" "v_pk_mul_f32 v[118:119], v[118:119], v[92:93] op_sel:[0,1]\n\t" \
              "v_cvt_pk_bf16_f32 v116, v116, v117\n\t" "v_cvt_pk_bf16_f32 v117, v118, v119\n\t" \
              "s_mov_b64 exec, s[78:79]\n\t" \
              "v_pk_mul_f32 v[124:125], v[124:125], v[92:93] op_sel:[0,1]\n\t" "v_pk_mul_f32 v[126:127], v[126:127], v[92:93] op_sel:[0,1]\n\t" \
              "v_cvt_pk_bf16_f32 v116, v124, v125\n\t" "v_cvt_pk_bf16_f32 v117, v126, v127\n\t" \
              "s_mov_b64 exec, -1\n\t"
#define MX3(a, b) "v_max3_u32 v95, v95, " REG(a) ", " REG(b) "\n\t"
#define R2MAX ".Lmx%=:\n\t" "v_max_u32 v95, v60, v61\n\t" MX3(62, 63) MX3(64, 65) MX3(66, 67) MX3(68, 69) MX3(70, 71) MX3(76, 77) MX3(78, 79) \
              "v_cmp_eq_u32 vcc, s54, v95\n\t" "s_cbranch_vccnz .Lfail%=\n\t" OTAIL \
              "s_cmp_lg_u32 s65, 0\n\t" "s_cbranch_scc1 .Lend%=\n\t" "s_mov_b32 s65, 1\n\t" \
              "v_max3_f32 v93, v112, v113, v94\n\t" \
              "v_max3_f32 v93, v93, v114, v115\n\t" \
              "s_nop 1\n\t" \
              "v_max_f32_dpp v92, v93, v93 row_ror:8 row_mask:0xf bank_mask:0xf\n\t" \
              "v_max_f32_dpp v88, v93, v93 row_ror:8 row_mask:0xf bank_mask:0xf\n\t" \
              "s_nop 1\n\t" \
              "v_permlane16_swap_b32 v92, v88\n\t" \
              "v_max_f32 v93, v92, v88\n\t" "v_max_f32 v92, v92, v88\n\t" \
              "s_nop 1\n\t" \
              "v_permlane32_swap_b32 v93, v92\n\t" \
              "v_max_f32 v93, v93, v92\n\t" \
              "v_sub_f32 v94, v94, v93\n\t" "v_sub_f32 v112, v112, v93\n\t" "v_exp_f32 v92, v94\n\t" "v_sub_f32 v113, v113, v93\n\t" "v_sub_f32 v114, v114, v93\n\t" "v_sub_f32 v115, v115, v93\n\t" \
              "s_branch .Lex%=\n"
#define PS1(r) "v_add_f32 " REG(r) ", " REG(r) ", " REG((r) + 2) "\n\t"
#define NOP(n) "s_nop " STR(n) "\n\t"
#define PCA 13
#define PDA 8
#define PDC 8
#define PDF 7
#define SLV 0
#define QKRED(X) RDQ(80, 12288) RDQ(84, 12304) X RDQ(88, 0) RDQ(92, 16) RDQ(112, 4096) RDQ(116, 4112) RDQ(120, 8192) RDQ(124, 8208) \
        WLG(4) TSA(4) PKA(80, 80, 88) PKA(82, 82, 90) PKA(84, 84, 92) PKA(86, 86, 94) \
        WLG(2) PKA(80, 80, 112) PKA(82, 82, 114) PKA(84, 84, 116) PKA(86, 86, 118) \
        WLG(0) PKA(80, 80, 120) PKA(82, 82, 122) PKA(84, 84, 124) PKA(86, 86, 126) \
        "v_cvt_pk_bf16_f32 v72, v80, v81\n\t" "v_cvt_pk_bf16_f32 v73, v82, v83\n\t" "v_cvt_pk_bf16_f32 v74, v84, v85\n\t" "v_cvt_pk_bf16_f32 v75, v86, v87\n\t" \
        NOP(1) TSA(5) \
        "v_mfma_f32_16x16x32_bf16 v[88:91], v[72:75], v[72:75], 0\n\t" NOP(2) \
        "v_mfma_f32_16x16x32_bf16 v[80:83], a[208:211], v[72:75], 0\n\t" NOP(2) \
        "v_mfma_f32_16x16x32_bf16 v[84:87], a[212:215], v[72:75], 0\n\t"
#define QKPUB "buffer_store_dwordx4 v[88:91], v41, %[rs], s71 offen sc1\n\t" NOP(0) \
        "buffer_store_dwordx4 v[80:83], v40, %[rs], s71 offen sc1\n\t" NOP(2) \
        "buffer_store_dwordx4 v[84:87], v40, %[rs], s71 offen offset:512 sc1\n\t"
#define MRED(W, X) "ds_read_b128 v[80:83], v46 offset:12288\n\t" W "ds_read_b128 v[84:87], v46\n\t" "ds_read_b128 v[88:91], v46 offset:4096\n\t" "ds_read_b128 v[92:95], v46 offset:8192\n\t" X TSW(4) \
        WLG(2) TSW(5) PKA(80, 80, 84) PKA(82, 82, 86) WLG(0) PKA(88, 88, 92) PKA(90, 90, 94) PKA(84, 80, 88) PKA(86, 82, 90) \
        "v_cvt_pk_bf16_f32 v80, v84, v85\n\t" "v_cvt_pk_bf16_f32 v81, v86, v87\n\t"
#define ORD(a, b) "v_cmpx_o_f32 s[50:51], " REG(a) ", " REG(b) "\n\t"
#define REDO(L) "s_cmp_lg_u64 exec, -1\n\t" "s_mov_b64 exec, -1\n\t" "s_cbranch_scc1 " L "\n" L "r:\n\t"
#define CWAIT(L, o, S) L ":\n\t" "ds_read_b32 v62, v38 offset:" CNT(o) "\n\t" WLG(0) "v_readfirstlane_b32 s49, v62\n\t" "s_cmp_lt_u32 s49, " S "\n\t" "s_cbranch_scc1 " L "\n\t"
#define VPOST PSA(24) PSA(28) PSW(24, 2560) PSW(25, 2832) PSW(28, 3200) PSW(29, 3472)
#define BARA "s_barrier\n\t"
#define BARC "s_barrier\n\t"
#define PZADR(SB, M) "s_add_u32 s49, s45, 1\n\t" "s_xor_b32 s62, s71, " STR(XR2S) "\n\t" "v_readlane_b32 s63, v19, s49\n\t" "s_mov_b32 s49, " SB "\n\t" \
        ".if !((" STR(M) ") & 1)\n\t" "s_mov_b32 s63, 0x7ff00000\n\t" ".endif\n\t" ".if !((" STR(M) ") & 2)\n\t" "s_mov_b32 s49, 0x7ff00000\n\t" ".endif\n\t" \
        ".if !((" STR(M) ") & 4)\n\t" "s_mov_b32 s62, 0x7ff00000\n\t" ".endif\n\t" "s_cmp_eq_u32 %[wv], 0\n\t" \
        "s_cselect_b32 s49, s49, s62\n\t" "s_cselect_b32 s63, s63, s62\n\t" "v_add_u32 v48, s49, v23\n\t" "s_mov_b32 exec_hi, 0\n\t" "v_add_u32 v48, s63, v23\n\t" "s_mov_b32 exec_hi, -1\n\t"
#define PZST "buffer_store_dwordx4 v[92:95], v48, %[rs], 0 offen sc1\n\t"
#define MA 0
#define MC 0
#define MB (7 - MA - MC)
#define PZD 1
#define PZB0 PZADR("s63", MB)
#define PZB PZST
#define PZC0
#define PZC ""
#define PZNC 0
#define PZA0
#define PZA
#define PZNA 0
#define LASTC(L) RELV(L)
#define LASTA(L) ""
#define PCL PCA
#define ABODY(W, X, F, n) TSA(0) GLD(96, 0) NOP(PCA) GLD(100, 1024) NOP(PCA) GLD(104, 2048) W GLD(108, 3072) "s_mov_b32 s47, 0\n\t" X \
        WVM((n) + 3) TSA(1) SQ(0, 0) SQ(1, 0) SQ(2, 0) SQ(3, 0) \
        WVM((n) + 2) SQ(0, 1) SQ(1, 1) SQ(2, 1) SQ(3, 1) \
        WVM((n) + 1) SQ(0, 2) MXA0(96) MXB(96) SQ(1, 2) MXA(100) MXB(100) SQ(2, 2) MXA(104) MXB(104) SQ(3, 2) \
        WVM(n) TSA(2) SQ(0, 3) MXA(108) MXB(108) CMPB SQ(1, 3) "s_add_u32 s48, s38, 4\n\t" NOP(0) NOP(0) SQ(2, 3) "s_cbranch_vccnz " F "\n\t" PS1(64) PS1(65) SQ(3, 3) PS1(68) PS1(69) \
        NOP(0) PSW(64, 0) PSW(65, 272) PSW(68, 640) PSW(69, 912) PSA(72) PSA(76) PSW(72, 1280) PSW(73, 1552) PSW(76, 1920) PSW(77, 2192)
#define CISSUE(X, Y) TSW(0) GL1(96, 0) PC GL1(100, 1024) PC GL1(104, 2048) PC GL1(108, 3072) PC GL2(112, 0) PC GL2(116, 1024) \
        "s_mov_b32 s48, s36\n\t" "s_mov_b32 s36, s37\n\t" "s_add_u32 s45, s45, 1\n\t" "s_add_u32 s38, s38, 4\n\t" "v_readlane_b32 s37, v19, s45\n\t" \
        "s_add_u32 s39, s39, " STR(EXF) "\n\t" "s_add_u32 s40, s40, " STR(EXF) "\n\t" \
        "s_add_u32 s41, s41, " STR(EXK) "\n\t" "s_add_u32 s42, s42, " STR(EXK) "\n\t" "s_add_u32 s43, s43, s55\n\t" PC6 \
        GL2(120, 2048) "s_mov_b32 s47, 2\n\t" "s_mul_i32 s70, s69, 3\n\t" R2BASE "s_add_u32 s43, s43, s70\n\t" NOP(2) PC7 X \
        GL2(124, 3072) Y
#define ZALL ZER(64) ZER(68) ZER(72) ZER(76) ZER(80) ZER(84) ZER(24) ZER(28)
#define SHRD "ds_read_b32 v62, v38\n\t"
#define SHUP WLG(0) "v_readfirstlane_b32 s49, v62\n\t" "s_max_u32 s44, s44, s49\n\t"
#define DPA(d, r, m) "v_add_f32_dpp " REG(d) ", " REG(r) ", " REG((r) + 2) " quad_perm:[0,1,2,3] row_mask:" m " bank_mask:0xf\n\t"
#define CBODY(m, X, n, F) CBODY_(80, m, X, n, F)
#define CBODY_(a, m, X, n, F) \
        WVM((m) + 7) TSW(1) WBLK(a, m, 0, MXA0) WBLK(a, m, 1, MXA) X WBLK(a, n, 2, MXA) WBLK(a, n, 3, MXA) WBLK(a, n, 4, MXA) WBLK(a, n, 5, MXA) WBLK(a, n, 6, MXA) \
        WVM(n) TSW(2) SW(a, 0, 7) MXA(124) MXB(124) CMPB SW(a, 1, 7) NOP(0) NOP(1) "s_cbranch_vccnz " F "\n\t" \
        DPA(88, a, "0x3") DPA(89, (a) + 1, "0x3") NOP(1) DPA(88, (a) + 4, "0xc") DPA(89, (a) + 5, "0xc") NOP(1) \
        "v_permlane32_swap_b32 v88, v89\n\t" "v_add_f32 v88, v88, v89\n\t" TSW(3) PSW(88, 3840)
#define FINEO
#define NEXTP "s_branch .LA%=\n"
#define NEXTN "s_branch .LNA%=\n"

__device__ __forceinline__ void clk_edge(u32& cyc, u32& tick) {
  const u32 t = rt32();
  for (int i = 0; i < 64; ++i) { tick = rt32(); if (tick != t) break; }
  cyc = (u32)__builtin_amdgcn_s_memtime();
}
__device__ __forceinline__ float clk_rate(u32 cyc0, u32 tk0) { u32 c, t; clk_edge(c, t); return (float)(c - cyc0) / (float)max(t - tk0, 1u); }
__device__ __forceinline__ void dma16(__amdgpu_buffer_rsrc_t rs, int voff, int soff, int sstep, int ldsa) {
#pragma unroll
  for (int i = 0; i < 16; ++i)
    asm volatile("s_mov_b32 m0, %0\n\ts_nop 3\n\tbuffer_load_dwordx4 %1, %2, %3 offen lds" :: "s"(ldsa + i * 1024), "v"(voff), "s"(rs), "s"(soff ^ (i * sstep)) : "memory");
}

#define KH(t) (192 + 8 * (t))
#define KMF(t, a, b) "v_mfma_f32_16x16x32_bf16 " RNG("v", 80 + 4 * (t), 4) ", " RNG("v", KH(t) + 4 * (a), 4) ", " RNG("v", KH(3) + 4 * (b), 4) ", " RNG("v", 80 + 4 * (t), 4) "\n\t"
#define KX(j) (92 + 4 * ((j) & 1))
#define KA(t, j) "v_pk_mul_f32 " RNG("v", KX(j), 2) ", " RNG("v", 64 + 4 * (j), 2) ", " RNG("v", 112 + ((t) & 2), 2) " op_sel:[0," STR((t) & 1) "] op_sel_hi:[1," STR((t) & 1) "]\n\t"
#define KF(j) "v_fract_f32 " REG(KX(j) + 1) ", " REG(KX(j) + 1) "\n\t"
#define KE(j, N) "v_exp_f32 " REG(KX(j)) ", " REG(KX(j)) "\n\t" "v_cos_f32 " REG(KX(j) + 2) ", " REG(KX(j) + 1) "\n\t" "v_sin_f32 " REG(KX(j) + 3) ", " N REG(KX(j) + 1) "\n\t"
#define KM(j) "v_pk_mul_f32 " RNG("v", KX(j) + 2, 2) ", " RNG("v", KX(j) + 2, 2) ", " RNG("v", KX(j), 2) " op_sel_hi:[1,0]\n\t"
#define KC(j) "v_pk_mul_f32 " RNG("v", KX(j), 2) ", " RNG("v", KX(j) + 2, 2) ", " RNG("v", 66 + 4 * (j), 2) " op_sel_hi:[1,0]\n\t" \
  "v_pk_fma_f32 " RNG("v", KX(j), 2) ", " RNG("v", KX(j) + 2, 2) ", " RNG("v", 66 + 4 * (j), 2) ", " RNG("v", KX(j), 2) " op_sel:[1,1,0] op_sel_hi:[0,1,1] neg_hi:[1,0,0]\n\t"
#define KD(t, j, r) "v_cvt_pk_bf16_f32 " REG(KH(t) + (j)) ", " REG(r) ", " REG((r) + 1) "\n\t" \
  "v_dot2c_f32_bf16 " REG(r) ", s47, " REG(KH(t) + (j)) "\n\t" "v_dot2c_f32_bf16 " REG((r) + 1) ", s48, " REG(KH(t) + (j)) "\n\t"
#define KL(t, j, r) "v_cvt_pk_bf16_f32 " REG(KH(t) + 4 + (j)) ", " REG(r) ", " REG((r) + 1) "\n\t"
#define KDM "s_add_u32 m0, m0, 1024\n\t" "s_add_u32 s43, s43, s42\n\t" "buffer_load_dwordx4 v110, s[36:39], s43 offen lds\n\t"
#define KP(j) "ds_read_b128 " RNG("v", 64 + 4 * (j), 4) ", v111 offset:" STR(16 * (j)) "\n\t"
#define KV(j, L, A) KE(j, "-") L KM(j) KC(j) A KD(3, j, KX(j))
#define KU(t, j, L, A) KE(j, "") L KM(j) A KD(t, j, KX(j) + 2)
#define KSUB                                                                                                        \
  KV(0, WLG(8) "s_add_u32 m0, m0, 1024\n\t", KA(3, 1)) KV(1, KL(3, 0, KX(0)), KA(3, 2))                              \
  "s_xor_b32 s43, s40, s41\n\t" "buffer_load_dwordx4 v110, s[36:39], s43 offen lds\n\t" KV(2, KL(3, 1, KX(1)), KA(3, 3)) \
  KV(3, KL(3, 2, KX(2)), KA(0, 0))                                                                                  \
  KU(0, 0, KL(3, 3, KX(3)), KA(0, 1)) KU(0, 1, KL(0, 0, KX(0) + 2), KA(0, 2)) KDM KU(0, 2, KL(0, 1, KX(1) + 2), KA(0, 3)) \
  KU(0, 3, KL(0, 2, KX(2) + 2), KA(1, 0)) KF(0)                                                                     \
  KU(1, 0, KL(0, 3, KX(3) + 2), KA(1, 1)) KF(1) KMF(0, 1, 0) KU(1, 1, KL(1, 0, KX(0) + 2), KA(1, 2)) KF(2) KDM      \
  KMF(0, 0, 1) KU(1, 2, KL(1, 1, KX(1) + 2), KA(1, 3)) KF(3) KMF(0, 0, 0) KU(1, 3, KL(1, 2, KX(2) + 2), KA(2, 0)) KF(0) KP(0) \
  KU(2, 0, KL(1, 3, KX(3) + 2), KA(2, 1)) KF(1) KP(1) KMF(1, 1, 0) KU(2, 1, KL(2, 0, KX(0) + 2), KA(2, 2)) KF(2) KP(2) KDM \
  KMF(1, 0, 1) KU(2, 2, KL(2, 1, KX(1) + 2), KA(2, 3)) KF(3) KP(3) KMF(1, 0, 0) KU(2, 3, KL(2, 2, KX(2) + 2), WLG(3) KA(3, 0)) \
  KMF(2, 0, 1) "v_add_u32 v111, 256, v111\n\t" "s_lshl2_add_u32 s41, s42, s41\n\t" KL(2, 3, KX(3) + 2)
#define KZERO ZER(80) ZER(84) ZER(88)
#define KCHUNK(PTR, VO, SO, ST, BUF, PF) "s_mov_b64 s[36:37], " PTR "\n\t" "s_mov_b32 m0, %[sc]\n\t" "v_mov_b32 v110, " VO "\n\t" SO "s_mov_b32 s41, 0\n\t" "s_mov_b32 s42, " STR(ST) "\n\t" \
  "buffer_load_dword " PF ", s[36:39], 0 offen lds\n\t" "s_add_u32 m0, %[sl], " STR((BUF) * 16384 - 1024) "\n\t"
#define KCALL "s_call_b64 s[44:45], .Lkg%=\n\t" WVM(16) KMF(2, 0, 0)
#define KTR(F, d, A, o) "ds_read_b64_tr_b16 " RNG(F, d, 2) ", " A " offset:" STR(o) "\n\t"
#define KTQ1(d, o) KTR("a", d, "%[aq]", o) KTR("a", (d) + 2, "%[aq]", (o) + 128) KTR("a", (d) + 4, "%[aq]", (o) + 2048) KTR("a", (d) + 6, "%[aq]", (o) + 2176)
#define KTQ(d, o) KCALL KTQ1(d, o) KMF(2, 1, 0) KTQ1((d) + 8, (o) + 32)
#define KTO1(d, o) KTR("v", d, "%[ao]", o) KTR("v", (d) + 2, "%[ao]", (o) + 64) KTR("v", (d) + 4, "%[ao]", (o) + 1024) KTR("v", (d) + 6, "%[ao]", (o) + 1088)
#define KTO(d, o) KCALL KTO1(d, o) KMF(2, 1, 0) KTO1((d) + 8, (o) + 2048)
#define KOUT(o) "s_add_u32 s43, %[ko], " STR(o) "\n\t"                                                              \
  "buffer_store_dwordx4 v[80:83], %[l16], %[rt], s43 offen sc1\n\t" "buffer_store_dwordx4 v[84:87], %[l16], %[rt], s43 offen offset:1024 sc1\n\t" "s_nop 3\n\t" \
  "buffer_store_dwordx4 v[88:91], %[l16], %[rt], s43 offen offset:2048 sc1\n\t"
struct SsmPtrs { const u16 *A_re, *A_im, *B_re, *B_im, *C_re, *C_im, *log_dt; };
__device__ __forceinline__ void r_role(unsigned char* lds, int b, const u16* __restrict__ mem0, const u16* __restrict__ Wq, const u16* __restrict__ Wk, const u16* __restrict__ Wv,
                                       const u16* __restrict__ Wo, int fb, u32 tag, u64 pc0, int clen, __amdgpu_buffer_rsrc_t rsx, __amdgpu_buffer_rsrc_t rsf,
                                       __amdgpu_buffer_rsrc_t rst, SsmPtrs sp, int tid, int lane, int w
                                       ) {
  const int lr = lane & 15, lg = lane >> 4;
  const int ldsb = (int)(size_t)(LAS unsigned char*)lds;
  const int idx = (lr & 2) ? 0xEEEE : 0x4444;
  u32 tcnt = 0; (void)tcnt;
  u32 cyc0 = 0, tk0 = 0;
  if (tid < 8) *(u32*)(lds + LR_CTL + tid * 4) = 0u;
  *(u32*)(lds + LR_CTL + 4096 + 256 + tid * 4) = 0u;
  const int xcd = b & 3, li = b >> 2;
  const int head = xcd * 8 + (li >> 2), ks = li & 3;
  const int vcu = head * 4 + ks;
  const int cg = (xcd >> 1) * 32 + li, rg = xcd & 1;
  const int rot1 = ((li >> 1) + w) & 3, rot3 = ((li >> 2) + 2 * w) & 7;
  const int pz1 = lane < 32 ? XR1A(lane >> 3, cg * 256 + rg * 128 + (lane & 7) * 16) : XR3A(lg & 1, vcu * 256 + lr * 16);
  const int pgo = PGO_X + head * 128;
  const int calo = XCAL + (li & 3) * 8192 + ((li >> 2) & 1) * 4096 + ((li >> 3) * 4 + xcd) * 128;
  u32 xp0;
  u32x32 wa[6], wo[4];
  {
    const __amdgpu_buffer_rsrc_t rwq = __builtin_amdgcn_make_buffer_rsrc((void*)Wq, 0, DM * HD * 2, 0x00020000);
    const int vq = (lane >> 2) * (HD * 2) + vcu * 64 + (lane & 3) * 16, sq = (256 * w + (rot1 << 6)) * (HD * 2);
    const int vo = (lane >> 1) * (DM * 2) + cg * 32 + (lane & 1) * 16, so = (1024 * w + (rot3 << 7)) * (DM * 2);
    const int sl = ldsb + LR_STG + w * 32768;
    const int ch0 = (b * 4 + w) * 2;
    u32x2 pin[6];
    u32 ldt;
    {
      const int po = (ch0 * SSM_N + 4 * lane) * 2;
      asm volatile("global_load_dwordx2 %0, %7, %9\n\tglobal_load_dwordx2 %1, %7, %10\n\tglobal_load_dwordx2 %2, %7, %11\n\tglobal_load_dwordx2 %3, %7, %12\n\t"
                   "global_load_dwordx2 %4, %7, %13\n\tglobal_load_dwordx2 %5, %7, %14\n\tglobal_load_ushort %6, %8, %15"
                   : "=&v"(pin[0]), "=&v"(pin[1]), "=&v"(pin[2]), "=&v"(pin[3]), "=&v"(pin[4]), "=&v"(pin[5]), "=&v"(ldt)
                   : "v"(po), "v"((ch0 + (lane >> 5)) * 2), "s"(sp.A_re), "s"(sp.A_im), "s"(sp.B_re), "s"(sp.B_im), "s"(sp.C_re), "s"(sp.C_im), "s"(sp.log_dt) : "memory");
    }
    u32x4 m0v;
    asm volatile("global_load_dwordx4 %0, %1, %2" : "=&v"(m0v) : "v"(((4 * rg + ((lane >> 1) & 3)) * DM + 16 * cg + 8 * (lane & 1)) * 2), "s"(mem0) : "memory");
    const u16* const wp[4] = {Wq, Wk, Wv, Wo};
    int pfo[5];
#pragma unroll
    for (int c = 0; c < 5; ++c) {
      const int lo_ = (int)(size_t)wp[c < 3 ? c : 3], s3 = (384 - lo_) & 1023, s63 = (8064 - lo_) & 8191;
      pfo[c] = c < 3 ? (lane < 16 ? (256 * w + li * 8 + (lane & 7)) * (HD * 2) + xcd * 2048 + (lane >> 3) * 1024 + s3
                                  : (lane < 24 && (s63 >> 11) == xcd ? (256 * w + li * 8 + (lane & 7)) * (HD * 2) + s63 : OOR))
                     : (lane < 16 ? (1024 * w + li * 16 + lane + (((c - 3) * 512) ^ ((((s3 >> 7) + 2 * w) & 4) << 7))) * (DM * 2) + (xcd >> 1) * 1024 + s3 : OOR);
    }
    const int scr = ldsb + LR_RED + 1024 + w * 256;
    asm volatile("s_mov_b32 m0, %0\n\ts_nop 3\n\tbuffer_load_dword %1, %2, 0 offen lds" :: "s"(scr), "v"(pfo[0]), "s"(rwq) : "memory");
#define KP0 16
#pragma unroll
    for (int i = 0; i < KP0; ++i)
      asm volatile("s_mov_b32 m0, %0\n\ts_nop 3\n\tbuffer_load_dwordx4 %1, %2, %3 offen lds" :: "s"(sl + i * 1024), "v"(vq), "s"(rwq), "s"(sq ^ (i * 16 * HD * 2)) : "memory");
    asm volatile("s_waitcnt vmcnt(1+" STR(KP0) ")" : "+v"(pin[0]), "+v"(pin[1]), "+v"(pin[2]), "+v"(pin[3]), "+v"(pin[4]), "+v"(pin[5]), "+v"(ldt), "+v"(m0v)::"memory");
#define PZE 4
    auto ring_pz = [&]() {
      const u32x4 pz = (u32x4){SENT, SENT, SENT, SENT};
      if (w < 3) {
        u32x4 v = pz;
        if (w == 0 && lane < 32) v = (u32x4){nosent(m0v[0]), nosent(m0v[1]), nosent(m0v[2]), nosent(m0v[3])};
#pragma unroll
        for (int hf = 0; hf < 2; ++hf) {
          __builtin_amdgcn_raw_buffer_store_b128(v, rsx, pz1, XSB(w) + hf * 8192, 16);
          __builtin_amdgcn_raw_buffer_store_b128(pz, rsx, XR2(vcu) + lane * 16, XR2B + (w & 1) * XR2S + hf * 8192, 16);
          __builtin_amdgcn_raw_buffer_store_b128(pz, rsx, lane < 16 ? XR2(vcu) + 1024 + lane * 16 : OOR, XR2B + (w & 1) * XR2S + hf * 8192, 16);
        }
      } else {
        __builtin_amdgcn_raw_buffer_store_b128(pz, rsx, lane < 8 ? lane * 16 : OOR, calo, 16);
        if (b < 32) {
          __builtin_amdgcn_raw_buffer_store_b128(pz, rsx, lane * 16, XSYN + b * 2048, 16);
          __builtin_amdgcn_raw_buffer_store_b128(pz, rsx, lane * 16, XSYN + b * 2048 + 1024, 16);
        } else if (b >= 48 && b < 48 + 41) {
          __builtin_amdgcn_raw_buffer_store_b128(pz, rsx, lane * 16, PGO_D + (b - 48) * 1024, 16);
        }
      }
      asm volatile("" ::: "memory");
    };
    auto late_pz = [&]() {
      const u32x4 pz = (u32x4){SENT, SENT, SENT, SENT};
#pragma unroll
      for (int i = XS0; i < NSEG; i += 4) __builtin_amdgcn_raw_buffer_store_b128(pz, rsf, (lane < 32 ? FO_KM : FO_VM - 512) + vcu * 512 + lane * 16, (i + w) * EXK, 16);
      if (w < 2) __builtin_amdgcn_raw_buffer_store_b128(pz, rsf, lane * 16, FO_O + (NSEG - 1) * 262144 + (vcu * 2 + w) * 1024, 16);
    };
    {
      if (PZE == 1 || PZE == 2) late_pz();
      if (PZE == 2) ring_pz();
      asm volatile("" ::: "memory");
    }
    f32x4* prm = (f32x4*)(lds + w * 4096);
    {
      const float dt = rbf(expf(bf2f((u16)ldt)));
#pragma unroll
      for (int i = 0; i < 4; ++i) {
        float v[6];
#pragma unroll
        for (int j = 0; j < 6; ++j) v[j] = __uint_as_float((i & 1) ? (pin[j][i >> 1] & 0xffff0000u) : (pin[j][i >> 1] << 16));
        const float are = dt * v[0], aim = dt * v[1], br = dt * v[2], bi = dt * v[3];
        const float cr = v[4] * br - v[5] * bi, ci = v[4] * bi + v[5] * br;
        const float rev = aim * 0.15915494309189535f;
        prm[4 * lane + i] = (f32x4){are * 1.4426950408889634f, rev - __builtin_rintf(rev), cr, ci};
      }
    }
#pragma unroll
    for (int i = KP0; i < 16; ++i)
      asm volatile("s_mov_b32 m0, %0\n\ts_nop 3\n\tbuffer_load_dwordx4 %1, %2, %3 offen lds" :: "s"(sl + i * 1024), "v"(vq), "s"(rwq), "s"(sq ^ (i * 16 * HD * 2)) : "memory");
    const f32x4 lv = {(float)(128 * (lr >> 3) + (lr & 7)), (float)(lr < 8 ? 256 + 128 * (lr >> 2) + 2 * (lr & 3) : 512 + 128 * ((lr - 8) >> 1) + 4 * (lr & 1)),
                         (float)(lr <= 8 ? 1024 + 128 * lr : 0), (float)(8 * lr)};
    const int trow = 16 * (lg >> 1) + 4 * (lg & 1) + ((lr >> 2) & 1) + 8 * (lr >> 3);
    asm volatile(
        "s_nop 4\n\t" "s_mov_b32 s46, m0\n\t" "s_mov_b32 s38, " STR(DM * HD * 2) "\n\t" "s_mov_b32 s39, 0x20000\n\t" "s_mov_b32 s47, 0xbf80\n\t" "s_mov_b32 s48, 0xbf800000\n\t"
        WLG(0)
        "ds_read_b128 v[64:67], %[pa]\n\t" "ds_read_b128 v[68:71], %[pa] offset:16\n\t" "ds_read_b128 v[72:75], %[pa] offset:32\n\t" "ds_read_b128 v[76:79], %[pa] offset:48\n\t"
        "v_add_u32 v111, 256, %[pa]\n\t" KZERO
        KCHUNK("%[wk]", "%[vq]", "s_mov_b32 s40, %[sq]\n\t", 16 * HD * 2, 1, "%[p1]")
        WLG(0) KA(3, 0)
        KTQ(0, 0) KTQ(32, 4096) KTQ(64, 8192) KTQ(96, 12288)
        KCHUNK("%[wv]", "%[vq]", "", 16 * HD * 2, 0, "%[p2]")
        KTQ(16, 16384) KTQ(48, 16384 + 4096) KTQ(80, 16384 + 8192) KTQ(112, 16384 + 12288)
        KOUT(0)
        KCHUNK("%[wo]", "%[vo]", "s_mov_b32 s40, %[so]\n\t", 32 * DM * 2, 1, "%[p3]")
        KZERO
        KTQ(128, 0) KTQ(144, 4096) KTQ(160, 8192) KTQ(176, 12288)
        KCHUNK("%[wo]", "%[vo]", "s_xor_b32 s40, s40, " STR(512 * DM * 2) "\n\t", 32 * DM * 2, 0, "%[p4]")
        KTO(128, 16384) KTO(144, 16384 + 4096) KTO(160, 16384 + 8192) KTO(176, 16384 + 12288)
        KOUT(3072)
        "s_branch .Lke%=\n"
        ".Lkg%=:\n\t"
        KSUB
        "s_setpc_b64 s[44:45]\n"
        ".Lke%=:\n\t"
        WLG(0) "s_mov_b32 m0, s46"
        : "=&{a[0:31]}"(wa[0]), "=&{a[32:63]}"(wa[1]), "=&{a[64:95]}"(wa[2]), "=&{a[96:127]}"(wa[3]), "=&{a[128:159]}"(wa[4]), "=&{a[160:191]}"(wa[5]),
          "=&{v[128:159]}"(wo[0]), "=&{v[160:191]}"(wo[1])
        : "{v[112:115]}"(lv), [pa] "v"(ldsb + w * 4096 + lg * 64), [vq] "v"(vq), [vo] "v"(vo),
          [p1] "v"(pfo[1]), [p2] "v"(pfo[2]), [p3] "v"(pfo[3]), [p4] "v"(pfo[4]), [aq] "v"(sl + trow * 64 + (lr & 3) * 8), [ao] "v"(sl + trow * 32 + (lr & 3) * 8), [l16] "v"(lane * 16),
          [wk] "s"(Wk), [wv] "s"(Wv), [wo] "s"(Wo), [sq] "s"(sq), [so] "s"(so), [sl] "s"(sl), [sc] "s"(scr), [rt] "s"(rst), [ko] "s"(2 * SSM_L * SSM_D * 2 + ch0 * 3072)
        : CL64, CL72, CL80, CL88, CL96, CL8(192, 193, 194, 195, 196, 197, 198, 199), CL8(200, 201, 202, 203, 204, 205, 206, 207), CL8(208, 209, 210, 211, 212, 213, 214, 215),
          CL8(216, 217, 218, 219, 220, 221, 222, 223), "s36", "s37", "s38", "s39", "s40", "s41", "s42", "s43", "s44", "s45", "s46", "s47", "s48", "scc", "memory");
    if (PZE == 0) late_pz();
    if (PZE != 2) ring_pz();
    xp0 = __builtin_amdgcn_raw_buffer_load_b32(rsx, (lane & 1) * 4096, pgo, 16);
    clk_edge(cyc0, tk0);
    asm volatile("s_waitcnt vmcnt(0)\n\ts_barrier" : "+v"(xp0)::"memory");
#define CLKF() clk_rate(cyc0, tk0)
    if (w == 0) {
      const int l0 = lane ? OOR : 0;
      const u64 c0 = __builtin_amdgcn_s_memtime();
      u32 v;
      asm volatile("s_waitcnt lgkmcnt(0)\n\tbuffer_load_dword %0, off, %1, %2 sc1\n\tbuffer_store_dword %3, %4, %1, %5 offen sc1\n\ts_waitcnt vmcnt(1)"
                   : "=&v"(v) : "s"(rsx), "s"(calo), "v"(tag), "v"(l0), "s"(PGO_Y + b * 128), "s"(c0) : "memory");
      const u32 lat = (u32)(__builtin_amdgcn_s_memtime() - c0);
      const float f = CLKF();
      typedef __attribute__((ext_vector_type(3))) unsigned int u32x3;
      __builtin_amdgcn_raw_buffer_store_b96((u32x3){__float_as_uint(f), lat, tag}, rsx, l0, PGO_Y + b * 128 + 4, 16);
      asm volatile("" ::: "memory");
    }
    {
      u32 r[1][8][8];
      tr_chunk<1, 8>((const u16*)(lds + LR_STG + w * 32768), lr, lg, r);
#pragma unroll
      for (int i = 0; i < 8; ++i)
#pragma unroll
        for (int e = 0; e < 8; ++e) { const int o = (8 + i) * 8 + e; wo[o >> 5][o & 31] = r[0][i][e]; }
      asm volatile("s_waitcnt lgkmcnt(0)" ::: "memory");
    }
    if (PZE == 4) late_pz();
    asm volatile("" ::: "memory");
  }
  const int hf = (lr >> 1) & 1;
  const int voff1 = lg * 256 + (lr >> 3) * 128 + (((lr >> 2) & 1) * 2 + (lr & 1)) * 32 + hf * 16;
  const int voff3 = (lr >> 3) * 512 + (lg >> 1) * 256 + (((lr >> 2) & 1) * 2 + (lr & 1)) * 16 + (lg & 1) * 128 + hf * 64;
  const int ard = ldsb + (2 * (lr >> 3) + (lg >> 1)) * 640 + (lr & 1) * 272 + ((lr >> 1) & 3) * 64 + (lg & 1) * 32;
  const int ctl = ldsb + LR_CTL, one = w == 0 ? 2 : (w == 3 ? 4 : 1);
  const int r8 = lane & 7, hk = (lane >> 3) & 1;
#define COA 1
#define COB 1
  const int st01 = lr < 8 ? XR2(vcu) + lr * 16 + lg * 128 : OOR, st2 = (lr < 8 && lg >= 2) ? XR2(vcu) + 1024 + lr * 16 + (lg - 2) * 128 : OOR;
  const int ld01 = hk * 512 + r8 * 16 + lg * 128, ld2 = XR2(head * 4 + lg) + 1024 + r8 * 16 + hk * 128;
  int sto = XR3A(r8 >> 2, vcu * 256 + (r8 & 3) * 16 + lg * 64 + hk * 8);
  const int stk = lr >= 8 ? (lg * 8 + (lr - 8)) * 16 : OOR;
  const int lo = X4C ? 2 * (lr & 7) + (lr >> 3) : lr;
  const int arw = ldsb + LR_CTL + lane * 4, arr = ldsb + 3840 + ((lo >> 3) + 2 * ((lo >> 2) & 1)) * 64 + (lo & 3) * 16;
  const int st1 = XR1A(lg, cg * 256 + rg * 128 + lo * 8);
  const int avw = 0;
  const int avc = ldsb + 2560 + (lr >> 3) * 640 + (8 * ((lr >> 2) & 1) + (lr & 3)) * 4;
  const int avr = avc + (lg & 1) * 272 + (lg >> 1) * 64, avf = avc + (lg & 1) * 128 + (lg >> 1) * 16;
  u32x16 kc = {(u32)voff1, (u32)voff3, (u32)idx, (u32)TEX(lane), (u32)(lane < 16 ? XR2(vcu) + 1024 + lane * 16 : OOR), (u32)ard, (u32)ctl, (u32)one, (u32)st01, (u32)st2, (u32)(XR2(vcu) + lane * 16), (u32)ld2, (u32)(lane * 16), (u32)arw, (u32)arr, (u32)stk};
  int o1c = XR1A(xcd, w * 4096) + rot1 * 1024, o3c = XR3A(rg, w * 8192) + rot3 * 1024;
  const int fok = FO_K + (head * 8 + ks) * 1024, fov = FO_V + (head * 8 + 2 * ks) * 1024;
#define XPROG(v) min(xprog(__builtin_amdgcn_readlane(v, 0), tag), xprog(__builtin_amdgcn_readlane(v, 1), tag))
  int xp = w == 3 ? XPROG(xp0) : 1000;
#define XWAIT(n) for (int it_ = 0; it_ < SPIN_CAP && xp < (n); ++it_) {                                               \
    if (it_) asm volatile("s_sleep 4" ::: "memory");                                                                  \
    const u32 v_ = __builtin_amdgcn_raw_buffer_load_b32(rsx, (lane & 1) * 4096, pgo, 16); xp = XPROG(v_); asm volatile("" ::: "memory"); }
  XWAIT(1)
  u32x16 fv;
  u32x8 fk;
  {
    const int lo_ = w == 3 ? lane * 16 : OOR;
    const u32x4 v0 = __builtin_amdgcn_raw_buffer_load_b128(rsf, lo_, fov, 16), v1 = __builtin_amdgcn_raw_buffer_load_b128(rsf, lo_, fov + 1024, 16);
    const u32x4 k0 = __builtin_amdgcn_raw_buffer_load_b128(rsf, lo_, fok, 16), k1 = __builtin_amdgcn_raw_buffer_load_b128(rsf, lo_, fok + 4096, 16);
    fv = (u32x16){v0[0], v0[1], 0u, 0u, v0[2], v0[3], 0u, 0u, v1[0], v1[1], 0u, 0u, v1[2], v1[3], 0u, 0u};
    fk = (u32x8){k0[0], k0[1], k0[2], k0[3], k1[0], k1[1], k1[2], k1[3]};
    asm volatile("" : "+{a[192:207]}"(fv), "+{a[208:215]}"(fk));
  }
#define XPN 1
#define CALM 40
  XWAIT(2)
#pragma unroll
  for (int i = 0; i < 3; ++i) *(u32x4*)(lds + w * 4096 + i * 1024 + lane * 16) = (u32x4){SENT, SENT, SENT, SENT};
  {
    u64 pc; asm volatile("s_getpc_b64 %0" : "=s"(pc));
    const u32 cv = __builtin_amdgcn_raw_buffer_load_b32(__builtin_amdgcn_make_buffer_rsrc((void*)pc0, 0, clen, 0x00020000), (int)(pc - pc0) + tid * 64, 0, 0);
    asm volatile("s_waitcnt vmcnt(0)" :: "v"(cv) : "memory");
  }
  if (w == 3) { const u32 v_ = __builtin_amdgcn_raw_buffer_load_b32(rsx, (lane & 1) * 4096, pgo, 16); xp = max(xp, XPROG(v_)); }
  u32x4 st;
  if (w == 1) {
    if (b == 0) {
      u32 tt = 0, best = 0;
      bool xok = false;
      const int sto_ = lane < 5 ? (lane ? XSTR(lane - 1) : XSTA) : OOR;
      for (int it = 0; it < SPIN_CAP; ++it) {
        asm volatile("" ::: "memory");
        const u32x4 r0 = __builtin_amdgcn_raw_buffer_load_b128(rsx, lane * 128, PGO_Y, 16), r1 = __builtin_amdgcn_raw_buffer_load_b128(rsx, lane * 128 + 8192, PGO_Y, 16);
        if (!xok) xok = __ballot(xprog(__builtin_amdgcn_raw_buffer_load_b32(rsx, lane * 128, PGO_X, 16), tag) < XPN) == 0;
        const bool c0 = r0[3] == tag, c1 = r1[3] == tag;
        if (tt == 0 && __popcll(__ballot(!c0 || !c1)) <= CALM) {
          float fm = (c0 ? __uint_as_float(r0[1]) : 0.f) + (c1 ? __uint_as_float(r1[1]) : 0.f), fn = (c0 ? 1.f : 0.f) + (c1 ? 1.f : 0.f);
#pragma unroll
          for (int o = 4; o < 64; o *= 2) { fm += __shfl_xor(fm, o); fn += __shfl_xor(fn, o); }
          fm = fn > 0.f ? fm / fn : 100.f;
          const float f0 = __uint_as_float(__builtin_amdgcn_readfirstlane(__float_as_uint(fm)));
          fm = fminf(fm, __shfl_xor(fm, 1)); fm = fminf(fm, __shfl_xor(fm, 2));
          const float f_ = CLKF() * fminf(fmaxf(fm / f0, 0.8f), 1.f);
          const float k_ = 1.f + TKS * (fminf(fmaxf(23.9f / f_, 1.f), 1.5f) - 1.f);
          const int nb_ = (int)(TDB * k_ + 0.5f), nc_ = nb_ + (int)((TDC - TDB) * k_ + 0.5f), np_ = nc_ + (int)((TP - TDC) * k_ + 0.5f);
          u32 lat = min(c0 ? r0[2] : 65535u, c1 ? r1[2] : 65535u);
          { auto r = __builtin_amdgcn_permlane16_swap(lat, lat, false, false); lat = min(r[0], r[1]); }
          { auto r = __builtin_amdgcn_permlane32_swap(lat, lat, false, false); lat = min(r[0], r[1]); }
          u32 mm[4];
#pragma unroll
          for (int k = 0; k < 4; ++k)
            mm[k] = __builtin_amdgcn_readlane(lat, 4 * k) + __builtin_amdgcn_readlane(lat, 4 * k + 2) + __builtin_amdgcn_readlane(lat, 4 * (k ^ 2) + 1) + __builtin_amdgcn_readlane(lat, 4 * (k ^ 2) + 3);
          const u32 hb = mm[0] + mm[2] <= mm[1] + mm[3] ? 0u : 1u;
          best = (hb << 13) | ((hb ? mm[1] <= mm[3] : mm[0] <= mm[2]) ? CLS0 : 16384u - CLS0);
          if (__ballot(lat == 65535u) == 0) tt = __builtin_amdgcn_readfirstlane(nb_ | (nc_ << 10) | (np_ << 20));
        }
        if (__ballot(r0[0] != tag || r1[0] != tag) == 0 && xok && tt) break;
      }
      st = (u32x4){rt32() + TD0, tt, best, tag};
      __builtin_amdgcn_raw_buffer_store_b128(st, rsx, sto_, 0, 16);
    } else st = wait_start(rsx, fb, tag, false, xcd);
    if (lane == 0) *(u32x4*)(lds + LR_CTL + 32) = st;
  }
  LBAR();
  st = *(const u32x4*)(lds + LR_CTL + 32);
  const int flip = __builtin_amdgcn_readfirstlane(st[2]) & 8192, cls = __builtin_amdgcn_readfirstlane(st[2]) & 16384;
  o1c ^= cls; o3c ^= cls; sto ^= cls; kc[4] ^= cls; kc[8] ^= cls; kc[9] ^= cls; kc[10] ^= cls; kc[11] ^= cls;
  { const u32 now = rt32(); if (st[0] - now + 100000u > 200000u) st[0] = now; }
  u32x16 kd = {(u32)((ld01 + XR2(vcu ^ 1)) ^ cls), (u32)((ld01 + XR2(vcu ^ 2)) ^ cls), (u32)((ld01 + XR2(vcu ^ 3)) ^ cls), (u32)(X4B && hk ? OOR : sto), (u32)avr, (u32)(avr + 4096), (u32)(avr + 8192),
               (u32)(avr + 12288), (u32)avw, (u32)(X4C && hk ? OOR : st1 ^ cls), (u32)((lane & 1) * 4096), (u32)(XSB(lane + 1) + flip), (u32)(lane * 8), (u32)avf,
               (u32)(b == 0 && lane == 0 ? XSYN + flip : OOR), (u32)(w == 0 ? pz1 ^ cls : (w == 1 ? kc[10] : kc[4]))};
  u32x16 sa = {(u32)flip, (u32)(XSB(1) + flip), 0u, (u32)fok, (u32)fov, (u32)(FO_KM + (head * 4 + ks) * 512),
               (u32)(FO_VM + (head * 8 + 2 * ks) * 256), st[0], 0u, 0u, (u32)SYO(1), 0u, 0u, (u32)TRY_CAP, 0u, 0u};
  u32x8 sc = {(u32)o1c, (u32)o3c, SENT, 0u, 0u, 0u, st[1], (u32)pgo};
#define TUNEO
#define KCNT
#define KHST
#define KTRC
#define TRCI
#define PREP 16
#define GAP 12
#define RETA(n, L) "s_cmp_eq_u32 s49, " #n "\n\t" "s_cmov_b32 s48, .Lpc%=-" L "\n\t"
  u32x32 keep;
  u32x2 ov;
  asm volatile("" : "={v[60:91]}"(keep));
  while (true) {
    sa[14] = xp;
#pragma unroll
    for (int i = 0; i < 16; ++i) sa[i] = __builtin_amdgcn_readfirstlane(sa[i]);
#pragma unroll
    for (int i = 0; i < 8; ++i) sc[i] = __builtin_amdgcn_readfirstlane(sc[i]);
    asm volatile(
        "s_nop 4\n\t" "s_mov_b32 s60, m0\n\t"
        "s_mov_b32 m0, %[mb]\n\t" "s_mov_b32 s64, s50\n\t" "s_mov_b32 s98, s49\n\t" "s_mov_b32 s68, 0\n\t"
        EXTRA R2BASE
        "s_cmp_lg_u32 %[wv], 3\n\t" "s_cbranch_scc1 .LN%=\n\t"
        "s_mov_b32 s78, 0xff00ff00\n\t" "s_mov_b32 s79, 0xff00ff00\n\t" "s_mov_b32 s62, 0x3e0293ee\n\t"
        "v_mov_b32 v52, 0x3f803f80\n\t" "v_mov_b32 v53, 0x3f803f80\n\t" "v_mov_b32 v54, 0x3f803f80\n\t" "v_mov_b32 v55, 0x3f803f80\n\t" "v_mov_b32 v51, 0\n\t" "v_mov_b32 v59, 0\n\t"
        "v_mov_b32 v60, %[tg]\n\t" "v_mov_b32 v61, -1\n\t" "s_mov_b32 s63, %[dm]\n\t"
        "s_branch .Lent%=\n"
        ".LA%=:\n\t"
        CLKW(".Lta%=", "0") REL(".Lua%=")
        "s_add_u32 s50, s36, s52\n\t" NOP(PDA)
        ABODY(NOP(PCL) LASTA(".Lwa%="), WVM(5) "v_readlane_b32 s50, v60, 0\n\t" "v_readlane_b32 s51, v60, 1\n\t"
              "s_sub_u32 s50, s50, %[tg]\n\t" "s_sub_u32 s51, s51, %[tg]\n\t" "s_min_u32 s63, s50, s51\n\t" "s_max_u32 s50, s50, s51\n\t" "s_cmp_le_u32 s50, 80\n\t"
              "s_cselect_b32 s50, s63, 0\n\t" "v_readfirstlane_b32 s49, v61\n\t" "s_max_i32 s64, s64, s50\n\t"
              "s_cmp_lg_u32 s49, -1\n\t" "s_cmov_b32 s58, s49\n\t" EXTRA "v_mov_b32 v62, s58\n\t" "ds_write_b32 v38, v62 offset:24\n\t", ".Lfail%=", 0) TSA(3)
        QKRED(BARA) ORD(88, 89) ORD(90, 91) QKPUB TSA(6) TSB(7) REDO(".Lqk%=")
        SVP(0, 0) SHRD SVP(1, 0) SVP(0, 1) SVP(1, 1) SVP(0, 2) SVP(1, 2) WLG(0) "v_readfirstlane_b32 s49, v62\n\t" SVP(0, 3) SVP(1, 3) "s_max_u32 s44, s44, s49\n\t" "s_cmp_eq_u32 s45, 63\n\t" TSA(7)
        "s_cbranch_scc1 .Llast%=\n"
        ".LB%=:\n\t"
        CLKW(".Ltb%=", "s66")
        "s_mov_b32 s47, 1\n\t"
        R2TEXT
        ".LC%=:\n\t"
        ZALL
        "s_mov_b32 s47, 5\n\t" "s_sub_i32 s63, s64, s45\n\t" "s_cmp_lt_i32 s63, 2\n\t" "s_cbranch_scc1 .Lend%=\n\t"
        "s_cmp_lt_i32 s63, " STR(XTH) "\n\t" "s_cselect_b32 s63, s59, %[dm]\n\t"
        CLKW(".Ltc%=", "s67") REL(".Luc%=")
        "s_add_u32 s50, s36, s53\n\t" NOP(PDC)
        CISSUE(LASTC(".Lwc%="), "")
        "buffer_load_dword v61, off, %[rs], s46 sc1\n\t" "s_add_u32 s46, s46, 64\n\t"
        "s_add_u32 s49, s39, 4096\n\t" NOP(PDF)
        "buffer_load_dwordx4 a[208:211], v44, %[rf], s39 offen sc1\n\t" "buffer_load_dwordx4 a[212:215], v44, %[rf], s49 offen sc1\n\t"
        "buffer_load_dwordx2 a[192:193], v44, %[rf], s40 offen sc1\n\t" "buffer_load_dwordx2 a[196:197], v44, %[rf], s40 offen offset:8 sc1\n\t"
        "buffer_load_dwordx2 a[200:201], v44, %[rf], s40 offen offset:1024 sc1\n\t" "buffer_load_dwordx2 a[204:205], v44, %[rf], s40 offen offset:1032 sc1\n\t"
#define NXC 8
        "buffer_load_dword v60, v18, %[rs], s63 offen sc1\n\t"
        CBODY(NXC, "", NXC, ".Lfail%=")
        MRED(BARC, "") ORD(84, 85) ORD(86, 87) STC TSW(6) REDO(".Lmr%=")
        SHRD SHUP TSW(7)
        NEXTP
        CWAIT(".Lqk%=", 8, "s48") QKRED("") NOP(1) QKPUB "s_branch .Lqk%=r\n"
        CWAIT(".Lmr%=", 16, "s38") WVM(0) MRED("", "") "v_min_u32 v80, -2, v80\n\t" "v_min_u32 v81, -2, v81\n\t" STC
        "s_branch .Lmr%=r\n"
        ".Llast%=:\n\t"
        "buffer_store_dwordx4 v[72:75], v47, %[rf], s41 offen sc1\n\t" "s_mov_b32 s47, 3\n\t" NOP(4) VPOST ARRIVE(12) "s_barrier\n\t"
        "s_branch .Lend%=\n"
        R2MAX
        ".Lnorm%=:\n\t"
        "s_cmp_lg_u32 %[wv], 3\n\t" "s_cbranch_scc1 .LNA%=\n\t" "s_cmp_eq_u32 s47, 0\n\t" "s_cbranch_scc1 .LA%=\n\t" "s_branch .LC%=\n"
        ".LN%=:\n\t"
        "v_mov_b64 v[92:93], -1\n\t" "v_mov_b64 v[94:95], -1\n\t" "s_mov_b32 s65, s44\n\t" "s_mov_b32 s78, -100\n\t" "s_mov_b32 s79, 4\n\t"
        "s_branch .Lent%=\n"
        ".LNA%=:\n\t" PZA0
        "s_add_u32 s50, s36, s52\n\t"
        RELN(".Lna%=")
        ABODY(NOP(PCA), LASTA(".Lxa%=") PZA, ".Lfail%=", PZNA) BARA ARRIVE(8) TSA(3)
        SV(0, 0) SV(1, 0) SV(0, 1) SV(1, 1) SV(0, 2) SV(1, 2) SV(0, 3) SV(1, 3) ZER(64) ZER(68) ZER(72) ZER(76)
        VPOST TSA(7) ZER(80) ZER(84) ZER(24) ZER(28)
        "v_add_u32 v88, 4096, v21\n\t" "v_add_u32 v89, 8192, v21\n\t" "v_add_u32 v90, 12288, v21\n\t"
        PZB0
        "s_lshl_b32 s61, s45, 7\n\t" "s_add_u32 s61, s61, " STR(PGO_R - XSYN) "\n\t" "s_cmp_lg_u32 %[wv], 0\n\t"
        WLG(0) "s_barrier\n\t" PZB
        PSW(92, 0) PSW(92, 272) PSW(92, 640) PSW(92, 912) PSW(92, 1280) PSW(92, 1552) PSW(92, 1920) PSW(92, 2192) PSW(92, 3840)
        "ds_read2_b32 v[50:51], v38 offset0:0 offset1:6\n\t"
        "s_cbranch_scc1 .LNB%=\n\t"
        "s_memrealtime s[72:73]\n"
        ".Lv0%=:\n\t"
        "ds_read_b32 v62, v38 offset:" CNT(12) "\n\t"
        "ds_read2_b32 v[112:113], v21 offset0:0 offset1:68\n\t" "ds_read2_b32 v[114:115], v21 offset0:16 offset1:84\n\t"
        "ds_read2_b32 v[116:117], v88 offset0:0 offset1:68\n\t" "ds_read2_b32 v[118:119], v88 offset0:16 offset1:84\n\t"
        "ds_read2_b32 v[120:121], v89 offset0:0 offset1:68\n\t" "ds_read2_b32 v[122:123], v89 offset0:16 offset1:84\n\t"
        "ds_read2_b32 v[124:125], v90 offset0:0 offset1:68\n\t" "ds_read2_b32 v[126:127], v90 offset0:16 offset1:84\n\t"
        WLG(0) "v_cmp_gt_u32 vcc, s48, v62\n\t" "s_cbranch_vccnz .Lv0%=\n\t"
        "s_add_u32 s50, s72, " STR(TVM) "\n\t" "s_lshl_b32 s50, s50, 17\n\t" "s_lshr_b32 s50, s50, 1\n\t"
        PKA(112, 112, 116) PKA(114, 114, 118) PKA(120, 120, 124) PKA(122, 122, 126) PKA(112, 112, 120) PKA(114, 114, 122)
        "v_lshrrev_b32 v91, 20, v51\n\t" "v_readfirstlane_b32 s49, v50\n\t"
        "v_cvt_pk_bf16_f32 v112, v112, v113\n\t" "v_cvt_pk_bf16_f32 v113, v114, v115\n\t" "v_or_b32 v114, s50, v91\n\t"
        "s_cmp_eq_u32 s45, 63\n\t" "s_cbranch_scc1 .Lv2%=\n\t"
        "s_sleep " STR(SLV) "\n"
        ".Lv2%=:\n\t"
        "buffer_store_dwordx2 v[112:113], v20, %[rf], s42 offen sc1\n\t"
        "buffer_store_dword v114, v22, %[rs], s61 offen sc1\n\t"
        "s_cmp_eq_u32 s49, s65\n\t" "s_cbranch_scc1 .LNB%=\n\t"
        "s_mov_b32 s65, s49\n\t" KHST "s_sub_u32 s49, s45, s78\n\t" "s_mov_b32 s78, s45\n\t"
        "s_cmp_gt_i32 s49, " STR(GAP) "\n\t" "s_cbranch_scc1 .LNB%=\n\t" "s_cmp_lt_u32 s45, s79\n\t" "s_cbranch_scc1 .LNB%=\n\t"
        "s_add_u32 s79, s45, 3\n\t" "s_lshl_b32 s49, s45, 6\n\t" "v_add_u32 v50, " STR(TLB + ((TLB + TLC) << 10) + ((TLB + TLC + TLP) << 20)) ", v51\n\t"
        "buffer_store_dword v50, v22, %[rs], s49 offen offset:128 sc1\n"
        ".LNB%=:\n\t"
        WLG(0) "v_readfirstlane_b32 s58, v51\n\t" EXTRA
        "s_mov_b32 s47, 3\n\t"
        "s_cmp_eq_u32 s45, 63\n\t"
        "s_cbranch_scc1 .Lend%=\n"
        ".LNC%=:\n\t" PZC0
        "s_add_u32 s50, s36, s53\n\t"
        WLG(0) RELN(".Lnc%=")
        CISSUE("", LASTC(".Lxc%="))
        CBODY(0, PZC, PZNC, ".Lfail%=") BARC
        ARRIVE(16) TSW(4)
        NEXTN
        ".Lfail%=:\n\t" KCNT
        "s_cmp_lg_u32 s47, 2\n\t" "s_cbranch_scc1 .Lnu%=\n\t"
        "s_mov_b32 s37, s36\n\t" "s_mov_b32 s36, s48\n\t" "s_sub_u32 s45, s45, 1\n\t" "s_sub_u32 s38, s38, 4\n\t" "s_sub_u32 s39, s39, " STR(EXF) "\n\t" "s_sub_u32 s40, s40, " STR(EXF) "\n\t"
        "s_sub_u32 s41, s41, " STR(EXK) "\n\t" "s_sub_u32 s42, s42, " STR(EXK) "\n\t" "s_sub_u32 s43, s43, s55\n\t" "s_sub_u32 s43, s43, s70\n\t" "s_sub_u32 s46, s46, 64\n"
        ".Lnu%=:\n\t"
        "s_mov_b32 s51, 1\n\t" "s_nop 7\n\t" WVM(0) "s_sub_u32 s98, s98, 1\n\t" "s_cbranch_scc1 .Lcap%=\n"
        ".Lent%=:\n\t"
        "s_cmp_eq_u32 s47, 1\n\t" "s_cselect_b32 s99, s66, s67\n\t" "s_cbranch_scc1 .Lnz%=\n\t" ZALL ".Lnz%=:\n\t"
        "s_cmp_eq_u32 s47, 0\n\t" "s_cselect_b32 s99, 0, s99\n\t" "s_cselect_b32 s50, s52, s53\n\t" "s_add_u32 s99, s99, s43\n\t" "s_add_u32 s50, s50, s36\n\t"
        SHRD "s_memrealtime s[72:73]\n\t"
        "s_cmp_eq_u32 %[wv], 3\n\t" "s_cselect_b32 s49, 0, 3\n\t" "s_add_u32 s49, s49, s47\n\t" "s_cmp_lg_u32 s51, 0\n\t" "s_cselect_b32 s49, s49, 7\n\t" "s_cselect_b64 vcc, -1, 0\n\t"
        "s_cselect_b32 s51, 0, 14\n\t" "s_mov_b32 s48, .Lpc%=-.Lnorm%=\n\t"
        RETA(0, ".Lua%=") RETA(1, ".Ltb%=y") RETA(2, ".Luc%=") RETA(3, ".Lna%=") RETA(5, ".Lnc%=")
        "s_getpc_b64 s[96:97]\n" ".Lpc%=:\n\t" "s_sub_u32 s96, s96, s48\n\t" "s_subb_u32 s97, s97, 0\n\t"
        SHUP "s_sub_u32 s49, s99, s72\n\t" "s_add_u32 s49, s49, s44\n"
        ".Lbump%=:\n\t"
        "s_cmp_ge_i32 s49, " STR(PREP) "\n\t" "s_cbranch_scc1 .Lok%=\n\t" "s_add_u32 s44, s44, %[tr]\n\t" "s_add_u32 s49, s49, %[tr]\n\t" "s_branch .Lbump%=\n"
        ".Lok%=:\n\t"
        "s_mov_b64 exec, 1\n\t" "v_mov_b32 v62, s44\n\t" "ds_max_u32 v38, v62\n\t" KTRC "s_mov_b64 exec, -1\n\t"
        "s_add_u32 s99, s99, s44\n\t" "s_sub_u32 s99, s99, s51\n\t"
        WLG(0) "s_memrealtime s[72:73]\n\t" "s_nop 3\n\t" "s_memrealtime s[74:75]\n\t" "s_nop 3\n"
        CLKL(".Lth%=", "s99")
        "s_setpc_b64 s[96:97]\n"
        ".Lcap%=:\n\t" "s_add_u32 s47, s47, 8\n"
        ".Lend%=:\n\t"
        "s_waitcnt vmcnt(0) lgkmcnt(0)\n\t"
        "s_mov_b32 s50, s64\n\t" "s_mov_b32 s49, s98\n\t" "s_mov_b32 s51, s68\n\t" "s_mov_b32 m0, s60"
        : "+{s[36:51]}"(sa), "+{s[52:59]}"(sc), "+{v[60:91]}"(keep), "=&{v[116:117]}"(ov), "+{a[192:207]}"(fv), "+{a[208:215]}"(fk), "+{v[8:23]}"(kd) FINEO TUNEO
        : "{v[32:47]}"(kc), [rs] "s"(rsx), [rf] "s"(rsf), [dm] "s"(__builtin_amdgcn_readfirstlane(PGO_Y + b * 128)), [tg] "s"(__builtin_amdgcn_readfirstlane(tag)), [r2] "s"(__builtin_amdgcn_readfirstlane(XR2B + flip)), [mb] "s"(__builtin_amdgcn_readfirstlane(ldsb + w * 4096)), [wv] "s"(__builtin_amdgcn_readfirstlane(w)), [tr] "s"(TR) TRCI,
          "{a[0:31]}"(wa[0]), "{a[32:63]}"(wa[1]), "{a[64:95]}"(wa[2]), "{a[96:127]}"(wa[3]), "{a[128:159]}"(wa[4]), "{a[160:191]}"(wa[5]),
          "{v[128:159]}"(wo[0]), "{v[160:191]}"(wo[1]), "{v[192:223]}"(wo[2]), "{v[224:255]}"(wo[3])
        : CL8(24, 25, 26, 27, 28, 29, 30, 31), "v48", "v49", "v50", "v51", CL8(52, 53, 54, 55, 56, 57, 58, 59), "v92", "v93", "v94", "v95", CL96, "v112", "v113", "v114", "v115", "v118", "v119", CL120,
          "s60", "s61", "s62", "s63", "s64", "s65", "s66", "s67", "s68", "s69", "s70", "s71", "s72", "s73", "s74", "s75", "s76", "s77", "s78", "s79", "s96", "s97", "s98", "s99", "vcc", "scc", "memory");
    const int code = __builtin_amdgcn_readfirstlane(sa[11]);
    xp = __builtin_amdgcn_readfirstlane(sa[14]);
    tcnt += sa[15];
    sa[15] = 0;
    if (code == 3) break;
    const int seg = __builtin_amdgcn_readfirstlane(sa[9]);
    if (code == 5) { XWAIT(seg + 2) xp = max(xp, seg + 2); sa[11] = 2; }
    else if (code == 1) { __builtin_amdgcn_raw_buffer_store_b64((u32x2){nosent(ov[0]), nosent(ov[1])}, rsx, sto, sa[0], 16); sa[11] = 2; }
    else { sc[2] += 1; sa[13] = 0; sa[11] = code - 8; sa[15] = 1; }
  }
  LBAR();
  if (tid == 0) __builtin_amdgcn_raw_buffer_store_b32(1u, rsx, 0, PGO_D + vcu * 128, 16);
  if (b == 0 && w == 1) {
    for (int it = 0; it < SPIN_CAP; ++it) {
      asm volatile("" ::: "memory");
      if (__ballot(__builtin_amdgcn_raw_buffer_load_b32(rsx, lane * 128, PGO_S, 16) != tag || __builtin_amdgcn_raw_buffer_load_b32(rsx, lane * 128 + 8192, PGO_S, 16) != tag) == 0) break;
    }
    if (lane == 0) __builtin_amdgcn_raw_buffer_store_b32(tag, rsx, 0, XEPO, 16);
  }
}

#define XSTAT 3
#define XVM 8
#define XP0 -1
#define XSC " sc1"
#define XPITCH 1056
#define XSLOT (32 * XPITCH)
#define XW(HB, t) "a[" STR(HB) "+8*xk+4*" #t ":" STR(HB) "+8*xk+4*" #t "+3]"
#define XF(rt) "v[96+8*(xk&3)+4*" #rt ":96+8*(xk&3)+4*" #rt "+3]"
#define XFN(rt) "v[96+8*((xk+3)&3)+4*" #rt ":96+8*((xk+3)&3)+4*" #rt "+3]"
#define XA(AC, o) "v[" STR(AC) "+" #o ":" STR(AC) "+" #o "+3]"
#define XM1(d, a, b, FIRST) ".if (xk == 0) && " STR(FIRST) "\n\tv_mfma_f32_16x16x32_bf16 " d ", " a ", " b ", 0\n\t.else\n\tv_mfma_f32_16x16x32_bf16 " d ", " a ", " b ", " d "\n\t.endif\n\t"
#define XMF_QK(i, HB, AC, F) XMF_QK##i(HB, AC, F)
#define XMF_QK0(HB, AC, F) XM1(XA(AC, 0), XW(HB, 0), XF(0), F)
#define XMF_QK1(HB, AC, F) XM1(XA(AC, 4), XW(HB, 1), XF(0), F)
#define XMF_QK2(HB, AC, F) XM1(XA(AC, 8), XW(HB, 0), XF(1), F)
#define XMF_QK3(HB, AC, F) XM1(XA(AC, 12), XW(HB, 1), XF(1), F)
#define XMF_V(i, HB, AC, F) XMF_V##i(HB, AC, F)
#define XMF_V0(HB, AC, F) XM1(XA(AC, 0), XF(0), XW(HB, 0), F)
#define XMF_V1(HB, AC, F) XM1(XA(AC, 8), XF(0), XW(HB, 1), F)
#define XMF_V2(HB, AC, F) XM1(XA(AC, 4), XF(1), XW(HB, 0), F)
#define XMF_V3(HB, AC, F) XM1(XA(AC, 12), XF(1), XW(HB, 1), F)
#define XRD(rt, CUR, NXT) ".if xk < 13\n\tds_read_b128 " XFN(rt) ", " CUR " offset:64*(xk+3)+" STR(rt * 16 * XPITCH) "\n\t.else\n\t" \
                          "ds_read_b128 " XFN(rt) ", " NXT " offset:64*(xk-13)+" STR(rt * 16 * XPITCH) "\n\t.endif\n\t"
#define XCV(EPI, PV, i) ".if " STR(EPI) " && (xk >= 1) && (xk < 3)\n\tv_cvt_pk_bf16_f32 v[128+4*(xk-1)+" #i "], v[" STR(PV) "+8*(xk-1)+2*" #i "], v[" STR(PV) "+8*(xk-1)+2*" #i "+1]\n\t.endif\n\t"
#define XPF 0
#define NPF 8
#define NPS 12
#define XTOUCH ".if " STR(XPF) " && (xk == 4)\n\t" "s_add_u32 %[pj], %[pj], 0x10000\n\t" "s_cmp_ge_i32 %[pj], 0\n\t" "s_cselect_b32 s54, %[pn], 0\n\t" ".endif\n\t" \
  ".if " STR(XPF) " && (xk == 7)\n\t" "s_and_b32 s56, %[pj], 0x1f0000\n\t" "s_xor_b32 s56, s56, %[pb]\n\t" ".endif\n\t" \
  ".if " STR(XPF) " && (xk == 12)\n\t" "buffer_load_dword %[pd], %[pv], s[52:55], s56 offen\n\t" ".endif\n\t"
#define XHALF(MF, HB, AC, PV, EPI, CUR, NXT, DSLOT, GOFF)                                                   \
  ".set xk, 0\n\t"                                                                                          \
  ".rept 16\n\t"                                                                                            \
  "s_waitcnt lgkmcnt(4)\n\t"                                                                                \
  MF(0, HB, AC, EPI) XRD(0, CUR, NXT) XCV(EPI, PV, 0)                                                       \
  ".if (xk >= 5) && (xk < 13)\n\t"                                                                          \
  "s_add_u32 m0, m0, " STR(XPITCH) "\n\t"                                                                   \
  ".endif\n\t"                                                                                              \
  MF(1, HB, AC, EPI) XRD(1, CUR, NXT) XCV(EPI, PV, 1)                                                       \
  ".if (xk >= 5) && (xk < 13)\n\t"                                                                          \
  "s_add_u32 %[dg], %[dg], 2048\n\t"                                                                        \
  ".endif\n\t"                                                                                              \
  MF(2, HB, AC, EPI) XCV(EPI, PV, 2)                                                                        \
  ".if (xk >= 4) && (xk < 12)\n\t"                                                                          \
  "buffer_load_dwordx4 %[l16], %[rx], %[dg] offen lds\n\t"                                                  \
  ".endif\n\t"                                                                                              \
  ".if xk == 0\n\t"                                                                                         \
  "s_add_u32 m0, %[dl], " STR((DSLOT) * XSLOT) "\n\t" "s_add_u32 %[dg], %[gb], " STR(GOFF) "\n\t"           \
  ".endif\n\t"                                                                                              \
  ".if " STR(EPI) " && (xk == " STR(XSTAT) ")\n\t"                                                                       \
  "buffer_store_dwordx4 v[128:131], v137, %[rf], %[sg] offen" XSC "\n\t"                                       \
  ".endif\n\t"                                                                                              \
  ".if " STR(EPI) " && (xk == 13)\n\t"                                                                      \
  "v_mov_b32 v137, %[l16]\n\t" "v_mov_b32 v138, %[lp2]\n\t"                                                 \
  ".endif\n\t"                                                                                              \
  ".if " STR(EPI) " && (xk == 14)\n\t"                                                                      \
  "s_add_u32 %[sg], %[sg], " STR(NH * 8 * 1024) "\n\t"                                                      \
  "v_add_u32 v136, 1, v136\n\t"                                                                             \
  ".endif\n\t"                                                                                              \
  ".if !" STR(EPI) " && (xk == 1)\n\t"                                                                      \
  "buffer_store_dword v136, %[fo], %[rp], 0 offen sc1\n\t"                                                  \
  ".endif\n\t"                                                                                              \
  MF(3, HB, AC, EPI) XCV(EPI, PV, 3)                                                                        \
  XTOUCH                                                                                                    \
  ".if xk == 0\n\t"                                                                                         \
  "s_waitcnt vmcnt(" STR(XVM) "+" STR(XPF) ")\n\t" "s_barrier\n\t"                                                               \
  ".endif\n\t"                                                                                              \
  ".if xk == 2\n\t"                                                                                         \
  "s_min_u32 %[dg], %[dg], " STR(S_LEN * DM * 2 - 16384 + 1024) "\n\t"                                      \
  ".endif\n\t"                                                                                              \
  ".if " STR(EPI) " && (xk == " STR(XSTAT) ")\n\t"                                                                       \
  "buffer_store_dwordx4 v[132:135], v138, %[rf], %[sg] offen" XSC "\n\t"                                       \
  ".endif\n\t"                                                                                              \
  ".set xk, xk+1\n\t"                                                                                       \
  ".endr\n\t"
#define XLOOP(MF)                                                                                           \
  "s_mov_b32 %[kp], m0\n\t" "s_mov_b64 s[52:53], %[wo]\n\t" "s_mov_b32 s55, 0x20000\n\t"                       \
  "v_mov_b32 v136, %[p0]\n\t" "v_mov_b32 v137, " STR(OOR) "\n\t" "v_mov_b32 v138, " STR(OOR) "\n\t"                                                                                \
  "ds_read_b128 v[96:99], %[r0]\n\tds_read_b128 v[100:103], %[r0] offset:" STR(16 * XPITCH) "\n\t"          \
  "ds_read_b128 v[104:107], %[r0] offset:64\n\tds_read_b128 v[108:111], %[r0] offset:64+" STR(16 * XPITCH) "\n\t"   \
  "ds_read_b128 v[112:115], %[r0] offset:128\n\tds_read_b128 v[116:119], %[r0] offset:128+" STR(16 * XPITCH) "\n"   \
  ".Lx%=:\n\t"                                                                                              \
  XHALF(MF, 0, 64, 80, 1, "%[r0]", "%[r1]", 3, 66560) XHALF(MF, 128, 64, 80, 0, "%[r1]", "%[r2]", 0, 131072) \
  XHALF(MF, 0, 80, 64, 1, "%[r2]", "%[r3]", 1, 132096) XHALF(MF, 128, 80, 64, 0, "%[r3]", "%[r0]", 2, 196608) \
  "s_add_u32 %[gb], %[gb], 131072\n\t"                                                                      \
  "s_sub_u32 %[cnt], %[cnt], 1\n\t"                                                                         \
  "s_cmp_lg_u32 %[cnt], 0\n\t"                                                                              \
  "s_cbranch_scc1 .Lx%=\n\t"                                                                                \
  "s_mov_b32 m0, %[kp]\n\t"                                                                                 \
  "s_nop 7\n\t" "s_waitcnt lgkmcnt(0)"

template <bool ISV>
__device__ __forceinline__ void x_role(unsigned char* lds, const u16* __restrict__ x, const u16* __restrict__ Wm, int fo0, int head,
                                       __amdgpu_buffer_rsrc_t rsf, u32* prog, u32 tag, int lane, int w, const u16* __restrict__ Wo, int gs) {
  const int ldsb = (int)(size_t)(LAS unsigned char*)lds;
  const __amdgpu_buffer_rsrc_t rsxin = __builtin_amdgcn_make_buffer_rsrc((void*)x, 0, S_LEN * DM * 2, 0x00020000);
  const __amdgpu_buffer_rsrc_t rsp = __builtin_amdgcn_make_buffer_rsrc((void*)prog, 0, 128, 0x00020000);
  const int lr = lane & 15, lg = lane >> 4;
  const int xrd = ldsb + lr * XPITCH + lg * 16;
  const int fob = fo0 + (ISV ? (head * 8 + 2 * w) : (head * 8 + w)) * 1024;
  const int p2 = ISV ? 1024 : 4096;
  u32x32 xw[8];
  {
    const __amdgpu_buffer_rsrc_t rw = __builtin_amdgcn_make_buffer_rsrc((void*)Wm, 0, DM * HD * 2, 0x00020000);
    const int vq = (lane >> 4) * (HD * 2) + head * 256 + (lane & 15) * 16;
#define XWISSUE(c) _Pragma("unroll") for (int j = 0; j < 8; ++j)                                                       \
      asm volatile("s_mov_b32 m0, %0\n\ts_nop 3\n\tbuffer_load_dwordx4 %1, %2, %3 offen lds"                           \
                   :: "s"(ldsb + ((c) & 3) * XSLOT + (8 * w + j) * XPITCH), "v"(vq), "s"(rw), "s"((128 * (c) + 32 * w + 4 * j) * (HD * 2)) : "memory");
#define XXISSUE(h) _Pragma("unroll") for (int j = 0; j < 8; ++j)                                                       \
      asm volatile("s_mov_b32 m0, %0\n\ts_nop 3\n\tbuffer_load_dwordx4 %1, %2, %3 offen lds"                           \
                   :: "s"(ldsb + (h) * XSLOT + (8 * w + j) * XPITCH), "v"(lane * 16), "s"(rsxin), "s"((((h) >> 1) * 32 + 8 * w + j) * 2048 + ((h) & 1) * 1024) : "memory");
    XWISSUE(0) XWISSUE(1) XWISSUE(2) XWISSUE(3)
    const int q = lr >> 2, p = lr & 3;
#pragma unroll
    for (int c = 0; c < 8; ++c) {
      asm volatile("s_waitcnt vmcnt(24)\n\ts_barrier" ::: "memory");
#pragma unroll
      for (int s = 0; s < 4; ++s)
#pragma unroll
        for (int t = 0; t < 2; ++t) {
          const unsigned char* base = lds + (c & 3) * XSLOT + (8 * s + 2 * lg) * XPITCH + q * 256 + w * 64 + p * 16 + t * 8;
          const u32x2 t0 = __builtin_bit_cast(u32x2, __builtin_amdgcn_ds_read_tr16_b64_v4i16((LAS v4i16_t*)(base)));
          const u32x2 t1 = __builtin_bit_cast(u32x2, __builtin_amdgcn_ds_read_tr16_b64_v4i16((LAS v4i16_t*)(base + XPITCH)));
          const int o = ((4 * c + s) * 2 + t) * 4;
          xw[o >> 5][(o & 31) + 0] = t0[0]; xw[o >> 5][(o & 31) + 1] = t0[1]; xw[o >> 5][(o & 31) + 2] = t1[0]; xw[o >> 5][(o & 31) + 3] = t1[1];
        }
      LBAR();
      if (c < 4) { XWISSUE(c + 4) } else if (c < 7) { XXISSUE(c - 4) }
    }
  }
  asm volatile("s_waitcnt vmcnt(8)\n\ts_barrier" ::: "memory");
  u32x16 xacc;
  u32 pd;
  {
    int pj = (((gs & 7) == (((384 - (int)(size_t)Wo) & 1023) >> 7) ? NPS : NPF) - 2 * NSEG - 1) * 65536;
    u32 kp, dg; int gb = 8 * w * 2048, cnt = NSEG / 2, sg = fob - NH * 8 * 1024;
#define XASM(MF)                                                                                            \
    asm volatile(XLOOP(MF)                                                                                  \
                 : "=&{v[80:95]}"(xacc), [kp] "=&s"(kp), [dg] "=&s"(dg), [gb] "+s"(gb), [cnt] "+s"(cnt), [sg] "+s"(sg), [pj] "+s"(pj), [pd] "=&v"(pd) \
                 : [r0] "v"(xrd), [r1] "v"(xrd + XSLOT), [r2] "v"(xrd + 2 * XSLOT), [r3] "v"(xrd + 3 * XSLOT), [l16] "v"(lane * 16), [lp2] "v"(lane * 16 + p2), \
                   [rx] "s"(rsxin), [rf] "s"(rsf), [rp] "s"(rsp), [fo] "v"(lane == 0 && w == 0 ? 0 : OOR), [dl] "s"(ldsb + 8 * w * XPITCH), [p0] "s"(tag + XP0), \
                   [wo] "s"(Wo), [pn] "s"(gs < 0 ? 0 : DM * HD * 2), [pb] "s"((16 * w + ((gs + 4 * w) & 15)) * 64 * DM * 2), [pv] "v"((lane >> 1) * (DM * 2) + (gs & 15) * 128 + (lane & 1) * 64), \
                   "{a[0:31]}"(xw[0]), "{a[32:63]}"(xw[1]), "{a[64:95]}"(xw[2]), "{a[96:127]}"(xw[3]),      \
                   "{a[128:159]}"(xw[4]), "{a[160:191]}"(xw[5]), "{a[192:223]}"(xw[6]), "{a[224:255]}"(xw[7]) \
                 : CL64, CL72, CL96, CL112, CL120, CL8(128, 129, 130, 131, 132, 133, 134, 135), "v136", "v137", "v138", "s52", "s53", "s54", "s55", "s56", "scc", "memory");
    if (ISV) { XASM(XMF_V) } else { XASM(XMF_QK) }
  }
#pragma unroll
  for (int jj = 0; jj < 2; ++jj) {
    u32x4 pk;
#pragma unroll
    for (int e = 0; e < 4; ++e) pk[e] = pack2(__uint_as_float(xacc[8 * jj + 2 * e]), __uint_as_float(xacc[8 * jj + 2 * e + 1]));
    __builtin_amdgcn_raw_buffer_store_b128(pk, rsf, lane * 16 + p2 * jj, fob + (NSEG - 1) * (NH * 8 * 1024), 16);
  }
  asm volatile("s_waitcnt vmcnt(0)\n\ts_barrier" : "+v"(pd) :: "memory");
  if (lane == 0 && w == 0) __hip_atomic_store(prog, tag + NSEG + 8, RLX_AGENT);
}

#define SU_CP 4288
#define SU_U 0
#define SU_K (2 * SU_CP)
#define SU_Y (SU_K + 10400)
#define SU_CH (SU_Y + 4096)
#define SST(i)
__device__ __forceinline__ int koff(int i) {
  const int x = i & 127;
  return i < 256 ? (32 * (i >> 7) + 16 * ((i >> 2) & 1) + (x >> 3)) * 16 + (i & 3) * 4
       : i < 384 ? 1024 + x * 4
       : i < 512 ? 1024 + (32 + 16 * (x >> 6) + ((x >> 1) & 15)) * 16 + (2 * ((x >> 5) & 1) + (x & 1)) * 4
                 : 2048 + (16 * ((i - 512) >> 6) + (i & 15)) * 16 + ((i >> 4) & 3) * 4;
}
struct SsmIn { u32x4 ka, kb; u32x2 kc; u32 kd, ke[4]; u32x4 uv[4]; u32x2 up, dsk; };
#define SSM_GOT(in) asm volatile("" : "+v"(in.ka), "+v"(in.kb), "+v"(in.kc), "+v"(in.kd), "+v"(in.ke[0]), "+v"(in.ke[1]), "+v"(in.ke[2]), "+v"(in.ke[3]), \
                                      "+v"(in.uv[0]), "+v"(in.uv[1]), "+v"(in.uv[2]), "+v"(in.uv[3]), "+v"(in.up), "+v"(in.dsk))
__device__ __forceinline__ void ssm_load(SsmIn& in, int d0, __amdgpu_buffer_rsrc_t rst, __amdgpu_buffer_rsrc_t rsd, int tid, int lane, int w) {
  const int so = 2 * SSM_L * SSM_D * 2 + (d0 + w) * 3072, ia = lane < 32 ? 8 * lane : 128 + 4 * lane;
  in.ka = __builtin_amdgcn_raw_buffer_load_b128(rst, koff(ia), so, 16);
  in.kb = __builtin_amdgcn_raw_buffer_load_b128(rst, koff(ia + 4), so, 16);
  in.kc = __builtin_amdgcn_raw_buffer_load_b64(rst, koff(384 + 2 * lane), so, 16);
  in.kd = __builtin_amdgcn_raw_buffer_load_b32(rst, koff(386 + 2 * lane), so, 16);
#pragma unroll
  for (int i = 0; i < 4; ++i) in.ke[i] = __builtin_amdgcn_raw_buffer_load_b32(rst, koff(512 + 64 * (i >> 1) + (i & 1) + lane), so, 16);
#pragma unroll
  for (int i = 0; i < 4; ++i) in.uv[i] = __builtin_amdgcn_raw_buffer_load_b128(rst, tid * 16, d0 * 4096 + i * 4096, 16);
  in.up = __builtin_amdgcn_raw_buffer_load_b64(rst, tid ? tid * 16 - 8 : OOR, d0 * 4096 + 3 * 4096, 16);
  in.dsk = __builtin_amdgcn_raw_buffer_load_b64(rsd, 0, d0 * 2, 0);
}
__device__ __forceinline__ float reslo(float v, u32 h) { return v - __uint_as_float(h << 16); }
__device__ __forceinline__ float reshi(float v, u32 h) { return v - __uint_as_float(h & 0xffff0000u); }
__device__ __forceinline__ int ssm_unit(unsigned char* sm, int d0, SsmIn& in, bool pend, __amdgpu_buffer_rsrc_t rst, __amdgpu_buffer_rsrc_t rsx, int fb,
                                        __amdgpu_buffer_rsrc_t rsd, int tid, int lane, int w) {
  unsigned char* cb = sm + w * SU_CH;
  SST(0)
  const int one = tid == 0 ? 0 : OOR;
  u32 tk = __builtin_amdgcn_raw_ptr_buffer_atomic_add_i32(1, rsx, one, XTKT, 0);
  const int r = lane & 31, h = lane >> 5;
#pragma unroll
  for (int c = 0; c < 4; ++c) {
    const u32 sel = (c & 1) ? 0x07060302u : 0x05040100u;
    u32x4 e, o;
#pragma unroll
    for (int i = 0; i < 4; ++i) {
      e[i] = __builtin_amdgcn_perm(in.uv[i][2 + (c >> 1)], in.uv[i][c >> 1], sel);
      o[i] = __builtin_amdgcn_perm(in.uv[i][c >> 1], i ? in.uv[i ? i - 1 : 0][2 + (c >> 1)] : in.up[c >> 1], sel);
    }
    *(u32x4*)(sm + c * SU_CH + SU_U + 96 + tid * 16) = e;
    *(u32x4*)(sm + c * SU_CH + SU_U + SU_CP + 96 + tid * 16) = o;
  }
  SST(1)
  {
    unsigned char* kr = cb + SU_K + ((lane >> 2) + 1) * 80 + (3 - (lane & 3)) * 16;
    const f32x4 a = __builtin_bit_cast(f32x4, in.ka), b = __builtin_bit_cast(f32x4, in.kb);
    const bool dn = lane < 32;
    const float v[8] = {a[0], dn ? a[1] : a[0], dn ? a[2] : a[1], dn ? a[3] : a[2], dn ? b[0] : a[2], dn ? b[1] : a[2], dn ? b[2] : a[3], dn ? b[3] : b[0]};
    u32x4 hi, lo;
#pragma unroll
    for (int e = 0; e < 4; ++e) { hi[e] = pack2(v[7 - 2 * e], v[6 - 2 * e]); lo[e] = pack2(reslo(v[7 - 2 * e], hi[e]), reshi(v[6 - 2 * e], hi[e])); }
    *(u32x4*)(kr) = hi; *(u32x4*)(kr + 5200) = lo;
    {
      const float v0 = __uint_as_float(in.kc[0]), v1 = __uint_as_float(in.kc[1]), v2 = __uint_as_float(in.kd);
      hi = (u32x4){pack2(v2, v2), pack2(v1, v1), pack2(v1, v0), pack2(v0, v0)};
      const float r0 = reshi(v0, hi[3]), r1 = reshi(v1, hi[1]), r2 = reshi(v2, hi[0]);
      *(u32x4*)(kr + 1280) = hi; *(u32x4*)(kr + 1280 + 5200) = (u32x4){pack2(r2, r2), pack2(r1, r1), pack2(r1, r0), pack2(r0, r0)};
    }
#pragma unroll
    for (int j = 0; j < 2; ++j) {
      const float v0 = __uint_as_float(in.ke[2 * j]), v1 = __uint_as_float(in.ke[2 * j + 1]);
      const u32 h0 = pack2(v0, v0), h1 = pack2(v1, v1), sel = (lane & 1) ? 0x01000100u : 0x05040100u;
      const float r0 = reshi(v0, h0), r1 = reshi(v1, h1);
      const u32 l0 = pack2(r0, r0), l1 = pack2(r1, r1);
      *(u32x4*)(kr + 2560 + 1280 * j) = (u32x4){h1, __builtin_amdgcn_perm(h0, h1, sel), h0, h0};
      *(u32x4*)(kr + 2560 + 1280 * j + 5200) = (u32x4){l1, __builtin_amdgcn_perm(l0, l1, sel), l0, l0};
    }
  }
  SST(2)
  f32x4 sk[8];
  {
    const u32 d01 = __builtin_amdgcn_readfirstlane(in.dsk[0]), d23 = __builtin_amdgcn_readfirstlane(in.dsk[1]);
#pragma unroll
    for (int i = 0; i < 4; ++i)
      asm("v_dot2_f32_bf16 %0, %8, %12, 0\n\tv_dot2_f32_bf16 %1, %8, %13, 0\n\tv_dot2_f32_bf16 %2, %9, %14, 0\n\tv_dot2_f32_bf16 %3, %9, %15, 0\n\t"
          "v_dot2_f32_bf16 %4, %10, %12, 0\n\tv_dot2_f32_bf16 %5, %10, %13, 0\n\tv_dot2_f32_bf16 %6, %11, %14, 0\n\tv_dot2_f32_bf16 %7, %11, %15, 0\n\t"
          "v_cvt_pk_bf16_f32 %0, 0, %0\n\tv_cvt_pk_bf16_f32 %1, 0, %1\n\tv_cvt_pk_bf16_f32 %2, 0, %2\n\tv_cvt_pk_bf16_f32 %3, 0, %3\n\t"
          "v_cvt_pk_bf16_f32 %4, 0, %4\n\tv_cvt_pk_bf16_f32 %5, 0, %5\n\tv_cvt_pk_bf16_f32 %6, 0, %6\n\tv_cvt_pk_bf16_f32 %7, 0, %7"
          : "=&v"(sk[2 * i][0]), "=&v"(sk[2 * i][1]), "=&v"(sk[2 * i][2]), "=&v"(sk[2 * i][3]),
            "=&v"(sk[2 * i + 1][0]), "=&v"(sk[2 * i + 1][1]), "=&v"(sk[2 * i + 1][2]), "=&v"(sk[2 * i + 1][3])
          : "v"(in.uv[i][0]), "v"(in.uv[i][1]), "v"(in.uv[i][2]), "v"(in.uv[i][3]), "s"(d01 & 0xffffu), "s"(d01 & 0xffff0000u), "s"(d23 & 0xffffu), "s"(d23 & 0xffff0000u));
  }
  asm volatile("s_waitcnt vmcnt(0)" : "+v"(tk), "+v"(sk[0]), "+v"(sk[1]), "+v"(sk[2]), "+v"(sk[3]), "+v"(sk[4]), "+v"(sk[5]), "+v"(sk[6]), "+v"(sk[7]) :: "memory");
  if (tid == 0) *(u32*)(sm + 4 * SU_CH) = tk + 65u;
  LBAR();
  SST(3)
  if (pend) __builtin_amdgcn_raw_ptr_buffer_atomic_add_i32(1, rsx, one, XYDN, 0);
  const int nxt = __builtin_amdgcn_readfirstlane(*(const u32*)(sm + 4 * SU_CH));
  if (nxt < 256) ssm_load(in, 4 * nxt, rst, rsd, tid, lane, w);
  SST(4)
  f32x16 cv0, cv1;
  {
    const int m0 = 48 - 15 + 8 * h + r;
    const int cbo = (int)(size_t)(LAS unsigned char*)cb;
    int ua = cbo + SU_U + (m0 & 1) * SU_CP + ((m0 + 1) & ~1) * 2 - 32, ub = ua + 2048;
    const int kb = cbo + SU_K + 16 * h;
    int a1 = kb + (33 + r) * 80, a0 = kb + (1 + r) * 80, cnt = 32;
#define CRA(S, ad, O) "ds_read2_b32 " RNG("v", S, 2) ", " ad " offset0:" STR((O) / 4) " offset1:" STR((O) / 4 + 1) "\n\t" \
                      "ds_read2_b32 " RNG("v", (S) + 2, 2) ", " ad " offset0:" STR((O) / 4 + 2) " offset1:" STR((O) / 4 + 3) "\n\t"
#define CRB(S, ad, O) "ds_read_b128 " RNG("v", S, 4) ", " ad " offset:" STR(O) "\n\t"
#define CMF(C, A, B) "v_mfma_f32_32x32x16_bf16 " RNG("v", C, 16) ", " RNG("v", A, 4) ", " RNG("v", B, 4) ", " RNG("v", C, 16) "\n\t"
#define CUPD "v_add_u32 %[ua], 64, %[ua]\n\t" "v_subrev_u32 %[a1], 80, %[a1]\n\t" "v_subrev_u32 %[a0], 80, %[a0]\n\t" "v_max_i32 %[a0], %[a0], %[kb]\n\t"
#define CHALF(S, N, O, UPD, UPD2)                                                                 \
    WLG(5) CMF(64, S, (S) + 8) UPD CRA(N, "%[ua]", O)                                             \
    WLG(6) CMF(80, S, (S) + 16) UPD2 CRB((N) + 8, "%[a0]", O) CRB((N) + 16, "%[a1]", O)           \
    WLG(6) CMF(80, (S) + 4, (S) + 8) CRA((N) + 4, "%[ub]", O)                                     \
    WLG(7) CMF(64, S, (S) + 12) CRB((N) + 12, "%[a0]", (O) + 5200)                                \
    WLG(7) CMF(80, S, (S) + 20) CRB((N) + 20, "%[a1]", (O) + 5200)                                \
    CMF(80, (S) + 4, (S) + 12)
    asm volatile(
        CRA(96, "%[ua]", 32) CRB(104, "%[a0]", 32) CRB(112, "%[a1]", 32) CRA(100, "%[ub]", 32) CRB(108, "%[a0]", 32 + 5200) CRB(116, "%[a1]", 32 + 5200)
        ZER(64) ZER(68) ZER(72) ZER(76) ZER(80) ZER(84) ZER(88) ZER(92)
        ".Lc%=:\n\t"
        CHALF(96, 120, 0, "", "")
        CHALF(120, 96, 32, CUPD, "v_add_u32 %[ub], 64, %[ub]\n\t")
        "s_sub_u32 %[cnt], %[cnt], 1\n\t"
        "s_cmp_lg_u32 %[cnt], 0\n\t"
        "s_cbranch_scc1 .Lc%=\n\t"
        "s_nop 7\n\t" "s_nop 3\n\t" WLG(0)
        : "=&{v[64:79]}"(cv0), "=&{v[80:95]}"(cv1), [ua] "+v"(ua), [ub] "+v"(ub), [a0] "+v"(a0), [a1] "+v"(a1), [cnt] "+s"(cnt)
        : [kb] "v"(kb)
        : CL96, CL112, CL120, CL8(128, 129, 130, 131, 132, 133, 134, 135), CL8(136, 137, 138, 139, 140, 141, 142, 143), "scc", "memory");
  }
  SST(5)
  SSM_GOT(in);
#pragma unroll
  for (int g = 0; g < 2; ++g)
#pragma unroll
    for (int i = 0; i < 4; ++i) {
      const f32x16& c = g ? cv1 : cv0;
      *(u32x2*)(cb + SU_Y + (32 * g + r) * 64 + ((i ^ (r >> 1)) & 3) * 16 + 8 * h) = (u32x2){pack2(c[4 * i], c[4 * i + 1]), pack2(c[4 * i + 2], c[4 * i + 3])};
    }
  LBAR();
  SST(6)
  {
    u32x4 c[4];
#pragma unroll
    for (int j = 0; j < 4; ++j) c[j] = *(const u32x4*)(sm + j * SU_CH + SU_Y + (tid >> 2) * 64 + ((tid ^ (tid >> 3)) & 3) * 16);
#pragma unroll
    for (int i = 0; i < 4; ++i) {
      u32x4 y;
      asm("v_dot2_f32_bf16 %4, %12, %16, %4\n\tv_dot2_f32_bf16 %5, %13, %16, %5\n\tv_dot2_f32_bf16 %6, %14, %16, %6\n\tv_dot2_f32_bf16 %7, %15, %16, %7\n\t"
          "v_dot2_f32_bf16 %8, %12, %17, %8\n\tv_dot2_f32_bf16 %9, %13, %17, %9\n\tv_dot2_f32_bf16 %10, %14, %17, %10\n\tv_dot2_f32_bf16 %11, %15, %17, %11\n\t"
          "v_cvt_pk_bf16_f32 %0, %4, %5\n\tv_cvt_pk_bf16_f32 %1, %6, %7\n\tv_cvt_pk_bf16_f32 %2, %8, %9\n\tv_cvt_pk_bf16_f32 %3, %10, %11"
          : "=&v"(y[0]), "=&v"(y[1]), "=&v"(y[2]), "=&v"(y[3]),
            "+v"(sk[2 * i][0]), "+v"(sk[2 * i][1]), "+v"(sk[2 * i][2]), "+v"(sk[2 * i][3]),
            "+v"(sk[2 * i + 1][0]), "+v"(sk[2 * i + 1][1]), "+v"(sk[2 * i + 1][2]), "+v"(sk[2 * i + 1][3])
          : "v"(c[0][i]), "v"(c[1][i]), "v"(c[2][i]), "v"(c[3][i]), "s"(0x00003f80u), "s"(0x3f800000u));
      __builtin_amdgcn_raw_buffer_store_b128(y, rst, tid * 16, SSM_L * SSM_D * 2 + d0 * 4096 + i * 4096, 16);
    }
  }
  if (nxt >= 256) {
    asm volatile("s_waitcnt vmcnt(0)\n\ts_barrier" ::: "memory");
    __builtin_amdgcn_raw_ptr_buffer_atomic_add_i32(1, rsx, one, XYDN, 0);
  }
  SST(7)
  return nxt;
}

#define L_TOTAL 163840
__device__ __forceinline__ int ldw(__amdgpu_buffer_rsrc_t rs, int off) {
  asm volatile("" ::: "memory");
  return __builtin_amdgcn_readfirstlane(__builtin_amdgcn_raw_buffer_load_b32(rs, 0, off, 16));
}
#define GF(rt) "v[64+8*(xk&7)+4*" #rt ":64+8*(xk&7)+4*" #rt "+3]"
#define GW2 "v[128+4*xk:128+4*xk+3]"
#define GW3 "v[24+4*(xk&1):24+4*(xk&1)+3]"
#define GLOAD(S, K) "s_xor_b32 %[tq], " S ", 1024*(" K ")\n\t"
#define GTR(K) "ds_read_b128 v[24+4*((" K ")&1):24+4*((" K ")&1)+3], %[wl] offset:1024*((" K ")&31)\n\t"
#define SLOWC(s_) (((s_) & 7) == (((384 - (int)(size_t)Wo) & 1023) >> 7))
#define GPACE 0
#define YAUX 16
#define UDELAY 100
#define USH 3
#define GNOSSM 1
#define GNG 3
#define GS0 25
__device__ __forceinline__ void g_role(unsigned char* lds, const u16* __restrict__ Wo, u16* __restrict__ out, __amdgpu_buffer_rsrc_t rsx,
                                       __amdgpu_buffer_rsrc_t rsf, int fb, u32 tag, int grp, int sl, int w, u32* udn, const u16* __restrict__ u, __amdgpu_buffer_rsrc_t rst) {
  const int lane = fresh_lane();
  const int lr = lane & 15, lg = lane >> 4;
  const int rot = (2 * sl + 8 * w) & 31;
  const int ldsb = (int)(size_t)(LAS unsigned char*)lds;
  u32x32 xw[8], w2[4];
#define GSX 0
  const bool slow = SLOWC(sl);
  const int gs0 = GS0 + (slow ? GSX : 0);
#define GC2 0
#define GAUX ""
  const int c0 = GC2 ? 32 * sl : 64 * sl, c1 = GC2 ? 512 + 32 * (sl ^ 8) : 64 * sl + 32;
  LBAR();
  {
    const __amdgpu_buffer_rsrc_t rw = __builtin_amdgcn_make_buffer_rsrc((void*)Wo, 0, DM * HD * 2, 0x00020000);
    const int vq = (lane >> 3) * (DM * 2) + ((lane & 4) ? c1 : c0) * 2 + (lane & 3) * 16;
    const int sq = (1024 * w + rot * 32) * (DM * 2);
#define GSLOT(c) ((c) < 8 ? (((c) & 3) == 3 ? 131072 + w * 8192 : w * 32768 + 8192 * (((c) & 3) + 1)) : (((c) - 8) % 3 == 2 ? 131072 + w * 8192 : w * 32768 + 8192 * (((c) - 8) % 3 + 2)))
#define GISSUE(c) _Pragma("unroll") for (int j = 0; j < 8; ++j)                                                        \
      asm volatile("s_mov_b32 m0, %0\n\ts_nop 3\n\tbuffer_load_dwordx4 %1, %2, %3 offen" GAUX " lds"                           \
                   :: "s"(ldsb + GSLOT(c) + j * 1024), "v"(vq), "s"(rw), "s"(sq ^ ((64 * (c) + 8 * j) * DM * 2)) : "memory");
    GISSUE(0) GISSUE(1) GISSUE(2) GISSUE(3)
#define GPF 0
#pragma unroll
    for (int b = 2 - GPF; b < 2; ++b) {
      const int s3 = (384 - (int)(size_t)Wo) & 1023, ss = (s3 >> 7) + 8 * (lane >> 5);
      const int pf = (sl & 7) == (s3 >> 7) ? OOR : (1024 * w + 64 * (((ss + 4 * w) & 15) ^ (8 * b + ((lane >> 2) & 7))) + 4 * sl + (lane & 3)) * (DM * 2) + (lane >> 5) * 1024 + s3;
      asm volatile("s_mov_b32 m0, %0\n\ts_nop 3\n\tbuffer_load_dword %1, %2, 0 offen lds" :: "s"(ldsb + w * 32768 + 7936), "v"(pf), "s"(rw) : "memory");
    }
    const int q = lr >> 2, p = lr & 3;
    u32x4 w3[16];
#pragma unroll
    for (int c = 0; c < 16; ++c) {
      if (c < 5) waitvm<24>(); else if (c < 14) waitvm<16>(); else if (c == 14) waitvm<8>(); else waitvm<0>();
      u32x4 f3[2];
#pragma unroll
      for (int s = 0; s < 2; ++s)
#pragma unroll
        for (int t = 0; t < 4; ++t) {
          const unsigned char* base = lds + GSLOT(c) + (32 * s + 8 * lg + q) * 128 + (t < 2 ? p * 16 + t * 8 : t * 32 + p * 8);
          const u32x2 t0 = __builtin_bit_cast(u32x2, __builtin_amdgcn_ds_read_tr16_b64_v4i16((LAS v4i16_t*)(base)));
          const u32x2 t1 = __builtin_bit_cast(u32x2, __builtin_amdgcn_ds_read_tr16_b64_v4i16((LAS v4i16_t*)(base + 512)));
          const int o = t < 2 ? ((2 * c + s) * 2 + t) * 4 : (2 * c + s) * 4;
          if (t < 2) { xw[o >> 5][(o & 31) + 0] = t0[0]; xw[o >> 5][(o & 31) + 1] = t0[1]; xw[o >> 5][(o & 31) + 2] = t1[0]; xw[o >> 5][(o & 31) + 3] = t1[1]; }
          else if (t == 2) { w2[o >> 5][(o & 31) + 0] = t0[0]; w2[o >> 5][(o & 31) + 1] = t0[1]; w2[o >> 5][(o & 31) + 2] = t1[0]; w2[o >> 5][(o & 31) + 3] = t1[1]; }
          else f3[s] = (u32x4){t0[0], t0[1], t1[0], t1[1]};
        }
      asm volatile("s_waitcnt lgkmcnt(0)" ::: "memory");
      if (c < 8) { *(u32x4*)(lds + w * 32768 + (2 * c) * 1024 + lane * 16) = f3[0]; *(u32x4*)(lds + w * 32768 + (2 * c + 1) * 1024 + lane * 16) = f3[1]; }
      else { w3[2 * (c - 8)] = f3[0]; w3[2 * (c - 8) + 1] = f3[1]; }
      if (GPACE) __builtin_amdgcn_s_sleep(GPACE);
      if (c < 4) { GISSUE(c + 4) } else if (c >= 5 && c < 13) { GISSUE(c + 3) }
    }
#pragma unroll
    for (int i = 0; i < 16; ++i) *(u32x4*)(lds + w * 32768 + (16 + i) * 1024 + lane * 16) = w3[i];
    LBAR();
  }
  if (grp == 0 && (USH == 2 || (USH == 3 && slow))) {
    const int tid = w * 64 + lane;
    unsigned char* im = lds + 131072;
    const __amdgpu_buffer_rsrc_t ru = __builtin_amdgcn_make_buffer_rsrc((void*)u, 0, SSM_L * SSM_D * 2, 0x00020000);
    const int vo = (tid >> 5) * (SSM_D * 2) + (tid & 31) * 16, rp = 4 * (tid & 7) + ((tid >> 3) & 3);
    u32x4 t[2][8];
#pragma unroll
    for (int q = 0; q < 2; ++q)
#pragma unroll
      for (int i = 0; i < 8; ++i) t[q][i] = __builtin_amdgcn_raw_buffer_load_b128(ru, vo, (sl * 64 + i * 8) * (SSM_D * 2) + q * 512, 0);
#pragma unroll
    for (int q = 0; q < 4; ++q) {
#pragma unroll
      for (int i = 0; i < 8; ++i) { const int r = i * 8 + (tid >> 5); *(u32x4*)(im + r * 512 + (((tid & 31) ^ (r >> 1)) * 16)) = t[q & 1][i]; }
      if (q < 2) {
#pragma unroll
        for (int i = 0; i < 8; ++i) t[q][i] = __builtin_amdgcn_raw_buffer_load_b128(ru, vo, (sl * 64 + i * 8) * (SSM_D * 2) + (q + 2) * 512, 0);
      }
      LBAR();
#pragma unroll
      for (int i = 0; i < 8; ++i) {
        const int o = rp * 1024 + (((i * 4 + w) ^ rp) * 16) + (lane >> 5) * 8;
        const u32x2 a = *(const u32x2*)(im + o), c = *(const u32x2*)(im + o + 512);
        __builtin_amdgcn_raw_buffer_store_b128((u32x4){a[0], a[1], c[0], c[1]}, rst, (tid >> 3) * 4096 + (tid & 7) * 16, (q * 256 + i * 32) * 4096 + sl * 128, 16);
      }
      if (q < 3) LBAR();
    }
    asm volatile("s_waitcnt vmcnt(0) lgkmcnt(0)\n\ts_barrier" ::: "memory");
  }
  wait_start(rsx, fb, tag, true);
  if (USH && grp == 0 && w == 0 && lane == 0) __hip_atomic_fetch_add(udn, 1u, RLX_AGENT);
  const int wl = ldsb + w * 32768 + lane * 16;
  const __amdgpu_buffer_rsrc_t rso = __builtin_amdgcn_make_buffer_rsrc((void*)out, 0, S_LEN * OUTLD * 2, 0x00020000);
  const int rw_ = ldsb + 131072 + w * 8192 + lane * 16, rr_ = ldsb + 131072 + w * 2048 + lane * 16;
  const int oa = ((16 * (w & 1) + lr) * OUTLD + (w < 2 ? c0 + 8 * lg : c1 + 4 * lg)) * 2, ob = oa + (w < 2 ? 8 : 32);
  const int hq = (grp + GNG - (62 - gs0) % GNG) % GNG - 1, hf = hq < 2 ? hq : -1;
#define GRW(i) "ds_write_b128 %[rw], " RNG("v", 32 + 4 * (i), 4) " offset:" STR(1024 * (i)) "\n\t"
#define GRR(i) "ds_read_b128 " RNG("v", 32 + 4 * (i), 4) ", %[rr] offset:" STR(8192 * ((i) >> 1) + 1024 * ((i) & 1)) "\n\t"
#define GNXT(D, S) "s_cmp_lt_u32 " S ", %[gs]\n\t" "s_cselect_b32 " D ", 1, " STR(GNG) "\n\t" "s_add_u32 " D ", " D ", " S "\n\t"
#define GDN1(S) "s_min_u32 s41, " S ", 63\n\t" "s_lshl_b32 s41, s41, 5\n\t" "v_add_u32 v23, s41, %[u0]\n\t" "v_lshrrev_b32 v22, 6, v23\n\t"
#define GDN2 "v_and_b32 v23, 63, v23\n\t" "v_lshl_add_u32 v23, v23, 7, %[u4]\n\t" "s_lshr_b32 s41, s41, 6\n\t" "s_max_u32 s41, s41, 29\n\t"
#define GDN3 "s_lshl_b32 s41, s41, 13\n\t" "s_add_u32 s41, s41, %[pa]\n\t" "buffer_load_dword v23, v23, %[rs], s41 offen sc1\n\t"
#define GDN(S) GDN1(S) GDN2 GDN3
#define GDEF 1
#define GB2 16
#define GCK 15
#define GOK(D) "v_cmp_le_i32 vcc, v23, v22\n\t" "s_cmp_eq_u64 vcc, 0\n\t" "s_cselect_b32 " D ", 1, 0\n\t"
static_assert(NAW == 64, "shifts above");
#define GEPI(MID, OA, OB) \
      GRW(0) GRW(1) GRW(2) GRW(3) GRW(4) GRW(5) GRW(6) GRW(7) \
      MID \
      WLG(0) "s_barrier\n\t" \
      GRR(0) GRR(1) GRR(2) GRR(3) GRR(4) GRR(5) GRR(6) GRR(7) \
      WLG(4) PKA(32, 32, 40) PKA(34, 34, 42) PKA(36, 36, 44) PKA(38, 38, 46) \
      WLG(2) PKA(32, 32, 48) PKA(34, 34, 50) PKA(36, 36, 52) PKA(38, 38, 54) \
      WLG(0) PKA(32, 32, 56) PKA(34, 34, 58) PKA(36, 36, 60) PKA(38, 38, 62) \
      "v_cvt_pk_bf16_f32 v20, v32, v33\n\t" "v_cvt_pk_bf16_f32 v21, v34, v35\n\t" "v_cvt_pk_bf16_f32 v18, v36, v37\n\t" "v_cvt_pk_bf16_f32 v19, v38, v39\n\t" \
      "buffer_store_dwordx2 v[20:21], " OA ", %[ro], s42 offen\n\t" \
      "buffer_store_dwordx2 v[18:19], " OB ", %[ro], s42 offen\n\t"
#define GH "v[64+4*(xk&15):64+4*(xk&15)+3]"
#define GSTAMP
#define GSTIN
#define GSTCL
#define GT1
#define GT2
#define GTO
  asm volatile(
      "s_nop 4\n\t"
      "v_add_u32 v16, 131072, %[va]\n\t"
      "s_mov_b32 s36, %[s0]\n\t" "s_mov_b32 s43, " STR(SPIN_CAP) "\n\t"
      "s_mov_b32 s37, 0\n\t" "v_mov_b32 v17, " STR(OOR) "\n\t" "v_mov_b32 v12, v17\n\t" "v_mov_b32 v15, v17\n\t"
      "s_cmp_ge_i32 %[hf], 0\n\t" "s_cbranch_scc1 .Li%=\n"
      ".Lp%=:\n\t"
      GDN("s36") WVM(0) GOK("s40")
      "s_cbranch_scc1 .Lr%=\n\t"
      "s_sleep 2\n\t" "s_sub_u32 s43, s43, 1\n\t" "s_cmp_gt_i32 s43, 0\n\t" "s_cbranch_scc1 .Lp%=\n"
      ".Lr%=:\n\t"
      "s_lshl_b32 s38, s36, 18\n\t" "s_add_u32 s38, s38, %[gb]\n\t"
      ".set xk, 0\n\t.rept 8\n\t" GLOAD("s38", "xk")
      "buffer_load_dwordx4 " GF(0) ", %[va], %[rf], %[tq] offen sc1\n\t" "buffer_load_dwordx4 " GF(1) ", v16, %[rf], %[tq] offen sc1\n\t"
      ".set xk, xk+1\n\t.endr\n"
#define GWD(D, i) ".if " #D " && (xk == 0)\n\t" GRW(i) ".endif\n\t"
#define GAD(i) ".if (xk >= 2) && (xk < 10) && ((xk-2)&3)\n\t" "v_add_f32 v[12+" #i "], v[12+" #i "], v[18+" #i "]\n\t" ".endif\n\t"
#define GSTEP(D) \
      "s_waitcnt vmcnt(14)\n\t" \
      GWD(D, 0) XM1(XA(32, 0), XW(0, 0), GF(0), 1) \
      ".if (xk == 2) || (xk == 6)\n\t" "ds_read_b128 v[12:15], %[rr] offset:256*(xk-2)\n\t" ".elseif (xk > 2) && (xk < 10)\n\t" "ds_read_b128 v[18:21], %[rr] offset:8192*((xk-2)&3)+1024*((xk-2)>>2)\n\t" ".endif\n\t" \
      GTR("xk+1") GWD(D, 1) XM1(XA(32, 4), XW(0, 1), GF(0), 1) \
      ".if xk < 24\n\t" GLOAD("s38", "xk+8") ".else\n\t" GLOAD("s39", "xk-24") ".endif\n\t" \
      GWD(D, 2) XM1(XA(32, 8), XW(0, 0), GF(1), 1) \
      ".if xk == " STR(GCK) "-1\n\t" GNXT("s37", "s36") ".endif\n\t" \
      ".if xk == " STR(GCK) "\n\t" GDN1("s37") ".endif\n\t" \
      ".if xk == 23\n\t" GOK("s40") ".endif\n\t" \
      GWD(D, 3) XM1(XA(32, 12), XW(0, 1), GF(1), 1) \
      ".if xk == 7\n\t" "s_add_u32 s42, s42, %[od]\n\t" ".endif\n\t" \
      ".if xk == " STR(GCK) "-1\n\t" "s_lshl_b32 s42, s36, 17\n\t" ".endif\n\t" \
      ".if xk == " STR(GCK) "\n\t" GDN2 ".endif\n\t" \
      ".if xk == 23\n\t" "s_cmp_lt_u32 s37, 63\n\t" "s_cselect_b32 s40, s40, 0\n\t" "s_cmp_lg_u32 s40, 0\n\t" ".endif\n\t" \
      GWD(D, 4) XM1(XA(32, 16), GW2, GF(0), 1) \
      ".if xk == " STR(GCK) "\n\t" GDN3 ".endif\n\t" \
      ".if xk == 23\n\t" "s_cselect_b32 s39, s37, s36\n\t" "s_lshl_b32 s39, s39, 18\n\t" "s_add_u32 s39, s39, %[gb]\n\t" ".endif\n\t" \
      ".if " #D " && (xk == 0)\n\t" GRW(5) WLG(7) ".else\n\t" WLG(1) ".endif\n\t" XM1(XA(32, 20), GW3, GF(0), 1) \
      "buffer_load_dwordx4 " GF(0) ", %[va], %[rf], %[tq] offen sc1\n\t" GAD(0) GAD(1) \
      GWD(D, 6) XM1(XA(32, 24), GW2, GF(1), 1) GAD(2) GAD(3) GWD(D, 7) XM1(XA(32, 28), GW3, GF(1), 1) \
      "buffer_load_dwordx4 " GF(1) ", v16, %[rf], %[tq] offen sc1\n\t" \
      ".if (xk == 5) || (xk == 9)\n\t" "v_cvt_pk_bf16_f32 v22, v12, v13\n\t" "v_cvt_pk_bf16_f32 v23, v14, v15\n\t" "buffer_store_dwordx2 v[22:23], v17, %[ro], s42 offen\n\t" ".endif\n\t" \
      ".if (xk == " STR(GB2) ") || (" #D " && (xk == 1))\n\t" "s_barrier\n\t" ".endif\n\t" \
      ".set xk, xk+1\n\t"
      GTR("0") "v_mov_b32 v17, " STR(OOR) "\n\t" "s_mov_b32 s42, 0\n\t"
      ".set xk, 0\n\t.rept 2\n\t" GSTEP(0) ".endr\n\t"
      "s_branch .Lc%=\n"
      ".Lb%=:\n\t"
      ".set xk, 0\n\t.rept 2\n\t" GSTEP(1) ".endr\n\t"
      "v_mov_b32 v17, %[oa]\n"
      ".Lc%=:\n\t"
      ".rept 30\n\t" GSTEP(0) ".endr\n\t"
      GSTAMP
      "s_mov_b32 s36, s37\n\t" "s_mov_b32 s38, s39\n\t"
      "s_cmp_lg_u32 s40, 0\n\t" "s_cbranch_scc1 .Lb%=\n\t"
      GEPI("", "%[oa]", "%[ob]")
      "s_cmp_lt_u32 s36, 63\n\t" "s_cbranch_scc1 .Lp%=\n\t"
      "s_cmp_lt_i32 %[hf], 0\n\t" "s_cbranch_scc1 .Le%=\n\t"
      "s_mov_b32 s37, 4\n\t" "v_mov_b32 v17, %[va]\n\t" "v_mov_b32 v12, %[oc]\n\t" "v_sub_u32 v15, %[ob], %[oa]\n\t" "v_add_u32 v15, v15, %[oc]\n\t"
      "s_barrier\n"
      ".Lh%=:\n\t"
      "v_cmp_eq_u32 vcc, -1, v23\n\t" "s_cbranch_vccz .Lt%=\n\t" "buffer_load_dword v23, %[uh], %[rs], 0 offen sc1\n\t" WVM(0)
      "s_sleep 1\n\t" "s_sub_u32 s43, s43, 1\n\t" "s_cmp_gt_i32 s43, 0\n\t" "s_cbranch_scc1 .Lh%=\n"
      ".Lt%=:\n\t"
      "v_readfirstlane_b32 s39, v23\n\t" "s_cmp_ge_i32 s39, 0\n\t" "s_cbranch_scc1 .Li%=\n"
      ".Lw%=:\n\t"
      "s_memrealtime s[40:41]\n\t" WLG(0) "s_sub_u32 s40, s39, s40\n\t" "s_lshl_b32 s40, s40, 1\n\t" "s_sub_u32 s40, s40, 1\n\t" "s_cmp_lt_u32 s40, 4000\n\t" "s_cbranch_scc1 .Lw%=\n\t"
      "s_branch .Li%=\n"
      ".Lx%=:\n\t"
      "s_sub_u32 s37, s37, 1\n\t" "s_cmp_gt_u32 s37, 1\n\t" "s_cbranch_scc1 .Li%=\n"
      ".Ly%=:\n\t"
      "buffer_load_dword v23, %[uh], %[rs], 0 offen sc1\n\t" WVM(0) "v_cmp_ge_i32 vcc, 31, v23\n\t" "s_cbranch_vccz .Li%=\n\t"
      "s_sub_u32 s43, s43, 1\n\t" "s_cmp_gt_i32 s43, 0\n\t" "s_cbranch_scc1 .Ly%=\n"
      ".Li%=:\n\t" GT1
      "s_lshl_b32 s38, %[hf], 17\n\t" "s_add_u32 s38, s38, 63*262144\n\t" "s_add_u32 s38, s38, %[gb]\n\t"
      ".set xk, 0\n\t.rept 16\n\t" GLOAD("s38", "xk") "buffer_load_dwordx4 " GH ", v17, %[rf], %[tq] offen sc1\n\t" ".set xk, xk+1\n\t.endr\n\t"
      GTR("0")
      "s_lshl_b32 s42, %[hf], 16\n\t" "s_add_u32 s42, s42, 63*131072\n\t" "v_mov_b32 v22, 0\n\t" "s_cmp_gt_u32 s37, 1\n\t" "s_cselect_b32 s39, -2, -1\n\t"
      ".set xk, 0\n\t.rept 32\n\t"
      ".if xk < 17\n\t" "s_waitcnt vmcnt(15)\n\t" ".else\n\t" "s_waitcnt vmcnt(31-xk)\n\t" ".endif\n\t"
      XM1(XA(32, 0), XW(0, 0), GH, 1) GTR("xk+1") XM1(XA(32, 4), XW(0, 1), GH, 1) "v_max3_u32 v22, v22, v[64+4*(xk&15)], v[65+4*(xk&15)]\n\t"
      XM1(XA(32, 16), GW2, GH, 1) "v_max3_u32 v22, v22, v[66+4*(xk&15)], v[67+4*(xk&15)]\n\t" "s_waitcnt lgkmcnt(1)\n\t" XM1(XA(32, 20), GW3, GH, 1)
      ".if xk & 1\n\t" "v_cmp_lt_u32 vcc, s39, v22\n\t" "s_cbranch_vccnz .Lx%=\n\t" ".endif\n\t"
      ".if xk < 16\n\t" GLOAD("s38", "xk+16") "buffer_load_dwordx4 " GH ", v17, %[rf], %[tq] offen sc1\n\t" ".endif\n\t"
      ".set xk, xk+1\n\t.endr\n\t"
      "s_nop 7\n\t"
      GEPI("", "v12", "v15")
      "s_cmp_eq_u32 s37, 0\n\t" "s_cbranch_scc1 .Lp%=\n"
      ".Le%=:\n\t"
      WVM(0)
      GT2
      : GTO [tq] "=&s"(sl)
      : GSTIN [rs] "s"(rsx), [s0] "s"(__builtin_amdgcn_readfirstlane(grp ? gs0 + grp : 0)), [gs] "s"(gs0), [gb] "s"(FO_O - 32768 + (32 * w + rot) * 1024), [va] "v"(lane * 16 + 32768),
        [rf] "s"(rsf), [wl] "v"(wl), [rw] "v"(rw_), [rr] "v"(rr_), [oa] "v"(oa), [ob] "v"(ob), [ro] "s"(rso), [od] "s"(w < 2 ? 8 : 32), [u0] "v"(8 * w + (lane & 7)), [u4] "v"((lane >> 1) & 4), [uh] "v"(PGO_A + (160 + 8 * w + (lane & 7)) * 128 + 4 * (hf & 1)), [pa] "s"(PGO_A - 29 * 8192), [hf] "s"(__builtin_amdgcn_readfirstlane(hf)), [oc] "v"((w & 1) ? OOR - 64 : oa),
        "{a[0:31]}"(xw[0]), "{a[32:63]}"(xw[1]), "{a[64:95]}"(xw[2]), "{a[96:127]}"(xw[3]),
        "{a[128:159]}"(xw[4]), "{a[160:191]}"(xw[5]), "{a[192:223]}"(xw[6]), "{a[224:255]}"(xw[7]),
        "{v[128:159]}"(w2[0]), "{v[160:191]}"(w2[1]), "{v[192:223]}"(w2[2]), "{v[224:255]}"(w2[3])
      : GSTCL "v12", "v13", "v14", "v15", "v16", "v17", "v18", "v19", "v20", "v21", "v22", "v23", "vcc", CL8(24, 25, 26, 27, 28, 29, 30, 31), CL8(32, 33, 34, 35, 36, 37, 38, 39), CL8(40, 41, 42, 43, 44, 45, 46, 47),
        CL8(48, 49, 50, 51, 52, 53, 54, 55), CL8(56, 57, 58, 59, 60, 61, 62, 63), CL64, CL72, CL80, CL88, CL96, CL112, CL120,
        "s36", "s37", "s38", "s39", "s40", "s41", "s42", "s43", "scc", "memory");
}
static_assert(GNOSSM == 1 && GNG == 3 && 4 * SU_CH + 16 <= L_TOTAL && 4 * XSLOT <= L_TOTAL, "lds");
__global__ void __launch_bounds__(256, 1)
main_kernel(const u16* __restrict__ x, const u16* __restrict__ mem0, const u16* __restrict__ Wq, const u16* __restrict__ Wk, const u16* __restrict__ Wv, const u16* __restrict__ Wo,
            unsigned char* ws, const u16* __restrict__ u, const u16* __restrict__ A_re, const u16* __restrict__ A_im, const u16* __restrict__ B_re,
            const u16* __restrict__ B_im, const u16* __restrict__ C_re, const u16* __restrict__ C_im,
            const u16* __restrict__ log_dt, const u16* __restrict__ Dskip, u16* __restrict__ out
            ) {
  __shared__ __attribute__((aligned(8192))) unsigned char lds[L_TOTAL];
  const int w = __builtin_amdgcn_readfirstlane(threadIdx.x >> 6);
  u64 pc0;
  int clen;
  {
    asm volatile("s_getpc_b64 %0\n.Lkpc0:\n\ts_mov_b32 %1, .Lkpc1-.Lkpc0" : "=s"(pc0), "=s"(clen));
  }
#define LOCAL_IDS const int lane = fresh_lane(), tid = w * 64 + lane; (void)tid;
  const int si = (blockIdx.x >> 3) * 4 + (blockIdx.x & 3);
  unsigned char* xb = (unsigned char*)(((size_t)ws + XPAGE - 1) & ~(size_t)(XPAGE - 1));
  const int fb = (int)(ws + WS_F - xb);
  u32 tag;
  asm volatile("s_load_dword %0, %1, 0x0\n\ts_waitcnt lgkmcnt(0)\n\ts_add_u32 %0, %0, 256" : "=s"(tag) : "s"(ws + WS_F) : "scc", "memory");
  const __amdgpu_buffer_rsrc_t rsx = __builtin_amdgcn_make_buffer_rsrc(xb, 0, fb + (int)WS_F_SZ, 0x00020000);
  const __amdgpu_buffer_rsrc_t rsf = __builtin_amdgcn_make_buffer_rsrc(ws + WS_QX, 0, (int)(4 * WS_QKV_SZ + 2 * WS_KM_SZ), 0x00020000);
  const __amdgpu_buffer_rsrc_t rst = __builtin_amdgcn_make_buffer_rsrc(ws + WS_UT, 0, 2 * SSM_L * SSM_D * 2 + SSM_D * 3072, 0x00020000);
  if ((blockIdx.x & 7) < 4) {
    { LOCAL_IDS
    r_role(lds, si, mem0, Wq, Wk, Wv, Wo, fb, tag, pc0, clen, rsx, rsf, rst, SsmPtrs{A_re, A_im, B_re, B_im, C_re, C_im, log_dt}, tid, lane, w
           ); }
    LOCAL_IDS
    {
      for (int it = 0; it < SPIN_CAP; ++it) {
        if (ldw(rsx, XYDN) >= 255) break;
        asm volatile("s_sleep 8" ::: "memory");
      }
      asm volatile("" ::: "memory");
      asm volatile(".Lkpc1:" ::: "memory");
      unsigned char* im = lds + 65536;
      const __amdgpu_buffer_rsrc_t rso = __builtin_amdgcn_make_buffer_rsrc((void*)out, 0, S_LEN * OUTLD * 2, 0x00020000);
      u32x4 t[8];
#pragma unroll
      for (int i = 0; i < 8; ++i) { const int p = i * 256 + tid; t[i] = __builtin_amdgcn_raw_buffer_load_b128(rst, (p >> 3) * 4096 + (p & 7) * 16, SSM_L * SSM_D * 2 + (si & 3) * 64 * 16384 + (si >> 2) * 128, 16); }
#pragma unroll
      for (int i = 0; i < 8; ++i) {
        const int p = i * 256 + tid, o = (p & 7) * (8 * 512 + 16) + ((p >> 3) & 3) * 1024 + (p >> 5) * 8;
        *(u32x2*)(im + o) = (u32x2){t[i][0], t[i][1]};
        *(u32x2*)(im + o + 512) = (u32x2){t[i][2], t[i][3]};
      }
      LBAR();
#pragma unroll
      for (int i = 0; i < 8; ++i)
        __builtin_amdgcn_raw_buffer_store_b128(*(const u32x4*)(im + i * (8 * 512 + 16) + tid * 16), rso, ((tid >> 5) * OUTLD + DM + (tid & 31) * 8) * 2,
                                               (((si >> 2) * 64 + 8 * i) * OUTLD + (si & 3) * 256) * 2, YAUX);
    }
  } else {
#define GSP 0
    int g = blockIdx.x & 3, li = blockIdx.x >> 3;
    if ((GSP & 1) && g >= 2 && li < 16) { const int h = li >> 3; li = (li & 7) + 8 * (g - 2); g = 2 + h; }
    if ((GSP & 2) && g < 2 && (li >> 3) == 3 - g) { li ^= 8; g ^= 1; }
    const int xi = g == 0 ? (li < 16 ? li : -1) : (li < 26 + (g < 3) ? 16 + 27 * (g - 1) + li : -1);
    const int ei = g == 0 ? li - 16 : 16 + 5 * (g - 1) + (31 - li);
    if (xi >= 0) {
      LOCAL_IDS
      const int head = xi & 31, mi = xi >> 5, gs = (g >= 5 - GNG && li < 16) ? li : -1;
      u32* prog = (u32*)(xb + PGO_X + xi * 128);
      if (mi == 1) x_role<true>(lds, x, Wv, FO_V, head, rsf, prog, tag, lane, w, Wo, gs);
      else x_role<false>(lds, x, mi == 0 ? Wk : Wq, mi == 0 ? FO_K : FO_Q, head, rsf, prog, tag, lane, w, Wo, gs);
    } else {
      LOCAL_IDS
      u32 acc = 0;
#pragma unroll
      for (int i = 0; i < 4; ++i) acc += *(const u32*)(x + (size_t)((i * 32 + ei) * 256 + tid) * 64);
#define UV 0
#define WWARM(n, first) {    \
        const __amdgpu_buffer_rsrc_t rwo = __builtin_amdgcn_make_buffer_rsrc((void*)Wo, 0, DM * HD * 2, 0x00020000); \
        const int ldsb = (int)(size_t)(LAS unsigned char*)lds; \
        _Pragma("unroll 1") for (int c = 0; c < (n); ++c) { dma16(rwo, lane * 16, ((first) + c) * 16384, 1024, ldsb + w * 32768 + (c & 1) * 16384); waitvm<32>(); } }
      const bool wf = ei < 16 && (USH == 2 || (USH == 3 && SLOWC(ei)));
      if ((UV == 0 && !(USH == 3 && wf)) || (UV == 1 && ei < 16)) WWARM(4, (ei * 4 + w) * 4)
      if (UV == 2 && ei < 16) WWARM(8, (ei * 4 + w) * 8)
      if (UV == 3 && ei >= 16) WWARM(8, ((ei - 16) * 4 + w) * 8)
      if (UV == 4 && ei >= 16) WWARM(4, (ei * 4 + w) * 4)
      if (ei == 0 && tid < 16) acc += *(const u32*)(Dskip + tid * 64);
      asm volatile("" :: "v"(acc));
      if (wf) asm volatile("s_waitcnt vmcnt(0)" ::: "memory");
      if ((USH && !wf) || ei >= 16) {
      { const u32 t0 = rt32(); for (int i = 0; i < 1000 && (int)(rt32() - t0) < UDELAY; ++i) asm volatile("s_sleep 16" ::: "memory"); }
      asm volatile("s_waitcnt vmcnt(0)\n\ts_barrier" ::: "memory");
      for (int ps = 0; ps < 2 - !!USH; ++ps) {
      const int r0 = USH ? ei * 64 : ((ei - 16) * 2 + ps) * 64;
#pragma unroll
      for (int hb = 0; hb < 2; ++hb) {
        u32x4 t[16];
#pragma unroll
        for (int i = 0; i < 16; ++i) t[i] = *(const u32x4*)(u + (size_t)r0 * SSM_D + ((hb * 16 + i) * 256 + tid) * 8);
#pragma unroll
        for (int i = 0; i < 16; ++i) { const int p = (hb * 16 + i) * 256 + tid; *(u32x4*)(lds + (p >> 7) * 2064 + (p & 127) * 16) = t[i]; }
      }
      LBAR();
#define NP0 0
#define NP1 0
      const int np0 = ei >= 16 ? 0 : (ei & 7) == (((384 - (int)(size_t)Wo) & 1023) >> 7) ? NP1 : NP0;
      for (int i = 0; i < 32; ++i) {
        if ((i & 3) == 0 && (i >> 2) < np0)
          asm volatile("s_mov_b32 m0, %0\n\ts_nop 3\n\tbuffer_load_dword %1, %2, %3 offen lds" :: "s"((int)(size_t)(LAS unsigned char*)lds + 160000 + w * 256), "v"(lane * (DM * 2) + ei * 128),
                       "s"(__builtin_amdgcn_make_buffer_rsrc((void*)Wo, 0, DM * HD * 2, 0x00020000)), "s"((16 * w + (((ei + 4 * w) & 15) ^ (i >> 2))) * 64 * DM * 2) : "memory");
        const int p = i * 256 + tid, un = p >> 5, rp = 4 * (p & 7) + ((p >> 3) & 3);
        const u32x2 a = *(const u32x2*)(lds + (2 * rp) * 2064 + un * 8), c = *(const u32x2*)(lds + (2 * rp + 1) * 2064 + un * 8);
        __builtin_amdgcn_raw_buffer_store_b128((u32x4){a[0], a[1], c[0], c[1]}, rst, (p >> 3) * 4096 + (p & 7) * 16, r0 * 2, 16);
      }
      asm volatile("s_waitcnt vmcnt(0) lgkmcnt(0)\n\ts_barrier" ::: "memory");
      }
      if (UV == 1) { WWARM(4, (ei * 4 + w) * 4) waitvm<0>(); }
      if (ei >= 16) {
        wait_start(rsx, fb, tag, true);
        if (tid == 0) __hip_atomic_fetch_add((u32*)(xb + XUDN), 1u, RLX_AGENT);
      }
      }
    }
    asm volatile("s_waitcnt lgkmcnt(0)\n\ts_barrier" :: "s"(tag) : "memory");
    if (w == 0 && fresh_lane() == 0) __builtin_amdgcn_raw_buffer_store_b32(tag, rsx, 0, PGO_S + si * 128, 16);
    const bool isg = g == 0 ? li >= 16 : (g >= 5 - GNG && li < 16);
    if (xi >= 0 && !(GNOSSM && isg)) {
      LOCAL_IDS
      wait_start(rsx, fb, tag, true);
      for (int it = 0; it < SPIN_CAP; ++it) {
        if (ldw(rsx, XUDN) >= 15 + 16 * !!USH) break;
        asm volatile("s_sleep 8" ::: "memory");
      }
      asm volatile("" ::: "memory");
      int un = g == 0 ? li : g == 1 ? 16 + li : 5 + 11 * g + li;
      SsmIn in;
      const __amdgpu_buffer_rsrc_t rsd = __builtin_amdgcn_make_buffer_rsrc((void*)Dskip, 0, SSM_D * 2, 0x00020000);
      ssm_load(in, 4 * un, rst, rsd, tid, lane, w);
      if (lane < 20) *(u32x4*)(lds + w * SU_CH + (lane < 12 ? SU_U + (lane / 6) * SU_CP + (lane % 6) * 16 : SU_K + ((lane - 12) >> 2) * 5200 + (lane & 3) * 16)) = (u32x4){0u, 0u, 0u, 0u};
      SSM_GOT(in);
#define NU_
      for (bool pend = false; un < 256; pend = true NU_) un = ssm_unit(lds, 4 * un, in, pend, rst, rsx, fb, rsd, tid, lane, w);
    }
    if (isg) {
      g_role(lds, Wo, out, rsx, rsf, fb, tag, g ? g + GNG - 4 : 0, li & 15, w, (u32*)(xb + XUDN), u, rst);
    }
    else {
      const int lane = fresh_lane();
      if (xi < 0) a_role(rsx, rsf, (ei - 16) * 4 + w, (NSEG - 4) * NH, (ei - 16) * 4 + w, 0, lane, fb, tag);
      for (int xp = -1; ; xp = NSEG) {
        const int t = a_late(rsx, rsf, lane, fb, tag, xp);
        if (t >= 256 || (xi >= 0 && t >= LLP)) break;
      }
    }
  }
}

extern "C" void kernel_launch(void* const* d_in, const int* in_sizes, int n_in, void* d_out, int out_size,
                              void* d_ws, size_t ws_size, hipStream_t stream) {
  const u16* x = (const u16*)d_in[0];
  const u16* mem0 = (const u16*)d_in[1];
  const u16* Wq = (const u16*)d_in[2];
  const u16* Wk = (const u16*)d_in[3];
  const u16* Wv = (const u16*)d_in[4];
  const u16* Wo = (const u16*)d_in[5];
  const u16* u = (const u16*)d_in[7];
  const u16* A_re = (const u16*)d_in[8];
  const u16* A_im = (const u16*)d_in[9];
  const u16* B_re = (const u16*)d_in[10];
  const u16* B_im = (const u16*)d_in[11];
  const u16* C_re = (const u16*)d_in[12];
  const u16* C_im = (const u16*)d_in[13];
  const u16* log_dt = (const u16*)d_in[14];
  const u16* Dskip = (const u16*)d_in[15];
  u16* out = (u16*)d_out;
  char* ws = (char*)d_ws;

  main_kernel<<<256, 256, 0, stream>>>(x, mem0, Wq, Wk, Wv, Wo, (unsigned char*)ws, u, A_re, A_im, B_re, B_im, C_re, C_im, log_dt, Dskip, out
      );
}
```
